```python
import torch

def _softmax(x, axis=-1):
    return torch.softmax(x, dim=axis)
def _gelu(x):
    return torch.nn.functional.gelu(x, approximate="tanh")
def _silu(x):
    return torch.nn.functional.silu(x)
def _sigmoid(x):
    return torch.sigmoid(x)
def _layernorm(x, axis=-1, eps=1e-5):
    m = x.mean(dim=axis, keepdim=True)
    v = x.var(dim=axis, keepdim=True, unbiased=False)
    return (x - m) / torch.sqrt(v + eps)
def _scatter_add(tgt, idx, src, axis=0):
    return tgt.clone().index_add_(axis, idx, src)
def _segment_sum(src, seg, n):
    out = torch.zeros((n,) + src.shape[1:], dtype=src.dtype)
    return out.index_add_(0, seg, src)
def _pad(x, pw):
    flat = tuple(v for pair in reversed(pw) for v in pair)
    return torch.nn.functional.pad(x, flat)

kernel_name = 'gated_mix__ssm_conv_h3__compressed_kv_cla__prompt'

def setup_inputs():

    _w = (lambda a: (lambda t: t.to(torch.bfloat16) if t.is_floating_point() else t)(torch.as_tensor(a)))
    def _A_setup():
        B, S, D = 1, 2048, 1024
        ks = 4
        return {
            "q": _w((lambda *s: torch.randn(*s, dtype=torch.bfloat16) * 0.05)(B, S, D)),
            "k": _w((lambda *s: torch.randn(*s, dtype=torch.bfloat16) * 0.4)(B, S, D)),
            "v": _w((lambda *s: torch.randn(*s, dtype=torch.bfloat16) * 0.4)(B, S, D)),
            "shift_k": _w((lambda *s: torch.randn(*s, dtype=torch.bfloat16) * 0.4)(D, ks) * 0.5),
            "ssm_k": _w((lambda *s: torch.randn(*s, dtype=torch.bfloat16) * 0.8)(D, S) * (1.0 / S ** 0.5)),
            "ssm_D": _w((lambda *s: torch.randn(*s, dtype=torch.bfloat16) * 0.4)(D)),
        }
    _A_in = _A_setup()

    def _B_setup():
        B, H, Sq, Sk, D, M = 1, 32, 2048, 2048, 128, 1024
        Dc = 4 * D
        return {
            "x": _w((lambda *s: torch.randn(*s, dtype=torch.bfloat16) * 0.05)(B, Sq, M)),
            "c_kv": _w((lambda *s: torch.randn(*s, dtype=torch.bfloat16) * 0.4)(B, Sk, Dc)),
            "w_q": _w((lambda *s: torch.randn(*s, dtype=torch.bfloat16) * 6.4)(M, H, D) * (M ** -0.5)),
            "w_uk": _w((lambda *s: torch.randn(*s, dtype=torch.bfloat16) * 6.4)(Dc, H, D) * (Dc ** -0.5)),
            "w_uv": _w((lambda *s: torch.randn(*s, dtype=torch.bfloat16) * 0.4)(Dc, H, D) * (Dc ** -0.5)),
            "w_o": _w((lambda *s: torch.randn(*s, dtype=torch.bfloat16) * 0.4)(H, D, M) * ((H * D) ** -0.5)),
        }
    _B_in = _B_setup()

    return {
        "A_q": _A_in["q"],
        "A_k": _A_in["k"],
        "A_v": _A_in["v"],
        "A_shift_k": _A_in["shift_k"],
        "A_ssm_k": _A_in["ssm_k"],
        "A_ssm_D": _A_in["ssm_D"],
        "B_x": _B_in["x"],
        "B_c_kv": _B_in["c_kv"],
        "B_w_q": _B_in["w_q"],
        "B_w_uk": _B_in["w_uk"],
        "B_w_uv": _B_in["w_uv"],
        "B_w_o": _B_in["w_o"],
        "gate_w": _w((lambda *s: torch.randn(*s, dtype=torch.bfloat16) * 0.05)(1024) * 16.0),
    }

def reference(A_q, A_k, A_v, A_shift_k, A_ssm_k, A_ssm_D, B_x, B_c_kv, B_w_q, B_w_uk, B_w_uv, B_w_o, gate_w):
    def _A_ref(q, k, v, shift_k, ssm_k, ssm_D):
        B, S, D = q.shape
        ks = shift_k.shape[-1]
        k_pad = _pad(k, ((0, 0), (ks - 1, 0), (0, 0)))
        k_shift = torch.zeros_like(k)
        for t in range(ks):
            k_shift = k_shift + k_pad[:, t:t + S, :] * shift_k[:, ks - 1 - t][None, None, :]
        kv = k_shift * v
        L = 2 * S
        kv_pad = _pad(kv, ((0, 0), (0, S), (0, 0)))
        kf_pad = _pad(ssm_k, ((0, 0), (0, S)))
        kv_f = (lambda x, axes=(-2,-1): torch.fft.rfft2(x.float(), dim=axes))(kv_pad[:, None, :, :], axes=(1, 2))
        kf_f = (lambda x, axes=(-2,-1): torch.fft.rfft2(x.float(), dim=axes))(kf_pad[:, None, :], axes=(1, 2))
        kf_f = (lambda x, *d: x.permute(*d))(kf_f, 1, 2, 0)
        y_f = kv_f * kf_f[None, :, :, :]
        y = (lambda x, s, axes=(-2,-1): torch.fft.irfft2(x, s=s, dim=axes).to(torch.bfloat16))(y_f, (1, L), axes=(1, 2))[:, 0, :S, :]
        y = y + kv * ssm_D[None, None, :]
        return q * y

    def _B_ref(x, c_kv, w_q, w_uk, w_uv, w_o):
        B, Sq, M = x.shape[0], x.shape[1], x.shape[2]
        Sk = c_kv.shape[1]
        H, D = w_q.shape[1], w_q.shape[2]
        q = torch.einsum("bqm,mhd->bhqd", x, w_q)
        k = torch.einsum("bkc,chd->bhkd", c_kv, w_uk)
        v = torch.einsum("bkc,chd->bhkd", c_kv, w_uv)
        scores = torch.einsum("bhqd,bhkd->bhqk", q, k) * (D ** -0.5)
        off = Sk - Sq
        qi = torch.arange(Sq)[:, None]
        kj = torch.arange(Sk)[None, :]
        cmask = kj <= (qi + off)
        scores = torch.where(cmask[None, None], scores, -1e9)
        p = _softmax(scores, axis=-1)
        ctx = torch.einsum("bhqk,bhkd->bhqd", p, v)
        return torch.einsum("bhqd,hdm->bqm", ctx, w_o)

    x = A_q
    g = _sigmoid((x * gate_w).sum(-1, keepdims=True))
    a = _A_ref(x, A_k, A_v, A_shift_k, A_ssm_k, A_ssm_D)
    b = _B_ref(x, B_c_kv, B_w_q, B_w_uk, B_w_uv, B_w_o)
    return g * a + (1 - g) * b
```

```cpp
#include <hip/hip_runtime.h>
#include <stdint.h>
#define QKV_ASM_CLOBBERS "v0", "v1", "v2", "v3", "v4", "v5", "v6", "v7", "v8", "v9", "v10", "v11", "v12", "v13", "v14", "v15", "v16", "v17", "v18", "v19", "v20", "v21", "v22", "v23", "v24", "v25", "v26", "v27", "v28", "v29", "v30", "v31", "v32", "v33", "v34", "v35", "v36", "v37", "v38", "v39", "v40", "v41", "v42", "v43", "v44", "v45", "v46", "v47", "v48", "v49", "v50", "v51", "v52", "v53", "v54", "v55", "v56", "v57", "v58", "v59", "v60", "v61", "v62", "v63", "v64", "v65", "v66", "v67", "v68", "v69", "v70", "v71", "v72", "v73", "v74", "v75", "v76", "v77", "v78", "v79", "v80", "v81", "v82", "v83", "v84", "v85", "v86", "v87", "v88", "v89", "v90", "v91", "v92", "v93", "v94", "v95", "v96", "v97", "v98", "v99", "v100", "v101", "v102", "v103", "v104", "v105", "v106", "v107", "v108", "v109", "v110", "v111", "v112", "v113", "v114", "v115", "v116", "v117", "v118", "v119", "v120", "v121", "v122", "v123", "v124", "v125", "v126", "v127", "v128", "v129", "v130", "v131", "v132", "v133", "v134", "v135", "v136", "v137", "v138", "v139", "v140", "v141", "v142", "v143", "v144", "v145", "v146", "v147", "v148", "v149", "v150", "v151", "v152", "v153", "v154", "v155", "v156", "v157", "v158", "v159", "v160", "v161", "v162", "v163", "v164", "v165", "v166", "v167", "v168", "v169", "v170", "v171", "v172", "v173", "v174", "v175", "v176", "v177", "v178", "v179", "v180", "v181", "v182", "v183", "v184", "v185", "v186", "v187", "v188", "v189", "v190", "v191", "v192", "v193", "v194", "v195", "v196", "v197", "v198", "v199", "v200", "v201", "v202", "v203", "v204", "v205", "v206", "v207", "v208", "v209", "v210", "v211", "v212", "v213", "v214", "v215", "v216", "v217", "v218", "v219", "v220", "v221", "v222", "v223", "v224", "v225", "v226", "v227", "v228", "v229", "v230", "v231", "v232", "v233", "v234", "v235", "v236", "v237", "v238", "v239", "v240", "v241", "v242", "v243", "v244", "v245", "v246", "v247", "v248", "v249", "v250", "v251", "v252", "v253", "v254", "v255", "a0", "a1", "a2", "a3", "a4", "a5", "a6", "a7", "a8", "a9", "a10", "a11", "a12", "a13", "a14", "a15", "a16", "a17", "a18", "a19", "a20", "a21", "a22", "a23", "a24", "a25", "a26", "a27", "a28", "a29", "a30", "a31", "a32", "a33", "a34", "a35", "a36", "a37", "a38", "a39", "a40", "a41", "a42", "a43", "a44", "a45", "a46", "a47", "a48", "a49", "a50", "a51", "a52", "a53", "a54", "a55", "a56", "a57", "a58", "a59", "a60", "a61", "a62", "a63", "a64", "a65", "a66", "a67", "a68", "a69", "a70", "a71", "a72", "a73", "a74", "a75", "a76", "a77", "a78", "a79", "a80", "a81", "a82", "a83", "a84", "a85", "a86", "a87", "a88", "a89", "a90", "a91", "a92", "a93", "a94", "a95", "a96", "a97", "a98", "a99", "a100", "a101", "a102", "a103", "a104", "a105", "a106", "a107", "a108", "a109", "a110", "a111", "a112", "a113", "a114", "a115", "a116", "a117", "a118", "a119", "a120", "a121", "a122", "a123", "a124", "a125", "a126", "a127", "a128", "a129", "a130", "a131", "a132", "a133", "a134", "a135", "a136", "a137", "a138", "a139", "a140", "a141", "a142", "a143", "a144", "a145", "a146", "a147", "a148", "a149", "a150", "a151", "a152", "a153", "a154", "a155", "a156", "a157", "a158", "a159", "a160", "a161", "a162", "a163", "a164", "a165", "a166", "a167", "a168", "a169", "a170", "a171", "a172", "a173", "a174", "a175", "a176", "a177", "a178", "a179", "a180", "a181", "a182", "a183", "a184", "a185", "a186", "a187", "a188", "a189", "a190", "a191", "a192", "a193", "a194", "a195", "a196", "a197", "a198", "a199", "a200", "a201", "a202", "a203", "a204", "a205", "a206", "a207", "a208", "a209", "a210", "a211", "a212", "a213", "a214", "a215", "a216", "a217", "a218", "a219", "a220", "a221", "a222", "a223", "a224", "a225", "a226", "a227", "a228", "a229", "a230", "a231", "a232", "a233", "a234", "a235", "a236", "a237", "a238", "a239", "a240", "a241", "a242", "a243", "a244", "a245", "a246", "a247", "a248", "a249", "a250", "a251", "a252", "a253", "a254", "a255", "s4", "s5", "s6", "s7", "s8", "s9", "s10", "s11", "s12", "s13", "s14", "s15", "s16", "s17", "s18", "s19", "s20", "s21", "s22", "s23", "s24", "s25", "s26", "s27", "s28", "s29", "s30", "s31", "s32", "s33", "s34", "s35", "s36", "s37", "s38", "s39", "s40", "s41", "s42", "s43", "s44", "s45", "s46", "s47", "s48", "s49", "s50", "s51", "s52", "s53", "s54", "s55", "s56", "s57", "s58", "s59", "s60", "s61", "s62", "s63", "s64", "s65", "s66", "s67", "s68", "s69", "s70", "s71", "s72", "s73", "s74", "s75", "s76", "s77", "s78", "s79", "s80", "s81", "s82", "s83", "s84", "s85", "s86", "s87", "s88", "s89", "s90", "s91", "s92", "s93", "s94", "s95", "s96", "s97", "s98", "s99", "s100", "s101", "vcc", "scc", "memory"
#define QKV_ASM_TEXT \
".macro M d,a,b,c\nv_mfma_f32_16x16x32_bf16 \\d,\\a,\\b,\\c\n.endm\n.macro C d,a,b\nv_cvt_pk_bf16_f32 \\d,\\a,\\b\n.endm\ns_memrealtime s[18:19]\ns_load_dwordx16 s[64:79],%0,0x0\n" \
"s_load_dwordx8 s[80:87],%0,0x40\ns_load_dwordx4 s[88:91],%0,0x60\nv_mbcnt_lo_u32_b32 v23,-1,0\nv_mbcnt_hi_u32_b32 v23,-1,v23\ns_bfe_u32 s7,%1,0x30003\ns_and_b32 s6,%1,7\ns_lshl_b32 s6,s6,2\ns_lshr_b32 s60,%1,6\n" \
"s_add_u32 s6,s6,s60\nv_lshlrev_b32 v4,4,v23\ns_getpc_b64 s[40:41]\nLCTBEG_%=:\ns_add_u32 s40,s40,LBEG_%=-LCTBEG_%=\ns_addc_u32 s41,s41,0\ns_and_b32 s41,s41,0xffff\ns_mov_b32 s42,LEND_%=-LBEG_%=\n" \
"s_mov_b32 s43,0x00020000\ns_lshr_b32 s62,%1,3\ns_lshl_b32 s62,s62,2\ns_add_u32 s62,s62,%2\ns_and_b32 s62,s62,31\ns_lshl_b32 s62,s62,13\nv_lshlrev_b32 v64,3,v4\nbuffer_load_dword v64,v64,s[40:43],s62 offen\n" \
"s_lshl_b32 s60,%2,2\nv_lshrrev_b32 v24,4,v23\nv_add_u32 v25,s60,v24\nv_xor_b32 v25,v25,v23\nv_and_b32 v25,7,v25\ns_lshl_b32 s61,%2,3\nv_lshrrev_b32 v26,3,v23\nv_add_u32 v26,s61,v26\n" \
"v_lshlrev_b32 v26,10,v26\nv_lshl_add_u32 v13,v25,4,v26\nv_add_u32 v63,v13,v26\ns_lshl_b32 s58,s7,17\ns_sub_u32 s59,1966080,s58\nv_add_u32 v17,s59,v13\nv_add_u32 v13,s58,v13\nv_add_u32 v14,0x8000,v13\n" \
"v_add_u32 v18,0x8000,v17\nv_add_u32 v15,0x8000,v14\nv_add_u32 v19,0x8000,v18\nv_add_u32 v16,0x8000,v15\nv_add_u32 v20,0x8000,v19\nv_bfe_u32 v25,v23,2,3\nv_add_u32 v25,s61,v25\nv_lshlrev_b32 v25,13,v25\n" \
"s_lshl_b32 s61,%2,1\nv_and_b32 v26,1,v24\nv_add_u32 v26,s61,v26\nv_xor_b32 v26,v26,v23\nv_and_b32 v26,3,v26\nv_lshrrev_b32 v27,5,v23\nv_lshl_add_u32 v26,v27,2,v26\nv_lshl_add_u32 v21,v26,4,v25\n" \
"s_lshl_b32 s50,%2,10\ns_add_u32 s50,s50,65536\ns_lshl_b32 s22,%2,12\ns_lshl_b32 s51,%2,11\ns_mov_b32 s31,0x00020000\ns_mov_b32 s35,0x00020000\ns_mov_b32 s95,0x00020000\ns_mov_b32 s30,2097152\n" \
"s_lshl_b32 s98,s6,8\ns_sub_u32 s34,4194304,s98\ns_mov_b32 s94,s34\ns_sub_u32 s49,8388608,s98\ns_cmp_lt_u32 %2,2\ns_cselect_b32 s57,s58,s59\ns_lshr_b32 s57,s57,1\ns_lshl_b32 s60,s6,20\n" \
"s_add_u32 s57,s57,s60\ns_and_b32 s54,%2,1\ns_lshl_b32 s60,s54,13\ns_add_u32 s57,s57,s60\ns_add_u32 s57,s57,16777216\ns_bitcmp1_b32 %2,0\ns_cselect_b32 s20,1.0,0x3e0293ee\ns_lshl_b32 s54,s54,11\n" \
"s_lshl_b32 s24,s6,3\ns_add_u32 s24,s24,s7\ns_lshl_b32 s24,s24,3\ns_lshl_b32 s60,%2,27\ns_add_u32 s24,s24,s60\ns_add_u32 s24,s24,94375936\ns_and_b32 s8,%1,7\ns_lshl_b32 s23,s8,8\n" \
"s_add_u32 s23,s23,s60\ns_and_b32 s60,%1,0xf8\ns_add_u32 s23,s23,s60\ns_add_u32 s23,s23,94380032\ns_lshl_b32 s8,s8,5\ns_lshr_b32 s58,%1,3\ns_add_u32 s8,s8,s58\nv_lshrrev_b32 v23,4,v4\n" \
"v_and_b32 v24,15,v23\nv_lshrrev_b32 v25,4,v23\ns_lshr_b32 s58,%2,1\ns_lshl_b32 s58,s58,14\ns_add_u32 s58,s58,65536\nv_lshrrev_b32 v26,1,v24\nv_xor_b32 v26,v26,v25\nv_lshlrev_b32 v26,4,v26\n" \
"v_lshl_add_u32 v26,v24,7,v26\ns_waitcnt lgkmcnt(0)\ns_mov_b64 s[28:29],s[76:77]\ns_and_b32 s29,s29,0xffff\ns_add_u32 s32,s80,s98\ns_addc_u32 s33,s81,0\ns_and_b32 s33,s33,0xffff\ns_add_u32 s92,s82,s98\n" \
"s_addc_u32 s93,s83,0\ns_and_b32 s93,s93,0xffff\ns_mov_b32 s46,0\ns_add_u32 m0,s50,65536\ns_mov_b64 s[36:37],s[90:91]\nbuffer_load_dwordx4 v13,s[28:31],s46 offen lds\ns_and_b32 s37,s37,0xffff\ns_mov_b32 s38,134217728\n" \
"s_mov_b32 s39,0x00020000\ns_sub_u32 s14,s18,2000\ns_subb_u32 s15,s19,0\ns_add_u32 m0,s50,69632\ns_add_u32 s16,s14,16777216\ns_addc_u32 s17,s15,0\nbuffer_load_dwordx4 v14,s[28:31],s46 offen lds\nv_add_u32 v5,s58,v26\n" \
"v_xor_b32 v6,64,v5\nv_add_u32 v7,0x10000,v5\nv_add_u32 v8,0x10000,v6\ns_add_u32 m0,s50,73728\nv_lshrrev_b32 v26,2,v24\nbuffer_load_dwordx4 v15,s[28:31],s46 offen lds\nv_lshlrev_b32 v26,6,v26\nv_and_b32 v27,1,v23\n" \
"v_lshl_add_u32 v26,v27,3,v26\nv_bfe_u32 v27,v23,1,1\ns_add_u32 m0,s50,77824\nv_lshlrev_b32 v28,1,v25\nbuffer_load_dwordx4 v16,s[28:31],s46 offen lds\nv_xor_b32 v27,v27,v28\nv_and_b32 v27,3,v27\nv_lshl_add_u32 v26,v27,4,v26\n" \
"v_lshl_add_u32 v9,v25,12,v26\ns_mov_b32 s47,0\ns_add_u32 m0,s22,0\nv_add_u32 v9,s54,v9\nbuffer_load_dwordx4 v21,s[32:35],s47 offen offset:0 lds\nv_lshl_add_u32 v62,v25,11,s54\nv_xor_b32 v10,0x110,v9\nv_xor_b32 v11,32,v9\n" \
"v_xor_b32 v12,0x130,v9\ns_mov_b32 s46,0\ns_add_u32 m0,s50,81920\ns_and_b32 s62,%1,7\nbuffer_load_dwordx4 v17,s[28:31],s46 offen lds\ns_lshl_b32 s62,s62,2\ns_add_u32 s62,s62,%2\ns_lshl_b32 s62,s62,5\n" \
"s_and_b32 s63,%1,0xf8\ns_add_u32 m0,s22,896\nv_bfe_u32 v24,v4,4,3\nbuffer_load_dwordx4 v21,s[32:35],s47 offen offset:128 lds\nv_lshrrev_b32 v23,7,v4\nv_lshl_add_u32 v24,v24,2,s62\nv_add_u32 v25,s63,v23\nv_lshlrev_b32 v26,14,v25\n" \
"s_add_u32 m0,s50,86016\nv_lshl_add_u32 v59,v24,1,v26\nbuffer_load_dwordx4 v18,s[28:31],s46 offen lds\nv_add_u32 v54,0xffffe800,v59\nv_add_u32 v55,0xfffff800,v59\nv_lshlrev_b32 v60,12,v24\nv_lshl_add_u32 v60,v25,4,v60\ns_add_u32 m0,s22,2048\n" \
"v_lshlrev_b32 v27,3,v24\nbuffer_load_dwordx4 v21,s[92:95],s47 offen offset:0 lds\ns_lshl_b32 s62,%1,2\ns_add_u32 s62,s62,%2\ns_lshl_b32 s63,s62,12\nv_add_u32 v26,s63,v4\ns_add_u32 m0,s50,90112\ns_lshl_b32 s62,s62,3\n" \
"buffer_load_dwordx4 v19,s[28:31],s46 offen lds\ns_add_u32 s62,s62,71303168\nv_and_b32 v61,4,v23\nv_add_u32 v61,s62,v61\nv_lshlrev_b32 v2,3,v4\ns_add_u32 m0,s22,2944\nv_and_b32 v1,0x380,v2\nbuffer_load_dwordx4 v21,s[92:95],s47 offen offset:128 lds\n" \
"v_lshrrev_b32 v23,7,v4\nv_lshl_add_u32 v1,v23,13,v1\ns_lshr_b32 s58,%1,3\ns_lshl_b32 s58,s58,2\ns_add_u32 m0,s50,94208\ns_add_u32 s58,s58,%2\nbuffer_load_dwordx4 v20,s[28:31],s46 offen lds\ns_and_b32 s60,%1,7\n" \
"s_lshl_b32 s60,s60,10\ns_lshl_b32 s59,s58,14\ns_add_u32 s59,s59,s60\ns_mov_b32 s47,262144\ns_add_u32 m0,s22,16384\ns_mov_b64 s[4:5],s[90:91]\nbuffer_load_dwordx4 v21,s[32:35],s47 offen offset:0 lds\ns_lshl_b32 s99,s58,11\n" \
"s_lshl_b32 s96,s60,8\ns_add_u32 s99,s99,s96\ns_add_u32 s96,s76,s99\ns_addc_u32 s97,s77,0\ns_add_u32 m0,s22,17280\ns_add_u32 s76,s78,s59\ns_addc_u32 s77,s79,0\nbuffer_load_dwordx4 v21,s[32:35],s47 offen offset:128 lds\n" \
"s_and_b32 s59,s59,1048575\ns_cmp_lt_u32 s58,64\ns_cselect_b64 s[84:85],s[80:81],s[82:83]\ns_add_u32 s84,s84,s59\ns_addc_u32 s85,s85,0\ns_lshl_b32 s59,s8,2\ns_add_u32 m0,s22,18432\ns_add_u32 s59,s59,%2\n" \
"buffer_load_dwordx4 v21,s[92:95],s47 offen offset:0 lds\ns_lshl_b32 s59,s59,12\ns_add_u32 s88,s72,s59\ns_addc_u32 s89,s73,0\ns_lshr_b32 s62,%1,3\ns_lshl_b32 s62,s62,2\ns_add_u32 m0,s22,19328\ns_add_u32 s62,s62,%2\n" \
"buffer_load_dwordx4 v21,s[92:95],s47 offen offset:128 lds\ns_lshl_b32 s62,s62,10\ns_min_u32 s62,s62,LEND_%=-LBEG_%=-1024\ns_getpc_b64 s[100:101]\nLRT_%=:\ns_add_u32 s62,s62,LBEG_%=-LRT_%=\ns_add_u32 s100,s100,s62\ns_addc_u32 s101,s101,0\n" \
"s_mov_b32 s47,524288\ns_add_u32 m0,s22,32768\ns_mov_b64 s[4:5],s[90:91]\nbuffer_load_dwordx4 v21,s[32:35],s47 offen offset:0 lds\ns_mov_b64 s[10:11],s[72:73]\ns_mov_b64 s[12:13],s[74:75]\ns_add_u32 s44,s78,s98\ns_addc_u32 s45,s79,0\n" \
"s_and_b32 s45,s45,0xffff\ns_add_u32 m0,s22,33664\ns_mov_b64 s[40:41],s[66:67]\nbuffer_load_dwordx4 v21,s[32:35],s47 offen offset:128 lds\ns_and_b32 s41,s41,0xffff\ns_mov_b32 s42,4194304\ns_mov_b32 s43,0x00020000\ns_mov_b64 s[80:81],s[68:69]\n" \
"s_add_u32 m0,s22,34816\ns_and_b32 s81,s81,0xffff\nbuffer_load_dwordx4 v21,s[92:95],s47 offen offset:0 lds\ns_mov_b32 s82,4194304\ns_mov_b32 s83,0x00020000\ns_add_u32 m0,s22,35712\ns_nop 0\nbuffer_load_dwordx4 v21,s[92:95],s47 offen offset:128 lds\n" \
"s_mov_b32 s46,128\ns_add_u32 m0,s50,0\ns_nop 0\nbuffer_load_dwordx4 v13,s[28:31],s46 offen lds\ns_add_u32 m0,s50,4096\ns_nop 0\nbuffer_load_dwordx4 v14,s[28:31],s46 offen lds\ns_mov_b64 exec,0xffff\n" \
"global_load_dword v0,v1,s[84:85]\ns_mov_b64 exec,-1\ns_mov_b64 exec,0xffff\nglobal_load_dword v0,v2,s[96:97]\ns_mov_b64 exec,-1\ns_mov_b64 exec,0\ns_cmp_eq_u32 %2,0\ns_cbranch_scc1 LG0_%=\n" \
"s_cmp_eq_u32 %2,1\ns_cbranch_scc1 LG1_%=\ns_cmp_eq_u32 %2,2\ns_cbranch_scc1 LG2_%=\ns_cmp_eq_u32 %2,3\ns_cbranch_scc1 LG3_%=\nLHR0_%=:\ns_mov_b64 exec,-1\n" \
"s_waitcnt vmcnt(8)\ns_barrier\ns_add_u32 m0,s50,8192\nds_read_b128 a[0:3],v7 offset:0\nds_read_b64_tr_b16 a[32:33],v9 offset:0\nds_read_b64_tr_b16 a[34:35],v10 offset:0\nds_read_b128 a[4:7],v7 offset:2048\nbuffer_load_dwordx4 v15,s[28:31],s46 offen lds\n" \
"s_add_u32 m0,s50,12288\nds_read_b64_tr_b16 a[36:37],v11 offset:0\nds_read_b64_tr_b16 a[38:39],v12 offset:0\nds_read_b128 a[8:11],v7 offset:4096\nds_read_b64_tr_b16 a[40:41],v9 offset:512\nbuffer_load_dwordx4 v16,s[28:31],s46 offen lds\ns_mov_b32 s46,128\ns_add_u32 m0,s50,16384\n" \
"ds_read_b64_tr_b16 a[42:43],v10 offset:512\nds_read_b128 a[12:15],v7 offset:6144\nds_read_b64_tr_b16 a[44:45],v11 offset:512\nds_read_b64_tr_b16 a[46:47],v12 offset:512\nbuffer_load_dwordx4 v17,s[28:31],s46 offen lds\ns_add_u32 m0,s50,20480\nds_read_b128 a[16:19],v7 offset:8192\nds_read_b64_tr_b16 a[48:49],v9 offset:1024\n" \
"ds_read_b64_tr_b16 a[50:51],v10 offset:1024\nds_read_b128 a[20:23],v7 offset:10240\nbuffer_load_dwordx4 v18,s[28:31],s46 offen lds\ns_add_u32 m0,s50,24576\nds_read_b64_tr_b16 a[52:53],v11 offset:1024\nds_read_b64_tr_b16 a[54:55],v12 offset:1024\nds_read_b128 a[24:27],v7 offset:12288\nds_read_b64_tr_b16 a[56:57],v9 offset:1536\n" \
"buffer_load_dwordx4 v19,s[28:31],s46 offen lds\ns_add_u32 m0,s50,28672\nds_read_b64_tr_b16 a[58:59],v10 offset:1536\nds_read_b128 a[28:31],v7 offset:14336\nds_read_b64_tr_b16 a[60:61],v11 offset:1536\nds_read_b64_tr_b16 a[62:63],v12 offset:1536\nbuffer_load_dwordx4 v20,s[28:31],s46 offen lds\ns_waitcnt lgkmcnt(0)\n" \
"s_branch LBEG_%=\n.p2align 12\nLBEG_%=:\ns_bitcmp1_b32 %2,0\ns_cbranch_scc1 LAV_%=\nM v[64:67],a[32:35],a[24:27],0\nds_read_b128 a[64:67],v8 offset:0\nM v[68:71],a[36:39],a[24:27],0\n" \
"M v[72:75],a[32:35],a[28:31],0\ns_mov_b32 s47,786432\ns_add_u32 m0,s22,49152\nds_read_b64_tr_b16 a[96:97],v9 offset:16384\nM v[76:79],a[36:39],a[28:31],0\nbuffer_load_dwordx4 v21,s[32:35],s47 offen offset:0 lds\nM v[80:83],a[40:43],a[24:27],0\nds_read_b64_tr_b16 a[98:99],v10 offset:16384\n" \
"M v[84:87],a[44:47],a[24:27],0\nM v[88:91],a[40:43],a[28:31],0\ns_add_u32 m0,s22,50048\nds_read_b128 a[68:71],v8 offset:2048\nM v[92:95],a[44:47],a[28:31],0\nbuffer_load_dwordx4 v21,s[32:35],s47 offen offset:128 lds\nM v[96:99],a[48:51],a[24:27],0\nds_read_b64_tr_b16 a[100:101],v11 offset:16384\n" \
"M v[100:103],a[52:55],a[24:27],0\nM v[104:107],a[48:51],a[28:31],0\ns_add_u32 m0,s22,51200\nds_read_b64_tr_b16 a[102:103],v12 offset:16384\nM v[108:111],a[52:55],a[28:31],0\nbuffer_load_dwordx4 v21,s[92:95],s47 offen offset:0 lds\nM v[112:115],a[56:59],a[24:27],0\nds_read_b128 a[72:75],v8 offset:4096\n" \
"M v[116:119],a[60:63],a[24:27],0\nM v[120:123],a[56:59],a[28:31],0\ns_add_u32 m0,s22,52096\nds_read_b64_tr_b16 a[104:105],v9 offset:16896\nM v[124:127],a[60:63],a[28:31],0\nbuffer_load_dwordx4 v21,s[92:95],s47 offen offset:128 lds\nM v[128:131],a[32:35],a[16:19],0\nds_read_b64_tr_b16 a[106:107],v10 offset:16896\n" \
"M v[132:135],a[36:39],a[16:19],0\nM v[136:139],a[32:35],a[20:23],0\ns_mov_b32 s46,256\ns_add_u32 m0,s50,32768\nds_read_b128 a[76:79],v8 offset:6144\nM v[140:143],a[36:39],a[20:23],0\nbuffer_load_dwordx4 v13,s[28:31],s46 offen lds\nM v[144:147],a[40:43],a[16:19],0\n" \
"ds_read_b64_tr_b16 a[108:109],v11 offset:16896\nM v[148:151],a[44:47],a[16:19],0\nM v[152:155],a[40:43],a[20:23],0\ns_add_u32 m0,s50,36864\nds_read_b64_tr_b16 a[110:111],v12 offset:16896\nM v[156:159],a[44:47],a[20:23],0\nbuffer_load_dwordx4 v14,s[28:31],s46 offen lds\nM v[160:163],a[48:51],a[16:19],0\n" \
"ds_read_b128 a[80:83],v8 offset:8192\nM v[164:167],a[52:55],a[16:19],0\nM v[168:171],a[48:51],a[20:23],0\ns_add_u32 m0,s50,40960\nds_read_b64_tr_b16 a[112:113],v9 offset:17408\nM v[172:175],a[52:55],a[20:23],0\nbuffer_load_dwordx4 v15,s[28:31],s46 offen lds\nM v[176:179],a[56:59],a[16:19],0\n" \
"ds_read_b64_tr_b16 a[114:115],v10 offset:17408\nM v[180:183],a[60:63],a[16:19],0\nM v[184:187],a[56:59],a[20:23],0\ns_add_u32 m0,s50,45056\nds_read_b128 a[84:87],v8 offset:10240\nM v[188:191],a[60:63],a[20:23],0\nbuffer_load_dwordx4 v16,s[28:31],s46 offen lds\nM v[192:195],a[32:35],a[0:3],0\n" \
"ds_read_b64_tr_b16 a[116:117],v11 offset:17408\nM v[196:199],a[36:39],a[0:3],0\nM v[200:203],a[32:35],a[4:7],0\ns_mov_b32 s46,256\ns_add_u32 m0,s50,49152\nds_read_b64_tr_b16 a[118:119],v12 offset:17408\nM v[204:207],a[36:39],a[4:7],0\nbuffer_load_dwordx4 v17,s[28:31],s46 offen lds\n" \
"M v[208:211],a[40:43],a[0:3],0\nds_read_b128 a[88:91],v8 offset:12288\nM v[212:215],a[44:47],a[0:3],0\nM v[216:219],a[40:43],a[4:7],0\ns_add_u32 m0,s50,53248\nds_read_b64_tr_b16 a[120:121],v9 offset:17920\nM v[220:223],a[44:47],a[4:7],0\nbuffer_load_dwordx4 v18,s[28:31],s46 offen lds\n" \
"M v[224:227],a[48:51],a[0:3],0\nds_read_b64_tr_b16 a[122:123],v10 offset:17920\nM v[228:231],a[52:55],a[0:3],0\nM v[232:235],a[48:51],a[4:7],0\ns_add_u32 m0,s50,57344\nds_read_b128 a[92:95],v8 offset:14336\nM v[236:239],a[52:55],a[4:7],0\nbuffer_load_dwordx4 v19,s[28:31],s46 offen lds\n" \
"M v[240:243],a[56:59],a[0:3],0\nds_read_b64_tr_b16 a[124:125],v11 offset:17920\nM v[244:247],a[60:63],a[0:3],0\nM v[248:251],a[56:59],a[4:7],0\ns_add_u32 m0,s50,61440\nds_read_b64_tr_b16 a[126:127],v12 offset:17920\nM v[252:255],a[60:63],a[4:7],0\nbuffer_load_dwordx4 v20,s[28:31],s46 offen lds\n" \
"M a[128:131],a[32:35],a[8:11],0\nM a[132:135],a[36:39],a[8:11],0\nM a[136:139],a[32:35],a[12:15],0\nM a[140:143],a[36:39],a[12:15],0\nglobal_load_dwordx4 v[46:49],v4,s[86:87] offset:0\nM a[144:147],a[40:43],a[8:11],0\nM a[148:151],a[44:47],a[8:11],0\nM a[152:155],a[40:43],a[12:15],0\n" \
"M a[156:159],a[44:47],a[12:15],0\ns_waitcnt vmcnt(13) lgkmcnt(0)\nM a[160:163],a[48:51],a[8:11],0\ns_barrier\nM a[164:167],a[52:55],a[8:11],0\nM a[168:171],a[48:51],a[12:15],0\nM a[172:175],a[52:55],a[12:15],0\nglobal_load_dwordx4 v[50:53],v4,s[86:87] offset:1024\n" \
"M a[176:179],a[56:59],a[8:11],0\nM a[180:183],a[60:63],a[8:11],0\nM a[184:187],a[56:59],a[12:15],0\nM a[188:191],a[60:63],a[12:15],0\nglobal_load_dwordx4 v[30:33],v26,s[64:65] offset:0\nM v[64:67],a[96:99],a[88:91],v[64:67]\nds_read_b128 a[0:3],v5 offset:0\nM v[68:71],a[100:103],a[88:91],v[68:71]\n" \
"M v[72:75],a[96:99],a[92:95],v[72:75]\nds_read_b64_tr_b16 a[32:33],v9 offset:32768\nM v[76:79],a[100:103],a[92:95],v[76:79]\nglobal_load_dwordx4 v[34:37],v26,s[64:65] offset:1024\nM v[80:83],a[104:107],a[88:91],v[80:83]\nds_read_b64_tr_b16 a[34:35],v10 offset:32768\nM v[84:87],a[108:111],a[88:91],v[84:87]\nM v[88:91],a[104:107],a[92:95],v[88:91]\n" \
"ds_read_b128 a[4:7],v5 offset:2048\nM v[92:95],a[108:111],a[92:95],v[92:95]\nglobal_load_dwordx4 v[38:41],v26,s[64:65] offset:2048\nM v[96:99],a[112:115],a[88:91],v[96:99]\nds_read_b64_tr_b16 a[36:37],v11 offset:32768\nM v[100:103],a[116:119],a[88:91],v[100:103]\nM v[104:107],a[112:115],a[92:95],v[104:107]\nds_read_b64_tr_b16 a[38:39],v12 offset:32768\n" \
"M v[108:111],a[116:119],a[92:95],v[108:111]\nglobal_load_dwordx4 v[42:45],v26,s[64:65] offset:3072\nM v[112:115],a[120:123],a[88:91],v[112:115]\nds_read_b128 a[8:11],v5 offset:4096\nM v[116:119],a[124:127],a[88:91],v[116:119]\nM v[120:123],a[120:123],a[92:95],v[120:123]\ns_mov_b32 s47,1048576\ns_add_u32 m0,s22,0\n" \
"ds_read_b64_tr_b16 a[40:41],v9 offset:33280\nM v[124:127],a[124:127],a[92:95],v[124:127]\nbuffer_load_dwordx4 v21,s[32:35],s47 offen offset:0 lds\nM v[128:131],a[96:99],a[80:83],v[128:131]\nds_read_b64_tr_b16 a[42:43],v10 offset:33280\nM v[132:135],a[100:103],a[80:83],v[132:135]\nM v[136:139],a[96:99],a[84:87],v[136:139]\ns_add_u32 m0,s22,896\n" \
"ds_read_b128 a[12:15],v5 offset:6144\nM v[140:143],a[100:103],a[84:87],v[140:143]\nbuffer_load_dwordx4 v21,s[32:35],s47 offen offset:128 lds\nM v[144:147],a[104:107],a[80:83],v[144:147]\nds_read_b64_tr_b16 a[44:45],v11 offset:33280\nM v[148:151],a[108:111],a[80:83],v[148:151]\nM v[152:155],a[104:107],a[84:87],v[152:155]\ns_add_u32 m0,s22,2048\n" \
"ds_read_b64_tr_b16 a[46:47],v12 offset:33280\nM v[156:159],a[108:111],a[84:87],v[156:159]\nbuffer_load_dwordx4 v21,s[92:95],s47 offen offset:0 lds\nM v[160:163],a[112:115],a[80:83],v[160:163]\nds_read_b128 a[16:19],v5 offset:8192\nM v[164:167],a[116:119],a[80:83],v[164:167]\nM v[168:171],a[112:115],a[84:87],v[168:171]\ns_add_u32 m0,s22,2944\n" \
"ds_read_b64_tr_b16 a[48:49],v9 offset:33792\nM v[172:175],a[116:119],a[84:87],v[172:175]\nbuffer_load_dwordx4 v21,s[92:95],s47 offen offset:128 lds\nM v[176:179],a[120:123],a[80:83],v[176:179]\nds_read_b64_tr_b16 a[50:51],v10 offset:33792\nM v[180:183],a[124:127],a[80:83],v[180:183]\nM v[184:187],a[120:123],a[84:87],v[184:187]\nds_read_b128 a[20:23],v5 offset:10240\n" \
"M v[188:191],a[124:127],a[84:87],v[188:191]\nglobal_load_dwordx4 a[230:233],v27,s[70:71] offset:0\nM v[192:195],a[96:99],a[64:67],v[192:195]\nds_read_b64_tr_b16 a[52:53],v11 offset:33792\nM v[196:199],a[100:103],a[64:67],v[196:199]\nM v[200:203],a[96:99],a[68:71],v[200:203]\nds_read_b64_tr_b16 a[54:55],v12 offset:33792\nM v[204:207],a[100:103],a[68:71],v[204:207]\n" \
"global_load_dwordx4 a[234:237],v27,s[70:71] offset:16\nM v[208:211],a[104:107],a[64:67],v[208:211]\nds_read_b128 a[24:27],v5 offset:12288\nM v[212:215],a[108:111],a[64:67],v[212:215]\nM v[216:219],a[104:107],a[68:71],v[216:219]\nds_read_b64_tr_b16 a[56:57],v9 offset:34304\nM v[220:223],a[108:111],a[68:71],v[220:223]\nbuffer_load_dwordx2 a[192:193],v54,s[40:43],0 offen offset:0\n" \
"M v[224:227],a[112:115],a[64:67],v[224:227]\nds_read_b64_tr_b16 a[58:59],v10 offset:34304\nM v[228:231],a[116:119],a[64:67],v[228:231]\nM v[232:235],a[112:115],a[68:71],v[232:235]\nds_read_b128 a[28:31],v5 offset:14336\nM v[236:239],a[116:119],a[68:71],v[236:239]\nbuffer_load_dwordx2 a[194:195],v54,s[40:43],0 offen offset:2048\nM v[240:243],a[120:123],a[64:67],v[240:243]\n" \
"ds_read_b64_tr_b16 a[60:61],v11 offset:34304\nM v[244:247],a[124:127],a[64:67],v[244:247]\nM v[248:251],a[120:123],a[68:71],v[248:251]\nds_read_b64_tr_b16 a[62:63],v12 offset:34304\nM v[252:255],a[124:127],a[68:71],v[252:255]\nbuffer_load_dwordx2 a[196:197],v55,s[40:43],0 offen offset:0\nM a[128:131],a[96:99],a[72:75],a[128:131]\nM a[132:135],a[100:103],a[72:75],a[132:135]\n" \
"M a[136:139],a[96:99],a[76:79],a[136:139]\ns_mov_b32 s52,0\nM a[140:143],a[100:103],a[76:79],a[140:143]\nbuffer_load_dwordx2 a[198:199],v59,s[40:43],s52 offen offset:0\nM a[144:147],a[104:107],a[72:75],a[144:147]\nM a[148:151],a[108:111],a[72:75],a[148:151]\nM a[152:155],a[104:107],a[76:79],a[152:155]\nM a[156:159],a[108:111],a[76:79],a[156:159]\n" \
"s_waitcnt vmcnt(24) lgkmcnt(0)\nM a[160:163],a[112:115],a[72:75],a[160:163]\ns_barrier\nM a[164:167],a[116:119],a[72:75],a[164:167]\nM a[168:171],a[112:115],a[76:79],a[168:171]\nM a[172:175],a[116:119],a[76:79],a[172:175]\nbuffer_load_dwordx2 a[214:215],v59,s[80:83],s52 offen offset:0\nM a[176:179],a[120:123],a[72:75],a[176:179]\n" \
"M a[180:183],a[124:127],a[72:75],a[180:183]\ns_waitcnt vmcnt(11)\nv_lshlrev_b32 v25,16,v46\nM a[184:187],a[120:123],a[76:79],a[184:187]\nv_and_b32 v46,0xffff0000,v46\nv_lshlrev_b32 v26,16,v30\nM a[188:191],a[124:127],a[76:79],a[188:191]\nbuffer_load_dwordx2 a[200:201],v59,s[40:43],s52 offen offset:2048\n" \
"M v[64:67],a[32:35],a[24:27],v[64:67]\nds_read_b128 a[64:67],v6 offset:0\nv_and_b32 v30,0xffff0000,v30\nM v[68:71],a[36:39],a[24:27],v[68:71]\nv_mul_f32 v23,v26,v25\nv_fmac_f32 v23,v30,v46\nM v[72:75],a[32:35],a[28:31],v[72:75]\ns_mov_b32 s47,1310720\n" \
"s_add_u32 m0,s22,16384\nds_read_b64_tr_b16 a[96:97],v9 offset:49152\nM v[76:79],a[36:39],a[28:31],v[76:79]\nbuffer_load_dwordx4 v21,s[32:35],s47 offen offset:0 lds\nM v[80:83],a[40:43],a[24:27],v[80:83]\nds_read_b64_tr_b16 a[98:99],v10 offset:49152\nv_lshlrev_b32 v26,16,v38\nM v[84:87],a[44:47],a[24:27],v[84:87]\n" \
"v_and_b32 v38,0xffff0000,v38\nv_mul_f32 v24,v26,v25\nM v[88:91],a[40:43],a[28:31],v[88:91]\ns_add_u32 m0,s22,17280\nds_read_b128 a[68:71],v6 offset:2048\nv_fmac_f32 v24,v38,v46\nM v[92:95],a[44:47],a[28:31],v[92:95]\nbuffer_load_dwordx4 v21,s[32:35],s47 offen offset:128 lds\n" \
"M v[96:99],a[48:51],a[24:27],v[96:99]\nds_read_b64_tr_b16 a[100:101],v11 offset:49152\nv_lshlrev_b32 v25,16,v47\nM v[100:103],a[52:55],a[24:27],v[100:103]\nv_and_b32 v47,0xffff0000,v47\nv_lshlrev_b32 v26,16,v31\nM v[104:107],a[48:51],a[28:31],v[104:107]\ns_add_u32 m0,s22,18432\n" \
"ds_read_b64_tr_b16 a[102:103],v12 offset:49152\nv_and_b32 v31,0xffff0000,v31\nM v[108:111],a[52:55],a[28:31],v[108:111]\nbuffer_load_dwordx4 v21,s[92:95],s47 offen offset:0 lds\nM v[112:115],a[56:59],a[24:27],v[112:115]\nds_read_b128 a[72:75],v6 offset:4096\nv_fmac_f32 v23,v26,v25\nM v[116:119],a[60:63],a[24:27],v[116:119]\n" \
"v_fmac_f32 v23,v31,v47\nv_lshlrev_b32 v26,16,v39\nM v[120:123],a[56:59],a[28:31],v[120:123]\ns_add_u32 m0,s22,19328\nds_read_b64_tr_b16 a[104:105],v9 offset:49664\nv_and_b32 v39,0xffff0000,v39\nM v[124:127],a[60:63],a[28:31],v[124:127]\nbuffer_load_dwordx4 v21,s[92:95],s47 offen offset:128 lds\n" \
"M v[128:131],a[32:35],a[16:19],v[128:131]\nds_read_b64_tr_b16 a[106:107],v10 offset:49664\nv_fmac_f32 v24,v26,v25\nM v[132:135],a[36:39],a[16:19],v[132:135]\nv_fmac_f32 v24,v39,v47\nv_lshlrev_b32 v25,16,v48\nM v[136:139],a[32:35],a[20:23],v[136:139]\nds_read_b128 a[76:79],v6 offset:6144\n" \
"v_and_b32 v48,0xffff0000,v48\nM v[140:143],a[36:39],a[20:23],v[140:143]\nbuffer_load_dwordx2 a[216:217],v59,s[80:83],s52 offen offset:2048\nM v[144:147],a[40:43],a[16:19],v[144:147]\nds_read_b64_tr_b16 a[108:109],v11 offset:49664\nv_lshlrev_b32 v26,16,v32\nM v[148:151],a[44:47],a[16:19],v[148:151]\nv_and_b32 v32,0xffff0000,v32\n" \
"v_fmac_f32 v23,v26,v25\nM v[152:155],a[40:43],a[20:23],v[152:155]\ns_mov_b32 s52,4096\nds_read_b64_tr_b16 a[110:111],v12 offset:49664\nv_fmac_f32 v23,v32,v48\nM v[156:159],a[44:47],a[20:23],v[156:159]\nbuffer_load_dwordx2 a[202:203],v59,s[40:43],s52 offen offset:0\nM v[160:163],a[48:51],a[16:19],v[160:163]\n" \
"ds_read_b128 a[80:83],v6 offset:8192\nv_lshlrev_b32 v26,16,v40\nM v[164:167],a[52:55],a[16:19],v[164:167]\nv_and_b32 v40,0xffff0000,v40\nv_fmac_f32 v24,v26,v25\nM v[168:171],a[48:51],a[20:23],v[168:171]\nds_read_b64_tr_b16 a[112:113],v9 offset:50176\nv_fmac_f32 v24,v40,v48\n" \
"M v[172:175],a[52:55],a[20:23],v[172:175]\nbuffer_load_dwordx2 a[218:219],v59,s[80:83],s52 offen offset:0\nM v[176:179],a[56:59],a[16:19],v[176:179]\nds_read_b64_tr_b16 a[114:115],v10 offset:50176\nv_lshlrev_b32 v25,16,v49\nM v[180:183],a[60:63],a[16:19],v[180:183]\nv_and_b32 v49,0xffff0000,v49\nv_lshlrev_b32 v26,16,v33\n" \
"M v[184:187],a[56:59],a[20:23],v[184:187]\nds_read_b128 a[84:87],v6 offset:10240\nv_and_b32 v33,0xffff0000,v33\nM v[188:191],a[60:63],a[20:23],v[188:191]\nbuffer_load_dwordx2 a[204:205],v59,s[40:43],s52 offen offset:2048\nM v[192:195],a[32:35],a[0:3],v[192:195]\nds_read_b64_tr_b16 a[116:117],v11 offset:50176\nv_fmac_f32 v23,v26,v25\n" \
"M v[196:199],a[36:39],a[0:3],v[196:199]\nv_fmac_f32 v23,v33,v49\nv_lshlrev_b32 v26,16,v41\nM v[200:203],a[32:35],a[4:7],v[200:203]\nds_read_b64_tr_b16 a[118:119],v12 offset:50176\nv_and_b32 v41,0xffff0000,v41\nM v[204:207],a[36:39],a[4:7],v[204:207]\nbuffer_load_dwordx2 a[220:221],v59,s[80:83],s52 offen offset:2048\n" \
"M v[208:211],a[40:43],a[0:3],v[208:211]\nds_read_b128 a[88:91],v6 offset:12288\nv_fmac_f32 v24,v26,v25\nM v[212:215],a[44:47],a[0:3],v[212:215]\nv_fmac_f32 v24,v41,v49\nv_lshlrev_b32 v25,16,v50\nM v[216:219],a[40:43],a[4:7],v[216:219]\ns_mov_b32 s52,8192\n" \
"ds_read_b64_tr_b16 a[120:121],v9 offset:50688\nv_and_b32 v50,0xffff0000,v50\nM v[220:223],a[44:47],a[4:7],v[220:223]\nbuffer_load_dwordx2 a[206:207],v59,s[40:43],s52 offen offset:0\nM v[224:227],a[48:51],a[0:3],v[224:227]\nds_read_b64_tr_b16 a[122:123],v10 offset:50688\nv_lshlrev_b32 v26,16,v34\nM v[228:231],a[52:55],a[0:3],v[228:231]\n" \
"v_and_b32 v34,0xffff0000,v34\nv_fmac_f32 v23,v26,v25\nM v[232:235],a[48:51],a[4:7],v[232:235]\nds_read_b128 a[92:95],v6 offset:14336\nv_fmac_f32 v23,v34,v50\nM v[236:239],a[52:55],a[4:7],v[236:239]\nbuffer_load_dwordx2 a[222:223],v59,s[80:83],s52 offen offset:0\nM v[240:243],a[56:59],a[0:3],v[240:243]\n" \
"ds_read_b64_tr_b16 a[124:125],v11 offset:50688\nv_lshlrev_b32 v26,16,v42\nM v[244:247],a[60:63],a[0:3],v[244:247]\nv_and_b32 v42,0xffff0000,v42\nv_fmac_f32 v24,v26,v25\nM v[248:251],a[56:59],a[4:7],v[248:251]\ns_mov_b32 s47,1572864\ns_add_u32 m0,s22,32768\n" \
"ds_read_b64_tr_b16 a[126:127],v12 offset:50688\nM v[252:255],a[60:63],a[4:7],v[252:255]\nbuffer_load_dwordx4 v21,s[32:35],s47 offen offset:0 lds\nM a[128:131],a[32:35],a[8:11],a[128:131]\nv_fmac_f32 v24,v42,v50\nv_lshlrev_b32 v25,16,v51\nM a[132:135],a[36:39],a[8:11],a[132:135]\nv_and_b32 v51,0xffff0000,v51\n" \
"v_lshlrev_b32 v26,16,v35\nM a[136:139],a[32:35],a[12:15],a[136:139]\ns_add_u32 m0,s22,33664\nv_and_b32 v35,0xffff0000,v35\nM a[140:143],a[36:39],a[12:15],a[140:143]\nbuffer_load_dwordx4 v21,s[32:35],s47 offen offset:128 lds\nM a[144:147],a[40:43],a[8:11],a[144:147]\nv_fmac_f32 v23,v26,v25\n" \
"v_fmac_f32 v23,v35,v51\nM a[148:151],a[44:47],a[8:11],a[148:151]\nv_lshlrev_b32 v26,16,v43\nv_and_b32 v43,0xffff0000,v43\nM a[152:155],a[40:43],a[12:15],a[152:155]\nv_fmac_f32 v24,v26,v25\nv_fmac_f32 v24,v43,v51\nM a[156:159],a[44:47],a[12:15],a[156:159]\n" \
"s_waitcnt vmcnt(21) lgkmcnt(0)\nv_lshlrev_b32 v25,16,v52\nM a[160:163],a[48:51],a[8:11],a[160:163]\ns_barrier\nM a[164:167],a[52:55],a[8:11],a[164:167]\nv_and_b32 v52,0xffff0000,v52\nv_lshlrev_b32 v26,16,v36\nM a[168:171],a[48:51],a[12:15],a[168:171]\n" \
"s_add_u32 m0,s22,34816\nv_and_b32 v36,0xffff0000,v36\nM a[172:175],a[52:55],a[12:15],a[172:175]\nbuffer_load_dwordx4 v21,s[92:95],s47 offen offset:0 lds\nM a[176:179],a[56:59],a[8:11],a[176:179]\ns_add_u32 m0,s22,35712\nv_fmac_f32 v23,v26,v25\nM a[180:183],a[60:63],a[8:11],a[180:183]\n" \
"buffer_load_dwordx4 v21,s[92:95],s47 offen offset:128 lds\nM a[184:187],a[56:59],a[12:15],a[184:187]\ns_add_u32 s84,s84,1048576\ns_addc_u32 s85,s85,0\nv_fmac_f32 v23,v36,v52\nM a[188:191],a[60:63],a[12:15],a[188:191]\ns_mov_b64 exec,0xffff\nglobal_load_dword v0,v1,s[84:85]\n" \
"s_mov_b64 exec,-1\nM v[64:67],a[96:99],a[88:91],v[64:67]\nds_read_b128 a[0:3],v5 offset:32768\nv_lshlrev_b32 v26,16,v44\nv_and_b32 v44,0xffff0000,v44\nM v[68:71],a[100:103],a[88:91],v[68:71]\nv_fmac_f32 v24,v26,v25\nv_fmac_f32 v24,v44,v52\n" \
"M v[72:75],a[96:99],a[92:95],v[72:75]\ns_mov_b32 s46,384\ns_add_u32 m0,s50,65536\nds_read_b64_tr_b16 a[32:33],v9 offset:0\nM v[76:79],a[100:103],a[92:95],v[76:79]\nbuffer_load_dwordx4 v13,s[28:31],s46 offen lds\nM v[80:83],a[104:107],a[88:91],v[80:83]\nds_read_b64_tr_b16 a[34:35],v10 offset:0\n" \
"v_lshlrev_b32 v25,16,v53\nv_and_b32 v53,0xffff0000,v53\nM v[84:87],a[108:111],a[88:91],v[84:87]\nv_lshlrev_b32 v26,16,v37\nv_and_b32 v37,0xffff0000,v37\nM v[88:91],a[104:107],a[92:95],v[88:91]\ns_add_u32 m0,s50,69632\nds_read_b128 a[4:7],v5 offset:34816\n" \
"v_fmac_f32 v23,v26,v25\nM v[92:95],a[108:111],a[92:95],v[92:95]\nbuffer_load_dwordx4 v14,s[28:31],s46 offen lds\nM v[96:99],a[112:115],a[88:91],v[96:99]\nds_read_b64_tr_b16 a[36:37],v11 offset:0\nv_fmac_f32 v23,v37,v53\nv_lshlrev_b32 v26,16,v45\nM v[100:103],a[116:119],a[88:91],v[100:103]\n" \
"v_and_b32 v45,0xffff0000,v45\nv_fmac_f32 v24,v26,v25\nM v[104:107],a[112:115],a[92:95],v[104:107]\ns_add_u32 m0,s50,73728\nds_read_b64_tr_b16 a[38:39],v12 offset:0\nv_fmac_f32 v24,v45,v53\nM v[108:111],a[116:119],a[92:95],v[108:111]\nbuffer_load_dwordx4 v15,s[28:31],s46 offen lds\n" \
"s_waitcnt vmcnt(10)\nM v[112:115],a[120:123],a[88:91],v[112:115]\nds_read_b128 a[8:11],v5 offset:36864\nv_accvgpr_read_b32 v43,a230\nv_permlane32_swap_b32 v23,v24\nM v[116:119],a[124:127],a[88:91],v[116:119]\nv_add_f32 v23,v23,v24\nv_mov_b32 v24,v23\n" \
"M v[120:123],a[120:123],a[92:95],v[120:123]\ns_add_u32 m0,s50,77824\nds_read_b64_tr_b16 a[40:41],v9 offset:512\nv_lshlrev_b32 v42,16,v43\nM v[124:127],a[124:127],a[92:95],v[124:127]\nbuffer_load_dwordx4 v16,s[28:31],s46 offen lds\nM v[128:131],a[96:99],a[80:83],v[128:131]\nds_read_b64_tr_b16 a[42:43],v10 offset:512\n" \
"v_and_b32 v43,0xffff0000,v43\nv_permlane16_swap_b32 v23,v24\nM v[132:135],a[100:103],a[80:83],v[132:135]\nv_add_f32 v23,v23,v24\nv_accvgpr_read_b32 v45,a231\nM v[136:139],a[96:99],a[84:87],v[136:139]\ns_mov_b32 s46,384\ns_add_u32 m0,s50,81920\n" \
"ds_read_b128 a[12:15],v5 offset:38912\nM v[140:143],a[100:103],a[84:87],v[140:143]\nbuffer_load_dwordx4 v17,s[28:31],s46 offen lds\nM v[144:147],a[104:107],a[80:83],v[144:147]\nds_read_b64_tr_b16 a[44:45],v11 offset:512\nv_lshlrev_b32 v44,16,v45\nv_add_f32 v23,v23,v23 row_ror:8 row_mask:0xf bank_mask:0xf\nM v[148:151],a[108:111],a[80:83],v[148:151]\n" \
"v_and_b32 v45,0xffff0000,v45\nv_accvgpr_read_b32 v47,a232\nM v[152:155],a[104:107],a[84:87],v[152:155]\ns_add_u32 m0,s50,86016\nds_read_b64_tr_b16 a[46:47],v12 offset:512\nv_add_f32 v23,v23,v23 row_ror:4 row_mask:0xf bank_mask:0xf\nM v[156:159],a[108:111],a[84:87],v[156:159]\nbuffer_load_dwordx4 v18,s[28:31],s46 offen lds\n" \
"M v[160:163],a[112:115],a[80:83],v[160:163]\nds_read_b128 a[16:19],v5 offset:40960\nv_lshlrev_b32 v46,16,v47\nv_and_b32 v47,0xffff0000,v47\nM v[164:167],a[116:119],a[80:83],v[164:167]\nv_add_f32 v23,v23,v23 row_ror:2 row_mask:0xf bank_mask:0xf\nv_accvgpr_read_b32 v49,a233\nM v[168:171],a[112:115],a[84:87],v[168:171]\n" \
"s_add_u32 m0,s50,90112\nds_read_b64_tr_b16 a[48:49],v9 offset:1024\nv_lshlrev_b32 v48,16,v49\nM v[172:175],a[116:119],a[84:87],v[172:175]\nbuffer_load_dwordx4 v19,s[28:31],s46 offen lds\nM v[176:179],a[120:123],a[80:83],v[176:179]\nds_read_b64_tr_b16 a[50:51],v10 offset:1024\nv_add_f32 v23,v23,v23 row_ror:1 row_mask:0xf bank_mask:0xf\n" \
"C v23,v23,v23\nM v[180:183],a[124:127],a[80:83],v[180:183]\nv_and_b32 v23,0xffff0000,v23\nv_mul_f32 v23,0xbfb8aa3b,v23\nM v[184:187],a[120:123],a[84:87],v[184:187]\ns_add_u32 m0,s50,94208\nds_read_b128 a[20:23],v5 offset:43008\nv_exp_f32 v23,v23\n" \
"M v[188:191],a[124:127],a[84:87],v[188:191]\nbuffer_load_dwordx4 v20,s[28:31],s46 offen lds\nM v[192:195],a[96:99],a[64:67],v[192:195]\nds_read_b64_tr_b16 a[52:53],v11 offset:1024\nv_and_b32 v49,0xffff0000,v49\nv_accvgpr_read_b32 v36,a192\nM v[196:199],a[100:103],a[64:67],v[196:199]\nv_add_f32 v23,1.0,v23\n" \
"v_rcp_f32 v23,v23\nM v[200:203],a[96:99],a[68:71],v[200:203]\ns_mov_b32 s47,1835008\ns_add_u32 m0,s22,49152\nds_read_b64_tr_b16 a[54:55],v12 offset:1024\nM v[204:207],a[100:103],a[68:71],v[204:207]\nbuffer_load_dwordx4 v21,s[32:35],s47 offen offset:0 lds\nM v[208:211],a[104:107],a[64:67],v[208:211]\n" \
"ds_read_b128 a[24:27],v5 offset:45056\nv_lshlrev_b32 v30,16,v36\nv_and_b32 v36,0xffff0000,v36\nM v[212:215],a[108:111],a[64:67],v[212:215]\nC v23,v23,v23\nv_and_b32 v23,0xffff0000,v23\nM v[216:219],a[104:107],a[68:71],v[216:219]\ns_add_u32 m0,s22,50048\n" \
"ds_read_b64_tr_b16 a[56:57],v9 offset:1536\nv_accvgpr_read_b32 v37,a194\nM v[220:223],a[108:111],a[68:71],v[220:223]\nbuffer_load_dwordx4 v21,s[32:35],s47 offen offset:128 lds\nM v[224:227],a[112:115],a[64:67],v[224:227]\nLG0_%=:\ns_cbranch_execz LG4_%=\nds_read_b64_tr_b16 a[58:59],v10 offset:1536\n" \
"v_lshlrev_b32 v31,16,v37\nv_and_b32 v37,0xffff0000,v37\nM v[228:231],a[116:119],a[64:67],v[228:231]\nv_accvgpr_read_b32 v38,a196\nv_lshlrev_b32 v32,16,v38\nM v[232:235],a[112:115],a[68:71],v[232:235]\ns_add_u32 m0,s22,51200\nds_read_b128 a[28:31],v5 offset:47104\n" \
"v_and_b32 v38,0xffff0000,v38\nM v[236:239],a[116:119],a[68:71],v[236:239]\nbuffer_load_dwordx4 v21,s[92:95],s47 offen offset:0 lds\nM v[240:243],a[120:123],a[64:67],v[240:243]\nds_read_b64_tr_b16 a[60:61],v11 offset:1536\nv_accvgpr_read_b32 v39,a198\nv_lshlrev_b32 v33,16,v39\nM v[244:247],a[124:127],a[64:67],v[244:247]\n" \
"v_and_b32 v39,0xffff0000,v39\nv_mul_f32 v25,v30,v45\nM v[248:251],a[120:123],a[68:71],v[248:251]\ns_add_u32 m0,s22,52096\nds_read_b64_tr_b16 a[62:63],v12 offset:1536\nv_mul_f32 v26,v36,v49\nM v[252:255],a[124:127],a[68:71],v[252:255]\nbuffer_load_dwordx4 v21,s[92:95],s47 offen offset:128 lds\n" \
"M a[128:131],a[96:99],a[72:75],a[128:131]\nv_fmac_f32 v25,v31,v44\nv_fmac_f32 v26,v37,v48\nM a[132:135],a[100:103],a[72:75],a[132:135]\nv_fmac_f32 v25,v32,v43\nv_fmac_f32 v26,v38,v47\nM a[136:139],a[96:99],a[76:79],a[136:139]\nv_fmac_f32 v25,v33,v42\n" \
"v_fmac_f32 v26,v39,v46\nM a[140:143],a[100:103],a[76:79],a[140:143]\nbuffer_load_dwordx2 a[208:209],v59,s[40:43],s52 offen offset:2048\nM a[144:147],a[104:107],a[72:75],a[144:147]\nv_accvgpr_read_b32 v28,a214\nC v26,v25,v26\nM a[148:151],a[108:111],a[72:75],a[148:151]\nv_lshlrev_b32 v27,16,v28\n" \
"v_lshlrev_b32 v25,16,v26\nM a[152:155],a[104:107],a[76:79],a[152:155]\nv_and_b32 v28,0xffff0000,v28\nv_and_b32 v26,0xffff0000,v26\nM a[156:159],a[108:111],a[76:79],a[156:159]\ns_waitcnt vmcnt(25) lgkmcnt(0)\nv_mul_f32 v29,v25,v27\nv_mul_f32 v58,v26,v28\n" \
"M a[160:163],a[112:115],a[72:75],a[160:163]\ns_barrier\nM a[164:167],a[116:119],a[72:75],a[164:167]\nv_accvgpr_read_b32 v40,a200\nv_lshlrev_b32 v34,16,v40\nM a[168:171],a[112:115],a[76:79],a[168:171]\nv_and_b32 v40,0xffff0000,v40\nv_mul_f32 v25,v31,v45\n" \
"M a[172:175],a[116:119],a[76:79],a[172:175]\nbuffer_load_dwordx2 a[224:225],v59,s[80:83],s52 offen offset:2048\nM a[176:179],a[120:123],a[72:75],a[176:179]\nv_mul_f32 v26,v37,v49\nv_fmac_f32 v25,v32,v44\nM a[180:183],a[124:127],a[72:75],a[180:183]\nv_fmac_f32 v26,v38,v48\nv_fmac_f32 v25,v33,v43\n" \
"M a[184:187],a[120:123],a[76:79],a[184:187]\ns_mov_b32 s52,12288\nv_fmac_f32 v26,v39,v47\nv_fmac_f32 v25,v34,v42\nM a[188:191],a[124:127],a[76:79],a[188:191]\nbuffer_load_dwordx2 a[210:211],v59,s[40:43],s52 offen offset:0\nM v[64:67],a[32:35],a[24:27],v[64:67]\nds_read_b128 a[64:67],v6 offset:32768\n" \
"v_fmac_f32 v26,v40,v46\nv_accvgpr_read_b32 v28,a216\nM v[68:71],a[36:39],a[24:27],v[68:71]\nC v26,v25,v26\nv_lshlrev_b32 v27,16,v28\nM v[72:75],a[32:35],a[28:31],v[72:75]\nds_read_b64_tr_b16 a[96:97],v9 offset:16384\nv_lshlrev_b32 v25,16,v26\n" \
"v_and_b32 v28,0xffff0000,v28\nM v[76:79],a[36:39],a[28:31],v[76:79]\nbuffer_load_dwordx2 a[226:227],v59,s[80:83],s52 offen offset:0\nM v[80:83],a[40:43],a[24:27],v[80:83]\nds_read_b64_tr_b16 a[98:99],v10 offset:16384\nv_and_b32 v26,0xffff0000,v26\nv_mul_f32 v25,v25,v27\nM v[84:87],a[44:47],a[24:27],v[84:87]\n" \
"v_mul_f32 v26,v26,v28\nC v53,v25,v29\nM v[88:91],a[40:43],a[28:31],v[88:91]\nds_read_b128 a[68:71],v6 offset:34816\nC v57,v26,v58\nv_accvgpr_read_b32 v41,a202\nM v[92:95],a[44:47],a[28:31],v[92:95]\nbuffer_load_dwordx2 a[212:213],v59,s[40:43],s52 offen offset:2048\n" \
"M v[96:99],a[48:51],a[24:27],v[96:99]\nds_read_b64_tr_b16 a[100:101],v11 offset:16384\nv_lshlrev_b32 v35,16,v41\nv_and_b32 v41,0xffff0000,v41\nM v[100:103],a[52:55],a[24:27],v[100:103]\nv_mul_f32 v25,v32,v45\nv_mul_f32 v26,v38,v49\nM v[104:107],a[48:51],a[28:31],v[104:107]\n" \
"ds_read_b64_tr_b16 a[102:103],v12 offset:16384\nv_fmac_f32 v25,v33,v44\nv_fmac_f32 v26,v39,v48\nM v[108:111],a[52:55],a[28:31],v[108:111]\nbuffer_load_dwordx2 a[228:229],v59,s[80:83],s52 offen offset:2048\nM v[112:115],a[56:59],a[24:27],v[112:115]\nds_read_b128 a[72:75],v6 offset:36864\nv_fmac_f32 v25,v34,v43\n" \
"v_fmac_f32 v26,v40,v47\nM v[116:119],a[60:63],a[24:27],v[116:119]\nv_fmac_f32 v25,v35,v42\nv_fmac_f32 v26,v41,v46\nM v[120:123],a[56:59],a[28:31],v[120:123]\ns_mov_b32 s46,512\ns_add_u32 m0,s50,0\nds_read_b64_tr_b16 a[104:105],v9 offset:16896\n" \
"M v[124:127],a[60:63],a[28:31],v[124:127]\nbuffer_load_dwordx4 v13,s[28:31],s46 offen lds\nM v[128:131],a[32:35],a[16:19],v[128:131]\nds_read_b64_tr_b16 a[106:107],v10 offset:16896\nv_accvgpr_read_b32 v28,a218\nC v26,v25,v26\nM v[132:135],a[36:39],a[16:19],v[132:135]\nv_lshlrev_b32 v27,16,v28\n" \
"v_lshlrev_b32 v25,16,v26\nM v[136:139],a[32:35],a[20:23],v[136:139]\ns_add_u32 m0,s50,4096\nds_read_b128 a[76:79],v6 offset:38912\nv_and_b32 v28,0xffff0000,v28\nM v[140:143],a[36:39],a[20:23],v[140:143]\nbuffer_load_dwordx4 v14,s[28:31],s46 offen lds\nM v[144:147],a[40:43],a[16:19],v[144:147]\n" \
"ds_read_b64_tr_b16 a[108:109],v11 offset:16896\nv_and_b32 v26,0xffff0000,v26\nv_mul_f32 v29,v25,v27\nM v[148:151],a[44:47],a[16:19],v[148:151]\nv_mul_f32 v58,v26,v28\nv_accvgpr_read_b32 v36,a204\nM v[152:155],a[40:43],a[20:23],v[152:155]\ns_add_u32 m0,s50,8192\n" \
"ds_read_b64_tr_b16 a[110:111],v12 offset:16896\nv_lshlrev_b32 v30,16,v36\nM v[156:159],a[44:47],a[20:23],v[156:159]\nbuffer_load_dwordx4 v15,s[28:31],s46 offen lds\nM v[160:163],a[48:51],a[16:19],v[160:163]\nds_read_b128 a[80:83],v6 offset:40960\nv_and_b32 v36,0xffff0000,v36\nv_mul_f32 v25,v33,v45\n" \
"M v[164:167],a[52:55],a[16:19],v[164:167]\nv_mul_f32 v26,v39,v49\nv_fmac_f32 v25,v34,v44\nM v[168:171],a[48:51],a[20:23],v[168:171]\ns_add_u32 m0,s50,12288\nds_read_b64_tr_b16 a[112:113],v9 offset:17408\nv_fmac_f32 v26,v40,v48\nM v[172:175],a[52:55],a[20:23],v[172:175]\n" \
"buffer_load_dwordx4 v16,s[28:31],s46 offen lds\nM v[176:179],a[56:59],a[16:19],v[176:179]\nds_read_b64_tr_b16 a[114:115],v10 offset:17408\nv_fmac_f32 v25,v35,v43\nv_fmac_f32 v26,v41,v47\nM v[180:183],a[60:63],a[16:19],v[180:183]\nv_fmac_f32 v25,v30,v42\nv_fmac_f32 v26,v36,v46\n" \
"M v[184:187],a[56:59],a[20:23],v[184:187]\ns_mov_b32 s46,512\ns_add_u32 m0,s50,16384\nds_read_b128 a[84:87],v6 offset:43008\nM v[188:191],a[60:63],a[20:23],v[188:191]\nbuffer_load_dwordx4 v17,s[28:31],s46 offen lds\nM v[192:195],a[32:35],a[0:3],v[192:195]\nds_read_b64_tr_b16 a[116:117],v11 offset:17408\n" \
"v_accvgpr_read_b32 v28,a220\nC v26,v25,v26\nM v[196:199],a[36:39],a[0:3],v[196:199]\nv_lshlrev_b32 v27,16,v28\nv_lshlrev_b32 v25,16,v26\nM v[200:203],a[32:35],a[4:7],v[200:203]\ns_add_u32 m0,s50,20480\nds_read_b64_tr_b16 a[118:119],v12 offset:17408\n" \
"v_and_b32 v28,0xffff0000,v28\nM v[204:207],a[36:39],a[4:7],v[204:207]\nbuffer_load_dwordx4 v18,s[28:31],s46 offen lds\nM v[208:211],a[40:43],a[0:3],v[208:211]\nds_read_b128 a[88:91],v6 offset:45056\nv_and_b32 v26,0xffff0000,v26\nv_mul_f32 v25,v25,v27\nM v[212:215],a[44:47],a[0:3],v[212:215]\n" \
"v_mul_f32 v26,v26,v28\nC v52,v25,v29\nM v[216:219],a[40:43],a[4:7],v[216:219]\ns_add_u32 m0,s50,24576\nds_read_b64_tr_b16 a[120:121],v9 offset:17920\nC v56,v26,v58\nM v[220:223],a[44:47],a[4:7],v[220:223]\nbuffer_load_dwordx4 v19,s[28:31],s46 offen lds\n" \
"M v[224:227],a[48:51],a[0:3],v[224:227]\nds_read_b64_tr_b16 a[122:123],v10 offset:17920\nM v[228:231],a[52:55],a[0:3],v[228:231]\nM v[232:235],a[48:51],a[4:7],v[232:235]\ns_add_u32 m0,s50,28672\nds_read_b128 a[92:95],v6 offset:47104\nM v[236:239],a[52:55],a[4:7],v[236:239]\nbuffer_load_dwordx4 v20,s[28:31],s46 offen lds\n" \
"M v[240:243],a[56:59],a[0:3],v[240:243]\nds_read_b64_tr_b16 a[124:125],v11 offset:17920\nM v[244:247],a[60:63],a[0:3],v[244:247]\nM v[248:251],a[56:59],a[4:7],v[248:251]\ns_mov_b32 s47,2097152\ns_add_u32 m0,s22,0\nds_read_b64_tr_b16 a[126:127],v12 offset:17920\nM v[252:255],a[60:63],a[4:7],v[252:255]\n" \
"buffer_load_dwordx4 v21,s[32:35],s47 offen offset:0 lds\nM a[128:131],a[32:35],a[8:11],a[128:131]\nM a[132:135],a[36:39],a[8:11],a[132:135]\nM a[136:139],a[32:35],a[12:15],a[136:139]\ns_add_u32 m0,s22,896\nM a[140:143],a[36:39],a[12:15],a[140:143]\nbuffer_load_dwordx4 v21,s[32:35],s47 offen offset:128 lds\nM a[144:147],a[40:43],a[8:11],a[144:147]\n" \
"M a[148:151],a[44:47],a[8:11],a[148:151]\nM a[152:155],a[40:43],a[12:15],a[152:155]\ns_waitcnt vmcnt(10)\nv_accvgpr_read_b32 v37,a206\nv_lshlrev_b32 v31,16,v37\nM a[156:159],a[44:47],a[12:15],a[156:159]\ns_waitcnt vmcnt(20) lgkmcnt(0)\nv_and_b32 v37,0xffff0000,v37\n" \
"v_mul_f32 v25,v34,v45\nM a[160:163],a[48:51],a[8:11],a[160:163]\ns_barrier\nM a[164:167],a[52:55],a[8:11],a[164:167]\nv_mul_f32 v26,v40,v49\nv_fmac_f32 v25,v35,v44\nM a[168:171],a[48:51],a[12:15],a[168:171]\ns_add_u32 m0,s22,2048\n" \
"v_fmac_f32 v26,v41,v48\nv_fmac_f32 v25,v30,v43\nM a[172:175],a[52:55],a[12:15],a[172:175]\nbuffer_load_dwordx4 v21,s[92:95],s47 offen offset:0 lds\nM a[176:179],a[56:59],a[8:11],a[176:179]\ns_add_u32 m0,s22,2944\nv_fmac_f32 v26,v36,v47\nv_fmac_f32 v25,v31,v42\n" \
"M a[180:183],a[60:63],a[8:11],a[180:183]\nbuffer_load_dwordx4 v21,s[92:95],s47 offen offset:128 lds\nM a[184:187],a[56:59],a[12:15],a[184:187]\ns_add_u32 s84,s84,1048576\ns_addc_u32 s85,s85,0\nv_fmac_f32 v26,v37,v46\nv_accvgpr_read_b32 v28,a222\nM a[188:191],a[60:63],a[12:15],a[188:191]\n" \
"s_mov_b64 exec,0xffff\nglobal_load_dword v0,v1,s[84:85]\ns_mov_b64 exec,-1\nM v[64:67],a[96:99],a[88:91],v[64:67]\nds_read_b128 a[0:3],v7 offset:0\nC v26,v25,v26\nv_lshlrev_b32 v27,16,v28\nM v[68:71],a[100:103],a[88:91],v[68:71]\n" \
"v_lshlrev_b32 v25,16,v26\nv_and_b32 v28,0xffff0000,v28\nM v[72:75],a[96:99],a[92:95],v[72:75]\ns_mov_b32 s47,2359296\ns_add_u32 m0,s22,16384\nds_read_b64_tr_b16 a[32:33],v9 offset:32768\nM v[76:79],a[100:103],a[92:95],v[76:79]\nbuffer_load_dwordx4 v21,s[32:35],s47 offen offset:0 lds\n" \
"M v[80:83],a[104:107],a[88:91],v[80:83]\nds_read_b64_tr_b16 a[34:35],v10 offset:32768\nv_and_b32 v26,0xffff0000,v26\nv_mul_f32 v29,v25,v27\nM v[84:87],a[108:111],a[88:91],v[84:87]\nv_mul_f32 v58,v26,v28\nv_accvgpr_read_b32 v38,a208\nM v[88:91],a[104:107],a[92:95],v[88:91]\n" \
"ds_read_b128 a[4:7],v7 offset:2048\nv_lshlrev_b32 v32,16,v38\nv_and_b32 v38,0xffff0000,v38\nM v[92:95],a[108:111],a[92:95],v[92:95]\nv_mul_f32 v25,v35,v45\nv_mul_f32 v26,v41,v49\nM v[96:99],a[112:115],a[88:91],v[96:99]\ns_mov_b32 s46,640\n" \
"s_add_u32 m0,s50,32768\nds_read_b64_tr_b16 a[36:37],v11 offset:32768\nM v[100:103],a[116:119],a[88:91],v[100:103]\nbuffer_load_dwordx4 v13,s[28:31],s46 offen lds\nM v[104:107],a[112:115],a[92:95],v[104:107]\nds_read_b64_tr_b16 a[38:39],v12 offset:32768\nv_fmac_f32 v25,v30,v44\nv_fmac_f32 v26,v36,v48\n" \
"M v[108:111],a[116:119],a[92:95],v[108:111]\nv_fmac_f32 v25,v31,v43\nv_fmac_f32 v26,v37,v47\nM v[112:115],a[120:123],a[88:91],v[112:115]\nds_read_b128 a[8:11],v7 offset:4096\nv_fmac_f32 v25,v32,v42\nv_fmac_f32 v26,v38,v46\nM v[116:119],a[124:127],a[88:91],v[116:119]\n" \
"v_accvgpr_read_b32 v28,a224\nC v26,v25,v26\nM v[120:123],a[120:123],a[92:95],v[120:123]\ns_add_u32 m0,s22,17280\nds_read_b64_tr_b16 a[40:41],v9 offset:33280\nv_lshlrev_b32 v27,16,v28\nM v[124:127],a[124:127],a[92:95],v[124:127]\nbuffer_load_dwordx4 v21,s[32:35],s47 offen offset:128 lds\n" \
"M v[128:131],a[96:99],a[80:83],v[128:131]\nds_read_b64_tr_b16 a[42:43],v10 offset:33280\nv_lshlrev_b32 v25,16,v26\nv_and_b32 v28,0xffff0000,v28\nM v[132:135],a[100:103],a[80:83],v[132:135]\nv_and_b32 v26,0xffff0000,v26\nv_mul_f32 v25,v25,v27\nM v[136:139],a[96:99],a[84:87],v[136:139]\n" \
"ds_read_b128 a[12:15],v7 offset:6144\nv_mul_f32 v26,v26,v28\nC v51,v25,v29\nM v[140:143],a[100:103],a[84:87],v[140:143]\nC v55,v26,v58\nv_accvgpr_read_b32 v39,a210\nM v[144:147],a[104:107],a[80:83],v[144:147]\ns_add_u32 m0,s50,36864\n" \
"ds_read_b64_tr_b16 a[44:45],v11 offset:33280\nv_lshlrev_b32 v33,16,v39\nM v[148:151],a[108:111],a[80:83],v[148:151]\nbuffer_load_dwordx4 v14,s[28:31],s46 offen lds\nM v[152:155],a[104:107],a[84:87],v[152:155]\nds_read_b64_tr_b16 a[46:47],v12 offset:33280\nv_and_b32 v39,0xffff0000,v39\nv_mul_f32 v25,v30,v45\n" \
"M v[156:159],a[108:111],a[84:87],v[156:159]\nv_mul_f32 v26,v36,v49\nv_fmac_f32 v25,v31,v44\nM v[160:163],a[112:115],a[80:83],v[160:163]\nds_read_b128 a[16:19],v7 offset:8192\nv_fmac_f32 v26,v37,v48\nv_fmac_f32 v25,v32,v43\nM v[164:167],a[116:119],a[80:83],v[164:167]\n" \
"v_fmac_f32 v26,v38,v47\nv_fmac_f32 v25,v33,v42\nM v[168:171],a[112:115],a[84:87],v[168:171]\ns_add_u32 m0,s22,18432\nds_read_b64_tr_b16 a[48:49],v9 offset:33792\nv_fmac_f32 v26,v39,v46\nM v[172:175],a[116:119],a[84:87],v[172:175]\nbuffer_load_dwordx4 v21,s[92:95],s47 offen offset:0 lds\n" \
"M v[176:179],a[120:123],a[80:83],v[176:179]\nds_read_b64_tr_b16 a[50:51],v10 offset:33792\nv_accvgpr_read_b32 v28,a226\nC v26,v25,v26\nM v[180:183],a[124:127],a[80:83],v[180:183]\nv_lshlrev_b32 v27,16,v28\nv_lshlrev_b32 v25,16,v26\nM v[184:187],a[120:123],a[84:87],v[184:187]\n" \
"ds_read_b128 a[20:23],v7 offset:10240\nv_and_b32 v28,0xffff0000,v28\nv_and_b32 v26,0xffff0000,v26\nM v[188:191],a[124:127],a[84:87],v[188:191]\nv_mul_f32 v29,v25,v27\nv_mul_f32 v58,v26,v28\nM v[192:195],a[96:99],a[64:67],v[192:195]\ns_add_u32 m0,s50,40960\n" \
"ds_read_b64_tr_b16 a[52:53],v11 offset:33792\nv_accvgpr_read_b32 v40,a212\nM v[196:199],a[100:103],a[64:67],v[196:199]\nbuffer_load_dwordx4 v15,s[28:31],s46 offen lds\nM v[200:203],a[96:99],a[68:71],v[200:203]\nds_read_b64_tr_b16 a[54:55],v12 offset:33792\nv_lshlrev_b32 v34,16,v40\nv_and_b32 v40,0xffff0000,v40\n" \
"M v[204:207],a[100:103],a[68:71],v[204:207]\nv_mul_f32 v25,v31,v45\nv_mul_f32 v26,v37,v49\nM v[208:211],a[104:107],a[64:67],v[208:211]\nds_read_b128 a[24:27],v7 offset:12288\nv_fmac_f32 v25,v32,v44\nv_fmac_f32 v26,v38,v48\nM v[212:215],a[108:111],a[64:67],v[212:215]\n" \
"v_fmac_f32 v25,v33,v43\nv_fmac_f32 v26,v39,v47\nM v[216:219],a[104:107],a[68:71],v[216:219]\ns_add_u32 m0,s22,19328\nds_read_b64_tr_b16 a[56:57],v9 offset:34304\nv_fmac_f32 v25,v34,v42\nM v[220:223],a[108:111],a[68:71],v[220:223]\nbuffer_load_dwordx4 v21,s[92:95],s47 offen offset:128 lds\n" \
"M v[224:227],a[112:115],a[64:67],v[224:227]\nds_read_b64_tr_b16 a[58:59],v10 offset:34304\nv_fmac_f32 v26,v40,v46\nv_accvgpr_read_b32 v28,a228\nM v[228:231],a[116:119],a[64:67],v[228:231]\nC v26,v25,v26\nv_lshlrev_b32 v27,16,v28\nM v[232:235],a[112:115],a[68:71],v[232:235]\n" \
"ds_read_b128 a[28:31],v7 offset:14336\nv_lshlrev_b32 v25,16,v26\nv_and_b32 v28,0xffff0000,v28\nM v[236:239],a[116:119],a[68:71],v[236:239]\nv_and_b32 v26,0xffff0000,v26\nv_mul_f32 v25,v25,v27\nM v[240:243],a[120:123],a[64:67],v[240:243]\ns_add_u32 m0,s50,45056\n" \
"ds_read_b64_tr_b16 a[60:61],v11 offset:34304\nv_mul_f32 v26,v26,v28\nM v[244:247],a[124:127],a[64:67],v[244:247]\nbuffer_load_dwordx4 v16,s[28:31],s46 offen lds\nM v[248:251],a[120:123],a[68:71],v[248:251]\nds_read_b64_tr_b16 a[62:63],v12 offset:34304\nC v50,v25,v29\nC v54,v26,v58\n" \
"M v[252:255],a[124:127],a[68:71],v[252:255]\nv_accvgpr_read_b32 v43,a234\nv_lshlrev_b32 v42,16,v43\nM a[128:131],a[96:99],a[72:75],a[128:131]\nv_and_b32 v43,0xffff0000,v43\nv_accvgpr_read_b32 v45,a235\nM a[132:135],a[100:103],a[72:75],a[132:135]\nbuffer_store_dword v23,v61,s[36:39],0 offen sc1\n" \
"s_mov_b32 s98,79691776\nM a[136:139],a[96:99],a[76:79],a[136:139]\nv_lshlrev_b32 v44,16,v45\nv_and_b32 v45,0xffff0000,v45\nM a[140:143],a[100:103],a[76:79],a[140:143]\nv_accvgpr_read_b32 v47,a236\nv_lshlrev_b32 v46,16,v47\nM a[144:147],a[104:107],a[72:75],a[144:147]\n" \
"v_and_b32 v47,0xffff0000,v47\nv_accvgpr_read_b32 v49,a237\nM a[148:151],a[108:111],a[72:75],a[148:151]\nbuffer_store_dwordx4 v[50:53],v60,s[36:39],s98 offen offset:0 sc1\ns_mov_b32 s99,79695872\nM a[152:155],a[104:107],a[76:79],a[152:155]\nv_lshlrev_b32 v48,16,v49\nv_and_b32 v49,0xffff0000,v49\n" \
"M a[156:159],a[108:111],a[76:79],a[156:159]\ns_waitcnt vmcnt(29) lgkmcnt(0)\nv_accvgpr_read_b32 v36,a193\nv_lshlrev_b32 v30,16,v36\nM a[160:163],a[112:115],a[72:75],a[160:163]\ns_barrier\nM a[164:167],a[116:119],a[72:75],a[164:167]\nv_and_b32 v36,0xffff0000,v36\n" \
"v_accvgpr_read_b32 v37,a195\nM a[168:171],a[112:115],a[76:79],a[168:171]\nv_lshlrev_b32 v31,16,v37\nv_and_b32 v37,0xffff0000,v37\nM a[172:175],a[116:119],a[76:79],a[172:175]\nbuffer_store_dwordx4 v[54:57],v60,s[36:39],s99 offen offset:0 sc1\nM a[176:179],a[120:123],a[72:75],a[176:179]\nv_accvgpr_read_b32 v38,a197\n" \
"v_lshlrev_b32 v32,16,v38\nM a[180:183],a[124:127],a[72:75],a[180:183]\nv_and_b32 v38,0xffff0000,v38\nv_accvgpr_read_b32 v39,a199\nM a[184:187],a[120:123],a[76:79],a[184:187]\nv_lshlrev_b32 v33,16,v39\nv_and_b32 v39,0xffff0000,v39\nM a[188:191],a[124:127],a[76:79],a[188:191]\n" \
"v_mul_f32 v25,v30,v45\nv_mul_f32 v26,v36,v49\nM v[64:67],a[32:35],a[24:27],v[64:67]\nds_read_b128 a[64:67],v8 offset:0\nv_fmac_f32 v25,v31,v44\nv_fmac_f32 v26,v37,v48\nM v[68:71],a[36:39],a[24:27],v[68:71]\nv_fmac_f32 v25,v32,v43\n" \
"v_fmac_f32 v26,v38,v47\nM v[72:75],a[32:35],a[28:31],v[72:75]\ns_mov_b32 s47,2621440\ns_add_u32 m0,s22,32768\nds_read_b64_tr_b16 a[96:97],v9 offset:49152\nM v[76:79],a[36:39],a[28:31],v[76:79]\nbuffer_load_dwordx4 v21,s[32:35],s47 offen offset:0 lds\nM v[80:83],a[40:43],a[24:27],v[80:83]\n" \
"ds_read_b64_tr_b16 a[98:99],v10 offset:49152\nv_fmac_f32 v25,v33,v42\nv_fmac_f32 v26,v39,v46\nM v[84:87],a[44:47],a[24:27],v[84:87]\nv_accvgpr_read_b32 v28,a215\nC v26,v25,v26\nM v[88:91],a[40:43],a[28:31],v[88:91]\nds_read_b128 a[68:71],v8 offset:2048\n" \
"v_lshlrev_b32 v27,16,v28\nv_lshlrev_b32 v25,16,v26\nM v[92:95],a[44:47],a[28:31],v[92:95]\nv_and_b32 v28,0xffff0000,v28\nv_and_b32 v26,0xffff0000,v26\nM v[96:99],a[48:51],a[24:27],v[96:99]\ns_mov_b32 s46,640\ns_add_u32 m0,s50,49152\n" \
"ds_read_b64_tr_b16 a[100:101],v11 offset:49152\nM v[100:103],a[52:55],a[24:27],v[100:103]\nbuffer_load_dwordx4 v17,s[28:31],s46 offen lds\nM v[104:107],a[48:51],a[28:31],v[104:107]\nds_read_b64_tr_b16 a[102:103],v12 offset:49152\nv_mul_f32 v29,v25,v27\nv_mul_f32 v58,v26,v28\nM v[108:111],a[52:55],a[28:31],v[108:111]\n" \
"v_accvgpr_read_b32 v40,a201\nv_lshlrev_b32 v34,16,v40\nM v[112:115],a[56:59],a[24:27],v[112:115]\nds_read_b128 a[72:75],v8 offset:4096\nv_and_b32 v40,0xffff0000,v40\nv_mul_f32 v25,v31,v45\nM v[116:119],a[60:63],a[24:27],v[116:119]\nv_mul_f32 v26,v37,v49\n" \
"v_fmac_f32 v25,v32,v44\nM v[120:123],a[56:59],a[28:31],v[120:123]\ns_add_u32 m0,s22,33664\nds_read_b64_tr_b16 a[104:105],v9 offset:49664\nv_fmac_f32 v26,v38,v48\nM v[124:127],a[60:63],a[28:31],v[124:127]\nbuffer_load_dwordx4 v21,s[32:35],s47 offen offset:128 lds\nM v[128:131],a[32:35],a[16:19],v[128:131]\n" \
"ds_read_b64_tr_b16 a[106:107],v10 offset:49664\nv_fmac_f32 v25,v33,v43\nv_fmac_f32 v26,v39,v47\nM v[132:135],a[36:39],a[16:19],v[132:135]\nv_fmac_f32 v25,v34,v42\nv_fmac_f32 v26,v40,v46\nM v[136:139],a[32:35],a[20:23],v[136:139]\nds_read_b128 a[76:79],v8 offset:6144\n" \
"v_accvgpr_read_b32 v28,a217\nC v26,v25,v26\nM v[140:143],a[36:39],a[20:23],v[140:143]\nv_lshlrev_b32 v27,16,v28\nv_lshlrev_b32 v25,16,v26\nM v[144:147],a[40:43],a[16:19],v[144:147]\ns_add_u32 m0,s50,53248\nds_read_b64_tr_b16 a[108:109],v11 offset:49664\n" \
"v_and_b32 v28,0xffff0000,v28\nM v[148:151],a[44:47],a[16:19],v[148:151]\nbuffer_load_dwordx4 v18,s[28:31],s46 offen lds\nM v[152:155],a[40:43],a[20:23],v[152:155]\nds_read_b64_tr_b16 a[110:111],v12 offset:49664\nv_and_b32 v26,0xffff0000,v26\nv_mul_f32 v25,v25,v27\nM v[156:159],a[44:47],a[20:23],v[156:159]\n" \
"v_mul_f32 v26,v26,v28\nC v53,v25,v29\nM v[160:163],a[48:51],a[16:19],v[160:163]\nds_read_b128 a[80:83],v8 offset:8192\nC v57,v26,v58\nv_accvgpr_read_b32 v41,a203\nM v[164:167],a[52:55],a[16:19],v[164:167]\nv_lshlrev_b32 v35,16,v41\n" \
"v_and_b32 v41,0xffff0000,v41\nM v[168:171],a[48:51],a[20:23],v[168:171]\ns_add_u32 m0,s22,34816\nds_read_b64_tr_b16 a[112:113],v9 offset:50176\nv_mul_f32 v25,v32,v45\nM v[172:175],a[52:55],a[20:23],v[172:175]\nbuffer_load_dwordx4 v21,s[92:95],s47 offen offset:0 lds\nM v[176:179],a[56:59],a[16:19],v[176:179]\n" \
"ds_read_b64_tr_b16 a[114:115],v10 offset:50176\nv_mul_f32 v26,v38,v49\nv_fmac_f32 v25,v33,v44\nM v[180:183],a[60:63],a[16:19],v[180:183]\nv_fmac_f32 v26,v39,v48\nv_fmac_f32 v25,v34,v43\nM v[184:187],a[56:59],a[20:23],v[184:187]\nds_read_b128 a[84:87],v8 offset:10240\n" \
"v_fmac_f32 v26,v40,v47\nv_fmac_f32 v25,v35,v42\nM v[188:191],a[60:63],a[20:23],v[188:191]\nv_fmac_f32 v26,v41,v46\nv_accvgpr_read_b32 v28,a219\nM v[192:195],a[32:35],a[0:3],v[192:195]\ns_add_u32 m0,s50,57344\nds_read_b64_tr_b16 a[116:117],v11 offset:50176\n" \
"C v26,v25,v26\nM v[196:199],a[36:39],a[0:3],v[196:199]\nbuffer_load_dwordx4 v19,s[28:31],s46 offen lds\nM v[200:203],a[32:35],a[4:7],v[200:203]\nds_read_b64_tr_b16 a[118:119],v12 offset:50176\nv_lshlrev_b32 v27,16,v28\nv_lshlrev_b32 v25,16,v26\nM v[204:207],a[36:39],a[4:7],v[204:207]\n" \
"v_and_b32 v28,0xffff0000,v28\nv_and_b32 v26,0xffff0000,v26\nM v[208:211],a[40:43],a[0:3],v[208:211]\nds_read_b128 a[88:91],v8 offset:12288\nv_mul_f32 v29,v25,v27\nv_mul_f32 v58,v26,v28\nM v[212:215],a[44:47],a[0:3],v[212:215]\nv_accvgpr_read_b32 v36,a205\n" \
"v_lshlrev_b32 v30,16,v36\nM v[216:219],a[40:43],a[4:7],v[216:219]\nLG1_%=:\ns_cbranch_execz LG5_%=\ns_add_u32 m0,s22,35712\nds_read_b64_tr_b16 a[120:121],v9 offset:50688\nv_and_b32 v36,0xffff0000,v36\nM v[220:223],a[44:47],a[4:7],v[220:223]\n" \
"buffer_load_dwordx4 v21,s[92:95],s47 offen offset:128 lds\nM v[224:227],a[48:51],a[0:3],v[224:227]\nds_read_b64_tr_b16 a[122:123],v10 offset:50688\nv_mul_f32 v25,v33,v45\nv_mul_f32 v26,v39,v49\nM v[228:231],a[52:55],a[0:3],v[228:231]\nv_fmac_f32 v25,v34,v44\nv_fmac_f32 v26,v40,v48\n" \
"M v[232:235],a[48:51],a[4:7],v[232:235]\nds_read_b128 a[92:95],v8 offset:14336\nv_fmac_f32 v25,v35,v43\nv_fmac_f32 v26,v41,v47\nM v[236:239],a[52:55],a[4:7],v[236:239]\nv_fmac_f32 v25,v30,v42\nv_fmac_f32 v26,v36,v46\nM v[240:243],a[56:59],a[0:3],v[240:243]\n" \
"s_add_u32 m0,s50,61440\nds_read_b64_tr_b16 a[124:125],v11 offset:50688\nv_accvgpr_read_b32 v28,a221\nM v[244:247],a[60:63],a[0:3],v[244:247]\nbuffer_load_dwordx4 v20,s[28:31],s46 offen lds\nM v[248:251],a[56:59],a[4:7],v[248:251]\nds_read_b64_tr_b16 a[126:127],v12 offset:50688\nC v26,v25,v26\n" \
"v_lshlrev_b32 v27,16,v28\nM v[252:255],a[60:63],a[4:7],v[252:255]\nv_lshlrev_b32 v25,16,v26\nv_and_b32 v28,0xffff0000,v28\nM a[128:131],a[32:35],a[8:11],a[128:131]\ns_add_u32 s84,s84,1048576\ns_addc_u32 s85,s85,0\nv_and_b32 v26,0xffff0000,v26\n" \
"v_mul_f32 v25,v25,v27\nM a[132:135],a[36:39],a[8:11],a[132:135]\ns_mov_b64 exec,0xffff\nglobal_load_dword v0,v1,s[84:85]\ns_mov_b64 exec,-1\nM a[136:139],a[32:35],a[12:15],a[136:139]\nv_mul_f32 v26,v26,v28\nC v52,v25,v29\n" \
"M a[140:143],a[36:39],a[12:15],a[140:143]\nC v56,v26,v58\nv_accvgpr_read_b32 v37,a207\nM a[144:147],a[40:43],a[8:11],a[144:147]\nv_lshlrev_b32 v31,16,v37\nv_and_b32 v37,0xffff0000,v37\nM a[148:151],a[44:47],a[8:11],a[148:151]\nv_mul_f32 v25,v34,v45\n" \
"v_mul_f32 v26,v40,v49\nM a[152:155],a[40:43],a[12:15],a[152:155]\nv_fmac_f32 v25,v35,v44\nv_fmac_f32 v26,v41,v48\nM a[156:159],a[44:47],a[12:15],a[156:159]\ns_waitcnt vmcnt(21) lgkmcnt(0)\nv_fmac_f32 v25,v30,v43\nv_fmac_f32 v26,v36,v47\n" \
"M a[160:163],a[48:51],a[8:11],a[160:163]\ns_barrier\nM a[164:167],a[52:55],a[8:11],a[164:167]\nv_fmac_f32 v25,v31,v42\nv_fmac_f32 v26,v37,v46\nM a[168:171],a[48:51],a[12:15],a[168:171]\nv_accvgpr_read_b32 v28,a223\nC v26,v25,v26\n" \
"M a[172:175],a[52:55],a[12:15],a[172:175]\nv_lshlrev_b32 v27,16,v28\nv_lshlrev_b32 v25,16,v26\nM a[176:179],a[56:59],a[8:11],a[176:179]\nv_and_b32 v28,0xffff0000,v28\nv_and_b32 v26,0xffff0000,v26\nM a[180:183],a[60:63],a[8:11],a[180:183]\nv_mul_f32 v29,v25,v27\n" \
"v_mul_f32 v58,v26,v28\nM a[184:187],a[56:59],a[12:15],a[184:187]\nv_accvgpr_read_b32 v38,a209\nv_lshlrev_b32 v32,16,v38\nM a[188:191],a[60:63],a[12:15],a[188:191]\nv_and_b32 v38,0xffff0000,v38\nv_mul_f32 v25,v35,v45\nM v[64:67],a[96:99],a[88:91],v[64:67]\n" \
"ds_read_b128 a[0:3],v5 offset:0\nv_mul_f32 v26,v41,v49\nv_fmac_f32 v25,v30,v44\nM v[68:71],a[100:103],a[88:91],v[68:71]\nv_fmac_f32 v26,v36,v48\nv_fmac_f32 v25,v31,v43\nM v[72:75],a[96:99],a[92:95],v[72:75]\ns_mov_b32 s47,2883584\n" \
"s_add_u32 m0,s22,49152\nds_read_b64_tr_b16 a[32:33],v9 offset:0\nM v[76:79],a[100:103],a[92:95],v[76:79]\nbuffer_load_dwordx4 v21,s[32:35],s47 offen offset:0 lds\nM v[80:83],a[104:107],a[88:91],v[80:83]\nds_read_b64_tr_b16 a[34:35],v10 offset:0\nv_fmac_f32 v26,v37,v47\nv_fmac_f32 v25,v32,v42\n" \
"M v[84:87],a[108:111],a[88:91],v[84:87]\nv_fmac_f32 v26,v38,v46\nv_accvgpr_read_b32 v28,a225\nM v[88:91],a[104:107],a[92:95],v[88:91]\nds_read_b128 a[4:7],v5 offset:2048\nC v26,v25,v26\nv_lshlrev_b32 v27,16,v28\nM v[92:95],a[108:111],a[92:95],v[92:95]\n" \
"v_lshlrev_b32 v25,16,v26\nv_and_b32 v28,0xffff0000,v28\nM v[96:99],a[112:115],a[88:91],v[96:99]\ns_mov_b32 s46,768\ns_add_u32 m0,s50,65536\nds_read_b64_tr_b16 a[36:37],v11 offset:0\nM v[100:103],a[116:119],a[88:91],v[100:103]\nbuffer_load_dwordx4 v13,s[28:31],s46 offen lds\n" \
"M v[104:107],a[112:115],a[92:95],v[104:107]\nds_read_b64_tr_b16 a[38:39],v12 offset:0\nv_and_b32 v26,0xffff0000,v26\nv_mul_f32 v25,v25,v27\nM v[108:111],a[116:119],a[92:95],v[108:111]\nv_mul_f32 v26,v26,v28\nC v51,v25,v29\nM v[112:115],a[120:123],a[88:91],v[112:115]\n" \
"ds_read_b128 a[8:11],v5 offset:4096\nC v55,v26,v58\nv_accvgpr_read_b32 v39,a211\nM v[116:119],a[124:127],a[88:91],v[116:119]\nv_lshlrev_b32 v33,16,v39\nv_and_b32 v39,0xffff0000,v39\nM v[120:123],a[120:123],a[92:95],v[120:123]\ns_add_u32 m0,s22,50048\n" \
"ds_read_b64_tr_b16 a[40:41],v9 offset:512\nv_mul_f32 v25,v30,v45\nM v[124:127],a[124:127],a[92:95],v[124:127]\nbuffer_load_dwordx4 v21,s[32:35],s47 offen offset:128 lds\nM v[128:131],a[96:99],a[80:83],v[128:131]\nds_read_b64_tr_b16 a[42:43],v10 offset:512\nv_mul_f32 v26,v36,v49\nv_fmac_f32 v25,v31,v44\n" \
"M v[132:135],a[100:103],a[80:83],v[132:135]\nv_fmac_f32 v26,v37,v48\nv_fmac_f32 v25,v32,v43\nM v[136:139],a[96:99],a[84:87],v[136:139]\nds_read_b128 a[12:15],v5 offset:6144\nv_fmac_f32 v26,v38,v47\nv_fmac_f32 v25,v33,v42\nM v[140:143],a[100:103],a[84:87],v[140:143]\n" \
"v_fmac_f32 v26,v39,v46\nv_accvgpr_read_b32 v28,a227\nM v[144:147],a[104:107],a[80:83],v[144:147]\ns_add_u32 m0,s50,69632\nds_read_b64_tr_b16 a[44:45],v11 offset:512\nC v26,v25,v26\nM v[148:151],a[108:111],a[80:83],v[148:151]\nbuffer_load_dwordx4 v14,s[28:31],s46 offen lds\n" \
"M v[152:155],a[104:107],a[84:87],v[152:155]\nds_read_b64_tr_b16 a[46:47],v12 offset:512\nv_lshlrev_b32 v27,16,v28\nv_lshlrev_b32 v25,16,v26\nM v[156:159],a[108:111],a[84:87],v[156:159]\nv_and_b32 v28,0xffff0000,v28\nv_and_b32 v26,0xffff0000,v26\nM v[160:163],a[112:115],a[80:83],v[160:163]\n" \
"ds_read_b128 a[16:19],v5 offset:8192\nv_mul_f32 v29,v25,v27\nv_mul_f32 v58,v26,v28\nM v[164:167],a[116:119],a[80:83],v[164:167]\nv_accvgpr_read_b32 v40,a213\nv_lshlrev_b32 v34,16,v40\nM v[168:171],a[112:115],a[84:87],v[168:171]\ns_add_u32 m0,s22,51200\n" \
"ds_read_b64_tr_b16 a[48:49],v9 offset:1024\nv_and_b32 v40,0xffff0000,v40\nM v[172:175],a[116:119],a[84:87],v[172:175]\nbuffer_load_dwordx4 v21,s[92:95],s47 offen offset:0 lds\nM v[176:179],a[120:123],a[80:83],v[176:179]\nds_read_b64_tr_b16 a[50:51],v10 offset:1024\nv_mul_f32 v25,v31,v45\nv_mul_f32 v26,v37,v49\n" \
"M v[180:183],a[124:127],a[80:83],v[180:183]\nv_fmac_f32 v25,v32,v44\nv_fmac_f32 v26,v38,v48\nM v[184:187],a[120:123],a[84:87],v[184:187]\nds_read_b128 a[20:23],v5 offset:10240\nv_fmac_f32 v25,v33,v43\nv_fmac_f32 v26,v39,v47\nM v[188:191],a[124:127],a[84:87],v[188:191]\n" \
"v_fmac_f32 v25,v34,v42\nv_fmac_f32 v26,v40,v46\nM v[192:195],a[96:99],a[64:67],v[192:195]\ns_add_u32 m0,s50,73728\nds_read_b64_tr_b16 a[52:53],v11 offset:1024\nv_accvgpr_read_b32 v28,a229\nM v[196:199],a[100:103],a[64:67],v[196:199]\nbuffer_load_dwordx4 v15,s[28:31],s46 offen lds\n" \
"M v[200:203],a[96:99],a[68:71],v[200:203]\nds_read_b64_tr_b16 a[54:55],v12 offset:1024\nC v26,v25,v26\nv_lshlrev_b32 v27,16,v28\nM v[204:207],a[100:103],a[68:71],v[204:207]\nv_lshlrev_b32 v25,16,v26\nv_and_b32 v28,0xffff0000,v28\nM v[208:211],a[104:107],a[64:67],v[208:211]\n" \
"ds_read_b128 a[24:27],v5 offset:12288\nv_and_b32 v26,0xffff0000,v26\nv_mul_f32 v25,v25,v27\nM v[212:215],a[108:111],a[64:67],v[212:215]\nv_mul_f32 v26,v26,v28\nC v50,v25,v29\nM v[216:219],a[104:107],a[68:71],v[216:219]\ns_add_u32 m0,s22,52096\n" \
"ds_read_b64_tr_b16 a[56:57],v9 offset:1536\nC v54,v26,v58\nM v[220:223],a[108:111],a[68:71],v[220:223]\nbuffer_load_dwordx4 v21,s[92:95],s47 offen offset:128 lds\ns_mov_b32 s98,79699968\nM v[224:227],a[112:115],a[64:67],v[224:227]\nds_read_b64_tr_b16 a[58:59],v10 offset:1536\nM v[228:231],a[116:119],a[64:67],v[228:231]\n" \
"M v[232:235],a[112:115],a[68:71],v[232:235]\nds_read_b128 a[28:31],v5 offset:14336\nM v[236:239],a[116:119],a[68:71],v[236:239]\nM v[240:243],a[120:123],a[64:67],v[240:243]\ns_add_u32 m0,s50,77824\nds_read_b64_tr_b16 a[60:61],v11 offset:1536\nM v[244:247],a[124:127],a[64:67],v[244:247]\nbuffer_load_dwordx4 v16,s[28:31],s46 offen lds\n" \
"M v[248:251],a[120:123],a[68:71],v[248:251]\nds_read_b64_tr_b16 a[62:63],v12 offset:1536\nM v[252:255],a[124:127],a[68:71],v[252:255]\nM a[128:131],a[96:99],a[72:75],a[128:131]\nM a[132:135],a[100:103],a[72:75],a[132:135]\ns_mov_b64 exec,0xffff\nglobal_load_dword v0,v2,s[88:89]\ns_mov_b64 exec,-1\n" \
"M a[136:139],a[96:99],a[76:79],a[136:139]\nM a[140:143],a[100:103],a[76:79],a[140:143]\nM a[144:147],a[104:107],a[72:75],a[144:147]\nM a[148:151],a[108:111],a[72:75],a[148:151]\nbuffer_store_dwordx4 v[50:53],v60,s[36:39],s98 offen offset:0 sc1\ns_mov_b32 s99,79704064\nM a[152:155],a[104:107],a[76:79],a[152:155]\nM a[156:159],a[108:111],a[76:79],a[156:159]\n" \
"s_waitcnt vmcnt(23) lgkmcnt(0)\nM a[160:163],a[112:115],a[72:75],a[160:163]\ns_barrier\nM a[164:167],a[116:119],a[72:75],a[164:167]\nM a[168:171],a[112:115],a[76:79],a[168:171]\nM a[172:175],a[116:119],a[76:79],a[172:175]\nbuffer_store_dwordx4 v[54:57],v60,s[36:39],s99 offen offset:0 sc1\nM a[176:179],a[120:123],a[72:75],a[176:179]\n" \
"s_lshl_b32 s62,s8,2\ns_add_u32 s62,s62,%2\ns_lshl_b32 s63,s62,12\nM a[180:183],a[124:127],a[72:75],a[180:183]\ns_add_u32 s68,s10,s63\ns_addc_u32 s69,s11,0\ns_and_b32 s69,s69,0xffff\ns_mov_b32 s70,4096\n" \
"M a[184:187],a[120:123],a[76:79],a[184:187]\ns_mov_b32 s71,0x00020000\ns_lshr_b32 s21,s63,5\ns_add_u32 s21,s21,67108864\nM a[188:191],a[124:127],a[76:79],a[188:191]\ns_add_u32 s63,s63,79691776\ns_add_u32 s72,s4,s63\ns_addc_u32 s73,s5,0\n" \
"s_and_b32 s73,s73,0xffff\nM v[64:67],a[32:35],a[24:27],v[64:67]\nds_read_b128 a[64:67],v6 offset:0\ns_mov_b32 s74,4096\ns_mov_b32 s75,0x00020000\nM v[68:71],a[36:39],a[24:27],v[68:71]\ns_memrealtime s[18:19]\nv_add_u32 v23,-32,v4\n" \
"v_add_u32 v24,992,v4\nM v[72:75],a[32:35],a[28:31],v[72:75]\ns_mov_b32 s47,3145728\ns_add_u32 m0,s22,0\nds_read_b64_tr_b16 a[96:97],v9 offset:16384\nM v[76:79],a[36:39],a[28:31],v[76:79]\nbuffer_load_dwordx4 v21,s[32:35],s47 offen offset:0 lds\nM v[80:83],a[40:43],a[24:27],v[80:83]\n" \
"ds_read_b64_tr_b16 a[98:99],v10 offset:16384\nv_add_u32 v25,-16,v4\nv_add_u32 v26,-512,v4\nM v[84:87],a[44:47],a[24:27],v[84:87]\nv_add_u32 v27,512,v4\ns_lshr_b32 s63,%2,1\ns_lshl_b32 s63,s63,16\nM v[88:91],a[40:43],a[28:31],v[88:91]\n" \
"ds_read_b128 a[68:71],v6 offset:2048\ns_lshl_b32 s56,%2,14\ns_add_u32 s56,s56,s63\nM v[92:95],a[44:47],a[28:31],v[92:95]\nv_add_u32 v55,s56,v4\nv_mov_b32 v28,0\ns_lshl_b32 s62,s62,1\nM v[96:99],a[48:51],a[24:27],v[96:99]\n" \
"s_mov_b32 s46,768\ns_add_u32 m0,s50,81920\nds_read_b64_tr_b16 a[100:101],v11 offset:16384\nM v[100:103],a[52:55],a[24:27],v[100:103]\nbuffer_load_dwordx4 v17,s[28:31],s46 offen lds\nM v[104:107],a[48:51],a[28:31],v[104:107]\nds_read_b64_tr_b16 a[102:103],v12 offset:16384\nv_mov_b32 v29,s62\n" \
"v_lshlrev_b32 v56,23,v4\nM v[108:111],a[52:55],a[28:31],v[108:111]\ns_and_b32 s62,%1,7\ns_lshl_b32 s62,s62,8\ns_add_u32 s62,s62,94380032\nM v[112:115],a[56:59],a[24:27],v[112:115]\nds_read_b128 a[72:75],v6 offset:4096\nv_bfe_u32 v57,v4,4,5\n" \
"v_lshl_add_u32 v57,v57,3,s62\nM v[116:119],a[60:63],a[24:27],v[116:119]\nM v[120:123],a[56:59],a[28:31],v[120:123]\ns_add_u32 m0,s22,896\nds_read_b64_tr_b16 a[104:105],v9 offset:16896\nM v[124:127],a[60:63],a[28:31],v[124:127]\nbuffer_load_dwordx4 v21,s[32:35],s47 offen offset:128 lds\nM v[128:131],a[32:35],a[16:19],v[128:131]\n" \
"ds_read_b64_tr_b16 a[106:107],v10 offset:16896\nM v[132:135],a[36:39],a[16:19],v[132:135]\nM v[136:139],a[32:35],a[20:23],v[136:139]\nds_read_b128 a[76:79],v6 offset:6144\nM v[140:143],a[36:39],a[20:23],v[140:143]\nM v[144:147],a[40:43],a[16:19],v[144:147]\ns_add_u32 m0,s50,86016\nds_read_b64_tr_b16 a[108:109],v11 offset:16896\n" \
"M v[148:151],a[44:47],a[16:19],v[148:151]\nbuffer_load_dwordx4 v18,s[28:31],s46 offen lds\nM v[152:155],a[40:43],a[20:23],v[152:155]\nds_read_b64_tr_b16 a[110:111],v12 offset:16896\nM v[156:159],a[44:47],a[20:23],v[156:159]\nM v[160:163],a[48:51],a[16:19],v[160:163]\nds_read_b128 a[80:83],v6 offset:8192\nM v[164:167],a[52:55],a[16:19],v[164:167]\n" \
"M v[168:171],a[48:51],a[20:23],v[168:171]\ns_add_u32 m0,s22,2048\nds_read_b64_tr_b16 a[112:113],v9 offset:17408\nM v[172:175],a[52:55],a[20:23],v[172:175]\nbuffer_load_dwordx4 v21,s[92:95],s47 offen offset:0 lds\nM v[176:179],a[56:59],a[16:19],v[176:179]\nds_read_b64_tr_b16 a[114:115],v10 offset:17408\nM v[180:183],a[60:63],a[16:19],v[180:183]\n" \
"M v[184:187],a[56:59],a[20:23],v[184:187]\nds_read_b128 a[84:87],v6 offset:10240\nM v[188:191],a[60:63],a[20:23],v[188:191]\nM v[192:195],a[32:35],a[0:3],v[192:195]\ns_add_u32 m0,s50,90112\nds_read_b64_tr_b16 a[116:117],v11 offset:17408\nM v[196:199],a[36:39],a[0:3],v[196:199]\nbuffer_load_dwordx4 v19,s[28:31],s46 offen lds\n" \
"M v[200:203],a[32:35],a[4:7],v[200:203]\nds_read_b64_tr_b16 a[118:119],v12 offset:17408\nM v[204:207],a[36:39],a[4:7],v[204:207]\nM v[208:211],a[40:43],a[0:3],v[208:211]\nds_read_b128 a[88:91],v6 offset:12288\nM v[212:215],a[44:47],a[0:3],v[212:215]\nM v[216:219],a[40:43],a[4:7],v[216:219]\ns_add_u32 m0,s22,2944\n" \
"ds_read_b64_tr_b16 a[120:121],v9 offset:17920\nM v[220:223],a[44:47],a[4:7],v[220:223]\nbuffer_load_dwordx4 v21,s[92:95],s47 offen offset:128 lds\nM v[224:227],a[48:51],a[0:3],v[224:227]\nds_read_b64_tr_b16 a[122:123],v10 offset:17920\nM v[228:231],a[52:55],a[0:3],v[228:231]\nM v[232:235],a[48:51],a[4:7],v[232:235]\nds_read_b128 a[92:95],v6 offset:14336\n" \
"M v[236:239],a[52:55],a[4:7],v[236:239]\nM v[240:243],a[56:59],a[0:3],v[240:243]\ns_add_u32 m0,s50,94208\nds_read_b64_tr_b16 a[124:125],v11 offset:17920\nM v[244:247],a[60:63],a[0:3],v[244:247]\nbuffer_load_dwordx4 v20,s[28:31],s46 offen lds\nM v[248:251],a[56:59],a[4:7],v[248:251]\nds_read_b64_tr_b16 a[126:127],v12 offset:17920\n" \
"M v[252:255],a[60:63],a[4:7],v[252:255]\nM a[128:131],a[32:35],a[8:11],a[128:131]\ns_add_u32 s88,s88,2048\ns_addc_u32 s89,s89,0\nM a[132:135],a[36:39],a[8:11],a[132:135]\ns_mov_b64 exec,0xffff\nglobal_load_dword v0,v2,s[88:89]\ns_mov_b64 exec,-1\n" \
"M a[136:139],a[32:35],a[12:15],a[136:139]\nM a[140:143],a[36:39],a[12:15],a[140:143]\nM a[144:147],a[40:43],a[8:11],a[144:147]\nM a[148:151],a[44:47],a[8:11],a[148:151]\nM a[152:155],a[40:43],a[12:15],a[152:155]\nM a[156:159],a[44:47],a[12:15],a[156:159]\ns_waitcnt vmcnt(9) lgkmcnt(0)\nM a[160:163],a[48:51],a[8:11],a[160:163]\n" \
"s_barrier\nM a[164:167],a[52:55],a[8:11],a[164:167]\nv_mov_b32 v58,s18\nM a[168:171],a[48:51],a[12:15],a[168:171]\nv_mov_b32 v59,s19\nM a[172:175],a[52:55],a[12:15],a[172:175]\nM a[176:179],a[56:59],a[8:11],a[176:179]\nM a[180:183],a[60:63],a[8:11],a[180:183]\n" \
"M a[184:187],a[56:59],a[12:15],a[184:187]\nM a[188:191],a[60:63],a[12:15],a[188:191]\nM v[64:67],a[96:99],a[88:91],v[64:67]\nds_read_b128 a[0:3],v5 offset:32768\nM v[68:71],a[100:103],a[88:91],v[68:71]\nM v[72:75],a[96:99],a[92:95],v[72:75]\nds_read_b64_tr_b16 a[32:33],v9 offset:32768\nM v[76:79],a[100:103],a[92:95],v[76:79]\n" \
"buffer_store_dwordx2 v[58:59],v56,s[36:39],s23 offen sc1\nM v[80:83],a[104:107],a[88:91],v[80:83]\nds_read_b64_tr_b16 a[34:35],v10 offset:32768\nM v[84:87],a[108:111],a[88:91],v[84:87]\nM v[88:91],a[104:107],a[92:95],v[88:91]\ns_mov_b32 s47,3407872\ns_add_u32 m0,s22,16384\nds_read_b128 a[4:7],v5 offset:34816\n" \
"M v[92:95],a[108:111],a[92:95],v[92:95]\nbuffer_load_dwordx4 v21,s[32:35],s47 offen offset:0 lds\nM v[96:99],a[112:115],a[88:91],v[96:99]\nds_read_b64_tr_b16 a[36:37],v11 offset:32768\nM v[100:103],a[116:119],a[88:91],v[100:103]\nM v[104:107],a[112:115],a[92:95],v[104:107]\ns_mov_b32 s46,896\ns_add_u32 m0,s50,0\n" \
"ds_read_b64_tr_b16 a[38:39],v12 offset:32768\nM v[108:111],a[116:119],a[92:95],v[108:111]\nbuffer_load_dwordx4 v13,s[28:31],s46 offen lds\nM v[112:115],a[120:123],a[88:91],v[112:115]\nds_read_b128 a[8:11],v5 offset:36864\nM v[116:119],a[124:127],a[88:91],v[116:119]\nM v[120:123],a[120:123],a[92:95],v[120:123]\ns_add_u32 m0,s22,17280\n" \
"ds_read_b64_tr_b16 a[40:41],v9 offset:33280\nM v[124:127],a[124:127],a[92:95],v[124:127]\nbuffer_load_dwordx4 v21,s[32:35],s47 offen offset:128 lds\nM v[128:131],a[96:99],a[80:83],v[128:131]\nds_read_b64_tr_b16 a[42:43],v10 offset:33280\nM v[132:135],a[100:103],a[80:83],v[132:135]\nM v[136:139],a[96:99],a[84:87],v[136:139]\ns_add_u32 m0,s50,4096\n" \
"ds_read_b128 a[12:15],v5 offset:38912\nM v[140:143],a[100:103],a[84:87],v[140:143]\nbuffer_load_dwordx4 v14,s[28:31],s46 offen lds\nM v[144:147],a[104:107],a[80:83],v[144:147]\nds_read_b64_tr_b16 a[44:45],v11 offset:33280\nM v[148:151],a[108:111],a[80:83],v[148:151]\nM v[152:155],a[104:107],a[84:87],v[152:155]\ns_add_u32 m0,s22,18432\n" \
"ds_read_b64_tr_b16 a[46:47],v12 offset:33280\nM v[156:159],a[108:111],a[84:87],v[156:159]\nbuffer_load_dwordx4 v21,s[92:95],s47 offen offset:0 lds\nM v[160:163],a[112:115],a[80:83],v[160:163]\nds_read_b128 a[16:19],v5 offset:40960\nM v[164:167],a[116:119],a[80:83],v[164:167]\nM v[168:171],a[112:115],a[84:87],v[168:171]\ns_add_u32 m0,s50,8192\n" \
"ds_read_b64_tr_b16 a[48:49],v9 offset:33792\nM v[172:175],a[116:119],a[84:87],v[172:175]\nbuffer_load_dwordx4 v15,s[28:31],s46 offen lds\nM v[176:179],a[120:123],a[80:83],v[176:179]\nds_read_b64_tr_b16 a[50:51],v10 offset:33792\nM v[180:183],a[124:127],a[80:83],v[180:183]\nM v[184:187],a[120:123],a[84:87],v[184:187]\ns_add_u32 m0,s22,19328\n" \
"ds_read_b128 a[20:23],v5 offset:43008\nM v[188:191],a[124:127],a[84:87],v[188:191]\nbuffer_load_dwordx4 v21,s[92:95],s47 offen offset:128 lds\nM v[192:195],a[96:99],a[64:67],v[192:195]\nds_read_b64_tr_b16 a[52:53],v11 offset:33792\nM v[196:199],a[100:103],a[64:67],v[196:199]\nM v[200:203],a[96:99],a[68:71],v[200:203]\ns_add_u32 m0,s50,12288\n" \
"ds_read_b64_tr_b16 a[54:55],v12 offset:33792\nM v[204:207],a[100:103],a[68:71],v[204:207]\nbuffer_load_dwordx4 v16,s[28:31],s46 offen lds\nM v[208:211],a[104:107],a[64:67],v[208:211]\nds_read_b128 a[24:27],v5 offset:45056\nM v[212:215],a[108:111],a[64:67],v[212:215]\nM v[216:219],a[104:107],a[68:71],v[216:219]\nds_read_b64_tr_b16 a[56:57],v9 offset:34304\n" \
"M v[220:223],a[108:111],a[68:71],v[220:223]\nbuffer_load_dwordx4 v[30:33],v23,s[68:71],0 offen offset:0\nM v[224:227],a[112:115],a[64:67],v[224:227]\nds_read_b64_tr_b16 a[58:59],v10 offset:34304\nM v[228:231],a[116:119],a[64:67],v[228:231]\nM v[232:235],a[112:115],a[68:71],v[232:235]\nds_read_b128 a[28:31],v5 offset:47104\nM v[236:239],a[116:119],a[68:71],v[236:239]\n" \
"buffer_load_dword v50,v25,s[68:71],0 offen offset:0\nM v[240:243],a[120:123],a[64:67],v[240:243]\nds_read_b64_tr_b16 a[60:61],v11 offset:34304\nM v[244:247],a[124:127],a[64:67],v[244:247]\nM v[248:251],a[120:123],a[68:71],v[248:251]\nds_read_b64_tr_b16 a[62:63],v12 offset:34304\nM v[252:255],a[124:127],a[68:71],v[252:255]\nbuffer_load_dwordx4 v[34:37],v24,s[68:71],0 offen offset:0\n" \
"M a[128:131],a[96:99],a[72:75],a[128:131]\nM a[132:135],a[100:103],a[72:75],a[132:135]\nM a[136:139],a[96:99],a[76:79],a[136:139]\nM a[140:143],a[100:103],a[76:79],a[140:143]\nbuffer_load_dword v51,v24,s[68:71],0 offen offset:16\nM a[144:147],a[104:107],a[72:75],a[144:147]\nM a[148:151],a[108:111],a[72:75],a[148:151]\nM a[152:155],a[104:107],a[76:79],a[152:155]\n" \
"M a[156:159],a[108:111],a[76:79],a[156:159]\ns_waitcnt vmcnt(26) lgkmcnt(0)\nM a[160:163],a[112:115],a[72:75],a[160:163]\ns_barrier\nM a[164:167],a[116:119],a[72:75],a[164:167]\nM a[168:171],a[112:115],a[76:79],a[168:171]\nM a[172:175],a[116:119],a[76:79],a[172:175]\nbuffer_load_dwordx4 v[38:41],v24,s[68:71],0 offen offset:1024\n" \
"M a[176:179],a[120:123],a[72:75],a[176:179]\nM a[180:183],a[124:127],a[72:75],a[180:183]\nM a[184:187],a[120:123],a[76:79],a[184:187]\nM a[188:191],a[124:127],a[76:79],a[188:191]\nbuffer_load_dword v52,v24,s[68:71],0 offen offset:1040\nM v[64:67],a[32:35],a[24:27],v[64:67]\nds_read_b128 a[64:67],v6 offset:32768\nM v[68:71],a[36:39],a[24:27],v[68:71]\n" \
"M v[72:75],a[32:35],a[28:31],v[72:75]\nds_read_b64_tr_b16 a[96:97],v9 offset:49152\nM v[76:79],a[36:39],a[28:31],v[76:79]\nbuffer_load_dwordx4 v[42:45],v24,s[68:71],0 offen offset:2048\nM v[80:83],a[40:43],a[24:27],v[80:83]\nds_read_b64_tr_b16 a[98:99],v10 offset:49152\nM v[84:87],a[44:47],a[24:27],v[84:87]\nM v[88:91],a[40:43],a[28:31],v[88:91]\n" \
"ds_read_b128 a[68:71],v6 offset:34816\nM v[92:95],a[44:47],a[28:31],v[92:95]\nbuffer_load_dword v53,v24,s[68:71],0 offen offset:2064\nM v[96:99],a[48:51],a[24:27],v[96:99]\nds_read_b64_tr_b16 a[100:101],v11 offset:49152\nM v[100:103],a[52:55],a[24:27],v[100:103]\nM v[104:107],a[48:51],a[28:31],v[104:107]\nds_read_b64_tr_b16 a[102:103],v12 offset:49152\n" \
"M v[108:111],a[52:55],a[28:31],v[108:111]\nbuffer_load_dwordx4 v[46:49],v24,s[68:71],0 offen offset:3072\nM v[112:115],a[56:59],a[24:27],v[112:115]\nds_read_b128 a[72:75],v6 offset:36864\nM v[116:119],a[60:63],a[24:27],v[116:119]\nM v[120:123],a[56:59],a[28:31],v[120:123]\nds_read_b64_tr_b16 a[104:105],v9 offset:49664\nM v[124:127],a[60:63],a[28:31],v[124:127]\n" \
"buffer_load_dword v54,v24,s[68:71],0 offen offset:3088\nM v[128:131],a[32:35],a[16:19],v[128:131]\nds_read_b64_tr_b16 a[106:107],v10 offset:49664\nM v[132:135],a[36:39],a[16:19],v[132:135]\nM v[136:139],a[32:35],a[20:23],v[136:139]\nds_read_b128 a[76:79],v6 offset:38912\nM v[140:143],a[36:39],a[20:23],v[140:143]\nglobal_load_short_d16_hi v28,v29,s[12:13]\n" \
"M v[144:147],a[40:43],a[16:19],v[144:147]\nds_read_b64_tr_b16 a[108:109],v11 offset:49664\nM v[148:151],a[44:47],a[16:19],v[148:151]\nM v[152:155],a[40:43],a[20:23],v[152:155]\nLG2_%=:\ns_cbranch_execz LG6_%=\ns_mov_b32 s47,3670016\ns_add_u32 m0,s22,32768\n" \
"ds_read_b64_tr_b16 a[110:111],v12 offset:49664\nM v[156:159],a[44:47],a[20:23],v[156:159]\nbuffer_load_dwordx4 v21,s[32:35],s47 offen offset:0 lds\nM v[160:163],a[48:51],a[16:19],v[160:163]\nds_read_b128 a[80:83],v6 offset:40960\nM v[164:167],a[52:55],a[16:19],v[164:167]\nM v[168:171],a[48:51],a[20:23],v[168:171]\ns_mov_b32 s46,896\n" \
"s_add_u32 m0,s50,16384\nds_read_b64_tr_b16 a[112:113],v9 offset:50176\nM v[172:175],a[52:55],a[20:23],v[172:175]\nbuffer_load_dwordx4 v17,s[28:31],s46 offen lds\nM v[176:179],a[56:59],a[16:19],v[176:179]\nds_read_b64_tr_b16 a[114:115],v10 offset:50176\nM v[180:183],a[60:63],a[16:19],v[180:183]\nM v[184:187],a[56:59],a[20:23],v[184:187]\n" \
"s_add_u32 m0,s22,33664\nds_read_b128 a[84:87],v6 offset:43008\nM v[188:191],a[60:63],a[20:23],v[188:191]\nbuffer_load_dwordx4 v21,s[32:35],s47 offen offset:128 lds\nM v[192:195],a[32:35],a[0:3],v[192:195]\nds_read_b64_tr_b16 a[116:117],v11 offset:50176\nM v[196:199],a[36:39],a[0:3],v[196:199]\nM v[200:203],a[32:35],a[4:7],v[200:203]\n" \
"s_add_u32 m0,s50,20480\nds_read_b64_tr_b16 a[118:119],v12 offset:50176\nM v[204:207],a[36:39],a[4:7],v[204:207]\nbuffer_load_dwordx4 v18,s[28:31],s46 offen lds\nM v[208:211],a[40:43],a[0:3],v[208:211]\nds_read_b128 a[88:91],v6 offset:45056\nM v[212:215],a[44:47],a[0:3],v[212:215]\nM v[216:219],a[40:43],a[4:7],v[216:219]\n" \
"s_add_u32 m0,s22,34816\nds_read_b64_tr_b16 a[120:121],v9 offset:50688\nM v[220:223],a[44:47],a[4:7],v[220:223]\nbuffer_load_dwordx4 v21,s[92:95],s47 offen offset:0 lds\nM v[224:227],a[48:51],a[0:3],v[224:227]\nds_read_b64_tr_b16 a[122:123],v10 offset:50688\nM v[228:231],a[52:55],a[0:3],v[228:231]\nM v[232:235],a[48:51],a[4:7],v[232:235]\n" \
"s_add_u32 m0,s50,24576\nds_read_b128 a[92:95],v6 offset:47104\nM v[236:239],a[52:55],a[4:7],v[236:239]\nbuffer_load_dwordx4 v19,s[28:31],s46 offen lds\nM v[240:243],a[56:59],a[0:3],v[240:243]\nds_read_b64_tr_b16 a[124:125],v11 offset:50688\nM v[244:247],a[60:63],a[0:3],v[244:247]\nM v[248:251],a[56:59],a[4:7],v[248:251]\n" \
"s_add_u32 m0,s22,35712\nds_read_b64_tr_b16 a[126:127],v12 offset:50688\nM v[252:255],a[60:63],a[4:7],v[252:255]\nbuffer_load_dwordx4 v21,s[92:95],s47 offen offset:128 lds\nM a[128:131],a[32:35],a[8:11],a[128:131]\nM a[132:135],a[36:39],a[8:11],a[132:135]\nM a[136:139],a[32:35],a[12:15],a[136:139]\ns_add_u32 m0,s50,28672\n" \
"M a[140:143],a[36:39],a[12:15],a[140:143]\nbuffer_load_dwordx4 v20,s[28:31],s46 offen lds\nM a[144:147],a[40:43],a[8:11],a[144:147]\nM a[148:151],a[44:47],a[8:11],a[148:151]\nM a[152:155],a[40:43],a[12:15],a[152:155]\nM a[156:159],a[44:47],a[12:15],a[156:159]\ns_waitcnt vmcnt(29) lgkmcnt(0)\nM a[160:163],a[48:51],a[8:11],a[160:163]\n" \
"s_barrier\nM a[164:167],a[52:55],a[8:11],a[164:167]\nM a[168:171],a[48:51],a[12:15],a[168:171]\nM a[172:175],a[52:55],a[12:15],a[172:175]\nM a[176:179],a[56:59],a[8:11],a[176:179]\nM a[180:183],a[60:63],a[8:11],a[180:183]\nM a[184:187],a[56:59],a[12:15],a[184:187]\nM a[188:191],a[60:63],a[12:15],a[188:191]\n" \
"M v[64:67],a[96:99],a[88:91],v[64:67]\nds_read_b128 a[0:3],v7 offset:0\nM v[68:71],a[100:103],a[88:91],v[68:71]\ns_mov_b64 s[28:29],s[64:65]\nM v[72:75],a[96:99],a[92:95],v[72:75]\ns_mov_b32 s47,3932160\ns_add_u32 m0,s22,49152\nds_read_b64_tr_b16 a[32:33],v9 offset:0\n" \
"M v[76:79],a[100:103],a[92:95],v[76:79]\nbuffer_load_dwordx4 v21,s[32:35],s47 offen offset:0 lds\nM v[80:83],a[104:107],a[88:91],v[80:83]\nds_read_b64_tr_b16 a[34:35],v10 offset:0\nM v[84:87],a[108:111],a[88:91],v[84:87]\ns_and_b32 s29,s29,0xffff\nM v[88:91],a[104:107],a[92:95],v[88:91]\nds_read_b128 a[4:7],v7 offset:2048\n" \
"M v[92:95],a[108:111],a[92:95],v[92:95]\ns_mov_b32 s30,4194304\nM v[96:99],a[112:115],a[88:91],v[96:99]\ns_add_u32 m0,s22,50048\nds_read_b64_tr_b16 a[36:37],v11 offset:0\nM v[100:103],a[116:119],a[88:91],v[100:103]\nbuffer_load_dwordx4 v21,s[32:35],s47 offen offset:128 lds\nM v[104:107],a[112:115],a[92:95],v[104:107]\n" \
"ds_read_b64_tr_b16 a[38:39],v12 offset:0\nM v[108:111],a[116:119],a[92:95],v[108:111]\ns_lshl_b32 s60,s7,18\nM v[112:115],a[120:123],a[88:91],v[112:115]\nds_read_b128 a[8:11],v7 offset:4096\nM v[116:119],a[124:127],a[88:91],v[116:119]\ns_sub_u32 s61,3932160,s60\nM v[120:123],a[120:123],a[92:95],v[120:123]\n" \
"s_add_u32 m0,s22,51200\nds_read_b64_tr_b16 a[40:41],v9 offset:512\nM v[124:127],a[124:127],a[92:95],v[124:127]\nbuffer_load_dwordx4 v21,s[92:95],s47 offen offset:0 lds\nM v[128:131],a[96:99],a[80:83],v[128:131]\nds_read_b64_tr_b16 a[42:43],v10 offset:512\nM v[132:135],a[100:103],a[80:83],v[132:135]\nv_add_u32 v13,s60,v63\n" \
"M v[136:139],a[96:99],a[84:87],v[136:139]\nds_read_b128 a[12:15],v7 offset:6144\nM v[140:143],a[100:103],a[84:87],v[140:143]\nv_add_u32 v17,s61,v63\nM v[144:147],a[104:107],a[80:83],v[144:147]\ns_add_u32 m0,s22,52096\nds_read_b64_tr_b16 a[44:45],v11 offset:512\nM v[148:151],a[108:111],a[80:83],v[148:151]\n" \
"buffer_load_dwordx4 v21,s[92:95],s47 offen offset:128 lds\nM v[152:155],a[104:107],a[84:87],v[152:155]\nds_read_b64_tr_b16 a[46:47],v12 offset:512\nM v[156:159],a[108:111],a[84:87],v[156:159]\nv_add_u32 v14,0x10000,v13\nM v[160:163],a[112:115],a[80:83],v[160:163]\nds_read_b128 a[16:19],v7 offset:8192\nM v[164:167],a[116:119],a[80:83],v[164:167]\n" \
"v_add_u32 v18,0x10000,v17\nM v[168:171],a[112:115],a[84:87],v[168:171]\nds_read_b64_tr_b16 a[48:49],v9 offset:1024\nM v[172:175],a[116:119],a[84:87],v[172:175]\nM v[176:179],a[120:123],a[80:83],v[176:179]\nds_read_b64_tr_b16 a[50:51],v10 offset:1024\nM v[180:183],a[124:127],a[80:83],v[180:183]\nv_add_u32 v15,0x10000,v14\n" \
"M v[184:187],a[120:123],a[84:87],v[184:187]\nds_read_b128 a[20:23],v7 offset:10240\nM v[188:191],a[124:127],a[84:87],v[188:191]\nv_add_u32 v19,0x10000,v18\nM v[192:195],a[96:99],a[64:67],v[192:195]\nds_read_b64_tr_b16 a[52:53],v11 offset:1024\nM v[196:199],a[100:103],a[64:67],v[196:199]\nM v[200:203],a[96:99],a[68:71],v[200:203]\n" \
"ds_read_b64_tr_b16 a[54:55],v12 offset:1024\nM v[204:207],a[100:103],a[68:71],v[204:207]\nv_add_u32 v16,0x10000,v15\nM v[208:211],a[104:107],a[64:67],v[208:211]\nds_read_b128 a[24:27],v7 offset:12288\nM v[212:215],a[108:111],a[64:67],v[212:215]\nv_add_u32 v20,0x10000,v19\nM v[216:219],a[104:107],a[68:71],v[216:219]\n" \
"ds_read_b64_tr_b16 a[56:57],v9 offset:1536\nM v[220:223],a[108:111],a[68:71],v[220:223]\nM v[224:227],a[112:115],a[64:67],v[224:227]\nds_read_b64_tr_b16 a[58:59],v10 offset:1536\nM v[228:231],a[116:119],a[64:67],v[228:231]\nM v[232:235],a[112:115],a[68:71],v[232:235]\nds_read_b128 a[28:31],v7 offset:14336\nM v[236:239],a[116:119],a[68:71],v[236:239]\n" \
"M v[240:243],a[120:123],a[64:67],v[240:243]\nds_read_b64_tr_b16 a[60:61],v11 offset:1536\nM v[244:247],a[124:127],a[64:67],v[244:247]\nM v[248:251],a[120:123],a[68:71],v[248:251]\nds_read_b64_tr_b16 a[62:63],v12 offset:1536\nM v[252:255],a[124:127],a[68:71],v[252:255]\nM a[128:131],a[96:99],a[72:75],a[128:131]\nM a[132:135],a[100:103],a[72:75],a[132:135]\n" \
"M a[136:139],a[96:99],a[76:79],a[136:139]\nM a[140:143],a[100:103],a[76:79],a[140:143]\nM a[144:147],a[104:107],a[72:75],a[144:147]\nM a[148:151],a[108:111],a[72:75],a[148:151]\nM a[152:155],a[104:107],a[76:79],a[152:155]\nM a[156:159],a[108:111],a[76:79],a[156:159]\ns_waitcnt vmcnt(24) lgkmcnt(0)\nM a[160:163],a[112:115],a[72:75],a[160:163]\n" \
"s_barrier\nM a[164:167],a[116:119],a[72:75],a[164:167]\nM a[168:171],a[112:115],a[76:79],a[168:171]\nM a[172:175],a[116:119],a[76:79],a[172:175]\nM a[176:179],a[120:123],a[72:75],a[176:179]\nM a[180:183],a[124:127],a[72:75],a[180:183]\nM a[184:187],a[120:123],a[76:79],a[184:187]\nM a[188:191],a[124:127],a[76:79],a[188:191]\n" \
"M v[64:67],a[32:35],a[24:27],v[64:67]\nds_read_b128 a[64:67],v8 offset:0\nM v[68:71],a[36:39],a[24:27],v[68:71]\ns_mov_b64 s[32:33],s[44:45]\nM v[72:75],a[32:35],a[28:31],v[72:75]\nds_read_b64_tr_b16 a[96:97],v9 offset:16384\nM v[76:79],a[36:39],a[28:31],v[76:79]\nbuffer_load_dwordx2 v[58:59],v57,s[36:39],0 offen sc1\n" \
"M v[80:83],a[40:43],a[24:27],v[80:83]\nds_read_b64_tr_b16 a[98:99],v10 offset:16384\nM v[84:87],a[44:47],a[24:27],v[84:87]\ns_mov_b32 s34,s49\nM v[88:91],a[40:43],a[28:31],v[88:91]\nds_read_b128 a[68:71],v8 offset:2048\nM v[92:95],a[44:47],a[28:31],v[92:95]\nM v[96:99],a[48:51],a[24:27],v[96:99]\n" \
"ds_read_b64_tr_b16 a[100:101],v11 offset:16384\nM v[100:103],a[52:55],a[24:27],v[100:103]\ns_mov_b64 exec,0xff\nglobal_load_dword v0,v2,s[100:101]\ns_mov_b64 exec,-1\nM v[104:107],a[48:51],a[28:31],v[104:107]\nds_read_b64_tr_b16 a[102:103],v12 offset:16384\nM v[108:111],a[52:55],a[28:31],v[108:111]\n" \
"M v[112:115],a[56:59],a[24:27],v[112:115]\nds_read_b128 a[72:75],v8 offset:4096\nM v[116:119],a[60:63],a[24:27],v[116:119]\nM v[120:123],a[56:59],a[28:31],v[120:123]\nds_read_b64_tr_b16 a[104:105],v9 offset:16896\nM v[124:127],a[60:63],a[28:31],v[124:127]\ns_mov_b64 exec,0xffff\nglobal_load_dword v0,v1,s[76:77]\n" \
"s_mov_b64 exec,-1\nM v[128:131],a[32:35],a[16:19],v[128:131]\nds_read_b64_tr_b16 a[106:107],v10 offset:16896\nM v[132:135],a[36:39],a[16:19],v[132:135]\nM v[136:139],a[32:35],a[20:23],v[136:139]\nds_read_b128 a[76:79],v8 offset:6144\nM v[140:143],a[36:39],a[20:23],v[140:143]\nM v[144:147],a[40:43],a[16:19],v[144:147]\n" \
"ds_read_b64_tr_b16 a[108:109],v11 offset:16896\nM v[148:151],a[44:47],a[16:19],v[148:151]\nM v[152:155],a[40:43],a[20:23],v[152:155]\nds_read_b64_tr_b16 a[110:111],v12 offset:16896\nM v[156:159],a[44:47],a[20:23],v[156:159]\nM v[160:163],a[48:51],a[16:19],v[160:163]\nds_read_b128 a[80:83],v8 offset:8192\nM v[164:167],a[52:55],a[16:19],v[164:167]\n" \
"M v[168:171],a[48:51],a[20:23],v[168:171]\nds_read_b64_tr_b16 a[112:113],v9 offset:17408\nM v[172:175],a[52:55],a[20:23],v[172:175]\nM v[176:179],a[56:59],a[16:19],v[176:179]\nds_read_b64_tr_b16 a[114:115],v10 offset:17408\nM v[180:183],a[60:63],a[16:19],v[180:183]\nM v[184:187],a[56:59],a[20:23],v[184:187]\nds_read_b128 a[84:87],v8 offset:10240\n" \
"M v[188:191],a[60:63],a[20:23],v[188:191]\nM v[192:195],a[32:35],a[0:3],v[192:195]\nds_read_b64_tr_b16 a[116:117],v11 offset:17408\nM v[196:199],a[36:39],a[0:3],v[196:199]\nM v[200:203],a[32:35],a[4:7],v[200:203]\nds_read_b64_tr_b16 a[118:119],v12 offset:17408\nM v[204:207],a[36:39],a[4:7],v[204:207]\nM v[208:211],a[40:43],a[0:3],v[208:211]\n" \
"ds_read_b128 a[88:91],v8 offset:12288\nM v[212:215],a[44:47],a[0:3],v[212:215]\nM v[216:219],a[40:43],a[4:7],v[216:219]\nds_read_b64_tr_b16 a[120:121],v9 offset:17920\nM v[220:223],a[44:47],a[4:7],v[220:223]\nM v[224:227],a[48:51],a[0:3],v[224:227]\nds_read_b64_tr_b16 a[122:123],v10 offset:17920\nM v[228:231],a[52:55],a[0:3],v[228:231]\n" \
"M v[232:235],a[48:51],a[4:7],v[232:235]\nds_read_b128 a[92:95],v8 offset:14336\nM v[236:239],a[52:55],a[4:7],v[236:239]\nM v[240:243],a[56:59],a[0:3],v[240:243]\nds_read_b64_tr_b16 a[124:125],v11 offset:17920\nM v[244:247],a[60:63],a[0:3],v[244:247]\nM v[248:251],a[56:59],a[4:7],v[248:251]\nds_read_b64_tr_b16 a[126:127],v12 offset:17920\n" \
"M v[252:255],a[60:63],a[4:7],v[252:255]\nM a[128:131],a[32:35],a[8:11],a[128:131]\nM a[132:135],a[36:39],a[8:11],a[132:135]\nM a[136:139],a[32:35],a[12:15],a[136:139]\nM a[140:143],a[36:39],a[12:15],a[140:143]\nM a[144:147],a[40:43],a[8:11],a[144:147]\nM a[148:151],a[44:47],a[8:11],a[148:151]\nM a[152:155],a[40:43],a[12:15],a[152:155]\n" \
"M a[156:159],a[44:47],a[12:15],a[156:159]\ns_waitcnt vmcnt(2) lgkmcnt(0)\nM a[160:163],a[48:51],a[8:11],a[160:163]\ns_barrier\nM a[164:167],a[52:55],a[8:11],a[164:167]\nv_cmp_gt_u64 vcc,s[14:15],v[58:59]\nM a[168:171],a[48:51],a[12:15],a[168:171]\nv_cmp_le_u64 s[18:19],s[16:17],v[58:59]\n" \
"M a[172:175],a[52:55],a[12:15],a[172:175]\ns_or_b64 vcc,vcc,s[18:19]\ns_cbranch_scc1 LPUK_%=\nLPUKR_%=:\nM a[176:179],a[56:59],a[8:11],a[176:179]\nM a[180:183],a[60:63],a[8:11],a[180:183]\nM a[184:187],a[56:59],a[12:15],a[184:187]\nM a[188:191],a[60:63],a[12:15],a[188:191]\n" \
"M v[64:67],a[96:99],a[88:91],v[64:67]\nds_read_b128 a[0:3],v5 offset:0\nM v[68:71],a[100:103],a[88:91],v[68:71]\nM v[72:75],a[96:99],a[92:95],v[72:75]\ns_add_u32 m0,s56,10304\nds_read_b64_tr_b16 a[32:33],v9 offset:32768\nM v[76:79],a[100:103],a[92:95],v[76:79]\nbuffer_load_dwordx4 v26,s[72:75],0 offen offset:0 sc1 lds\n" \
"M v[80:83],a[104:107],a[88:91],v[80:83]\nds_read_b64_tr_b16 a[34:35],v10 offset:32768\nM v[84:87],a[108:111],a[88:91],v[84:87]\nM v[88:91],a[104:107],a[92:95],v[88:91]\ns_add_u32 m0,s56,11328\nds_read_b128 a[4:7],v5 offset:2048\nM v[92:95],a[108:111],a[92:95],v[92:95]\nbuffer_load_dwordx4 v27,s[72:75],0 offen offset:0 sc1 lds\n" \
"M v[96:99],a[112:115],a[88:91],v[96:99]\nds_read_b64_tr_b16 a[36:37],v11 offset:32768\nM v[100:103],a[116:119],a[88:91],v[100:103]\nM v[104:107],a[112:115],a[92:95],v[104:107]\ns_add_u32 m0,s56,11328\nds_read_b64_tr_b16 a[38:39],v12 offset:32768\nM v[108:111],a[116:119],a[92:95],v[108:111]\nbuffer_load_dwordx4 v27,s[72:75],0 offen offset:1024 sc1 lds\n" \
"M v[112:115],a[120:123],a[88:91],v[112:115]\nds_read_b128 a[8:11],v5 offset:4096\nM v[116:119],a[124:127],a[88:91],v[116:119]\nM v[120:123],a[120:123],a[92:95],v[120:123]\ns_add_u32 m0,s56,11328\nds_read_b64_tr_b16 a[40:41],v9 offset:33280\nM v[124:127],a[124:127],a[92:95],v[124:127]\nbuffer_load_dwordx4 v27,s[72:75],0 offen offset:2048 sc1 lds\n" \
"M v[128:131],a[96:99],a[80:83],v[128:131]\nds_read_b64_tr_b16 a[42:43],v10 offset:33280\nM v[132:135],a[100:103],a[80:83],v[132:135]\nM v[136:139],a[96:99],a[84:87],v[136:139]\ns_add_u32 m0,s56,11328\nds_read_b128 a[12:15],v5 offset:6144\nM v[140:143],a[100:103],a[84:87],v[140:143]\nbuffer_load_dwordx4 v27,s[72:75],0 offen offset:3072 sc1 lds\n" \
"M v[144:147],a[104:107],a[80:83],v[144:147]\nds_read_b64_tr_b16 a[44:45],v11 offset:33280\ns_waitcnt vmcnt(20)\nv_alignbyte_b32 v30,v31,v30,2\nM v[148:151],a[108:111],a[80:83],v[148:151]\nv_alignbyte_b32 v31,v32,v31,2\nv_alignbyte_b32 v32,v33,v32,2\nM v[152:155],a[104:107],a[84:87],v[152:155]\n" \
"s_add_u32 m0,s56,0\nds_read_b64_tr_b16 a[46:47],v12 offset:33280\nv_alignbyte_b32 v33,v50,v33,2\nM v[156:159],a[108:111],a[84:87],v[156:159]\nbuffer_load_dwordx4 v23,s[68:71],0 offen offset:0 lds\nM v[160:163],a[112:115],a[80:83],v[160:163]\nds_read_b128 a[16:19],v5 offset:8192\nM v[164:167],a[116:119],a[80:83],v[164:167]\n" \
"ds_write_b128 v55,v[30:33] offset:5184\nv_alignbyte_b32 v34,v35,v34,2\nM v[168:171],a[112:115],a[84:87],v[168:171]\ns_add_u32 m0,s56,1024\nds_read_b64_tr_b16 a[48:49],v9 offset:33792\nv_alignbyte_b32 v35,v36,v35,2\nM v[172:175],a[116:119],a[84:87],v[172:175]\nbuffer_load_dwordx4 v24,s[68:71],0 offen offset:0 lds\n" \
"M v[176:179],a[120:123],a[80:83],v[176:179]\nds_read_b64_tr_b16 a[50:51],v10 offset:33792\nv_alignbyte_b32 v36,v37,v36,2\nM v[180:183],a[124:127],a[80:83],v[180:183]\nv_alignbyte_b32 v37,v51,v37,2\nds_write_b128 v55,v[34:37] offset:6208\nM v[184:187],a[120:123],a[84:87],v[184:187]\ns_add_u32 m0,s56,1024\n" \
"ds_read_b128 a[20:23],v5 offset:10240\nv_alignbyte_b32 v38,v39,v38,2\nM v[188:191],a[124:127],a[84:87],v[188:191]\nbuffer_load_dwordx4 v24,s[68:71],0 offen offset:1024 lds\nM v[192:195],a[96:99],a[64:67],v[192:195]\nds_read_b64_tr_b16 a[52:53],v11 offset:33792\nv_alignbyte_b32 v39,v40,v39,2\nM v[196:199],a[100:103],a[64:67],v[196:199]\n" \
"v_alignbyte_b32 v40,v41,v40,2\nv_alignbyte_b32 v41,v52,v41,2\nds_write_b128 v55,v[38:41] offset:7232\nM v[200:203],a[96:99],a[68:71],v[200:203]\ns_add_u32 m0,s56,1024\nds_read_b64_tr_b16 a[54:55],v12 offset:33792\nv_alignbyte_b32 v42,v43,v42,2\nM v[204:207],a[100:103],a[68:71],v[204:207]\n" \
"buffer_load_dwordx4 v24,s[68:71],0 offen offset:2048 lds\nM v[208:211],a[104:107],a[64:67],v[208:211]\nds_read_b128 a[24:27],v5 offset:12288\nv_alignbyte_b32 v43,v44,v43,2\nM v[212:215],a[108:111],a[64:67],v[212:215]\nv_alignbyte_b32 v44,v45,v44,2\nv_alignbyte_b32 v45,v53,v45,2\nds_write_b128 v55,v[42:45] offset:8256\n" \
"M v[216:219],a[104:107],a[68:71],v[216:219]\ns_add_u32 m0,s56,1024\nds_read_b64_tr_b16 a[56:57],v9 offset:34304\nv_alignbyte_b32 v46,v47,v46,2\nM v[220:223],a[108:111],a[68:71],v[220:223]\nbuffer_load_dwordx4 v24,s[68:71],0 offen offset:3072 lds\nM v[224:227],a[112:115],a[64:67],v[224:227]\nds_read_b64_tr_b16 a[58:59],v10 offset:34304\n" \
"v_alignbyte_b32 v47,v48,v47,2\nM v[228:231],a[116:119],a[64:67],v[228:231]\nv_alignbyte_b32 v48,v49,v48,2\nv_alignbyte_b32 v49,v54,v49,2\nds_write_b128 v55,v[46:49] offset:9280\nM v[232:235],a[112:115],a[68:71],v[232:235]\ns_mov_b32 s46,0\ns_add_u32 m0,s50,65536\n" \
"ds_read_b128 a[28:31],v5 offset:14336\nM v[236:239],a[116:119],a[68:71],v[236:239]\nbuffer_load_dwordx4 v13,s[28:31],s46 offen lds\nM v[240:243],a[120:123],a[64:67],v[240:243]\nds_read_b64_tr_b16 a[60:61],v11 offset:34304\nv_lshrrev_b32 v30,4,v4\nM v[244:247],a[124:127],a[64:67],v[244:247]\nv_and_b32 v31,15,v30\n" \
"v_lshrrev_b32 v32,4,v30\nM v[248:251],a[120:123],a[68:71],v[248:251]\ns_add_u32 m0,s50,69632\nds_read_b64_tr_b16 a[62:63],v12 offset:34304\nv_add_u32 v33,1,v31\nM v[252:255],a[124:127],a[68:71],v[252:255]\nbuffer_load_dwordx4 v14,s[28:31],s46 offen lds\nM a[128:131],a[96:99],a[72:75],a[128:131]\n" \
"v_and_b32 v34,1,v33\nv_and_b32 v33,30,v33\nM a[132:135],a[100:103],a[72:75],a[132:135]\nv_mul_u32_u24 v34,5184,v34\nv_lshl_add_u32 v33,v33,1,v34\nM a[136:139],a[96:99],a[76:79],a[136:139]\ns_add_u32 m0,s50,73728\nv_lshl_add_u32 v33,v32,4,v33\n" \
"M a[140:143],a[100:103],a[76:79],a[140:143]\nbuffer_load_dwordx4 v15,s[28:31],s46 offen lds\nM a[144:147],a[104:107],a[72:75],a[144:147]\nv_add_u32 v46,s56,v33\nv_add_u32 v47,1024,v46\nM a[148:151],a[108:111],a[72:75],a[148:151]\nv_add_u32 v48,2048,v46\nv_add_u32 v49,3072,v46\n" \
"M a[152:155],a[104:107],a[76:79],a[152:155]\nv_lshlrev_b32 v33,1,v31\nv_sub_u32 v33,v33,v32\nM a[156:159],a[108:111],a[76:79],a[156:159]\ns_waitcnt vmcnt(16) lgkmcnt(0)\nv_lshlrev_b32 v33,4,v33\nM a[160:163],a[112:115],a[72:75],a[160:163]\ns_barrier\n" \
"M a[164:167],a[116:119],a[72:75],a[164:167]\nv_add_u32 v33,10320,v33\nv_add_u32 v50,s56,v33\nM a[168:171],a[112:115],a[76:79],a[168:171]\ns_add_u32 m0,s50,77824\nv_bfe_u32 v33,v4,6,3\nM a[172:175],a[116:119],a[76:79],a[172:175]\nbuffer_load_dwordx4 v16,s[28:31],s46 offen lds\n" \
"M a[176:179],a[120:123],a[72:75],a[176:179]\nv_lshlrev_b32 v60,17,v33\nv_and_b32 v33,16,v4\nM a[180:183],a[124:127],a[72:75],a[180:183]\nv_lshl_add_u32 v60,v33,2,v60\nv_and_b32 v33,32,v4\nM a[184:187],a[120:123],a[76:79],a[184:187]\nv_add_u32 v60,v60,v33\n" \
"v_lshrrev_b32 v33,9,v4\nM a[188:191],a[124:127],a[76:79],a[188:191]\nv_lshl_add_u32 v60,v33,4,v60\nv_lshl_add_u32 v33,v32,3,v31\nM v[64:67],a[32:35],a[24:27],v[64:67]\nds_read_b128 a[64:67],v6 offset:0\nv_sub_u32 v33,15,v33\nM v[68:71],a[36:39],a[24:27],v[68:71]\n" \
"v_and_b32 v34,16,v4\nv_lshrrev_b32 v34,v34,v28\nM v[72:75],a[32:35],a[28:31],v[72:75]\ns_mov_b32 s46,0\ns_add_u32 m0,s50,81920\nds_read_b64_tr_b16 a[96:97],v9 offset:49152\nM v[76:79],a[36:39],a[28:31],v[76:79]\nbuffer_load_dwordx4 v17,s[28:31],s46 offen lds\n" \
"M v[80:83],a[40:43],a[24:27],v[80:83]\nds_read_b64_tr_b16 a[98:99],v10 offset:49152\nv_lshrrev_b32 v33,1,v33\nM v[84:87],a[44:47],a[24:27],v[84:87]\nv_cmp_eq_u32 vcc,0,v33\ns_nop 1\nM v[88:91],a[40:43],a[28:31],v[88:91]\nds_read_b128 a[68:71],v6 offset:2048\n" \
"v_cndmask_b32 v52,0,v34,vcc\nM v[92:95],a[44:47],a[28:31],v[92:95]\nv_cmp_eq_u32 vcc,1,v33\ns_nop 1\nM v[96:99],a[48:51],a[24:27],v[96:99]\ns_add_u32 m0,s50,86016\nds_read_b64_tr_b16 a[100:101],v11 offset:49152\nv_cndmask_b32 v53,0,v34,vcc\n" \
"M v[100:103],a[52:55],a[24:27],v[100:103]\nbuffer_load_dwordx4 v18,s[28:31],s46 offen lds\nM v[104:107],a[48:51],a[28:31],v[104:107]\nds_read_b64_tr_b16 a[102:103],v12 offset:49152\nv_cmp_eq_u32 vcc,2,v33\ns_nop 1\nM v[108:111],a[52:55],a[28:31],v[108:111]\nv_cndmask_b32 v54,0,v34,vcc\n" \
"v_cmp_eq_u32 vcc,3,v33\ns_nop 1\nM v[112:115],a[56:59],a[24:27],v[112:115]\nds_read_b128 a[72:75],v6 offset:4096\nv_cndmask_b32 v55,0,v34,vcc\nM v[116:119],a[60:63],a[24:27],v[116:119]\nM v[120:123],a[56:59],a[28:31],v[120:123]\ns_add_u32 m0,s50,90112\n" \
"ds_read_b64_tr_b16 a[104:105],v9 offset:49664\nM v[124:127],a[60:63],a[28:31],v[124:127]\nbuffer_load_dwordx4 v19,s[28:31],s46 offen lds\nM v[128:131],a[32:35],a[16:19],v[128:131]\nds_read_b64_tr_b16 a[106:107],v10 offset:49664\nM v[132:135],a[36:39],a[16:19],v[132:135]\nM v[136:139],a[32:35],a[20:23],v[136:139]\nds_read_b128 a[76:79],v6 offset:6144\n" \
"M v[140:143],a[36:39],a[20:23],v[140:143]\nM v[144:147],a[40:43],a[16:19],v[144:147]\ns_add_u32 m0,s50,94208\nds_read_b64_tr_b16 a[108:109],v11 offset:49664\nM v[148:151],a[44:47],a[16:19],v[148:151]\nbuffer_load_dwordx4 v20,s[28:31],s46 offen lds\nM v[152:155],a[40:43],a[20:23],v[152:155]\nds_read_b64_tr_b16 a[110:111],v12 offset:49664\n" \
"M v[156:159],a[44:47],a[20:23],v[156:159]\nM v[160:163],a[48:51],a[16:19],v[160:163]\nds_read_b128 a[80:83],v6 offset:8192\nM v[164:167],a[52:55],a[16:19],v[164:167]\nM v[168:171],a[48:51],a[20:23],v[168:171]\ns_mov_b32 s47,0\ns_add_u32 m0,s51,32768\nds_read_b64_tr_b16 a[112:113],v9 offset:50176\n" \
"M v[172:175],a[52:55],a[20:23],v[172:175]\nbuffer_load_dwordx4 v21,s[32:35],s47 offen offset:0 lds\nM v[176:179],a[56:59],a[16:19],v[176:179]\nds_read_b64_tr_b16 a[114:115],v10 offset:50176\nM v[180:183],a[60:63],a[16:19],v[180:183]\nM v[184:187],a[56:59],a[20:23],v[184:187]\nds_read_b128 a[84:87],v6 offset:10240\nM v[188:191],a[60:63],a[20:23],v[188:191]\n" \
"M v[192:195],a[32:35],a[0:3],v[192:195]\ns_add_u32 m0,s51,33664\nds_read_b64_tr_b16 a[116:117],v11 offset:50176\nM v[196:199],a[36:39],a[0:3],v[196:199]\nLG3_%=:\ns_cbranch_execz LG7_%=\nbuffer_load_dwordx4 v21,s[32:35],s47 offen offset:128 lds\nM v[200:203],a[32:35],a[4:7],v[200:203]\n" \
"ds_read_b64_tr_b16 a[118:119],v12 offset:50176\nM v[204:207],a[36:39],a[4:7],v[204:207]\nM v[208:211],a[40:43],a[0:3],v[208:211]\nds_read_b128 a[88:91],v6 offset:12288\nM v[212:215],a[44:47],a[0:3],v[212:215]\nM v[216:219],a[40:43],a[4:7],v[216:219]\ns_mov_b32 s47,262144\ns_add_u32 m0,s51,40960\n" \
"ds_read_b64_tr_b16 a[120:121],v9 offset:50688\nM v[220:223],a[44:47],a[4:7],v[220:223]\nbuffer_load_dwordx4 v21,s[32:35],s47 offen offset:0 lds\nM v[224:227],a[48:51],a[0:3],v[224:227]\nds_read_b64_tr_b16 a[122:123],v10 offset:50688\nM v[228:231],a[52:55],a[0:3],v[228:231]\nM v[232:235],a[48:51],a[4:7],v[232:235]\nds_read_b128 a[92:95],v6 offset:14336\n" \
"M v[236:239],a[52:55],a[4:7],v[236:239]\nM v[240:243],a[56:59],a[0:3],v[240:243]\ns_add_u32 m0,s51,41856\nds_read_b64_tr_b16 a[124:125],v11 offset:50688\nM v[244:247],a[60:63],a[0:3],v[244:247]\nbuffer_load_dwordx4 v21,s[32:35],s47 offen offset:128 lds\nM v[248:251],a[56:59],a[4:7],v[248:251]\nds_read_b64_tr_b16 a[126:127],v12 offset:50688\n" \
"M v[252:255],a[60:63],a[4:7],v[252:255]\nv_add_u32 v5,s54,v5\nM a[128:131],a[32:35],a[8:11],a[128:131]\ns_add_u32 s76,s76,2097152\ns_addc_u32 s77,s77,0\nv_add_u32 v6,s54,v6\nM a[132:135],a[36:39],a[8:11],a[132:135]\ns_mov_b64 exec,0xffff\n" \
"global_load_dword v0,v1,s[76:77]\ns_mov_b64 exec,-1\nM a[136:139],a[32:35],a[12:15],a[136:139]\nv_add_u32 v7,s54,v7\nM a[140:143],a[36:39],a[12:15],a[140:143]\nv_add_u32 v8,s54,v8\nM a[144:147],a[40:43],a[8:11],a[144:147]\nv_sub_u32 v9,v9,v62\n" \
"M a[148:151],a[44:47],a[8:11],a[148:151]\nM a[152:155],a[40:43],a[12:15],a[152:155]\nv_sub_u32 v10,v10,v62\nM a[156:159],a[44:47],a[12:15],a[156:159]\ns_waitcnt lgkmcnt(0)\nM a[160:163],a[48:51],a[8:11],a[160:163]\ns_barrier\nM a[164:167],a[52:55],a[8:11],a[164:167]\n" \
"v_sub_u32 v11,v11,v62\nM a[168:171],a[48:51],a[12:15],a[168:171]\nv_sub_u32 v12,v12,v62\nM a[172:175],a[52:55],a[12:15],a[172:175]\nM a[176:179],a[56:59],a[8:11],a[176:179]\nM a[180:183],a[60:63],a[8:11],a[180:183]\nM a[184:187],a[56:59],a[12:15],a[184:187]\nM a[188:191],a[60:63],a[12:15],a[188:191]\n" \
"M v[64:67],a[96:99],a[88:91],v[64:67]\ns_waitcnt vmcnt(13) lgkmcnt(0)\nM v[68:71],a[100:103],a[88:91],v[68:71]\nM v[72:75],a[96:99],a[92:95],v[72:75]\ns_mov_b32 s46,128\ns_add_u32 m0,s50,0\nds_read_b128 a[32:35],v50 offset:512\nM v[76:79],a[100:103],a[92:95],v[76:79]\n" \
"buffer_load_dwordx4 v13,s[28:31],s46 offen lds\ns_add_u32 s96,s57,49152\nM v[80:83],a[104:107],a[88:91],v[80:83]\nds_read2_b32 a[0:1],v46 offset0:0 offset1:1\nv_mul_f32 v64,s20,v64\nM v[84:87],a[108:111],a[88:91],v[84:87]\nv_mul_f32 v65,s20,v65\nv_mul_f32 v66,s20,v66\n" \
"M v[88:91],a[104:107],a[92:95],v[88:91]\ns_add_u32 m0,s50,4096\nds_read2_b32 a[2:3],v46 offset0:2 offset1:3\nv_mul_f32 v67,s20,v67\nM v[92:95],a[108:111],a[92:95],v[92:95]\nbuffer_load_dwordx4 v14,s[28:31],s46 offen lds\nM v[96:99],a[112:115],a[88:91],v[96:99]\nds_read_b128 a[36:39],v50 offset:1024\n" \
"C v30,v64,v65\nM v[100:103],a[116:119],a[88:91],v[100:103]\nC v31,v66,v67\nv_mul_f32 v68,s20,v68\nM v[104:107],a[112:115],a[92:95],v[104:107]\ns_add_u32 m0,s50,8192\nds_read2_b32 a[4:5],v46 offset0:128 offset1:129\nv_mul_f32 v69,s20,v69\n" \
"M v[108:111],a[116:119],a[92:95],v[108:111]\nbuffer_load_dwordx4 v15,s[28:31],s46 offen lds\nM v[112:115],a[120:123],a[88:91],v[112:115]\nds_read2_b32 a[6:7],v46 offset0:130 offset1:131\nv_mul_f32 v70,s20,v70\nM v[116:119],a[124:127],a[88:91],v[116:119]\nv_mul_f32 v71,s20,v71\nC v32,v68,v69\n" \
"M v[120:123],a[120:123],a[92:95],v[120:123]\ns_add_u32 m0,s50,12288\nds_read_b128 a[40:43],v50 offset:1536\nC v33,v70,v71\nM v[124:127],a[124:127],a[92:95],v[124:127]\nbuffer_load_dwordx4 v16,s[28:31],s46 offen lds\nM v[128:131],a[96:99],a[80:83],v[128:131]\nds_read2_b32 a[8:9],v47 offset0:0 offset1:1\n" \
"v_accvgpr_write_b32 a192,v30\nM v[132:135],a[100:103],a[80:83],v[132:135]\nv_accvgpr_write_b32 a193,v31\nv_accvgpr_write_b32 a194,v32\nM v[136:139],a[96:99],a[84:87],v[136:139]\ns_mov_b32 s46,128\ns_add_u32 m0,s50,16384\nds_read2_b32 a[10:11],v47 offset0:2 offset1:3\n" \
"M v[140:143],a[100:103],a[84:87],v[140:143]\nbuffer_load_dwordx4 v17,s[28:31],s46 offen lds\nM v[144:147],a[104:107],a[80:83],v[144:147]\nds_read_b128 a[44:47],v50 offset:2048\nv_accvgpr_write_b32 a195,v33\nM v[148:151],a[108:111],a[80:83],v[148:151]\nv_mul_f32 v72,s20,v72\nv_mul_f32 v73,s20,v73\n" \
"M v[152:155],a[104:107],a[84:87],v[152:155]\ns_add_u32 m0,s50,20480\nds_read2_b32 a[12:13],v47 offset0:128 offset1:129\nv_mul_f32 v74,s20,v74\nM v[156:159],a[108:111],a[84:87],v[156:159]\nbuffer_load_dwordx4 v18,s[28:31],s46 offen lds\nM v[160:163],a[112:115],a[80:83],v[160:163]\nds_read2_b32 a[14:15],v47 offset0:130 offset1:131\n" \
"v_mul_f32 v75,s20,v75\nM v[164:167],a[116:119],a[80:83],v[164:167]\nC v34,v72,v73\nC v35,v74,v75\nM v[168:171],a[112:115],a[84:87],v[168:171]\ns_add_u32 m0,s50,24576\nds_read_b128 a[48:51],v50 offset:2560\nv_mul_f32 v76,s20,v76\n" \
"M v[172:175],a[116:119],a[84:87],v[172:175]\nbuffer_load_dwordx4 v19,s[28:31],s46 offen lds\nM v[176:179],a[120:123],a[80:83],v[176:179]\nds_read2_b32 a[16:17],v48 offset0:0 offset1:1\nv_mul_f32 v77,s20,v77\nM v[180:183],a[124:127],a[80:83],v[180:183]\nv_mul_f32 v78,s20,v78\nv_mul_f32 v79,s20,v79\n" \
"M v[184:187],a[120:123],a[84:87],v[184:187]\ns_add_u32 m0,s50,28672\nds_read2_b32 a[18:19],v48 offset0:2 offset1:3\nC v36,v76,v77\nM v[188:191],a[124:127],a[84:87],v[188:191]\nbuffer_load_dwordx4 v20,s[28:31],s46 offen lds\nM v[192:195],a[96:99],a[64:67],v[192:195]\nds_read_b128 a[52:55],v50 offset:3072\n" \
"C v37,v78,v79\nM v[196:199],a[100:103],a[64:67],v[196:199]\nv_accvgpr_write_b32 a196,v34\nv_accvgpr_write_b32 a197,v35\nM v[200:203],a[96:99],a[68:71],v[200:203]\ns_mov_b32 s47,524288\ns_add_u32 m0,s51,49152\nds_read2_b32 a[20:21],v48 offset0:128 offset1:129\n" \
"M v[204:207],a[100:103],a[68:71],v[204:207]\nbuffer_load_dwordx4 v21,s[32:35],s47 offen offset:0 lds\nM v[208:211],a[104:107],a[64:67],v[208:211]\nds_read2_b32 a[22:23],v48 offset0:130 offset1:131\nv_accvgpr_write_b32 a198,v36\nM v[212:215],a[108:111],a[64:67],v[212:215]\nv_accvgpr_write_b32 a199,v37\nv_mul_f32 v80,s20,v80\n" \
"M v[216:219],a[104:107],a[68:71],v[216:219]\ns_add_u32 m0,s51,50048\nds_read_b128 a[56:59],v50 offset:3584\nv_mul_f32 v81,s20,v81\nM v[220:223],a[108:111],a[68:71],v[220:223]\nbuffer_load_dwordx4 v21,s[32:35],s47 offen offset:128 lds\nM v[224:227],a[112:115],a[64:67],v[224:227]\nds_read2_b32 a[24:25],v49 offset0:0 offset1:1\n" \
"v_mul_f32 v82,s20,v82\nM v[228:231],a[116:119],a[64:67],v[228:231]\nv_mul_f32 v83,s20,v83\nC v30,v80,v81\nM v[232:235],a[112:115],a[68:71],v[232:235]\ns_mov_b32 s47,786432\ns_add_u32 m0,s51,57344\nds_read2_b32 a[26:27],v49 offset0:2 offset1:3\n" \
"M v[236:239],a[116:119],a[68:71],v[236:239]\nbuffer_load_dwordx4 v21,s[32:35],s47 offen offset:0 lds\nM v[240:243],a[120:123],a[64:67],v[240:243]\nds_read_b128 a[60:63],v50 offset:4096\nC v31,v82,v83\nM v[244:247],a[124:127],a[64:67],v[244:247]\nv_mul_f32 v84,s20,v84\nv_mul_f32 v85,s20,v85\n" \
"M v[248:251],a[120:123],a[68:71],v[248:251]\ns_add_u32 m0,s51,58240\nds_read2_b32 a[28:29],v49 offset0:128 offset1:129\nv_mul_f32 v86,s20,v86\nM v[252:255],a[124:127],a[68:71],v[252:255]\nbuffer_load_dwordx4 v21,s[32:35],s47 offen offset:128 lds\nM a[128:131],a[96:99],a[72:75],a[128:131]\nds_read2_b32 a[30:31],v49 offset0:130 offset1:131\n" \
"v_mul_f32 v87,s20,v87\nM a[132:135],a[100:103],a[72:75],a[132:135]\nC v32,v84,v85\nC v33,v86,v87\nM a[136:139],a[96:99],a[76:79],a[136:139]\ns_add_u32 s76,s76,2097152\ns_addc_u32 s77,s77,0\nv_accvgpr_write_b32 a200,v30\n" \
"M a[140:143],a[100:103],a[76:79],a[140:143]\ns_mov_b64 exec,0xffff\nglobal_load_dword v0,v1,s[76:77]\ns_mov_b64 exec,-1\nM a[144:147],a[104:107],a[72:75],a[144:147]\nv_accvgpr_write_b32 a201,v31\nv_accvgpr_write_b32 a202,v32\nM a[148:151],a[108:111],a[72:75],a[148:151]\n" \
"v_accvgpr_write_b32 a203,v33\nv_mul_f32 v88,s20,v88\nM a[152:155],a[104:107],a[76:79],a[152:155]\nv_mul_f32 v89,s20,v89\nv_mul_f32 v90,s20,v90\nM a[156:159],a[108:111],a[76:79],a[156:159]\nv_mul_f32 v91,s20,v91\nC v34,v88,v89\n" \
"M a[160:163],a[112:115],a[72:75],a[160:163]\nC v35,v90,v91\nv_mul_f32 v92,s20,v92\nM a[164:167],a[116:119],a[72:75],a[164:167]\nv_mul_f32 v93,s20,v93\nv_mul_f32 v94,s20,v94\nM a[168:171],a[112:115],a[76:79],a[168:171]\ns_add_u32 s76,s76,2097152\n" \
"s_addc_u32 s77,s77,0\nv_mul_f32 v95,s20,v95\nM a[172:175],a[116:119],a[76:79],a[172:175]\ns_mov_b64 exec,0xffff\nglobal_load_dword v0,v1,s[76:77]\ns_mov_b64 exec,-1\nM a[176:179],a[120:123],a[72:75],a[176:179]\nC v36,v92,v93\n" \
"C v37,v94,v95\nM a[180:183],a[124:127],a[72:75],a[180:183]\nv_accvgpr_write_b32 a204,v34\nv_accvgpr_write_b32 a205,v35\nM a[184:187],a[120:123],a[76:79],a[184:187]\ns_waitcnt lgkmcnt(0)\nv_accvgpr_write_b32 a206,v36\nM a[188:191],a[124:127],a[76:79],a[188:191]\n" \
"buffer_store_dwordx4 a[192:195],v4,s[36:39],s96 offen offset:0 sc1\ns_branch LC_%=\nLAV_%=:\nM v[64:67],a[24:27],a[32:35],0\nds_read_b128 a[64:67],v8 offset:0\nM v[68:71],a[28:31],a[32:35],0\nM v[72:75],a[24:27],a[36:39],0\ns_mov_b32 s47,786432\n" \
"s_add_u32 m0,s22,49152\nds_read_b64_tr_b16 a[96:97],v9 offset:16384\nM v[76:79],a[28:31],a[36:39],0\nbuffer_load_dwordx4 v21,s[32:35],s47 offen offset:0 lds\nM v[80:83],a[24:27],a[40:43],0\nds_read_b64_tr_b16 a[98:99],v10 offset:16384\nM v[84:87],a[28:31],a[40:43],0\nM v[88:91],a[24:27],a[44:47],0\n" \
"s_add_u32 m0,s22,50048\nds_read_b128 a[68:71],v8 offset:2048\nM v[92:95],a[28:31],a[44:47],0\nbuffer_load_dwordx4 v21,s[32:35],s47 offen offset:128 lds\nM v[96:99],a[24:27],a[48:51],0\nds_read_b64_tr_b16 a[100:101],v11 offset:16384\nM v[100:103],a[28:31],a[48:51],0\nM v[104:107],a[24:27],a[52:55],0\n" \
"s_add_u32 m0,s22,51200\nds_read_b64_tr_b16 a[102:103],v12 offset:16384\nM v[108:111],a[28:31],a[52:55],0\nbuffer_load_dwordx4 v21,s[92:95],s47 offen offset:0 lds\nM v[112:115],a[24:27],a[56:59],0\nds_read_b128 a[72:75],v8 offset:4096\nM v[116:119],a[28:31],a[56:59],0\nM v[120:123],a[24:27],a[60:63],0\n" \
"s_add_u32 m0,s22,52096\nds_read_b64_tr_b16 a[104:105],v9 offset:16896\nM v[124:127],a[28:31],a[60:63],0\nbuffer_load_dwordx4 v21,s[92:95],s47 offen offset:128 lds\nM v[128:131],a[16:19],a[32:35],0\nds_read_b64_tr_b16 a[106:107],v10 offset:16896\nM v[132:135],a[20:23],a[32:35],0\nM v[136:139],a[16:19],a[36:39],0\n" \
"s_mov_b32 s46,256\ns_add_u32 m0,s50,32768\nds_read_b128 a[76:79],v8 offset:6144\nM v[140:143],a[20:23],a[36:39],0\nbuffer_load_dwordx4 v13,s[28:31],s46 offen lds\nM v[144:147],a[16:19],a[40:43],0\nds_read_b64_tr_b16 a[108:109],v11 offset:16896\nM v[148:151],a[20:23],a[40:43],0\n" \
"M v[152:155],a[16:19],a[44:47],0\ns_add_u32 m0,s50,36864\nds_read_b64_tr_b16 a[110:111],v12 offset:16896\nM v[156:159],a[20:23],a[44:47],0\nbuffer_load_dwordx4 v14,s[28:31],s46 offen lds\nM v[160:163],a[16:19],a[48:51],0\nds_read_b128 a[80:83],v8 offset:8192\nM v[164:167],a[20:23],a[48:51],0\n" \
"M v[168:171],a[16:19],a[52:55],0\ns_add_u32 m0,s50,40960\nds_read_b64_tr_b16 a[112:113],v9 offset:17408\nM v[172:175],a[20:23],a[52:55],0\nbuffer_load_dwordx4 v15,s[28:31],s46 offen lds\nM v[176:179],a[16:19],a[56:59],0\nds_read_b64_tr_b16 a[114:115],v10 offset:17408\nM v[180:183],a[20:23],a[56:59],0\n" \
"M v[184:187],a[16:19],a[60:63],0\ns_add_u32 m0,s50,45056\nds_read_b128 a[84:87],v8 offset:10240\nM v[188:191],a[20:23],a[60:63],0\nbuffer_load_dwordx4 v16,s[28:31],s46 offen lds\nM v[192:195],a[0:3],a[32:35],0\nds_read_b64_tr_b16 a[116:117],v11 offset:17408\nM v[196:199],a[4:7],a[32:35],0\n" \
"M v[200:203],a[0:3],a[36:39],0\ns_mov_b32 s46,256\ns_add_u32 m0,s50,49152\nds_read_b64_tr_b16 a[118:119],v12 offset:17408\nM v[204:207],a[4:7],a[36:39],0\nbuffer_load_dwordx4 v17,s[28:31],s46 offen lds\nM v[208:211],a[0:3],a[40:43],0\nds_read_b128 a[88:91],v8 offset:12288\n" \
"M v[212:215],a[4:7],a[40:43],0\nM v[216:219],a[0:3],a[44:47],0\ns_add_u32 m0,s50,53248\nds_read_b64_tr_b16 a[120:121],v9 offset:17920\nM v[220:223],a[4:7],a[44:47],0\nbuffer_load_dwordx4 v18,s[28:31],s46 offen lds\nM v[224:227],a[0:3],a[48:51],0\nds_read_b64_tr_b16 a[122:123],v10 offset:17920\n" \
"M v[228:231],a[4:7],a[48:51],0\nM v[232:235],a[0:3],a[52:55],0\ns_add_u32 m0,s50,57344\nds_read_b128 a[92:95],v8 offset:14336\nM v[236:239],a[4:7],a[52:55],0\nbuffer_load_dwordx4 v19,s[28:31],s46 offen lds\nM v[240:243],a[0:3],a[56:59],0\nds_read_b64_tr_b16 a[124:125],v11 offset:17920\n" \
"M v[244:247],a[4:7],a[56:59],0\nM v[248:251],a[0:3],a[60:63],0\ns_add_u32 m0,s50,61440\nds_read_b64_tr_b16 a[126:127],v12 offset:17920\nM v[252:255],a[4:7],a[60:63],0\nbuffer_load_dwordx4 v20,s[28:31],s46 offen lds\nM a[128:131],a[8:11],a[32:35],0\nM a[132:135],a[12:15],a[32:35],0\n" \
"M a[136:139],a[8:11],a[36:39],0\nM a[140:143],a[12:15],a[36:39],0\nglobal_load_dwordx4 v[46:49],v4,s[86:87] offset:0\nM a[144:147],a[8:11],a[40:43],0\nM a[148:151],a[12:15],a[40:43],0\nM a[152:155],a[8:11],a[44:47],0\nM a[156:159],a[12:15],a[44:47],0\ns_waitcnt vmcnt(13) lgkmcnt(0)\n" \
"M a[160:163],a[8:11],a[48:51],0\ns_barrier\nM a[164:167],a[12:15],a[48:51],0\nM a[168:171],a[8:11],a[52:55],0\nM a[172:175],a[12:15],a[52:55],0\nglobal_load_dwordx4 v[50:53],v4,s[86:87] offset:1024\nM a[176:179],a[8:11],a[56:59],0\nM a[180:183],a[12:15],a[56:59],0\n" \
"M a[184:187],a[8:11],a[60:63],0\nM a[188:191],a[12:15],a[60:63],0\nglobal_load_dwordx4 v[30:33],v26,s[64:65] offset:0\nM v[64:67],a[88:91],a[96:99],v[64:67]\nds_read_b128 a[0:3],v5 offset:0\nM v[68:71],a[92:95],a[96:99],v[68:71]\nM v[72:75],a[88:91],a[100:103],v[72:75]\nds_read_b64_tr_b16 a[32:33],v9 offset:32768\n" \
"M v[76:79],a[92:95],a[100:103],v[76:79]\nglobal_load_dwordx4 v[34:37],v26,s[64:65] offset:1024\nM v[80:83],a[88:91],a[104:107],v[80:83]\nds_read_b64_tr_b16 a[34:35],v10 offset:32768\nM v[84:87],a[92:95],a[104:107],v[84:87]\nM v[88:91],a[88:91],a[108:111],v[88:91]\nds_read_b128 a[4:7],v5 offset:2048\nM v[92:95],a[92:95],a[108:111],v[92:95]\n" \
"global_load_dwordx4 v[38:41],v26,s[64:65] offset:2048\nM v[96:99],a[88:91],a[112:115],v[96:99]\nds_read_b64_tr_b16 a[36:37],v11 offset:32768\nM v[100:103],a[92:95],a[112:115],v[100:103]\nM v[104:107],a[88:91],a[116:119],v[104:107]\nds_read_b64_tr_b16 a[38:39],v12 offset:32768\nM v[108:111],a[92:95],a[116:119],v[108:111]\nglobal_load_dwordx4 v[42:45],v26,s[64:65] offset:3072\n" \
"M v[112:115],a[88:91],a[120:123],v[112:115]\nds_read_b128 a[8:11],v5 offset:4096\nM v[116:119],a[92:95],a[120:123],v[116:119]\nM v[120:123],a[88:91],a[124:127],v[120:123]\ns_mov_b32 s47,1048576\ns_add_u32 m0,s22,0\nds_read_b64_tr_b16 a[40:41],v9 offset:33280\nM v[124:127],a[92:95],a[124:127],v[124:127]\n" \
"buffer_load_dwordx4 v21,s[32:35],s47 offen offset:0 lds\nM v[128:131],a[80:83],a[96:99],v[128:131]\nds_read_b64_tr_b16 a[42:43],v10 offset:33280\nM v[132:135],a[84:87],a[96:99],v[132:135]\nM v[136:139],a[80:83],a[100:103],v[136:139]\ns_add_u32 m0,s22,896\nds_read_b128 a[12:15],v5 offset:6144\nM v[140:143],a[84:87],a[100:103],v[140:143]\n" \
"buffer_load_dwordx4 v21,s[32:35],s47 offen offset:128 lds\nM v[144:147],a[80:83],a[104:107],v[144:147]\nds_read_b64_tr_b16 a[44:45],v11 offset:33280\nM v[148:151],a[84:87],a[104:107],v[148:151]\nM v[152:155],a[80:83],a[108:111],v[152:155]\ns_add_u32 m0,s22,2048\nds_read_b64_tr_b16 a[46:47],v12 offset:33280\nM v[156:159],a[84:87],a[108:111],v[156:159]\n" \
"buffer_load_dwordx4 v21,s[92:95],s47 offen offset:0 lds\nM v[160:163],a[80:83],a[112:115],v[160:163]\nds_read_b128 a[16:19],v5 offset:8192\nM v[164:167],a[84:87],a[112:115],v[164:167]\nM v[168:171],a[80:83],a[116:119],v[168:171]\ns_add_u32 m0,s22,2944\nds_read_b64_tr_b16 a[48:49],v9 offset:33792\nM v[172:175],a[84:87],a[116:119],v[172:175]\n" \
"buffer_load_dwordx4 v21,s[92:95],s47 offen offset:128 lds\nM v[176:179],a[80:83],a[120:123],v[176:179]\nds_read_b64_tr_b16 a[50:51],v10 offset:33792\nM v[180:183],a[84:87],a[120:123],v[180:183]\nM v[184:187],a[80:83],a[124:127],v[184:187]\nds_read_b128 a[20:23],v5 offset:10240\nM v[188:191],a[84:87],a[124:127],v[188:191]\nglobal_load_dwordx4 a[230:233],v27,s[70:71] offset:0\n" \
"M v[192:195],a[64:67],a[96:99],v[192:195]\nds_read_b64_tr_b16 a[52:53],v11 offset:33792\nM v[196:199],a[68:71],a[96:99],v[196:199]\nM v[200:203],a[64:67],a[100:103],v[200:203]\nds_read_b64_tr_b16 a[54:55],v12 offset:33792\nM v[204:207],a[68:71],a[100:103],v[204:207]\nglobal_load_dwordx4 a[234:237],v27,s[70:71] offset:16\nM v[208:211],a[64:67],a[104:107],v[208:211]\n" \
"ds_read_b128 a[24:27],v5 offset:12288\nM v[212:215],a[68:71],a[104:107],v[212:215]\nM v[216:219],a[64:67],a[108:111],v[216:219]\nds_read_b64_tr_b16 a[56:57],v9 offset:34304\nM v[220:223],a[68:71],a[108:111],v[220:223]\nbuffer_load_dwordx2 a[192:193],v54,s[40:43],0 offen offset:0\nM v[224:227],a[64:67],a[112:115],v[224:227]\nds_read_b64_tr_b16 a[58:59],v10 offset:34304\n" \
"M v[228:231],a[68:71],a[112:115],v[228:231]\nM v[232:235],a[64:67],a[116:119],v[232:235]\nds_read_b128 a[28:31],v5 offset:14336\nM v[236:239],a[68:71],a[116:119],v[236:239]\nbuffer_load_dwordx2 a[194:195],v54,s[40:43],0 offen offset:2048\nM v[240:243],a[64:67],a[120:123],v[240:243]\nds_read_b64_tr_b16 a[60:61],v11 offset:34304\nM v[244:247],a[68:71],a[120:123],v[244:247]\n" \
"M v[248:251],a[64:67],a[124:127],v[248:251]\nds_read_b64_tr_b16 a[62:63],v12 offset:34304\nM v[252:255],a[68:71],a[124:127],v[252:255]\nbuffer_load_dwordx2 a[196:197],v55,s[40:43],0 offen offset:0\nM a[128:131],a[72:75],a[96:99],a[128:131]\nM a[132:135],a[76:79],a[96:99],a[132:135]\nM a[136:139],a[72:75],a[100:103],a[136:139]\ns_mov_b32 s52,0\n" \
"M a[140:143],a[76:79],a[100:103],a[140:143]\nbuffer_load_dwordx2 a[198:199],v59,s[40:43],s52 offen offset:0\nM a[144:147],a[72:75],a[104:107],a[144:147]\nM a[148:151],a[76:79],a[104:107],a[148:151]\nM a[152:155],a[72:75],a[108:111],a[152:155]\nM a[156:159],a[76:79],a[108:111],a[156:159]\ns_waitcnt vmcnt(24) lgkmcnt(0)\nM a[160:163],a[72:75],a[112:115],a[160:163]\n" \
"s_barrier\nM a[164:167],a[76:79],a[112:115],a[164:167]\nM a[168:171],a[72:75],a[116:119],a[168:171]\nM a[172:175],a[76:79],a[116:119],a[172:175]\nbuffer_load_dwordx2 a[214:215],v59,s[80:83],s52 offen offset:0\nM a[176:179],a[72:75],a[120:123],a[176:179]\nM a[180:183],a[76:79],a[120:123],a[180:183]\ns_waitcnt vmcnt(11)\n" \
"v_lshlrev_b32 v25,16,v46\nM a[184:187],a[72:75],a[124:127],a[184:187]\nv_and_b32 v46,0xffff0000,v46\nv_lshlrev_b32 v26,16,v30\nM a[188:191],a[76:79],a[124:127],a[188:191]\nbuffer_load_dwordx2 a[200:201],v59,s[40:43],s52 offen offset:2048\nM v[64:67],a[24:27],a[32:35],v[64:67]\nds_read_b128 a[64:67],v6 offset:0\n" \
"v_and_b32 v30,0xffff0000,v30\nM v[68:71],a[28:31],a[32:35],v[68:71]\nv_mul_f32 v23,v26,v25\nv_fmac_f32 v23,v30,v46\nM v[72:75],a[24:27],a[36:39],v[72:75]\ns_mov_b32 s47,1310720\ns_add_u32 m0,s22,16384\nds_read_b64_tr_b16 a[96:97],v9 offset:49152\n" \
"M v[76:79],a[28:31],a[36:39],v[76:79]\nbuffer_load_dwordx4 v21,s[32:35],s47 offen offset:0 lds\nM v[80:83],a[24:27],a[40:43],v[80:83]\nds_read_b64_tr_b16 a[98:99],v10 offset:49152\nv_lshlrev_b32 v26,16,v38\nM v[84:87],a[28:31],a[40:43],v[84:87]\nv_and_b32 v38,0xffff0000,v38\nv_mul_f32 v24,v26,v25\n" \
"M v[88:91],a[24:27],a[44:47],v[88:91]\ns_add_u32 m0,s22,17280\nds_read_b128 a[68:71],v6 offset:2048\nv_fmac_f32 v24,v38,v46\nM v[92:95],a[28:31],a[44:47],v[92:95]\nbuffer_load_dwordx4 v21,s[32:35],s47 offen offset:128 lds\nM v[96:99],a[24:27],a[48:51],v[96:99]\nds_read_b64_tr_b16 a[100:101],v11 offset:49152\n" \
"v_lshlrev_b32 v25,16,v47\nM v[100:103],a[28:31],a[48:51],v[100:103]\nv_and_b32 v47,0xffff0000,v47\nv_lshlrev_b32 v26,16,v31\nM v[104:107],a[24:27],a[52:55],v[104:107]\ns_add_u32 m0,s22,18432\nds_read_b64_tr_b16 a[102:103],v12 offset:49152\nv_and_b32 v31,0xffff0000,v31\n" \
"M v[108:111],a[28:31],a[52:55],v[108:111]\nbuffer_load_dwordx4 v21,s[92:95],s47 offen offset:0 lds\nM v[112:115],a[24:27],a[56:59],v[112:115]\nds_read_b128 a[72:75],v6 offset:4096\nv_fmac_f32 v23,v26,v25\nM v[116:119],a[28:31],a[56:59],v[116:119]\nv_fmac_f32 v23,v31,v47\nv_lshlrev_b32 v26,16,v39\n" \
"M v[120:123],a[24:27],a[60:63],v[120:123]\ns_add_u32 m0,s22,19328\nds_read_b64_tr_b16 a[104:105],v9 offset:49664\nv_and_b32 v39,0xffff0000,v39\nM v[124:127],a[28:31],a[60:63],v[124:127]\nbuffer_load_dwordx4 v21,s[92:95],s47 offen offset:128 lds\nM v[128:131],a[16:19],a[32:35],v[128:131]\nds_read_b64_tr_b16 a[106:107],v10 offset:49664\n" \
"v_fmac_f32 v24,v26,v25\nM v[132:135],a[20:23],a[32:35],v[132:135]\nv_fmac_f32 v24,v39,v47\nv_lshlrev_b32 v25,16,v48\nM v[136:139],a[16:19],a[36:39],v[136:139]\nds_read_b128 a[76:79],v6 offset:6144\nv_and_b32 v48,0xffff0000,v48\nM v[140:143],a[20:23],a[36:39],v[140:143]\n" \
"buffer_load_dwordx2 a[216:217],v59,s[80:83],s52 offen offset:2048\nM v[144:147],a[16:19],a[40:43],v[144:147]\nLG4_%=:\ns_cbranch_execz LG8_%=\nds_read_b64_tr_b16 a[108:109],v11 offset:49664\nv_lshlrev_b32 v26,16,v32\nM v[148:151],a[20:23],a[40:43],v[148:151]\nv_and_b32 v32,0xffff0000,v32\n" \
"v_fmac_f32 v23,v26,v25\nM v[152:155],a[16:19],a[44:47],v[152:155]\ns_mov_b32 s52,4096\nds_read_b64_tr_b16 a[110:111],v12 offset:49664\nv_fmac_f32 v23,v32,v48\nM v[156:159],a[20:23],a[44:47],v[156:159]\nbuffer_load_dwordx2 a[202:203],v59,s[40:43],s52 offen offset:0\nM v[160:163],a[16:19],a[48:51],v[160:163]\n" \
"ds_read_b128 a[80:83],v6 offset:8192\nv_lshlrev_b32 v26,16,v40\nM v[164:167],a[20:23],a[48:51],v[164:167]\nv_and_b32 v40,0xffff0000,v40\nv_fmac_f32 v24,v26,v25\nM v[168:171],a[16:19],a[52:55],v[168:171]\nds_read_b64_tr_b16 a[112:113],v9 offset:50176\nv_fmac_f32 v24,v40,v48\n" \
"M v[172:175],a[20:23],a[52:55],v[172:175]\nbuffer_load_dwordx2 a[218:219],v59,s[80:83],s52 offen offset:0\nM v[176:179],a[16:19],a[56:59],v[176:179]\nds_read_b64_tr_b16 a[114:115],v10 offset:50176\nv_lshlrev_b32 v25,16,v49\nM v[180:183],a[20:23],a[56:59],v[180:183]\nv_and_b32 v49,0xffff0000,v49\nv_lshlrev_b32 v26,16,v33\n" \
"M v[184:187],a[16:19],a[60:63],v[184:187]\nds_read_b128 a[84:87],v6 offset:10240\nv_and_b32 v33,0xffff0000,v33\nM v[188:191],a[20:23],a[60:63],v[188:191]\nbuffer_load_dwordx2 a[204:205],v59,s[40:43],s52 offen offset:2048\nM v[192:195],a[0:3],a[32:35],v[192:195]\nds_read_b64_tr_b16 a[116:117],v11 offset:50176\nv_fmac_f32 v23,v26,v25\n" \
"M v[196:199],a[4:7],a[32:35],v[196:199]\nv_fmac_f32 v23,v33,v49\nv_lshlrev_b32 v26,16,v41\nM v[200:203],a[0:3],a[36:39],v[200:203]\nds_read_b64_tr_b16 a[118:119],v12 offset:50176\nv_and_b32 v41,0xffff0000,v41\nM v[204:207],a[4:7],a[36:39],v[204:207]\nbuffer_load_dwordx2 a[220:221],v59,s[80:83],s52 offen offset:2048\n" \
"M v[208:211],a[0:3],a[40:43],v[208:211]\nds_read_b128 a[88:91],v6 offset:12288\nv_fmac_f32 v24,v26,v25\nM v[212:215],a[4:7],a[40:43],v[212:215]\nv_fmac_f32 v24,v41,v49\nv_lshlrev_b32 v25,16,v50\nM v[216:219],a[0:3],a[44:47],v[216:219]\ns_mov_b32 s52,8192\n" \
"ds_read_b64_tr_b16 a[120:121],v9 offset:50688\nv_and_b32 v50,0xffff0000,v50\nM v[220:223],a[4:7],a[44:47],v[220:223]\nbuffer_load_dwordx2 a[206:207],v59,s[40:43],s52 offen offset:0\nM v[224:227],a[0:3],a[48:51],v[224:227]\nds_read_b64_tr_b16 a[122:123],v10 offset:50688\nv_lshlrev_b32 v26,16,v34\nM v[228:231],a[4:7],a[48:51],v[228:231]\n" \
"v_and_b32 v34,0xffff0000,v34\nv_fmac_f32 v23,v26,v25\nM v[232:235],a[0:3],a[52:55],v[232:235]\nds_read_b128 a[92:95],v6 offset:14336\nv_fmac_f32 v23,v34,v50\nM v[236:239],a[4:7],a[52:55],v[236:239]\nbuffer_load_dwordx2 a[222:223],v59,s[80:83],s52 offen offset:0\nM v[240:243],a[0:3],a[56:59],v[240:243]\n" \
"ds_read_b64_tr_b16 a[124:125],v11 offset:50688\nv_lshlrev_b32 v26,16,v42\nM v[244:247],a[4:7],a[56:59],v[244:247]\nv_and_b32 v42,0xffff0000,v42\nv_fmac_f32 v24,v26,v25\nM v[248:251],a[0:3],a[60:63],v[248:251]\ns_mov_b32 s47,1572864\ns_add_u32 m0,s22,32768\n" \
"ds_read_b64_tr_b16 a[126:127],v12 offset:50688\nM v[252:255],a[4:7],a[60:63],v[252:255]\nbuffer_load_dwordx4 v21,s[32:35],s47 offen offset:0 lds\nM a[128:131],a[8:11],a[32:35],a[128:131]\nv_fmac_f32 v24,v42,v50\nv_lshlrev_b32 v25,16,v51\nM a[132:135],a[12:15],a[32:35],a[132:135]\nv_and_b32 v51,0xffff0000,v51\n" \
"v_lshlrev_b32 v26,16,v35\nM a[136:139],a[8:11],a[36:39],a[136:139]\ns_add_u32 m0,s22,33664\nv_and_b32 v35,0xffff0000,v35\nM a[140:143],a[12:15],a[36:39],a[140:143]\nbuffer_load_dwordx4 v21,s[32:35],s47 offen offset:128 lds\nM a[144:147],a[8:11],a[40:43],a[144:147]\nv_fmac_f32 v23,v26,v25\n" \
"v_fmac_f32 v23,v35,v51\nM a[148:151],a[12:15],a[40:43],a[148:151]\nv_lshlrev_b32 v26,16,v43\nv_and_b32 v43,0xffff0000,v43\nM a[152:155],a[8:11],a[44:47],a[152:155]\nv_fmac_f32 v24,v26,v25\nv_fmac_f32 v24,v43,v51\nM a[156:159],a[12:15],a[44:47],a[156:159]\n" \
"s_waitcnt vmcnt(21) lgkmcnt(0)\nv_lshlrev_b32 v25,16,v52\nM a[160:163],a[8:11],a[48:51],a[160:163]\ns_barrier\nM a[164:167],a[12:15],a[48:51],a[164:167]\nv_and_b32 v52,0xffff0000,v52\nv_lshlrev_b32 v26,16,v36\nM a[168:171],a[8:11],a[52:55],a[168:171]\n" \
"s_add_u32 m0,s22,34816\nv_and_b32 v36,0xffff0000,v36\nM a[172:175],a[12:15],a[52:55],a[172:175]\nbuffer_load_dwordx4 v21,s[92:95],s47 offen offset:0 lds\nM a[176:179],a[8:11],a[56:59],a[176:179]\ns_add_u32 m0,s22,35712\nv_fmac_f32 v23,v26,v25\nM a[180:183],a[12:15],a[56:59],a[180:183]\n" \
"buffer_load_dwordx4 v21,s[92:95],s47 offen offset:128 lds\nM a[184:187],a[8:11],a[60:63],a[184:187]\ns_add_u32 s84,s84,1048576\ns_addc_u32 s85,s85,0\nv_fmac_f32 v23,v36,v52\nM a[188:191],a[12:15],a[60:63],a[188:191]\ns_mov_b64 exec,0xffff\nglobal_load_dword v0,v1,s[84:85]\n" \
"s_mov_b64 exec,-1\nM v[64:67],a[88:91],a[96:99],v[64:67]\nds_read_b128 a[0:3],v5 offset:32768\nv_lshlrev_b32 v26,16,v44\nv_and_b32 v44,0xffff0000,v44\nM v[68:71],a[92:95],a[96:99],v[68:71]\nv_fmac_f32 v24,v26,v25\nv_fmac_f32 v24,v44,v52\n" \
"M v[72:75],a[88:91],a[100:103],v[72:75]\ns_mov_b32 s46,384\ns_add_u32 m0,s50,65536\nds_read_b64_tr_b16 a[32:33],v9 offset:0\nM v[76:79],a[92:95],a[100:103],v[76:79]\nbuffer_load_dwordx4 v13,s[28:31],s46 offen lds\nM v[80:83],a[88:91],a[104:107],v[80:83]\nds_read_b64_tr_b16 a[34:35],v10 offset:0\n" \
"v_lshlrev_b32 v25,16,v53\nv_and_b32 v53,0xffff0000,v53\nM v[84:87],a[92:95],a[104:107],v[84:87]\nv_lshlrev_b32 v26,16,v37\nv_and_b32 v37,0xffff0000,v37\nM v[88:91],a[88:91],a[108:111],v[88:91]\ns_add_u32 m0,s50,69632\nds_read_b128 a[4:7],v5 offset:34816\n" \
"v_fmac_f32 v23,v26,v25\nM v[92:95],a[92:95],a[108:111],v[92:95]\nbuffer_load_dwordx4 v14,s[28:31],s46 offen lds\nM v[96:99],a[88:91],a[112:115],v[96:99]\nds_read_b64_tr_b16 a[36:37],v11 offset:0\nv_fmac_f32 v23,v37,v53\nv_lshlrev_b32 v26,16,v45\nM v[100:103],a[92:95],a[112:115],v[100:103]\n" \
"v_and_b32 v45,0xffff0000,v45\nv_fmac_f32 v24,v26,v25\nM v[104:107],a[88:91],a[116:119],v[104:107]\ns_add_u32 m0,s50,73728\nds_read_b64_tr_b16 a[38:39],v12 offset:0\nv_fmac_f32 v24,v45,v53\nM v[108:111],a[92:95],a[116:119],v[108:111]\nbuffer_load_dwordx4 v15,s[28:31],s46 offen lds\n" \
"s_waitcnt vmcnt(10)\nM v[112:115],a[88:91],a[120:123],v[112:115]\nds_read_b128 a[8:11],v5 offset:36864\nv_accvgpr_read_b32 v43,a230\nv_permlane32_swap_b32 v23,v24\nM v[116:119],a[92:95],a[120:123],v[116:119]\nv_add_f32 v23,v23,v24\nv_mov_b32 v24,v23\n" \
"M v[120:123],a[88:91],a[124:127],v[120:123]\ns_add_u32 m0,s50,77824\nds_read_b64_tr_b16 a[40:41],v9 offset:512\nv_lshlrev_b32 v42,16,v43\nM v[124:127],a[92:95],a[124:127],v[124:127]\nbuffer_load_dwordx4 v16,s[28:31],s46 offen lds\nM v[128:131],a[80:83],a[96:99],v[128:131]\nds_read_b64_tr_b16 a[42:43],v10 offset:512\n" \
"v_and_b32 v43,0xffff0000,v43\nv_permlane16_swap_b32 v23,v24\nM v[132:135],a[84:87],a[96:99],v[132:135]\nv_add_f32 v23,v23,v24\nv_accvgpr_read_b32 v45,a231\nM v[136:139],a[80:83],a[100:103],v[136:139]\ns_mov_b32 s46,384\ns_add_u32 m0,s50,81920\n" \
"ds_read_b128 a[12:15],v5 offset:38912\nM v[140:143],a[84:87],a[100:103],v[140:143]\nbuffer_load_dwordx4 v17,s[28:31],s46 offen lds\nM v[144:147],a[80:83],a[104:107],v[144:147]\nds_read_b64_tr_b16 a[44:45],v11 offset:512\nv_lshlrev_b32 v44,16,v45\nv_add_f32 v23,v23,v23 row_ror:8 row_mask:0xf bank_mask:0xf\nM v[148:151],a[84:87],a[104:107],v[148:151]\n" \
"v_and_b32 v45,0xffff0000,v45\nv_accvgpr_read_b32 v47,a232\nM v[152:155],a[80:83],a[108:111],v[152:155]\ns_add_u32 m0,s50,86016\nds_read_b64_tr_b16 a[46:47],v12 offset:512\nv_add_f32 v23,v23,v23 row_ror:4 row_mask:0xf bank_mask:0xf\nM v[156:159],a[84:87],a[108:111],v[156:159]\nbuffer_load_dwordx4 v18,s[28:31],s46 offen lds\n" \
"M v[160:163],a[80:83],a[112:115],v[160:163]\nds_read_b128 a[16:19],v5 offset:40960\nv_lshlrev_b32 v46,16,v47\nv_and_b32 v47,0xffff0000,v47\nM v[164:167],a[84:87],a[112:115],v[164:167]\nv_add_f32 v23,v23,v23 row_ror:2 row_mask:0xf bank_mask:0xf\nv_accvgpr_read_b32 v49,a233\nM v[168:171],a[80:83],a[116:119],v[168:171]\n" \
"s_add_u32 m0,s50,90112\nds_read_b64_tr_b16 a[48:49],v9 offset:1024\nv_lshlrev_b32 v48,16,v49\nM v[172:175],a[84:87],a[116:119],v[172:175]\nbuffer_load_dwordx4 v19,s[28:31],s46 offen lds\nM v[176:179],a[80:83],a[120:123],v[176:179]\nds_read_b64_tr_b16 a[50:51],v10 offset:1024\nv_add_f32 v23,v23,v23 row_ror:1 row_mask:0xf bank_mask:0xf\n" \
"C v23,v23,v23\nM v[180:183],a[84:87],a[120:123],v[180:183]\nv_and_b32 v23,0xffff0000,v23\nv_mul_f32 v23,0xbfb8aa3b,v23\nM v[184:187],a[80:83],a[124:127],v[184:187]\ns_add_u32 m0,s50,94208\nds_read_b128 a[20:23],v5 offset:43008\nv_exp_f32 v23,v23\n" \
"M v[188:191],a[84:87],a[124:127],v[188:191]\nbuffer_load_dwordx4 v20,s[28:31],s46 offen lds\nM v[192:195],a[64:67],a[96:99],v[192:195]\nds_read_b64_tr_b16 a[52:53],v11 offset:1024\nv_and_b32 v49,0xffff0000,v49\nv_accvgpr_read_b32 v36,a192\nM v[196:199],a[68:71],a[96:99],v[196:199]\nv_add_f32 v23,1.0,v23\n" \
"v_rcp_f32 v23,v23\nM v[200:203],a[64:67],a[100:103],v[200:203]\ns_mov_b32 s47,1835008\ns_add_u32 m0,s22,49152\nds_read_b64_tr_b16 a[54:55],v12 offset:1024\nM v[204:207],a[68:71],a[100:103],v[204:207]\nbuffer_load_dwordx4 v21,s[32:35],s47 offen offset:0 lds\nM v[208:211],a[64:67],a[104:107],v[208:211]\n" \
"ds_read_b128 a[24:27],v5 offset:45056\nv_lshlrev_b32 v30,16,v36\nv_and_b32 v36,0xffff0000,v36\nM v[212:215],a[68:71],a[104:107],v[212:215]\nC v23,v23,v23\nv_and_b32 v23,0xffff0000,v23\nM v[216:219],a[64:67],a[108:111],v[216:219]\ns_add_u32 m0,s22,50048\n" \
"ds_read_b64_tr_b16 a[56:57],v9 offset:1536\nv_accvgpr_read_b32 v37,a194\nM v[220:223],a[68:71],a[108:111],v[220:223]\nbuffer_load_dwordx4 v21,s[32:35],s47 offen offset:128 lds\nM v[224:227],a[64:67],a[112:115],v[224:227]\nds_read_b64_tr_b16 a[58:59],v10 offset:1536\nv_lshlrev_b32 v31,16,v37\nv_and_b32 v37,0xffff0000,v37\n" \
"M v[228:231],a[68:71],a[112:115],v[228:231]\nv_accvgpr_read_b32 v38,a196\nv_lshlrev_b32 v32,16,v38\nM v[232:235],a[64:67],a[116:119],v[232:235]\ns_add_u32 m0,s22,51200\nds_read_b128 a[28:31],v5 offset:47104\nv_and_b32 v38,0xffff0000,v38\nM v[236:239],a[68:71],a[116:119],v[236:239]\n" \
"buffer_load_dwordx4 v21,s[92:95],s47 offen offset:0 lds\nM v[240:243],a[64:67],a[120:123],v[240:243]\nds_read_b64_tr_b16 a[60:61],v11 offset:1536\nv_accvgpr_read_b32 v39,a198\nv_lshlrev_b32 v33,16,v39\nM v[244:247],a[68:71],a[120:123],v[244:247]\nv_and_b32 v39,0xffff0000,v39\nv_mul_f32 v25,v30,v45\n" \
"M v[248:251],a[64:67],a[124:127],v[248:251]\ns_add_u32 m0,s22,52096\nds_read_b64_tr_b16 a[62:63],v12 offset:1536\nv_mul_f32 v26,v36,v49\nM v[252:255],a[68:71],a[124:127],v[252:255]\nbuffer_load_dwordx4 v21,s[92:95],s47 offen offset:128 lds\nM a[128:131],a[72:75],a[96:99],a[128:131]\nv_fmac_f32 v25,v31,v44\n" \
"v_fmac_f32 v26,v37,v48\nM a[132:135],a[76:79],a[96:99],a[132:135]\nv_fmac_f32 v25,v32,v43\nv_fmac_f32 v26,v38,v47\nM a[136:139],a[72:75],a[100:103],a[136:139]\nv_fmac_f32 v25,v33,v42\nv_fmac_f32 v26,v39,v46\nM a[140:143],a[76:79],a[100:103],a[140:143]\n" \
"buffer_load_dwordx2 a[208:209],v59,s[40:43],s52 offen offset:2048\nM a[144:147],a[72:75],a[104:107],a[144:147]\nv_accvgpr_read_b32 v28,a214\nC v26,v25,v26\nM a[148:151],a[76:79],a[104:107],a[148:151]\nv_lshlrev_b32 v27,16,v28\nv_lshlrev_b32 v25,16,v26\nM a[152:155],a[72:75],a[108:111],a[152:155]\n" \
"v_and_b32 v28,0xffff0000,v28\nv_and_b32 v26,0xffff0000,v26\nM a[156:159],a[76:79],a[108:111],a[156:159]\ns_waitcnt vmcnt(25) lgkmcnt(0)\nv_mul_f32 v29,v25,v27\nv_mul_f32 v58,v26,v28\nM a[160:163],a[72:75],a[112:115],a[160:163]\ns_barrier\n" \
"M a[164:167],a[76:79],a[112:115],a[164:167]\nv_accvgpr_read_b32 v40,a200\nv_lshlrev_b32 v34,16,v40\nM a[168:171],a[72:75],a[116:119],a[168:171]\nv_and_b32 v40,0xffff0000,v40\nv_mul_f32 v25,v31,v45\nM a[172:175],a[76:79],a[116:119],a[172:175]\nbuffer_load_dwordx2 a[224:225],v59,s[80:83],s52 offen offset:2048\n" \
"M a[176:179],a[72:75],a[120:123],a[176:179]\nv_mul_f32 v26,v37,v49\nv_fmac_f32 v25,v32,v44\nM a[180:183],a[76:79],a[120:123],a[180:183]\nv_fmac_f32 v26,v38,v48\nv_fmac_f32 v25,v33,v43\nM a[184:187],a[72:75],a[124:127],a[184:187]\ns_mov_b32 s52,12288\n" \
"v_fmac_f32 v26,v39,v47\nv_fmac_f32 v25,v34,v42\nM a[188:191],a[76:79],a[124:127],a[188:191]\nbuffer_load_dwordx2 a[210:211],v59,s[40:43],s52 offen offset:0\nM v[64:67],a[24:27],a[32:35],v[64:67]\nds_read_b128 a[64:67],v6 offset:32768\nv_fmac_f32 v26,v40,v46\nv_accvgpr_read_b32 v28,a216\n" \
"M v[68:71],a[28:31],a[32:35],v[68:71]\nC v26,v25,v26\nv_lshlrev_b32 v27,16,v28\nM v[72:75],a[24:27],a[36:39],v[72:75]\nds_read_b64_tr_b16 a[96:97],v9 offset:16384\nv_lshlrev_b32 v25,16,v26\nv_and_b32 v28,0xffff0000,v28\nM v[76:79],a[28:31],a[36:39],v[76:79]\n" \
"buffer_load_dwordx2 a[226:227],v59,s[80:83],s52 offen offset:0\nM v[80:83],a[24:27],a[40:43],v[80:83]\nds_read_b64_tr_b16 a[98:99],v10 offset:16384\nv_and_b32 v26,0xffff0000,v26\nv_mul_f32 v25,v25,v27\nM v[84:87],a[28:31],a[40:43],v[84:87]\nv_mul_f32 v26,v26,v28\nC v53,v25,v29\n" \
"M v[88:91],a[24:27],a[44:47],v[88:91]\nds_read_b128 a[68:71],v6 offset:34816\nC v57,v26,v58\nv_accvgpr_read_b32 v41,a202\nM v[92:95],a[28:31],a[44:47],v[92:95]\nbuffer_load_dwordx2 a[212:213],v59,s[40:43],s52 offen offset:2048\nM v[96:99],a[24:27],a[48:51],v[96:99]\nds_read_b64_tr_b16 a[100:101],v11 offset:16384\n" \
"v_lshlrev_b32 v35,16,v41\nv_and_b32 v41,0xffff0000,v41\nM v[100:103],a[28:31],a[48:51],v[100:103]\nv_mul_f32 v25,v32,v45\nv_mul_f32 v26,v38,v49\nM v[104:107],a[24:27],a[52:55],v[104:107]\nds_read_b64_tr_b16 a[102:103],v12 offset:16384\nv_fmac_f32 v25,v33,v44\n" \
"v_fmac_f32 v26,v39,v48\nM v[108:111],a[28:31],a[52:55],v[108:111]\nbuffer_load_dwordx2 a[228:229],v59,s[80:83],s52 offen offset:2048\nM v[112:115],a[24:27],a[56:59],v[112:115]\nds_read_b128 a[72:75],v6 offset:36864\nv_fmac_f32 v25,v34,v43\nv_fmac_f32 v26,v40,v47\nM v[116:119],a[28:31],a[56:59],v[116:119]\n" \
"v_fmac_f32 v25,v35,v42\nv_fmac_f32 v26,v41,v46\nM v[120:123],a[24:27],a[60:63],v[120:123]\ns_mov_b32 s46,512\ns_add_u32 m0,s50,0\nds_read_b64_tr_b16 a[104:105],v9 offset:16896\nM v[124:127],a[28:31],a[60:63],v[124:127]\nbuffer_load_dwordx4 v13,s[28:31],s46 offen lds\n" \
"M v[128:131],a[16:19],a[32:35],v[128:131]\nds_read_b64_tr_b16 a[106:107],v10 offset:16896\nv_accvgpr_read_b32 v28,a218\nC v26,v25,v26\nM v[132:135],a[20:23],a[32:35],v[132:135]\nv_lshlrev_b32 v27,16,v28\nv_lshlrev_b32 v25,16,v26\nM v[136:139],a[16:19],a[36:39],v[136:139]\n" \
"s_add_u32 m0,s50,4096\nds_read_b128 a[76:79],v6 offset:38912\nv_and_b32 v28,0xffff0000,v28\nM v[140:143],a[20:23],a[36:39],v[140:143]\nbuffer_load_dwordx4 v14,s[28:31],s46 offen lds\nM v[144:147],a[16:19],a[40:43],v[144:147]\nds_read_b64_tr_b16 a[108:109],v11 offset:16896\nv_and_b32 v26,0xffff0000,v26\n" \
"v_mul_f32 v29,v25,v27\nM v[148:151],a[20:23],a[40:43],v[148:151]\nv_mul_f32 v58,v26,v28\nv_accvgpr_read_b32 v36,a204\nM v[152:155],a[16:19],a[44:47],v[152:155]\ns_add_u32 m0,s50,8192\nds_read_b64_tr_b16 a[110:111],v12 offset:16896\nv_lshlrev_b32 v30,16,v36\n" \
"M v[156:159],a[20:23],a[44:47],v[156:159]\nbuffer_load_dwordx4 v15,s[28:31],s46 offen lds\nM v[160:163],a[16:19],a[48:51],v[160:163]\nds_read_b128 a[80:83],v6 offset:40960\nv_and_b32 v36,0xffff0000,v36\nv_mul_f32 v25,v33,v45\nM v[164:167],a[20:23],a[48:51],v[164:167]\nv_mul_f32 v26,v39,v49\n" \
"v_fmac_f32 v25,v34,v44\nM v[168:171],a[16:19],a[52:55],v[168:171]\ns_add_u32 m0,s50,12288\nds_read_b64_tr_b16 a[112:113],v9 offset:17408\nv_fmac_f32 v26,v40,v48\nM v[172:175],a[20:23],a[52:55],v[172:175]\nbuffer_load_dwordx4 v16,s[28:31],s46 offen lds\nM v[176:179],a[16:19],a[56:59],v[176:179]\n" \
"ds_read_b64_tr_b16 a[114:115],v10 offset:17408\nv_fmac_f32 v25,v35,v43\nv_fmac_f32 v26,v41,v47\nM v[180:183],a[20:23],a[56:59],v[180:183]\nv_fmac_f32 v25,v30,v42\nv_fmac_f32 v26,v36,v46\nM v[184:187],a[16:19],a[60:63],v[184:187]\ns_mov_b32 s46,512\n" \
"s_add_u32 m0,s50,16384\nds_read_b128 a[84:87],v6 offset:43008\nM v[188:191],a[20:23],a[60:63],v[188:191]\nbuffer_load_dwordx4 v17,s[28:31],s46 offen lds\nM v[192:195],a[0:3],a[32:35],v[192:195]\nds_read_b64_tr_b16 a[116:117],v11 offset:17408\nv_accvgpr_read_b32 v28,a220\nC v26,v25,v26\n" \
"M v[196:199],a[4:7],a[32:35],v[196:199]\nv_lshlrev_b32 v27,16,v28\nv_lshlrev_b32 v25,16,v26\nM v[200:203],a[0:3],a[36:39],v[200:203]\ns_add_u32 m0,s50,20480\nds_read_b64_tr_b16 a[118:119],v12 offset:17408\nv_and_b32 v28,0xffff0000,v28\nM v[204:207],a[4:7],a[36:39],v[204:207]\n" \
"buffer_load_dwordx4 v18,s[28:31],s46 offen lds\nM v[208:211],a[0:3],a[40:43],v[208:211]\nds_read_b128 a[88:91],v6 offset:45056\nv_and_b32 v26,0xffff0000,v26\nv_mul_f32 v25,v25,v27\nM v[212:215],a[4:7],a[40:43],v[212:215]\nv_mul_f32 v26,v26,v28\nC v52,v25,v29\n" \
"M v[216:219],a[0:3],a[44:47],v[216:219]\ns_add_u32 m0,s50,24576\nds_read_b64_tr_b16 a[120:121],v9 offset:17920\nC v56,v26,v58\nM v[220:223],a[4:7],a[44:47],v[220:223]\nbuffer_load_dwordx4 v19,s[28:31],s46 offen lds\nM v[224:227],a[0:3],a[48:51],v[224:227]\nds_read_b64_tr_b16 a[122:123],v10 offset:17920\n" \
"M v[228:231],a[4:7],a[48:51],v[228:231]\nM v[232:235],a[0:3],a[52:55],v[232:235]\ns_add_u32 m0,s50,28672\nds_read_b128 a[92:95],v6 offset:47104\nM v[236:239],a[4:7],a[52:55],v[236:239]\nbuffer_load_dwordx4 v20,s[28:31],s46 offen lds\nM v[240:243],a[0:3],a[56:59],v[240:243]\nds_read_b64_tr_b16 a[124:125],v11 offset:17920\n" \
"M v[244:247],a[4:7],a[56:59],v[244:247]\nM v[248:251],a[0:3],a[60:63],v[248:251]\ns_mov_b32 s47,2097152\ns_add_u32 m0,s22,0\nds_read_b64_tr_b16 a[126:127],v12 offset:17920\nM v[252:255],a[4:7],a[60:63],v[252:255]\nbuffer_load_dwordx4 v21,s[32:35],s47 offen offset:0 lds\nM a[128:131],a[8:11],a[32:35],a[128:131]\n" \
"M a[132:135],a[12:15],a[32:35],a[132:135]\nM a[136:139],a[8:11],a[36:39],a[136:139]\ns_add_u32 m0,s22,896\nM a[140:143],a[12:15],a[36:39],a[140:143]\nbuffer_load_dwordx4 v21,s[32:35],s47 offen offset:128 lds\nM a[144:147],a[8:11],a[40:43],a[144:147]\nM a[148:151],a[12:15],a[40:43],a[148:151]\nM a[152:155],a[8:11],a[44:47],a[152:155]\n" \
"s_waitcnt vmcnt(10)\nv_accvgpr_read_b32 v37,a206\nv_lshlrev_b32 v31,16,v37\nM a[156:159],a[12:15],a[44:47],a[156:159]\ns_waitcnt vmcnt(20) lgkmcnt(0)\nv_and_b32 v37,0xffff0000,v37\nv_mul_f32 v25,v34,v45\nM a[160:163],a[8:11],a[48:51],a[160:163]\n" \
"s_barrier\nM a[164:167],a[12:15],a[48:51],a[164:167]\nv_mul_f32 v26,v40,v49\nv_fmac_f32 v25,v35,v44\nM a[168:171],a[8:11],a[52:55],a[168:171]\ns_add_u32 m0,s22,2048\nv_fmac_f32 v26,v41,v48\nv_fmac_f32 v25,v30,v43\n" \
"M a[172:175],a[12:15],a[52:55],a[172:175]\nbuffer_load_dwordx4 v21,s[92:95],s47 offen offset:0 lds\nM a[176:179],a[8:11],a[56:59],a[176:179]\ns_add_u32 m0,s22,2944\nv_fmac_f32 v26,v36,v47\nv_fmac_f32 v25,v31,v42\nM a[180:183],a[12:15],a[56:59],a[180:183]\nbuffer_load_dwordx4 v21,s[92:95],s47 offen offset:128 lds\n" \
"M a[184:187],a[8:11],a[60:63],a[184:187]\ns_add_u32 s84,s84,1048576\ns_addc_u32 s85,s85,0\nv_fmac_f32 v26,v37,v46\nv_accvgpr_read_b32 v28,a222\nM a[188:191],a[12:15],a[60:63],a[188:191]\ns_mov_b64 exec,0xffff\nglobal_load_dword v0,v1,s[84:85]\n" \
"s_mov_b64 exec,-1\nM v[64:67],a[88:91],a[96:99],v[64:67]\nds_read_b128 a[0:3],v7 offset:0\nC v26,v25,v26\nv_lshlrev_b32 v27,16,v28\nM v[68:71],a[92:95],a[96:99],v[68:71]\nv_lshlrev_b32 v25,16,v26\nv_and_b32 v28,0xffff0000,v28\n" \
"M v[72:75],a[88:91],a[100:103],v[72:75]\ns_mov_b32 s47,2359296\ns_add_u32 m0,s22,16384\nds_read_b64_tr_b16 a[32:33],v9 offset:32768\nM v[76:79],a[92:95],a[100:103],v[76:79]\nbuffer_load_dwordx4 v21,s[32:35],s47 offen offset:0 lds\nM v[80:83],a[88:91],a[104:107],v[80:83]\nds_read_b64_tr_b16 a[34:35],v10 offset:32768\n" \
"v_and_b32 v26,0xffff0000,v26\nv_mul_f32 v29,v25,v27\nM v[84:87],a[92:95],a[104:107],v[84:87]\nv_mul_f32 v58,v26,v28\nv_accvgpr_read_b32 v38,a208\nM v[88:91],a[88:91],a[108:111],v[88:91]\nds_read_b128 a[4:7],v7 offset:2048\nv_lshlrev_b32 v32,16,v38\n" \
"v_and_b32 v38,0xffff0000,v38\nM v[92:95],a[92:95],a[108:111],v[92:95]\nv_mul_f32 v25,v35,v45\nv_mul_f32 v26,v41,v49\nM v[96:99],a[88:91],a[112:115],v[96:99]\ns_mov_b32 s46,640\ns_add_u32 m0,s50,32768\nds_read_b64_tr_b16 a[36:37],v11 offset:32768\n" \
"M v[100:103],a[92:95],a[112:115],v[100:103]\nbuffer_load_dwordx4 v13,s[28:31],s46 offen lds\nM v[104:107],a[88:91],a[116:119],v[104:107]\nds_read_b64_tr_b16 a[38:39],v12 offset:32768\nv_fmac_f32 v25,v30,v44\nv_fmac_f32 v26,v36,v48\nM v[108:111],a[92:95],a[116:119],v[108:111]\nv_fmac_f32 v25,v31,v43\n" \
"v_fmac_f32 v26,v37,v47\nM v[112:115],a[88:91],a[120:123],v[112:115]\nds_read_b128 a[8:11],v7 offset:4096\nv_fmac_f32 v25,v32,v42\nv_fmac_f32 v26,v38,v46\nM v[116:119],a[92:95],a[120:123],v[116:119]\nv_accvgpr_read_b32 v28,a224\nC v26,v25,v26\n" \
"M v[120:123],a[88:91],a[124:127],v[120:123]\ns_add_u32 m0,s22,17280\nds_read_b64_tr_b16 a[40:41],v9 offset:33280\nv_lshlrev_b32 v27,16,v28\nM v[124:127],a[92:95],a[124:127],v[124:127]\nbuffer_load_dwordx4 v21,s[32:35],s47 offen offset:128 lds\nM v[128:131],a[80:83],a[96:99],v[128:131]\nds_read_b64_tr_b16 a[42:43],v10 offset:33280\n" \
"v_lshlrev_b32 v25,16,v26\nv_and_b32 v28,0xffff0000,v28\nM v[132:135],a[84:87],a[96:99],v[132:135]\nv_and_b32 v26,0xffff0000,v26\nv_mul_f32 v25,v25,v27\nM v[136:139],a[80:83],a[100:103],v[136:139]\nds_read_b128 a[12:15],v7 offset:6144\nv_mul_f32 v26,v26,v28\n" \
"C v51,v25,v29\nM v[140:143],a[84:87],a[100:103],v[140:143]\nC v55,v26,v58\nv_accvgpr_read_b32 v39,a210\nM v[144:147],a[80:83],a[104:107],v[144:147]\ns_add_u32 m0,s50,36864\nds_read_b64_tr_b16 a[44:45],v11 offset:33280\nv_lshlrev_b32 v33,16,v39\n" \
"M v[148:151],a[84:87],a[104:107],v[148:151]\nLG5_%=:\ns_cbranch_execz LG9_%=\nbuffer_load_dwordx4 v14,s[28:31],s46 offen lds\nM v[152:155],a[80:83],a[108:111],v[152:155]\nds_read_b64_tr_b16 a[46:47],v12 offset:33280\nv_and_b32 v39,0xffff0000,v39\nv_mul_f32 v25,v30,v45\n" \
"M v[156:159],a[84:87],a[108:111],v[156:159]\nv_mul_f32 v26,v36,v49\nv_fmac_f32 v25,v31,v44\nM v[160:163],a[80:83],a[112:115],v[160:163]\nds_read_b128 a[16:19],v7 offset:8192\nv_fmac_f32 v26,v37,v48\nv_fmac_f32 v25,v32,v43\nM v[164:167],a[84:87],a[112:115],v[164:167]\n" \
"v_fmac_f32 v26,v38,v47\nv_fmac_f32 v25,v33,v42\nM v[168:171],a[80:83],a[116:119],v[168:171]\ns_add_u32 m0,s22,18432\nds_read_b64_tr_b16 a[48:49],v9 offset:33792\nv_fmac_f32 v26,v39,v46\nM v[172:175],a[84:87],a[116:119],v[172:175]\nbuffer_load_dwordx4 v21,s[92:95],s47 offen offset:0 lds\n" \
"M v[176:179],a[80:83],a[120:123],v[176:179]\nds_read_b64_tr_b16 a[50:51],v10 offset:33792\nv_accvgpr_read_b32 v28,a226\nC v26,v25,v26\nM v[180:183],a[84:87],a[120:123],v[180:183]\nv_lshlrev_b32 v27,16,v28\nv_lshlrev_b32 v25,16,v26\nM v[184:187],a[80:83],a[124:127],v[184:187]\n" \
"ds_read_b128 a[20:23],v7 offset:10240\nv_and_b32 v28,0xffff0000,v28\nv_and_b32 v26,0xffff0000,v26\nM v[188:191],a[84:87],a[124:127],v[188:191]\nv_mul_f32 v29,v25,v27\nv_mul_f32 v58,v26,v28\nM v[192:195],a[64:67],a[96:99],v[192:195]\ns_add_u32 m0,s50,40960\n" \
"ds_read_b64_tr_b16 a[52:53],v11 offset:33792\nv_accvgpr_read_b32 v40,a212\nM v[196:199],a[68:71],a[96:99],v[196:199]\nbuffer_load_dwordx4 v15,s[28:31],s46 offen lds\nM v[200:203],a[64:67],a[100:103],v[200:203]\nds_read_b64_tr_b16 a[54:55],v12 offset:33792\nv_lshlrev_b32 v34,16,v40\nv_and_b32 v40,0xffff0000,v40\n" \
"M v[204:207],a[68:71],a[100:103],v[204:207]\nv_mul_f32 v25,v31,v45\nv_mul_f32 v26,v37,v49\nM v[208:211],a[64:67],a[104:107],v[208:211]\nds_read_b128 a[24:27],v7 offset:12288\nv_fmac_f32 v25,v32,v44\nv_fmac_f32 v26,v38,v48\nM v[212:215],a[68:71],a[104:107],v[212:215]\n" \
"v_fmac_f32 v25,v33,v43\nv_fmac_f32 v26,v39,v47\nM v[216:219],a[64:67],a[108:111],v[216:219]\ns_add_u32 m0,s22,19328\nds_read_b64_tr_b16 a[56:57],v9 offset:34304\nv_fmac_f32 v25,v34,v42\nM v[220:223],a[68:71],a[108:111],v[220:223]\nbuffer_load_dwordx4 v21,s[92:95],s47 offen offset:128 lds\n" \
"M v[224:227],a[64:67],a[112:115],v[224:227]\nds_read_b64_tr_b16 a[58:59],v10 offset:34304\nv_fmac_f32 v26,v40,v46\nv_accvgpr_read_b32 v28,a228\nM v[228:231],a[68:71],a[112:115],v[228:231]\nC v26,v25,v26\nv_lshlrev_b32 v27,16,v28\nM v[232:235],a[64:67],a[116:119],v[232:235]\n" \
"ds_read_b128 a[28:31],v7 offset:14336\nv_lshlrev_b32 v25,16,v26\nv_and_b32 v28,0xffff0000,v28\nM v[236:239],a[68:71],a[116:119],v[236:239]\nv_and_b32 v26,0xffff0000,v26\nv_mul_f32 v25,v25,v27\nM v[240:243],a[64:67],a[120:123],v[240:243]\ns_add_u32 m0,s50,45056\n" \
"ds_read_b64_tr_b16 a[60:61],v11 offset:34304\nv_mul_f32 v26,v26,v28\nM v[244:247],a[68:71],a[120:123],v[244:247]\nbuffer_load_dwordx4 v16,s[28:31],s46 offen lds\nM v[248:251],a[64:67],a[124:127],v[248:251]\nds_read_b64_tr_b16 a[62:63],v12 offset:34304\nC v50,v25,v29\nC v54,v26,v58\n" \
"M v[252:255],a[68:71],a[124:127],v[252:255]\nv_accvgpr_read_b32 v43,a234\nv_lshlrev_b32 v42,16,v43\nM a[128:131],a[72:75],a[96:99],a[128:131]\nv_and_b32 v43,0xffff0000,v43\nv_accvgpr_read_b32 v45,a235\nM a[132:135],a[76:79],a[96:99],a[132:135]\nbuffer_store_dword v23,v61,s[36:39],0 offen sc1\n" \
"s_mov_b32 s98,79691776\nM a[136:139],a[72:75],a[100:103],a[136:139]\nv_lshlrev_b32 v44,16,v45\nv_and_b32 v45,0xffff0000,v45\nM a[140:143],a[76:79],a[100:103],a[140:143]\nv_accvgpr_read_b32 v47,a236\nv_lshlrev_b32 v46,16,v47\nM a[144:147],a[72:75],a[104:107],a[144:147]\n" \
"v_and_b32 v47,0xffff0000,v47\nv_accvgpr_read_b32 v49,a237\nM a[148:151],a[76:79],a[104:107],a[148:151]\nbuffer_store_dwordx4 v[50:53],v60,s[36:39],s98 offen offset:0 sc1\ns_mov_b32 s99,79695872\nM a[152:155],a[72:75],a[108:111],a[152:155]\nv_lshlrev_b32 v48,16,v49\nv_and_b32 v49,0xffff0000,v49\n" \
"M a[156:159],a[76:79],a[108:111],a[156:159]\ns_waitcnt vmcnt(29) lgkmcnt(0)\nv_accvgpr_read_b32 v36,a193\nv_lshlrev_b32 v30,16,v36\nM a[160:163],a[72:75],a[112:115],a[160:163]\ns_barrier\nM a[164:167],a[76:79],a[112:115],a[164:167]\nv_and_b32 v36,0xffff0000,v36\n" \
"v_accvgpr_read_b32 v37,a195\nM a[168:171],a[72:75],a[116:119],a[168:171]\nv_lshlrev_b32 v31,16,v37\nv_and_b32 v37,0xffff0000,v37\nM a[172:175],a[76:79],a[116:119],a[172:175]\nbuffer_store_dwordx4 v[54:57],v60,s[36:39],s99 offen offset:0 sc1\nM a[176:179],a[72:75],a[120:123],a[176:179]\nv_accvgpr_read_b32 v38,a197\n" \
"v_lshlrev_b32 v32,16,v38\nM a[180:183],a[76:79],a[120:123],a[180:183]\nv_and_b32 v38,0xffff0000,v38\nv_accvgpr_read_b32 v39,a199\nM a[184:187],a[72:75],a[124:127],a[184:187]\nv_lshlrev_b32 v33,16,v39\nv_and_b32 v39,0xffff0000,v39\nM a[188:191],a[76:79],a[124:127],a[188:191]\n" \
"v_mul_f32 v25,v30,v45\nv_mul_f32 v26,v36,v49\nM v[64:67],a[24:27],a[32:35],v[64:67]\nds_read_b128 a[64:67],v8 offset:0\nv_fmac_f32 v25,v31,v44\nv_fmac_f32 v26,v37,v48\nM v[68:71],a[28:31],a[32:35],v[68:71]\nv_fmac_f32 v25,v32,v43\n" \
"v_fmac_f32 v26,v38,v47\nM v[72:75],a[24:27],a[36:39],v[72:75]\ns_mov_b32 s47,2621440\ns_add_u32 m0,s22,32768\nds_read_b64_tr_b16 a[96:97],v9 offset:49152\nM v[76:79],a[28:31],a[36:39],v[76:79]\nbuffer_load_dwordx4 v21,s[32:35],s47 offen offset:0 lds\nM v[80:83],a[24:27],a[40:43],v[80:83]\n" \
"ds_read_b64_tr_b16 a[98:99],v10 offset:49152\nv_fmac_f32 v25,v33,v42\nv_fmac_f32 v26,v39,v46\nM v[84:87],a[28:31],a[40:43],v[84:87]\nv_accvgpr_read_b32 v28,a215\nC v26,v25,v26\nM v[88:91],a[24:27],a[44:47],v[88:91]\nds_read_b128 a[68:71],v8 offset:2048\n" \
"v_lshlrev_b32 v27,16,v28\nv_lshlrev_b32 v25,16,v26\nM v[92:95],a[28:31],a[44:47],v[92:95]\nv_and_b32 v28,0xffff0000,v28\nv_and_b32 v26,0xffff0000,v26\nM v[96:99],a[24:27],a[48:51],v[96:99]\ns_mov_b32 s46,640\ns_add_u32 m0,s50,49152\n" \
"ds_read_b64_tr_b16 a[100:101],v11 offset:49152\nM v[100:103],a[28:31],a[48:51],v[100:103]\nbuffer_load_dwordx4 v17,s[28:31],s46 offen lds\nM v[104:107],a[24:27],a[52:55],v[104:107]\nds_read_b64_tr_b16 a[102:103],v12 offset:49152\nv_mul_f32 v29,v25,v27\nv_mul_f32 v58,v26,v28\nM v[108:111],a[28:31],a[52:55],v[108:111]\n" \
"v_accvgpr_read_b32 v40,a201\nv_lshlrev_b32 v34,16,v40\nM v[112:115],a[24:27],a[56:59],v[112:115]\nds_read_b128 a[72:75],v8 offset:4096\nv_and_b32 v40,0xffff0000,v40\nv_mul_f32 v25,v31,v45\nM v[116:119],a[28:31],a[56:59],v[116:119]\nv_mul_f32 v26,v37,v49\n" \
"v_fmac_f32 v25,v32,v44\nM v[120:123],a[24:27],a[60:63],v[120:123]\ns_add_u32 m0,s22,33664\nds_read_b64_tr_b16 a[104:105],v9 offset:49664\nv_fmac_f32 v26,v38,v48\nM v[124:127],a[28:31],a[60:63],v[124:127]\nbuffer_load_dwordx4 v21,s[32:35],s47 offen offset:128 lds\nM v[128:131],a[16:19],a[32:35],v[128:131]\n" \
"ds_read_b64_tr_b16 a[106:107],v10 offset:49664\nv_fmac_f32 v25,v33,v43\nv_fmac_f32 v26,v39,v47\nM v[132:135],a[20:23],a[32:35],v[132:135]\nv_fmac_f32 v25,v34,v42\nv_fmac_f32 v26,v40,v46\nM v[136:139],a[16:19],a[36:39],v[136:139]\nds_read_b128 a[76:79],v8 offset:6144\n" \
"v_accvgpr_read_b32 v28,a217\nC v26,v25,v26\nM v[140:143],a[20:23],a[36:39],v[140:143]\nv_lshlrev_b32 v27,16,v28\nv_lshlrev_b32 v25,16,v26\nM v[144:147],a[16:19],a[40:43],v[144:147]\ns_add_u32 m0,s50,53248\nds_read_b64_tr_b16 a[108:109],v11 offset:49664\n" \
"v_and_b32 v28,0xffff0000,v28\nM v[148:151],a[20:23],a[40:43],v[148:151]\nbuffer_load_dwordx4 v18,s[28:31],s46 offen lds\nM v[152:155],a[16:19],a[44:47],v[152:155]\nds_read_b64_tr_b16 a[110:111],v12 offset:49664\nv_and_b32 v26,0xffff0000,v26\nv_mul_f32 v25,v25,v27\nM v[156:159],a[20:23],a[44:47],v[156:159]\n" \
"v_mul_f32 v26,v26,v28\nC v53,v25,v29\nM v[160:163],a[16:19],a[48:51],v[160:163]\nds_read_b128 a[80:83],v8 offset:8192\nC v57,v26,v58\nv_accvgpr_read_b32 v41,a203\nM v[164:167],a[20:23],a[48:51],v[164:167]\nv_lshlrev_b32 v35,16,v41\n" \
"v_and_b32 v41,0xffff0000,v41\nM v[168:171],a[16:19],a[52:55],v[168:171]\ns_add_u32 m0,s22,34816\nds_read_b64_tr_b16 a[112:113],v9 offset:50176\nv_mul_f32 v25,v32,v45\nM v[172:175],a[20:23],a[52:55],v[172:175]\nbuffer_load_dwordx4 v21,s[92:95],s47 offen offset:0 lds\nM v[176:179],a[16:19],a[56:59],v[176:179]\n" \
"ds_read_b64_tr_b16 a[114:115],v10 offset:50176\nv_mul_f32 v26,v38,v49\nv_fmac_f32 v25,v33,v44\nM v[180:183],a[20:23],a[56:59],v[180:183]\nv_fmac_f32 v26,v39,v48\nv_fmac_f32 v25,v34,v43\nM v[184:187],a[16:19],a[60:63],v[184:187]\nds_read_b128 a[84:87],v8 offset:10240\n" \
"v_fmac_f32 v26,v40,v47\nv_fmac_f32 v25,v35,v42\nM v[188:191],a[20:23],a[60:63],v[188:191]\nv_fmac_f32 v26,v41,v46\nv_accvgpr_read_b32 v28,a219\nM v[192:195],a[0:3],a[32:35],v[192:195]\ns_add_u32 m0,s50,57344\nds_read_b64_tr_b16 a[116:117],v11 offset:50176\n" \
"C v26,v25,v26\nM v[196:199],a[4:7],a[32:35],v[196:199]\nbuffer_load_dwordx4 v19,s[28:31],s46 offen lds\nM v[200:203],a[0:3],a[36:39],v[200:203]\nds_read_b64_tr_b16 a[118:119],v12 offset:50176\nv_lshlrev_b32 v27,16,v28\nv_lshlrev_b32 v25,16,v26\nM v[204:207],a[4:7],a[36:39],v[204:207]\n" \
"v_and_b32 v28,0xffff0000,v28\nv_and_b32 v26,0xffff0000,v26\nM v[208:211],a[0:3],a[40:43],v[208:211]\nds_read_b128 a[88:91],v8 offset:12288\nv_mul_f32 v29,v25,v27\nv_mul_f32 v58,v26,v28\nM v[212:215],a[4:7],a[40:43],v[212:215]\nv_accvgpr_read_b32 v36,a205\n" \
"v_lshlrev_b32 v30,16,v36\nM v[216:219],a[0:3],a[44:47],v[216:219]\ns_add_u32 m0,s22,35712\nds_read_b64_tr_b16 a[120:121],v9 offset:50688\nv_and_b32 v36,0xffff0000,v36\nM v[220:223],a[4:7],a[44:47],v[220:223]\nbuffer_load_dwordx4 v21,s[92:95],s47 offen offset:128 lds\nM v[224:227],a[0:3],a[48:51],v[224:227]\n" \
"ds_read_b64_tr_b16 a[122:123],v10 offset:50688\nv_mul_f32 v25,v33,v45\nv_mul_f32 v26,v39,v49\nM v[228:231],a[4:7],a[48:51],v[228:231]\nv_fmac_f32 v25,v34,v44\nv_fmac_f32 v26,v40,v48\nM v[232:235],a[0:3],a[52:55],v[232:235]\nds_read_b128 a[92:95],v8 offset:14336\n" \
"v_fmac_f32 v25,v35,v43\nv_fmac_f32 v26,v41,v47\nM v[236:239],a[4:7],a[52:55],v[236:239]\nv_fmac_f32 v25,v30,v42\nv_fmac_f32 v26,v36,v46\nM v[240:243],a[0:3],a[56:59],v[240:243]\ns_add_u32 m0,s50,61440\nds_read_b64_tr_b16 a[124:125],v11 offset:50688\n" \
"v_accvgpr_read_b32 v28,a221\nM v[244:247],a[4:7],a[56:59],v[244:247]\nbuffer_load_dwordx4 v20,s[28:31],s46 offen lds\nM v[248:251],a[0:3],a[60:63],v[248:251]\nds_read_b64_tr_b16 a[126:127],v12 offset:50688\nC v26,v25,v26\nv_lshlrev_b32 v27,16,v28\nM v[252:255],a[4:7],a[60:63],v[252:255]\n" \
"v_lshlrev_b32 v25,16,v26\nv_and_b32 v28,0xffff0000,v28\nM a[128:131],a[8:11],a[32:35],a[128:131]\ns_add_u32 s84,s84,1048576\ns_addc_u32 s85,s85,0\nv_and_b32 v26,0xffff0000,v26\nv_mul_f32 v25,v25,v27\nM a[132:135],a[12:15],a[32:35],a[132:135]\n" \
"s_mov_b64 exec,0xffff\nglobal_load_dword v0,v1,s[84:85]\ns_mov_b64 exec,-1\nM a[136:139],a[8:11],a[36:39],a[136:139]\nv_mul_f32 v26,v26,v28\nC v52,v25,v29\nM a[140:143],a[12:15],a[36:39],a[140:143]\nC v56,v26,v58\n" \
"v_accvgpr_read_b32 v37,a207\nM a[144:147],a[8:11],a[40:43],a[144:147]\nv_lshlrev_b32 v31,16,v37\nv_and_b32 v37,0xffff0000,v37\nM a[148:151],a[12:15],a[40:43],a[148:151]\nv_mul_f32 v25,v34,v45\nv_mul_f32 v26,v40,v49\nM a[152:155],a[8:11],a[44:47],a[152:155]\n" \
"v_fmac_f32 v25,v35,v44\nv_fmac_f32 v26,v41,v48\nM a[156:159],a[12:15],a[44:47],a[156:159]\ns_waitcnt vmcnt(21) lgkmcnt(0)\nv_fmac_f32 v25,v30,v43\nv_fmac_f32 v26,v36,v47\nM a[160:163],a[8:11],a[48:51],a[160:163]\ns_barrier\n" \
"M a[164:167],a[12:15],a[48:51],a[164:167]\nv_fmac_f32 v25,v31,v42\nv_fmac_f32 v26,v37,v46\nM a[168:171],a[8:11],a[52:55],a[168:171]\nv_accvgpr_read_b32 v28,a223\nC v26,v25,v26\nM a[172:175],a[12:15],a[52:55],a[172:175]\nv_lshlrev_b32 v27,16,v28\n" \
"v_lshlrev_b32 v25,16,v26\nM a[176:179],a[8:11],a[56:59],a[176:179]\nv_and_b32 v28,0xffff0000,v28\nv_and_b32 v26,0xffff0000,v26\nM a[180:183],a[12:15],a[56:59],a[180:183]\nv_mul_f32 v29,v25,v27\nv_mul_f32 v58,v26,v28\nM a[184:187],a[8:11],a[60:63],a[184:187]\n" \
"v_accvgpr_read_b32 v38,a209\nv_lshlrev_b32 v32,16,v38\nM a[188:191],a[12:15],a[60:63],a[188:191]\nv_and_b32 v38,0xffff0000,v38\nv_mul_f32 v25,v35,v45\nM v[64:67],a[88:91],a[96:99],v[64:67]\nds_read_b128 a[0:3],v5 offset:0\nv_mul_f32 v26,v41,v49\n" \
"v_fmac_f32 v25,v30,v44\nM v[68:71],a[92:95],a[96:99],v[68:71]\nv_fmac_f32 v26,v36,v48\nv_fmac_f32 v25,v31,v43\nM v[72:75],a[88:91],a[100:103],v[72:75]\ns_mov_b32 s47,2883584\ns_add_u32 m0,s22,49152\nds_read_b64_tr_b16 a[32:33],v9 offset:0\n" \
"M v[76:79],a[92:95],a[100:103],v[76:79]\nbuffer_load_dwordx4 v21,s[32:35],s47 offen offset:0 lds\nM v[80:83],a[88:91],a[104:107],v[80:83]\nds_read_b64_tr_b16 a[34:35],v10 offset:0\nv_fmac_f32 v26,v37,v47\nv_fmac_f32 v25,v32,v42\nM v[84:87],a[92:95],a[104:107],v[84:87]\nv_fmac_f32 v26,v38,v46\n" \
"v_accvgpr_read_b32 v28,a225\nM v[88:91],a[88:91],a[108:111],v[88:91]\nds_read_b128 a[4:7],v5 offset:2048\nC v26,v25,v26\nv_lshlrev_b32 v27,16,v28\nM v[92:95],a[92:95],a[108:111],v[92:95]\nv_lshlrev_b32 v25,16,v26\nv_and_b32 v28,0xffff0000,v28\n" \
"M v[96:99],a[88:91],a[112:115],v[96:99]\ns_mov_b32 s46,768\ns_add_u32 m0,s50,65536\nds_read_b64_tr_b16 a[36:37],v11 offset:0\nM v[100:103],a[92:95],a[112:115],v[100:103]\nbuffer_load_dwordx4 v13,s[28:31],s46 offen lds\nM v[104:107],a[88:91],a[116:119],v[104:107]\nds_read_b64_tr_b16 a[38:39],v12 offset:0\n" \
"v_and_b32 v26,0xffff0000,v26\nv_mul_f32 v25,v25,v27\nM v[108:111],a[92:95],a[116:119],v[108:111]\nv_mul_f32 v26,v26,v28\nC v51,v25,v29\nM v[112:115],a[88:91],a[120:123],v[112:115]\nds_read_b128 a[8:11],v5 offset:4096\nC v55,v26,v58\n" \
"v_accvgpr_read_b32 v39,a211\nM v[116:119],a[92:95],a[120:123],v[116:119]\nv_lshlrev_b32 v33,16,v39\nv_and_b32 v39,0xffff0000,v39\nM v[120:123],a[88:91],a[124:127],v[120:123]\ns_add_u32 m0,s22,50048\nds_read_b64_tr_b16 a[40:41],v9 offset:512\nv_mul_f32 v25,v30,v45\n" \
"M v[124:127],a[92:95],a[124:127],v[124:127]\nbuffer_load_dwordx4 v21,s[32:35],s47 offen offset:128 lds\nM v[128:131],a[80:83],a[96:99],v[128:131]\nds_read_b64_tr_b16 a[42:43],v10 offset:512\nv_mul_f32 v26,v36,v49\nv_fmac_f32 v25,v31,v44\nM v[132:135],a[84:87],a[96:99],v[132:135]\nv_fmac_f32 v26,v37,v48\n" \
"v_fmac_f32 v25,v32,v43\nM v[136:139],a[80:83],a[100:103],v[136:139]\nds_read_b128 a[12:15],v5 offset:6144\nv_fmac_f32 v26,v38,v47\nv_fmac_f32 v25,v33,v42\nM v[140:143],a[84:87],a[100:103],v[140:143]\nv_fmac_f32 v26,v39,v46\nv_accvgpr_read_b32 v28,a227\n" \
"M v[144:147],a[80:83],a[104:107],v[144:147]\ns_add_u32 m0,s50,69632\nds_read_b64_tr_b16 a[44:45],v11 offset:512\nC v26,v25,v26\nM v[148:151],a[84:87],a[104:107],v[148:151]\nbuffer_load_dwordx4 v14,s[28:31],s46 offen lds\nM v[152:155],a[80:83],a[108:111],v[152:155]\nds_read_b64_tr_b16 a[46:47],v12 offset:512\n" \
"v_lshlrev_b32 v27,16,v28\nv_lshlrev_b32 v25,16,v26\nM v[156:159],a[84:87],a[108:111],v[156:159]\nv_and_b32 v28,0xffff0000,v28\nv_and_b32 v26,0xffff0000,v26\nM v[160:163],a[80:83],a[112:115],v[160:163]\nds_read_b128 a[16:19],v5 offset:8192\nv_mul_f32 v29,v25,v27\n" \
"v_mul_f32 v58,v26,v28\nM v[164:167],a[84:87],a[112:115],v[164:167]\nv_accvgpr_read_b32 v40,a213\nv_lshlrev_b32 v34,16,v40\nM v[168:171],a[80:83],a[116:119],v[168:171]\ns_add_u32 m0,s22,51200\nds_read_b64_tr_b16 a[48:49],v9 offset:1024\nv_and_b32 v40,0xffff0000,v40\n" \
"M v[172:175],a[84:87],a[116:119],v[172:175]\nbuffer_load_dwordx4 v21,s[92:95],s47 offen offset:0 lds\nM v[176:179],a[80:83],a[120:123],v[176:179]\nds_read_b64_tr_b16 a[50:51],v10 offset:1024\nv_mul_f32 v25,v31,v45\nv_mul_f32 v26,v37,v49\nM v[180:183],a[84:87],a[120:123],v[180:183]\nv_fmac_f32 v25,v32,v44\n" \
"v_fmac_f32 v26,v38,v48\nM v[184:187],a[80:83],a[124:127],v[184:187]\nds_read_b128 a[20:23],v5 offset:10240\nv_fmac_f32 v25,v33,v43\nv_fmac_f32 v26,v39,v47\nM v[188:191],a[84:87],a[124:127],v[188:191]\nv_fmac_f32 v25,v34,v42\nv_fmac_f32 v26,v40,v46\n" \
"M v[192:195],a[64:67],a[96:99],v[192:195]\ns_add_u32 m0,s50,73728\nds_read_b64_tr_b16 a[52:53],v11 offset:1024\nv_accvgpr_read_b32 v28,a229\nM v[196:199],a[68:71],a[96:99],v[196:199]\nbuffer_load_dwordx4 v15,s[28:31],s46 offen lds\nM v[200:203],a[64:67],a[100:103],v[200:203]\nds_read_b64_tr_b16 a[54:55],v12 offset:1024\n" \
"C v26,v25,v26\nv_lshlrev_b32 v27,16,v28\nM v[204:207],a[68:71],a[100:103],v[204:207]\nv_lshlrev_b32 v25,16,v26\nv_and_b32 v28,0xffff0000,v28\nM v[208:211],a[64:67],a[104:107],v[208:211]\nds_read_b128 a[24:27],v5 offset:12288\nv_and_b32 v26,0xffff0000,v26\n" \
"v_mul_f32 v25,v25,v27\nM v[212:215],a[68:71],a[104:107],v[212:215]\nv_mul_f32 v26,v26,v28\nC v50,v25,v29\nM v[216:219],a[64:67],a[108:111],v[216:219]\ns_add_u32 m0,s22,52096\nds_read_b64_tr_b16 a[56:57],v9 offset:1536\nC v54,v26,v58\n" \
"M v[220:223],a[68:71],a[108:111],v[220:223]\nbuffer_load_dwordx4 v21,s[92:95],s47 offen offset:128 lds\ns_mov_b32 s98,79699968\nM v[224:227],a[64:67],a[112:115],v[224:227]\nds_read_b64_tr_b16 a[58:59],v10 offset:1536\nM v[228:231],a[68:71],a[112:115],v[228:231]\nM v[232:235],a[64:67],a[116:119],v[232:235]\nds_read_b128 a[28:31],v5 offset:14336\n" \
"M v[236:239],a[68:71],a[116:119],v[236:239]\nM v[240:243],a[64:67],a[120:123],v[240:243]\ns_add_u32 m0,s50,77824\nds_read_b64_tr_b16 a[60:61],v11 offset:1536\nM v[244:247],a[68:71],a[120:123],v[244:247]\nbuffer_load_dwordx4 v16,s[28:31],s46 offen lds\nM v[248:251],a[64:67],a[124:127],v[248:251]\nds_read_b64_tr_b16 a[62:63],v12 offset:1536\n" \
"M v[252:255],a[68:71],a[124:127],v[252:255]\nM a[128:131],a[72:75],a[96:99],a[128:131]\nM a[132:135],a[76:79],a[96:99],a[132:135]\ns_mov_b64 exec,0xffff\nglobal_load_dword v0,v2,s[88:89]\ns_mov_b64 exec,-1\nM a[136:139],a[72:75],a[100:103],a[136:139]\nM a[140:143],a[76:79],a[100:103],a[140:143]\n" \
"M a[144:147],a[72:75],a[104:107],a[144:147]\nM a[148:151],a[76:79],a[104:107],a[148:151]\nbuffer_store_dwordx4 v[50:53],v60,s[36:39],s98 offen offset:0 sc1\ns_mov_b32 s99,79704064\nM a[152:155],a[72:75],a[108:111],a[152:155]\nM a[156:159],a[76:79],a[108:111],a[156:159]\ns_waitcnt vmcnt(23) lgkmcnt(0)\nM a[160:163],a[72:75],a[112:115],a[160:163]\n" \
"s_barrier\nM a[164:167],a[76:79],a[112:115],a[164:167]\nM a[168:171],a[72:75],a[116:119],a[168:171]\nM a[172:175],a[76:79],a[116:119],a[172:175]\nbuffer_store_dwordx4 v[54:57],v60,s[36:39],s99 offen offset:0 sc1\nM a[176:179],a[72:75],a[120:123],a[176:179]\ns_lshl_b32 s62,s8,2\ns_add_u32 s62,s62,%2\n" \
"s_lshl_b32 s63,s62,12\nM a[180:183],a[76:79],a[120:123],a[180:183]\ns_add_u32 s68,s10,s63\ns_addc_u32 s69,s11,0\ns_and_b32 s69,s69,0xffff\ns_mov_b32 s70,4096\nM a[184:187],a[72:75],a[124:127],a[184:187]\ns_mov_b32 s71,0x00020000\n" \
"s_lshr_b32 s21,s63,5\ns_add_u32 s21,s21,67108864\nM a[188:191],a[76:79],a[124:127],a[188:191]\ns_add_u32 s63,s63,79691776\ns_add_u32 s72,s4,s63\ns_addc_u32 s73,s5,0\ns_and_b32 s73,s73,0xffff\nM v[64:67],a[24:27],a[32:35],v[64:67]\n" \
"ds_read_b128 a[64:67],v6 offset:0\ns_mov_b32 s74,4096\ns_mov_b32 s75,0x00020000\nM v[68:71],a[28:31],a[32:35],v[68:71]\ns_memrealtime s[18:19]\nv_add_u32 v23,-32,v4\nv_add_u32 v24,992,v4\nM v[72:75],a[24:27],a[36:39],v[72:75]\n" \
"s_mov_b32 s47,3145728\ns_add_u32 m0,s22,0\nds_read_b64_tr_b16 a[96:97],v9 offset:16384\nM v[76:79],a[28:31],a[36:39],v[76:79]\nbuffer_load_dwordx4 v21,s[32:35],s47 offen offset:0 lds\nM v[80:83],a[24:27],a[40:43],v[80:83]\nds_read_b64_tr_b16 a[98:99],v10 offset:16384\nv_add_u32 v25,-16,v4\n" \
"v_add_u32 v26,-512,v4\nM v[84:87],a[28:31],a[40:43],v[84:87]\nv_add_u32 v27,512,v4\ns_lshr_b32 s63,%2,1\ns_lshl_b32 s63,s63,16\nM v[88:91],a[24:27],a[44:47],v[88:91]\nds_read_b128 a[68:71],v6 offset:2048\ns_lshl_b32 s56,%2,14\n" \
"s_add_u32 s56,s56,s63\nM v[92:95],a[28:31],a[44:47],v[92:95]\nv_add_u32 v55,s56,v4\nv_mov_b32 v28,0\ns_lshl_b32 s62,s62,1\nM v[96:99],a[24:27],a[48:51],v[96:99]\ns_mov_b32 s46,768\ns_add_u32 m0,s50,81920\n" \
"ds_read_b64_tr_b16 a[100:101],v11 offset:16384\nM v[100:103],a[28:31],a[48:51],v[100:103]\nbuffer_load_dwordx4 v17,s[28:31],s46 offen lds\nM v[104:107],a[24:27],a[52:55],v[104:107]\nds_read_b64_tr_b16 a[102:103],v12 offset:16384\nv_mov_b32 v29,s62\nv_lshlrev_b32 v56,23,v4\nM v[108:111],a[28:31],a[52:55],v[108:111]\n" \
"s_and_b32 s62,%1,7\ns_lshl_b32 s62,s62,8\ns_add_u32 s62,s62,94380032\nM v[112:115],a[24:27],a[56:59],v[112:115]\nds_read_b128 a[72:75],v6 offset:4096\nv_bfe_u32 v57,v4,4,5\nv_lshl_add_u32 v57,v57,3,s62\nM v[116:119],a[28:31],a[56:59],v[116:119]\n" \
"M v[120:123],a[24:27],a[60:63],v[120:123]\ns_add_u32 m0,s22,896\nds_read_b64_tr_b16 a[104:105],v9 offset:16896\nM v[124:127],a[28:31],a[60:63],v[124:127]\nbuffer_load_dwordx4 v21,s[32:35],s47 offen offset:128 lds\nM v[128:131],a[16:19],a[32:35],v[128:131]\nds_read_b64_tr_b16 a[106:107],v10 offset:16896\nM v[132:135],a[20:23],a[32:35],v[132:135]\n" \
"M v[136:139],a[16:19],a[36:39],v[136:139]\nds_read_b128 a[76:79],v6 offset:6144\nM v[140:143],a[20:23],a[36:39],v[140:143]\nLG6_%=:\ns_cbranch_execz LG10_%=\nM v[144:147],a[16:19],a[40:43],v[144:147]\ns_add_u32 m0,s50,86016\nds_read_b64_tr_b16 a[108:109],v11 offset:16896\n" \
"M v[148:151],a[20:23],a[40:43],v[148:151]\nbuffer_load_dwordx4 v18,s[28:31],s46 offen lds\nM v[152:155],a[16:19],a[44:47],v[152:155]\nds_read_b64_tr_b16 a[110:111],v12 offset:16896\nM v[156:159],a[20:23],a[44:47],v[156:159]\nM v[160:163],a[16:19],a[48:51],v[160:163]\nds_read_b128 a[80:83],v6 offset:8192\nM v[164:167],a[20:23],a[48:51],v[164:167]\n" \
"M v[168:171],a[16:19],a[52:55],v[168:171]\ns_add_u32 m0,s22,2048\nds_read_b64_tr_b16 a[112:113],v9 offset:17408\nM v[172:175],a[20:23],a[52:55],v[172:175]\nbuffer_load_dwordx4 v21,s[92:95],s47 offen offset:0 lds\nM v[176:179],a[16:19],a[56:59],v[176:179]\nds_read_b64_tr_b16 a[114:115],v10 offset:17408\nM v[180:183],a[20:23],a[56:59],v[180:183]\n" \
"M v[184:187],a[16:19],a[60:63],v[184:187]\nds_read_b128 a[84:87],v6 offset:10240\nM v[188:191],a[20:23],a[60:63],v[188:191]\nM v[192:195],a[0:3],a[32:35],v[192:195]\ns_add_u32 m0,s50,90112\nds_read_b64_tr_b16 a[116:117],v11 offset:17408\nM v[196:199],a[4:7],a[32:35],v[196:199]\nbuffer_load_dwordx4 v19,s[28:31],s46 offen lds\n" \
"M v[200:203],a[0:3],a[36:39],v[200:203]\nds_read_b64_tr_b16 a[118:119],v12 offset:17408\nM v[204:207],a[4:7],a[36:39],v[204:207]\nM v[208:211],a[0:3],a[40:43],v[208:211]\nds_read_b128 a[88:91],v6 offset:12288\nM v[212:215],a[4:7],a[40:43],v[212:215]\nM v[216:219],a[0:3],a[44:47],v[216:219]\ns_add_u32 m0,s22,2944\n" \
"ds_read_b64_tr_b16 a[120:121],v9 offset:17920\nM v[220:223],a[4:7],a[44:47],v[220:223]\nbuffer_load_dwordx4 v21,s[92:95],s47 offen offset:128 lds\nM v[224:227],a[0:3],a[48:51],v[224:227]\nds_read_b64_tr_b16 a[122:123],v10 offset:17920\nM v[228:231],a[4:7],a[48:51],v[228:231]\nM v[232:235],a[0:3],a[52:55],v[232:235]\nds_read_b128 a[92:95],v6 offset:14336\n" \
"M v[236:239],a[4:7],a[52:55],v[236:239]\nM v[240:243],a[0:3],a[56:59],v[240:243]\ns_add_u32 m0,s50,94208\nds_read_b64_tr_b16 a[124:125],v11 offset:17920\nM v[244:247],a[4:7],a[56:59],v[244:247]\nbuffer_load_dwordx4 v20,s[28:31],s46 offen lds\nM v[248:251],a[0:3],a[60:63],v[248:251]\nds_read_b64_tr_b16 a[126:127],v12 offset:17920\n" \
"M v[252:255],a[4:7],a[60:63],v[252:255]\nM a[128:131],a[8:11],a[32:35],a[128:131]\ns_add_u32 s88,s88,2048\ns_addc_u32 s89,s89,0\nM a[132:135],a[12:15],a[32:35],a[132:135]\ns_mov_b64 exec,0xffff\nglobal_load_dword v0,v2,s[88:89]\ns_mov_b64 exec,-1\n" \
"M a[136:139],a[8:11],a[36:39],a[136:139]\nM a[140:143],a[12:15],a[36:39],a[140:143]\nM a[144:147],a[8:11],a[40:43],a[144:147]\nM a[148:151],a[12:15],a[40:43],a[148:151]\nM a[152:155],a[8:11],a[44:47],a[152:155]\nM a[156:159],a[12:15],a[44:47],a[156:159]\ns_waitcnt vmcnt(9) lgkmcnt(0)\nM a[160:163],a[8:11],a[48:51],a[160:163]\n" \
"s_barrier\nM a[164:167],a[12:15],a[48:51],a[164:167]\nv_mov_b32 v58,s18\nM a[168:171],a[8:11],a[52:55],a[168:171]\nv_mov_b32 v59,s19\nM a[172:175],a[12:15],a[52:55],a[172:175]\nM a[176:179],a[8:11],a[56:59],a[176:179]\nM a[180:183],a[12:15],a[56:59],a[180:183]\n" \
"M a[184:187],a[8:11],a[60:63],a[184:187]\nM a[188:191],a[12:15],a[60:63],a[188:191]\nM v[64:67],a[88:91],a[96:99],v[64:67]\nds_read_b128 a[0:3],v5 offset:32768\nM v[68:71],a[92:95],a[96:99],v[68:71]\nM v[72:75],a[88:91],a[100:103],v[72:75]\nds_read_b64_tr_b16 a[32:33],v9 offset:32768\nM v[76:79],a[92:95],a[100:103],v[76:79]\n" \
"buffer_store_dwordx2 v[58:59],v56,s[36:39],s23 offen sc1\nM v[80:83],a[88:91],a[104:107],v[80:83]\nds_read_b64_tr_b16 a[34:35],v10 offset:32768\nM v[84:87],a[92:95],a[104:107],v[84:87]\nM v[88:91],a[88:91],a[108:111],v[88:91]\ns_mov_b32 s47,3407872\ns_add_u32 m0,s22,16384\nds_read_b128 a[4:7],v5 offset:34816\n" \
"M v[92:95],a[92:95],a[108:111],v[92:95]\nbuffer_load_dwordx4 v21,s[32:35],s47 offen offset:0 lds\nM v[96:99],a[88:91],a[112:115],v[96:99]\nds_read_b64_tr_b16 a[36:37],v11 offset:32768\nM v[100:103],a[92:95],a[112:115],v[100:103]\nM v[104:107],a[88:91],a[116:119],v[104:107]\ns_mov_b32 s46,896\ns_add_u32 m0,s50,0\n" \
"ds_read_b64_tr_b16 a[38:39],v12 offset:32768\nM v[108:111],a[92:95],a[116:119],v[108:111]\nbuffer_load_dwordx4 v13,s[28:31],s46 offen lds\nM v[112:115],a[88:91],a[120:123],v[112:115]\nds_read_b128 a[8:11],v5 offset:36864\nM v[116:119],a[92:95],a[120:123],v[116:119]\nM v[120:123],a[88:91],a[124:127],v[120:123]\ns_add_u32 m0,s22,17280\n" \
"ds_read_b64_tr_b16 a[40:41],v9 offset:33280\nM v[124:127],a[92:95],a[124:127],v[124:127]\nbuffer_load_dwordx4 v21,s[32:35],s47 offen offset:128 lds\nM v[128:131],a[80:83],a[96:99],v[128:131]\nds_read_b64_tr_b16 a[42:43],v10 offset:33280\nM v[132:135],a[84:87],a[96:99],v[132:135]\nM v[136:139],a[80:83],a[100:103],v[136:139]\ns_add_u32 m0,s50,4096\n" \
"ds_read_b128 a[12:15],v5 offset:38912\nM v[140:143],a[84:87],a[100:103],v[140:143]\nbuffer_load_dwordx4 v14,s[28:31],s46 offen lds\nM v[144:147],a[80:83],a[104:107],v[144:147]\nds_read_b64_tr_b16 a[44:45],v11 offset:33280\nM v[148:151],a[84:87],a[104:107],v[148:151]\nM v[152:155],a[80:83],a[108:111],v[152:155]\ns_add_u32 m0,s22,18432\n" \
"ds_read_b64_tr_b16 a[46:47],v12 offset:33280\nM v[156:159],a[84:87],a[108:111],v[156:159]\nbuffer_load_dwordx4 v21,s[92:95],s47 offen offset:0 lds\nM v[160:163],a[80:83],a[112:115],v[160:163]\nds_read_b128 a[16:19],v5 offset:40960\nM v[164:167],a[84:87],a[112:115],v[164:167]\nM v[168:171],a[80:83],a[116:119],v[168:171]\ns_add_u32 m0,s50,8192\n" \
"ds_read_b64_tr_b16 a[48:49],v9 offset:33792\nM v[172:175],a[84:87],a[116:119],v[172:175]\nbuffer_load_dwordx4 v15,s[28:31],s46 offen lds\nM v[176:179],a[80:83],a[120:123],v[176:179]\nds_read_b64_tr_b16 a[50:51],v10 offset:33792\nM v[180:183],a[84:87],a[120:123],v[180:183]\nM v[184:187],a[80:83],a[124:127],v[184:187]\ns_add_u32 m0,s22,19328\n" \
"ds_read_b128 a[20:23],v5 offset:43008\nM v[188:191],a[84:87],a[124:127],v[188:191]\nbuffer_load_dwordx4 v21,s[92:95],s47 offen offset:128 lds\nM v[192:195],a[64:67],a[96:99],v[192:195]\nds_read_b64_tr_b16 a[52:53],v11 offset:33792\nM v[196:199],a[68:71],a[96:99],v[196:199]\nM v[200:203],a[64:67],a[100:103],v[200:203]\ns_add_u32 m0,s50,12288\n" \
"ds_read_b64_tr_b16 a[54:55],v12 offset:33792\nM v[204:207],a[68:71],a[100:103],v[204:207]\nbuffer_load_dwordx4 v16,s[28:31],s46 offen lds\nM v[208:211],a[64:67],a[104:107],v[208:211]\nds_read_b128 a[24:27],v5 offset:45056\nM v[212:215],a[68:71],a[104:107],v[212:215]\nM v[216:219],a[64:67],a[108:111],v[216:219]\nds_read_b64_tr_b16 a[56:57],v9 offset:34304\n" \
"M v[220:223],a[68:71],a[108:111],v[220:223]\nbuffer_load_dwordx4 v[30:33],v23,s[68:71],0 offen offset:0\nM v[224:227],a[64:67],a[112:115],v[224:227]\nds_read_b64_tr_b16 a[58:59],v10 offset:34304\nM v[228:231],a[68:71],a[112:115],v[228:231]\nM v[232:235],a[64:67],a[116:119],v[232:235]\nds_read_b128 a[28:31],v5 offset:47104\nM v[236:239],a[68:71],a[116:119],v[236:239]\n" \
"buffer_load_dword v50,v25,s[68:71],0 offen offset:0\nM v[240:243],a[64:67],a[120:123],v[240:243]\nds_read_b64_tr_b16 a[60:61],v11 offset:34304\nM v[244:247],a[68:71],a[120:123],v[244:247]\nM v[248:251],a[64:67],a[124:127],v[248:251]\nds_read_b64_tr_b16 a[62:63],v12 offset:34304\nM v[252:255],a[68:71],a[124:127],v[252:255]\nbuffer_load_dwordx4 v[34:37],v24,s[68:71],0 offen offset:0\n" \
"M a[128:131],a[72:75],a[96:99],a[128:131]\nM a[132:135],a[76:79],a[96:99],a[132:135]\nM a[136:139],a[72:75],a[100:103],a[136:139]\nM a[140:143],a[76:79],a[100:103],a[140:143]\nbuffer_load_dword v51,v24,s[68:71],0 offen offset:16\nM a[144:147],a[72:75],a[104:107],a[144:147]\nM a[148:151],a[76:79],a[104:107],a[148:151]\nM a[152:155],a[72:75],a[108:111],a[152:155]\n" \
"M a[156:159],a[76:79],a[108:111],a[156:159]\ns_waitcnt vmcnt(26) lgkmcnt(0)\nM a[160:163],a[72:75],a[112:115],a[160:163]\ns_barrier\nM a[164:167],a[76:79],a[112:115],a[164:167]\nM a[168:171],a[72:75],a[116:119],a[168:171]\nM a[172:175],a[76:79],a[116:119],a[172:175]\nbuffer_load_dwordx4 v[38:41],v24,s[68:71],0 offen offset:1024\n" \
"M a[176:179],a[72:75],a[120:123],a[176:179]\nM a[180:183],a[76:79],a[120:123],a[180:183]\nM a[184:187],a[72:75],a[124:127],a[184:187]\nM a[188:191],a[76:79],a[124:127],a[188:191]\nbuffer_load_dword v52,v24,s[68:71],0 offen offset:1040\nM v[64:67],a[24:27],a[32:35],v[64:67]\nds_read_b128 a[64:67],v6 offset:32768\nM v[68:71],a[28:31],a[32:35],v[68:71]\n" \
"M v[72:75],a[24:27],a[36:39],v[72:75]\nds_read_b64_tr_b16 a[96:97],v9 offset:49152\nM v[76:79],a[28:31],a[36:39],v[76:79]\nbuffer_load_dwordx4 v[42:45],v24,s[68:71],0 offen offset:2048\nM v[80:83],a[24:27],a[40:43],v[80:83]\nds_read_b64_tr_b16 a[98:99],v10 offset:49152\nM v[84:87],a[28:31],a[40:43],v[84:87]\nM v[88:91],a[24:27],a[44:47],v[88:91]\n" \
"ds_read_b128 a[68:71],v6 offset:34816\nM v[92:95],a[28:31],a[44:47],v[92:95]\nbuffer_load_dword v53,v24,s[68:71],0 offen offset:2064\nM v[96:99],a[24:27],a[48:51],v[96:99]\nds_read_b64_tr_b16 a[100:101],v11 offset:49152\nM v[100:103],a[28:31],a[48:51],v[100:103]\nM v[104:107],a[24:27],a[52:55],v[104:107]\nds_read_b64_tr_b16 a[102:103],v12 offset:49152\n" \
"M v[108:111],a[28:31],a[52:55],v[108:111]\nbuffer_load_dwordx4 v[46:49],v24,s[68:71],0 offen offset:3072\nM v[112:115],a[24:27],a[56:59],v[112:115]\nds_read_b128 a[72:75],v6 offset:36864\nM v[116:119],a[28:31],a[56:59],v[116:119]\nM v[120:123],a[24:27],a[60:63],v[120:123]\nds_read_b64_tr_b16 a[104:105],v9 offset:49664\nM v[124:127],a[28:31],a[60:63],v[124:127]\n" \
"buffer_load_dword v54,v24,s[68:71],0 offen offset:3088\nM v[128:131],a[16:19],a[32:35],v[128:131]\nds_read_b64_tr_b16 a[106:107],v10 offset:49664\nM v[132:135],a[20:23],a[32:35],v[132:135]\nM v[136:139],a[16:19],a[36:39],v[136:139]\nds_read_b128 a[76:79],v6 offset:38912\nM v[140:143],a[20:23],a[36:39],v[140:143]\nglobal_load_short_d16_hi v28,v29,s[12:13]\n" \
"M v[144:147],a[16:19],a[40:43],v[144:147]\nds_read_b64_tr_b16 a[108:109],v11 offset:49664\nM v[148:151],a[20:23],a[40:43],v[148:151]\nM v[152:155],a[16:19],a[44:47],v[152:155]\ns_mov_b32 s47,3670016\ns_add_u32 m0,s22,32768\nds_read_b64_tr_b16 a[110:111],v12 offset:49664\nM v[156:159],a[20:23],a[44:47],v[156:159]\n" \
"buffer_load_dwordx4 v21,s[32:35],s47 offen offset:0 lds\nM v[160:163],a[16:19],a[48:51],v[160:163]\nds_read_b128 a[80:83],v6 offset:40960\nM v[164:167],a[20:23],a[48:51],v[164:167]\nM v[168:171],a[16:19],a[52:55],v[168:171]\ns_mov_b32 s46,896\ns_add_u32 m0,s50,16384\nds_read_b64_tr_b16 a[112:113],v9 offset:50176\n" \
"M v[172:175],a[20:23],a[52:55],v[172:175]\nbuffer_load_dwordx4 v17,s[28:31],s46 offen lds\nM v[176:179],a[16:19],a[56:59],v[176:179]\nds_read_b64_tr_b16 a[114:115],v10 offset:50176\nM v[180:183],a[20:23],a[56:59],v[180:183]\nM v[184:187],a[16:19],a[60:63],v[184:187]\ns_add_u32 m0,s22,33664\nds_read_b128 a[84:87],v6 offset:43008\n" \
"M v[188:191],a[20:23],a[60:63],v[188:191]\nbuffer_load_dwordx4 v21,s[32:35],s47 offen offset:128 lds\nM v[192:195],a[0:3],a[32:35],v[192:195]\nds_read_b64_tr_b16 a[116:117],v11 offset:50176\nM v[196:199],a[4:7],a[32:35],v[196:199]\nM v[200:203],a[0:3],a[36:39],v[200:203]\ns_add_u32 m0,s50,20480\nds_read_b64_tr_b16 a[118:119],v12 offset:50176\n" \
"M v[204:207],a[4:7],a[36:39],v[204:207]\nbuffer_load_dwordx4 v18,s[28:31],s46 offen lds\nM v[208:211],a[0:3],a[40:43],v[208:211]\nds_read_b128 a[88:91],v6 offset:45056\nM v[212:215],a[4:7],a[40:43],v[212:215]\nM v[216:219],a[0:3],a[44:47],v[216:219]\ns_add_u32 m0,s22,34816\nds_read_b64_tr_b16 a[120:121],v9 offset:50688\n" \
"M v[220:223],a[4:7],a[44:47],v[220:223]\nbuffer_load_dwordx4 v21,s[92:95],s47 offen offset:0 lds\nM v[224:227],a[0:3],a[48:51],v[224:227]\nds_read_b64_tr_b16 a[122:123],v10 offset:50688\nM v[228:231],a[4:7],a[48:51],v[228:231]\nM v[232:235],a[0:3],a[52:55],v[232:235]\ns_add_u32 m0,s50,24576\nds_read_b128 a[92:95],v6 offset:47104\n" \
"M v[236:239],a[4:7],a[52:55],v[236:239]\nbuffer_load_dwordx4 v19,s[28:31],s46 offen lds\nM v[240:243],a[0:3],a[56:59],v[240:243]\nds_read_b64_tr_b16 a[124:125],v11 offset:50688\nM v[244:247],a[4:7],a[56:59],v[244:247]\nM v[248:251],a[0:3],a[60:63],v[248:251]\ns_add_u32 m0,s22,35712\nds_read_b64_tr_b16 a[126:127],v12 offset:50688\n" \
"M v[252:255],a[4:7],a[60:63],v[252:255]\nbuffer_load_dwordx4 v21,s[92:95],s47 offen offset:128 lds\nM a[128:131],a[8:11],a[32:35],a[128:131]\nM a[132:135],a[12:15],a[32:35],a[132:135]\nM a[136:139],a[8:11],a[36:39],a[136:139]\ns_add_u32 m0,s50,28672\nM a[140:143],a[12:15],a[36:39],a[140:143]\nbuffer_load_dwordx4 v20,s[28:31],s46 offen lds\n" \
"M a[144:147],a[8:11],a[40:43],a[144:147]\nM a[148:151],a[12:15],a[40:43],a[148:151]\nM a[152:155],a[8:11],a[44:47],a[152:155]\nM a[156:159],a[12:15],a[44:47],a[156:159]\ns_waitcnt vmcnt(29) lgkmcnt(0)\nM a[160:163],a[8:11],a[48:51],a[160:163]\ns_barrier\nM a[164:167],a[12:15],a[48:51],a[164:167]\n" \
"M a[168:171],a[8:11],a[52:55],a[168:171]\nM a[172:175],a[12:15],a[52:55],a[172:175]\nM a[176:179],a[8:11],a[56:59],a[176:179]\nM a[180:183],a[12:15],a[56:59],a[180:183]\nM a[184:187],a[8:11],a[60:63],a[184:187]\nM a[188:191],a[12:15],a[60:63],a[188:191]\nM v[64:67],a[88:91],a[96:99],v[64:67]\nds_read_b128 a[0:3],v7 offset:0\n" \
"M v[68:71],a[92:95],a[96:99],v[68:71]\ns_mov_b64 s[28:29],s[64:65]\nM v[72:75],a[88:91],a[100:103],v[72:75]\ns_mov_b32 s47,3932160\ns_add_u32 m0,s22,49152\nds_read_b64_tr_b16 a[32:33],v9 offset:0\nM v[76:79],a[92:95],a[100:103],v[76:79]\nbuffer_load_dwordx4 v21,s[32:35],s47 offen offset:0 lds\n" \
"M v[80:83],a[88:91],a[104:107],v[80:83]\nds_read_b64_tr_b16 a[34:35],v10 offset:0\nM v[84:87],a[92:95],a[104:107],v[84:87]\ns_and_b32 s29,s29,0xffff\nM v[88:91],a[88:91],a[108:111],v[88:91]\nds_read_b128 a[4:7],v7 offset:2048\nM v[92:95],a[92:95],a[108:111],v[92:95]\ns_mov_b32 s30,4194304\n" \
"M v[96:99],a[88:91],a[112:115],v[96:99]\ns_add_u32 m0,s22,50048\nds_read_b64_tr_b16 a[36:37],v11 offset:0\nM v[100:103],a[92:95],a[112:115],v[100:103]\nbuffer_load_dwordx4 v21,s[32:35],s47 offen offset:128 lds\nM v[104:107],a[88:91],a[116:119],v[104:107]\nds_read_b64_tr_b16 a[38:39],v12 offset:0\nM v[108:111],a[92:95],a[116:119],v[108:111]\n" \
"s_lshl_b32 s60,s7,18\nM v[112:115],a[88:91],a[120:123],v[112:115]\nds_read_b128 a[8:11],v7 offset:4096\nM v[116:119],a[92:95],a[120:123],v[116:119]\ns_sub_u32 s61,3932160,s60\nM v[120:123],a[88:91],a[124:127],v[120:123]\ns_add_u32 m0,s22,51200\nds_read_b64_tr_b16 a[40:41],v9 offset:512\n" \
"M v[124:127],a[92:95],a[124:127],v[124:127]\nbuffer_load_dwordx4 v21,s[92:95],s47 offen offset:0 lds\nM v[128:131],a[80:83],a[96:99],v[128:131]\nds_read_b64_tr_b16 a[42:43],v10 offset:512\nM v[132:135],a[84:87],a[96:99],v[132:135]\nv_add_u32 v13,s60,v63\nM v[136:139],a[80:83],a[100:103],v[136:139]\nds_read_b128 a[12:15],v7 offset:6144\n" \
"M v[140:143],a[84:87],a[100:103],v[140:143]\nv_add_u32 v17,s61,v63\nM v[144:147],a[80:83],a[104:107],v[144:147]\ns_add_u32 m0,s22,52096\nds_read_b64_tr_b16 a[44:45],v11 offset:512\nM v[148:151],a[84:87],a[104:107],v[148:151]\nbuffer_load_dwordx4 v21,s[92:95],s47 offen offset:128 lds\nM v[152:155],a[80:83],a[108:111],v[152:155]\n" \
"ds_read_b64_tr_b16 a[46:47],v12 offset:512\nM v[156:159],a[84:87],a[108:111],v[156:159]\nv_add_u32 v14,0x10000,v13\nM v[160:163],a[80:83],a[112:115],v[160:163]\nds_read_b128 a[16:19],v7 offset:8192\nM v[164:167],a[84:87],a[112:115],v[164:167]\nv_add_u32 v18,0x10000,v17\nM v[168:171],a[80:83],a[116:119],v[168:171]\n" \
"ds_read_b64_tr_b16 a[48:49],v9 offset:1024\nM v[172:175],a[84:87],a[116:119],v[172:175]\nM v[176:179],a[80:83],a[120:123],v[176:179]\nds_read_b64_tr_b16 a[50:51],v10 offset:1024\nM v[180:183],a[84:87],a[120:123],v[180:183]\nv_add_u32 v15,0x10000,v14\nM v[184:187],a[80:83],a[124:127],v[184:187]\nds_read_b128 a[20:23],v7 offset:10240\n" \
"M v[188:191],a[84:87],a[124:127],v[188:191]\nv_add_u32 v19,0x10000,v18\nM v[192:195],a[64:67],a[96:99],v[192:195]\nds_read_b64_tr_b16 a[52:53],v11 offset:1024\nM v[196:199],a[68:71],a[96:99],v[196:199]\nM v[200:203],a[64:67],a[100:103],v[200:203]\nds_read_b64_tr_b16 a[54:55],v12 offset:1024\nM v[204:207],a[68:71],a[100:103],v[204:207]\n" \
"v_add_u32 v16,0x10000,v15\nM v[208:211],a[64:67],a[104:107],v[208:211]\nds_read_b128 a[24:27],v7 offset:12288\nM v[212:215],a[68:71],a[104:107],v[212:215]\nv_add_u32 v20,0x10000,v19\nM v[216:219],a[64:67],a[108:111],v[216:219]\nds_read_b64_tr_b16 a[56:57],v9 offset:1536\nM v[220:223],a[68:71],a[108:111],v[220:223]\n" \
"M v[224:227],a[64:67],a[112:115],v[224:227]\nds_read_b64_tr_b16 a[58:59],v10 offset:1536\nM v[228:231],a[68:71],a[112:115],v[228:231]\nM v[232:235],a[64:67],a[116:119],v[232:235]\nds_read_b128 a[28:31],v7 offset:14336\nM v[236:239],a[68:71],a[116:119],v[236:239]\nM v[240:243],a[64:67],a[120:123],v[240:243]\nds_read_b64_tr_b16 a[60:61],v11 offset:1536\n" \
"M v[244:247],a[68:71],a[120:123],v[244:247]\nM v[248:251],a[64:67],a[124:127],v[248:251]\nds_read_b64_tr_b16 a[62:63],v12 offset:1536\nM v[252:255],a[68:71],a[124:127],v[252:255]\nM a[128:131],a[72:75],a[96:99],a[128:131]\nM a[132:135],a[76:79],a[96:99],a[132:135]\nM a[136:139],a[72:75],a[100:103],a[136:139]\nM a[140:143],a[76:79],a[100:103],a[140:143]\n" \
"M a[144:147],a[72:75],a[104:107],a[144:147]\nM a[148:151],a[76:79],a[104:107],a[148:151]\nM a[152:155],a[72:75],a[108:111],a[152:155]\nM a[156:159],a[76:79],a[108:111],a[156:159]\ns_waitcnt vmcnt(24) lgkmcnt(0)\nM a[160:163],a[72:75],a[112:115],a[160:163]\ns_barrier\nM a[164:167],a[76:79],a[112:115],a[164:167]\n" \
"M a[168:171],a[72:75],a[116:119],a[168:171]\nM a[172:175],a[76:79],a[116:119],a[172:175]\nM a[176:179],a[72:75],a[120:123],a[176:179]\nM a[180:183],a[76:79],a[120:123],a[180:183]\nM a[184:187],a[72:75],a[124:127],a[184:187]\nM a[188:191],a[76:79],a[124:127],a[188:191]\nM v[64:67],a[24:27],a[32:35],v[64:67]\nds_read_b128 a[64:67],v8 offset:0\n" \
"M v[68:71],a[28:31],a[32:35],v[68:71]\ns_mov_b64 s[32:33],s[44:45]\nM v[72:75],a[24:27],a[36:39],v[72:75]\nds_read_b64_tr_b16 a[96:97],v9 offset:16384\nM v[76:79],a[28:31],a[36:39],v[76:79]\nbuffer_load_dwordx2 v[58:59],v57,s[36:39],0 offen sc1\nM v[80:83],a[24:27],a[40:43],v[80:83]\nds_read_b64_tr_b16 a[98:99],v10 offset:16384\n" \
"M v[84:87],a[28:31],a[40:43],v[84:87]\ns_mov_b32 s34,s49\nM v[88:91],a[24:27],a[44:47],v[88:91]\nds_read_b128 a[68:71],v8 offset:2048\nM v[92:95],a[28:31],a[44:47],v[92:95]\nM v[96:99],a[24:27],a[48:51],v[96:99]\nds_read_b64_tr_b16 a[100:101],v11 offset:16384\nM v[100:103],a[28:31],a[48:51],v[100:103]\n" \
"s_mov_b64 exec,0xff\nglobal_load_dword v0,v2,s[100:101]\ns_mov_b64 exec,-1\nM v[104:107],a[24:27],a[52:55],v[104:107]\nds_read_b64_tr_b16 a[102:103],v12 offset:16384\nM v[108:111],a[28:31],a[52:55],v[108:111]\nM v[112:115],a[24:27],a[56:59],v[112:115]\nds_read_b128 a[72:75],v8 offset:4096\n" \
"M v[116:119],a[28:31],a[56:59],v[116:119]\nM v[120:123],a[24:27],a[60:63],v[120:123]\nds_read_b64_tr_b16 a[104:105],v9 offset:16896\nM v[124:127],a[28:31],a[60:63],v[124:127]\ns_mov_b64 exec,0xffff\nglobal_load_dword v0,v1,s[76:77]\ns_mov_b64 exec,-1\nM v[128:131],a[16:19],a[32:35],v[128:131]\n" \
"ds_read_b64_tr_b16 a[106:107],v10 offset:16896\nM v[132:135],a[20:23],a[32:35],v[132:135]\nM v[136:139],a[16:19],a[36:39],v[136:139]\nds_read_b128 a[76:79],v8 offset:6144\nM v[140:143],a[20:23],a[36:39],v[140:143]\nM v[144:147],a[16:19],a[40:43],v[144:147]\nds_read_b64_tr_b16 a[108:109],v11 offset:16896\nM v[148:151],a[20:23],a[40:43],v[148:151]\n" \
"M v[152:155],a[16:19],a[44:47],v[152:155]\nds_read_b64_tr_b16 a[110:111],v12 offset:16896\nM v[156:159],a[20:23],a[44:47],v[156:159]\nM v[160:163],a[16:19],a[48:51],v[160:163]\nds_read_b128 a[80:83],v8 offset:8192\nM v[164:167],a[20:23],a[48:51],v[164:167]\nM v[168:171],a[16:19],a[52:55],v[168:171]\nds_read_b64_tr_b16 a[112:113],v9 offset:17408\n" \
"M v[172:175],a[20:23],a[52:55],v[172:175]\nM v[176:179],a[16:19],a[56:59],v[176:179]\nds_read_b64_tr_b16 a[114:115],v10 offset:17408\nM v[180:183],a[20:23],a[56:59],v[180:183]\nM v[184:187],a[16:19],a[60:63],v[184:187]\nds_read_b128 a[84:87],v8 offset:10240\nM v[188:191],a[20:23],a[60:63],v[188:191]\nM v[192:195],a[0:3],a[32:35],v[192:195]\n" \
"ds_read_b64_tr_b16 a[116:117],v11 offset:17408\nM v[196:199],a[4:7],a[32:35],v[196:199]\nM v[200:203],a[0:3],a[36:39],v[200:203]\nds_read_b64_tr_b16 a[118:119],v12 offset:17408\nM v[204:207],a[4:7],a[36:39],v[204:207]\nM v[208:211],a[0:3],a[40:43],v[208:211]\nds_read_b128 a[88:91],v8 offset:12288\nM v[212:215],a[4:7],a[40:43],v[212:215]\n" \
"M v[216:219],a[0:3],a[44:47],v[216:219]\nds_read_b64_tr_b16 a[120:121],v9 offset:17920\nM v[220:223],a[4:7],a[44:47],v[220:223]\nM v[224:227],a[0:3],a[48:51],v[224:227]\nds_read_b64_tr_b16 a[122:123],v10 offset:17920\nM v[228:231],a[4:7],a[48:51],v[228:231]\nM v[232:235],a[0:3],a[52:55],v[232:235]\nds_read_b128 a[92:95],v8 offset:14336\n" \
"M v[236:239],a[4:7],a[52:55],v[236:239]\nM v[240:243],a[0:3],a[56:59],v[240:243]\nds_read_b64_tr_b16 a[124:125],v11 offset:17920\nM v[244:247],a[4:7],a[56:59],v[244:247]\nM v[248:251],a[0:3],a[60:63],v[248:251]\nds_read_b64_tr_b16 a[126:127],v12 offset:17920\nM v[252:255],a[4:7],a[60:63],v[252:255]\nM a[128:131],a[8:11],a[32:35],a[128:131]\n" \
"M a[132:135],a[12:15],a[32:35],a[132:135]\nM a[136:139],a[8:11],a[36:39],a[136:139]\nM a[140:143],a[12:15],a[36:39],a[140:143]\nM a[144:147],a[8:11],a[40:43],a[144:147]\nM a[148:151],a[12:15],a[40:43],a[148:151]\nM a[152:155],a[8:11],a[44:47],a[152:155]\nM a[156:159],a[12:15],a[44:47],a[156:159]\ns_waitcnt vmcnt(2) lgkmcnt(0)\n" \
"M a[160:163],a[8:11],a[48:51],a[160:163]\ns_barrier\nM a[164:167],a[12:15],a[48:51],a[164:167]\nv_cmp_gt_u64 vcc,s[14:15],v[58:59]\nM a[168:171],a[8:11],a[52:55],a[168:171]\nv_cmp_le_u64 s[18:19],s[16:17],v[58:59]\nM a[172:175],a[12:15],a[52:55],a[172:175]\ns_or_b64 vcc,vcc,s[18:19]\n" \
"s_cbranch_scc1 LPUV_%=\nLPUVR_%=:\nM a[176:179],a[8:11],a[56:59],a[176:179]\nM a[180:183],a[12:15],a[56:59],a[180:183]\nM a[184:187],a[8:11],a[60:63],a[184:187]\nM a[188:191],a[12:15],a[60:63],a[188:191]\nM v[64:67],a[88:91],a[96:99],v[64:67]\nds_read_b128 a[0:3],v5 offset:0\n" \
"M v[68:71],a[92:95],a[96:99],v[68:71]\nM v[72:75],a[88:91],a[100:103],v[72:75]\nLG7_%=:\ns_cbranch_execz LG11_%=\ns_add_u32 m0,s56,10304\nds_read_b64_tr_b16 a[32:33],v9 offset:32768\nM v[76:79],a[92:95],a[100:103],v[76:79]\nbuffer_load_dwordx4 v26,s[72:75],0 offen offset:0 sc1 lds\n" \
"M v[80:83],a[88:91],a[104:107],v[80:83]\nds_read_b64_tr_b16 a[34:35],v10 offset:32768\nM v[84:87],a[92:95],a[104:107],v[84:87]\nM v[88:91],a[88:91],a[108:111],v[88:91]\ns_add_u32 m0,s56,11328\nds_read_b128 a[4:7],v5 offset:2048\nM v[92:95],a[92:95],a[108:111],v[92:95]\nbuffer_load_dwordx4 v27,s[72:75],0 offen offset:0 sc1 lds\n" \
"M v[96:99],a[88:91],a[112:115],v[96:99]\nds_read_b64_tr_b16 a[36:37],v11 offset:32768\nM v[100:103],a[92:95],a[112:115],v[100:103]\nM v[104:107],a[88:91],a[116:119],v[104:107]\ns_add_u32 m0,s56,11328\nds_read_b64_tr_b16 a[38:39],v12 offset:32768\nM v[108:111],a[92:95],a[116:119],v[108:111]\nbuffer_load_dwordx4 v27,s[72:75],0 offen offset:1024 sc1 lds\n" \
"M v[112:115],a[88:91],a[120:123],v[112:115]\nds_read_b128 a[8:11],v5 offset:4096\nM v[116:119],a[92:95],a[120:123],v[116:119]\nM v[120:123],a[88:91],a[124:127],v[120:123]\ns_add_u32 m0,s56,11328\nds_read_b64_tr_b16 a[40:41],v9 offset:33280\nM v[124:127],a[92:95],a[124:127],v[124:127]\nbuffer_load_dwordx4 v27,s[72:75],0 offen offset:2048 sc1 lds\n" \
"M v[128:131],a[80:83],a[96:99],v[128:131]\nds_read_b64_tr_b16 a[42:43],v10 offset:33280\nM v[132:135],a[84:87],a[96:99],v[132:135]\nM v[136:139],a[80:83],a[100:103],v[136:139]\ns_add_u32 m0,s56,11328\nds_read_b128 a[12:15],v5 offset:6144\nM v[140:143],a[84:87],a[100:103],v[140:143]\nbuffer_load_dwordx4 v27,s[72:75],0 offen offset:3072 sc1 lds\n" \
"M v[144:147],a[80:83],a[104:107],v[144:147]\nds_read_b64_tr_b16 a[44:45],v11 offset:33280\ns_waitcnt vmcnt(20)\nv_alignbyte_b32 v30,v31,v30,2\nM v[148:151],a[84:87],a[104:107],v[148:151]\nv_alignbyte_b32 v31,v32,v31,2\nv_alignbyte_b32 v32,v33,v32,2\nM v[152:155],a[80:83],a[108:111],v[152:155]\n" \
"s_add_u32 m0,s56,0\nds_read_b64_tr_b16 a[46:47],v12 offset:33280\nv_alignbyte_b32 v33,v50,v33,2\nM v[156:159],a[84:87],a[108:111],v[156:159]\nbuffer_load_dwordx4 v23,s[68:71],0 offen offset:0 lds\nM v[160:163],a[80:83],a[112:115],v[160:163]\nds_read_b128 a[16:19],v5 offset:8192\nM v[164:167],a[84:87],a[112:115],v[164:167]\n" \
"ds_write_b128 v55,v[30:33] offset:5184\nv_alignbyte_b32 v34,v35,v34,2\nM v[168:171],a[80:83],a[116:119],v[168:171]\ns_add_u32 m0,s56,1024\nds_read_b64_tr_b16 a[48:49],v9 offset:33792\nv_alignbyte_b32 v35,v36,v35,2\nM v[172:175],a[84:87],a[116:119],v[172:175]\nbuffer_load_dwordx4 v24,s[68:71],0 offen offset:0 lds\n" \
"M v[176:179],a[80:83],a[120:123],v[176:179]\nds_read_b64_tr_b16 a[50:51],v10 offset:33792\nv_alignbyte_b32 v36,v37,v36,2\nM v[180:183],a[84:87],a[120:123],v[180:183]\nv_alignbyte_b32 v37,v51,v37,2\nds_write_b128 v55,v[34:37] offset:6208\nM v[184:187],a[80:83],a[124:127],v[184:187]\ns_add_u32 m0,s56,1024\n" \
"ds_read_b128 a[20:23],v5 offset:10240\nv_alignbyte_b32 v38,v39,v38,2\nM v[188:191],a[84:87],a[124:127],v[188:191]\nbuffer_load_dwordx4 v24,s[68:71],0 offen offset:1024 lds\nM v[192:195],a[64:67],a[96:99],v[192:195]\nds_read_b64_tr_b16 a[52:53],v11 offset:33792\nv_alignbyte_b32 v39,v40,v39,2\nM v[196:199],a[68:71],a[96:99],v[196:199]\n" \
"v_alignbyte_b32 v40,v41,v40,2\nv_alignbyte_b32 v41,v52,v41,2\nds_write_b128 v55,v[38:41] offset:7232\nM v[200:203],a[64:67],a[100:103],v[200:203]\ns_add_u32 m0,s56,1024\nds_read_b64_tr_b16 a[54:55],v12 offset:33792\nv_alignbyte_b32 v42,v43,v42,2\nM v[204:207],a[68:71],a[100:103],v[204:207]\n" \
"buffer_load_dwordx4 v24,s[68:71],0 offen offset:2048 lds\nM v[208:211],a[64:67],a[104:107],v[208:211]\nds_read_b128 a[24:27],v5 offset:12288\nv_alignbyte_b32 v43,v44,v43,2\nM v[212:215],a[68:71],a[104:107],v[212:215]\nv_alignbyte_b32 v44,v45,v44,2\nv_alignbyte_b32 v45,v53,v45,2\nds_write_b128 v55,v[42:45] offset:8256\n" \
"M v[216:219],a[64:67],a[108:111],v[216:219]\ns_add_u32 m0,s56,1024\nds_read_b64_tr_b16 a[56:57],v9 offset:34304\nv_alignbyte_b32 v46,v47,v46,2\nM v[220:223],a[68:71],a[108:111],v[220:223]\nbuffer_load_dwordx4 v24,s[68:71],0 offen offset:3072 lds\nM v[224:227],a[64:67],a[112:115],v[224:227]\nds_read_b64_tr_b16 a[58:59],v10 offset:34304\n" \
"v_alignbyte_b32 v47,v48,v47,2\nM v[228:231],a[68:71],a[112:115],v[228:231]\nv_alignbyte_b32 v48,v49,v48,2\nv_alignbyte_b32 v49,v54,v49,2\nds_write_b128 v55,v[46:49] offset:9280\nM v[232:235],a[64:67],a[116:119],v[232:235]\ns_mov_b32 s46,0\ns_add_u32 m0,s50,65536\n" \
"ds_read_b128 a[28:31],v5 offset:14336\nM v[236:239],a[68:71],a[116:119],v[236:239]\nbuffer_load_dwordx4 v13,s[28:31],s46 offen lds\nM v[240:243],a[64:67],a[120:123],v[240:243]\nds_read_b64_tr_b16 a[60:61],v11 offset:34304\nv_lshrrev_b32 v30,4,v4\nM v[244:247],a[68:71],a[120:123],v[244:247]\nv_and_b32 v31,15,v30\n" \
"v_lshrrev_b32 v32,4,v30\nM v[248:251],a[64:67],a[124:127],v[248:251]\ns_add_u32 m0,s50,69632\nds_read_b64_tr_b16 a[62:63],v12 offset:34304\nv_add_u32 v33,1,v31\nM v[252:255],a[68:71],a[124:127],v[252:255]\nbuffer_load_dwordx4 v14,s[28:31],s46 offen lds\nM a[128:131],a[72:75],a[96:99],a[128:131]\n" \
"v_and_b32 v34,1,v33\nv_and_b32 v33,30,v33\nM a[132:135],a[76:79],a[96:99],a[132:135]\nv_mul_u32_u24 v34,5184,v34\nv_lshl_add_u32 v33,v33,1,v34\nM a[136:139],a[72:75],a[100:103],a[136:139]\ns_add_u32 m0,s50,73728\nv_lshl_add_u32 v33,v32,4,v33\n" \
"M a[140:143],a[76:79],a[100:103],a[140:143]\nbuffer_load_dwordx4 v15,s[28:31],s46 offen lds\nM a[144:147],a[72:75],a[104:107],a[144:147]\nv_add_u32 v46,s56,v33\nv_add_u32 v47,1024,v46\nM a[148:151],a[76:79],a[104:107],a[148:151]\nv_add_u32 v48,2048,v46\nv_add_u32 v49,3072,v46\n" \
"M a[152:155],a[72:75],a[108:111],a[152:155]\nv_lshlrev_b32 v33,1,v31\nv_sub_u32 v33,v33,v32\nM a[156:159],a[76:79],a[108:111],a[156:159]\ns_waitcnt vmcnt(16) lgkmcnt(0)\nv_lshlrev_b32 v33,4,v33\nM a[160:163],a[72:75],a[112:115],a[160:163]\ns_barrier\n" \
"M a[164:167],a[76:79],a[112:115],a[164:167]\nv_add_u32 v33,10320,v33\nv_add_u32 v50,s56,v33\nM a[168:171],a[72:75],a[116:119],a[168:171]\ns_add_u32 m0,s50,77824\nv_bfe_u32 v33,v4,6,3\nM a[172:175],a[76:79],a[116:119],a[172:175]\nbuffer_load_dwordx4 v16,s[28:31],s46 offen lds\n" \
"M a[176:179],a[72:75],a[120:123],a[176:179]\nv_lshlrev_b32 v60,17,v33\nv_and_b32 v33,16,v4\nM a[180:183],a[76:79],a[120:123],a[180:183]\nv_lshl_add_u32 v60,v33,2,v60\nv_and_b32 v33,32,v4\nM a[184:187],a[72:75],a[124:127],a[184:187]\nv_add_u32 v60,v60,v33\n" \
"v_lshrrev_b32 v33,9,v4\nM a[188:191],a[76:79],a[124:127],a[188:191]\nv_lshl_add_u32 v60,v33,4,v60\nv_lshl_add_u32 v33,v32,3,v31\nM v[64:67],a[24:27],a[32:35],v[64:67]\nds_read_b128 a[64:67],v6 offset:0\nv_sub_u32 v33,15,v33\nM v[68:71],a[28:31],a[32:35],v[68:71]\n" \
"v_and_b32 v34,16,v4\nv_lshrrev_b32 v34,v34,v28\nM v[72:75],a[24:27],a[36:39],v[72:75]\ns_mov_b32 s46,0\ns_add_u32 m0,s50,81920\nds_read_b64_tr_b16 a[96:97],v9 offset:49152\nM v[76:79],a[28:31],a[36:39],v[76:79]\nbuffer_load_dwordx4 v17,s[28:31],s46 offen lds\n" \
"M v[80:83],a[24:27],a[40:43],v[80:83]\nds_read_b64_tr_b16 a[98:99],v10 offset:49152\nv_lshrrev_b32 v33,1,v33\nM v[84:87],a[28:31],a[40:43],v[84:87]\nv_cmp_eq_u32 vcc,0,v33\ns_nop 1\nM v[88:91],a[24:27],a[44:47],v[88:91]\nds_read_b128 a[68:71],v6 offset:2048\n" \
"v_cndmask_b32 v52,0,v34,vcc\nM v[92:95],a[28:31],a[44:47],v[92:95]\nv_cmp_eq_u32 vcc,1,v33\ns_nop 1\nM v[96:99],a[24:27],a[48:51],v[96:99]\ns_add_u32 m0,s50,86016\nds_read_b64_tr_b16 a[100:101],v11 offset:49152\nv_cndmask_b32 v53,0,v34,vcc\n" \
"M v[100:103],a[28:31],a[48:51],v[100:103]\nbuffer_load_dwordx4 v18,s[28:31],s46 offen lds\nM v[104:107],a[24:27],a[52:55],v[104:107]\nds_read_b64_tr_b16 a[102:103],v12 offset:49152\nv_cmp_eq_u32 vcc,2,v33\ns_nop 1\nM v[108:111],a[28:31],a[52:55],v[108:111]\nv_cndmask_b32 v54,0,v34,vcc\n" \
"v_cmp_eq_u32 vcc,3,v33\ns_nop 1\nM v[112:115],a[24:27],a[56:59],v[112:115]\nds_read_b128 a[72:75],v6 offset:4096\nv_cndmask_b32 v55,0,v34,vcc\nM v[116:119],a[28:31],a[56:59],v[116:119]\nM v[120:123],a[24:27],a[60:63],v[120:123]\ns_add_u32 m0,s50,90112\n" \
"ds_read_b64_tr_b16 a[104:105],v9 offset:49664\nM v[124:127],a[28:31],a[60:63],v[124:127]\nbuffer_load_dwordx4 v19,s[28:31],s46 offen lds\nM v[128:131],a[16:19],a[32:35],v[128:131]\nds_read_b64_tr_b16 a[106:107],v10 offset:49664\nM v[132:135],a[20:23],a[32:35],v[132:135]\nM v[136:139],a[16:19],a[36:39],v[136:139]\nds_read_b128 a[76:79],v6 offset:6144\n" \
"M v[140:143],a[20:23],a[36:39],v[140:143]\nM v[144:147],a[16:19],a[40:43],v[144:147]\ns_add_u32 m0,s50,94208\nds_read_b64_tr_b16 a[108:109],v11 offset:49664\nM v[148:151],a[20:23],a[40:43],v[148:151]\nbuffer_load_dwordx4 v20,s[28:31],s46 offen lds\nM v[152:155],a[16:19],a[44:47],v[152:155]\nds_read_b64_tr_b16 a[110:111],v12 offset:49664\n" \
"M v[156:159],a[20:23],a[44:47],v[156:159]\nM v[160:163],a[16:19],a[48:51],v[160:163]\nds_read_b128 a[80:83],v6 offset:8192\nM v[164:167],a[20:23],a[48:51],v[164:167]\nM v[168:171],a[16:19],a[52:55],v[168:171]\ns_mov_b32 s47,0\ns_add_u32 m0,s51,32768\nds_read_b64_tr_b16 a[112:113],v9 offset:50176\n" \
"M v[172:175],a[20:23],a[52:55],v[172:175]\nbuffer_load_dwordx4 v21,s[32:35],s47 offen offset:0 lds\nM v[176:179],a[16:19],a[56:59],v[176:179]\nds_read_b64_tr_b16 a[114:115],v10 offset:50176\nM v[180:183],a[20:23],a[56:59],v[180:183]\nM v[184:187],a[16:19],a[60:63],v[184:187]\nds_read_b128 a[84:87],v6 offset:10240\nM v[188:191],a[20:23],a[60:63],v[188:191]\n" \
"M v[192:195],a[0:3],a[32:35],v[192:195]\ns_add_u32 m0,s51,33664\nds_read_b64_tr_b16 a[116:117],v11 offset:50176\nM v[196:199],a[4:7],a[32:35],v[196:199]\nbuffer_load_dwordx4 v21,s[32:35],s47 offen offset:128 lds\nM v[200:203],a[0:3],a[36:39],v[200:203]\nds_read_b64_tr_b16 a[118:119],v12 offset:50176\nM v[204:207],a[4:7],a[36:39],v[204:207]\n" \
"M v[208:211],a[0:3],a[40:43],v[208:211]\nds_read_b128 a[88:91],v6 offset:12288\nM v[212:215],a[4:7],a[40:43],v[212:215]\nM v[216:219],a[0:3],a[44:47],v[216:219]\ns_mov_b32 s47,262144\ns_add_u32 m0,s51,40960\nds_read_b64_tr_b16 a[120:121],v9 offset:50688\nM v[220:223],a[4:7],a[44:47],v[220:223]\n" \
"buffer_load_dwordx4 v21,s[32:35],s47 offen offset:0 lds\nM v[224:227],a[0:3],a[48:51],v[224:227]\nds_read_b64_tr_b16 a[122:123],v10 offset:50688\nM v[228:231],a[4:7],a[48:51],v[228:231]\nM v[232:235],a[0:3],a[52:55],v[232:235]\nds_read_b128 a[92:95],v6 offset:14336\nM v[236:239],a[4:7],a[52:55],v[236:239]\nM v[240:243],a[0:3],a[56:59],v[240:243]\n" \
"s_add_u32 m0,s51,41856\nds_read_b64_tr_b16 a[124:125],v11 offset:50688\nM v[244:247],a[4:7],a[56:59],v[244:247]\nbuffer_load_dwordx4 v21,s[32:35],s47 offen offset:128 lds\nM v[248:251],a[0:3],a[60:63],v[248:251]\nds_read_b64_tr_b16 a[126:127],v12 offset:50688\nM v[252:255],a[4:7],a[60:63],v[252:255]\nv_add_u32 v5,s54,v5\n" \
"M a[128:131],a[8:11],a[32:35],a[128:131]\ns_add_u32 s76,s76,2097152\ns_addc_u32 s77,s77,0\nv_add_u32 v6,s54,v6\nM a[132:135],a[12:15],a[32:35],a[132:135]\ns_mov_b64 exec,0xffff\nglobal_load_dword v0,v1,s[76:77]\ns_mov_b64 exec,-1\n" \
"M a[136:139],a[8:11],a[36:39],a[136:139]\nv_add_u32 v7,s54,v7\nM a[140:143],a[12:15],a[36:39],a[140:143]\nv_add_u32 v8,s54,v8\nM a[144:147],a[8:11],a[40:43],a[144:147]\nv_sub_u32 v9,v9,v62\nM a[148:151],a[12:15],a[40:43],a[148:151]\nM a[152:155],a[8:11],a[44:47],a[152:155]\n" \
"v_sub_u32 v10,v10,v62\nM a[156:159],a[12:15],a[44:47],a[156:159]\ns_waitcnt lgkmcnt(0)\nM a[160:163],a[8:11],a[48:51],a[160:163]\ns_barrier\nM a[164:167],a[12:15],a[48:51],a[164:167]\nv_sub_u32 v11,v11,v62\nM a[168:171],a[8:11],a[52:55],a[168:171]\n" \
"v_sub_u32 v12,v12,v62\nM a[172:175],a[12:15],a[52:55],a[172:175]\nM a[176:179],a[8:11],a[56:59],a[176:179]\nM a[180:183],a[12:15],a[56:59],a[180:183]\nM a[184:187],a[8:11],a[60:63],a[184:187]\nM a[188:191],a[12:15],a[60:63],a[188:191]\nM v[64:67],a[88:91],a[96:99],v[64:67]\ns_waitcnt vmcnt(13) lgkmcnt(0)\n" \
"M v[68:71],a[92:95],a[96:99],v[68:71]\nM v[72:75],a[88:91],a[100:103],v[72:75]\ns_mov_b32 s46,128\ns_add_u32 m0,s50,0\nds_read_b128 a[32:35],v50 offset:512\nM v[76:79],a[92:95],a[100:103],v[76:79]\nbuffer_load_dwordx4 v13,s[28:31],s46 offen lds\ns_add_u32 s96,s57,49152\n" \
"M v[80:83],a[88:91],a[104:107],v[80:83]\nds_read2_b32 a[0:1],v46 offset0:0 offset1:1\nv_mul_f32 v64,s20,v64\nM v[84:87],a[92:95],a[104:107],v[84:87]\nv_mul_f32 v65,s20,v65\nv_mul_f32 v66,s20,v66\nM v[88:91],a[88:91],a[108:111],v[88:91]\ns_add_u32 m0,s50,4096\n" \
"ds_read2_b32 a[2:3],v46 offset0:2 offset1:3\nv_mul_f32 v67,s20,v67\nM v[92:95],a[92:95],a[108:111],v[92:95]\nbuffer_load_dwordx4 v14,s[28:31],s46 offen lds\nM v[96:99],a[88:91],a[112:115],v[96:99]\nds_read_b128 a[36:39],v50 offset:1024\nC v30,v64,v65\nM v[100:103],a[92:95],a[112:115],v[100:103]\n" \
"C v31,v66,v67\nv_mul_f32 v68,s20,v68\nM v[104:107],a[88:91],a[116:119],v[104:107]\ns_add_u32 m0,s50,8192\nds_read2_b32 a[4:5],v46 offset0:128 offset1:129\nv_mul_f32 v69,s20,v69\nM v[108:111],a[92:95],a[116:119],v[108:111]\nbuffer_load_dwordx4 v15,s[28:31],s46 offen lds\n" \
"M v[112:115],a[88:91],a[120:123],v[112:115]\nds_read2_b32 a[6:7],v46 offset0:130 offset1:131\nv_mul_f32 v70,s20,v70\nM v[116:119],a[92:95],a[120:123],v[116:119]\nv_mul_f32 v71,s20,v71\nC v32,v68,v69\nM v[120:123],a[88:91],a[124:127],v[120:123]\ns_add_u32 m0,s50,12288\n" \
"ds_read_b128 a[40:43],v50 offset:1536\nC v33,v70,v71\nM v[124:127],a[92:95],a[124:127],v[124:127]\nbuffer_load_dwordx4 v16,s[28:31],s46 offen lds\nM v[128:131],a[80:83],a[96:99],v[128:131]\nds_read2_b32 a[8:9],v47 offset0:0 offset1:1\nv_accvgpr_write_b32 a192,v30\nM v[132:135],a[84:87],a[96:99],v[132:135]\n" \
"v_accvgpr_write_b32 a193,v31\nv_accvgpr_write_b32 a194,v32\nM v[136:139],a[80:83],a[100:103],v[136:139]\ns_mov_b32 s46,128\ns_add_u32 m0,s50,16384\nds_read2_b32 a[10:11],v47 offset0:2 offset1:3\nM v[140:143],a[84:87],a[100:103],v[140:143]\nbuffer_load_dwordx4 v17,s[28:31],s46 offen lds\n" \
"M v[144:147],a[80:83],a[104:107],v[144:147]\nds_read_b128 a[44:47],v50 offset:2048\nv_accvgpr_write_b32 a195,v33\nM v[148:151],a[84:87],a[104:107],v[148:151]\nv_mul_f32 v72,s20,v72\nv_mul_f32 v73,s20,v73\nM v[152:155],a[80:83],a[108:111],v[152:155]\ns_add_u32 m0,s50,20480\n" \
"ds_read2_b32 a[12:13],v47 offset0:128 offset1:129\nv_mul_f32 v74,s20,v74\nM v[156:159],a[84:87],a[108:111],v[156:159]\nbuffer_load_dwordx4 v18,s[28:31],s46 offen lds\nM v[160:163],a[80:83],a[112:115],v[160:163]\nds_read2_b32 a[14:15],v47 offset0:130 offset1:131\nv_mul_f32 v75,s20,v75\nM v[164:167],a[84:87],a[112:115],v[164:167]\n" \
"C v34,v72,v73\nC v35,v74,v75\nM v[168:171],a[80:83],a[116:119],v[168:171]\ns_add_u32 m0,s50,24576\nds_read_b128 a[48:51],v50 offset:2560\nv_mul_f32 v76,s20,v76\nM v[172:175],a[84:87],a[116:119],v[172:175]\nbuffer_load_dwordx4 v19,s[28:31],s46 offen lds\n" \
"M v[176:179],a[80:83],a[120:123],v[176:179]\nds_read2_b32 a[16:17],v48 offset0:0 offset1:1\nv_mul_f32 v77,s20,v77\nM v[180:183],a[84:87],a[120:123],v[180:183]\nv_mul_f32 v78,s20,v78\nv_mul_f32 v79,s20,v79\nM v[184:187],a[80:83],a[124:127],v[184:187]\ns_add_u32 m0,s50,28672\n" \
"ds_read2_b32 a[18:19],v48 offset0:2 offset1:3\nC v36,v76,v77\nM v[188:191],a[84:87],a[124:127],v[188:191]\nbuffer_load_dwordx4 v20,s[28:31],s46 offen lds\nM v[192:195],a[64:67],a[96:99],v[192:195]\nds_read_b128 a[52:55],v50 offset:3072\nC v37,v78,v79\nM v[196:199],a[68:71],a[96:99],v[196:199]\n" \
"v_accvgpr_write_b32 a196,v34\nv_accvgpr_write_b32 a197,v35\nM v[200:203],a[64:67],a[100:103],v[200:203]\ns_mov_b32 s47,524288\ns_add_u32 m0,s51,49152\nds_read2_b32 a[20:21],v48 offset0:128 offset1:129\nM v[204:207],a[68:71],a[100:103],v[204:207]\nbuffer_load_dwordx4 v21,s[32:35],s47 offen offset:0 lds\n" \
"M v[208:211],a[64:67],a[104:107],v[208:211]\nds_read2_b32 a[22:23],v48 offset0:130 offset1:131\nv_accvgpr_write_b32 a198,v36\nM v[212:215],a[68:71],a[104:107],v[212:215]\nv_accvgpr_write_b32 a199,v37\nv_mul_f32 v80,s20,v80\nM v[216:219],a[64:67],a[108:111],v[216:219]\ns_add_u32 m0,s51,50048\n" \
"ds_read_b128 a[56:59],v50 offset:3584\nv_mul_f32 v81,s20,v81\nM v[220:223],a[68:71],a[108:111],v[220:223]\nbuffer_load_dwordx4 v21,s[32:35],s47 offen offset:128 lds\nM v[224:227],a[64:67],a[112:115],v[224:227]\nds_read2_b32 a[24:25],v49 offset0:0 offset1:1\nv_mul_f32 v82,s20,v82\nM v[228:231],a[68:71],a[112:115],v[228:231]\n" \
"v_mul_f32 v83,s20,v83\nC v30,v80,v81\nM v[232:235],a[64:67],a[116:119],v[232:235]\ns_mov_b32 s47,786432\ns_add_u32 m0,s51,57344\nds_read2_b32 a[26:27],v49 offset0:2 offset1:3\nM v[236:239],a[68:71],a[116:119],v[236:239]\nbuffer_load_dwordx4 v21,s[32:35],s47 offen offset:0 lds\n" \
"M v[240:243],a[64:67],a[120:123],v[240:243]\nds_read_b128 a[60:63],v50 offset:4096\nC v31,v82,v83\nM v[244:247],a[68:71],a[120:123],v[244:247]\nv_mul_f32 v84,s20,v84\nv_mul_f32 v85,s20,v85\nM v[248:251],a[64:67],a[124:127],v[248:251]\ns_add_u32 m0,s51,58240\n" \
"ds_read2_b32 a[28:29],v49 offset0:128 offset1:129\nv_mul_f32 v86,s20,v86\nM v[252:255],a[68:71],a[124:127],v[252:255]\nbuffer_load_dwordx4 v21,s[32:35],s47 offen offset:128 lds\nM a[128:131],a[72:75],a[96:99],a[128:131]\nds_read2_b32 a[30:31],v49 offset0:130 offset1:131\nv_mul_f32 v87,s20,v87\nM a[132:135],a[76:79],a[96:99],a[132:135]\n" \
"C v32,v84,v85\nC v33,v86,v87\nM a[136:139],a[72:75],a[100:103],a[136:139]\ns_add_u32 s76,s76,2097152\ns_addc_u32 s77,s77,0\nv_accvgpr_write_b32 a200,v30\nM a[140:143],a[76:79],a[100:103],a[140:143]\ns_mov_b64 exec,0xffff\n" \
"global_load_dword v0,v1,s[76:77]\ns_mov_b64 exec,-1\nM a[144:147],a[72:75],a[104:107],a[144:147]\nv_accvgpr_write_b32 a201,v31\nv_accvgpr_write_b32 a202,v32\nM a[148:151],a[76:79],a[104:107],a[148:151]\nv_accvgpr_write_b32 a203,v33\nv_mul_f32 v88,s20,v88\n" \
"M a[152:155],a[72:75],a[108:111],a[152:155]\nv_mul_f32 v89,s20,v89\nv_mul_f32 v90,s20,v90\nM a[156:159],a[76:79],a[108:111],a[156:159]\nv_mul_f32 v91,s20,v91\nC v34,v88,v89\nM a[160:163],a[72:75],a[112:115],a[160:163]\nC v35,v90,v91\n" \
"v_mul_f32 v92,s20,v92\nM a[164:167],a[76:79],a[112:115],a[164:167]\nv_mul_f32 v93,s20,v93\nv_mul_f32 v94,s20,v94\nM a[168:171],a[72:75],a[116:119],a[168:171]\ns_add_u32 s76,s76,2097152\ns_addc_u32 s77,s77,0\nv_mul_f32 v95,s20,v95\n" \
"M a[172:175],a[76:79],a[116:119],a[172:175]\ns_mov_b64 exec,0xffff\nglobal_load_dword v0,v1,s[76:77]\ns_mov_b64 exec,-1\nM a[176:179],a[72:75],a[120:123],a[176:179]\nC v36,v92,v93\nC v37,v94,v95\nM a[180:183],a[76:79],a[120:123],a[180:183]\n" \
"v_accvgpr_write_b32 a204,v34\nv_accvgpr_write_b32 a205,v35\nM a[184:187],a[72:75],a[124:127],a[184:187]\ns_waitcnt lgkmcnt(0)\nv_accvgpr_write_b32 a206,v36\nM a[188:191],a[76:79],a[124:127],a[188:191]\nbuffer_store_dwordx4 a[192:195],v4,s[36:39],s96 offen offset:0 sc1\nLC_%=:\n" \
"M v[64:67],v[52:55],a[32:35],0\nds_read_b128 a[96:99],v50 offset:448\nv_accvgpr_write_b32 a207,v37\nM v[68:71],v[52:55],a[36:39],0\nds_read2_b32 a[64:65],v46 offset0:16 offset1:17\nv_mul_f32 v96,s20,v96\nM v[72:75],v[52:55],a[40:43],0\nds_read2_b32 a[66:67],v46 offset0:18 offset1:19\n" \
"v_mul_f32 v97,s20,v97\nM v[76:79],v[52:55],a[44:47],0\nds_read_b128 a[100:103],v50 offset:960\nv_mul_f32 v98,s20,v98\nM v[80:83],v[52:55],a[48:51],0\nds_read2_b32 a[68:69],v46 offset0:144 offset1:145\nv_mul_f32 v99,s20,v99\nM v[84:87],v[52:55],a[52:55],0\n" \
"ds_read2_b32 a[70:71],v46 offset0:146 offset1:147\nC v30,v96,v97\nM v[88:91],v[52:55],a[56:59],0\nds_read_b128 a[104:107],v50 offset:1472\nC v31,v98,v99\nM v[92:95],v[52:55],a[60:63],0\nds_read2_b32 a[72:73],v47 offset0:16 offset1:17\nv_mul_f32 v100,s20,v100\n" \
"M v[64:67],a[0:3],a[32:35],v[64:67]\nds_read2_b32 a[74:75],v47 offset0:18 offset1:19\nv_mul_f32 v101,s20,v101\nM v[68:71],a[0:3],a[36:39],v[68:71]\nds_read_b128 a[108:111],v50 offset:1984\nv_mul_f32 v102,s20,v102\nM v[68:71],a[4:7],a[32:35],v[68:71]\nds_read2_b32 a[76:77],v47 offset0:144 offset1:145\n" \
"v_mul_f32 v103,s20,v103\nM v[72:75],a[4:7],a[36:39],v[72:75]\nds_read2_b32 a[78:79],v47 offset0:146 offset1:147\nC v32,v100,v101\nM v[72:75],a[0:3],a[40:43],v[72:75]\nds_read_b128 a[112:115],v50 offset:2496\nC v33,v102,v103\nM v[76:79],a[4:7],a[40:43],v[76:79]\n" \
"ds_read2_b32 a[80:81],v48 offset0:16 offset1:17\nv_accvgpr_write_b32 a208,v30\nM v[72:75],a[8:11],a[32:35],v[72:75]\nds_read2_b32 a[82:83],v48 offset0:18 offset1:19\nv_accvgpr_write_b32 a209,v31\nM v[76:79],a[8:11],a[36:39],v[76:79]\nds_read_b128 a[116:119],v50 offset:3008\nv_accvgpr_write_b32 a210,v32\n" \
"M v[80:83],a[8:11],a[40:43],v[80:83]\nds_read2_b32 a[84:85],v48 offset0:144 offset1:145\nv_accvgpr_write_b32 a211,v33\nM v[76:79],a[0:3],a[44:47],v[76:79]\nds_read2_b32 a[86:87],v48 offset0:146 offset1:147\nv_mul_f32 v104,s20,v104\nM v[80:83],a[4:7],a[44:47],v[80:83]\nds_read_b128 a[120:123],v50 offset:3520\n" \
"v_mul_f32 v105,s20,v105\nM v[84:87],a[8:11],a[44:47],v[84:87]\nds_read2_b32 a[88:89],v49 offset0:16 offset1:17\nv_mul_f32 v106,s20,v106\nM v[76:79],a[12:15],a[32:35],v[76:79]\nds_read2_b32 a[90:91],v49 offset0:18 offset1:19\nv_mul_f32 v107,s20,v107\nM v[80:83],a[12:15],a[36:39],v[80:83]\n" \
"LG8_%=:\ns_cbranch_execz LHR0_%=\nds_read_b128 a[124:127],v50 offset:4032\nC v34,v104,v105\nM v[84:87],a[12:15],a[40:43],v[84:87]\nds_read2_b32 a[92:93],v49 offset0:144 offset1:145\nC v35,v106,v107\nM v[88:91],a[12:15],a[44:47],v[88:91]\n" \
"ds_read2_b32 a[94:95],v49 offset0:146 offset1:147\nv_mul_f32 v108,s20,v108\nM v[80:83],a[0:3],a[48:51],v[80:83]\nv_mul_f32 v109,s20,v109\nv_mul_f32 v110,s20,v110\nM v[84:87],a[4:7],a[48:51],v[84:87]\nbuffer_store_dwordx4 a[196:199],v4,s[36:39],s96 offen offset:1024 sc1\nM v[88:91],a[8:11],a[48:51],v[88:91]\n" \
"v_mul_f32 v111,s20,v111\nC v36,v108,v109\nM v[92:95],a[12:15],a[48:51],v[92:95]\nC v37,v110,v111\nv_accvgpr_write_b32 a212,v34\nM v[80:83],a[16:19],a[32:35],v[80:83]\nv_accvgpr_write_b32 a213,v35\nv_accvgpr_write_b32 a214,v36\n" \
"M v[84:87],a[16:19],a[36:39],v[84:87]\nbuffer_store_dwordx4 a[200:203],v4,s[36:39],s96 offen offset:2048 sc1\nM v[88:91],a[16:19],a[40:43],v[88:91]\nv_accvgpr_write_b32 a215,v37\nv_mul_f32 v112,s20,v112\nM v[92:95],a[16:19],a[44:47],v[92:95]\nv_mul_f32 v113,s20,v113\nv_mul_f32 v114,s20,v114\n" \
"M v[84:87],a[0:3],a[52:55],v[84:87]\nv_mul_f32 v115,s20,v115\nC v30,v112,v113\nM v[88:91],a[4:7],a[52:55],v[88:91]\nbuffer_store_dwordx4 a[204:207],v4,s[36:39],s96 offen offset:3072 sc1\ns_add_u32 s96,s57,53248\nM v[92:95],a[8:11],a[52:55],v[92:95]\nC v31,v114,v115\n" \
"v_mul_f32 v116,s20,v116\nM v[84:87],a[20:23],a[32:35],v[84:87]\nv_mul_f32 v117,s20,v117\nv_mul_f32 v118,s20,v118\nM v[88:91],a[20:23],a[36:39],v[88:91]\nv_mul_f32 v119,s20,v119\nC v32,v116,v117\nM v[92:95],a[20:23],a[40:43],v[92:95]\n" \
"buffer_store_dwordx4 a[208:211],v4,s[36:39],s96 offen offset:0 sc1\nM v[88:91],a[0:3],a[56:59],v[88:91]\nC v33,v118,v119\nv_accvgpr_write_b32 a216,v30\nM v[92:95],a[4:7],a[56:59],v[92:95]\nv_accvgpr_write_b32 a217,v31\nv_accvgpr_write_b32 a218,v32\nM v[88:91],a[24:27],a[32:35],v[88:91]\n" \
"v_accvgpr_write_b32 a219,v33\nv_mul_f32 v120,s20,v120\nM v[92:95],a[24:27],a[36:39],v[92:95]\nbuffer_store_dwordx4 a[212:215],v4,s[36:39],s96 offen offset:1024 sc1\nM v[92:95],a[0:3],a[60:63],v[92:95]\ns_waitcnt lgkmcnt(0)\nv_mul_f32 v121,s20,v121\nM v[92:95],a[28:31],a[32:35],v[92:95]\n" \
"v_mul_f32 v122,s20,v122\nv_mul_f32 v123,s20,v123\nM v[64:67],a[64:67],a[96:99],v[64:67]\nds_read_b128 a[32:35],v50 offset:384\nC v34,v120,v121\nM v[68:71],a[64:67],a[100:103],v[68:71]\nds_read2_b32 a[0:1],v46 offset0:32 offset1:33\nC v35,v122,v123\n" \
"M v[68:71],a[68:71],a[96:99],v[68:71]\nds_read2_b32 a[2:3],v46 offset0:34 offset1:35\nv_mul_f32 v124,s20,v124\nM v[72:75],a[68:71],a[100:103],v[72:75]\nds_read_b128 a[36:39],v50 offset:896\nv_mul_f32 v125,s20,v125\nM v[72:75],a[64:67],a[104:107],v[72:75]\nds_read2_b32 a[4:5],v46 offset0:160 offset1:161\n" \
"v_mul_f32 v126,s20,v126\nM v[76:79],a[68:71],a[104:107],v[76:79]\nds_read2_b32 a[6:7],v46 offset0:162 offset1:163\nv_mul_f32 v127,s20,v127\nM v[72:75],a[72:75],a[96:99],v[72:75]\nds_read_b128 a[40:43],v50 offset:1408\nC v36,v124,v125\nM v[76:79],a[72:75],a[100:103],v[76:79]\n" \
"ds_read2_b32 a[8:9],v47 offset0:32 offset1:33\nC v37,v126,v127\nM v[80:83],a[72:75],a[104:107],v[80:83]\nds_read2_b32 a[10:11],v47 offset0:34 offset1:35\nv_accvgpr_write_b32 a220,v34\nM v[76:79],a[64:67],a[108:111],v[76:79]\nds_read_b128 a[44:47],v50 offset:1920\nv_accvgpr_write_b32 a221,v35\n" \
"M v[80:83],a[68:71],a[108:111],v[80:83]\nds_read2_b32 a[12:13],v47 offset0:160 offset1:161\nv_accvgpr_write_b32 a222,v36\nM v[84:87],a[72:75],a[108:111],v[84:87]\nds_read2_b32 a[14:15],v47 offset0:162 offset1:163\nv_accvgpr_write_b32 a223,v37\nM v[76:79],a[76:79],a[96:99],v[76:79]\nds_read_b128 a[48:51],v50 offset:2432\n" \
"v_mul_f32 v128,s20,v128\nM v[80:83],a[76:79],a[100:103],v[80:83]\nds_read2_b32 a[16:17],v48 offset0:32 offset1:33\nv_mul_f32 v129,s20,v129\nM v[84:87],a[76:79],a[104:107],v[84:87]\nds_read2_b32 a[18:19],v48 offset0:34 offset1:35\nv_mul_f32 v130,s20,v130\nM v[88:91],a[76:79],a[108:111],v[88:91]\n" \
"ds_read_b128 a[52:55],v50 offset:2944\nv_mul_f32 v131,s20,v131\nM v[80:83],a[64:67],a[112:115],v[80:83]\nds_read2_b32 a[20:21],v48 offset0:160 offset1:161\nC v30,v128,v129\nM v[84:87],a[68:71],a[112:115],v[84:87]\nds_read2_b32 a[22:23],v48 offset0:162 offset1:163\nC v31,v130,v131\n" \
"M v[88:91],a[72:75],a[112:115],v[88:91]\nds_read_b128 a[56:59],v50 offset:3456\nv_mul_f32 v132,s20,v132\nM v[92:95],a[76:79],a[112:115],v[92:95]\nds_read2_b32 a[24:25],v49 offset0:32 offset1:33\nv_mul_f32 v133,s20,v133\nM v[80:83],a[80:83],a[96:99],v[80:83]\nds_read2_b32 a[26:27],v49 offset0:34 offset1:35\n" \
"v_mul_f32 v134,s20,v134\nM v[84:87],a[80:83],a[100:103],v[84:87]\nds_read_b128 a[60:63],v50 offset:3968\nv_mul_f32 v135,s20,v135\nM v[88:91],a[80:83],a[104:107],v[88:91]\nds_read2_b32 a[28:29],v49 offset0:160 offset1:161\nC v32,v132,v133\nM v[92:95],a[80:83],a[108:111],v[92:95]\n" \
"ds_read2_b32 a[30:31],v49 offset0:162 offset1:163\nC v33,v134,v135\nM v[84:87],a[64:67],a[116:119],v[84:87]\nv_accvgpr_write_b32 a224,v30\nv_accvgpr_write_b32 a225,v31\nM v[88:91],a[68:71],a[116:119],v[88:91]\nbuffer_store_dwordx4 a[216:219],v4,s[36:39],s96 offen offset:2048 sc1\nM v[92:95],a[72:75],a[116:119],v[92:95]\n" \
"v_accvgpr_write_b32 a226,v32\nv_accvgpr_write_b32 a227,v33\nM v[84:87],a[84:87],a[96:99],v[84:87]\nv_mul_f32 v136,s20,v136\nv_mul_f32 v137,s20,v137\nM v[88:91],a[84:87],a[100:103],v[88:91]\nv_mul_f32 v138,s20,v138\nv_mul_f32 v139,s20,v139\n" \
"M v[92:95],a[84:87],a[104:107],v[92:95]\nbuffer_store_dwordx4 a[220:223],v4,s[36:39],s96 offen offset:3072 sc1\ns_add_u32 s96,s57,32768\nM v[88:91],a[64:67],a[120:123],v[88:91]\nC v34,v136,v137\nC v35,v138,v139\nM v[92:95],a[68:71],a[120:123],v[92:95]\nv_mul_f32 v140,s20,v140\n" \
"v_mul_f32 v141,s20,v141\nM v[88:91],a[88:91],a[96:99],v[88:91]\nv_mul_f32 v142,s20,v142\nv_mul_f32 v143,s20,v143\nM v[92:95],a[88:91],a[100:103],v[92:95]\nbuffer_store_dwordx4 a[224:227],v4,s[36:39],s96 offen offset:0 sc1\nM v[92:95],a[64:67],a[124:127],v[92:95]\ns_waitcnt lgkmcnt(0)\n" \
"C v36,v140,v141\nM v[92:95],a[92:95],a[96:99],v[92:95]\nC v37,v142,v143\nv_accvgpr_write_b32 a228,v34\nM v[64:67],a[0:3],a[32:35],v[64:67]\nds_read_b128 a[96:99],v50 offset:320\nv_accvgpr_write_b32 a229,v35\nM v[68:71],a[0:3],a[36:39],v[68:71]\n" \
"ds_read2_b32 a[64:65],v46 offset0:48 offset1:49\nv_accvgpr_write_b32 a230,v36\nM v[68:71],a[4:7],a[32:35],v[68:71]\nds_read2_b32 a[66:67],v46 offset0:50 offset1:51\nv_accvgpr_write_b32 a231,v37\nM v[72:75],a[4:7],a[36:39],v[72:75]\nds_read_b128 a[100:103],v50 offset:832\nv_mul_f32 v144,s20,v144\n" \
"M v[72:75],a[0:3],a[40:43],v[72:75]\nds_read2_b32 a[68:69],v46 offset0:176 offset1:177\nv_mul_f32 v145,s20,v145\nM v[76:79],a[4:7],a[40:43],v[76:79]\nds_read2_b32 a[70:71],v46 offset0:178 offset1:179\nv_mul_f32 v146,s20,v146\nM v[72:75],a[8:11],a[32:35],v[72:75]\nds_read_b128 a[104:107],v50 offset:1344\n" \
"v_mul_f32 v147,s20,v147\nM v[76:79],a[8:11],a[36:39],v[76:79]\nds_read2_b32 a[72:73],v47 offset0:48 offset1:49\nC v30,v144,v145\nM v[80:83],a[8:11],a[40:43],v[80:83]\nds_read2_b32 a[74:75],v47 offset0:50 offset1:51\nC v31,v146,v147\nM v[76:79],a[0:3],a[44:47],v[76:79]\n" \
"ds_read_b128 a[108:111],v50 offset:1856\nv_mul_f32 v148,s20,v148\nM v[80:83],a[4:7],a[44:47],v[80:83]\nds_read2_b32 a[76:77],v47 offset0:176 offset1:177\nv_mul_f32 v149,s20,v149\nM v[84:87],a[8:11],a[44:47],v[84:87]\nds_read2_b32 a[78:79],v47 offset0:178 offset1:179\nv_mul_f32 v150,s20,v150\n" \
"M v[76:79],a[12:15],a[32:35],v[76:79]\nds_read_b128 a[112:115],v50 offset:2368\nv_mul_f32 v151,s20,v151\nM v[80:83],a[12:15],a[36:39],v[80:83]\nds_read2_b32 a[80:81],v48 offset0:48 offset1:49\nC v32,v148,v149\nM v[84:87],a[12:15],a[40:43],v[84:87]\nds_read2_b32 a[82:83],v48 offset0:50 offset1:51\n" \
"C v33,v150,v151\nM v[88:91],a[12:15],a[44:47],v[88:91]\nds_read_b128 a[116:119],v50 offset:2880\nv_accvgpr_write_b32 a232,v30\nM v[80:83],a[0:3],a[48:51],v[80:83]\nds_read2_b32 a[84:85],v48 offset0:176 offset1:177\nv_accvgpr_write_b32 a233,v31\nM v[84:87],a[4:7],a[48:51],v[84:87]\n" \
"ds_read2_b32 a[86:87],v48 offset0:178 offset1:179\nv_accvgpr_write_b32 a234,v32\nM v[88:91],a[8:11],a[48:51],v[88:91]\nds_read_b128 a[120:123],v50 offset:3392\nv_accvgpr_write_b32 a235,v33\nM v[92:95],a[12:15],a[48:51],v[92:95]\nds_read2_b32 a[88:89],v49 offset0:48 offset1:49\nv_mul_f32 v152,s20,v152\n" \
"M v[80:83],a[16:19],a[32:35],v[80:83]\nds_read2_b32 a[90:91],v49 offset0:50 offset1:51\nv_mul_f32 v153,s20,v153\nM v[84:87],a[16:19],a[36:39],v[84:87]\nds_read_b128 a[124:127],v50 offset:3904\nv_mul_f32 v154,s20,v154\nM v[88:91],a[16:19],a[40:43],v[88:91]\nds_read2_b32 a[92:93],v49 offset0:176 offset1:177\n" \
"v_mul_f32 v155,s20,v155\nM v[92:95],a[16:19],a[44:47],v[92:95]\nds_read2_b32 a[94:95],v49 offset0:178 offset1:179\nC v34,v152,v153\nM v[84:87],a[0:3],a[52:55],v[84:87]\nC v35,v154,v155\nv_mul_f32 v156,s20,v156\nM v[88:91],a[4:7],a[52:55],v[88:91]\n" \
"buffer_store_dwordx4 a[228:231],v4,s[36:39],s96 offen offset:1024 sc1\nM v[92:95],a[8:11],a[52:55],v[92:95]\nv_mul_f32 v157,s20,v157\nv_mul_f32 v158,s20,v158\nM v[84:87],a[20:23],a[32:35],v[84:87]\nv_mul_f32 v159,s20,v159\nC v36,v156,v157\nM v[88:91],a[20:23],a[36:39],v[88:91]\n" \
"C v37,v158,v159\nv_accvgpr_write_b32 a236,v34\nM v[92:95],a[20:23],a[40:43],v[92:95]\nbuffer_store_dwordx4 a[232:235],v4,s[36:39],s96 offen offset:2048 sc1\nM v[88:91],a[0:3],a[56:59],v[88:91]\nv_accvgpr_write_b32 a237,v35\nv_accvgpr_write_b32 a238,v36\nM v[92:95],a[4:7],a[56:59],v[92:95]\n" \
"v_accvgpr_write_b32 a239,v37\nv_mul_f32 v160,s20,v160\nM v[88:91],a[24:27],a[32:35],v[88:91]\nv_mul_f32 v161,s20,v161\nv_mul_f32 v162,s20,v162\nM v[92:95],a[24:27],a[36:39],v[92:95]\nbuffer_store_dwordx4 a[236:239],v4,s[36:39],s96 offen offset:3072 sc1\ns_add_u32 s96,s57,36864\n" \
"M v[92:95],a[0:3],a[60:63],v[92:95]\ns_waitcnt lgkmcnt(0)\nv_mul_f32 v163,s20,v163\nM v[92:95],a[28:31],a[32:35],v[92:95]\nC v30,v160,v161\nC v31,v162,v163\nM v[64:67],a[64:67],a[96:99],v[64:67]\nds_read_b128 a[32:35],v50 offset:256\n" \
"v_mul_f32 v164,s20,v164\nM v[68:71],a[64:67],a[100:103],v[68:71]\nds_read2_b32 a[0:1],v46 offset0:64 offset1:65\nv_mul_f32 v165,s20,v165\nM v[68:71],a[68:71],a[96:99],v[68:71]\nds_read2_b32 a[2:3],v46 offset0:66 offset1:67\nv_mul_f32 v166,s20,v166\nM v[72:75],a[68:71],a[100:103],v[72:75]\n" \
"ds_read_b128 a[36:39],v50 offset:768\nv_mul_f32 v167,s20,v167\nM v[72:75],a[64:67],a[104:107],v[72:75]\nds_read2_b32 a[4:5],v46 offset0:192 offset1:193\nC v32,v164,v165\nM v[76:79],a[68:71],a[104:107],v[76:79]\nds_read2_b32 a[6:7],v46 offset0:194 offset1:195\nC v33,v166,v167\n" \
"M v[72:75],a[72:75],a[96:99],v[72:75]\nds_read_b128 a[40:43],v50 offset:1280\nv_accvgpr_write_b32 a240,v30\nM v[76:79],a[72:75],a[100:103],v[76:79]\nds_read2_b32 a[8:9],v47 offset0:64 offset1:65\nv_accvgpr_write_b32 a241,v31\nM v[80:83],a[72:75],a[104:107],v[80:83]\nds_read2_b32 a[10:11],v47 offset0:66 offset1:67\n" \
"v_accvgpr_write_b32 a242,v32\nM v[76:79],a[64:67],a[108:111],v[76:79]\nds_read_b128 a[44:47],v50 offset:1792\nv_accvgpr_write_b32 a243,v33\nM v[80:83],a[68:71],a[108:111],v[80:83]\nds_read2_b32 a[12:13],v47 offset0:192 offset1:193\nv_mul_f32 v168,s20,v168\nM v[84:87],a[72:75],a[108:111],v[84:87]\n" \
"ds_read2_b32 a[14:15],v47 offset0:194 offset1:195\nv_mul_f32 v169,s20,v169\nM v[76:79],a[76:79],a[96:99],v[76:79]\nds_read_b128 a[48:51],v50 offset:2304\nv_mul_f32 v170,s20,v170\nM v[80:83],a[76:79],a[100:103],v[80:83]\nds_read2_b32 a[16:17],v48 offset0:64 offset1:65\nv_mul_f32 v171,s20,v171\n" \
"M v[84:87],a[76:79],a[104:107],v[84:87]\nds_read2_b32 a[18:19],v48 offset0:66 offset1:67\nC v34,v168,v169\nM v[88:91],a[76:79],a[108:111],v[88:91]\nds_read_b128 a[52:55],v50 offset:2816\nC v35,v170,v171\nM v[80:83],a[64:67],a[112:115],v[80:83]\nds_read2_b32 a[20:21],v48 offset0:192 offset1:193\n" \
"v_mul_f32 v172,s20,v172\nM v[84:87],a[68:71],a[112:115],v[84:87]\nds_read2_b32 a[22:23],v48 offset0:194 offset1:195\nv_mul_f32 v173,s20,v173\nM v[88:91],a[72:75],a[112:115],v[88:91]\nds_read_b128 a[56:59],v50 offset:3328\nv_mul_f32 v174,s20,v174\nM v[92:95],a[76:79],a[112:115],v[92:95]\n" \
"ds_read2_b32 a[24:25],v49 offset0:64 offset1:65\nv_mul_f32 v175,s20,v175\nM v[80:83],a[80:83],a[96:99],v[80:83]\nds_read2_b32 a[26:27],v49 offset0:66 offset1:67\nC v36,v172,v173\nM v[84:87],a[80:83],a[100:103],v[84:87]\nds_read_b128 a[60:63],v50 offset:3840\nC v37,v174,v175\n" \
"M v[88:91],a[80:83],a[104:107],v[88:91]\nds_read2_b32 a[28:29],v49 offset0:192 offset1:193\nv_accvgpr_write_b32 a244,v34\nM v[92:95],a[80:83],a[108:111],v[92:95]\nds_read2_b32 a[30:31],v49 offset0:194 offset1:195\nv_accvgpr_write_b32 a245,v35\nM v[84:87],a[64:67],a[116:119],v[84:87]\nv_accvgpr_write_b32 a246,v36\n" \
"v_accvgpr_write_b32 a247,v37\nM v[88:91],a[68:71],a[116:119],v[88:91]\nbuffer_store_dwordx4 a[240:243],v4,s[36:39],s96 offen offset:0 sc1\nM v[92:95],a[72:75],a[116:119],v[92:95]\nv_mul_f32 v176,s20,v176\nv_mul_f32 v177,s20,v177\nM v[84:87],a[84:87],a[96:99],v[84:87]\nv_mul_f32 v178,s20,v178\n" \
"v_mul_f32 v179,s20,v179\nM v[88:91],a[84:87],a[100:103],v[88:91]\nC v30,v176,v177\nC v31,v178,v179\nM v[92:95],a[84:87],a[104:107],v[92:95]\nbuffer_store_dwordx4 a[244:247],v4,s[36:39],s96 offen offset:1024 sc1\nM v[88:91],a[64:67],a[120:123],v[88:91]\nv_mul_f32 v180,s20,v180\n" \
"v_mul_f32 v181,s20,v181\nM v[92:95],a[68:71],a[120:123],v[92:95]\nv_mul_f32 v182,s20,v182\nv_mul_f32 v183,s20,v183\nM v[88:91],a[88:91],a[96:99],v[88:91]\nC v32,v180,v181\nC v33,v182,v183\nM v[92:95],a[88:91],a[100:103],v[92:95]\n" \
"v_accvgpr_write_b32 a248,v30\nv_accvgpr_write_b32 a249,v31\nM v[92:95],a[64:67],a[124:127],v[92:95]\ns_waitcnt lgkmcnt(0)\nv_accvgpr_write_b32 a250,v32\nM v[92:95],a[92:95],a[96:99],v[92:95]\nv_accvgpr_write_b32 a251,v33\nv_mul_f32 v184,s20,v184\n" \
"M v[64:67],a[0:3],a[32:35],v[64:67]\nds_read_b128 a[96:99],v50 offset:192\nv_mul_f32 v185,s20,v185\nM v[68:71],a[0:3],a[36:39],v[68:71]\nds_read2_b32 a[64:65],v46 offset0:80 offset1:81\nv_mul_f32 v186,s20,v186\nM v[68:71],a[4:7],a[32:35],v[68:71]\nds_read2_b32 a[66:67],v46 offset0:82 offset1:83\n" \
"v_mul_f32 v187,s20,v187\nM v[72:75],a[4:7],a[36:39],v[72:75]\nds_read_b128 a[100:103],v50 offset:704\nC v34,v184,v185\nM v[72:75],a[0:3],a[40:43],v[72:75]\nds_read2_b32 a[68:69],v46 offset0:208 offset1:209\nC v35,v186,v187\nM v[76:79],a[4:7],a[40:43],v[76:79]\n" \
"ds_read2_b32 a[70:71],v46 offset0:210 offset1:211\nv_mul_f32 v188,s20,v188\nM v[72:75],a[8:11],a[32:35],v[72:75]\nds_read_b128 a[104:107],v50 offset:1216\nv_mul_f32 v189,s20,v189\nM v[76:79],a[8:11],a[36:39],v[76:79]\nds_read2_b32 a[72:73],v47 offset0:80 offset1:81\nv_mul_f32 v190,s20,v190\n" \
"M v[80:83],a[8:11],a[40:43],v[80:83]\nds_read2_b32 a[74:75],v47 offset0:82 offset1:83\nv_mul_f32 v191,s20,v191\nM v[76:79],a[0:3],a[44:47],v[76:79]\nds_read_b128 a[108:111],v50 offset:1728\nC v36,v188,v189\nM v[80:83],a[4:7],a[44:47],v[80:83]\nds_read2_b32 a[76:77],v47 offset0:208 offset1:209\n" \
"C v37,v190,v191\nM v[84:87],a[8:11],a[44:47],v[84:87]\nds_read2_b32 a[78:79],v47 offset0:210 offset1:211\nv_accvgpr_write_b32 a252,v34\nM v[76:79],a[12:15],a[32:35],v[76:79]\nds_read_b128 a[112:115],v50 offset:2240\nv_accvgpr_write_b32 a253,v35\nM v[80:83],a[12:15],a[36:39],v[80:83]\n" \
"ds_read2_b32 a[80:81],v48 offset0:80 offset1:81\nv_accvgpr_write_b32 a254,v36\nM v[84:87],a[12:15],a[40:43],v[84:87]\nds_read2_b32 a[82:83],v48 offset0:82 offset1:83\nv_accvgpr_write_b32 a255,v37\nM v[88:91],a[12:15],a[44:47],v[88:91]\nds_read_b128 a[116:119],v50 offset:2752\nv_mul_f32 v192,s20,v192\n" \
"M v[80:83],a[0:3],a[48:51],v[80:83]\nds_read2_b32 a[84:85],v48 offset0:208 offset1:209\nv_mul_f32 v193,s20,v193\nM v[84:87],a[4:7],a[48:51],v[84:87]\nds_read2_b32 a[86:87],v48 offset0:210 offset1:211\nv_mul_f32 v194,s20,v194\nM v[88:91],a[8:11],a[48:51],v[88:91]\nds_read_b128 a[120:123],v50 offset:3264\n" \
"v_mul_f32 v195,s20,v195\nM v[92:95],a[12:15],a[48:51],v[92:95]\nds_read2_b32 a[88:89],v49 offset0:80 offset1:81\nC v192,v192,v193\nM v[80:83],a[16:19],a[32:35],v[80:83]\nds_read2_b32 a[90:91],v49 offset0:82 offset1:83\nC v193,v194,v195\nM v[84:87],a[16:19],a[36:39],v[84:87]\n" \
"ds_read_b128 a[124:127],v50 offset:3776\nv_mul_f32 v196,s20,v196\nM v[88:91],a[16:19],a[40:43],v[88:91]\nds_read2_b32 a[92:93],v49 offset0:208 offset1:209\nv_mul_f32 v197,s20,v197\nM v[92:95],a[16:19],a[44:47],v[92:95]\nds_read2_b32 a[94:95],v49 offset0:210 offset1:211\nv_mul_f32 v198,s20,v198\n" \
"M v[84:87],a[0:3],a[52:55],v[84:87]\nv_mul_f32 v199,s20,v199\nC v194,v196,v197\nM v[88:91],a[4:7],a[52:55],v[88:91]\nbuffer_store_dwordx4 a[248:251],v4,s[36:39],s96 offen offset:2048 sc1\nM v[92:95],a[8:11],a[52:55],v[92:95]\nC v195,v198,v199\nv_mul_f32 v200,s20,v200\n" \
"M v[84:87],a[20:23],a[32:35],v[84:87]\nv_mul_f32 v201,s20,v201\nv_mul_f32 v202,s20,v202\nM v[88:91],a[20:23],a[36:39],v[88:91]\nv_mul_f32 v203,s20,v203\nC v196,v200,v201\nM v[92:95],a[20:23],a[40:43],v[92:95]\nbuffer_store_dwordx4 a[252:255],v4,s[36:39],s96 offen offset:3072 sc1\n" \
"s_add_u32 s96,s57,0\nM v[88:91],a[0:3],a[56:59],v[88:91]\nC v197,v202,v203\nv_mul_f32 v204,s20,v204\nM v[92:95],a[4:7],a[56:59],v[92:95]\nv_mul_f32 v205,s20,v205\nv_mul_f32 v206,s20,v206\nM v[88:91],a[24:27],a[32:35],v[88:91]\n" \
"v_mul_f32 v207,s20,v207\nC v198,v204,v205\nM v[92:95],a[24:27],a[36:39],v[92:95]\nbuffer_store_dwordx4 v[192:195],v4,s[36:39],s96 offen offset:0 sc1\nM v[92:95],a[0:3],a[60:63],v[92:95]\ns_waitcnt lgkmcnt(0)\nC v199,v206,v207\nM v[92:95],a[28:31],a[32:35],v[92:95]\n" \
"v_mul_f32 v208,s20,v208\nv_mul_f32 v209,s20,v209\nM v[64:67],a[64:67],a[96:99],v[64:67]\nds_read_b128 a[32:35],v50 offset:128\nv_mul_f32 v210,s20,v210\nM v[68:71],a[64:67],a[100:103],v[68:71]\nds_read2_b32 a[0:1],v46 offset0:96 offset1:97\nv_mul_f32 v211,s20,v211\n" \
"M v[68:71],a[68:71],a[96:99],v[68:71]\nds_read2_b32 a[2:3],v46 offset0:98 offset1:99\nC v200,v208,v209\nM v[72:75],a[68:71],a[100:103],v[72:75]\nds_read_b128 a[36:39],v50 offset:640\nC v201,v210,v211\nM v[72:75],a[64:67],a[104:107],v[72:75]\nds_read2_b32 a[4:5],v46 offset0:224 offset1:225\n" \
"v_mul_f32 v212,s20,v212\nM v[76:79],a[68:71],a[104:107],v[76:79]\nds_read2_b32 a[6:7],v46 offset0:226 offset1:227\nv_mul_f32 v213,s20,v213\nM v[72:75],a[72:75],a[96:99],v[72:75]\nds_read_b128 a[40:43],v50 offset:1152\nv_mul_f32 v214,s20,v214\nM v[76:79],a[72:75],a[100:103],v[76:79]\n" \
"ds_read2_b32 a[8:9],v47 offset0:96 offset1:97\nv_mul_f32 v215,s20,v215\nM v[80:83],a[72:75],a[104:107],v[80:83]\nds_read2_b32 a[10:11],v47 offset0:98 offset1:99\nC v202,v212,v213\nM v[76:79],a[64:67],a[108:111],v[76:79]\nds_read_b128 a[44:47],v50 offset:1664\nC v203,v214,v215\n" \
"M v[80:83],a[68:71],a[108:111],v[80:83]\nds_read2_b32 a[12:13],v47 offset0:224 offset1:225\nv_mul_f32 v216,s20,v216\nM v[84:87],a[72:75],a[108:111],v[84:87]\nds_read2_b32 a[14:15],v47 offset0:226 offset1:227\nv_mul_f32 v217,s20,v217\nM v[76:79],a[76:79],a[96:99],v[76:79]\nds_read_b128 a[48:51],v50 offset:2176\n" \
"v_mul_f32 v218,s20,v218\nM v[80:83],a[76:79],a[100:103],v[80:83]\nds_read2_b32 a[16:17],v48 offset0:96 offset1:97\nv_mul_f32 v219,s20,v219\nM v[84:87],a[76:79],a[104:107],v[84:87]\nds_read2_b32 a[18:19],v48 offset0:98 offset1:99\nC v204,v216,v217\nM v[88:91],a[76:79],a[108:111],v[88:91]\n" \
"ds_read_b128 a[52:55],v50 offset:2688\nC v205,v218,v219\nM v[80:83],a[64:67],a[112:115],v[80:83]\nds_read2_b32 a[20:21],v48 offset0:224 offset1:225\nv_mul_f32 v220,s20,v220\nM v[84:87],a[68:71],a[112:115],v[84:87]\nds_read2_b32 a[22:23],v48 offset0:226 offset1:227\nv_mul_f32 v221,s20,v221\n" \
"M v[88:91],a[72:75],a[112:115],v[88:91]\nds_read_b128 a[56:59],v50 offset:3200\nv_mul_f32 v222,s20,v222\nM v[92:95],a[76:79],a[112:115],v[92:95]\nds_read2_b32 a[24:25],v49 offset0:96 offset1:97\nv_mul_f32 v223,s20,v223\nM v[80:83],a[80:83],a[96:99],v[80:83]\nds_read2_b32 a[26:27],v49 offset0:98 offset1:99\n" \
"C v206,v220,v221\nM v[84:87],a[80:83],a[100:103],v[84:87]\nds_read_b128 a[60:63],v50 offset:3712\nC v207,v222,v223\nM v[88:91],a[80:83],a[104:107],v[88:91]\nds_read2_b32 a[28:29],v49 offset0:224 offset1:225\nv_mul_f32 v224,s20,v224\nM v[92:95],a[80:83],a[108:111],v[92:95]\n" \
"ds_read2_b32 a[30:31],v49 offset0:226 offset1:227\nv_mul_f32 v225,s20,v225\nM v[84:87],a[64:67],a[116:119],v[84:87]\nv_mul_f32 v226,s20,v226\nv_mul_f32 v227,s20,v227\nM v[88:91],a[68:71],a[116:119],v[88:91]\nbuffer_store_dwordx4 v[196:199],v4,s[36:39],s96 offen offset:1024 sc1\nM v[92:95],a[72:75],a[116:119],v[92:95]\n" \
"C v208,v224,v225\nC v209,v226,v227\nM v[84:87],a[84:87],a[96:99],v[84:87]\nv_mul_f32 v228,s20,v228\nv_mul_f32 v229,s20,v229\nM v[88:91],a[84:87],a[100:103],v[88:91]\nv_mul_f32 v230,s20,v230\nv_mul_f32 v231,s20,v231\n" \
"M v[92:95],a[84:87],a[104:107],v[92:95]\nbuffer_store_dwordx4 v[200:203],v4,s[36:39],s96 offen offset:2048 sc1\nM v[88:91],a[64:67],a[120:123],v[88:91]\nLG9_%=:\ns_cbranch_execz LHR0_%=\nC v210,v228,v229\nC v211,v230,v231\nM v[92:95],a[68:71],a[120:123],v[92:95]\n" \
"v_mul_f32 v232,s20,v232\nv_mul_f32 v233,s20,v233\nM v[88:91],a[88:91],a[96:99],v[88:91]\nv_mul_f32 v234,s20,v234\nv_mul_f32 v235,s20,v235\nM v[92:95],a[88:91],a[100:103],v[92:95]\nbuffer_store_dwordx4 v[204:207],v4,s[36:39],s96 offen offset:3072 sc1\ns_add_u32 s96,s57,4096\n" \
"M v[92:95],a[64:67],a[124:127],v[92:95]\ns_waitcnt lgkmcnt(0)\nC v212,v232,v233\nM v[92:95],a[92:95],a[96:99],v[92:95]\nC v213,v234,v235\nv_mul_f32 v236,s20,v236\nM v[64:67],a[0:3],a[32:35],v[64:67]\nds_read_b128 a[96:99],v50 offset:64\n" \
"v_mul_f32 v237,s20,v237\nM v[68:71],a[0:3],a[36:39],v[68:71]\nds_read2_b32 a[64:65],v46 offset0:112 offset1:113\nv_mul_f32 v238,s20,v238\nM v[68:71],a[4:7],a[32:35],v[68:71]\nds_read2_b32 a[66:67],v46 offset0:114 offset1:115\nv_mul_f32 v239,s20,v239\nM v[72:75],a[4:7],a[36:39],v[72:75]\n" \
"ds_read_b128 a[100:103],v50 offset:576\nC v214,v236,v237\nM v[72:75],a[0:3],a[40:43],v[72:75]\nds_read2_b32 a[68:69],v46 offset0:240 offset1:241\nC v215,v238,v239\nM v[76:79],a[4:7],a[40:43],v[76:79]\nds_read2_b32 a[70:71],v46 offset0:242 offset1:243\nv_mul_f32 v240,s20,v240\n" \
"M v[72:75],a[8:11],a[32:35],v[72:75]\nds_read_b128 a[104:107],v50 offset:1088\nv_mul_f32 v241,s20,v241\nM v[76:79],a[8:11],a[36:39],v[76:79]\nds_read2_b32 a[72:73],v47 offset0:112 offset1:113\nv_mul_f32 v242,s20,v242\nM v[80:83],a[8:11],a[40:43],v[80:83]\nds_read2_b32 a[74:75],v47 offset0:114 offset1:115\n" \
"v_mul_f32 v243,s20,v243\nM v[76:79],a[0:3],a[44:47],v[76:79]\nds_read_b128 a[108:111],v50 offset:1600\nC v216,v240,v241\nM v[80:83],a[4:7],a[44:47],v[80:83]\nds_read2_b32 a[76:77],v47 offset0:240 offset1:241\nC v217,v242,v243\nM v[84:87],a[8:11],a[44:47],v[84:87]\n" \
"ds_read2_b32 a[78:79],v47 offset0:242 offset1:243\nv_mul_f32 v244,s20,v244\nM v[76:79],a[12:15],a[32:35],v[76:79]\nds_read_b128 a[112:115],v50 offset:2112\nv_mul_f32 v245,s20,v245\nM v[80:83],a[12:15],a[36:39],v[80:83]\nds_read2_b32 a[80:81],v48 offset0:112 offset1:113\nv_mul_f32 v246,s20,v246\n" \
"M v[84:87],a[12:15],a[40:43],v[84:87]\nds_read2_b32 a[82:83],v48 offset0:114 offset1:115\nv_mul_f32 v247,s20,v247\nM v[88:91],a[12:15],a[44:47],v[88:91]\nds_read_b128 a[116:119],v50 offset:2624\nC v218,v244,v245\nM v[80:83],a[0:3],a[48:51],v[80:83]\nds_read2_b32 a[84:85],v48 offset0:240 offset1:241\n" \
"C v219,v246,v247\nM v[84:87],a[4:7],a[48:51],v[84:87]\nds_read2_b32 a[86:87],v48 offset0:242 offset1:243\nv_mul_f32 v248,s20,v248\nM v[88:91],a[8:11],a[48:51],v[88:91]\nds_read_b128 a[120:123],v50 offset:3136\nv_mul_f32 v249,s20,v249\nM v[92:95],a[12:15],a[48:51],v[92:95]\n" \
"ds_read2_b32 a[88:89],v49 offset0:112 offset1:113\nv_mul_f32 v250,s20,v250\nM v[80:83],a[16:19],a[32:35],v[80:83]\nds_read2_b32 a[90:91],v49 offset0:114 offset1:115\nv_mul_f32 v251,s20,v251\nM v[84:87],a[16:19],a[36:39],v[84:87]\nds_read_b128 a[124:127],v50 offset:3648\nC v220,v248,v249\n" \
"M v[88:91],a[16:19],a[40:43],v[88:91]\nds_read2_b32 a[92:93],v49 offset0:240 offset1:241\nC v221,v250,v251\nM v[92:95],a[16:19],a[44:47],v[92:95]\nds_read2_b32 a[94:95],v49 offset0:242 offset1:243\nv_mul_f32 v252,s20,v252\nM v[84:87],a[0:3],a[52:55],v[84:87]\nv_mul_f32 v253,s20,v253\n" \
"v_mul_f32 v254,s20,v254\nM v[88:91],a[4:7],a[52:55],v[88:91]\nbuffer_store_dwordx4 v[208:211],v4,s[36:39],s96 offen offset:0 sc1\nM v[92:95],a[8:11],a[52:55],v[92:95]\nv_mul_f32 v255,s20,v255\nC v222,v252,v253\nM v[84:87],a[20:23],a[32:35],v[84:87]\nC v223,v254,v255\n" \
"v_accvgpr_read_b32 v38,a128\nM v[88:91],a[20:23],a[36:39],v[88:91]\nv_accvgpr_read_b32 v39,a129\nv_accvgpr_read_b32 v40,a130\nM v[92:95],a[20:23],a[40:43],v[92:95]\nbuffer_store_dwordx4 v[212:215],v4,s[36:39],s96 offen offset:1024 sc1\nM v[88:91],a[0:3],a[56:59],v[88:91]\nv_accvgpr_read_b32 v41,a131\n" \
"v_mul_f32 v38,s20,v38\nM v[92:95],a[4:7],a[56:59],v[92:95]\nv_mul_f32 v39,s20,v39\nv_mul_f32 v40,s20,v40\nM v[88:91],a[24:27],a[32:35],v[88:91]\nv_mul_f32 v41,s20,v41\nC v224,v38,v39\nM v[92:95],a[24:27],a[36:39],v[92:95]\n" \
"buffer_store_dwordx4 v[216:219],v4,s[36:39],s96 offen offset:2048 sc1\nM v[92:95],a[0:3],a[60:63],v[92:95]\ns_waitcnt lgkmcnt(0)\nC v225,v40,v41\nM v[92:95],a[28:31],a[32:35],v[92:95]\nv_accvgpr_read_b32 v42,a132\nv_accvgpr_read_b32 v43,a133\nM v[64:67],a[64:67],a[96:99],v[64:67]\n" \
"s_waitcnt vmcnt(38) lgkmcnt(0)\nv_accvgpr_read_b32 v44,a134\nM v[68:71],a[64:67],a[100:103],v[68:71]\ns_barrier\nM v[68:71],a[68:71],a[96:99],v[68:71]\ns_mov_b32 s47,1048576\ns_add_u32 m0,s51,0\nds_read_b64_tr_b16 a[0:1],v9 offset:32768\n" \
"M v[72:75],a[64:67],a[104:107],v[72:75]\nbuffer_load_dwordx4 v21,s[32:35],s47 offen offset:0 lds\nM v[72:75],a[68:71],a[100:103],v[72:75]\nds_read_b64_tr_b16 a[2:3],v10 offset:32768\nC v64,v64,v65\nM v[72:75],a[72:75],a[96:99],v[72:75]\nds_read_b128 a[32:35],v7 offset:0\nC v65,v66,v67\n" \
"M v[76:79],a[64:67],a[108:111],v[76:79]\ns_add_u32 m0,s51,896\nds_read_b128 a[36:39],v7 offset:4096\nC v66,v68,v69\nM v[76:79],a[68:71],a[104:107],v[76:79]\nbuffer_load_dwordx4 v21,s[32:35],s47 offen offset:128 lds\nM v[76:79],a[72:75],a[100:103],v[76:79]\nds_read_b64_tr_b16 a[4:5],v11 offset:32768\n" \
"C v67,v70,v71\ns_nop 1\nM v[76:79],a[76:79],a[96:99],v[76:79]\nds_read_b64_tr_b16 a[6:7],v12 offset:32768\nv_permlane16_swap_b32 v64,v66\nM v[80:83],a[64:67],a[112:115],v[80:83]\nds_read_b64_tr_b16 a[8:9],v9 offset:33280\nv_permlane16_swap_b32 v65,v67\n" \
"M v[80:83],a[68:71],a[108:111],v[80:83]\nbuffer_store_dwordx4 v[64:67],v60,s[36:39],s21 offen offset:0 sc1\ns_add_u32 s21,s21,1048576\nM v[80:83],a[72:75],a[104:107],v[80:83]\nds_read_b64_tr_b16 a[10:11],v10 offset:33280\nC v72,v72,v73\nM v[80:83],a[76:79],a[100:103],v[80:83]\nds_read_b64_tr_b16 a[12:13],v11 offset:33280\n" \
"C v73,v74,v75\nM v[80:83],a[80:83],a[96:99],v[80:83]\ns_mov_b32 s47,1310720\ns_add_u32 m0,s51,8192\nds_read_b64_tr_b16 a[14:15],v12 offset:33280\nM v[84:87],a[64:67],a[116:119],v[84:87]\nbuffer_load_dwordx4 v21,s[32:35],s47 offen offset:0 lds\nM v[84:87],a[68:71],a[112:115],v[84:87]\n" \
"ds_read_b64_tr_b16 a[16:17],v9 offset:33792\nC v74,v76,v77\nM v[84:87],a[72:75],a[108:111],v[84:87]\nds_read_b64_tr_b16 a[18:19],v10 offset:33792\nC v75,v78,v79\ns_nop 1\nM v[84:87],a[76:79],a[104:107],v[84:87]\ns_add_u32 m0,s51,9088\n" \
"ds_read_b64_tr_b16 a[20:21],v11 offset:33792\nv_permlane16_swap_b32 v72,v74\nM v[84:87],a[80:83],a[100:103],v[84:87]\nbuffer_load_dwordx4 v21,s[32:35],s47 offen offset:128 lds\nM v[84:87],a[84:87],a[96:99],v[84:87]\nds_read_b64_tr_b16 a[22:23],v12 offset:33792\nv_permlane16_swap_b32 v73,v75\nM v[88:91],a[64:67],a[120:123],v[88:91]\n" \
"ds_read_b64_tr_b16 a[24:25],v9 offset:34304\nv_accvgpr_read_b32 v45,a135\nM v[88:91],a[68:71],a[116:119],v[88:91]\nds_read_b64_tr_b16 a[26:27],v10 offset:34304\nC v80,v80,v81\nM v[88:91],a[72:75],a[112:115],v[88:91]\nbuffer_store_dwordx4 v[72:75],v60,s[36:39],s21 offen offset:0 sc1\ns_add_u32 s21,s21,1048576\n" \
"M v[88:91],a[76:79],a[108:111],v[88:91]\nds_read_b64_tr_b16 a[28:29],v11 offset:34304\nC v81,v82,v83\nM v[88:91],a[80:83],a[104:107],v[88:91]\nds_read_b64_tr_b16 a[30:31],v12 offset:34304\nC v82,v84,v85\nM v[88:91],a[84:87],a[100:103],v[88:91]\ns_mov_b32 s46,256\n" \
"s_add_u32 m0,s50,32768\nds_read_b128 a[40:43],v7 offset:8192\nM v[88:91],a[88:91],a[96:99],v[88:91]\nbuffer_load_dwordx4 v13,s[28:31],s46 offen lds\nM v[92:95],a[64:67],a[124:127],v[92:95]\nds_read_b128 a[44:47],v7 offset:12288\nC v83,v86,v87\ns_nop 1\n" \
"M v[92:95],a[68:71],a[120:123],v[92:95]\nv_permlane16_swap_b32 v80,v82\nv_permlane16_swap_b32 v81,v83\nM v[92:95],a[72:75],a[116:119],v[92:95]\nv_mul_f32 v42,s20,v42\nv_mul_f32 v43,s20,v43\nM v[92:95],a[76:79],a[112:115],v[92:95]\nbuffer_store_dwordx4 v[80:83],v60,s[36:39],s21 offen offset:0 sc1\n" \
"s_add_u32 s21,s21,1048576\nM v[92:95],a[80:83],a[108:111],v[92:95]\nv_mul_f32 v44,s20,v44\nv_mul_f32 v45,s20,v45\nM v[92:95],a[84:87],a[104:107],v[92:95]\nC v226,v42,v43\nC v227,v44,v45\nM v[92:95],a[88:91],a[100:103],v[92:95]\n" \
"s_add_u32 m0,s50,36864\ns_waitcnt lgkmcnt(0)\nv_accvgpr_read_b32 v38,a136\nM v[92:95],a[92:95],a[96:99],v[92:95]\nbuffer_load_dwordx4 v14,s[28:31],s46 offen lds\nM v[128:131],a[0:3],a[32:35],0\ns_add_u32 m0,s50,40960\nds_read_b64_tr_b16 a[48:49],v9 offset:40960\n" \
"v_accvgpr_read_b32 v39,a137\nM v[160:163],a[0:3],a[36:39],0\nbuffer_load_dwordx4 v15,s[28:31],s46 offen lds\nM v[132:135],a[4:7],a[32:35],0\nds_read_b64_tr_b16 a[50:51],v10 offset:40960\nC v88,v88,v89\nM v[164:167],a[4:7],a[36:39],0\nds_read_b128 a[80:83],v8 offset:0\n" \
"C v89,v90,v91\nM v[136:139],a[8:11],a[32:35],0\ns_add_u32 m0,s50,45056\nds_read_b128 a[84:87],v8 offset:4096\nC v90,v92,v93\nM v[168:171],a[8:11],a[36:39],0\nbuffer_load_dwordx4 v16,s[28:31],s46 offen lds\nM v[140:143],a[12:15],a[32:35],0\n" \
"ds_read_b64_tr_b16 a[52:53],v11 offset:40960\nC v91,v94,v95\ns_nop 1\nM v[172:175],a[12:15],a[36:39],0\nds_read_b64_tr_b16 a[54:55],v12 offset:40960\nv_permlane16_swap_b32 v88,v90\nM v[144:147],a[16:19],a[32:35],0\ns_mov_b32 s46,256\n" \
"s_add_u32 m0,s50,49152\nds_read_b64_tr_b16 a[56:57],v9 offset:41472\nM v[176:179],a[16:19],a[36:39],0\nbuffer_load_dwordx4 v17,s[28:31],s46 offen lds\nM v[148:151],a[20:23],a[32:35],0\nds_read_b64_tr_b16 a[58:59],v10 offset:41472\nv_permlane16_swap_b32 v89,v91\nM v[180:183],a[20:23],a[36:39],0\n" \
"ds_read_b64_tr_b16 a[60:61],v11 offset:41472\nv_accvgpr_read_b32 v40,a138\nM v[152:155],a[24:27],a[32:35],0\nds_read_b64_tr_b16 a[62:63],v12 offset:41472\nv_accvgpr_read_b32 v41,a139\nM v[184:187],a[24:27],a[36:39],0\nbuffer_store_dwordx4 v[88:91],v60,s[36:39],s21 offen offset:0 sc1\nM v[156:159],a[28:31],a[32:35],0\n" \
"ds_read_b64_tr_b16 a[64:65],v9 offset:41984\nv_mul_f32 v38,s20,v38\nM v[188:191],a[28:31],a[36:39],0\nds_read_b64_tr_b16 a[66:67],v10 offset:41984\nv_mul_f32 v39,s20,v39\nM v[96:99],a[0:3],a[40:43],0\ns_add_u32 m0,s50,53248\nds_read_b64_tr_b16 a[68:69],v11 offset:41984\n" \
"v_mul_f32 v40,s20,v40\nM v[64:67],a[0:3],a[44:47],0\nbuffer_load_dwordx4 v18,s[28:31],s46 offen lds\nM v[100:103],a[4:7],a[40:43],0\nds_read_b64_tr_b16 a[70:71],v12 offset:41984\nv_mul_f32 v41,s20,v41\nM v[68:71],a[4:7],a[44:47],0\nds_read_b64_tr_b16 a[72:73],v9 offset:42496\n" \
"C v228,v38,v39\nM v[104:107],a[8:11],a[40:43],0\ns_add_u32 m0,s50,57344\nds_read_b64_tr_b16 a[74:75],v10 offset:42496\nC v229,v40,v41\nM v[72:75],a[8:11],a[44:47],0\nbuffer_load_dwordx4 v19,s[28:31],s46 offen lds\nM v[108:111],a[12:15],a[40:43],0\n" \
"ds_read_b64_tr_b16 a[76:77],v11 offset:42496\nv_accvgpr_read_b32 v42,a140\nM v[76:79],a[12:15],a[44:47],0\nds_read_b64_tr_b16 a[78:79],v12 offset:42496\nv_accvgpr_read_b32 v43,a141\nM v[112:115],a[16:19],a[40:43],0\ns_add_u32 m0,s50,61440\nds_read_b128 a[88:91],v8 offset:8192\n" \
"v_accvgpr_read_b32 v44,a142\nM v[80:83],a[16:19],a[44:47],0\nbuffer_load_dwordx4 v20,s[28:31],s46 offen lds\nM v[116:119],a[20:23],a[40:43],0\nds_read_b128 a[92:95],v8 offset:12288\nv_accvgpr_read_b32 v45,a143\nM v[84:87],a[20:23],a[44:47],0\nv_mul_f32 v42,s20,v42\n" \
"v_mul_f32 v43,s20,v43\nM v[120:123],a[24:27],a[40:43],0\ns_waitcnt vmcnt(41) lgkmcnt(0)\nv_mul_f32 v44,s20,v44\nM v[88:91],a[24:27],a[44:47],0\ns_barrier\nM v[124:127],a[28:31],a[40:43],0\nv_mul_f32 v45,s20,v45\n" \
"C v230,v42,v43\nM v[92:95],a[28:31],a[44:47],0\nC v231,v44,v45\nv_accvgpr_read_b32 v38,a144\nM v[128:131],a[48:51],a[80:83],v[128:131]\ns_mov_b32 s46,384\ns_add_u32 m0,s50,65536\nds_read_b64_tr_b16 a[0:1],v9 offset:49152\n" \
"M v[160:163],a[48:51],a[84:87],v[160:163]\nbuffer_load_dwordx4 v13,s[28:31],s46 offen lds\nM v[132:135],a[52:55],a[80:83],v[132:135]\nds_read_b64_tr_b16 a[2:3],v10 offset:49152\nv_accvgpr_read_b32 v39,a145\nM v[164:167],a[52:55],a[84:87],v[164:167]\nds_read_b128 a[32:35],v5 offset:0\nv_accvgpr_read_b32 v40,a146\n" \
"M v[136:139],a[56:59],a[80:83],v[136:139]\ns_add_u32 m0,s50,69632\nds_read_b128 a[36:39],v5 offset:4096\nv_accvgpr_read_b32 v41,a147\nM v[168:171],a[56:59],a[84:87],v[168:171]\nbuffer_load_dwordx4 v14,s[28:31],s46 offen lds\nM v[140:143],a[60:63],a[80:83],v[140:143]\nds_read_b64_tr_b16 a[4:5],v11 offset:49152\n" \
"v_mul_f32 v38,s20,v38\nM v[172:175],a[60:63],a[84:87],v[172:175]\nds_read_b64_tr_b16 a[6:7],v12 offset:49152\nv_mul_f32 v39,s20,v39\nM v[144:147],a[64:67],a[80:83],v[144:147]\ns_add_u32 m0,s50,73728\nds_read_b64_tr_b16 a[8:9],v9 offset:49664\nv_mul_f32 v40,s20,v40\n" \
"M v[176:179],a[64:67],a[84:87],v[176:179]\nbuffer_load_dwordx4 v15,s[28:31],s46 offen lds\nM v[148:151],a[68:71],a[80:83],v[148:151]\nds_read_b64_tr_b16 a[10:11],v10 offset:49664\nv_mul_f32 v41,s20,v41\nM v[180:183],a[68:71],a[84:87],v[180:183]\nds_read_b64_tr_b16 a[12:13],v11 offset:49664\nC v232,v38,v39\n" \
"M v[152:155],a[72:75],a[80:83],v[152:155]\ns_add_u32 m0,s50,77824\nds_read_b64_tr_b16 a[14:15],v12 offset:49664\nC v233,v40,v41\nM v[184:187],a[72:75],a[84:87],v[184:187]\nbuffer_load_dwordx4 v16,s[28:31],s46 offen lds\nM v[156:159],a[76:79],a[80:83],v[156:159]\nds_read_b64_tr_b16 a[16:17],v9 offset:50176\n" \
"v_accvgpr_read_b32 v42,a148\nM v[188:191],a[76:79],a[84:87],v[188:191]\nds_read_b64_tr_b16 a[18:19],v10 offset:50176\nv_accvgpr_read_b32 v43,a149\nM v[96:99],a[48:51],a[88:91],v[96:99]\ns_mov_b32 s46,384\ns_add_u32 m0,s50,81920\nds_read_b64_tr_b16 a[20:21],v11 offset:50176\n" \
"M v[64:67],a[48:51],a[92:95],v[64:67]\nbuffer_load_dwordx4 v17,s[28:31],s46 offen lds\nM v[100:103],a[52:55],a[88:91],v[100:103]\nds_read_b64_tr_b16 a[22:23],v12 offset:50176\nv_accvgpr_read_b32 v44,a150\nM v[68:71],a[52:55],a[92:95],v[68:71]\nds_read_b64_tr_b16 a[24:25],v9 offset:50688\nv_accvgpr_read_b32 v45,a151\n" \
"M v[104:107],a[56:59],a[88:91],v[104:107]\ns_add_u32 m0,s50,86016\nds_read_b64_tr_b16 a[26:27],v10 offset:50688\nv_mul_f32 v42,s20,v42\nM v[72:75],a[56:59],a[92:95],v[72:75]\nbuffer_load_dwordx4 v18,s[28:31],s46 offen lds\nM v[108:111],a[60:63],a[88:91],v[108:111]\nds_read_b64_tr_b16 a[28:29],v11 offset:50688\n" \
"v_mul_f32 v43,s20,v43\nM v[76:79],a[60:63],a[92:95],v[76:79]\nds_read_b64_tr_b16 a[30:31],v12 offset:50688\nv_mul_f32 v44,s20,v44\nM v[112:115],a[64:67],a[88:91],v[112:115]\ns_add_u32 m0,s50,90112\nds_read_b128 a[40:43],v5 offset:8192\nv_mul_f32 v45,s20,v45\n" \
"M v[80:83],a[64:67],a[92:95],v[80:83]\nbuffer_load_dwordx4 v19,s[28:31],s46 offen lds\nM v[116:119],a[68:71],a[88:91],v[116:119]\nds_read_b128 a[44:47],v5 offset:12288\nC v234,v42,v43\nM v[84:87],a[68:71],a[92:95],v[84:87]\nC v235,v44,v45\nv_accvgpr_read_b32 v38,a152\n" \
"M v[120:123],a[72:75],a[88:91],v[120:123]\ns_add_u32 m0,s50,94208\nv_accvgpr_read_b32 v39,a153\nM v[88:91],a[72:75],a[92:95],v[88:91]\nbuffer_load_dwordx4 v20,s[28:31],s46 offen lds\nM v[124:127],a[76:79],a[88:91],v[124:127]\ns_waitcnt lgkmcnt(0)\nv_accvgpr_read_b32 v40,a154\n" \
"M v[92:95],a[76:79],a[92:95],v[92:95]\nv_accvgpr_read_b32 v41,a155\nv_mul_f32 v38,s20,v38\nM v[128:131],a[0:3],a[32:35],v[128:131]\ns_mov_b32 s47,1572864\ns_add_u32 m0,s51,16384\nds_read_b64_tr_b16 a[48:49],v9 offset:57344\nM v[160:163],a[0:3],a[36:39],v[160:163]\n" \
"buffer_load_dwordx4 v21,s[32:35],s47 offen offset:0 lds\ns_lshl_b32 s62,s6,20\ns_add_u32 s62,s62,16777216\nM v[132:135],a[4:7],a[32:35],v[132:135]\nds_read_b64_tr_b16 a[50:51],v10 offset:57344\nv_mul_f32 v39,s20,v39\ns_add_u32 s40,s4,s62\ns_addc_u32 s41,s5,0\n" \
"M v[164:167],a[4:7],a[36:39],v[164:167]\nds_read_b128 a[80:83],v6 offset:0\nv_mul_f32 v40,s20,v40\ns_and_b32 s41,s41,0xffff\nM v[136:139],a[8:11],a[32:35],v[136:139]\ns_add_u32 m0,s51,17280\nds_read_b128 a[84:87],v6 offset:4096\nv_mul_f32 v41,s20,v41\n" \
"M v[168:171],a[8:11],a[36:39],v[168:171]\nbuffer_load_dwordx4 v21,s[32:35],s47 offen offset:128 lds\ns_lshl_b32 s62,s7,16\ns_sub_u32 s63,983040,s62\nM v[140:143],a[12:15],a[32:35],v[140:143]\nds_read_b64_tr_b16 a[52:53],v11 offset:57344\nC v236,v38,v39\ns_cmp_lt_u32 %2,2\n" \
"s_cselect_b32 s44,s63,s62\ns_cselect_b32 s42,1048576,s63\nM v[172:175],a[12:15],a[36:39],v[172:175]\nds_read_b64_tr_b16 a[54:55],v12 offset:57344\nC v237,v40,v41\ns_cmp_lt_u32 %2,2\ns_cselect_b32 s49,1,-1\ns_cselect_b32 s45,-1,60\n" \
"M v[144:147],a[16:19],a[32:35],v[144:147]\ns_mov_b32 s47,1835008\ns_add_u32 m0,s51,24576\nds_read_b64_tr_b16 a[56:57],v9 offset:57856\nM v[176:179],a[16:19],a[36:39],v[176:179]\nbuffer_load_dwordx4 v21,s[32:35],s47 offen offset:0 lds\ns_lshl_b32 s49,s49,14\ns_lshl_b32 s45,s45,14\n" \
"M v[148:151],a[20:23],a[32:35],v[148:151]\nds_read_b64_tr_b16 a[58:59],v10 offset:57856\nv_accvgpr_read_b32 v42,a156\ns_add_u32 s48,s45,8192\nM v[180:183],a[20:23],a[36:39],v[180:183]\nds_read_b64_tr_b16 a[60:61],v11 offset:57856\nv_accvgpr_read_b32 v43,a157\ns_and_b32 s63,%2,1\n" \
"M v[152:155],a[24:27],a[32:35],v[152:155]\ns_add_u32 m0,s51,25472\nds_read_b64_tr_b16 a[62:63],v12 offset:57856\nv_accvgpr_read_b32 v44,a158\nM v[184:187],a[24:27],a[36:39],v[184:187]\nbuffer_load_dwordx4 v21,s[32:35],s47 offen offset:128 lds\ns_lshl_b32 s63,s63,12\nM v[156:159],a[28:31],a[32:35],v[156:159]\n" \
"ds_read_b64_tr_b16 a[64:65],v9 offset:58368\nv_accvgpr_read_b32 v45,a159\nM v[188:191],a[28:31],a[36:39],v[188:191]\nds_read_b64_tr_b16 a[66:67],v10 offset:58368\nv_mul_f32 v42,s20,v42\nM v[96:99],a[0:3],a[40:43],v[96:99]\ns_mov_b32 s47,2097152\ns_add_u32 m0,s51,32768\n" \
"ds_read_b64_tr_b16 a[68:69],v11 offset:58368\nM v[64:67],a[0:3],a[44:47],v[64:67]\nbuffer_load_dwordx4 v21,s[32:35],s47 offen offset:0 lds\nM v[100:103],a[4:7],a[40:43],v[100:103]\nds_read_b64_tr_b16 a[70:71],v12 offset:58368\nv_mul_f32 v43,s20,v43\nM v[68:71],a[4:7],a[44:47],v[68:71]\nds_read_b64_tr_b16 a[72:73],v9 offset:58880\n" \
"v_mul_f32 v44,s20,v44\nM v[104:107],a[8:11],a[40:43],v[104:107]\ns_add_u32 m0,s51,33664\nds_read_b64_tr_b16 a[74:75],v10 offset:58880\nv_mul_f32 v45,s20,v45\nM v[72:75],a[8:11],a[44:47],v[72:75]\nbuffer_load_dwordx4 v21,s[32:35],s47 offen offset:128 lds\nM v[108:111],a[12:15],a[40:43],v[108:111]\n" \
"ds_read_b64_tr_b16 a[76:77],v11 offset:58880\nC v238,v42,v43\nM v[76:79],a[12:15],a[44:47],v[76:79]\nds_read_b64_tr_b16 a[78:79],v12 offset:58880\nC v239,v44,v45\nM v[112:115],a[16:19],a[40:43],v[112:115]\nds_read_b128 a[88:91],v6 offset:8192\nv_accvgpr_read_b32 v38,a160\n" \
"M v[80:83],a[16:19],a[44:47],v[80:83]\nv_accvgpr_read_b32 v39,a161\nv_accvgpr_read_b32 v40,a162\nM v[116:119],a[20:23],a[40:43],v[116:119]\nds_read_b128 a[92:95],v6 offset:12288\nv_accvgpr_read_b32 v41,a163\nM v[84:87],a[20:23],a[44:47],v[84:87]\nv_mul_f32 v38,s20,v38\n" \
"v_mul_f32 v39,s20,v39\nM v[120:123],a[24:27],a[40:43],v[120:123]\ns_waitcnt vmcnt(14) lgkmcnt(0)\nv_mul_f32 v40,s20,v40\nM v[88:91],a[24:27],a[44:47],v[88:91]\ns_barrier\nM v[124:127],a[28:31],a[40:43],v[124:127]\nv_mul_f32 v41,s20,v41\n" \
"C v240,v38,v39\nM v[92:95],a[28:31],a[44:47],v[92:95]\nC v241,v40,v41\nv_accvgpr_read_b32 v42,a164\nM v[128:131],a[48:51],a[80:83],v[128:131]\ns_mov_b32 s46,512\ns_add_u32 m0,s50,0\nds_read_b64_tr_b16 a[0:1],v9 offset:0\n" \
"M v[160:163],a[48:51],a[84:87],v[160:163]\nbuffer_load_dwordx4 v13,s[28:31],s46 offen lds\nM v[132:135],a[52:55],a[80:83],v[132:135]\nds_read_b64_tr_b16 a[2:3],v10 offset:0\nv_accvgpr_read_b32 v43,a165\nM v[164:167],a[52:55],a[84:87],v[164:167]\nds_read_b128 a[32:35],v5 offset:32768\nv_accvgpr_read_b32 v44,a166\n" \
"M v[136:139],a[56:59],a[80:83],v[136:139]\ns_add_u32 m0,s50,4096\nds_read_b128 a[36:39],v5 offset:36864\nv_accvgpr_read_b32 v45,a167\nM v[168:171],a[56:59],a[84:87],v[168:171]\nbuffer_load_dwordx4 v14,s[28:31],s46 offen lds\nM v[140:143],a[60:63],a[80:83],v[140:143]\nds_read_b64_tr_b16 a[4:5],v11 offset:0\n" \
"v_mul_f32 v42,s20,v42\nM v[172:175],a[60:63],a[84:87],v[172:175]\nds_read_b64_tr_b16 a[6:7],v12 offset:0\nv_mul_f32 v43,s20,v43\nM v[144:147],a[64:67],a[80:83],v[144:147]\nLG10_%=:\ns_cbranch_execz LHR0_%=\ns_add_u32 m0,s50,8192\n" \
"ds_read_b64_tr_b16 a[8:9],v9 offset:512\nv_mul_f32 v44,s20,v44\nM v[176:179],a[64:67],a[84:87],v[176:179]\nbuffer_load_dwordx4 v15,s[28:31],s46 offen lds\nM v[148:151],a[68:71],a[80:83],v[148:151]\nds_read_b64_tr_b16 a[10:11],v10 offset:512\nv_mul_f32 v45,s20,v45\nM v[180:183],a[68:71],a[84:87],v[180:183]\n" \
"ds_read_b64_tr_b16 a[12:13],v11 offset:512\nC v242,v42,v43\nM v[152:155],a[72:75],a[80:83],v[152:155]\ns_add_u32 m0,s50,12288\nds_read_b64_tr_b16 a[14:15],v12 offset:512\nC v243,v44,v45\nM v[184:187],a[72:75],a[84:87],v[184:187]\nbuffer_load_dwordx4 v16,s[28:31],s46 offen lds\n" \
"M v[156:159],a[76:79],a[80:83],v[156:159]\nds_read_b64_tr_b16 a[16:17],v9 offset:1024\nv_accvgpr_read_b32 v38,a168\nM v[188:191],a[76:79],a[84:87],v[188:191]\nds_read_b64_tr_b16 a[18:19],v10 offset:1024\nv_accvgpr_read_b32 v39,a169\nM v[96:99],a[48:51],a[88:91],v[96:99]\ns_mov_b32 s46,512\n" \
"s_add_u32 m0,s50,16384\nds_read_b64_tr_b16 a[20:21],v11 offset:1024\nM v[64:67],a[48:51],a[92:95],v[64:67]\nbuffer_load_dwordx4 v17,s[28:31],s46 offen lds\nM v[100:103],a[52:55],a[88:91],v[100:103]\nds_read_b64_tr_b16 a[22:23],v12 offset:1024\nv_accvgpr_read_b32 v40,a170\nM v[68:71],a[52:55],a[92:95],v[68:71]\n" \
"ds_read_b64_tr_b16 a[24:25],v9 offset:1536\nv_accvgpr_read_b32 v41,a171\nM v[104:107],a[56:59],a[88:91],v[104:107]\ns_add_u32 m0,s50,20480\nds_read_b64_tr_b16 a[26:27],v10 offset:1536\nv_mul_f32 v38,s20,v38\nM v[72:75],a[56:59],a[92:95],v[72:75]\nbuffer_load_dwordx4 v18,s[28:31],s46 offen lds\n" \
"M v[108:111],a[60:63],a[88:91],v[108:111]\nds_read_b64_tr_b16 a[28:29],v11 offset:1536\nv_mul_f32 v39,s20,v39\nM v[76:79],a[60:63],a[92:95],v[76:79]\nds_read_b64_tr_b16 a[30:31],v12 offset:1536\nv_mul_f32 v40,s20,v40\nM v[112:115],a[64:67],a[88:91],v[112:115]\ns_add_u32 m0,s50,24576\n" \
"ds_read_b128 a[40:43],v5 offset:40960\nv_mul_f32 v41,s20,v41\nM v[80:83],a[64:67],a[92:95],v[80:83]\nbuffer_load_dwordx4 v19,s[28:31],s46 offen lds\nM v[116:119],a[68:71],a[88:91],v[116:119]\nds_read_b128 a[44:47],v5 offset:45056\nC v244,v38,v39\nM v[84:87],a[68:71],a[92:95],v[84:87]\n" \
"C v245,v40,v41\nv_accvgpr_read_b32 v42,a172\nM v[120:123],a[72:75],a[88:91],v[120:123]\ns_add_u32 m0,s50,28672\nv_accvgpr_read_b32 v43,a173\nM v[88:91],a[72:75],a[92:95],v[88:91]\nbuffer_load_dwordx4 v20,s[28:31],s46 offen lds\nM v[124:127],a[76:79],a[88:91],v[124:127]\n" \
"s_waitcnt lgkmcnt(0)\nv_accvgpr_read_b32 v44,a174\nM v[92:95],a[76:79],a[92:95],v[92:95]\nv_accvgpr_read_b32 v45,a175\nv_mul_f32 v42,s20,v42\nM v[128:131],a[0:3],a[32:35],v[128:131]\ns_mov_b32 s47,2359296\ns_add_u32 m0,s51,40960\n" \
"ds_read_b64_tr_b16 a[48:49],v9 offset:8192\nM v[160:163],a[0:3],a[36:39],v[160:163]\nbuffer_load_dwordx4 v21,s[32:35],s47 offen offset:0 lds\nM v[132:135],a[4:7],a[32:35],v[132:135]\nds_read_b64_tr_b16 a[50:51],v10 offset:8192\nv_mul_f32 v43,s20,v43\nM v[164:167],a[4:7],a[36:39],v[164:167]\nds_read_b128 a[80:83],v6 offset:32768\n" \
"v_mul_f32 v44,s20,v44\nM v[136:139],a[8:11],a[32:35],v[136:139]\ns_add_u32 m0,s51,41856\nds_read_b128 a[84:87],v6 offset:36864\nv_mul_f32 v45,s20,v45\nM v[168:171],a[8:11],a[36:39],v[168:171]\nbuffer_load_dwordx4 v21,s[32:35],s47 offen offset:128 lds\nM v[140:143],a[12:15],a[32:35],v[140:143]\n" \
"ds_read_b64_tr_b16 a[52:53],v11 offset:8192\nC v246,v42,v43\nM v[172:175],a[12:15],a[36:39],v[172:175]\nds_read_b64_tr_b16 a[54:55],v12 offset:8192\nC v247,v44,v45\nM v[144:147],a[16:19],a[32:35],v[144:147]\ns_mov_b32 s47,2621440\ns_add_u32 m0,s51,49152\n" \
"ds_read_b64_tr_b16 a[56:57],v9 offset:8704\nM v[176:179],a[16:19],a[36:39],v[176:179]\nbuffer_load_dwordx4 v21,s[32:35],s47 offen offset:0 lds\nM v[148:151],a[20:23],a[32:35],v[148:151]\nds_read_b64_tr_b16 a[58:59],v10 offset:8704\nv_accvgpr_read_b32 v38,a176\nM v[180:183],a[20:23],a[36:39],v[180:183]\nds_read_b64_tr_b16 a[60:61],v11 offset:8704\n" \
"v_accvgpr_read_b32 v39,a177\nM v[152:155],a[24:27],a[32:35],v[152:155]\ns_add_u32 m0,s51,50048\nds_read_b64_tr_b16 a[62:63],v12 offset:8704\nv_accvgpr_read_b32 v40,a178\nM v[184:187],a[24:27],a[36:39],v[184:187]\nbuffer_load_dwordx4 v21,s[32:35],s47 offen offset:128 lds\nM v[156:159],a[28:31],a[32:35],v[156:159]\n" \
"ds_read_b64_tr_b16 a[64:65],v9 offset:9216\nv_accvgpr_read_b32 v41,a179\nM v[188:191],a[28:31],a[36:39],v[188:191]\nds_read_b64_tr_b16 a[66:67],v10 offset:9216\nv_mul_f32 v38,s20,v38\nM v[96:99],a[0:3],a[40:43],v[96:99]\ns_mov_b32 s47,2883584\ns_add_u32 m0,s51,57344\n" \
"ds_read_b64_tr_b16 a[68:69],v11 offset:9216\nM v[64:67],a[0:3],a[44:47],v[64:67]\nbuffer_load_dwordx4 v21,s[32:35],s47 offen offset:0 lds\nM v[100:103],a[4:7],a[40:43],v[100:103]\nds_read_b64_tr_b16 a[70:71],v12 offset:9216\nv_mul_f32 v39,s20,v39\nM v[68:71],a[4:7],a[44:47],v[68:71]\nds_read_b64_tr_b16 a[72:73],v9 offset:9728\n" \
"v_mul_f32 v40,s20,v40\nM v[104:107],a[8:11],a[40:43],v[104:107]\ns_add_u32 m0,s51,58240\nds_read_b64_tr_b16 a[74:75],v10 offset:9728\nv_mul_f32 v41,s20,v41\nM v[72:75],a[8:11],a[44:47],v[72:75]\nbuffer_load_dwordx4 v21,s[32:35],s47 offen offset:128 lds\nM v[108:111],a[12:15],a[40:43],v[108:111]\n" \
"ds_read_b64_tr_b16 a[76:77],v11 offset:9728\nC v248,v38,v39\nM v[76:79],a[12:15],a[44:47],v[76:79]\nds_read_b64_tr_b16 a[78:79],v12 offset:9728\nC v249,v40,v41\nM v[112:115],a[16:19],a[40:43],v[112:115]\nds_read_b128 a[88:91],v6 offset:40960\nv_accvgpr_read_b32 v42,a180\n" \
"M v[80:83],a[16:19],a[44:47],v[80:83]\nv_accvgpr_read_b32 v43,a181\nv_accvgpr_read_b32 v44,a182\nM v[116:119],a[20:23],a[40:43],v[116:119]\nds_read_b128 a[92:95],v6 offset:45056\nv_accvgpr_read_b32 v45,a183\nM v[84:87],a[20:23],a[44:47],v[84:87]\nv_mul_f32 v42,s20,v42\n" \
"v_mul_f32 v43,s20,v43\nM v[120:123],a[24:27],a[40:43],v[120:123]\ns_waitcnt vmcnt(16) lgkmcnt(0)\nv_mul_f32 v44,s20,v44\nM v[88:91],a[24:27],a[44:47],v[88:91]\ns_barrier\nM v[124:127],a[28:31],a[40:43],v[124:127]\nv_mul_f32 v45,s20,v45\n" \
"C v250,v42,v43\nM v[92:95],a[28:31],a[44:47],v[92:95]\nC v251,v44,v45\nv_accvgpr_read_b32 v38,a184\nM v[128:131],a[48:51],a[80:83],v[128:131]\ns_mov_b32 s46,640\ns_add_u32 m0,s50,32768\nds_read_b64_tr_b16 a[0:1],v9 offset:16384\n" \
"M v[160:163],a[48:51],a[84:87],v[160:163]\nbuffer_load_dwordx4 v13,s[28:31],s46 offen lds\nM v[132:135],a[52:55],a[80:83],v[132:135]\nds_read_b64_tr_b16 a[2:3],v10 offset:16384\nv_accvgpr_read_b32 v39,a185\nM v[164:167],a[52:55],a[84:87],v[164:167]\nds_read_b128 a[32:35],v7 offset:0\nv_accvgpr_read_b32 v40,a186\n" \
"M v[136:139],a[56:59],a[80:83],v[136:139]\ns_add_u32 m0,s50,36864\nds_read_b128 a[36:39],v7 offset:4096\nv_accvgpr_read_b32 v41,a187\nM v[168:171],a[56:59],a[84:87],v[168:171]\nbuffer_load_dwordx4 v14,s[28:31],s46 offen lds\nM v[140:143],a[60:63],a[80:83],v[140:143]\nds_read_b64_tr_b16 a[4:5],v11 offset:16384\n" \
"v_mul_f32 v38,s20,v38\nM v[172:175],a[60:63],a[84:87],v[172:175]\nds_read_b64_tr_b16 a[6:7],v12 offset:16384\nv_mul_f32 v39,s20,v39\nM v[144:147],a[64:67],a[80:83],v[144:147]\ns_add_u32 m0,s50,40960\nds_read_b64_tr_b16 a[8:9],v9 offset:16896\nv_mul_f32 v40,s20,v40\n" \
"M v[176:179],a[64:67],a[84:87],v[176:179]\nbuffer_load_dwordx4 v15,s[28:31],s46 offen lds\nM v[148:151],a[68:71],a[80:83],v[148:151]\nds_read_b64_tr_b16 a[10:11],v10 offset:16896\nv_mul_f32 v41,s20,v41\nM v[180:183],a[68:71],a[84:87],v[180:183]\nds_read_b64_tr_b16 a[12:13],v11 offset:16896\nC v252,v38,v39\n" \
"M v[152:155],a[72:75],a[80:83],v[152:155]\ns_add_u32 m0,s50,45056\nds_read_b64_tr_b16 a[14:15],v12 offset:16896\nC v253,v40,v41\nM v[184:187],a[72:75],a[84:87],v[184:187]\nbuffer_load_dwordx4 v16,s[28:31],s46 offen lds\nM v[156:159],a[76:79],a[80:83],v[156:159]\nds_read_b64_tr_b16 a[16:17],v9 offset:17408\n" \
"v_accvgpr_read_b32 v42,a188\nM v[188:191],a[76:79],a[84:87],v[188:191]\nds_read_b64_tr_b16 a[18:19],v10 offset:17408\nv_accvgpr_read_b32 v43,a189\nM v[96:99],a[48:51],a[88:91],v[96:99]\ns_mov_b32 s46,640\ns_add_u32 m0,s50,49152\nds_read_b64_tr_b16 a[20:21],v11 offset:17408\n" \
"M v[64:67],a[48:51],a[92:95],v[64:67]\nbuffer_load_dwordx4 v17,s[28:31],s46 offen lds\nM v[100:103],a[52:55],a[88:91],v[100:103]\nds_read_b64_tr_b16 a[22:23],v12 offset:17408\nv_accvgpr_read_b32 v44,a190\nM v[68:71],a[52:55],a[92:95],v[68:71]\nds_read_b64_tr_b16 a[24:25],v9 offset:17920\nv_accvgpr_read_b32 v45,a191\n" \
"M v[104:107],a[56:59],a[88:91],v[104:107]\ns_add_u32 m0,s50,53248\nds_read_b64_tr_b16 a[26:27],v10 offset:17920\nv_mul_f32 v42,s20,v42\nM v[72:75],a[56:59],a[92:95],v[72:75]\nbuffer_load_dwordx4 v18,s[28:31],s46 offen lds\nM v[108:111],a[60:63],a[88:91],v[108:111]\nds_read_b64_tr_b16 a[28:29],v11 offset:17920\n" \
"v_mul_f32 v43,s20,v43\nM v[76:79],a[60:63],a[92:95],v[76:79]\nds_read_b64_tr_b16 a[30:31],v12 offset:17920\nv_mul_f32 v44,s20,v44\nM v[112:115],a[64:67],a[88:91],v[112:115]\ns_add_u32 m0,s50,57344\nds_read_b128 a[40:43],v7 offset:8192\nv_mul_f32 v45,s20,v45\n" \
"M v[80:83],a[64:67],a[92:95],v[80:83]\nbuffer_load_dwordx4 v19,s[28:31],s46 offen lds\nM v[116:119],a[68:71],a[88:91],v[116:119]\nds_read_b128 a[44:47],v7 offset:12288\nC v254,v42,v43\nM v[84:87],a[68:71],a[92:95],v[84:87]\nC v255,v44,v45\nv_add_u32 v22,s63,v4\n" \
"s_lshl_b32 s62,s6,6\nM v[120:123],a[72:75],a[88:91],v[120:123]\ns_add_u32 m0,s50,61440\ns_add_u32 s62,s62,94375936\nv_bfe_u32 v57,v4,4,3\nM v[88:91],a[72:75],a[92:95],v[88:91]\nbuffer_load_dwordx4 v20,s[28:31],s46 offen lds\nM v[124:127],a[76:79],a[88:91],v[124:127]\n" \
"s_waitcnt lgkmcnt(0)\nv_lshl_add_u32 v57,v57,3,s62\nM v[92:95],a[76:79],a[92:95],v[92:95]\ns_load_dwordx2 s[84:85],%0,0x50\ns_load_dwordx2 s[88:89],%0,0x60\ns_load_dwordx2 s[12:13],%0,0x0\nM v[128:131],a[0:3],a[32:35],v[128:131]\ns_mov_b32 s47,3145728\n" \
"s_add_u32 m0,s51,0\nds_read_b64_tr_b16 a[48:49],v9 offset:24576\nM v[160:163],a[0:3],a[36:39],v[160:163]\nbuffer_load_dwordx4 v21,s[32:35],s47 offen offset:0 lds\ns_mov_b32 s52,-1\ns_mov_b32 s75,0\nM v[132:135],a[4:7],a[32:35],v[132:135]\nds_read_b64_tr_b16 a[50:51],v10 offset:24576\n" \
"s_lshl_b32 s62,s7,1\ns_cmp_lt_u32 %2,2\ns_cselect_b32 s4,s62,50\ns_cselect_b32 s54,8192,0\nM v[164:167],a[4:7],a[36:39],v[164:167]\nds_read_b128 a[80:83],v8 offset:0\ns_cmp_lt_u32 %2,2\ns_cselect_b32 s65,0,-4096\n" \
"s_sub_u32 s55,s4,1\nM v[136:139],a[8:11],a[32:35],v[136:139]\ns_add_u32 m0,s51,896\nds_read_b128 a[84:87],v8 offset:4096\ns_min_u32 s56,s4,8\nM v[168:171],a[8:11],a[36:39],v[168:171]\nbuffer_load_dwordx4 v21,s[32:35],s47 offen offset:128 lds\ns_lshl_b32 s70,s4,1\n" \
"s_cmp_eq_u32 s4,0\ns_cselect_b64 s[66:67],-1,0\nM v[140:143],a[12:15],a[32:35],v[140:143]\nds_read_b64_tr_b16 a[52:53],v11 offset:24576\ns_lshl_b32 s62,s7,2\ns_sub_u32 s63,60,s62\nM v[172:175],a[12:15],a[36:39],v[172:175]\nds_read_b64_tr_b16 a[54:55],v12 offset:24576\n" \
"s_cmp_lt_u32 %2,2\ns_cselect_b32 s5,s62,s63\ns_lshl_b32 s71,s6,20\nM v[144:147],a[16:19],a[32:35],v[144:147]\ns_mov_b32 s47,3407872\ns_add_u32 m0,s51,8192\nds_read_b64_tr_b16 a[56:57],v9 offset:25088\nM v[176:179],a[16:19],a[36:39],v[176:179]\n" \
"buffer_load_dwordx4 v21,s[32:35],s47 offen offset:0 lds\ns_add_u32 s71,s71,16777216\ns_and_b32 s63,%2,1\nM v[148:151],a[20:23],a[32:35],v[148:151]\nds_read_b64_tr_b16 a[58:59],v10 offset:25088\ns_add_u32 s69,s62,s63\ns_add_u32 s68,s69,s62\nM v[180:183],a[20:23],a[36:39],v[180:183]\n" \
"ds_read_b64_tr_b16 a[60:61],v11 offset:25088\ns_lshl_b32 s68,s68,5\ns_add_u32 s68,s68,s6\nM v[152:155],a[24:27],a[32:35],v[152:155]\ns_add_u32 m0,s51,9088\nds_read_b64_tr_b16 a[62:63],v12 offset:25088\ns_lshl_b32 s68,s68,12\nM v[184:187],a[24:27],a[36:39],v[184:187]\n" \
"buffer_load_dwordx4 v21,s[32:35],s47 offen offset:128 lds\ns_add_u32 s68,s68,50331648\ns_lshl_b32 s69,s69,5\nM v[156:159],a[28:31],a[32:35],v[156:159]\nds_read_b64_tr_b16 a[64:65],v9 offset:25600\ns_add_u32 s69,s69,s6\ns_lshl_b32 s69,s69,3\nM v[188:191],a[28:31],a[36:39],v[188:191]\n" \
"ds_read_b64_tr_b16 a[66:67],v10 offset:25600\ns_add_u32 s69,s69,94388224\ns_lshl_b32 s72,%1,2\nM v[96:99],a[0:3],a[40:43],v[96:99]\nds_read_b64_tr_b16 a[68:69],v11 offset:25600\ns_add_u32 s72,s72,%2\ns_mul_i32 s72,s72,12288\nM v[64:67],a[0:3],a[44:47],v[64:67]\n" \
"s_add_u32 s72,s72,109060096\nM v[100:103],a[4:7],a[40:43],v[100:103]\nds_read_b64_tr_b16 a[70:71],v12 offset:25600\nM v[68:71],a[4:7],a[44:47],v[68:71]\nds_read_b64_tr_b16 a[72:73],v9 offset:26112\nM v[104:107],a[8:11],a[40:43],v[104:107]\nds_read_b64_tr_b16 a[74:75],v10 offset:26112\nM v[72:75],a[8:11],a[44:47],v[72:75]\n" \
"M v[108:111],a[12:15],a[40:43],v[108:111]\nds_read_b64_tr_b16 a[76:77],v11 offset:26112\nM v[76:79],a[12:15],a[44:47],v[76:79]\nds_read_b64_tr_b16 a[78:79],v12 offset:26112\nM v[112:115],a[16:19],a[40:43],v[112:115]\nds_read_b128 a[88:91],v8 offset:8192\nM v[80:83],a[16:19],a[44:47],v[80:83]\nbuffer_store_dwordx4 v[220:223],v4,s[36:39],s96 offen offset:3072 sc1\n" \
"s_add_u32 s96,s57,16384\nM v[116:119],a[20:23],a[40:43],v[116:119]\nds_read_b128 a[92:95],v8 offset:12288\nM v[84:87],a[20:23],a[44:47],v[84:87]\nM v[120:123],a[24:27],a[40:43],v[120:123]\ns_waitcnt vmcnt(17) lgkmcnt(0)\nM v[88:91],a[24:27],a[44:47],v[88:91]\ns_barrier\n" \
"M v[124:127],a[28:31],a[40:43],v[124:127]\nM v[92:95],a[28:31],a[44:47],v[92:95]\nM v[128:131],a[48:51],a[80:83],v[128:131]\ns_mov_b32 s46,768\ns_add_u32 m0,s50,65536\nds_read_b64_tr_b16 a[0:1],v9 offset:32768\nM v[160:163],a[48:51],a[84:87],v[160:163]\nbuffer_load_dwordx4 v13,s[28:31],s46 offen lds\n" \
"M v[132:135],a[52:55],a[80:83],v[132:135]\nds_read_b64_tr_b16 a[2:3],v10 offset:32768\nM v[164:167],a[52:55],a[84:87],v[164:167]\nds_read_b128 a[32:35],v5 offset:0\nM v[136:139],a[56:59],a[80:83],v[136:139]\ns_add_u32 m0,s50,69632\nds_read_b128 a[36:39],v5 offset:4096\nM v[168:171],a[56:59],a[84:87],v[168:171]\n" \
"buffer_load_dwordx4 v14,s[28:31],s46 offen lds\nM v[140:143],a[60:63],a[80:83],v[140:143]\nds_read_b64_tr_b16 a[4:5],v11 offset:32768\nM v[172:175],a[60:63],a[84:87],v[172:175]\nds_read_b64_tr_b16 a[6:7],v12 offset:32768\nM v[144:147],a[64:67],a[80:83],v[144:147]\ns_add_u32 m0,s50,73728\nds_read_b64_tr_b16 a[8:9],v9 offset:33280\n" \
"M v[176:179],a[64:67],a[84:87],v[176:179]\nbuffer_load_dwordx4 v15,s[28:31],s46 offen lds\nM v[148:151],a[68:71],a[80:83],v[148:151]\nds_read_b64_tr_b16 a[10:11],v10 offset:33280\nM v[180:183],a[68:71],a[84:87],v[180:183]\nds_read_b64_tr_b16 a[12:13],v11 offset:33280\nM v[152:155],a[72:75],a[80:83],v[152:155]\ns_add_u32 m0,s50,77824\n" \
"ds_read_b64_tr_b16 a[14:15],v12 offset:33280\nM v[184:187],a[72:75],a[84:87],v[184:187]\nbuffer_load_dwordx4 v16,s[28:31],s46 offen lds\nM v[156:159],a[76:79],a[80:83],v[156:159]\nds_read_b64_tr_b16 a[16:17],v9 offset:33792\nM v[188:191],a[76:79],a[84:87],v[188:191]\nds_read_b64_tr_b16 a[18:19],v10 offset:33792\nM v[96:99],a[48:51],a[88:91],v[96:99]\n" \
"s_mov_b32 s46,768\ns_add_u32 m0,s50,81920\nds_read_b64_tr_b16 a[20:21],v11 offset:33792\nM v[64:67],a[48:51],a[92:95],v[64:67]\nbuffer_load_dwordx4 v17,s[28:31],s46 offen lds\nM v[100:103],a[52:55],a[88:91],v[100:103]\nds_read_b64_tr_b16 a[22:23],v12 offset:33792\nM v[68:71],a[52:55],a[92:95],v[68:71]\n" \
"ds_read_b64_tr_b16 a[24:25],v9 offset:34304\nM v[104:107],a[56:59],a[88:91],v[104:107]\ns_add_u32 m0,s50,86016\nds_read_b64_tr_b16 a[26:27],v10 offset:34304\nM v[72:75],a[56:59],a[92:95],v[72:75]\nbuffer_load_dwordx4 v18,s[28:31],s46 offen lds\nM v[108:111],a[60:63],a[88:91],v[108:111]\nds_read_b64_tr_b16 a[28:29],v11 offset:34304\n" \
"M v[76:79],a[60:63],a[92:95],v[76:79]\nds_read_b64_tr_b16 a[30:31],v12 offset:34304\nM v[112:115],a[64:67],a[88:91],v[112:115]\ns_add_u32 m0,s50,90112\nds_read_b128 a[40:43],v5 offset:8192\nM v[80:83],a[64:67],a[92:95],v[80:83]\nbuffer_load_dwordx4 v19,s[28:31],s46 offen lds\nM v[116:119],a[68:71],a[88:91],v[116:119]\n" \
"ds_read_b128 a[44:47],v5 offset:12288\nM v[84:87],a[68:71],a[92:95],v[84:87]\nM v[120:123],a[72:75],a[88:91],v[120:123]\ns_add_u32 m0,s50,94208\nM v[88:91],a[72:75],a[92:95],v[88:91]\nbuffer_load_dwordx4 v20,s[28:31],s46 offen lds\nM v[124:127],a[76:79],a[88:91],v[124:127]\ns_waitcnt lgkmcnt(0)\n" \
"M v[92:95],a[76:79],a[92:95],v[92:95]\nM v[128:131],a[0:3],a[32:35],v[128:131]\ns_mov_b32 s47,3670016\ns_add_u32 m0,s51,16384\nds_read_b64_tr_b16 a[48:49],v9 offset:40960\nM v[160:163],a[0:3],a[36:39],v[160:163]\nbuffer_load_dwordx4 v21,s[32:35],s47 offen offset:0 lds\nM v[132:135],a[4:7],a[32:35],v[132:135]\n" \
"ds_read_b64_tr_b16 a[50:51],v10 offset:40960\nM v[164:167],a[4:7],a[36:39],v[164:167]\nds_read_b128 a[80:83],v6 offset:0\nM v[136:139],a[8:11],a[32:35],v[136:139]\ns_add_u32 m0,s51,17280\nds_read_b128 a[84:87],v6 offset:4096\nM v[168:171],a[8:11],a[36:39],v[168:171]\nbuffer_load_dwordx4 v21,s[32:35],s47 offen offset:128 lds\n" \
"M v[140:143],a[12:15],a[32:35],v[140:143]\nds_read_b64_tr_b16 a[52:53],v11 offset:40960\nM v[172:175],a[12:15],a[36:39],v[172:175]\nds_read_b64_tr_b16 a[54:55],v12 offset:40960\nM v[144:147],a[16:19],a[32:35],v[144:147]\ns_mov_b32 s47,3932160\ns_add_u32 m0,s51,24576\nds_read_b64_tr_b16 a[56:57],v9 offset:41472\n" \
"M v[176:179],a[16:19],a[36:39],v[176:179]\nbuffer_load_dwordx4 v21,s[32:35],s47 offen offset:0 lds\nM v[148:151],a[20:23],a[32:35],v[148:151]\nds_read_b64_tr_b16 a[58:59],v10 offset:41472\nM v[180:183],a[20:23],a[36:39],v[180:183]\nds_read_b64_tr_b16 a[60:61],v11 offset:41472\nM v[152:155],a[24:27],a[32:35],v[152:155]\ns_add_u32 m0,s51,25472\n" \
"ds_read_b64_tr_b16 a[62:63],v12 offset:41472\nM v[184:187],a[24:27],a[36:39],v[184:187]\nbuffer_load_dwordx4 v21,s[32:35],s47 offen offset:128 lds\nM v[156:159],a[28:31],a[32:35],v[156:159]\nds_read_b64_tr_b16 a[64:65],v9 offset:41984\nM v[188:191],a[28:31],a[36:39],v[188:191]\nds_read_b64_tr_b16 a[66:67],v10 offset:41984\nM v[96:99],a[0:3],a[40:43],v[96:99]\n" \
"ds_read_b64_tr_b16 a[68:69],v11 offset:41984\nM v[64:67],a[0:3],a[44:47],v[64:67]\ns_mov_b64 exec,0xff\nglobal_load_dword v0,v2,s[100:101]\ns_mov_b64 exec,-1\nM v[100:103],a[4:7],a[40:43],v[100:103]\nds_read_b64_tr_b16 a[70:71],v12 offset:41984\nM v[68:71],a[4:7],a[44:47],v[68:71]\n" \
"ds_read_b64_tr_b16 a[72:73],v9 offset:42496\nM v[104:107],a[8:11],a[40:43],v[104:107]\nds_read_b64_tr_b16 a[74:75],v10 offset:42496\nM v[72:75],a[8:11],a[44:47],v[72:75]\nM v[108:111],a[12:15],a[40:43],v[108:111]\nds_read_b64_tr_b16 a[76:77],v11 offset:42496\nM v[76:79],a[12:15],a[44:47],v[76:79]\nds_read_b64_tr_b16 a[78:79],v12 offset:42496\n" \
"M v[112:115],a[16:19],a[40:43],v[112:115]\nds_read_b128 a[88:91],v6 offset:8192\nM v[80:83],a[16:19],a[44:47],v[80:83]\nbuffer_store_dwordx4 v[224:227],v4,s[36:39],s96 offen offset:0 sc1\nM v[116:119],a[20:23],a[40:43],v[116:119]\nds_read_b128 a[92:95],v6 offset:12288\nM v[84:87],a[20:23],a[44:47],v[84:87]\nM v[120:123],a[24:27],a[40:43],v[120:123]\n" \
"s_waitcnt vmcnt(19) lgkmcnt(0)\nM v[88:91],a[24:27],a[44:47],v[88:91]\ns_barrier\nM v[124:127],a[28:31],a[40:43],v[124:127]\nM v[92:95],a[28:31],a[44:47],v[92:95]\nM v[128:131],a[48:51],a[80:83],v[128:131]\ns_mov_b32 s46,896\ns_add_u32 m0,s50,0\n" \
"ds_read_b64_tr_b16 a[0:1],v9 offset:49152\nM v[160:163],a[48:51],a[84:87],v[160:163]\nbuffer_load_dwordx4 v13,s[28:31],s46 offen lds\nM v[132:135],a[52:55],a[80:83],v[132:135]\nds_read_b64_tr_b16 a[2:3],v10 offset:49152\nM v[164:167],a[52:55],a[84:87],v[164:167]\nds_read_b128 a[32:35],v5 offset:32768\nM v[136:139],a[56:59],a[80:83],v[136:139]\n" \
"s_add_u32 m0,s50,4096\nds_read_b128 a[36:39],v5 offset:36864\nM v[168:171],a[56:59],a[84:87],v[168:171]\nbuffer_load_dwordx4 v14,s[28:31],s46 offen lds\nM v[140:143],a[60:63],a[80:83],v[140:143]\nds_read_b64_tr_b16 a[4:5],v11 offset:49152\nM v[172:175],a[60:63],a[84:87],v[172:175]\nds_read_b64_tr_b16 a[6:7],v12 offset:49152\n" \
"M v[144:147],a[64:67],a[80:83],v[144:147]\ns_add_u32 m0,s50,8192\nds_read_b64_tr_b16 a[8:9],v9 offset:49664\nM v[176:179],a[64:67],a[84:87],v[176:179]\nbuffer_load_dwordx4 v15,s[28:31],s46 offen lds\nM v[148:151],a[68:71],a[80:83],v[148:151]\nds_read_b64_tr_b16 a[10:11],v10 offset:49664\nM v[180:183],a[68:71],a[84:87],v[180:183]\n" \
"ds_read_b64_tr_b16 a[12:13],v11 offset:49664\nM v[152:155],a[72:75],a[80:83],v[152:155]\ns_add_u32 m0,s50,12288\nds_read_b64_tr_b16 a[14:15],v12 offset:49664\nM v[184:187],a[72:75],a[84:87],v[184:187]\nbuffer_load_dwordx4 v16,s[28:31],s46 offen lds\nM v[156:159],a[76:79],a[80:83],v[156:159]\nds_read_b64_tr_b16 a[16:17],v9 offset:50176\n" \
"M v[188:191],a[76:79],a[84:87],v[188:191]\nds_read_b64_tr_b16 a[18:19],v10 offset:50176\nM v[96:99],a[48:51],a[88:91],v[96:99]\ns_mov_b32 s46,896\ns_add_u32 m0,s50,16384\nds_read_b64_tr_b16 a[20:21],v11 offset:50176\nM v[64:67],a[48:51],a[92:95],v[64:67]\nbuffer_load_dwordx4 v17,s[28:31],s46 offen lds\n" \
"M v[100:103],a[52:55],a[88:91],v[100:103]\nLG11_%=:\ns_cbranch_execz LHR0_%=\nds_read_b64_tr_b16 a[22:23],v12 offset:50176\nM v[68:71],a[52:55],a[92:95],v[68:71]\nds_read_b64_tr_b16 a[24:25],v9 offset:50688\nM v[104:107],a[56:59],a[88:91],v[104:107]\ns_add_u32 m0,s50,20480\n" \
"ds_read_b64_tr_b16 a[26:27],v10 offset:50688\nM v[72:75],a[56:59],a[92:95],v[72:75]\nbuffer_load_dwordx4 v18,s[28:31],s46 offen lds\nM v[108:111],a[60:63],a[88:91],v[108:111]\nds_read_b64_tr_b16 a[28:29],v11 offset:50688\nM v[76:79],a[60:63],a[92:95],v[76:79]\nds_read_b64_tr_b16 a[30:31],v12 offset:50688\nM v[112:115],a[64:67],a[88:91],v[112:115]\n" \
"s_add_u32 m0,s50,24576\nds_read_b128 a[40:43],v5 offset:40960\nM v[80:83],a[64:67],a[92:95],v[80:83]\nbuffer_load_dwordx4 v19,s[28:31],s46 offen lds\nM v[116:119],a[68:71],a[88:91],v[116:119]\nds_read_b128 a[44:47],v5 offset:45056\nM v[84:87],a[68:71],a[92:95],v[84:87]\nM v[120:123],a[72:75],a[88:91],v[120:123]\n" \
"s_add_u32 m0,s50,28672\nM v[88:91],a[72:75],a[92:95],v[88:91]\nbuffer_load_dwordx4 v20,s[28:31],s46 offen lds\nM v[124:127],a[76:79],a[88:91],v[124:127]\ns_waitcnt lgkmcnt(0)\nM v[92:95],a[76:79],a[92:95],v[92:95]\nM v[128:131],a[0:3],a[32:35],v[128:131]\ns_mov_b32 s47,4194304\n" \
"s_add_u32 m0,s51,32768\nds_read_b64_tr_b16 a[48:49],v9 offset:57344\nM v[160:163],a[0:3],a[36:39],v[160:163]\nbuffer_load_dwordx4 v21,s[32:35],s47 offen offset:0 lds\nM v[132:135],a[4:7],a[32:35],v[132:135]\nds_read_b64_tr_b16 a[50:51],v10 offset:57344\nM v[164:167],a[4:7],a[36:39],v[164:167]\nds_read_b128 a[80:83],v6 offset:32768\n" \
"M v[136:139],a[8:11],a[32:35],v[136:139]\ns_add_u32 m0,s51,33664\nds_read_b128 a[84:87],v6 offset:36864\nM v[168:171],a[8:11],a[36:39],v[168:171]\nbuffer_load_dwordx4 v21,s[32:35],s47 offen offset:128 lds\nM v[140:143],a[12:15],a[32:35],v[140:143]\nds_read_b64_tr_b16 a[52:53],v11 offset:57344\nM v[172:175],a[12:15],a[36:39],v[172:175]\n" \
"ds_read_b64_tr_b16 a[54:55],v12 offset:57344\nM v[144:147],a[16:19],a[32:35],v[144:147]\ns_mov_b32 s47,4456448\ns_add_u32 m0,s51,40960\nds_read_b64_tr_b16 a[56:57],v9 offset:57856\nM v[176:179],a[16:19],a[36:39],v[176:179]\nbuffer_load_dwordx4 v21,s[32:35],s47 offen offset:0 lds\nM v[148:151],a[20:23],a[32:35],v[148:151]\n" \
"ds_read_b64_tr_b16 a[58:59],v10 offset:57856\nM v[180:183],a[20:23],a[36:39],v[180:183]\nds_read_b64_tr_b16 a[60:61],v11 offset:57856\nM v[152:155],a[24:27],a[32:35],v[152:155]\ns_add_u32 m0,s51,41856\nds_read_b64_tr_b16 a[62:63],v12 offset:57856\nM v[184:187],a[24:27],a[36:39],v[184:187]\nbuffer_load_dwordx4 v21,s[32:35],s47 offen offset:128 lds\n" \
"M v[156:159],a[28:31],a[32:35],v[156:159]\nds_read_b64_tr_b16 a[64:65],v9 offset:58368\nM v[188:191],a[28:31],a[36:39],v[188:191]\nds_read_b64_tr_b16 a[66:67],v10 offset:58368\nM v[96:99],a[0:3],a[40:43],v[96:99]\nds_read_b64_tr_b16 a[68:69],v11 offset:58368\nM v[64:67],a[0:3],a[44:47],v[64:67]\nM v[100:103],a[4:7],a[40:43],v[100:103]\n" \
"ds_read_b64_tr_b16 a[70:71],v12 offset:58368\nM v[68:71],a[4:7],a[44:47],v[68:71]\nds_read_b64_tr_b16 a[72:73],v9 offset:58880\nM v[104:107],a[8:11],a[40:43],v[104:107]\nds_read_b64_tr_b16 a[74:75],v10 offset:58880\nM v[72:75],a[8:11],a[44:47],v[72:75]\nM v[108:111],a[12:15],a[40:43],v[108:111]\nds_read_b64_tr_b16 a[76:77],v11 offset:58880\n" \
"M v[76:79],a[12:15],a[44:47],v[76:79]\nds_read_b64_tr_b16 a[78:79],v12 offset:58880\nM v[112:115],a[16:19],a[40:43],v[112:115]\nds_read_b128 a[88:91],v6 offset:40960\nM v[80:83],a[16:19],a[44:47],v[80:83]\nbuffer_store_dwordx4 v[228:231],v4,s[36:39],s96 offen offset:1024 sc1\nM v[116:119],a[20:23],a[40:43],v[116:119]\nds_read_b128 a[92:95],v6 offset:45056\n" \
"M v[84:87],a[20:23],a[44:47],v[84:87]\nM v[120:123],a[24:27],a[40:43],v[120:123]\ns_waitcnt vmcnt(19) lgkmcnt(0)\nM v[88:91],a[24:27],a[44:47],v[88:91]\ns_barrier\nM v[124:127],a[28:31],a[40:43],v[124:127]\nM v[92:95],a[28:31],a[44:47],v[92:95]\nM v[128:131],a[48:51],a[80:83],v[128:131]\n" \
"s_mov_b32 s46,1024\ns_add_u32 m0,s50,32768\nds_read_b64_tr_b16 a[0:1],v9 offset:0\nM v[160:163],a[48:51],a[84:87],v[160:163]\nbuffer_load_dwordx4 v13,s[28:31],s46 offen lds\nM v[132:135],a[52:55],a[80:83],v[132:135]\nds_read_b64_tr_b16 a[2:3],v10 offset:0\nM v[164:167],a[52:55],a[84:87],v[164:167]\n" \
"ds_read_b128 a[32:35],v7 offset:0\nM v[136:139],a[56:59],a[80:83],v[136:139]\ns_add_u32 m0,s50,36864\nds_read_b128 a[36:39],v7 offset:4096\nM v[168:171],a[56:59],a[84:87],v[168:171]\nbuffer_load_dwordx4 v14,s[28:31],s46 offen lds\nM v[140:143],a[60:63],a[80:83],v[140:143]\nds_read_b64_tr_b16 a[4:5],v11 offset:0\n" \
"M v[172:175],a[60:63],a[84:87],v[172:175]\nds_read_b64_tr_b16 a[6:7],v12 offset:0\nM v[144:147],a[64:67],a[80:83],v[144:147]\ns_add_u32 m0,s50,40960\nds_read_b64_tr_b16 a[8:9],v9 offset:512\nM v[176:179],a[64:67],a[84:87],v[176:179]\nbuffer_load_dwordx4 v15,s[28:31],s46 offen lds\nM v[148:151],a[68:71],a[80:83],v[148:151]\n" \
"ds_read_b64_tr_b16 a[10:11],v10 offset:512\nM v[180:183],a[68:71],a[84:87],v[180:183]\nds_read_b64_tr_b16 a[12:13],v11 offset:512\nM v[152:155],a[72:75],a[80:83],v[152:155]\ns_add_u32 m0,s50,45056\nds_read_b64_tr_b16 a[14:15],v12 offset:512\nM v[184:187],a[72:75],a[84:87],v[184:187]\nbuffer_load_dwordx4 v16,s[28:31],s46 offen lds\n" \
"M v[156:159],a[76:79],a[80:83],v[156:159]\nds_read_b64_tr_b16 a[16:17],v9 offset:1024\nM v[188:191],a[76:79],a[84:87],v[188:191]\nds_read_b64_tr_b16 a[18:19],v10 offset:1024\nM v[96:99],a[48:51],a[88:91],v[96:99]\ns_mov_b32 s46,1024\ns_add_u32 m0,s50,49152\nds_read_b64_tr_b16 a[20:21],v11 offset:1024\n" \
"M v[64:67],a[48:51],a[92:95],v[64:67]\nbuffer_load_dwordx4 v17,s[28:31],s46 offen lds\nM v[100:103],a[52:55],a[88:91],v[100:103]\nds_read_b64_tr_b16 a[22:23],v12 offset:1024\nM v[68:71],a[52:55],a[92:95],v[68:71]\nds_read_b64_tr_b16 a[24:25],v9 offset:1536\nM v[104:107],a[56:59],a[88:91],v[104:107]\ns_add_u32 m0,s50,53248\n" \
"ds_read_b64_tr_b16 a[26:27],v10 offset:1536\nM v[72:75],a[56:59],a[92:95],v[72:75]\nbuffer_load_dwordx4 v18,s[28:31],s46 offen lds\nM v[108:111],a[60:63],a[88:91],v[108:111]\nds_read_b64_tr_b16 a[28:29],v11 offset:1536\nM v[76:79],a[60:63],a[92:95],v[76:79]\nds_read_b64_tr_b16 a[30:31],v12 offset:1536\nM v[112:115],a[64:67],a[88:91],v[112:115]\n" \
"s_add_u32 m0,s50,57344\nds_read_b128 a[40:43],v7 offset:8192\nM v[80:83],a[64:67],a[92:95],v[80:83]\nbuffer_load_dwordx4 v19,s[28:31],s46 offen lds\nM v[116:119],a[68:71],a[88:91],v[116:119]\nds_read_b128 a[44:47],v7 offset:12288\nM v[84:87],a[68:71],a[92:95],v[84:87]\nM v[120:123],a[72:75],a[88:91],v[120:123]\n" \
"s_add_u32 m0,s50,61440\nM v[88:91],a[72:75],a[92:95],v[88:91]\nbuffer_load_dwordx4 v20,s[28:31],s46 offen lds\nM v[124:127],a[76:79],a[88:91],v[124:127]\ns_waitcnt lgkmcnt(0)\nM v[92:95],a[76:79],a[92:95],v[92:95]\nM v[128:131],a[0:3],a[32:35],v[128:131]\ns_mov_b32 s47,4718592\n" \
"s_add_u32 m0,s51,49152\nds_read_b64_tr_b16 a[48:49],v9 offset:8192\nM v[160:163],a[0:3],a[36:39],v[160:163]\nbuffer_load_dwordx4 v21,s[32:35],s47 offen offset:0 lds\nM v[132:135],a[4:7],a[32:35],v[132:135]\nds_read_b64_tr_b16 a[50:51],v10 offset:8192\nM v[164:167],a[4:7],a[36:39],v[164:167]\nds_read_b128 a[80:83],v8 offset:0\n" \
"M v[136:139],a[8:11],a[32:35],v[136:139]\ns_add_u32 m0,s51,50048\nds_read_b128 a[84:87],v8 offset:4096\nM v[168:171],a[8:11],a[36:39],v[168:171]\nbuffer_load_dwordx4 v21,s[32:35],s47 offen offset:128 lds\nM v[140:143],a[12:15],a[32:35],v[140:143]\nds_read_b64_tr_b16 a[52:53],v11 offset:8192\nM v[172:175],a[12:15],a[36:39],v[172:175]\n" \
"ds_read_b64_tr_b16 a[54:55],v12 offset:8192\nM v[144:147],a[16:19],a[32:35],v[144:147]\ns_mov_b32 s47,4980736\ns_add_u32 m0,s51,57344\nds_read_b64_tr_b16 a[56:57],v9 offset:8704\nM v[176:179],a[16:19],a[36:39],v[176:179]\nbuffer_load_dwordx4 v21,s[32:35],s47 offen offset:0 lds\nM v[148:151],a[20:23],a[32:35],v[148:151]\n" \
"ds_read_b64_tr_b16 a[58:59],v10 offset:8704\nM v[180:183],a[20:23],a[36:39],v[180:183]\nds_read_b64_tr_b16 a[60:61],v11 offset:8704\nM v[152:155],a[24:27],a[32:35],v[152:155]\ns_add_u32 m0,s51,58240\nds_read_b64_tr_b16 a[62:63],v12 offset:8704\nM v[184:187],a[24:27],a[36:39],v[184:187]\nbuffer_load_dwordx4 v21,s[32:35],s47 offen offset:128 lds\n" \
"M v[156:159],a[28:31],a[32:35],v[156:159]\nds_read_b64_tr_b16 a[64:65],v9 offset:9216\nM v[188:191],a[28:31],a[36:39],v[188:191]\nds_read_b64_tr_b16 a[66:67],v10 offset:9216\nM v[96:99],a[0:3],a[40:43],v[96:99]\nds_read_b64_tr_b16 a[68:69],v11 offset:9216\nM v[64:67],a[0:3],a[44:47],v[64:67]\nM v[100:103],a[4:7],a[40:43],v[100:103]\n" \
"ds_read_b64_tr_b16 a[70:71],v12 offset:9216\nM v[68:71],a[4:7],a[44:47],v[68:71]\nds_read_b64_tr_b16 a[72:73],v9 offset:9728\nM v[104:107],a[8:11],a[40:43],v[104:107]\nds_read_b64_tr_b16 a[74:75],v10 offset:9728\nM v[72:75],a[8:11],a[44:47],v[72:75]\nM v[108:111],a[12:15],a[40:43],v[108:111]\nds_read_b64_tr_b16 a[76:77],v11 offset:9728\n" \
"M v[76:79],a[12:15],a[44:47],v[76:79]\nds_read_b64_tr_b16 a[78:79],v12 offset:9728\nM v[112:115],a[16:19],a[40:43],v[112:115]\nds_read_b128 a[88:91],v8 offset:8192\nM v[80:83],a[16:19],a[44:47],v[80:83]\nbuffer_store_dwordx4 v[232:235],v4,s[36:39],s96 offen offset:2048 sc1\nM v[116:119],a[20:23],a[40:43],v[116:119]\nds_read_b128 a[92:95],v8 offset:12288\n" \
"M v[84:87],a[20:23],a[44:47],v[84:87]\nM v[120:123],a[24:27],a[40:43],v[120:123]\ns_waitcnt vmcnt(18) lgkmcnt(0)\nM v[88:91],a[24:27],a[44:47],v[88:91]\ns_barrier\nM v[124:127],a[28:31],a[40:43],v[124:127]\nM v[92:95],a[28:31],a[44:47],v[92:95]\nM v[128:131],a[48:51],a[80:83],v[128:131]\n" \
"s_mov_b32 s46,1152\ns_add_u32 m0,s50,65536\nds_read_b64_tr_b16 a[0:1],v9 offset:16384\nM v[160:163],a[48:51],a[84:87],v[160:163]\nbuffer_load_dwordx4 v13,s[28:31],s46 offen lds\nM v[132:135],a[52:55],a[80:83],v[132:135]\nds_read_b64_tr_b16 a[2:3],v10 offset:16384\nM v[164:167],a[52:55],a[84:87],v[164:167]\n" \
"ds_read_b128 a[32:35],v5 offset:0\nM v[136:139],a[56:59],a[80:83],v[136:139]\ns_add_u32 m0,s50,69632\nds_read_b128 a[36:39],v5 offset:4096\nM v[168:171],a[56:59],a[84:87],v[168:171]\nbuffer_load_dwordx4 v14,s[28:31],s46 offen lds\nM v[140:143],a[60:63],a[80:83],v[140:143]\nds_read_b64_tr_b16 a[4:5],v11 offset:16384\n" \
"M v[172:175],a[60:63],a[84:87],v[172:175]\nds_read_b64_tr_b16 a[6:7],v12 offset:16384\nM v[144:147],a[64:67],a[80:83],v[144:147]\ns_add_u32 m0,s50,73728\nds_read_b64_tr_b16 a[8:9],v9 offset:16896\nM v[176:179],a[64:67],a[84:87],v[176:179]\nbuffer_load_dwordx4 v15,s[28:31],s46 offen lds\nM v[148:151],a[68:71],a[80:83],v[148:151]\n" \
"ds_read_b64_tr_b16 a[10:11],v10 offset:16896\nM v[180:183],a[68:71],a[84:87],v[180:183]\nds_read_b64_tr_b16 a[12:13],v11 offset:16896\nM v[152:155],a[72:75],a[80:83],v[152:155]\ns_add_u32 m0,s50,77824\nds_read_b64_tr_b16 a[14:15],v12 offset:16896\nM v[184:187],a[72:75],a[84:87],v[184:187]\nbuffer_load_dwordx4 v16,s[28:31],s46 offen lds\n" \
"M v[156:159],a[76:79],a[80:83],v[156:159]\nds_read_b64_tr_b16 a[16:17],v9 offset:17408\nM v[188:191],a[76:79],a[84:87],v[188:191]\nds_read_b64_tr_b16 a[18:19],v10 offset:17408\nM v[96:99],a[48:51],a[88:91],v[96:99]\ns_mov_b32 s46,1152\ns_add_u32 m0,s50,81920\nds_read_b64_tr_b16 a[20:21],v11 offset:17408\n" \
"M v[64:67],a[48:51],a[92:95],v[64:67]\nbuffer_load_dwordx4 v17,s[28:31],s46 offen lds\nM v[100:103],a[52:55],a[88:91],v[100:103]\nds_read_b64_tr_b16 a[22:23],v12 offset:17408\nM v[68:71],a[52:55],a[92:95],v[68:71]\nds_read_b64_tr_b16 a[24:25],v9 offset:17920\nM v[104:107],a[56:59],a[88:91],v[104:107]\ns_add_u32 m0,s50,86016\n" \
"ds_read_b64_tr_b16 a[26:27],v10 offset:17920\nM v[72:75],a[56:59],a[92:95],v[72:75]\nbuffer_load_dwordx4 v18,s[28:31],s46 offen lds\nM v[108:111],a[60:63],a[88:91],v[108:111]\nds_read_b64_tr_b16 a[28:29],v11 offset:17920\nM v[76:79],a[60:63],a[92:95],v[76:79]\nds_read_b64_tr_b16 a[30:31],v12 offset:17920\nM v[112:115],a[64:67],a[88:91],v[112:115]\n" \
"s_add_u32 m0,s50,90112\nds_read_b128 a[40:43],v5 offset:8192\nM v[80:83],a[64:67],a[92:95],v[80:83]\nbuffer_load_dwordx4 v19,s[28:31],s46 offen lds\nM v[116:119],a[68:71],a[88:91],v[116:119]\nds_read_b128 a[44:47],v5 offset:12288\nM v[84:87],a[68:71],a[92:95],v[84:87]\nM v[120:123],a[72:75],a[88:91],v[120:123]\n" \
"s_add_u32 m0,s50,94208\nM v[88:91],a[72:75],a[92:95],v[88:91]\nbuffer_load_dwordx4 v20,s[28:31],s46 offen lds\nM v[124:127],a[76:79],a[88:91],v[124:127]\ns_waitcnt lgkmcnt(0)\nM v[92:95],a[76:79],a[92:95],v[92:95]\nM v[128:131],a[0:3],a[32:35],v[128:131]\ns_mov_b32 s47,5242880\n" \
"s_add_u32 m0,s51,0\nds_read_b64_tr_b16 a[48:49],v9 offset:24576\nM v[160:163],a[0:3],a[36:39],v[160:163]\nbuffer_load_dwordx4 v21,s[32:35],s47 offen offset:0 lds\nM v[132:135],a[4:7],a[32:35],v[132:135]\nds_read_b64_tr_b16 a[50:51],v10 offset:24576\nM v[164:167],a[4:7],a[36:39],v[164:167]\nds_read_b128 a[80:83],v6 offset:0\n" \
"M v[136:139],a[8:11],a[32:35],v[136:139]\ns_add_u32 m0,s51,896\nds_read_b128 a[84:87],v6 offset:4096\nM v[168:171],a[8:11],a[36:39],v[168:171]\nbuffer_load_dwordx4 v21,s[32:35],s47 offen offset:128 lds\nM v[140:143],a[12:15],a[32:35],v[140:143]\nds_read_b64_tr_b16 a[52:53],v11 offset:24576\nM v[172:175],a[12:15],a[36:39],v[172:175]\n" \
"ds_read_b64_tr_b16 a[54:55],v12 offset:24576\nM v[144:147],a[16:19],a[32:35],v[144:147]\ns_mov_b32 s47,5505024\ns_add_u32 m0,s51,8192\nds_read_b64_tr_b16 a[56:57],v9 offset:25088\nM v[176:179],a[16:19],a[36:39],v[176:179]\nbuffer_load_dwordx4 v21,s[32:35],s47 offen offset:0 lds\nM v[148:151],a[20:23],a[32:35],v[148:151]\n" \
"ds_read_b64_tr_b16 a[58:59],v10 offset:25088\nM v[180:183],a[20:23],a[36:39],v[180:183]\nds_read_b64_tr_b16 a[60:61],v11 offset:25088\nM v[152:155],a[24:27],a[32:35],v[152:155]\ns_add_u32 m0,s51,9088\nds_read_b64_tr_b16 a[62:63],v12 offset:25088\nM v[184:187],a[24:27],a[36:39],v[184:187]\nbuffer_load_dwordx4 v21,s[32:35],s47 offen offset:128 lds\n" \
"M v[156:159],a[28:31],a[32:35],v[156:159]\nds_read_b64_tr_b16 a[64:65],v9 offset:25600\nM v[188:191],a[28:31],a[36:39],v[188:191]\nds_read_b64_tr_b16 a[66:67],v10 offset:25600\nM v[96:99],a[0:3],a[40:43],v[96:99]\nds_read_b64_tr_b16 a[68:69],v11 offset:25600\nM v[64:67],a[0:3],a[44:47],v[64:67]\nM v[100:103],a[4:7],a[40:43],v[100:103]\n" \
"ds_read_b64_tr_b16 a[70:71],v12 offset:25600\nM v[68:71],a[4:7],a[44:47],v[68:71]\nds_read_b64_tr_b16 a[72:73],v9 offset:26112\nM v[104:107],a[8:11],a[40:43],v[104:107]\nds_read_b64_tr_b16 a[74:75],v10 offset:26112\nM v[72:75],a[8:11],a[44:47],v[72:75]\nM v[108:111],a[12:15],a[40:43],v[108:111]\nds_read_b64_tr_b16 a[76:77],v11 offset:26112\n" \
"M v[76:79],a[12:15],a[44:47],v[76:79]\nds_read_b64_tr_b16 a[78:79],v12 offset:26112\nM v[112:115],a[16:19],a[40:43],v[112:115]\nds_read_b128 a[88:91],v6 offset:8192\nM v[80:83],a[16:19],a[44:47],v[80:83]\nbuffer_store_dwordx4 v[236:239],v4,s[36:39],s96 offen offset:3072 sc1\ns_add_u32 s96,s57,20480\nM v[116:119],a[20:23],a[40:43],v[116:119]\n" \
"ds_read_b128 a[92:95],v6 offset:12288\nM v[84:87],a[20:23],a[44:47],v[84:87]\nM v[120:123],a[24:27],a[40:43],v[120:123]\ns_waitcnt vmcnt(18) lgkmcnt(0)\nM v[88:91],a[24:27],a[44:47],v[88:91]\ns_barrier\nM v[124:127],a[28:31],a[40:43],v[124:127]\nM v[92:95],a[28:31],a[44:47],v[92:95]\n" \
"M v[128:131],a[48:51],a[80:83],v[128:131]\ns_mov_b32 s46,1280\ns_add_u32 m0,s50,0\nds_read_b64_tr_b16 a[0:1],v9 offset:32768\nM v[160:163],a[48:51],a[84:87],v[160:163]\nbuffer_load_dwordx4 v13,s[28:31],s46 offen lds\nM v[132:135],a[52:55],a[80:83],v[132:135]\nds_read_b64_tr_b16 a[2:3],v10 offset:32768\n" \
"M v[164:167],a[52:55],a[84:87],v[164:167]\nds_read_b128 a[32:35],v5 offset:32768\nM v[136:139],a[56:59],a[80:83],v[136:139]\ns_add_u32 m0,s50,4096\nds_read_b128 a[36:39],v5 offset:36864\nM v[168:171],a[56:59],a[84:87],v[168:171]\nbuffer_load_dwordx4 v14,s[28:31],s46 offen lds\nM v[140:143],a[60:63],a[80:83],v[140:143]\n" \
"ds_read_b64_tr_b16 a[4:5],v11 offset:32768\nM v[172:175],a[60:63],a[84:87],v[172:175]\nds_read_b64_tr_b16 a[6:7],v12 offset:32768\nM v[144:147],a[64:67],a[80:83],v[144:147]\ns_add_u32 m0,s50,8192\nds_read_b64_tr_b16 a[8:9],v9 offset:33280\nM v[176:179],a[64:67],a[84:87],v[176:179]\nbuffer_load_dwordx4 v15,s[28:31],s46 offen lds\n" \
"M v[148:151],a[68:71],a[80:83],v[148:151]\nds_read_b64_tr_b16 a[10:11],v10 offset:33280\nM v[180:183],a[68:71],a[84:87],v[180:183]\nds_read_b64_tr_b16 a[12:13],v11 offset:33280\nM v[152:155],a[72:75],a[80:83],v[152:155]\ns_add_u32 m0,s50,12288\nds_read_b64_tr_b16 a[14:15],v12 offset:33280\nM v[184:187],a[72:75],a[84:87],v[184:187]\n" \
"buffer_load_dwordx4 v16,s[28:31],s46 offen lds\nM v[156:159],a[76:79],a[80:83],v[156:159]\nds_read_b64_tr_b16 a[16:17],v9 offset:33792\nM v[188:191],a[76:79],a[84:87],v[188:191]\nds_read_b64_tr_b16 a[18:19],v10 offset:33792\nM v[96:99],a[48:51],a[88:91],v[96:99]\ns_mov_b32 s46,1280\ns_add_u32 m0,s50,16384\n" \
"ds_read_b64_tr_b16 a[20:21],v11 offset:33792\nM v[64:67],a[48:51],a[92:95],v[64:67]\nbuffer_load_dwordx4 v17,s[28:31],s46 offen lds\nM v[100:103],a[52:55],a[88:91],v[100:103]\nds_read_b64_tr_b16 a[22:23],v12 offset:33792\nM v[68:71],a[52:55],a[92:95],v[68:71]\nds_read_b64_tr_b16 a[24:25],v9 offset:34304\nM v[104:107],a[56:59],a[88:91],v[104:107]\n" \
"s_add_u32 m0,s50,20480\nds_read_b64_tr_b16 a[26:27],v10 offset:34304\nM v[72:75],a[56:59],a[92:95],v[72:75]\nbuffer_load_dwordx4 v18,s[28:31],s46 offen lds\nM v[108:111],a[60:63],a[88:91],v[108:111]\nds_read_b64_tr_b16 a[28:29],v11 offset:34304\nM v[76:79],a[60:63],a[92:95],v[76:79]\nds_read_b64_tr_b16 a[30:31],v12 offset:34304\n" \
"M v[112:115],a[64:67],a[88:91],v[112:115]\ns_add_u32 m0,s50,24576\nds_read_b128 a[40:43],v5 offset:40960\nM v[80:83],a[64:67],a[92:95],v[80:83]\nbuffer_load_dwordx4 v19,s[28:31],s46 offen lds\nM v[116:119],a[68:71],a[88:91],v[116:119]\nds_read_b128 a[44:47],v5 offset:45056\nM v[84:87],a[68:71],a[92:95],v[84:87]\n" \
"M v[120:123],a[72:75],a[88:91],v[120:123]\ns_add_u32 m0,s50,28672\nM v[88:91],a[72:75],a[92:95],v[88:91]\nbuffer_load_dwordx4 v20,s[28:31],s46 offen lds\nM v[124:127],a[76:79],a[88:91],v[124:127]\ns_waitcnt lgkmcnt(0)\nM v[92:95],a[76:79],a[92:95],v[92:95]\nM v[128:131],a[0:3],a[32:35],v[128:131]\n" \
"s_mov_b32 s47,5767168\ns_add_u32 m0,s51,16384\nds_read_b64_tr_b16 a[48:49],v9 offset:40960\nM v[160:163],a[0:3],a[36:39],v[160:163]\nbuffer_load_dwordx4 v21,s[32:35],s47 offen offset:0 lds\ns_bfe_u32 s62,%1,0x20001\ns_lshl_b32 s62,s62,21\nM v[132:135],a[4:7],a[32:35],v[132:135]\n" \
"ds_read_b64_tr_b16 a[50:51],v10 offset:40960\ns_lshr_b32 s63,%1,5\ns_lshl_b32 s63,s63,8\nM v[164:167],a[4:7],a[36:39],v[164:167]\nds_read_b128 a[80:83],v6 offset:32768\ns_add_u32 s62,s62,s63\ns_sub_u32 s86,8388608,s62\nM v[136:139],a[8:11],a[32:35],v[136:139]\n" \
"s_add_u32 m0,s51,17280\nds_read_b128 a[84:87],v6 offset:36864\ns_mov_b32 s87,0x00020000\nM v[168:171],a[8:11],a[36:39],v[168:171]\nbuffer_load_dwordx4 v21,s[32:35],s47 offen offset:128 lds\ns_add_u32 s84,s84,s62\ns_addc_u32 s85,s85,0\ns_and_b32 s85,s85,0xffff\n" \
"M v[140:143],a[12:15],a[32:35],v[140:143]\nds_read_b64_tr_b16 a[52:53],v11 offset:40960\ns_and_b32 s89,s89,0xffff\ns_mov_b32 s90,4194304\nM v[172:175],a[12:15],a[36:39],v[172:175]\nds_read_b64_tr_b16 a[54:55],v12 offset:40960\ns_mov_b32 s91,0x00020000\nv_lshrrev_b32 v60,8,v4\n" \
"M v[144:147],a[16:19],a[32:35],v[144:147]\ns_mov_b32 s47,6029312\ns_add_u32 m0,s51,24576\nds_read_b64_tr_b16 a[56:57],v9 offset:41472\nM v[176:179],a[16:19],a[36:39],v[176:179]\nbuffer_load_dwordx4 v21,s[32:35],s47 offen offset:0 lds\nM v[148:151],a[20:23],a[32:35],v[148:151]\nds_read_b64_tr_b16 a[58:59],v10 offset:41472\n" \
"v_bfe_u32 v61,v4,4,4\nM v[180:183],a[20:23],a[36:39],v[180:183]\nds_read_b64_tr_b16 a[60:61],v11 offset:41472\nv_lshlrev_b32 v60,2,v60\nM v[152:155],a[24:27],a[32:35],v[152:155]\ns_add_u32 m0,s51,25472\nds_read_b64_tr_b16 a[62:63],v12 offset:41472\nv_sub_u32 v60,v60,v61\n" \
"M v[184:187],a[24:27],a[36:39],v[184:187]\nbuffer_load_dwordx4 v21,s[32:35],s47 offen offset:128 lds\ns_and_b32 s63,%2,1\ns_lshl_b32 s62,s63,4\nM v[156:159],a[28:31],a[32:35],v[156:159]\nds_read_b64_tr_b16 a[64:65],v9 offset:41984\nv_subrev_u32 v60,s62,v60\nM v[188:191],a[28:31],a[36:39],v[188:191]\n" \
"ds_read_b64_tr_b16 a[66:67],v10 offset:41984\nv_mov_b32 v61,0xff800000\nM v[96:99],a[0:3],a[40:43],v[96:99]\nds_read_b64_tr_b16 a[68:69],v11 offset:41984\nv_cmp_lt_i32 vcc,0,v60\ns_nop 1\nM v[64:67],a[0:3],a[44:47],v[64:67]\nv_cndmask_b32 v48,0,v61,vcc\n" \
"v_cmp_lt_i32 vcc,-1,v60\ns_nop 1\nM v[100:103],a[4:7],a[40:43],v[100:103]\nds_read_b64_tr_b16 a[70:71],v12 offset:41984\nv_cndmask_b32 v49,0,v61,vcc\nM v[68:71],a[4:7],a[44:47],v[68:71]\nds_read_b64_tr_b16 a[72:73],v9 offset:42496\nv_cmp_lt_i32 vcc,-2,v60\n" \
"s_nop 1\nM v[104:107],a[8:11],a[40:43],v[104:107]\nds_read_b64_tr_b16 a[74:75],v10 offset:42496\nv_cndmask_b32 v50,0,v61,vcc\nM v[72:75],a[8:11],a[44:47],v[72:75]\nv_cmp_lt_i32 vcc,-3,v60\ns_nop 1\nM v[108:111],a[12:15],a[40:43],v[108:111]\n" \
"ds_read_b64_tr_b16 a[76:77],v11 offset:42496\nv_cndmask_b32 v51,0,v61,vcc\nM v[76:79],a[12:15],a[44:47],v[76:79]\nds_read_b64_tr_b16 a[78:79],v12 offset:42496\nv_cmp_lt_i32 vcc,-16,v60\ns_nop 1\nM v[112:115],a[16:19],a[40:43],v[112:115]\nds_read_b128 a[88:91],v6 offset:40960\n" \
"v_cndmask_b32 v52,0,v61,vcc\nM v[80:83],a[16:19],a[44:47],v[80:83]\nbuffer_store_dwordx4 v[240:243],v4,s[36:39],s96 offen offset:0 sc1\nM v[116:119],a[20:23],a[40:43],v[116:119]\nds_read_b128 a[92:95],v6 offset:45056\nv_cmp_lt_i32 vcc,-17,v60\ns_nop 1\nM v[84:87],a[20:23],a[44:47],v[84:87]\n" \
"LG12_%=:\ns_cbranch_execz LG16_%=\nv_cndmask_b32 v53,0,v61,vcc\nv_cmp_lt_i32 vcc,-18,v60\ns_nop 1\nM v[120:123],a[24:27],a[40:43],v[120:123]\ns_waitcnt vmcnt(18) lgkmcnt(0)\nv_cndmask_b32 v54,0,v61,vcc\n" \
"M v[88:91],a[24:27],a[44:47],v[88:91]\ns_barrier\nM v[124:127],a[28:31],a[40:43],v[124:127]\nv_cmp_lt_i32 vcc,-19,v60\ns_nop 1\nM v[92:95],a[28:31],a[44:47],v[92:95]\nv_cndmask_b32 v55,0,v61,vcc\nv_mov_b32 v24,0\n" \
"M v[128:131],a[48:51],a[80:83],v[128:131]\ns_mov_b32 s46,1408\ns_add_u32 m0,s50,32768\nds_read_b64_tr_b16 a[0:1],v9 offset:49152\nM v[160:163],a[48:51],a[84:87],v[160:163]\nbuffer_load_dwordx4 v13,s[28:31],s46 offen lds\nM v[132:135],a[52:55],a[80:83],v[132:135]\nds_read_b64_tr_b16 a[2:3],v10 offset:49152\n" \
"v_mov_b32 v25,0\nM v[164:167],a[52:55],a[84:87],v[164:167]\nds_read_b128 a[32:35],v7 offset:0\nv_mov_b32 v26,0\nM v[136:139],a[56:59],a[80:83],v[136:139]\ns_add_u32 m0,s50,36864\nds_read_b128 a[36:39],v7 offset:4096\nv_mov_b32 v27,0\n" \
"M v[168:171],a[56:59],a[84:87],v[168:171]\nbuffer_load_dwordx4 v14,s[28:31],s46 offen lds\ns_xor_b32 s62,s22,0x2000\ns_add_u32 s62,s62,84992\nM v[140:143],a[60:63],a[80:83],v[140:143]\nds_read_b64_tr_b16 a[4:5],v11 offset:49152\nv_add_u32 v23,s62,v4\nM v[172:175],a[60:63],a[84:87],v[172:175]\n" \
"ds_read_b64_tr_b16 a[6:7],v12 offset:49152\ns_memrealtime s[10:11]\ns_and_b32 s62,%2,1\nM v[144:147],a[64:67],a[80:83],v[144:147]\ns_add_u32 m0,s50,40960\nds_read_b64_tr_b16 a[8:9],v9 offset:49664\ns_lshl_b32 s62,s62,15\nM v[176:179],a[64:67],a[84:87],v[176:179]\n" \
"buffer_load_dwordx4 v15,s[28:31],s46 offen lds\ns_add_u32 s62,s62,s54\ns_add_u32 s62,s62,32768\nM v[148:151],a[68:71],a[80:83],v[148:151]\nds_read_b64_tr_b16 a[10:11],v10 offset:49664\nv_add_u32 v1,s62,v4\nM v[180:183],a[68:71],a[84:87],v[180:183]\nds_read_b64_tr_b16 a[12:13],v11 offset:49664\n" \
"M v[152:155],a[72:75],a[80:83],v[152:155]\ns_add_u32 m0,s50,45056\nds_read_b64_tr_b16 a[14:15],v12 offset:49664\nM v[184:187],a[72:75],a[84:87],v[184:187]\nbuffer_load_dwordx4 v16,s[28:31],s46 offen lds\nM v[156:159],a[76:79],a[80:83],v[156:159]\nds_read_b64_tr_b16 a[16:17],v9 offset:50176\nM v[188:191],a[76:79],a[84:87],v[188:191]\n" \
"ds_read_b64_tr_b16 a[18:19],v10 offset:50176\nM v[96:99],a[48:51],a[88:91],v[96:99]\ns_mov_b32 s46,1408\ns_add_u32 m0,s50,49152\nds_read_b64_tr_b16 a[20:21],v11 offset:50176\nM v[64:67],a[48:51],a[92:95],v[64:67]\nbuffer_load_dwordx4 v17,s[28:31],s46 offen lds\nM v[100:103],a[52:55],a[88:91],v[100:103]\n" \
"ds_read_b64_tr_b16 a[22:23],v12 offset:50176\nM v[68:71],a[52:55],a[92:95],v[68:71]\nds_read_b64_tr_b16 a[24:25],v9 offset:50688\nM v[104:107],a[56:59],a[88:91],v[104:107]\ns_add_u32 m0,s50,53248\nds_read_b64_tr_b16 a[26:27],v10 offset:50688\nM v[72:75],a[56:59],a[92:95],v[72:75]\nbuffer_load_dwordx4 v18,s[28:31],s46 offen lds\n" \
"M v[108:111],a[60:63],a[88:91],v[108:111]\nds_read_b64_tr_b16 a[28:29],v11 offset:50688\nM v[76:79],a[60:63],a[92:95],v[76:79]\nds_read_b64_tr_b16 a[30:31],v12 offset:50688\nM v[112:115],a[64:67],a[88:91],v[112:115]\ns_add_u32 m0,s50,57344\nds_read_b128 a[40:43],v7 offset:8192\nM v[80:83],a[64:67],a[92:95],v[80:83]\n" \
"buffer_load_dwordx4 v19,s[28:31],s46 offen lds\nM v[116:119],a[68:71],a[88:91],v[116:119]\nds_read_b128 a[44:47],v7 offset:12288\nM v[84:87],a[68:71],a[92:95],v[84:87]\nM v[120:123],a[72:75],a[88:91],v[120:123]\ns_add_u32 m0,s50,61440\nM v[88:91],a[72:75],a[92:95],v[88:91]\nbuffer_load_dwordx4 v20,s[28:31],s46 offen lds\n" \
"M v[124:127],a[76:79],a[88:91],v[124:127]\ns_waitcnt lgkmcnt(0)\nM v[92:95],a[76:79],a[92:95],v[92:95]\nM v[128:131],a[0:3],a[32:35],v[128:131]\ns_mov_b32 s47,6291456\ns_add_u32 m0,s51,32768\nds_read_b64_tr_b16 a[48:49],v9 offset:57344\nM v[160:163],a[0:3],a[36:39],v[160:163]\n" \
"buffer_load_dwordx4 v21,s[32:35],s47 offen offset:0 lds\nM v[132:135],a[4:7],a[32:35],v[132:135]\nds_read_b64_tr_b16 a[50:51],v10 offset:57344\nM v[164:167],a[4:7],a[36:39],v[164:167]\nds_read_b128 a[80:83],v8 offset:0\nM v[136:139],a[8:11],a[32:35],v[136:139]\ns_add_u32 m0,s51,33664\nds_read_b128 a[84:87],v8 offset:4096\n" \
"M v[168:171],a[8:11],a[36:39],v[168:171]\nbuffer_load_dwordx4 v21,s[32:35],s47 offen offset:128 lds\nM v[140:143],a[12:15],a[32:35],v[140:143]\nds_read_b64_tr_b16 a[52:53],v11 offset:57344\nM v[172:175],a[12:15],a[36:39],v[172:175]\nds_read_b64_tr_b16 a[54:55],v12 offset:57344\nM v[144:147],a[16:19],a[32:35],v[144:147]\ns_mov_b32 s47,6553600\n" \
"s_add_u32 m0,s51,40960\nds_read_b64_tr_b16 a[56:57],v9 offset:57856\nM v[176:179],a[16:19],a[36:39],v[176:179]\nbuffer_load_dwordx4 v21,s[32:35],s47 offen offset:0 lds\nM v[148:151],a[20:23],a[32:35],v[148:151]\nds_read_b64_tr_b16 a[58:59],v10 offset:57856\nM v[180:183],a[20:23],a[36:39],v[180:183]\nds_read_b64_tr_b16 a[60:61],v11 offset:57856\n" \
"M v[152:155],a[24:27],a[32:35],v[152:155]\ns_add_u32 m0,s51,41856\nds_read_b64_tr_b16 a[62:63],v12 offset:57856\nM v[184:187],a[24:27],a[36:39],v[184:187]\nbuffer_load_dwordx4 v21,s[32:35],s47 offen offset:128 lds\nM v[156:159],a[28:31],a[32:35],v[156:159]\nds_read_b64_tr_b16 a[64:65],v9 offset:58368\nM v[188:191],a[28:31],a[36:39],v[188:191]\n" \
"ds_read_b64_tr_b16 a[66:67],v10 offset:58368\nM v[96:99],a[0:3],a[40:43],v[96:99]\nds_read_b64_tr_b16 a[68:69],v11 offset:58368\nM v[64:67],a[0:3],a[44:47],v[64:67]\nM v[100:103],a[4:7],a[40:43],v[100:103]\nds_read_b64_tr_b16 a[70:71],v12 offset:58368\nM v[68:71],a[4:7],a[44:47],v[68:71]\nds_read_b64_tr_b16 a[72:73],v9 offset:58880\n" \
"M v[104:107],a[8:11],a[40:43],v[104:107]\nds_read_b64_tr_b16 a[74:75],v10 offset:58880\nM v[72:75],a[8:11],a[44:47],v[72:75]\nM v[108:111],a[12:15],a[40:43],v[108:111]\nds_read_b64_tr_b16 a[76:77],v11 offset:58880\nM v[76:79],a[12:15],a[44:47],v[76:79]\nds_read_b64_tr_b16 a[78:79],v12 offset:58880\nM v[112:115],a[16:19],a[40:43],v[112:115]\n" \
"ds_read_b128 a[88:91],v8 offset:8192\nM v[80:83],a[16:19],a[44:47],v[80:83]\nbuffer_store_dwordx4 v[244:247],v4,s[36:39],s96 offen offset:1024 sc1\nM v[116:119],a[20:23],a[40:43],v[116:119]\nds_read_b128 a[92:95],v8 offset:12288\nM v[84:87],a[20:23],a[44:47],v[84:87]\nM v[120:123],a[24:27],a[40:43],v[120:123]\ns_waitcnt vmcnt(18) lgkmcnt(0)\n" \
"M v[88:91],a[24:27],a[44:47],v[88:91]\ns_barrier\nM v[124:127],a[28:31],a[40:43],v[124:127]\nM v[92:95],a[28:31],a[44:47],v[92:95]\nM v[128:131],a[48:51],a[80:83],v[128:131]\ns_mov_b32 s46,1536\ns_add_u32 m0,s50,65536\nds_read_b64_tr_b16 a[0:1],v9 offset:0\n" \
"M v[160:163],a[48:51],a[84:87],v[160:163]\nbuffer_load_dwordx4 v13,s[28:31],s46 offen lds\nM v[132:135],a[52:55],a[80:83],v[132:135]\nds_read_b64_tr_b16 a[2:3],v10 offset:0\nM v[164:167],a[52:55],a[84:87],v[164:167]\nds_read_b128 a[32:35],v5 offset:0\nM v[136:139],a[56:59],a[80:83],v[136:139]\ns_add_u32 m0,s50,69632\n" \
"ds_read_b128 a[36:39],v5 offset:4096\nM v[168:171],a[56:59],a[84:87],v[168:171]\nbuffer_load_dwordx4 v14,s[28:31],s46 offen lds\nM v[140:143],a[60:63],a[80:83],v[140:143]\nds_read_b64_tr_b16 a[4:5],v11 offset:0\nM v[172:175],a[60:63],a[84:87],v[172:175]\nds_read_b64_tr_b16 a[6:7],v12 offset:0\nM v[144:147],a[64:67],a[80:83],v[144:147]\n" \
"s_add_u32 m0,s50,73728\nds_read_b64_tr_b16 a[8:9],v9 offset:512\nM v[176:179],a[64:67],a[84:87],v[176:179]\nbuffer_load_dwordx4 v15,s[28:31],s46 offen lds\nM v[148:151],a[68:71],a[80:83],v[148:151]\nds_read_b64_tr_b16 a[10:11],v10 offset:512\nM v[180:183],a[68:71],a[84:87],v[180:183]\nds_read_b64_tr_b16 a[12:13],v11 offset:512\n" \
"M v[152:155],a[72:75],a[80:83],v[152:155]\ns_add_u32 m0,s50,77824\nds_read_b64_tr_b16 a[14:15],v12 offset:512\nM v[184:187],a[72:75],a[84:87],v[184:187]\nbuffer_load_dwordx4 v16,s[28:31],s46 offen lds\nM v[156:159],a[76:79],a[80:83],v[156:159]\nds_read_b64_tr_b16 a[16:17],v9 offset:1024\nM v[188:191],a[76:79],a[84:87],v[188:191]\n" \
"ds_read_b64_tr_b16 a[18:19],v10 offset:1024\nM v[96:99],a[48:51],a[88:91],v[96:99]\ns_mov_b32 s46,1536\ns_add_u32 m0,s50,81920\nds_read_b64_tr_b16 a[20:21],v11 offset:1024\nM v[64:67],a[48:51],a[92:95],v[64:67]\nbuffer_load_dwordx4 v17,s[28:31],s46 offen lds\nM v[100:103],a[52:55],a[88:91],v[100:103]\n" \
"ds_read_b64_tr_b16 a[22:23],v12 offset:1024\nM v[68:71],a[52:55],a[92:95],v[68:71]\nds_read_b64_tr_b16 a[24:25],v9 offset:1536\nM v[104:107],a[56:59],a[88:91],v[104:107]\ns_add_u32 m0,s50,86016\nds_read_b64_tr_b16 a[26:27],v10 offset:1536\nM v[72:75],a[56:59],a[92:95],v[72:75]\nbuffer_load_dwordx4 v18,s[28:31],s46 offen lds\n" \
"M v[108:111],a[60:63],a[88:91],v[108:111]\nds_read_b64_tr_b16 a[28:29],v11 offset:1536\nM v[76:79],a[60:63],a[92:95],v[76:79]\nds_read_b64_tr_b16 a[30:31],v12 offset:1536\nM v[112:115],a[64:67],a[88:91],v[112:115]\ns_add_u32 m0,s50,90112\nds_read_b128 a[40:43],v5 offset:8192\nM v[80:83],a[64:67],a[92:95],v[80:83]\n" \
"buffer_load_dwordx4 v19,s[28:31],s46 offen lds\nM v[116:119],a[68:71],a[88:91],v[116:119]\nds_read_b128 a[44:47],v5 offset:12288\nM v[84:87],a[68:71],a[92:95],v[84:87]\nM v[120:123],a[72:75],a[88:91],v[120:123]\ns_add_u32 m0,s50,94208\nM v[88:91],a[72:75],a[92:95],v[88:91]\nbuffer_load_dwordx4 v20,s[28:31],s46 offen lds\n" \
"M v[124:127],a[76:79],a[88:91],v[124:127]\ns_waitcnt lgkmcnt(0)\nM v[92:95],a[76:79],a[92:95],v[92:95]\nM v[128:131],a[0:3],a[32:35],v[128:131]\ns_mov_b32 s47,6815744\ns_add_u32 m0,s51,49152\nds_read_b64_tr_b16 a[48:49],v9 offset:8192\nM v[160:163],a[0:3],a[36:39],v[160:163]\n" \
"buffer_load_dwordx4 v21,s[32:35],s47 offen offset:0 lds\nM v[132:135],a[4:7],a[32:35],v[132:135]\nds_read_b64_tr_b16 a[50:51],v10 offset:8192\nM v[164:167],a[4:7],a[36:39],v[164:167]\nds_read_b128 a[80:83],v6 offset:0\nM v[136:139],a[8:11],a[32:35],v[136:139]\ns_add_u32 m0,s51,50048\nds_read_b128 a[84:87],v6 offset:4096\n" \
"M v[168:171],a[8:11],a[36:39],v[168:171]\nbuffer_load_dwordx4 v21,s[32:35],s47 offen offset:128 lds\nM v[140:143],a[12:15],a[32:35],v[140:143]\nds_read_b64_tr_b16 a[52:53],v11 offset:8192\nM v[172:175],a[12:15],a[36:39],v[172:175]\nds_read_b64_tr_b16 a[54:55],v12 offset:8192\nM v[144:147],a[16:19],a[32:35],v[144:147]\ns_mov_b32 s47,7077888\n" \
"s_add_u32 m0,s51,57344\nds_read_b64_tr_b16 a[56:57],v9 offset:8704\nM v[176:179],a[16:19],a[36:39],v[176:179]\nbuffer_load_dwordx4 v21,s[32:35],s47 offen offset:0 lds\nM v[148:151],a[20:23],a[32:35],v[148:151]\nds_read_b64_tr_b16 a[58:59],v10 offset:8704\nM v[180:183],a[20:23],a[36:39],v[180:183]\nds_read_b64_tr_b16 a[60:61],v11 offset:8704\n" \
"M v[152:155],a[24:27],a[32:35],v[152:155]\ns_add_u32 m0,s51,58240\nds_read_b64_tr_b16 a[62:63],v12 offset:8704\nM v[184:187],a[24:27],a[36:39],v[184:187]\nbuffer_load_dwordx4 v21,s[32:35],s47 offen offset:128 lds\nM v[156:159],a[28:31],a[32:35],v[156:159]\nds_read_b64_tr_b16 a[64:65],v9 offset:9216\nM v[188:191],a[28:31],a[36:39],v[188:191]\n" \
"ds_read_b64_tr_b16 a[66:67],v10 offset:9216\nM v[96:99],a[0:3],a[40:43],v[96:99]\nds_read_b64_tr_b16 a[68:69],v11 offset:9216\nM v[64:67],a[0:3],a[44:47],v[64:67]\ns_mov_b64 exec,0xff\nglobal_load_dword v0,v2,s[100:101]\ns_mov_b64 exec,-1\nM v[100:103],a[4:7],a[40:43],v[100:103]\n" \
"ds_read_b64_tr_b16 a[70:71],v12 offset:9216\nM v[68:71],a[4:7],a[44:47],v[68:71]\nds_read_b64_tr_b16 a[72:73],v9 offset:9728\nM v[104:107],a[8:11],a[40:43],v[104:107]\nds_read_b64_tr_b16 a[74:75],v10 offset:9728\nM v[72:75],a[8:11],a[44:47],v[72:75]\nM v[108:111],a[12:15],a[40:43],v[108:111]\nds_read_b64_tr_b16 a[76:77],v11 offset:9728\n" \
"M v[76:79],a[12:15],a[44:47],v[76:79]\nds_read_b64_tr_b16 a[78:79],v12 offset:9728\nM v[112:115],a[16:19],a[40:43],v[112:115]\nds_read_b128 a[88:91],v6 offset:8192\nM v[80:83],a[16:19],a[44:47],v[80:83]\nbuffer_store_dwordx4 v[248:251],v4,s[36:39],s96 offen offset:2048 sc1\nM v[116:119],a[20:23],a[40:43],v[116:119]\nds_read_b128 a[92:95],v6 offset:12288\n" \
"M v[84:87],a[20:23],a[44:47],v[84:87]\nM v[120:123],a[24:27],a[40:43],v[120:123]\ns_waitcnt vmcnt(19) lgkmcnt(0)\nM v[88:91],a[24:27],a[44:47],v[88:91]\ns_barrier\nM v[124:127],a[28:31],a[40:43],v[124:127]\nM v[92:95],a[28:31],a[44:47],v[92:95]\nM v[128:131],a[48:51],a[80:83],v[128:131]\n" \
"s_mov_b32 s46,1664\ns_add_u32 m0,s50,0\nds_read_b64_tr_b16 a[0:1],v9 offset:16384\nM v[160:163],a[48:51],a[84:87],v[160:163]\nbuffer_load_dwordx4 v13,s[28:31],s46 offen lds\nM v[132:135],a[52:55],a[80:83],v[132:135]\nds_read_b64_tr_b16 a[2:3],v10 offset:16384\nM v[164:167],a[52:55],a[84:87],v[164:167]\n" \
"ds_read_b128 a[32:35],v5 offset:32768\nM v[136:139],a[56:59],a[80:83],v[136:139]\ns_add_u32 m0,s50,4096\nds_read_b128 a[36:39],v5 offset:36864\nM v[168:171],a[56:59],a[84:87],v[168:171]\nbuffer_load_dwordx4 v14,s[28:31],s46 offen lds\nM v[140:143],a[60:63],a[80:83],v[140:143]\nds_read_b64_tr_b16 a[4:5],v11 offset:16384\n" \
"M v[172:175],a[60:63],a[84:87],v[172:175]\nds_read_b64_tr_b16 a[6:7],v12 offset:16384\nM v[144:147],a[64:67],a[80:83],v[144:147]\ns_add_u32 m0,s50,8192\nds_read_b64_tr_b16 a[8:9],v9 offset:16896\nM v[176:179],a[64:67],a[84:87],v[176:179]\nbuffer_load_dwordx4 v15,s[28:31],s46 offen lds\nM v[148:151],a[68:71],a[80:83],v[148:151]\n" \
"ds_read_b64_tr_b16 a[10:11],v10 offset:16896\nM v[180:183],a[68:71],a[84:87],v[180:183]\nds_read_b64_tr_b16 a[12:13],v11 offset:16896\nM v[152:155],a[72:75],a[80:83],v[152:155]\ns_add_u32 m0,s50,12288\nds_read_b64_tr_b16 a[14:15],v12 offset:16896\nM v[184:187],a[72:75],a[84:87],v[184:187]\nbuffer_load_dwordx4 v16,s[28:31],s46 offen lds\n" \
"M v[156:159],a[76:79],a[80:83],v[156:159]\nds_read_b64_tr_b16 a[16:17],v9 offset:17408\nM v[188:191],a[76:79],a[84:87],v[188:191]\nds_read_b64_tr_b16 a[18:19],v10 offset:17408\nM v[96:99],a[48:51],a[88:91],v[96:99]\ns_mov_b32 s46,1664\ns_add_u32 m0,s50,16384\nds_read_b64_tr_b16 a[20:21],v11 offset:17408\n" \
"M v[64:67],a[48:51],a[92:95],v[64:67]\nbuffer_load_dwordx4 v17,s[28:31],s46 offen lds\nM v[100:103],a[52:55],a[88:91],v[100:103]\nds_read_b64_tr_b16 a[22:23],v12 offset:17408\nM v[68:71],a[52:55],a[92:95],v[68:71]\nds_read_b64_tr_b16 a[24:25],v9 offset:17920\nM v[104:107],a[56:59],a[88:91],v[104:107]\ns_add_u32 m0,s50,20480\n" \
"ds_read_b64_tr_b16 a[26:27],v10 offset:17920\nM v[72:75],a[56:59],a[92:95],v[72:75]\nbuffer_load_dwordx4 v18,s[28:31],s46 offen lds\nM v[108:111],a[60:63],a[88:91],v[108:111]\nds_read_b64_tr_b16 a[28:29],v11 offset:17920\nM v[76:79],a[60:63],a[92:95],v[76:79]\nds_read_b64_tr_b16 a[30:31],v12 offset:17920\nM v[112:115],a[64:67],a[88:91],v[112:115]\n" \
"s_add_u32 m0,s50,24576\nds_read_b128 a[40:43],v5 offset:40960\nM v[80:83],a[64:67],a[92:95],v[80:83]\nbuffer_load_dwordx4 v19,s[28:31],s46 offen lds\nM v[116:119],a[68:71],a[88:91],v[116:119]\nds_read_b128 a[44:47],v5 offset:45056\nM v[84:87],a[68:71],a[92:95],v[84:87]\nM v[120:123],a[72:75],a[88:91],v[120:123]\n" \
"s_add_u32 m0,s50,28672\nM v[88:91],a[72:75],a[92:95],v[88:91]\nbuffer_load_dwordx4 v20,s[28:31],s46 offen lds\nM v[124:127],a[76:79],a[88:91],v[124:127]\ns_waitcnt lgkmcnt(0)\nM v[92:95],a[76:79],a[92:95],v[92:95]\nM v[128:131],a[0:3],a[32:35],v[128:131]\ns_mov_b32 s47,7340032\n" \
"s_add_u32 m0,s51,0\nds_read_b64_tr_b16 a[48:49],v9 offset:24576\nM v[160:163],a[0:3],a[36:39],v[160:163]\nbuffer_load_dwordx4 v21,s[32:35],s47 offen offset:0 lds\nM v[132:135],a[4:7],a[32:35],v[132:135]\nds_read_b64_tr_b16 a[50:51],v10 offset:24576\nM v[164:167],a[4:7],a[36:39],v[164:167]\nds_read_b128 a[80:83],v6 offset:32768\n" \
"M v[136:139],a[8:11],a[32:35],v[136:139]\ns_add_u32 m0,s51,896\nds_read_b128 a[84:87],v6 offset:36864\nM v[168:171],a[8:11],a[36:39],v[168:171]\nbuffer_load_dwordx4 v21,s[32:35],s47 offen offset:128 lds\nM v[140:143],a[12:15],a[32:35],v[140:143]\nds_read_b64_tr_b16 a[52:53],v11 offset:24576\nM v[172:175],a[12:15],a[36:39],v[172:175]\n" \
"ds_read_b64_tr_b16 a[54:55],v12 offset:24576\nM v[144:147],a[16:19],a[32:35],v[144:147]\ns_mov_b32 s47,7602176\ns_add_u32 m0,s51,8192\nds_read_b64_tr_b16 a[56:57],v9 offset:25088\nM v[176:179],a[16:19],a[36:39],v[176:179]\nbuffer_load_dwordx4 v21,s[32:35],s47 offen offset:0 lds\nM v[148:151],a[20:23],a[32:35],v[148:151]\n" \
"ds_read_b64_tr_b16 a[58:59],v10 offset:25088\nM v[180:183],a[20:23],a[36:39],v[180:183]\nds_read_b64_tr_b16 a[60:61],v11 offset:25088\nM v[152:155],a[24:27],a[32:35],v[152:155]\ns_add_u32 m0,s51,9088\nds_read_b64_tr_b16 a[62:63],v12 offset:25088\nM v[184:187],a[24:27],a[36:39],v[184:187]\nbuffer_load_dwordx4 v21,s[32:35],s47 offen offset:128 lds\n" \
"M v[156:159],a[28:31],a[32:35],v[156:159]\nds_read_b64_tr_b16 a[64:65],v9 offset:25600\nM v[188:191],a[28:31],a[36:39],v[188:191]\nds_read_b64_tr_b16 a[66:67],v10 offset:25600\nM v[96:99],a[0:3],a[40:43],v[96:99]\nds_read_b64_tr_b16 a[68:69],v11 offset:25600\nM v[64:67],a[0:3],a[44:47],v[64:67]\nM v[100:103],a[4:7],a[40:43],v[100:103]\n" \
"ds_read_b64_tr_b16 a[70:71],v12 offset:25600\nM v[68:71],a[4:7],a[44:47],v[68:71]\nds_read_b64_tr_b16 a[72:73],v9 offset:26112\nM v[104:107],a[8:11],a[40:43],v[104:107]\nds_read_b64_tr_b16 a[74:75],v10 offset:26112\nM v[72:75],a[8:11],a[44:47],v[72:75]\nM v[108:111],a[12:15],a[40:43],v[108:111]\nds_read_b64_tr_b16 a[76:77],v11 offset:26112\n" \
"M v[76:79],a[12:15],a[44:47],v[76:79]\nds_read_b64_tr_b16 a[78:79],v12 offset:26112\nM v[112:115],a[16:19],a[40:43],v[112:115]\nds_read_b128 a[88:91],v6 offset:40960\nM v[80:83],a[16:19],a[44:47],v[80:83]\nbuffer_store_dwordx4 v[252:255],v4,s[36:39],s96 offen offset:3072 sc1\nM v[116:119],a[20:23],a[40:43],v[116:119]\nds_read_b128 a[92:95],v6 offset:45056\n" \
"M v[84:87],a[20:23],a[44:47],v[84:87]\nM v[120:123],a[24:27],a[40:43],v[120:123]\ns_waitcnt vmcnt(19) lgkmcnt(0)\nM v[88:91],a[24:27],a[44:47],v[88:91]\ns_barrier\nM v[124:127],a[28:31],a[40:43],v[124:127]\nM v[92:95],a[28:31],a[44:47],v[92:95]\nM v[128:131],a[48:51],a[80:83],v[128:131]\n" \
"s_mov_b32 s46,1792\ns_add_u32 m0,s50,32768\nds_read_b64_tr_b16 a[0:1],v9 offset:32768\nM v[160:163],a[48:51],a[84:87],v[160:163]\nbuffer_load_dwordx4 v13,s[28:31],s46 offen lds\nM v[132:135],a[52:55],a[80:83],v[132:135]\nds_read_b64_tr_b16 a[2:3],v10 offset:32768\nM v[164:167],a[52:55],a[84:87],v[164:167]\n" \
"ds_read_b128 a[32:35],v7 offset:0\nM v[136:139],a[56:59],a[80:83],v[136:139]\ns_add_u32 m0,s50,36864\nds_read_b128 a[36:39],v7 offset:4096\nM v[168:171],a[56:59],a[84:87],v[168:171]\nbuffer_load_dwordx4 v14,s[28:31],s46 offen lds\nM v[140:143],a[60:63],a[80:83],v[140:143]\nds_read_b64_tr_b16 a[4:5],v11 offset:32768\n" \
"M v[172:175],a[60:63],a[84:87],v[172:175]\nds_read_b64_tr_b16 a[6:7],v12 offset:32768\nM v[144:147],a[64:67],a[80:83],v[144:147]\ns_add_u32 m0,s50,40960\nds_read_b64_tr_b16 a[8:9],v9 offset:33280\nM v[176:179],a[64:67],a[84:87],v[176:179]\nbuffer_load_dwordx4 v15,s[28:31],s46 offen lds\nM v[148:151],a[68:71],a[80:83],v[148:151]\n" \
"ds_read_b64_tr_b16 a[10:11],v10 offset:33280\nM v[180:183],a[68:71],a[84:87],v[180:183]\nds_read_b64_tr_b16 a[12:13],v11 offset:33280\nM v[152:155],a[72:75],a[80:83],v[152:155]\ns_add_u32 m0,s50,45056\nds_read_b64_tr_b16 a[14:15],v12 offset:33280\nM v[184:187],a[72:75],a[84:87],v[184:187]\nbuffer_load_dwordx4 v16,s[28:31],s46 offen lds\n" \
"M v[156:159],a[76:79],a[80:83],v[156:159]\nds_read_b64_tr_b16 a[16:17],v9 offset:33792\nM v[188:191],a[76:79],a[84:87],v[188:191]\nds_read_b64_tr_b16 a[18:19],v10 offset:33792\nM v[96:99],a[48:51],a[88:91],v[96:99]\ns_mov_b32 s46,1792\ns_add_u32 m0,s50,49152\nds_read_b64_tr_b16 a[20:21],v11 offset:33792\n" \
"M v[64:67],a[48:51],a[92:95],v[64:67]\nbuffer_load_dwordx4 v17,s[28:31],s46 offen lds\nM v[100:103],a[52:55],a[88:91],v[100:103]\nds_read_b64_tr_b16 a[22:23],v12 offset:33792\nM v[68:71],a[52:55],a[92:95],v[68:71]\nds_read_b64_tr_b16 a[24:25],v9 offset:34304\nM v[104:107],a[56:59],a[88:91],v[104:107]\ns_add_u32 m0,s50,53248\n" \
"ds_read_b64_tr_b16 a[26:27],v10 offset:34304\nM v[72:75],a[56:59],a[92:95],v[72:75]\nbuffer_load_dwordx4 v18,s[28:31],s46 offen lds\nM v[108:111],a[60:63],a[88:91],v[108:111]\nds_read_b64_tr_b16 a[28:29],v11 offset:34304\nM v[76:79],a[60:63],a[92:95],v[76:79]\nds_read_b64_tr_b16 a[30:31],v12 offset:34304\nM v[112:115],a[64:67],a[88:91],v[112:115]\n" \
"s_add_u32 m0,s50,57344\nds_read_b128 a[40:43],v7 offset:8192\nM v[80:83],a[64:67],a[92:95],v[80:83]\nbuffer_load_dwordx4 v19,s[28:31],s46 offen lds\nM v[116:119],a[68:71],a[88:91],v[116:119]\nds_read_b128 a[44:47],v7 offset:12288\nM v[84:87],a[68:71],a[92:95],v[84:87]\nM v[120:123],a[72:75],a[88:91],v[120:123]\n" \
"s_add_u32 m0,s50,61440\nM v[88:91],a[72:75],a[92:95],v[88:91]\nbuffer_load_dwordx4 v20,s[28:31],s46 offen lds\nM v[124:127],a[76:79],a[88:91],v[124:127]\ns_waitcnt lgkmcnt(0)\nM v[92:95],a[76:79],a[92:95],v[92:95]\nM v[128:131],a[0:3],a[32:35],v[128:131]\ns_mov_b32 s47,7864320\n" \
"s_add_u32 m0,s51,16384\nds_read_b64_tr_b16 a[48:49],v9 offset:40960\nM v[160:163],a[0:3],a[36:39],v[160:163]\nbuffer_load_dwordx4 v21,s[32:35],s47 offen offset:0 lds\nM v[132:135],a[4:7],a[32:35],v[132:135]\nds_read_b64_tr_b16 a[50:51],v10 offset:40960\nM v[164:167],a[4:7],a[36:39],v[164:167]\nds_read_b128 a[80:83],v8 offset:0\n" \
"M v[136:139],a[8:11],a[32:35],v[136:139]\ns_add_u32 m0,s51,17280\nds_read_b128 a[84:87],v8 offset:4096\nM v[168:171],a[8:11],a[36:39],v[168:171]\nbuffer_load_dwordx4 v21,s[32:35],s47 offen offset:128 lds\nM v[140:143],a[12:15],a[32:35],v[140:143]\nLG13_%=:\ns_cbranch_execz LG17_%=\n" \
"ds_read_b64_tr_b16 a[52:53],v11 offset:40960\nM v[172:175],a[12:15],a[36:39],v[172:175]\nds_read_b64_tr_b16 a[54:55],v12 offset:40960\nM v[144:147],a[16:19],a[32:35],v[144:147]\ns_mov_b32 s47,8126464\ns_add_u32 m0,s51,24576\nds_read_b64_tr_b16 a[56:57],v9 offset:41472\nM v[176:179],a[16:19],a[36:39],v[176:179]\n" \
"buffer_load_dwordx4 v21,s[32:35],s47 offen offset:0 lds\nM v[148:151],a[20:23],a[32:35],v[148:151]\nds_read_b64_tr_b16 a[58:59],v10 offset:41472\nM v[180:183],a[20:23],a[36:39],v[180:183]\nds_read_b64_tr_b16 a[60:61],v11 offset:41472\nM v[152:155],a[24:27],a[32:35],v[152:155]\ns_add_u32 m0,s51,25472\nds_read_b64_tr_b16 a[62:63],v12 offset:41472\n" \
"M v[184:187],a[24:27],a[36:39],v[184:187]\nbuffer_load_dwordx4 v21,s[32:35],s47 offen offset:128 lds\nM v[156:159],a[28:31],a[32:35],v[156:159]\nds_read_b64_tr_b16 a[64:65],v9 offset:41984\nM v[188:191],a[28:31],a[36:39],v[188:191]\nds_read_b64_tr_b16 a[66:67],v10 offset:41984\nM v[96:99],a[0:3],a[40:43],v[96:99]\nds_read_b64_tr_b16 a[68:69],v11 offset:41984\n" \
"M v[64:67],a[0:3],a[44:47],v[64:67]\nM v[100:103],a[4:7],a[40:43],v[100:103]\nds_read_b64_tr_b16 a[70:71],v12 offset:41984\nM v[68:71],a[4:7],a[44:47],v[68:71]\nds_read_b64_tr_b16 a[72:73],v9 offset:42496\nM v[104:107],a[8:11],a[40:43],v[104:107]\nds_read_b64_tr_b16 a[74:75],v10 offset:42496\nM v[72:75],a[8:11],a[44:47],v[72:75]\n" \
"M v[108:111],a[12:15],a[40:43],v[108:111]\nds_read_b64_tr_b16 a[76:77],v11 offset:42496\nM v[76:79],a[12:15],a[44:47],v[76:79]\nds_read_b64_tr_b16 a[78:79],v12 offset:42496\nM v[112:115],a[16:19],a[40:43],v[112:115]\nds_read_b128 a[88:91],v8 offset:8192\nM v[80:83],a[16:19],a[44:47],v[80:83]\nM v[116:119],a[20:23],a[40:43],v[116:119]\n" \
"ds_read_b128 a[92:95],v8 offset:12288\nM v[84:87],a[20:23],a[44:47],v[84:87]\nM v[120:123],a[24:27],a[40:43],v[120:123]\ns_waitcnt vmcnt(17) lgkmcnt(0)\nM v[88:91],a[24:27],a[44:47],v[88:91]\ns_barrier\nM v[124:127],a[28:31],a[40:43],v[124:127]\nM v[92:95],a[28:31],a[44:47],v[92:95]\n" \
"M v[128:131],a[48:51],a[80:83],v[128:131]\ns_mov_b32 s46,1920\ns_add_u32 m0,s50,65536\nds_read_b64_tr_b16 a[0:1],v9 offset:49152\nM v[160:163],a[48:51],a[84:87],v[160:163]\nbuffer_load_dwordx4 v13,s[28:31],s46 offen lds\nM v[132:135],a[52:55],a[80:83],v[132:135]\nds_read_b64_tr_b16 a[2:3],v10 offset:49152\n" \
"M v[164:167],a[52:55],a[84:87],v[164:167]\nds_read_b128 a[32:35],v5 offset:0\nM v[136:139],a[56:59],a[80:83],v[136:139]\ns_add_u32 m0,s50,69632\nds_read_b128 a[36:39],v5 offset:4096\nM v[168:171],a[56:59],a[84:87],v[168:171]\nbuffer_load_dwordx4 v14,s[28:31],s46 offen lds\nM v[140:143],a[60:63],a[80:83],v[140:143]\n" \
"ds_read_b64_tr_b16 a[4:5],v11 offset:49152\nM v[172:175],a[60:63],a[84:87],v[172:175]\nds_read_b64_tr_b16 a[6:7],v12 offset:49152\nM v[144:147],a[64:67],a[80:83],v[144:147]\ns_add_u32 m0,s50,73728\nds_read_b64_tr_b16 a[8:9],v9 offset:49664\nM v[176:179],a[64:67],a[84:87],v[176:179]\nbuffer_load_dwordx4 v15,s[28:31],s46 offen lds\n" \
"M v[148:151],a[68:71],a[80:83],v[148:151]\nds_read_b64_tr_b16 a[10:11],v10 offset:49664\nM v[180:183],a[68:71],a[84:87],v[180:183]\nds_read_b64_tr_b16 a[12:13],v11 offset:49664\nM v[152:155],a[72:75],a[80:83],v[152:155]\ns_add_u32 m0,s50,77824\nds_read_b64_tr_b16 a[14:15],v12 offset:49664\nM v[184:187],a[72:75],a[84:87],v[184:187]\n" \
"buffer_load_dwordx4 v16,s[28:31],s46 offen lds\nM v[156:159],a[76:79],a[80:83],v[156:159]\nds_read_b64_tr_b16 a[16:17],v9 offset:50176\nM v[188:191],a[76:79],a[84:87],v[188:191]\nds_read_b64_tr_b16 a[18:19],v10 offset:50176\nM v[96:99],a[48:51],a[88:91],v[96:99]\ns_mov_b32 s46,1920\ns_add_u32 m0,s50,81920\n" \
"ds_read_b64_tr_b16 a[20:21],v11 offset:50176\nM v[64:67],a[48:51],a[92:95],v[64:67]\nbuffer_load_dwordx4 v17,s[28:31],s46 offen lds\nM v[100:103],a[52:55],a[88:91],v[100:103]\nds_read_b64_tr_b16 a[22:23],v12 offset:50176\nM v[68:71],a[52:55],a[92:95],v[68:71]\nds_read_b64_tr_b16 a[24:25],v9 offset:50688\nM v[104:107],a[56:59],a[88:91],v[104:107]\n" \
"s_add_u32 m0,s50,86016\nds_read_b64_tr_b16 a[26:27],v10 offset:50688\nM v[72:75],a[56:59],a[92:95],v[72:75]\nbuffer_load_dwordx4 v18,s[28:31],s46 offen lds\nM v[108:111],a[60:63],a[88:91],v[108:111]\nds_read_b64_tr_b16 a[28:29],v11 offset:50688\nM v[76:79],a[60:63],a[92:95],v[76:79]\nds_read_b64_tr_b16 a[30:31],v12 offset:50688\n" \
"M v[112:115],a[64:67],a[88:91],v[112:115]\ns_add_u32 m0,s50,90112\nds_read_b128 a[40:43],v5 offset:8192\nM v[80:83],a[64:67],a[92:95],v[80:83]\nbuffer_load_dwordx4 v19,s[28:31],s46 offen lds\nM v[116:119],a[68:71],a[88:91],v[116:119]\nds_read_b128 a[44:47],v5 offset:12288\nM v[84:87],a[68:71],a[92:95],v[84:87]\n" \
"M v[120:123],a[72:75],a[88:91],v[120:123]\ns_add_u32 m0,s50,94208\nM v[88:91],a[72:75],a[92:95],v[88:91]\nbuffer_load_dwordx4 v20,s[28:31],s46 offen lds\nM v[124:127],a[76:79],a[88:91],v[124:127]\ns_waitcnt lgkmcnt(0)\nM v[92:95],a[76:79],a[92:95],v[92:95]\nM v[128:131],a[0:3],a[32:35],v[128:131]\n" \
"s_add_u32 m0,s22,98304\nds_read_b64_tr_b16 a[48:49],v9 offset:57344\nds_read_b64_tr_b16 a[50:51],v10 offset:57344\nM v[160:163],a[0:3],a[36:39],v[160:163]\ns_add_u32 s61,s44,16384\nds_read_b128 a[80:83],v6 offset:0\nv_add_u32 v13,s54,v4\nM v[132:135],a[4:7],a[32:35],v[132:135]\n" \
"s_and_b32 s63,%2,1\nds_read_b128 a[84:87],v6 offset:4096\nv_mov_b32 v58,s10\nM v[164:167],a[4:7],a[36:39],v[164:167]\ns_lshl_b32 s63,s63,14\nds_read_b64_tr_b16 a[52:53],v11 offset:57344\nv_mov_b32 v59,s11\nM v[136:139],a[8:11],a[32:35],v[136:139]\n" \
"s_add_u32 s63,s63,0x20000\nds_read_b64_tr_b16 a[54:55],v12 offset:57344\nds_read_b64_tr_b16 a[56:57],v9 offset:57856\nM v[168:171],a[8:11],a[36:39],v[168:171]\ns_mov_b32 s76,0xf149f2ca\nds_read_b64_tr_b16 a[58:59],v10 offset:57856\nv_add_u32 v21,s63,v4\nM v[140:143],a[12:15],a[32:35],v[140:143]\n" \
"s_add_u32 s63,s22,16384\nds_read_b64_tr_b16 a[60:61],v11 offset:57856\nv_add_u32 v20,12288,v21\nM v[172:175],a[12:15],a[36:39],v[172:175]\ns_add_u32 s45,s45,s49\nds_read_b64_tr_b16 a[62:63],v12 offset:57856\nds_read_b64_tr_b16 a[64:65],v9 offset:58368\nM v[144:147],a[16:19],a[32:35],v[144:147]\n" \
"s_add_u32 s48,s48,s49\nds_read_b64_tr_b16 a[66:67],v10 offset:58368\nv_add_u32 v62,s63,v4\nM v[176:179],a[16:19],a[36:39],v[176:179]\ns_cmp_lt_u32 %2,2\ns_cselect_b64 s[62:63],-1,0\nds_read_b64_tr_b16 a[68:69],v11 offset:58368\nv_cndmask_b32 v20,v20,v62,s[62:63]\n" \
"M v[148:151],a[20:23],a[32:35],v[148:151]\ns_mov_b32 s62,0x11111111\nds_read_b64_tr_b16 a[70:71],v12 offset:58368\nds_read_b64_tr_b16 a[72:73],v9 offset:58880\nM v[180:183],a[20:23],a[36:39],v[180:183]\ns_mov_b32 s63,s62\nds_read_b64_tr_b16 a[74:75],v10 offset:58880\nv_mov_b32 v63,0x3f803f80\n" \
"M v[152:155],a[24:27],a[32:35],v[152:155]\ns_mov_b32 s53,s22\nds_read_b64_tr_b16 a[76:77],v11 offset:58880\nv_cndmask_b32 v62,0,v63,s[62:63]\nM v[184:187],a[24:27],a[36:39],v[184:187]\ns_mov_b32 s62,0x22222222\nds_read_b64_tr_b16 a[78:79],v12 offset:58880\nds_read_b128 a[88:91],v6 offset:8192\n" \
"M v[156:159],a[28:31],a[32:35],v[156:159]\ns_mov_b32 s63,s62\nds_read_b128 a[92:95],v6 offset:12288\nv_add_u32 v14,0x10000,v13\nM v[188:191],a[28:31],a[36:39],v[188:191]\ns_mov_b32 s74,0\nM v[96:99],a[0:3],a[40:43],v[96:99]\ns_mov_b32 s46,0\n" \
"M v[64:67],a[0:3],a[44:47],v[64:67]\nM v[100:103],a[4:7],a[40:43],v[100:103]\nM v[68:71],a[4:7],a[44:47],v[68:71]\nM v[104:107],a[8:11],a[40:43],v[104:107]\nM v[72:75],a[8:11],a[44:47],v[72:75]\nM v[108:111],a[12:15],a[40:43],v[108:111]\ns_waitcnt vmcnt(12) lgkmcnt(0)\nM v[76:79],a[12:15],a[44:47],v[76:79]\n" \
"s_barrier\nds_read_b64_tr_b16 a[0:1],v9 offset:0\nM v[112:115],a[16:19],a[40:43],v[112:115]\nbuffer_store_dwordx2 v[58:59],v56,s[36:39],s24 offen sc1\nM v[80:83],a[16:19],a[44:47],v[80:83]\nds_write_b128 v1,a[192:195] offset:0\nM v[116:119],a[20:23],a[40:43],v[116:119]\nM v[84:87],a[20:23],a[44:47],v[84:87]\n" \
"M v[120:123],a[24:27],a[40:43],v[120:123]\nM v[88:91],a[24:27],a[44:47],v[88:91]\nds_write_b128 v1,a[196:199] offset:1024\nM v[124:127],a[28:31],a[40:43],v[124:127]\nM v[92:95],a[28:31],a[44:47],v[92:95]\nM v[128:131],a[48:51],a[80:83],v[128:131]\nM v[160:163],a[48:51],a[84:87],v[160:163]\nds_read_b64_tr_b16 a[2:3],v10 offset:0\n" \
"M v[132:135],a[52:55],a[80:83],v[132:135]\nds_write_b128 v1,a[200:203] offset:2048\nM v[164:167],a[52:55],a[84:87],v[164:167]\nM v[136:139],a[56:59],a[80:83],v[136:139]\nM v[168:171],a[56:59],a[84:87],v[168:171]\nM v[140:143],a[60:63],a[80:83],v[140:143]\nds_read_b128 a[32:35],v5 offset:32768\nM v[172:175],a[60:63],a[84:87],v[172:175]\n" \
"ds_read_b128 a[36:39],v5 offset:36864\nM v[144:147],a[64:67],a[80:83],v[144:147]\nds_read_b64_tr_b16 a[4:5],v11 offset:0\nM v[176:179],a[64:67],a[84:87],v[176:179]\nds_write_b128 v1,a[204:207] offset:3072\nM v[148:151],a[68:71],a[80:83],v[148:151]\nM v[180:183],a[68:71],a[84:87],v[180:183]\nM v[152:155],a[72:75],a[80:83],v[152:155]\n" \
"M v[184:187],a[72:75],a[84:87],v[184:187]\nds_read_b64_tr_b16 a[6:7],v12 offset:0\nM v[156:159],a[76:79],a[80:83],v[156:159]\nds_write_b128 v1,a[208:211] offset:4096\nM v[188:191],a[76:79],a[84:87],v[188:191]\nM v[96:99],a[48:51],a[88:91],v[96:99]\nM v[64:67],a[48:51],a[92:95],v[64:67]\nM v[100:103],a[52:55],a[88:91],v[100:103]\n" \
"ds_write_b128 v1,a[212:215] offset:5120\nM v[68:71],a[52:55],a[92:95],v[68:71]\nM v[104:107],a[56:59],a[88:91],v[104:107]\nM v[72:75],a[56:59],a[92:95],v[72:75]\nM v[108:111],a[60:63],a[88:91],v[108:111]\nds_read_b64_tr_b16 a[8:9],v9 offset:512\nM v[76:79],a[60:63],a[92:95],v[76:79]\nds_write_b128 v1,a[216:219] offset:6144\n" \
"M v[112:115],a[64:67],a[88:91],v[112:115]\nM v[80:83],a[64:67],a[92:95],v[80:83]\nds_read_b64_tr_b16 a[10:11],v10 offset:512\nM v[116:119],a[68:71],a[88:91],v[116:119]\nM v[84:87],a[68:71],a[92:95],v[84:87]\nds_read_b64_tr_b16 a[12:13],v11 offset:512\nds_read_b64_tr_b16 a[14:15],v12 offset:512\nM v[120:123],a[72:75],a[88:91],v[120:123]\n" \
"ds_read_b64_tr_b16 a[16:17],v9 offset:1024\nM v[88:91],a[72:75],a[92:95],v[88:91]\nds_read_b64_tr_b16 a[18:19],v10 offset:1024\nM v[124:127],a[76:79],a[88:91],v[124:127]\nds_write_b128 v1,a[220:223] offset:7168\nM v[92:95],a[76:79],a[92:95],v[92:95]\ns_waitcnt lgkmcnt(5)\nds_read_b64_tr_b16 a[20:21],v11 offset:1024\n" \
"ds_read_b64_tr_b16 a[22:23],v12 offset:1024\nM v[128:131],a[0:3],a[32:35],v[128:131]\nM v[160:163],a[0:3],a[36:39],v[160:163]\nds_read_b64_tr_b16 a[24:25],v9 offset:1536\nds_read_b64_tr_b16 a[26:27],v10 offset:1536\nM v[132:135],a[4:7],a[32:35],v[132:135]\nds_read_b64_tr_b16 a[28:29],v11 offset:1536\nM v[164:167],a[4:7],a[36:39],v[164:167]\n" \
"ds_read_b64_tr_b16 a[30:31],v12 offset:1536\nM v[136:139],a[8:11],a[32:35],v[136:139]\nds_write_b128 v1,a[224:227] offset:16384\nM v[168:171],a[8:11],a[36:39],v[168:171]\ns_waitcnt lgkmcnt(5)\nds_read_b128 a[40:43],v5 offset:40960\nv_accvgpr_write_b32 a224,v62\nM v[140:143],a[12:15],a[32:35],v[140:143]\n" \
"ds_read_b128 a[44:47],v5 offset:45056\nv_accvgpr_write_b32 a225,v62\nM v[172:175],a[12:15],a[36:39],v[172:175]\nv_accvgpr_write_b32 a226,v62\nv_accvgpr_write_b32 a227,v62\nM v[144:147],a[16:19],a[32:35],v[144:147]\nds_read_b64_tr_b16 a[48:49],v9 offset:8192\nv_cndmask_b32 v62,0,v63,s[62:63]\n" \
"M v[176:179],a[16:19],a[36:39],v[176:179]\ns_mov_b32 s62,0x44444444\nds_write_b128 v1,a[228:231] offset:17408\nM v[148:151],a[20:23],a[32:35],v[148:151]\ns_mov_b32 s63,s62\nv_accvgpr_write_b32 a228,v62\nv_accvgpr_write_b32 a229,v62\nM v[180:183],a[20:23],a[36:39],v[180:183]\n" \
"s_waitcnt lgkmcnt(2)\nv_accvgpr_write_b32 a230,v62\nv_accvgpr_write_b32 a231,v62\nM v[152:155],a[24:27],a[32:35],v[152:155]\nv_cndmask_b32 v62,0,v63,s[62:63]\nM v[184:187],a[24:27],a[36:39],v[184:187]\ns_mov_b32 s62,0x88888888\nds_read_b128 a[80:83],v6 offset:32768\n" \
"M v[156:159],a[28:31],a[32:35],v[156:159]\ns_mov_b32 s63,s62\nds_read_b128 a[84:87],v6 offset:36864\nM v[188:191],a[28:31],a[36:39],v[188:191]\nds_read_b64_tr_b16 a[50:51],v10 offset:8192\nM v[96:99],a[0:3],a[40:43],v[96:99]\nds_read_b64_tr_b16 a[52:53],v11 offset:8192\nds_read_b64_tr_b16 a[54:55],v12 offset:8192\n" \
"ds_read_b64_tr_b16 a[56:57],v9 offset:8704\nds_read_b64_tr_b16 a[58:59],v10 offset:8704\nds_read_b64_tr_b16 a[60:61],v11 offset:8704\nds_read_b64_tr_b16 a[62:63],v12 offset:8704\nds_read_b64_tr_b16 a[64:65],v9 offset:9216\nds_read_b64_tr_b16 a[66:67],v10 offset:9216\nds_read_b64_tr_b16 a[68:69],v11 offset:9216\nds_read_b64_tr_b16 a[70:71],v12 offset:9216\n" \
"ds_read_b64_tr_b16 a[72:73],v9 offset:9728\nds_read_b64_tr_b16 a[74:75],v10 offset:9728\nds_read_b64_tr_b16 a[76:77],v11 offset:9728\nds_read_b64_tr_b16 a[78:79],v12 offset:9728\nds_read_b128 a[88:91],v6 offset:40960\nds_read_b128 a[92:95],v6 offset:45056\nM v[64:67],a[0:3],a[44:47],v[64:67]\nM v[100:103],a[4:7],a[40:43],v[100:103]\n" \
"M v[68:71],a[4:7],a[44:47],v[68:71]\nds_write_b128 v1,a[232:235] offset:18432\nM v[104:107],a[8:11],a[40:43],v[104:107]\nv_accvgpr_write_b32 a232,v62\nv_accvgpr_write_b32 a233,v62\nM v[72:75],a[8:11],a[44:47],v[72:75]\nv_accvgpr_write_b32 a234,v62\nv_accvgpr_write_b32 a235,v62\n" \
"M v[108:111],a[12:15],a[40:43],v[108:111]\ns_waitcnt vmcnt(1) lgkmcnt(1)\nv_cndmask_b32 v62,0,v63,s[62:63]\nM v[76:79],a[12:15],a[44:47],v[76:79]\ns_lshr_b32 s62,%1,3\ns_barrier\nM v[112:115],a[16:19],a[40:43],v[112:115]\nbuffer_load_dwordx4 v22,s[40:43],s61 offen offset:0 sc1 lds\n" \
"M v[80:83],a[16:19],a[44:47],v[80:83]\ns_lshl_b32 s62,s62,2\nds_write_b128 v1,a[236:239] offset:19456\nM v[116:119],a[20:23],a[40:43],v[116:119]\ns_add_u32 s62,s62,%2\nv_accvgpr_write_b32 a236,v62\nv_accvgpr_write_b32 a237,v62\nM v[84:87],a[20:23],a[44:47],v[84:87]\n" \
"s_lshr_b32 s63,%1,5\nv_accvgpr_write_b32 a238,v62\nv_accvgpr_write_b32 a239,v62\nM v[120:123],a[24:27],a[40:43],v[120:123]\nbuffer_load_dwordx4 v22,s[40:43],s61 offen offset:1024 sc1 lds\nM v[88:91],a[24:27],a[44:47],v[88:91]\ns_lshl_b32 s62,s62,14\nds_write_b128 v1,a[240:243] offset:20480\n" \
"M v[124:127],a[28:31],a[40:43],v[124:127]\ns_lshl_b32 s63,s63,8\nM v[92:95],a[28:31],a[44:47],v[92:95]\ns_sub_u32 s62,s62,s63\nM v[128:131],a[48:51],a[80:83],v[128:131]\nbuffer_load_dwordx4 v22,s[40:43],s61 offen offset:2048 sc1 lds\nM v[160:163],a[48:51],a[84:87],v[160:163]\nds_read_b64_tr_b16 a[0:1],v9 offset:16384\n" \
"M v[132:135],a[52:55],a[80:83],v[132:135]\nds_read_b64_tr_b16 a[2:3],v10 offset:16384\nds_read_b64_tr_b16 a[4:5],v11 offset:16384\nM v[164:167],a[52:55],a[84:87],v[164:167]\nds_read_b64_tr_b16 a[6:7],v12 offset:16384\nM v[136:139],a[56:59],a[80:83],v[136:139]\nbuffer_load_dwordx4 v22,s[40:43],s61 offen offset:3072 sc1 lds\nM v[168:171],a[56:59],a[84:87],v[168:171]\n" \
"s_add_u32 m0,s22,114688\nds_write_b128 v1,a[244:247] offset:21504\nM v[140:143],a[60:63],a[80:83],v[140:143]\ns_add_u32 s61,s44,0\nM v[172:175],a[60:63],a[84:87],v[172:175]\nM v[144:147],a[64:67],a[80:83],v[144:147]\nbuffer_load_dwordx4 v22,s[40:43],s61 offen offset:0 sc1 lds\nM v[176:179],a[64:67],a[84:87],v[176:179]\n" \
"ds_read_b64_tr_b16 a[8:9],v9 offset:16896\nds_read_b64_tr_b16 a[10:11],v10 offset:16896\nM v[148:151],a[68:71],a[80:83],v[148:151]\nds_read_b64_tr_b16 a[12:13],v11 offset:16896\nM v[180:183],a[68:71],a[84:87],v[180:183]\nds_read_b64_tr_b16 a[14:15],v12 offset:16896\nM v[152:155],a[72:75],a[80:83],v[152:155]\nbuffer_load_dwordx4 v22,s[40:43],s61 offen offset:1024 sc1 lds\n" \
"M v[184:187],a[72:75],a[84:87],v[184:187]\nds_write_b128 v1,a[248:251] offset:22528\nM v[156:159],a[76:79],a[80:83],v[156:159]\nM v[188:191],a[76:79],a[84:87],v[188:191]\nM v[96:99],a[48:51],a[88:91],v[96:99]\nbuffer_load_dwordx4 v22,s[40:43],s61 offen offset:2048 sc1 lds\nM v[64:67],a[48:51],a[92:95],v[64:67]\nds_read_b128 a[44:47],v7 offset:12288\n" \
"M v[100:103],a[52:55],a[88:91],v[100:103]\nds_read_b64_tr_b16 a[16:17],v9 offset:17408\nds_read_b64_tr_b16 a[18:19],v10 offset:17408\nM v[68:71],a[52:55],a[92:95],v[68:71]\nds_read_b64_tr_b16 a[20:21],v11 offset:17408\nM v[104:107],a[56:59],a[88:91],v[104:107]\nbuffer_load_dwordx4 v22,s[40:43],s61 offen offset:3072 sc1 lds\nM v[72:75],a[56:59],a[92:95],v[72:75]\n" \
"s_add_u32 m0,s22,0\nds_write_b128 v1,a[252:255] offset:23552\nM v[108:111],a[60:63],a[88:91],v[108:111]\ns_add_u32 s61,s44,24576\nM v[76:79],a[60:63],a[92:95],v[76:79]\nM v[112:115],a[64:67],a[88:91],v[112:115]\nbuffer_load_dwordx4 v22,s[40:43],s61 offen offset:0 sc1 lds\nM v[80:83],a[64:67],a[92:95],v[80:83]\n" \
"ds_read_b64_tr_b16 a[22:23],v12 offset:17408\nds_read_b64_tr_b16 a[24:25],v9 offset:17920\nM v[116:119],a[68:71],a[88:91],v[116:119]\nds_read_b64_tr_b16 a[26:27],v10 offset:17920\nM v[84:87],a[68:71],a[92:95],v[84:87]\nds_read_b64_tr_b16 a[28:29],v11 offset:17920\nM v[120:123],a[72:75],a[88:91],v[120:123]\nds_read_b64_tr_b16 a[30:31],v12 offset:17920\n" \
"ds_read_b128 a[40:43],v7 offset:8192\nM v[88:91],a[72:75],a[92:95],v[88:91]\nbuffer_load_dwordx4 v22,s[40:43],s61 offen offset:1024 sc1 lds\nM v[124:127],a[76:79],a[88:91],v[124:127]\nds_read_b64_tr_b16 a[48:49],v9 offset:24576\nds_read_b64_tr_b16 a[50:51],v10 offset:24576\nM v[92:95],a[76:79],a[92:95],v[92:95]\ns_waitcnt lgkmcnt(5)\n" \
"ds_read_b64_tr_b16 a[52:53],v11 offset:24576\nM v[64:67],a[0:3],a[44:47],v[64:67]\nds_read_b64_tr_b16 a[54:55],v12 offset:24576\nds_read_b64_tr_b16 a[56:57],v9 offset:25088\nM v[68:71],a[4:7],a[44:47],v[68:71]\nbuffer_load_dwordx2 v[58:59],v57,s[36:39],0 offen sc1\nM v[72:75],a[8:11],a[44:47],v[72:75]\nds_read_b64_tr_b16 a[58:59],v10 offset:25088\n" \
"ds_read_b64_tr_b16 a[60:61],v11 offset:25088\nM v[76:79],a[12:15],a[44:47],v[76:79]\nds_read_b64_tr_b16 a[62:63],v12 offset:25088\nM v[80:83],a[16:19],a[44:47],v[80:83]\nds_read_b128 a[192:195],v13 offset:32768\nM v[84:87],a[20:23],a[44:47],v[84:87]\ns_waitcnt lgkmcnt(4)\nds_read_b128 a[92:95],v8 offset:12288\n" \
"M v[88:91],a[24:27],a[44:47],v[88:91]\nbuffer_load_dwordx4 v22,s[40:43],s61 offen offset:2048 sc1 lds\nM v[92:95],a[28:31],a[44:47],v[92:95]\nds_read_b128 a[196:199],v13 offset:34816\nds_read_b128 a[200:203],v13 offset:36864\nM v[96:99],a[0:3],a[40:43],v[96:99]\nds_read_b128 a[204:207],v13 offset:38912\nM v[100:103],a[4:7],a[40:43],v[100:103]\n" \
"ds_read_b128 a[208:211],v13 offset:33792\nds_read_b128 a[212:215],v13 offset:35840\nds_read_b128 a[216:219],v13 offset:37888\nds_read_b128 a[220:223],v13 offset:39936\nds_read_b128 a[80:83],v8 offset:0\nds_read_b128 a[84:87],v8 offset:4096\nds_read_b64_tr_b16 a[64:65],v9 offset:25600\nds_read_b64_tr_b16 a[66:67],v10 offset:25600\n" \
"ds_read_b64_tr_b16 a[68:69],v11 offset:25600\nds_read_b64_tr_b16 a[70:71],v12 offset:25600\nds_read_b64_tr_b16 a[72:73],v9 offset:26112\nds_read_b64_tr_b16 a[74:75],v10 offset:26112\nds_read_b64_tr_b16 a[76:77],v11 offset:26112\nds_read_b64_tr_b16 a[78:79],v12 offset:26112\nds_read_b128 a[88:91],v8 offset:8192\nM v[104:107],a[8:11],a[40:43],v[104:107]\n" \
"buffer_load_dwordx4 v22,s[40:43],s61 offen offset:3072 sc1 lds\nM v[108:111],a[12:15],a[40:43],v[108:111]\ns_add_u32 m0,s22,32768\nds_read_b128 a[36:39],v7 offset:4096\nM v[112:115],a[16:19],a[40:43],v[112:115]\ns_mov_b64 exec,0\ns_cmp_eq_u32 %2,0\ns_cbranch_scc1 LG26_%=\n" \
"s_cmp_eq_u32 %2,1\ns_cbranch_scc1 LG27_%=\ns_cmp_eq_u32 %2,2\ns_cbranch_scc1 LG12_%=\ns_cmp_eq_u32 %2,3\ns_cbranch_scc1 LG13_%=\nLHR1_%=:\ns_mov_b64 exec,-1\n" \
"ds_read_b128 a[32:35],v7 offset:0\nM v[116:119],a[20:23],a[40:43],v[116:119]\ns_add_u32 s61,s44,8192\nM v[120:123],a[24:27],a[40:43],v[120:123]\nM v[124:127],a[28:31],a[40:43],v[124:127]\ns_waitcnt lgkmcnt(2)\nM v[64:67],a[48:51],a[92:95],v[64:67]\ns_barrier\n" \
"ds_read_b128 a[96:99],v13 offset:49152\nM v[68:71],a[52:55],a[92:95],v[68:71]\nds_read_b128 a[100:103],v13 offset:51200\nds_read_b128 a[104:107],v13 offset:53248\nM v[72:75],a[56:59],a[92:95],v[72:75]\nC v64,v64,v65\nC v65,v66,v67\nM v[76:79],a[60:63],a[92:95],v[76:79]\n" \
"C v66,v68,v69\nC v67,v70,v71\nM v[80:83],a[64:67],a[92:95],v[80:83]\nds_write_b128 v20,v[64:67] offset:0\nM v[84:87],a[68:71],a[92:95],v[84:87]\nC v72,v72,v73\nC v73,v74,v75\nM v[88:91],a[72:75],a[92:95],v[88:91]\n" \
"C v74,v76,v77\nC v75,v78,v79\nM v[92:95],a[76:79],a[92:95],v[92:95]\nC v80,v80,v81\nC v81,v82,v83\nM v[96:99],a[48:51],a[88:91],v[96:99]\nds_write_b128 v20,v[72:75] offset:1024\nM v[100:103],a[52:55],a[88:91],v[100:103]\n" \
"C v82,v84,v85\nC v83,v86,v87\nM v[104:107],a[56:59],a[88:91],v[104:107]\nC v88,v88,v89\nC v89,v90,v91\nM v[108:111],a[60:63],a[88:91],v[108:111]\nC v90,v92,v93\nC v91,v94,v95\n" \
"M v[112:115],a[64:67],a[88:91],v[112:115]\nds_read_b128 a[176:179],v20 offset:0\nC v96,v96,v97\nM v[116:119],a[68:71],a[88:91],v[116:119]\nds_read_b128 a[180:183],v20 offset:1024\nC v97,v98,v99\nM v[120:123],a[72:75],a[88:91],v[120:123]\nC v98,v100,v101\n" \
"C v99,v102,v103\nM v[124:127],a[76:79],a[88:91],v[124:127]\ns_waitcnt lgkmcnt(2)\nds_write_b128 v20,v[80:83] offset:2048\nM v[160:163],a[0:3],a[36:39],v[160:163]\nC v104,v104,v105\nC v105,v106,v107\nM v[164:167],a[4:7],a[36:39],v[164:167]\n" \
"C v106,v108,v109\nC v107,v110,v111\nM v[168:171],a[8:11],a[36:39],v[168:171]\nLG14_%=:\ns_cbranch_execz LHR1_%=\nC v112,v112,v113\nC v113,v114,v115\nM v[172:175],a[12:15],a[36:39],v[172:175]\n" \
"ds_write_b128 v20,v[88:91] offset:3072\nM v[176:179],a[16:19],a[36:39],v[176:179]\nC v114,v116,v117\nC v115,v118,v119\nM v[180:183],a[20:23],a[36:39],v[180:183]\nC v120,v120,v121\nC v121,v122,v123\nM v[184:187],a[24:27],a[36:39],v[184:187]\n" \
"C v122,v124,v125\nC v123,v126,v127\nM v[188:191],a[28:31],a[36:39],v[188:191]\nds_read_b128 a[184:187],v20 offset:2048\nM v[160:163],a[48:51],a[84:87],v[160:163]\nds_read_b128 a[188:191],v20 offset:3072\nv_add_u32 v20,s65,v20\nM v[164:167],a[52:55],a[84:87],v[164:167]\n" \
"s_waitcnt vmcnt(2)\nv_cmp_gt_u64 s[98:99],s[14:15],v[58:59]\nv_cmp_le_u64 s[18:19],s[16:17],v[58:59]\nM v[168:171],a[56:59],a[84:87],v[168:171]\ns_or_b64 s[98:99],s[98:99],s[18:19]\ns_cbranch_scc1 LPK_%=\nLPKR_%=:\nds_write_b128 v20,v[96:99] offset:0\n" \
"M v[172:175],a[60:63],a[84:87],v[172:175]\nbuffer_load_dwordx4 v22,s[40:43],s45 offen offset:0 sc1 lds\nM v[176:179],a[64:67],a[84:87],v[176:179]\nC v160,v160,v161\nC v161,v162,v163\nM v[180:183],a[68:71],a[84:87],v[180:183]\nC v162,v164,v165\nC v163,v166,v167\n" \
"M v[184:187],a[72:75],a[84:87],v[184:187]\ns_waitcnt lgkmcnt(1)\nds_write_b128 v20,v[104:107] offset:1024\nM v[188:191],a[76:79],a[84:87],v[188:191]\nbuffer_load_dwordx4 v22,s[40:43],s45 offen offset:1024 sc1 lds\nM v[88:91],a[192:195],a[176:179],v[48:51]\nC v168,v168,v169\nC v169,v170,v171\n" \
"M v[88:91],a[196:199],a[180:183],v[88:91]\nC v170,v172,v173\nC v171,v174,v175\nM v[88:91],a[200:203],a[184:187],v[88:91]\nC v176,v176,v177\nC v177,v178,v179\nM v[88:91],a[204:207],a[188:191],v[88:91]\nbuffer_load_dwordx4 v22,s[40:43],s45 offen offset:2048 sc1 lds\n" \
"M v[92:95],a[208:211],a[176:179],v[52:55]\nds_write_b128 v20,v[112:115] offset:2048\nM v[92:95],a[212:215],a[180:183],v[92:95]\nC v178,v180,v181\nC v179,v182,v183\nM v[92:95],a[216:219],a[184:187],v[92:95]\nC v184,v184,v185\nC v185,v186,v187\n" \
"M v[92:95],a[220:223],a[188:191],v[92:95]\nbuffer_load_dwordx4 v22,s[40:43],s45 offen offset:3072 sc1 lds\nM v[128:131],a[0:3],a[32:35],v[128:131]\ns_add_u32 m0,s22,16384\nds_write_b128 v20,v[120:123] offset:3072\nM v[132:135],a[4:7],a[32:35],v[132:135]\ns_add_u32 s45,s45,s49\nC v186,v188,v189\n" \
"C v187,v190,v191\nM v[136:139],a[8:11],a[32:35],v[136:139]\nv_max3_f32 v60,v88,v89,v90\nv_max3_f32 v61,v91,v92,v93\nM v[140:143],a[12:15],a[32:35],v[140:143]\nv_max3_f32 v60,v60,v94,v95\nv_max3_f32 v60,v60,v61,s76\nM v[144:147],a[16:19],a[32:35],v[144:147]\n" \
"ds_read_b128 a[160:163],v20 offset:0\nv_mov_b32 v61,v60\nM v[148:151],a[20:23],a[32:35],v[148:151]\nds_read_b128 a[164:167],v20 offset:1024\nv_xor_b32 v59,0x2000,v23\nM v[152:155],a[24:27],a[32:35],v[152:155]\nds_read_b128 a[168:171],v20 offset:2048\nv_and_b32 v58,0xf0,v4\n" \
"M v[156:159],a[28:31],a[32:35],v[156:159]\nds_read_b128 a[172:175],v20 offset:3072\nv_add_u32 v20,s65,v20\nM v[128:131],a[48:51],a[80:83],v[128:131]\nds_read_b128 a[108:111],v13 offset:55296\nv_lshl_add_u32 v57,v58,3,s62\nM v[132:135],a[52:55],a[80:83],v[132:135]\nds_read_b128 a[112:115],v13 offset:50176\n" \
"ds_read_b128 a[116:119],v13 offset:52224\nds_read_b128 a[120:123],v13 offset:54272\nds_read_b128 a[124:127],v13 offset:56320\nds_read_b128 a[192:195],v14 offset:0\nds_read_b128 a[196:199],v14 offset:1024\nds_read_b128 a[200:203],v14 offset:2048\nds_read_b128 a[204:207],v14 offset:3072\nds_read_b128 a[208:211],v14 offset:4096\n" \
"ds_read_b128 a[212:215],v14 offset:5120\nds_read_b128 a[216:219],v14 offset:6144\nds_read_b128 a[220:223],v14 offset:7168\nM v[136:139],a[56:59],a[80:83],v[136:139]\nv_permlane32_swap_b32 v60,v61\nM v[140:143],a[60:63],a[80:83],v[140:143]\nv_max_f32 v60,v60,v61\nv_mov_b32 v61,v60\n" \
"M v[144:147],a[64:67],a[80:83],v[144:147]\nds_write_b128 v20,v[160:163] offset:0\nM v[148:151],a[68:71],a[80:83],v[148:151]\nv_permlane16_swap_b32 v60,v61\nM v[152:155],a[72:75],a[80:83],v[152:155]\nv_min_f32 v44,-v60,-v61\nv_add_f32 v88,v88,v44\nM v[156:159],a[76:79],a[80:83],v[156:159]\n" \
"s_waitcnt vmcnt(13) lgkmcnt(1)\nv_add_f32 v89,v89,v44\nv_add_f32 v90,v90,v44\nM v[112:115],a[96:99],a[160:163],v[48:51]\ns_barrier\nds_read_b128 a[144:147],v20 offset:0\nM v[112:115],a[100:103],a[164:167],v[112:115]\nds_write_b128 v20,v[168:171] offset:1024\n" \
"M v[112:115],a[104:107],a[168:171],v[112:115]\nv_add_f32 v91,v91,v44\nv_add_f32 v92,v92,v44\nM v[112:115],a[108:111],a[172:175],v[112:115]\nv_add_f32 v93,v93,v44\nv_add_f32 v94,v94,v44\nM v[116:119],a[112:115],a[160:163],v[52:55]\nv_exp_f32 v88,v88\n" \
"M v[116:119],a[116:119],a[164:167],v[116:119]\nv_exp_f32 v89,v89\nM v[116:119],a[120:123],a[168:171],v[116:119]\nv_min_f32 v45,-v60,-v61\nv_min_f32 v46,-v60,-v61\nv_min_f32 v47,-v60,-v61\nM v[116:119],a[124:127],a[172:175],v[116:119]\nds_write_b128 v20,v[176:179] offset:2048\n" \
"M v[120:123],a[96:99],a[176:179],v[44:47]\nv_exp_f32 v90,v90\nM v[120:123],a[100:103],a[180:183],v[120:123]\nv_exp_f32 v91,v91\nM v[120:123],a[104:107],a[184:187],v[120:123]\nv_exp_f32 v92,v92\nM v[120:123],a[108:111],a[188:191],v[120:123]\nv_exp_f32 v93,v93\n" \
"M v[124:127],a[112:115],a[176:179],v[44:47]\nv_exp_f32 v94,v94\nM v[124:127],a[116:119],a[180:183],v[124:127]\nv_add_f32 v95,v95,v44\nv_exp_f32 v95,v95\nv_xor_b32 v13,0x10000,v13\nM v[124:127],a[120:123],a[184:187],v[124:127]\nds_read_b128 a[96:99],v13 offset:32768\n" \
"C v88,v88,v89\nC v89,v90,v91\nC v90,v92,v93\nC v91,v94,v95\nM v[124:127],a[124:127],a[188:191],v[124:127]\nds_write_b128 v20,v[184:187] offset:3072\nds_read_b128 a[148:151],v20 offset:1024\nds_read_b128 a[100:103],v13 offset:34816\n" \
"M v[224:227],a[192:195],v[88:91],0\nds_read_b128 a[152:155],v20 offset:2048\nds_read_b128 a[104:107],v13 offset:36864\nM v[228:231],a[196:199],v[88:91],0\nds_read_b128 a[156:159],v20 offset:3072\nds_read_b128 a[108:111],v13 offset:38912\nM v[232:235],a[200:203],v[88:91],0\nds_read_b128 a[112:115],v13 offset:33792\n" \
"ds_read_b128 a[116:119],v13 offset:35840\nds_read_b128 a[120:123],v13 offset:37888\nds_read_b128 a[124:127],v13 offset:39936\nM v[236:239],a[204:207],v[88:91],0\nv_add_u32 v20,s65,v20\nC v128,v128,v129\nM v[240:243],a[208:211],v[88:91],0\nC v129,v130,v131\n" \
"C v130,v132,v133\nM v[244:247],a[212:215],v[88:91],0\nC v131,v134,v135\nC v136,v136,v137\nM v[248:251],a[216:219],v[88:91],0\nds_write_b128 v20,v[128:131] offset:0\nM v[252:255],a[220:223],v[88:91],0\nv_min_f32 v29,-v60,-v61\n" \
"v_max3_f32 v60,v112,v113,v114\nv_max3_f32 v61,v115,v116,v117\nv_max3_f32 v60,v60,v118,v119\nv_max3_f32 v60,v60,v61,s76\nv_mov_b32 v61,v60\nM v[16:19],a[236:239],v[88:91],v[24:27]\ns_waitcnt vmcnt(9) lgkmcnt(1)\nv_permlane32_swap_b32 v60,v61\n" \
"v_max_f32 v60,v60,v61\nv_mov_b32 v61,v60\nM v[72:75],a[96:99],a[144:147],v[48:51]\ns_barrier\nv_permlane16_swap_b32 v60,v61\nv_min_f32 v40,-v60,-v61\nv_add_f32 v112,v112,v40\nv_exp_f32 v112,v112\n" \
"M v[72:75],a[100:103],a[148:151],v[72:75]\nv_add_f32 v113,v113,v40\nv_exp_f32 v113,v113\nM v[72:75],a[104:107],a[152:155],v[72:75]\nv_add_f32 v114,v114,v40\nv_exp_f32 v114,v114\nM v[72:75],a[108:111],a[156:159],v[72:75]\nv_add_f32 v115,v115,v40\n" \
"v_exp_f32 v115,v115\nM v[76:79],a[112:115],a[144:147],v[52:55]\nv_add_f32 v116,v116,v40\nv_exp_f32 v116,v116\nM v[76:79],a[116:119],a[148:151],v[76:79]\nv_add_f32 v117,v117,v40\nv_exp_f32 v117,v117\nM v[76:79],a[120:123],a[152:155],v[76:79]\n" \
"v_add_f32 v118,v118,v40\nv_exp_f32 v118,v118\nv_min_f32 v41,-v60,-v61\nv_min_f32 v42,-v60,-v61\nv_min_f32 v43,-v60,-v61\nM v[76:79],a[124:127],a[156:159],v[76:79]\nv_add_f32 v119,v119,v40\nv_exp_f32 v119,v119\n" \
"M v[80:83],a[96:99],a[160:163],v[40:43]\nv_exp_f32 v120,v120\nM v[88:91],a[96:99],a[176:179],v[44:47]\nv_exp_f32 v121,v121\nM v[80:83],a[100:103],a[164:167],v[80:83]\nv_exp_f32 v122,v122\nM v[88:91],a[100:103],a[180:183],v[88:91]\nv_exp_f32 v123,v123\n" \
"M v[80:83],a[104:107],a[168:171],v[80:83]\nv_exp_f32 v124,v124\nM v[88:91],a[104:107],a[184:187],v[88:91]\nv_exp_f32 v125,v125\nC v112,v112,v113\nC v113,v114,v115\nC v114,v116,v117\nC v115,v118,v119\n" \
"M v[80:83],a[108:111],a[172:175],v[80:83]\nv_exp_f32 v126,v126\nM v[24:27],a[232:235],v[112:115],v[16:19]\nds_read_b128 a[192:195],v14 offset:16384\nv_exp_f32 v127,v127\nM v[88:91],a[108:111],a[188:191],v[88:91]\nC v137,v138,v139\nC v138,v140,v141\n" \
"C v139,v142,v143\nC v144,v144,v145\nC v145,v146,v147\nC v152,v152,v153\nC v146,v148,v149\nC v147,v150,v151\nC v153,v154,v155\nC v154,v156,v157\n" \
"C v155,v158,v159\nC v120,v120,v121\nC v121,v122,v123\nC v122,v124,v125\nC v123,v126,v127\nM v[84:87],a[112:115],a[160:163],v[40:43]\nds_write_b128 v20,v[136:139] offset:1024\nds_write_b128 v20,v[144:147] offset:2048\n" \
"ds_write_b128 v20,v[152:155] offset:3072\nds_read_b128 a[196:199],v14 offset:17408\nM v[24:27],a[236:239],v[120:123],v[24:27]\nv_min_f32 v28,-v60,-v61\nv_max3_f32 v60,v72,v73,v74\nM v[92:95],a[112:115],a[176:179],v[44:47]\nds_read_b128 a[128:131],v20 offset:0\nds_read_b128 a[132:135],v20 offset:1024\n" \
"ds_read_b128 a[136:139],v20 offset:2048\nds_read_b128 a[140:143],v20 offset:3072\nds_read_b128 a[200:203],v14 offset:18432\nM v[84:87],a[116:119],a[164:167],v[84:87]\nv_max3_f32 v61,v75,v76,v77\nv_max3_f32 v60,v60,v78,v79\nM v[92:95],a[116:119],a[180:183],v[92:95]\nds_read_b128 a[204:207],v14 offset:19456\n" \
"v_max3_f32 v60,v60,v61,s76\nM v[84:87],a[120:123],a[168:171],v[84:87]\nv_max3_f32 v15,v24,v25,v26\nv_max_f32 v15,v15,v27\nv_cmp_nge_f32 vcc,0x53800000,v15\nM v[92:95],a[120:123],a[184:187],v[92:95]\nds_read_b128 a[208:211],v14 offset:20480\nds_read_b128 a[212:215],v14 offset:21504\n" \
"M v[84:87],a[124:127],a[172:175],v[84:87]\ns_waitcnt lgkmcnt(4)\ns_cbranch_vccnz Lcs1_%=\nLcr2_%=:\nbuffer_load_dwordx4 v22,s[40:43],s61 offen offset:0 sc1 lds\nM v[92:95],a[124:127],a[188:191],v[92:95]\nds_read_b128 a[216:219],v14 offset:22528\nds_read_b128 a[220:223],v14 offset:23552\n" \
"M v[192:195],a[192:195],v[112:115],0\nds_read_b128 a[96:99],v13 offset:49152\nds_read_b128 a[100:103],v13 offset:51200\nds_read_b128 a[104:107],v13 offset:53248\nds_read_b128 a[108:111],v13 offset:55296\nds_read_b128 a[112:115],v13 offset:50176\nds_read_b128 a[116:119],v13 offset:52224\nds_read_b128 a[120:123],v13 offset:54272\n" \
"ds_read_b128 a[124:127],v13 offset:56320\nM v[224:227],a[192:195],v[120:123],v[224:227]\nv_mov_b32 v61,v60\nv_xor_b32 v14,0x10000,v14\nM v[196:199],a[196:199],v[112:115],0\nbuffer_load_dwordx4 v22,s[40:43],s61 offen offset:1024 sc1 lds\nM v[228:231],a[196:199],v[120:123],v[228:231]\ns_waitcnt lgkmcnt(10)\n" \
"v_permlane32_swap_b32 v60,v61\nM v[200:203],a[200:203],v[112:115],0\nv_max_f32 v60,v60,v61\nv_mov_b32 v61,v60\nM v[232:235],a[200:203],v[120:123],v[232:235]\ns_nop 0\nv_permlane16_swap_b32 v60,v61\nM v[204:207],a[204:207],v[112:115],0\n" \
"s_waitcnt vmcnt(6) lgkmcnt(0)\nbuffer_load_dwordx4 v22,s[40:43],s61 offen offset:2048 sc1 lds\nM v[236:239],a[204:207],v[120:123],v[236:239]\ns_barrier\nv_min_f32 v36,-v60,-v61\nM v[208:211],a[208:211],v[112:115],0\nv_add_f32 v72,v72,v36\nv_add_f32 v73,v73,v36\n" \
"M v[240:243],a[208:211],v[120:123],v[240:243]\nv_add_f32 v74,v74,v36\nv_add_f32 v75,v75,v36\nM v[212:215],a[212:215],v[112:115],0\nbuffer_load_dwordx4 v22,s[40:43],s61 offen offset:3072 sc1 lds\nM v[244:247],a[212:215],v[120:123],v[244:247]\ns_add_u32 m0,s22,49152\nv_exp_f32 v72,v72\n" \
"M v[216:219],a[216:219],v[112:115],0\nv_exp_f32 v73,v73\nM v[248:251],a[216:219],v[120:123],v[248:251]\nv_exp_f32 v74,v74\nM v[220:223],a[220:223],v[112:115],0\nbuffer_load_dwordx4 v22,s[40:43],s45 offen offset:0 sc1 lds\nM v[252:255],a[220:223],v[120:123],v[252:255]\nv_exp_f32 v75,v75\n" \
"M v[96:99],a[96:99],a[128:131],v[48:51]\nv_add_f32 v76,v76,v36\nv_add_f32 v77,v77,v36\nM v[96:99],a[100:103],a[132:135],v[96:99]\nv_exp_f32 v76,v76\nM v[96:99],a[104:107],a[136:139],v[96:99]\nbuffer_load_dwordx4 v22,s[40:43],s45 offen offset:1024 sc1 lds\nM v[96:99],a[108:111],a[140:143],v[96:99]\n" \
"v_exp_f32 v77,v77\nM v[100:103],a[112:115],a[128:131],v[52:55]\nv_add_f32 v78,v78,v36\nv_add_f32 v79,v79,v36\nM v[100:103],a[116:119],a[132:135],v[100:103]\nv_exp_f32 v78,v78\nM v[100:103],a[120:123],a[136:139],v[100:103]\nv_min_f32 v37,-v60,-v61\n" \
"v_min_f32 v38,-v60,-v61\nv_min_f32 v39,-v60,-v61\nM v[100:103],a[124:127],a[140:143],v[100:103]\nbuffer_load_dwordx4 v22,s[40:43],s45 offen offset:2048 sc1 lds\nM v[104:107],a[96:99],a[144:147],v[36:39]\nv_exp_f32 v79,v79\nM v[112:115],a[96:99],a[160:163],v[40:43]\nv_exp_f32 v80,v80\n" \
"M v[120:123],a[96:99],a[176:179],v[44:47]\nv_exp_f32 v81,v81\nM v[104:107],a[100:103],a[148:151],v[104:107]\nv_exp_f32 v82,v82\nM v[112:115],a[100:103],a[164:167],v[112:115]\nv_exp_f32 v83,v83\nM v[120:123],a[100:103],a[180:183],v[120:123]\nv_exp_f32 v84,v84\n" \
"M v[104:107],a[104:107],a[152:155],v[104:107]\nv_exp_f32 v85,v85\nM v[112:115],a[104:107],a[168:171],v[112:115]\nv_exp_f32 v86,v86\nM v[120:123],a[104:107],a[184:187],v[120:123]\nv_exp_f32 v87,v87\nM v[104:107],a[108:111],a[156:159],v[104:107]\nv_exp_f32 v88,v88\n" \
"M v[112:115],a[108:111],a[172:175],v[112:115]\nv_exp_f32 v89,v89\nM v[120:123],a[108:111],a[188:191],v[120:123]\nv_exp_f32 v90,v90\nC v72,v72,v73\nC v73,v74,v75\nC v74,v76,v77\nC v75,v78,v79\n" \
"M v[108:111],a[112:115],a[144:147],v[36:39]\nv_exp_f32 v91,v91\nM v[16:19],a[228:231],v[72:75],v[24:27]\nv_exp_f32 v92,v92\nM v[116:119],a[112:115],a[160:163],v[40:43]\nv_exp_f32 v93,v93\nC v80,v80,v81\nC v81,v82,v83\n" \
"C v82,v84,v85\nC v83,v86,v87\nM v[124:127],a[112:115],a[176:179],v[44:47]\nv_exp_f32 v94,v94\nM v[16:19],a[232:235],v[80:83],v[16:19]\nds_read_b128 a[192:195],v14 offset:0\nv_exp_f32 v95,v95\nM v[108:111],a[116:119],a[148:151],v[108:111]\n" \
"C v88,v88,v89\nC v89,v90,v91\nC v90,v92,v93\nC v91,v94,v95\nM v[116:119],a[116:119],a[164:167],v[116:119]\nbuffer_load_dwordx4 v22,s[40:43],s45 offen offset:3072 sc1 lds\nM v[16:19],a[236:239],v[88:91],v[16:19]\ns_add_u32 m0,s22,65536\n" \
"ds_read_b128 a[196:199],v14 offset:1024\nds_read_b128 a[200:203],v14 offset:2048\nM v[124:127],a[116:119],a[180:183],v[124:127]\ns_add_u32 s45,s45,s49\nds_read_b128 a[204:207],v14 offset:3072\nv_min_f32 v31,-v60,-v61\nM v[108:111],a[120:123],a[152:155],v[108:111]\nds_read_b128 a[208:211],v14 offset:4096\n" \
"v_max3_f32 v60,v96,v97,v98\nM v[116:119],a[120:123],a[168:171],v[116:119]\nv_max3_f32 v61,v99,v100,v101\nv_max3_f32 v60,v60,v102,v103\nv_max3_f32 v60,v60,v61,s76\nv_mov_b32 v61,v60\nv_max3_f32 v15,v16,v17,v18\nv_max_f32 v15,v15,v19\n" \
"v_cmp_nge_f32 vcc,0x53800000,v15\nM v[124:127],a[120:123],a[184:187],v[124:127]\ns_waitcnt lgkmcnt(4)\nv_permlane32_swap_b32 v60,v61\nv_max_f32 v60,v60,v61\nv_mov_b32 v61,v60\nM v[108:111],a[124:127],a[156:159],v[108:111]\ns_cbranch_vccnz Lcs3_%=\n" \
"Lcr4_%=:\nv_permlane16_swap_b32 v60,v61\nv_min_f32 v32,-v60,-v61\nv_xor_b32 v13,s54,v13\nv_min_f32 v33,-v60,-v61\nv_min_f32 v34,-v60,-v61\nv_min_f32 v35,-v60,-v61\nv_min_f32 v30,-v60,-v61\n" \
"v_add_f32 v96,v96,v32\nv_add_f32 v97,v97,v32\nv_add_f32 v98,v98,v32\nv_add_f32 v99,v99,v32\nv_add_f32 v100,v100,v32\nv_add_f32 v101,v101,v32\nv_add_f32 v102,v102,v32\nv_add_f32 v103,v103,v32\n" \
"M v[116:119],a[124:127],a[172:175],v[116:119]\ns_cmp_eq_u32 s52,s55\nds_write_b128 v23,v[28:31] offset:0\nds_read_b128 a[212:215],v14 offset:5120\nds_read_b128 a[216:219],v14 offset:6144\nds_read_b128 a[220:223],v14 offset:7168\nM v[124:127],a[124:127],a[188:191],v[124:127]\ns_cbranch_scc1 LH1_%=\n" \
"LH1R0_%=:\nv_exp_f32 v96,v96\nM v[160:163],a[192:195],v[72:75],0\nbuffer_load_dwordx4 v22,s[40:43],s48 offen offset:0 sc1 lds\nM v[192:195],a[192:195],v[80:83],v[192:195]\nv_exp_f32 v97,v97\nM v[224:227],a[192:195],v[88:91],v[224:227]\ns_waitcnt lgkmcnt(4)\n" \
"v_exp_f32 v98,v98\nM v[164:167],a[196:199],v[72:75],0\nv_exp_f32 v99,v99\nM v[196:199],a[196:199],v[80:83],v[196:199]\ns_waitcnt vmcnt(5) lgkmcnt(0)\nv_exp_f32 v100,v100\nM v[228:231],a[196:199],v[88:91],v[228:231]\ns_barrier\n" \
"ds_read_b128 v[28:31],v59 offset:0\nM v[168:171],a[200:203],v[72:75],0\nbuffer_load_dwordx4 v22,s[40:43],s48 offen offset:1024 sc1 lds\nM v[200:203],a[200:203],v[80:83],v[200:203]\nv_exp_f32 v101,v101\nM v[232:235],a[200:203],v[88:91],v[232:235]\nv_exp_f32 v102,v102\nM v[172:175],a[204:207],v[72:75],0\n" \
"v_exp_f32 v103,v103\nM v[204:207],a[204:207],v[80:83],v[204:207]\nbuffer_load_dwordx4 v22,s[40:43],s48 offen offset:2048 sc1 lds\nM v[236:239],a[204:207],v[88:91],v[236:239]\ns_waitcnt lgkmcnt(0)\nv_exp_f32 v104,v104\nM v[176:179],a[208:211],v[72:75],0\nv_xor_b32 v13,0x10000,v13\n" \
"C v96,v96,v97\nM v[208:211],a[208:211],v[80:83],v[208:211]\nds_read_b128 a[192:195],v13 offset:32768\nds_read_b128 a[196:199],v13 offset:34816\nM v[240:243],a[208:211],v[88:91],v[240:243]\nbuffer_load_dwordx4 v22,s[40:43],s48 offen offset:3072 sc1 lds\nM v[180:183],a[212:215],v[72:75],0\ns_add_u32 m0,s22,98304\n" \
"ds_read_b128 a[200:203],v13 offset:36864\nC v97,v98,v99\nM v[212:215],a[212:215],v[80:83],v[212:215]\ns_add_u32 s48,s48,s49\nv_exp_f32 v105,v105\nM v[244:247],a[212:215],v[88:91],v[244:247]\nv_exp_f32 v106,v106\nM v[184:187],a[216:219],v[72:75],0\n" \
"buffer_load_dwordx4 v22,s[40:43],s45 offen offset:0 sc1 lds\nM v[216:219],a[216:219],v[80:83],v[216:219]\nds_read_b128 a[204:207],v13 offset:38912\nv_cndmask_b32 v32,v32,v28,s[66:67]\nM v[248:251],a[216:219],v[88:91],v[248:251]\nv_exp_f32 v107,v107\nM v[188:191],a[220:223],v[72:75],0\nv_exp_f32 v108,v108\n" \
"M v[220:223],a[220:223],v[80:83],v[220:223]\nv_cndmask_b32 v33,v33,v28,s[66:67]\nv_cndmask_b32 v34,v34,v28,s[66:67]\nv_cndmask_b32 v35,v35,v28,s[66:67]\nM v[252:255],a[220:223],v[88:91],v[252:255]\ns_waitcnt lgkmcnt(1)\nv_cndmask_b32 v36,v36,v29,s[66:67]\nv_cndmask_b32 v37,v37,v29,s[66:67]\n" \
"v_cndmask_b32 v38,v38,v29,s[66:67]\nv_cndmask_b32 v39,v39,v29,s[66:67]\nM v[64:67],a[192:195],a[128:131],v[32:35]\nC v98,v100,v101\nC v99,v102,v103\nM v[72:75],a[192:195],a[144:147],v[36:39]\nv_cndmask_b32 v40,v40,v30,s[66:67]\nv_cndmask_b32 v41,v41,v30,s[66:67]\n" \
"v_cndmask_b32 v42,v42,v30,s[66:67]\nv_cndmask_b32 v43,v43,v30,s[66:67]\nM v[24:27],a[224:227],v[96:99],v[16:19]\nv_exp_f32 v109,v109\nv_cndmask_b32 v44,v44,v31,s[66:67]\nv_cndmask_b32 v45,v45,v31,s[66:67]\nv_cndmask_b32 v46,v46,v31,s[66:67]\nv_cndmask_b32 v47,v47,v31,s[66:67]\n" \
"M v[80:83],a[192:195],a[160:163],v[40:43]\nv_exp_f32 v110,v110\nM v[88:91],a[192:195],a[176:179],v[44:47]\nv_exp_f32 v111,v111\nM v[64:67],a[196:199],a[132:135],v[64:67]\nC v104,v104,v105\nC v105,v106,v107\nC v106,v108,v109\n" \
"C v107,v110,v111\nM v[72:75],a[196:199],a[148:151],v[72:75]\nbuffer_load_dwordx4 v22,s[40:43],s45 offen offset:1024 sc1 lds\nM v[24:27],a[228:231],v[104:107],v[24:27]\nds_read_b128 a[208:211],v13 offset:33792\nds_read_b128 a[212:215],v13 offset:35840\nM v[80:83],a[196:199],a[164:167],v[80:83]\nv_exp_f32 v112,v112\n" \
"M v[88:91],a[196:199],a[180:183],v[88:91]\nv_exp_f32 v113,v113\nM v[64:67],a[200:203],a[136:139],v[64:67]\nv_exp_f32 v114,v114\nM v[72:75],a[200:203],a[152:155],v[72:75]\nv_exp_f32 v115,v115\nM v[80:83],a[200:203],a[168:171],v[80:83]\nv_exp_f32 v116,v116\n" \
"M v[88:91],a[200:203],a[184:187],v[88:91]\ns_waitcnt lgkmcnt(0)\nv_exp_f32 v117,v117\nM v[64:67],a[204:207],a[140:143],v[64:67]\nv_exp_f32 v118,v118\nM v[72:75],a[204:207],a[156:159],v[72:75]\nv_exp_f32 v119,v119\nM v[80:83],a[204:207],a[172:175],v[80:83]\n" \
"C v112,v112,v113\nC v113,v114,v115\nC v114,v116,v117\nC v115,v118,v119\nM v[88:91],a[204:207],a[188:191],v[88:91]\nds_read_b128 a[216:219],v13 offset:37888\nv_exp_f32 v120,v120\nM v[24:27],a[232:235],v[112:115],v[24:27]\n" \
"v_exp_f32 v121,v121\nM v[68:71],a[208:211],a[128:131],v[32:35]\nv_exp_f32 v122,v122\nM v[76:79],a[208:211],a[144:147],v[36:39]\nv_exp_f32 v123,v123\nM v[84:87],a[208:211],a[160:163],v[40:43]\nds_read_b128 a[220:223],v13 offset:39936\nv_exp_f32 v124,v124\n" \
"M v[92:95],a[208:211],a[176:179],v[44:47]\nLG15_%=:\ns_cbranch_execz LHR1_%=\nv_exp_f32 v125,v125\nM v[68:71],a[212:215],a[132:135],v[68:71]\nv_exp_f32 v126,v126\nM v[76:79],a[212:215],a[148:151],v[76:79]\nv_exp_f32 v127,v127\n" \
"M v[84:87],a[212:215],a[164:167],v[84:87]\nds_read_b128 a[192:195],v14 offset:16384\nC v120,v120,v121\nC v121,v122,v123\nC v122,v124,v125\nC v123,v126,v127\nM v[92:95],a[212:215],a[180:183],v[92:95]\nbuffer_load_dwordx4 v22,s[40:43],s45 offen offset:2048 sc1 lds\n" \
"M v[24:27],a[236:239],v[120:123],v[24:27]\ns_waitcnt lgkmcnt(1)\nds_read_b128 a[196:199],v14 offset:17408\nds_read_b128 a[200:203],v14 offset:18432\nM v[68:71],a[216:219],a[136:139],v[68:71]\nds_read_b128 a[204:207],v14 offset:19456\nv_lshrrev_b32 v58,8,v4\nM v[76:79],a[216:219],a[152:155],v[76:79]\n" \
"ds_read_b128 a[208:211],v14 offset:20480\nv_max3_f32 v15,v24,v25,v26\nM v[84:87],a[216:219],a[168:171],v[84:87]\nbuffer_load_dwordx4 v22,s[40:43],s45 offen offset:3072 sc1 lds\nM v[92:95],a[216:219],a[184:187],v[92:95]\ns_add_u32 m0,s22,81920\nv_max_f32 v15,v15,v27\nv_cmp_nge_f32 vcc,0x53800000,v15\n" \
"M v[68:71],a[220:223],a[140:143],v[68:71]\ns_add_u32 s45,s45,s49\nds_read_b128 a[212:215],v14 offset:21504\nds_read_b128 a[216:219],v14 offset:22528\nM v[76:79],a[220:223],a[156:159],v[76:79]\ns_cbranch_vccnz Lcs5_%=\nLcr6_%=:\nv_exp_f32 v64,v64\n" \
"M v[84:87],a[220:223],a[172:175],v[84:87]\nbuffer_load_dwordx4 v22,s[40:43],s48 offen offset:0 sc1 lds\nM v[92:95],a[220:223],a[188:191],v[92:95]\ns_waitcnt lgkmcnt(2)\nds_read_b128 a[220:223],v14 offset:23552\nv_xor_b32 v14,s54,v14\nM v[128:131],a[192:195],v[96:99],0\nv_exp_f32 v65,v65\n" \
"M v[160:163],a[192:195],v[104:107],v[160:163]\nv_exp_f32 v66,v66\nM v[192:195],a[192:195],v[112:115],v[192:195]\nbuffer_load_dwordx4 v22,s[40:43],s48 offen offset:1024 sc1 lds\nM v[224:227],a[192:195],v[120:123],v[224:227]\nv_exp_f32 v67,v67\nM v[132:135],a[196:199],v[96:99],0\nv_exp_f32 v68,v68\n" \
"M v[164:167],a[196:199],v[104:107],v[164:167]\nv_exp_f32 v69,v69\nM v[196:199],a[196:199],v[112:115],v[196:199]\ns_waitcnt vmcnt(6) lgkmcnt(0)\nv_exp_f32 v70,v70\nM v[228:231],a[196:199],v[120:123],v[228:231]\ns_barrier\nds_read_b128 a[192:195],v13 offset:49152\n" \
"M v[136:139],a[200:203],v[96:99],0\nbuffer_load_dwordx4 v22,s[40:43],s48 offen offset:2048 sc1 lds\nM v[168:171],a[200:203],v[104:107],v[168:171]\nv_exp_f32 v71,v71\nM v[200:203],a[200:203],v[112:115],v[200:203]\nv_exp_f32 v72,v72\nM v[232:235],a[200:203],v[120:123],v[232:235]\nv_exp_f32 v73,v73\n" \
"M v[140:143],a[204:207],v[96:99],0\nbuffer_load_dwordx4 v22,s[40:43],s48 offen offset:3072 sc1 lds\nM v[172:175],a[204:207],v[104:107],v[172:175]\ns_add_u32 m0,s22,114688\nv_exp_f32 v74,v74\nM v[204:207],a[204:207],v[112:115],v[204:207]\ns_add_u32 s48,s48,s49\nv_exp_f32 v75,v75\n" \
"M v[236:239],a[204:207],v[120:123],v[236:239]\nv_exp_f32 v76,v76\nM v[144:147],a[208:211],v[96:99],0\nbuffer_load_dwordx4 v22,s[40:43],s45 offen offset:0 sc1 lds\nM v[176:179],a[208:211],v[104:107],v[176:179]\nds_read_b128 a[196:199],v13 offset:51200\nC v64,v64,v65\nM v[208:211],a[208:211],v[112:115],v[208:211]\n" \
"v_exp_f32 v77,v77\nM v[240:243],a[208:211],v[120:123],v[240:243]\nv_exp_f32 v78,v78\nM v[148:151],a[212:215],v[96:99],0\nbuffer_load_dwordx4 v22,s[40:43],s45 offen offset:1024 sc1 lds\nM v[180:183],a[212:215],v[104:107],v[180:183]\nv_exp_f32 v79,v79\nM v[212:215],a[212:215],v[112:115],v[212:215]\n" \
"s_add_u32 s52,s52,1\nds_read_b128 a[200:203],v13 offset:53248\nC v65,v66,v67\nM v[244:247],a[212:215],v[120:123],v[244:247]\nv_exp_f32 v80,v80\nM v[152:155],a[216:219],v[96:99],0\nbuffer_load_dwordx4 v22,s[40:43],s45 offen offset:2048 sc1 lds\nM v[184:187],a[216:219],v[104:107],v[184:187]\n" \
"C v66,v68,v69\nC v67,v70,v71\nM v[216:219],a[216:219],v[112:115],v[216:219]\nds_read_b128 a[204:207],v13 offset:55296\nC v72,v72,v73\nM v[248:251],a[216:219],v[120:123],v[248:251]\ns_waitcnt lgkmcnt(1)\nv_exp_f32 v81,v81\n" \
"M v[156:159],a[220:223],v[96:99],0\nbuffer_load_dwordx4 v22,s[40:43],s45 offen offset:3072 sc1 lds\nM v[188:191],a[220:223],v[104:107],v[188:191]\ns_add_u32 m0,s22,0\nv_exp_f32 v82,v82\nM v[220:223],a[220:223],v[112:115],v[220:223]\ns_cmp_eq_u32 s52,s56\nC v73,v74,v75\n" \
"C v74,v76,v77\nM v[252:255],a[220:223],v[120:123],v[252:255]\ns_cbranch_scc1 LEV_%=\nLEVR0_%=:\nv_exp_f32 v83,v83\nM v[96:99],a[192:195],a[128:131],v[32:35]\nbuffer_load_dwordx4 v22,s[40:43],s48 offen offset:0 sc1 lds\nM v[104:107],a[192:195],a[144:147],v[36:39]\n" \
"s_add_u32 s45,s45,s49\nds_read_b128 a[208:211],v13 offset:50176\nC v75,v78,v79\nM v[16:19],a[224:227],v[64:67],v[24:27]\nv_exp_f32 v84,v84\nM v[112:115],a[192:195],a[160:163],v[40:43]\nds_read_b128 a[212:215],v13 offset:52224\nC v80,v80,v81\n" \
"M v[120:123],a[192:195],a[176:179],v[44:47]\nbuffer_load_dwordx4 v22,s[40:43],s48 offen offset:1024 sc1 lds\nM v[96:99],a[196:199],a[132:135],v[96:99]\nv_exp_f32 v85,v85\nM v[104:107],a[196:199],a[148:151],v[104:107]\nv_exp_f32 v86,v86\nM v[16:19],a[228:231],v[72:75],v[16:19]\nv_exp_f32 v87,v87\n" \
"M v[112:115],a[196:199],a[164:167],v[112:115]\nbuffer_load_dwordx4 v22,s[40:43],s48 offen offset:2048 sc1 lds\nM v[120:123],a[196:199],a[180:183],v[120:123]\nds_read_b128 a[216:219],v13 offset:54272\nC v81,v82,v83\nM v[96:99],a[200:203],a[136:139],v[96:99]\nv_exp_f32 v88,v88\nM v[104:107],a[200:203],a[152:155],v[104:107]\n" \
"s_waitcnt lgkmcnt(1)\nv_exp_f32 v89,v89\nM v[112:115],a[200:203],a[168:171],v[112:115]\nbuffer_load_dwordx4 v22,s[40:43],s48 offen offset:3072 sc1 lds\nM v[120:123],a[200:203],a[184:187],v[120:123]\ns_add_u32 m0,s53,32768\nds_read_b128 a[220:223],v13 offset:56320\nC v82,v84,v85\n" \
"M v[96:99],a[204:207],a[140:143],v[96:99]\ns_add_u32 s48,s48,s49\nv_exp_f32 v90,v90\nM v[104:107],a[204:207],a[156:159],v[104:107]\nC v83,v86,v87\nC v88,v88,v89\nM v[112:115],a[204:207],a[172:175],v[112:115]\nbuffer_load_dwordx4 v22,s[40:43],s45 offen offset:0 sc1 lds\n" \
"M v[120:123],a[204:207],a[188:191],v[120:123]\nv_exp_f32 v91,v91\nM v[16:19],a[232:235],v[80:83],v[16:19]\nv_exp_f32 v92,v92\nM v[100:103],a[208:211],a[128:131],v[32:35]\nv_exp_f32 v93,v93\nM v[108:111],a[208:211],a[144:147],v[36:39]\nbuffer_load_dwordx4 v22,s[40:43],s45 offen offset:1024 sc1 lds\n" \
"M v[116:119],a[208:211],a[160:163],v[40:43]\nv_exp_f32 v94,v94\nM v[124:127],a[208:211],a[176:179],v[44:47]\nC v89,v90,v91\nC v90,v92,v93\nM v[100:103],a[212:215],a[132:135],v[100:103]\nv_exp_f32 v95,v95\nM v[108:111],a[212:215],a[148:151],v[108:111]\n" \
"v_xor_b32 v14,0x10000,v14\nC v91,v94,v95\nM v[116:119],a[212:215],a[164:167],v[116:119]\nds_read_b128 a[192:195],v14 offset:0\nds_read_b128 a[196:199],v14 offset:1024\nM v[124:127],a[212:215],a[180:183],v[124:127]\nbuffer_load_dwordx4 v22,s[40:43],s45 offen offset:2048 sc1 lds\nM v[16:19],a[236:239],v[88:91],v[16:19]\n" \
"s_waitcnt lgkmcnt(2)\nds_read_b128 a[200:203],v14 offset:2048\nds_read_b128 a[204:207],v14 offset:3072\nM v[100:103],a[216:219],a[136:139],v[100:103]\nds_read_b128 a[208:211],v14 offset:4096\nv_lshl_add_u32 v57,v58,27,v57\nM v[108:111],a[216:219],a[152:155],v[108:111]\nds_read_b128 a[212:215],v14 offset:5120\n" \
"v_max3_f32 v15,v16,v17,v18\nM v[116:119],a[216:219],a[168:171],v[116:119]\nbuffer_load_dwordx4 v22,s[40:43],s45 offen offset:3072 sc1 lds\nM v[124:127],a[216:219],a[184:187],v[124:127]\ns_add_u32 m0,s53,16384\nv_max_f32 v15,v15,v19\nv_cmp_nge_f32 vcc,0x53800000,v15\nM v[100:103],a[220:223],a[140:143],v[100:103]\n" \
"s_add_u32 s45,s45,s49\nds_read_b128 a[216:219],v14 offset:6144\nM v[108:111],a[220:223],a[156:159],v[108:111]\ns_cbranch_vccnz Lcs7_%=\nLcr8_%=:\nM v[116:119],a[220:223],a[172:175],v[116:119]\ns_cmp_eq_u32 s52,s55\nM v[124:127],a[220:223],a[188:191],v[124:127]\n" \
"s_cbranch_scc1 LH1_%=\nLH1R1_%=:\ns_waitcnt lgkmcnt(1)\nds_read_b128 a[220:223],v14 offset:7168\nv_xor_b32 v13,0x10000,v13\nM v[128:131],a[192:195],v[64:67],v[128:131]\nbuffer_load_dwordx4 v22,s[40:43],s48 offen offset:0 sc1 lds\nM v[160:163],a[192:195],v[72:75],v[160:163]\n" \
"v_exp_f32 v96,v96\nM v[192:195],a[192:195],v[80:83],v[192:195]\nv_exp_f32 v97,v97\nM v[224:227],a[192:195],v[88:91],v[224:227]\nv_exp_f32 v98,v98\nM v[132:135],a[196:199],v[64:67],v[132:135]\nbuffer_load_dwordx4 v22,s[40:43],s48 offen offset:1024 sc1 lds\nM v[164:167],a[196:199],v[72:75],v[164:167]\n" \
"v_exp_f32 v99,v99\nM v[196:199],a[196:199],v[80:83],v[196:199]\ns_waitcnt vmcnt(14) lgkmcnt(0)\nv_exp_f32 v100,v100\nM v[228:231],a[196:199],v[88:91],v[228:231]\ns_barrier\nds_read_b128 a[192:195],v13 offset:32768\nM v[136:139],a[200:203],v[64:67],v[136:139]\n" \
"buffer_load_dwordx4 v22,s[40:43],s48 offen offset:2048 sc1 lds\nM v[168:171],a[200:203],v[72:75],v[168:171]\nv_exp_f32 v101,v101\nM v[200:203],a[200:203],v[80:83],v[200:203]\nv_exp_f32 v102,v102\nM v[232:235],a[200:203],v[88:91],v[232:235]\nv_exp_f32 v103,v103\nM v[140:143],a[204:207],v[64:67],v[140:143]\n" \
"buffer_load_dwordx4 v22,s[40:43],s48 offen offset:3072 sc1 lds\nM v[172:175],a[204:207],v[72:75],v[172:175]\ns_add_u32 m0,s53,49152\nv_exp_f32 v104,v104\nM v[204:207],a[204:207],v[80:83],v[204:207]\ns_xor_b32 s53,s53,0x10000\nv_exp_f32 v105,v105\nM v[236:239],a[204:207],v[88:91],v[236:239]\n" \
"s_add_u32 s48,s48,s49\nv_exp_f32 v106,v106\nM v[144:147],a[208:211],v[64:67],v[144:147]\nbuffer_load_dwordx4 v22,s[40:43],s45 offen offset:0 sc1 lds\nM v[176:179],a[208:211],v[72:75],v[176:179]\nds_read_b128 a[196:199],v13 offset:34816\nC v96,v96,v97\nM v[208:211],a[208:211],v[80:83],v[208:211]\n" \
"v_exp_f32 v107,v107\nM v[240:243],a[208:211],v[88:91],v[240:243]\nv_exp_f32 v108,v108\nM v[148:151],a[212:215],v[64:67],v[148:151]\nbuffer_load_dwordx4 v22,s[40:43],s45 offen offset:1024 sc1 lds\nM v[180:183],a[212:215],v[72:75],v[180:183]\nv_exp_f32 v109,v109\nM v[212:215],a[212:215],v[80:83],v[212:215]\n" \
"ds_read_b128 a[200:203],v13 offset:36864\nC v97,v98,v99\nM v[244:247],a[212:215],v[88:91],v[244:247]\nv_exp_f32 v110,v110\nM v[152:155],a[216:219],v[64:67],v[152:155]\nbuffer_load_dwordx4 v22,s[40:43],s45 offen offset:2048 sc1 lds\nM v[184:187],a[216:219],v[72:75],v[184:187]\nv_exp_f32 v111,v111\n" \
"M v[216:219],a[216:219],v[80:83],v[216:219]\nds_read_b128 a[204:207],v13 offset:38912\nC v98,v100,v101\nM v[248:251],a[216:219],v[88:91],v[248:251]\nv_exp_f32 v112,v112\nM v[156:159],a[220:223],v[64:67],v[156:159]\ns_waitcnt lgkmcnt(1)\nv_exp_f32 v113,v113\n" \
"M v[188:191],a[220:223],v[72:75],v[188:191]\nbuffer_load_dwordx4 v22,s[40:43],s45 offen offset:3072 sc1 lds\nM v[220:223],a[220:223],v[80:83],v[220:223]\ns_add_u32 m0,s53,0\nC v99,v102,v103\nC v104,v104,v105\nM v[252:255],a[220:223],v[88:91],v[252:255]\ns_add_u32 s45,s45,s49\n" \
"v_exp_f32 v114,v114\nM v[64:67],a[192:195],a[128:131],v[32:35]\nds_read_b128 a[208:211],v13 offset:33792\nC v105,v106,v107\nM v[72:75],a[192:195],a[144:147],v[36:39]\nbuffer_load_dwordx4 v22,s[40:43],s48 offen offset:0 sc1 lds\nM v[24:27],a[224:227],v[96:99],v[16:19]\nv_exp_f32 v115,v115\n" \
"M v[80:83],a[192:195],a[160:163],v[40:43]\nds_read_b128 a[212:215],v13 offset:35840\nC v106,v108,v109\nM v[88:91],a[192:195],a[176:179],v[44:47]\nC v107,v110,v111\nC v112,v112,v113\nM v[64:67],a[196:199],a[132:135],v[64:67]\nv_exp_f32 v116,v116\n" \
"M v[72:75],a[196:199],a[148:151],v[72:75]\nbuffer_load_dwordx4 v22,s[40:43],s48 offen offset:1024 sc1 lds\nM v[24:27],a[228:231],v[104:107],v[24:27]\nv_exp_f32 v117,v117\nM v[80:83],a[196:199],a[164:167],v[80:83]\nv_exp_f32 v118,v118\nM v[88:91],a[196:199],a[180:183],v[88:91]\nds_read_b128 a[216:219],v13 offset:37888\n" \
"C v113,v114,v115\nM v[64:67],a[200:203],a[136:139],v[64:67]\nbuffer_load_dwordx4 v22,s[40:43],s48 offen offset:2048 sc1 lds\nM v[72:75],a[200:203],a[152:155],v[72:75]\nv_exp_f32 v119,v119\nM v[80:83],a[200:203],a[168:171],v[80:83]\nv_exp_f32 v120,v120\nM v[88:91],a[200:203],a[184:187],v[88:91]\n" \
"s_waitcnt lgkmcnt(1)\nds_read_b128 a[220:223],v13 offset:39936\nC v114,v116,v117\nM v[64:67],a[204:207],a[140:143],v[64:67]\nv_exp_f32 v121,v121\nM v[72:75],a[204:207],a[156:159],v[72:75]\nbuffer_load_dwordx4 v22,s[40:43],s48 offen offset:3072 sc1 lds\nM v[80:83],a[204:207],a[172:175],v[80:83]\n" \
"s_add_u32 m0,s53,32768\nC v115,v118,v119\nC v120,v120,v121\nM v[88:91],a[204:207],a[188:191],v[88:91]\ns_add_u32 s48,s48,s49\nds_read_b128 a[192:195],v14 offset:16384\nds_read_b128 a[196:199],v14 offset:17408\nM v[24:27],a[232:235],v[112:115],v[24:27]\n" \
"v_exp_f32 v122,v122\nM v[68:71],a[208:211],a[128:131],v[32:35]\nv_exp_f32 v123,v123\nM v[76:79],a[208:211],a[144:147],v[36:39]\nv_exp_f32 v124,v124\nM v[84:87],a[208:211],a[160:163],v[40:43]\nv_exp_f32 v125,v125\nM v[92:95],a[208:211],a[176:179],v[44:47]\n" \
"v_exp_f32 v126,v126\nM v[68:71],a[212:215],a[132:135],v[68:71]\nv_exp_f32 v127,v127\nM v[76:79],a[212:215],a[148:151],v[76:79]\nC v121,v122,v123\nC v122,v124,v125\nM v[84:87],a[212:215],a[164:167],v[84:87]\nds_read_b128 a[200:203],v14 offset:18432\n" \
"C v123,v126,v127\nM v[92:95],a[212:215],a[180:183],v[92:95]\nds_read_b128 a[204:207],v14 offset:19456\nds_read_b128 a[208:211],v14 offset:20480\nM v[24:27],a[236:239],v[120:123],v[24:27]\ns_waitcnt lgkmcnt(3)\nds_read_b128 a[212:215],v14 offset:21504\nM v[68:71],a[216:219],a[136:139],v[68:71]\n" \
"s_mov_b64 exec,0xff\nglobal_load_dword v0,v2,s[100:101]\ns_mov_b64 exec,-1\nM v[76:79],a[216:219],a[152:155],v[76:79]\nv_max3_f32 v15,v24,v25,v26\nv_max_f32 v15,v15,v27\nM v[84:87],a[216:219],a[168:171],v[84:87]\nv_cmp_nge_f32 vcc,0x53800000,v15\n" \
"M v[92:95],a[216:219],a[184:187],v[92:95]\nds_read_b128 a[216:219],v14 offset:22528\nM v[68:71],a[220:223],a[140:143],v[68:71]\nM v[76:79],a[220:223],a[156:159],v[76:79]\ns_cbranch_vccnz Lcs9_%=\nLcr10_%=:\nv_exp_f32 v64,v64\nM v[84:87],a[220:223],a[172:175],v[84:87]\n" \
"v_exp_f32 v65,v65\nM v[92:95],a[220:223],a[188:191],v[92:95]\nds_read_b128 a[220:223],v14 offset:23552\nC v64,v64,v65\nM v[128:131],a[192:195],v[96:99],v[128:131]\nv_exp_f32 v66,v66\nM v[160:163],a[192:195],v[104:107],v[160:163]\nv_exp_f32 v67,v67\n" \
"M v[192:195],a[192:195],v[112:115],v[192:195]\nbuffer_load_dword v58,v57,s[84:87],s74 offen\nM v[224:227],a[192:195],v[120:123],v[224:227]\ns_add_u32 s74,s74,2048\nv_exp_f32 v68,v68\nM v[132:135],a[196:199],v[96:99],v[132:135]\nv_exp_f32 v69,v69\nM v[164:167],a[196:199],v[104:107],v[164:167]\n" \
"v_exp_f32 v70,v70\nM v[196:199],a[196:199],v[112:115],v[196:199]\ns_waitcnt vmcnt(18) lgkmcnt(0)\nv_exp_f32 v71,v71\nM v[228:231],a[196:199],v[120:123],v[228:231]\ns_barrier\nds_read_b128 a[192:195],v13 offset:49152\nM v[136:139],a[200:203],v[96:99],v[136:139]\n" \
"buffer_load_dwordx4 v22,s[40:43],s45 offen offset:0 sc1 lds\nM v[168:171],a[200:203],v[104:107],v[168:171]\nv_exp_f32 v72,v72\nM v[200:203],a[200:203],v[112:115],v[200:203]\nv_exp_f32 v73,v73\nM v[232:235],a[200:203],v[120:123],v[232:235]\nv_exp_f32 v74,v74\nM v[140:143],a[204:207],v[96:99],v[140:143]\n" \
"buffer_load_dwordx4 v22,s[40:43],s45 offen offset:1024 sc1 lds\nM v[172:175],a[204:207],v[104:107],v[172:175]\nv_exp_f32 v75,v75\nM v[204:207],a[204:207],v[112:115],v[204:207]\nv_exp_f32 v76,v76\nM v[236:239],a[204:207],v[120:123],v[236:239]\nv_exp_f32 v77,v77\nM v[144:147],a[208:211],v[96:99],v[144:147]\n" \
"buffer_load_dwordx4 v22,s[40:43],s45 offen offset:2048 sc1 lds\nM v[176:179],a[208:211],v[104:107],v[176:179]\nds_read_b128 a[196:199],v13 offset:51200\nC v65,v66,v67\nM v[208:211],a[208:211],v[112:115],v[208:211]\nv_exp_f32 v78,v78\nM v[240:243],a[208:211],v[120:123],v[240:243]\nv_exp_f32 v79,v79\n" \
"M v[148:151],a[212:215],v[96:99],v[148:151]\nbuffer_load_dwordx4 v22,s[40:43],s45 offen offset:3072 sc1 lds\nM v[180:183],a[212:215],v[104:107],v[180:183]\ns_add_u32 m0,s53,16384\nv_exp_f32 v80,v80\nM v[212:215],a[212:215],v[112:115],v[212:215]\ns_add_u32 s52,s52,1\nds_read_b128 a[200:203],v13 offset:53248\n" \
"C v66,v68,v69\nM v[244:247],a[212:215],v[120:123],v[244:247]\ns_add_u32 s45,s45,s49\nv_exp_f32 v81,v81\nM v[152:155],a[216:219],v[96:99],v[152:155]\nbuffer_load_dwordx4 v22,s[40:43],s48 offen offset:0 sc1 lds\nM v[184:187],a[216:219],v[104:107],v[184:187]\nC v67,v70,v71\n" \
"C v72,v72,v73\nM v[216:219],a[216:219],v[112:115],v[216:219]\nds_read_b128 a[204:207],v13 offset:55296\nC v73,v74,v75\nM v[248:251],a[216:219],v[120:123],v[248:251]\nv_exp_f32 v82,v82\nM v[156:159],a[220:223],v[96:99],v[156:159]\nbuffer_load_dwordx4 v22,s[40:43],s48 offen offset:1024 sc1 lds\n" \
"M v[188:191],a[220:223],v[104:107],v[188:191]\ns_waitcnt lgkmcnt(1)\nv_exp_f32 v83,v83\nM v[220:223],a[220:223],v[112:115],v[220:223]\ns_cmp_eq_u32 s52,s56\nC v74,v76,v77\nC v75,v78,v79\nM v[252:255],a[220:223],v[120:123],v[252:255]\n" \
"s_cbranch_scc1 LEV_%=\nLEVR1_%=:\nv_exp_f32 v84,v84\nM v[96:99],a[192:195],a[128:131],v[32:35]\nbuffer_load_dwordx4 v22,s[40:43],s48 offen offset:2048 sc1 lds\nM v[104:107],a[192:195],a[144:147],v[36:39]\nds_read_b128 a[208:211],v13 offset:50176\nds_read_b128 a[212:215],v13 offset:52224\n" \
"M v[16:19],a[224:227],v[64:67],v[24:27]\nv_exp_f32 v85,v85\nM v[112:115],a[192:195],a[160:163],v[40:43]\nv_exp_f32 v86,v86\nM v[120:123],a[192:195],a[176:179],v[44:47]\nbuffer_load_dwordx4 v22,s[40:43],s48 offen offset:3072 sc1 lds\nM v[96:99],a[196:199],a[132:135],v[96:99]\ns_add_u32 m0,s53,49152\n" \
"v_exp_f32 v87,v87\nM v[104:107],a[196:199],a[148:151],v[104:107]\ns_xor_b32 s53,s53,0x10000\nv_exp_f32 v88,v88\nM v[16:19],a[228:231],v[72:75],v[16:19]\ns_add_u32 s48,s48,s49\nv_exp_f32 v89,v89\nM v[112:115],a[196:199],a[164:167],v[112:115]\n" \
"C v80,v80,v81\nC v81,v82,v83\nM v[120:123],a[196:199],a[180:183],v[120:123]\nds_read_b128 a[216:219],v13 offset:54272\nC v82,v84,v85\nM v[96:99],a[200:203],a[136:139],v[96:99]\nv_exp_f32 v90,v90\nM v[104:107],a[200:203],a[152:155],v[104:107]\n" \
"ds_read_b128 a[220:223],v13 offset:56320\nC v83,v86,v87\nM v[112:115],a[200:203],a[168:171],v[112:115]\nv_exp_f32 v91,v91\nM v[120:123],a[200:203],a[184:187],v[120:123]\ns_waitcnt lgkmcnt(2)\nv_exp_f32 v92,v92\nM v[96:99],a[204:207],a[140:143],v[96:99]\n" \
"v_exp_f32 v93,v93\nM v[104:107],a[204:207],a[156:159],v[104:107]\nv_exp_f32 v94,v94\nM v[112:115],a[204:207],a[172:175],v[112:115]\nC v88,v88,v89\nC v89,v90,v91\nM v[120:123],a[204:207],a[188:191],v[120:123]\nv_exp_f32 v95,v95\n" \
"M v[16:19],a[232:235],v[80:83],v[16:19]\nC v90,v92,v93\nv_xor_b32 v14,0x10000,v14\nM v[100:103],a[208:211],a[128:131],v[32:35]\nds_read_b128 a[192:195],v14 offset:0\nC v91,v94,v95\nM v[108:111],a[208:211],a[144:147],v[36:39]\nds_read_b128 a[196:199],v14 offset:1024\n" \
"ds_read_b128 a[200:203],v14 offset:2048\nM v[116:119],a[208:211],a[160:163],v[40:43]\nds_read_b128 a[204:207],v14 offset:3072\nM v[124:127],a[208:211],a[176:179],v[44:47]\nds_read_b128 a[208:211],v14 offset:4096\nM v[100:103],a[212:215],a[132:135],v[100:103]\nM v[108:111],a[212:215],a[148:151],v[108:111]\nM v[116:119],a[212:215],a[164:167],v[116:119]\n" \
"M v[124:127],a[212:215],a[180:183],v[124:127]\nds_read_b128 a[212:215],v14 offset:5120\nM v[16:19],a[236:239],v[88:91],v[16:19]\ns_waitcnt lgkmcnt(1)\nM v[100:103],a[216:219],a[136:139],v[100:103]\nM v[108:111],a[216:219],a[152:155],v[108:111]\nv_max3_f32 v15,v16,v17,v18\nv_max_f32 v15,v15,v19\n" \
"M v[116:119],a[216:219],a[168:171],v[116:119]\nv_cmp_nge_f32 vcc,0x53800000,v15\nM v[124:127],a[216:219],a[184:187],v[124:127]\nds_read_b128 a[216:219],v14 offset:6144\nM v[100:103],a[220:223],a[140:143],v[100:103]\nM v[108:111],a[220:223],a[156:159],v[108:111]\ns_cbranch_vccnz Lcs11_%=\nLcr12_%=:\n" \
"M v[116:119],a[220:223],a[172:175],v[116:119]\ns_cmp_eq_u32 s52,s55\nM v[124:127],a[220:223],a[188:191],v[124:127]\ns_cbranch_scc1 LH1_%=\nLH1R2_%=:\nds_read_b128 a[220:223],v14 offset:7168\nv_xor_b32 v13,0x10000,v13\nM v[128:131],a[192:195],v[64:67],v[128:131]\n" \
"v_exp_f32 v96,v96\nM v[160:163],a[192:195],v[72:75],v[160:163]\nv_exp_f32 v97,v97\nM v[192:195],a[192:195],v[80:83],v[192:195]\nv_exp_f32 v98,v98\nM v[224:227],a[192:195],v[88:91],v[224:227]\nv_exp_f32 v99,v99\nM v[132:135],a[196:199],v[64:67],v[132:135]\n" \
"v_exp_f32 v100,v100\nM v[164:167],a[196:199],v[72:75],v[164:167]\nv_exp_f32 v101,v101\nM v[196:199],a[196:199],v[80:83],v[196:199]\ns_waitcnt vmcnt(18) lgkmcnt(0)\nv_exp_f32 v102,v102\nM v[228:231],a[196:199],v[88:91],v[228:231]\ns_barrier\n" \
"ds_read_b128 a[192:195],v13 offset:32768\nM v[136:139],a[200:203],v[64:67],v[136:139]\nbuffer_load_dwordx4 v22,s[40:43],s45 offen offset:0 sc1 lds\nM v[168:171],a[200:203],v[72:75],v[168:171]\nv_exp_f32 v103,v103\nM v[200:203],a[200:203],v[80:83],v[200:203]\nv_exp_f32 v104,v104\nM v[232:235],a[200:203],v[88:91],v[232:235]\n" \
"v_exp_f32 v105,v105\nM v[140:143],a[204:207],v[64:67],v[140:143]\nbuffer_load_dwordx4 v22,s[40:43],s45 offen offset:1024 sc1 lds\nM v[172:175],a[204:207],v[72:75],v[172:175]\nv_exp_f32 v106,v106\nM v[204:207],a[204:207],v[80:83],v[204:207]\nv_exp_f32 v107,v107\nM v[236:239],a[204:207],v[88:91],v[236:239]\n" \
"v_exp_f32 v108,v108\nM v[144:147],a[208:211],v[64:67],v[144:147]\nbuffer_load_dwordx4 v22,s[40:43],s45 offen offset:2048 sc1 lds\nM v[176:179],a[208:211],v[72:75],v[176:179]\nds_read_b128 a[196:199],v13 offset:34816\nC v96,v96,v97\nM v[208:211],a[208:211],v[80:83],v[208:211]\nv_exp_f32 v109,v109\n" \
"M v[240:243],a[208:211],v[88:91],v[240:243]\nv_exp_f32 v110,v110\nM v[148:151],a[212:215],v[64:67],v[148:151]\nbuffer_load_dwordx4 v22,s[40:43],s45 offen offset:3072 sc1 lds\nM v[180:183],a[212:215],v[72:75],v[180:183]\ns_add_u32 m0,s53,0\nv_exp_f32 v111,v111\nM v[212:215],a[212:215],v[80:83],v[212:215]\n" \
"s_add_u32 s45,s45,s49\nds_read_b128 a[200:203],v13 offset:36864\nC v97,v98,v99\nM v[244:247],a[212:215],v[88:91],v[244:247]\nv_exp_f32 v112,v112\nM v[152:155],a[216:219],v[64:67],v[152:155]\nbuffer_load_dwordx4 v22,s[40:43],s48 offen offset:0 sc1 lds\nM v[184:187],a[216:219],v[72:75],v[184:187]\n" \
"LG16_%=:\ns_cbranch_execz LHR1_%=\nC v98,v100,v101\nC v99,v102,v103\nM v[216:219],a[216:219],v[80:83],v[216:219]\nds_read_b128 a[204:207],v13 offset:38912\nC v104,v104,v105\nM v[248:251],a[216:219],v[88:91],v[248:251]\n" \
"v_exp_f32 v113,v113\nM v[156:159],a[220:223],v[64:67],v[156:159]\nbuffer_load_dwordx4 v22,s[40:43],s48 offen offset:1024 sc1 lds\nM v[188:191],a[220:223],v[72:75],v[188:191]\nv_exp_f32 v114,v114\nM v[220:223],a[220:223],v[80:83],v[220:223]\nC v105,v106,v107\nC v106,v108,v109\n" \
"M v[252:255],a[220:223],v[88:91],v[252:255]\ns_waitcnt lgkmcnt(0)\nv_exp_f32 v115,v115\nM v[64:67],a[192:195],a[128:131],v[32:35]\nbuffer_load_dwordx4 v22,s[40:43],s48 offen offset:2048 sc1 lds\nM v[72:75],a[192:195],a[144:147],v[36:39]\nds_read_b128 a[208:211],v13 offset:33792\nC v107,v110,v111\n" \
"M v[24:27],a[224:227],v[96:99],v[16:19]\nv_exp_f32 v116,v116\nM v[80:83],a[192:195],a[160:163],v[40:43]\nds_read_b128 a[212:215],v13 offset:35840\nC v112,v112,v113\nM v[88:91],a[192:195],a[176:179],v[44:47]\nbuffer_load_dwordx4 v22,s[40:43],s48 offen offset:3072 sc1 lds\nM v[64:67],a[196:199],a[132:135],v[64:67]\n" \
"s_add_u32 m0,s53,32768\nv_exp_f32 v117,v117\nM v[72:75],a[196:199],a[148:151],v[72:75]\ns_add_u32 s48,s48,s49\nv_exp_f32 v118,v118\nM v[24:27],a[228:231],v[104:107],v[24:27]\nv_exp_f32 v119,v119\nM v[80:83],a[196:199],a[164:167],v[80:83]\n" \
"v_exp_f32 v120,v120\nM v[88:91],a[196:199],a[180:183],v[88:91]\nds_read_b128 a[216:219],v13 offset:37888\nC v113,v114,v115\nM v[64:67],a[200:203],a[136:139],v[64:67]\nv_exp_f32 v121,v121\nM v[72:75],a[200:203],a[152:155],v[72:75]\nv_exp_f32 v122,v122\n" \
"M v[80:83],a[200:203],a[168:171],v[80:83]\nds_read_b128 a[220:223],v13 offset:39936\nC v114,v116,v117\nM v[88:91],a[200:203],a[184:187],v[88:91]\nC v115,v118,v119\nC v120,v120,v121\nM v[64:67],a[204:207],a[140:143],v[64:67]\nv_exp_f32 v123,v123\n" \
"M v[72:75],a[204:207],a[156:159],v[72:75]\nv_exp_f32 v124,v124\nM v[80:83],a[204:207],a[172:175],v[80:83]\nv_exp_f32 v125,v125\nM v[88:91],a[204:207],a[188:191],v[88:91]\nds_read_b128 a[192:195],v14 offset:16384\nC v121,v122,v123\nM v[24:27],a[232:235],v[112:115],v[24:27]\n" \
"s_waitcnt lgkmcnt(1)\nv_exp_f32 v126,v126\nM v[68:71],a[208:211],a[128:131],v[32:35]\nv_exp_f32 v127,v127\nM v[76:79],a[208:211],a[144:147],v[36:39]\nC v122,v124,v125\nC v123,v126,v127\nM v[84:87],a[208:211],a[160:163],v[40:43]\n" \
"ds_read_b128 a[196:199],v14 offset:17408\nds_read_b128 a[200:203],v14 offset:18432\nM v[92:95],a[208:211],a[176:179],v[44:47]\nds_read_b128 a[204:207],v14 offset:19456\nM v[68:71],a[212:215],a[132:135],v[68:71]\nds_read_b128 a[208:211],v14 offset:20480\nM v[76:79],a[212:215],a[148:151],v[76:79]\nM v[84:87],a[212:215],a[164:167],v[84:87]\n" \
"M v[92:95],a[212:215],a[180:183],v[92:95]\nds_read_b128 a[212:215],v14 offset:21504\nM v[24:27],a[236:239],v[120:123],v[24:27]\nM v[68:71],a[216:219],a[136:139],v[68:71]\ns_mov_b64 exec,0xff\nglobal_load_dword v0,v2,s[100:101]\ns_mov_b64 exec,-1\nM v[76:79],a[216:219],a[152:155],v[76:79]\n" \
"v_max3_f32 v15,v24,v25,v26\nv_max_f32 v15,v15,v27\nM v[84:87],a[216:219],a[168:171],v[84:87]\nv_cmp_nge_f32 vcc,0x53800000,v15\nM v[92:95],a[216:219],a[184:187],v[92:95]\nds_read_b128 a[216:219],v14 offset:22528\nM v[68:71],a[220:223],a[140:143],v[68:71]\nM v[76:79],a[220:223],a[156:159],v[76:79]\n" \
"s_cbranch_vccnz Lcs13_%=\nLcr14_%=:\nv_exp_f32 v64,v64\nM v[84:87],a[220:223],a[172:175],v[84:87]\nv_exp_f32 v65,v65\nM v[92:95],a[220:223],a[188:191],v[92:95]\ns_waitcnt lgkmcnt(1)\nds_read_b128 a[220:223],v14 offset:23552\n" \
"C v64,v64,v65\nM v[128:131],a[192:195],v[96:99],v[128:131]\nv_exp_f32 v66,v66\nM v[160:163],a[192:195],v[104:107],v[160:163]\nv_exp_f32 v67,v67\nM v[192:195],a[192:195],v[112:115],v[192:195]\nbuffer_load_dword v58,v57,s[84:87],s74 offen\nM v[224:227],a[192:195],v[120:123],v[224:227]\n" \
"s_add_u32 s74,s74,2048\nv_exp_f32 v68,v68\nM v[132:135],a[196:199],v[96:99],v[132:135]\nv_exp_f32 v69,v69\nM v[164:167],a[196:199],v[104:107],v[164:167]\nv_exp_f32 v70,v70\nM v[196:199],a[196:199],v[112:115],v[196:199]\ns_waitcnt vmcnt(20) lgkmcnt(0)\n" \
"v_exp_f32 v71,v71\nM v[228:231],a[196:199],v[120:123],v[228:231]\ns_barrier\nds_read_b128 a[192:195],v13 offset:49152\nM v[136:139],a[200:203],v[96:99],v[136:139]\nbuffer_load_dwordx4 v22,s[40:43],s45 offen offset:0 sc1 lds\nM v[168:171],a[200:203],v[104:107],v[168:171]\nv_exp_f32 v72,v72\n" \
"M v[200:203],a[200:203],v[112:115],v[200:203]\nv_exp_f32 v73,v73\nM v[232:235],a[200:203],v[120:123],v[232:235]\nv_exp_f32 v74,v74\nM v[140:143],a[204:207],v[96:99],v[140:143]\nbuffer_load_dwordx4 v22,s[40:43],s45 offen offset:1024 sc1 lds\nM v[172:175],a[204:207],v[104:107],v[172:175]\nv_exp_f32 v75,v75\n" \
"M v[204:207],a[204:207],v[112:115],v[204:207]\nv_exp_f32 v76,v76\nM v[236:239],a[204:207],v[120:123],v[236:239]\nv_exp_f32 v77,v77\nM v[144:147],a[208:211],v[96:99],v[144:147]\nbuffer_load_dwordx4 v22,s[40:43],s45 offen offset:2048 sc1 lds\nM v[176:179],a[208:211],v[104:107],v[176:179]\nds_read_b128 a[196:199],v13 offset:51200\n" \
"C v65,v66,v67\nM v[208:211],a[208:211],v[112:115],v[208:211]\nv_exp_f32 v78,v78\nM v[240:243],a[208:211],v[120:123],v[240:243]\nv_exp_f32 v79,v79\nM v[148:151],a[212:215],v[96:99],v[148:151]\nbuffer_load_dwordx4 v22,s[40:43],s45 offen offset:3072 sc1 lds\nM v[180:183],a[212:215],v[104:107],v[180:183]\n" \
"s_add_u32 m0,s53,16384\nv_exp_f32 v80,v80\nM v[212:215],a[212:215],v[112:115],v[212:215]\ns_add_u32 s52,s52,1\nds_read_b128 a[200:203],v13 offset:53248\nC v66,v68,v69\nM v[244:247],a[212:215],v[120:123],v[244:247]\ns_add_u32 s45,s45,s49\n" \
"v_exp_f32 v81,v81\nM v[152:155],a[216:219],v[96:99],v[152:155]\nbuffer_load_dwordx4 v22,s[40:43],s48 offen offset:0 sc1 lds\nM v[184:187],a[216:219],v[104:107],v[184:187]\nC v67,v70,v71\nC v72,v72,v73\nM v[216:219],a[216:219],v[112:115],v[216:219]\nds_read_b128 a[204:207],v13 offset:55296\n" \
"C v73,v74,v75\nM v[248:251],a[216:219],v[120:123],v[248:251]\nv_exp_f32 v82,v82\nM v[156:159],a[220:223],v[96:99],v[156:159]\nbuffer_load_dwordx4 v22,s[40:43],s48 offen offset:1024 sc1 lds\nM v[188:191],a[220:223],v[104:107],v[188:191]\ns_waitcnt lgkmcnt(1)\nv_exp_f32 v83,v83\n" \
"M v[220:223],a[220:223],v[112:115],v[220:223]\ns_cmp_eq_u32 s52,s56\nC v74,v76,v77\nC v75,v78,v79\nM v[252:255],a[220:223],v[120:123],v[252:255]\ns_cbranch_scc0 LEVR1_%=\ns_branch LEV_%=\nLEVR8_%=:\n" \
"v_exp_f32 v84,v84\nM v[96:99],a[192:195],a[128:131],v[32:35]\nbuffer_load_dwordx4 v22,s[40:43],s48 offen offset:2048 sc1 lds\nM v[104:107],a[192:195],a[144:147],v[36:39]\nds_read_b128 a[208:211],v13 offset:50176\nds_read_b128 a[212:215],v13 offset:52224\nM v[16:19],a[224:227],v[64:67],v[24:27]\nv_exp_f32 v85,v85\n" \
"M v[112:115],a[192:195],a[160:163],v[40:43]\nv_exp_f32 v86,v86\nM v[120:123],a[192:195],a[176:179],v[44:47]\nbuffer_load_dwordx4 v22,s[40:43],s48 offen offset:3072 sc1 lds\nM v[96:99],a[196:199],a[132:135],v[96:99]\ns_add_u32 m0,s53,49152\nv_exp_f32 v87,v87\nM v[104:107],a[196:199],a[148:151],v[104:107]\n" \
"s_xor_b32 s53,s53,0x10000\nv_exp_f32 v88,v88\nM v[16:19],a[228:231],v[72:75],v[16:19]\ns_add_u32 s48,s48,s49\nv_exp_f32 v89,v89\nM v[112:115],a[196:199],a[164:167],v[112:115]\nC v80,v80,v81\nC v81,v82,v83\n" \
"M v[120:123],a[196:199],a[180:183],v[120:123]\nds_read_b128 a[216:219],v13 offset:54272\nC v82,v84,v85\nM v[96:99],a[200:203],a[136:139],v[96:99]\nv_exp_f32 v90,v90\nM v[104:107],a[200:203],a[152:155],v[104:107]\nds_read_b128 a[220:223],v13 offset:56320\nC v83,v86,v87\n" \
"M v[112:115],a[200:203],a[168:171],v[112:115]\nv_exp_f32 v91,v91\nM v[120:123],a[200:203],a[184:187],v[120:123]\ns_waitcnt lgkmcnt(2)\nv_exp_f32 v92,v92\nM v[96:99],a[204:207],a[140:143],v[96:99]\nv_exp_f32 v93,v93\nM v[104:107],a[204:207],a[156:159],v[104:107]\n" \
"v_exp_f32 v94,v94\nM v[112:115],a[204:207],a[172:175],v[112:115]\nC v88,v88,v89\nC v89,v90,v91\nM v[120:123],a[204:207],a[188:191],v[120:123]\nv_exp_f32 v95,v95\nM v[16:19],a[232:235],v[80:83],v[16:19]\nC v90,v92,v93\n" \
"v_xor_b32 v14,0x10000,v14\nM v[100:103],a[208:211],a[128:131],v[32:35]\nds_read_b128 a[192:195],v14 offset:0\nC v91,v94,v95\nM v[108:111],a[208:211],a[144:147],v[36:39]\nds_read_b128 a[196:199],v14 offset:1024\nds_read_b128 a[200:203],v14 offset:2048\nM v[116:119],a[208:211],a[160:163],v[40:43]\n" \
"ds_read_b128 a[204:207],v14 offset:3072\nM v[124:127],a[208:211],a[176:179],v[44:47]\nds_read_b128 a[208:211],v14 offset:4096\nM v[100:103],a[212:215],a[132:135],v[100:103]\nM v[108:111],a[212:215],a[148:151],v[108:111]\nM v[116:119],a[212:215],a[164:167],v[116:119]\nM v[124:127],a[212:215],a[180:183],v[124:127]\nds_read_b128 a[212:215],v14 offset:5120\n" \
"M v[16:19],a[236:239],v[88:91],v[16:19]\ns_waitcnt lgkmcnt(1)\nM v[100:103],a[216:219],a[136:139],v[100:103]\nM v[108:111],a[216:219],a[152:155],v[108:111]\nv_max3_f32 v15,v16,v17,v18\nv_max_f32 v15,v15,v19\nM v[116:119],a[216:219],a[168:171],v[116:119]\nv_cmp_nge_f32 vcc,0x53800000,v15\n" \
"M v[124:127],a[216:219],a[184:187],v[124:127]\nds_read_b128 a[216:219],v14 offset:6144\nM v[100:103],a[220:223],a[140:143],v[100:103]\nM v[108:111],a[220:223],a[156:159],v[108:111]\ns_cbranch_vccnz Lcs39_%=\nLcr40_%=:\nM v[116:119],a[220:223],a[172:175],v[116:119]\ns_cmp_eq_u32 s52,s55\n" \
"M v[124:127],a[220:223],a[188:191],v[124:127]\ns_cbranch_scc1 LH1_%=\nLH1R9_%=:\nds_read_b128 a[220:223],v14 offset:7168\nv_xor_b32 v13,0x10000,v13\nM v[128:131],a[192:195],v[64:67],v[128:131]\nv_exp_f32 v96,v96\nM v[160:163],a[192:195],v[72:75],v[160:163]\n" \
"v_exp_f32 v97,v97\nM v[192:195],a[192:195],v[80:83],v[192:195]\nv_exp_f32 v98,v98\nM v[224:227],a[192:195],v[88:91],v[224:227]\nv_exp_f32 v99,v99\nM v[132:135],a[196:199],v[64:67],v[132:135]\nv_exp_f32 v100,v100\nM v[164:167],a[196:199],v[72:75],v[164:167]\n" \
"v_exp_f32 v101,v101\nM v[196:199],a[196:199],v[80:83],v[196:199]\ns_waitcnt vmcnt(18) lgkmcnt(0)\nv_exp_f32 v102,v102\nM v[228:231],a[196:199],v[88:91],v[228:231]\ns_barrier\nds_read_b128 a[192:195],v13 offset:32768\nM v[136:139],a[200:203],v[64:67],v[136:139]\n" \
"buffer_load_dwordx4 v22,s[40:43],s45 offen offset:0 sc1 lds\nM v[168:171],a[200:203],v[72:75],v[168:171]\nv_exp_f32 v103,v103\nM v[200:203],a[200:203],v[80:83],v[200:203]\nv_exp_f32 v104,v104\nM v[232:235],a[200:203],v[88:91],v[232:235]\nv_exp_f32 v105,v105\nM v[140:143],a[204:207],v[64:67],v[140:143]\n" \
"buffer_load_dwordx4 v22,s[40:43],s45 offen offset:1024 sc1 lds\nM v[172:175],a[204:207],v[72:75],v[172:175]\nv_exp_f32 v106,v106\nM v[204:207],a[204:207],v[80:83],v[204:207]\nv_exp_f32 v107,v107\nM v[236:239],a[204:207],v[88:91],v[236:239]\nv_exp_f32 v108,v108\nM v[144:147],a[208:211],v[64:67],v[144:147]\n" \
"buffer_load_dwordx4 v22,s[40:43],s45 offen offset:2048 sc1 lds\nM v[176:179],a[208:211],v[72:75],v[176:179]\nds_read_b128 a[196:199],v13 offset:34816\nC v96,v96,v97\nM v[208:211],a[208:211],v[80:83],v[208:211]\nv_exp_f32 v109,v109\nM v[240:243],a[208:211],v[88:91],v[240:243]\nv_exp_f32 v110,v110\n" \
"M v[148:151],a[212:215],v[64:67],v[148:151]\nbuffer_load_dwordx4 v22,s[40:43],s45 offen offset:3072 sc1 lds\nM v[180:183],a[212:215],v[72:75],v[180:183]\ns_add_u32 m0,s53,0\nv_exp_f32 v111,v111\nM v[212:215],a[212:215],v[80:83],v[212:215]\ns_add_u32 s45,s45,s49\nds_read_b128 a[200:203],v13 offset:36864\n" \
"C v97,v98,v99\nM v[244:247],a[212:215],v[88:91],v[244:247]\nv_exp_f32 v112,v112\nM v[152:155],a[216:219],v[64:67],v[152:155]\nbuffer_load_dwordx4 v22,s[40:43],s48 offen offset:0 sc1 lds\nM v[184:187],a[216:219],v[72:75],v[184:187]\nC v98,v100,v101\nC v99,v102,v103\n" \
"M v[216:219],a[216:219],v[80:83],v[216:219]\nds_read_b128 a[204:207],v13 offset:38912\nC v104,v104,v105\nM v[248:251],a[216:219],v[88:91],v[248:251]\nv_exp_f32 v113,v113\nM v[156:159],a[220:223],v[64:67],v[156:159]\nbuffer_load_dwordx4 v22,s[40:43],s48 offen offset:1024 sc1 lds\nM v[188:191],a[220:223],v[72:75],v[188:191]\n" \
"v_exp_f32 v114,v114\nM v[220:223],a[220:223],v[80:83],v[220:223]\nC v105,v106,v107\nC v106,v108,v109\nM v[252:255],a[220:223],v[88:91],v[252:255]\ns_waitcnt lgkmcnt(0)\nv_exp_f32 v115,v115\nM v[64:67],a[192:195],a[128:131],v[32:35]\n" \
"buffer_load_dwordx4 v22,s[40:43],s48 offen offset:2048 sc1 lds\nM v[72:75],a[192:195],a[144:147],v[36:39]\nds_read_b128 a[208:211],v13 offset:33792\nC v107,v110,v111\nM v[24:27],a[224:227],v[96:99],v[16:19]\nv_exp_f32 v116,v116\nM v[80:83],a[192:195],a[160:163],v[40:43]\nds_read_b128 a[212:215],v13 offset:35840\n" \
"C v112,v112,v113\nM v[88:91],a[192:195],a[176:179],v[44:47]\nbuffer_load_dwordx4 v22,s[40:43],s48 offen offset:3072 sc1 lds\nM v[64:67],a[196:199],a[132:135],v[64:67]\ns_add_u32 m0,s53,32768\nv_exp_f32 v117,v117\nM v[72:75],a[196:199],a[148:151],v[72:75]\ns_add_u32 s48,s48,s49\n" \
"v_exp_f32 v118,v118\nM v[24:27],a[228:231],v[104:107],v[24:27]\nv_exp_f32 v119,v119\nM v[80:83],a[196:199],a[164:167],v[80:83]\nv_exp_f32 v120,v120\nM v[88:91],a[196:199],a[180:183],v[88:91]\nds_read_b128 a[216:219],v13 offset:37888\nC v113,v114,v115\n" \
"M v[64:67],a[200:203],a[136:139],v[64:67]\nv_exp_f32 v121,v121\nM v[72:75],a[200:203],a[152:155],v[72:75]\nv_exp_f32 v122,v122\nM v[80:83],a[200:203],a[168:171],v[80:83]\nC v114,v116,v117\nC v115,v118,v119\nM v[88:91],a[200:203],a[184:187],v[88:91]\n" \
"ds_read_b128 a[220:223],v13 offset:39936\nC v120,v120,v121\nM v[64:67],a[204:207],a[140:143],v[64:67]\nv_exp_f32 v123,v123\nM v[72:75],a[204:207],a[156:159],v[72:75]\nv_exp_f32 v124,v124\nM v[80:83],a[204:207],a[172:175],v[80:83]\nv_exp_f32 v125,v125\n" \
"M v[88:91],a[204:207],a[188:191],v[88:91]\nds_read_b128 a[192:195],v14 offset:16384\nC v121,v122,v123\nM v[24:27],a[232:235],v[112:115],v[24:27]\ns_waitcnt lgkmcnt(1)\nv_exp_f32 v126,v126\nM v[68:71],a[208:211],a[128:131],v[32:35]\nv_exp_f32 v127,v127\n" \
"M v[76:79],a[208:211],a[144:147],v[36:39]\nC v122,v124,v125\nC v123,v126,v127\nM v[84:87],a[208:211],a[160:163],v[40:43]\nds_read_b128 a[196:199],v14 offset:17408\nds_read_b128 a[200:203],v14 offset:18432\nM v[92:95],a[208:211],a[176:179],v[44:47]\nds_read_b128 a[204:207],v14 offset:19456\n" \
"M v[68:71],a[212:215],a[132:135],v[68:71]\nds_read_b128 a[208:211],v14 offset:20480\nM v[76:79],a[212:215],a[148:151],v[76:79]\nM v[84:87],a[212:215],a[164:167],v[84:87]\nM v[92:95],a[212:215],a[180:183],v[92:95]\nds_read_b128 a[212:215],v14 offset:21504\nM v[24:27],a[236:239],v[120:123],v[24:27]\nM v[68:71],a[216:219],a[136:139],v[68:71]\n" \
"s_mov_b64 exec,0xff\nglobal_load_dword v0,v2,s[100:101]\ns_mov_b64 exec,-1\nM v[76:79],a[216:219],a[152:155],v[76:79]\nv_max3_f32 v15,v24,v25,v26\nv_max_f32 v15,v15,v27\nM v[84:87],a[216:219],a[168:171],v[84:87]\nv_cmp_nge_f32 vcc,0x53800000,v15\n" \
"M v[92:95],a[216:219],a[184:187],v[92:95]\nds_read_b128 a[216:219],v14 offset:22528\nM v[68:71],a[220:223],a[140:143],v[68:71]\nM v[76:79],a[220:223],a[156:159],v[76:79]\ns_cbranch_vccnz Lcs41_%=\nLcr42_%=:\nv_exp_f32 v64,v64\nM v[84:87],a[220:223],a[172:175],v[84:87]\n" \
"v_exp_f32 v65,v65\nM v[92:95],a[220:223],a[188:191],v[92:95]\ns_waitcnt lgkmcnt(1)\nds_read_b128 a[220:223],v14 offset:23552\nC v64,v64,v65\nM v[128:131],a[192:195],v[96:99],v[128:131]\nv_exp_f32 v66,v66\nM v[160:163],a[192:195],v[104:107],v[160:163]\n" \
"v_exp_f32 v67,v67\nM v[192:195],a[192:195],v[112:115],v[192:195]\nbuffer_load_dword v58,v57,s[84:87],s74 offen\nM v[224:227],a[192:195],v[120:123],v[224:227]\ns_add_u32 s74,s74,2048\nv_exp_f32 v68,v68\nM v[132:135],a[196:199],v[96:99],v[132:135]\nv_exp_f32 v69,v69\n" \
"M v[164:167],a[196:199],v[104:107],v[164:167]\nv_exp_f32 v70,v70\nM v[196:199],a[196:199],v[112:115],v[196:199]\ns_waitcnt vmcnt(20) lgkmcnt(0)\nv_exp_f32 v71,v71\nM v[228:231],a[196:199],v[120:123],v[228:231]\ns_barrier\nds_read_b128 a[192:195],v13 offset:49152\n" \
"M v[136:139],a[200:203],v[96:99],v[136:139]\nbuffer_load_dwordx4 v22,s[40:43],s45 offen offset:0 sc1 lds\nM v[168:171],a[200:203],v[104:107],v[168:171]\nv_exp_f32 v72,v72\nM v[200:203],a[200:203],v[112:115],v[200:203]\nv_exp_f32 v73,v73\nM v[232:235],a[200:203],v[120:123],v[232:235]\nv_exp_f32 v74,v74\n" \
"M v[140:143],a[204:207],v[96:99],v[140:143]\nbuffer_load_dwordx4 v22,s[40:43],s45 offen offset:1024 sc1 lds\nM v[172:175],a[204:207],v[104:107],v[172:175]\nv_exp_f32 v75,v75\nM v[204:207],a[204:207],v[112:115],v[204:207]\nv_exp_f32 v76,v76\nM v[236:239],a[204:207],v[120:123],v[236:239]\nv_exp_f32 v77,v77\n" \
"M v[144:147],a[208:211],v[96:99],v[144:147]\nbuffer_load_dwordx4 v22,s[40:43],s45 offen offset:2048 sc1 lds\nM v[176:179],a[208:211],v[104:107],v[176:179]\nds_read_b128 a[196:199],v13 offset:51200\nC v65,v66,v67\nM v[208:211],a[208:211],v[112:115],v[208:211]\nv_exp_f32 v78,v78\nM v[240:243],a[208:211],v[120:123],v[240:243]\n" \
"v_exp_f32 v79,v79\nM v[148:151],a[212:215],v[96:99],v[148:151]\nbuffer_load_dwordx4 v22,s[40:43],s45 offen offset:3072 sc1 lds\nM v[180:183],a[212:215],v[104:107],v[180:183]\ns_add_u32 m0,s53,16384\nv_exp_f32 v80,v80\nM v[212:215],a[212:215],v[112:115],v[212:215]\ns_add_u32 s52,s52,1\n" \
"ds_read_b128 a[200:203],v13 offset:53248\nC v66,v68,v69\nM v[244:247],a[212:215],v[120:123],v[244:247]\ns_add_u32 s45,s45,s49\nv_exp_f32 v81,v81\nM v[152:155],a[216:219],v[96:99],v[152:155]\nbuffer_load_dwordx4 v22,s[40:43],s48 offen offset:0 sc1 lds\nM v[184:187],a[216:219],v[104:107],v[184:187]\n" \
"v_exp_f32 v82,v82\nM v[216:219],a[216:219],v[112:115],v[216:219]\nds_read_b128 a[204:207],v13 offset:55296\nC v67,v70,v71\nM v[248:251],a[216:219],v[120:123],v[248:251]\nv_exp_f32 v83,v83\nM v[156:159],a[220:223],v[96:99],v[156:159]\nbuffer_load_dwordx4 v22,s[40:43],s48 offen offset:1024 sc1 lds\n" \
"M v[188:191],a[220:223],v[104:107],v[188:191]\ns_waitcnt lgkmcnt(1)\nv_exp_f32 v84,v84\nM v[220:223],a[220:223],v[112:115],v[220:223]\ns_cmp_eq_u32 s52,s56\nC v72,v72,v73\nC v73,v74,v75\nM v[252:255],a[220:223],v[120:123],v[252:255]\n" \
"s_cbranch_scc1 LEV_%=\nLEVR9_%=:\nds_read_b128 a[208:211],v13 offset:50176\nC v74,v76,v77\nM v[96:99],a[192:195],a[128:131],v[32:35]\nbuffer_load_dwordx4 v22,s[40:43],s48 offen offset:2048 sc1 lds\nM v[104:107],a[192:195],a[144:147],v[36:39]\ns_lshl_b32 s58,%2,2\n" \
"v_exp_f32 v85,v85\nM v[16:19],a[224:227],v[64:67],v[24:27]\ns_lshl_b32 s59,%2,1\nv_exp_f32 v86,v86\nM v[112:115],a[192:195],a[160:163],v[40:43]\nds_read_b128 a[212:215],v13 offset:52224\nC v75,v78,v79\nM v[120:123],a[192:195],a[176:179],v[44:47]\n" \
"buffer_load_dwordx4 v22,s[40:43],s48 offen offset:3072 sc1 lds\nM v[96:99],a[196:199],a[132:135],v[96:99]\ns_add_u32 m0,s22,114688\nv_exp_f32 v87,v87\nM v[104:107],a[196:199],a[148:151],v[104:107]\ns_add_u32 s48,s48,s49\nv_exp_f32 v88,v88\nM v[16:19],a[228:231],v[72:75],v[16:19]\n" \
"v_exp_f32 v89,v89\nM v[112:115],a[196:199],a[164:167],v[112:115]\nds_read_b128 a[216:219],v13 offset:54272\nC v80,v80,v81\nM v[120:123],a[196:199],a[180:183],v[120:123]\nv_exp_f32 v90,v90\nM v[96:99],a[200:203],a[136:139],v[96:99]\nv_exp_f32 v91,v91\n" \
"M v[104:107],a[200:203],a[152:155],v[104:107]\nC v81,v82,v83\nC v82,v84,v85\nM v[112:115],a[200:203],a[168:171],v[112:115]\nv_exp_f32 v92,v92\nM v[120:123],a[200:203],a[184:187],v[120:123]\ns_waitcnt lgkmcnt(0)\nds_read_b128 a[220:223],v13 offset:56320\n" \
"C v83,v86,v87\nM v[96:99],a[204:207],a[140:143],v[96:99]\nv_exp_f32 v93,v93\nM v[104:107],a[204:207],a[156:159],v[104:107]\nC v88,v88,v89\nC v89,v90,v91\nM v[112:115],a[204:207],a[172:175],v[112:115]\nv_exp_f32 v94,v94\n" \
"M v[120:123],a[204:207],a[188:191],v[120:123]\nv_exp_f32 v95,v95\nM v[16:19],a[232:235],v[80:83],v[16:19]\nC v90,v92,v93\nv_xor_b32 v14,0x10000,v14\nM v[100:103],a[208:211],a[128:131],v[32:35]\nds_read_b128 a[192:195],v14 offset:0\nC v91,v94,v95\n" \
"M v[108:111],a[208:211],a[144:147],v[36:39]\nds_read_b128 a[196:199],v14 offset:1024\nv_bfe_u32 v52,v4,6,2\nM v[116:119],a[208:211],a[160:163],v[40:43]\nv_add_u32 v52,s58,v52\nv_lshrrev_b32 v5,1,v4\nM v[124:127],a[208:211],a[176:179],v[44:47]\ns_lshl_b32 s58,%2,9\n" \
"ds_read_b128 a[200:203],v14 offset:2048\nds_read_b128 a[204:207],v14 offset:3072\nM v[100:103],a[212:215],a[132:135],v[100:103]\ns_add_u32 s58,s58,94375936\nds_read_b128 a[208:211],v14 offset:4096\nv_add_u32 v5,s58,v5\nM v[108:111],a[212:215],a[148:151],v[108:111]\ns_lshl_b32 s58,%2,9\n" \
"v_bfe_u32 v53,v4,8,1\nv_lshl_add_u32 v52,v53,4,v52\nM v[116:119],a[212:215],a[164:167],v[116:119]\nv_add_u32 v53,s59,v53\nv_lshrrev_b32 v54,4,v4\nM v[124:127],a[212:215],a[180:183],v[124:127]\nds_read_b128 a[212:215],v14 offset:5120\nv_xor_b32 v53,v53,v54\n" \
"M v[16:19],a[236:239],v[88:91],v[16:19]\nv_lshlrev_b32 v52,11,v52\nv_and_b32 v53,3,v53\nM v[100:103],a[216:219],a[136:139],v[100:103]\nv_lshrrev_b32 v54,9,v4\nv_lshl_add_u32 v53,v54,2,v53\nM v[108:111],a[216:219],a[152:155],v[108:111]\nbuffer_load_dwordx2 v[10:11],v5,s[36:39],0 offen sc1\n" \
"M v[116:119],a[216:219],a[168:171],v[116:119]\nv_max3_f32 v15,v16,v17,v18\nv_max_f32 v15,v15,v19\nM v[124:127],a[216:219],a[184:187],v[124:127]\ns_waitcnt lgkmcnt(0)\nds_read_b128 a[216:219],v14 offset:6144\nv_cmp_nge_f32 vcc,0x53800000,v15\nM v[100:103],a[220:223],a[140:143],v[100:103]\n" \
"v_lshl_add_u32 v1,v53,4,v52\nv_mov_b32 v48,-1\nM v[108:111],a[220:223],a[156:159],v[108:111]\ns_cbranch_vccnz Lcs43_%=\nLcr44_%=:\nv_mov_b32 v49,-1\nv_mov_b32 v50,-1\nM v[116:119],a[220:223],a[172:175],v[116:119]\n" \
"LG17_%=:\ns_cbranch_execz LHR1_%=\ns_cmp_eq_u32 s52,s55\nv_mov_b32 v51,-1\nv_and_b32 v52,0xc0,v4\nM v[124:127],a[220:223],a[188:191],v[124:127]\ns_cbranch_scc1 LH1_%=\nLH1R10_%=:\n" \
"ds_read_b128 a[220:223],v14 offset:7168\nv_xor_b32 v13,0x10000,v13\nM v[128:131],a[192:195],v[64:67],v[128:131]\nv_exp_f32 v96,v96\nM v[160:163],a[192:195],v[72:75],v[160:163]\nv_exp_f32 v97,v97\nM v[192:195],a[192:195],v[80:83],v[192:195]\nv_exp_f32 v98,v98\n" \
"M v[224:227],a[192:195],v[88:91],v[224:227]\nv_exp_f32 v99,v99\nM v[132:135],a[196:199],v[64:67],v[132:135]\nv_exp_f32 v100,v100\nM v[164:167],a[196:199],v[72:75],v[164:167]\nv_exp_f32 v101,v101\nM v[196:199],a[196:199],v[80:83],v[196:199]\ns_waitcnt vmcnt(19) lgkmcnt(0)\n" \
"v_exp_f32 v102,v102\nM v[228:231],a[196:199],v[88:91],v[228:231]\ns_barrier\nds_read_b128 a[192:195],v13 offset:32768\nM v[136:139],a[200:203],v[64:67],v[136:139]\nbuffer_load_dwordx4 v1,s[84:87],s46 offen offset:0 lds\nM v[168:171],a[200:203],v[72:75],v[168:171]\ns_add_u32 m0,s22,115584\n" \
"v_exp_f32 v103,v103\nM v[200:203],a[200:203],v[80:83],v[200:203]\nv_exp_f32 v104,v104\nM v[232:235],a[200:203],v[88:91],v[232:235]\nv_exp_f32 v105,v105\nM v[140:143],a[204:207],v[64:67],v[140:143]\nbuffer_load_dwordx4 v1,s[84:87],s46 offen offset:128 lds\nM v[172:175],a[204:207],v[72:75],v[172:175]\n" \
"s_add_u32 m0,s22,116736\nv_exp_f32 v106,v106\nM v[204:207],a[204:207],v[80:83],v[204:207]\ns_mov_b32 s46,65536\nv_exp_f32 v107,v107\nM v[236:239],a[204:207],v[88:91],v[236:239]\nv_exp_f32 v108,v108\nM v[144:147],a[208:211],v[64:67],v[144:147]\n" \
"buffer_load_dwordx4 v1,s[84:87],s46 offen offset:0 lds\nM v[176:179],a[208:211],v[72:75],v[176:179]\ns_add_u32 m0,s22,117632\nv_exp_f32 v109,v109\nM v[208:211],a[208:211],v[80:83],v[208:211]\nds_read_b128 a[196:199],v13 offset:34816\nC v96,v96,v97\nM v[240:243],a[208:211],v[88:91],v[240:243]\n" \
"v_exp_f32 v110,v110\nM v[148:151],a[212:215],v[64:67],v[148:151]\nbuffer_load_dwordx4 v1,s[84:87],s46 offen offset:128 lds\nM v[180:183],a[212:215],v[72:75],v[180:183]\ns_add_u32 m0,s22,0\nds_read_b128 a[200:203],v13 offset:36864\nC v97,v98,v99\nM v[212:215],a[212:215],v[80:83],v[212:215]\n" \
"s_mov_b32 s46,131072\nv_exp_f32 v111,v111\nM v[244:247],a[212:215],v[88:91],v[244:247]\ns_and_b32 s61,%1,1\nv_exp_f32 v112,v112\nM v[152:155],a[216:219],v[64:67],v[152:155]\nbuffer_load_dwordx4 v1,s[84:87],s46 offen offset:0 lds\nM v[184:187],a[216:219],v[72:75],v[184:187]\n" \
"s_add_u32 m0,s22,896\nds_read_b128 a[204:207],v13 offset:38912\nC v98,v100,v101\nM v[216:219],a[216:219],v[80:83],v[216:219]\ns_lshl_b32 s61,s61,2\nv_exp_f32 v113,v113\nM v[248:251],a[216:219],v[88:91],v[248:251]\ns_bfe_u32 s60,%1,0x20001\n" \
"v_exp_f32 v114,v114\nM v[156:159],a[220:223],v[64:67],v[156:159]\nbuffer_load_dwordx4 v1,s[84:87],s46 offen offset:128 lds\nM v[188:191],a[220:223],v[72:75],v[188:191]\ns_add_u32 m0,s22,2048\nv_exp_f32 v115,v115\nM v[220:223],a[220:223],v[80:83],v[220:223]\ns_mov_b32 s46,196608\n" \
"v_exp_f32 v116,v116\nM v[252:255],a[220:223],v[88:91],v[252:255]\ns_cmp_eq_u32 s60,0\ns_cselect_b64 s[96:97],-1,0\ns_waitcnt lgkmcnt(0)\nds_read_b128 a[208:211],v13 offset:33792\nC v99,v102,v103\nM v[64:67],a[192:195],a[128:131],v[32:35]\n" \
"buffer_load_dwordx4 v1,s[84:87],s46 offen offset:0 lds\nM v[72:75],a[192:195],a[144:147],v[36:39]\ns_waitcnt vmcnt(7)\nv_cmp_gt_u64 s[98:99],s[14:15],v[10:11]\nv_cmp_le_u64 s[18:19],s[16:17],v[10:11]\nM v[24:27],a[224:227],v[96:99],v[16:19]\ns_or_b64 s[98:99],s[98:99],s[18:19]\ns_cbranch_scc1 LPZ_%=\n" \
"LPZR_%=:\nds_read_b128 a[212:215],v13 offset:35840\nC v104,v104,v105\nM v[80:83],a[192:195],a[160:163],v[40:43]\ns_add_u32 m0,s22,2944\nv_exp_f32 v117,v117\nM v[88:91],a[192:195],a[176:179],v[44:47]\nbuffer_load_dwordx4 v1,s[84:87],s46 offen offset:128 lds\n" \
"M v[64:67],a[196:199],a[132:135],v[64:67]\ns_add_u32 m0,s22,32768\nC v105,v106,v107\nC v106,v108,v109\nC v107,v110,v111\nM v[72:75],a[196:199],a[148:151],v[72:75]\ns_mov_b32 s46,262144\nv_exp_f32 v118,v118\n" \
"M v[24:27],a[228:231],v[104:107],v[24:27]\ns_add_u32 s14,s14,3000\ns_addc_u32 s15,s15,0\nv_exp_f32 v119,v119\nM v[80:83],a[196:199],a[164:167],v[80:83]\nds_read_b128 a[216:219],v13 offset:37888\nC v112,v112,v113\nM v[88:91],a[196:199],a[180:183],v[88:91]\n" \
"v_exp_f32 v120,v120\nM v[64:67],a[200:203],a[136:139],v[64:67]\nv_exp_f32 v121,v121\nM v[72:75],a[200:203],a[152:155],v[72:75]\nds_read_b128 a[220:223],v13 offset:39936\nC v113,v114,v115\nM v[80:83],a[200:203],a[168:171],v[80:83]\nv_exp_f32 v122,v122\n" \
"M v[88:91],a[200:203],a[184:187],v[88:91]\nv_exp_f32 v123,v123\nM v[64:67],a[204:207],a[140:143],v[64:67]\nv_exp_f32 v124,v124\nM v[72:75],a[204:207],a[156:159],v[72:75]\nC v114,v116,v117\nC v115,v118,v119\nM v[80:83],a[204:207],a[172:175],v[80:83]\n" \
"ds_read_b128 a[192:195],v14 offset:16384\nC v120,v120,v121\nM v[88:91],a[204:207],a[188:191],v[88:91]\nv_exp_f32 v125,v125\nM v[24:27],a[232:235],v[112:115],v[24:27]\ns_waitcnt lgkmcnt(1)\nv_exp_f32 v126,v126\nM v[68:71],a[208:211],a[128:131],v[32:35]\n" \
"v_exp_f32 v127,v127\nM v[76:79],a[208:211],a[144:147],v[36:39]\nC v121,v122,v123\nC v122,v124,v125\nM v[84:87],a[208:211],a[160:163],v[40:43]\nds_read_b128 a[196:199],v14 offset:17408\nC v123,v126,v127\nM v[92:95],a[208:211],a[176:179],v[44:47]\n" \
"ds_read_b128 a[200:203],v14 offset:18432\nds_read_b128 a[204:207],v14 offset:19456\nM v[68:71],a[212:215],a[132:135],v[68:71]\nds_read_b128 a[208:211],v14 offset:20480\nv_bfe_u32 v53,v4,4,1\nM v[76:79],a[212:215],a[148:151],v[76:79]\nv_lshl_add_u32 v52,v53,3,v52\nv_lshrrev_b32 v54,7,v4\n" \
"M v[84:87],a[212:215],a[164:167],v[84:87]\nv_bfe_u32 v53,v4,5,1\nv_and_b32 v54,2,v54\nM v[92:95],a[212:215],a[180:183],v[92:95]\nds_read_b128 a[212:215],v14 offset:21504\nv_xor_b32 v53,v53,v54\nM v[24:27],a[236:239],v[120:123],v[24:27]\nv_lshl_add_u32 v52,v53,4,v52\n" \
"v_lshrrev_b32 v53,8,v4\nM v[68:71],a[216:219],a[136:139],v[68:71]\nv_lshl_add_u32 v52,v53,12,v52\nv_add_u32 v6,s58,v52\nM v[76:79],a[216:219],a[152:155],v[76:79]\ns_bfe_u32 s58,%1,0x20003\nv_max3_f32 v15,v24,v25,v26\nv_max_f32 v15,v15,v27\n" \
"M v[84:87],a[216:219],a[168:171],v[84:87]\ns_add_u32 s61,s61,s58\nv_cmp_nge_f32 vcc,0x53800000,v15\nv_xor_b32 v7,0x110,v6\nM v[92:95],a[216:219],a[184:187],v[92:95]\ns_and_b32 s58,s61,6\nds_read_b128 a[216:219],v14 offset:22528\nv_xor_b32 v8,32,v6\n" \
"M v[68:71],a[220:223],a[140:143],v[68:71]\ns_add_u32 s58,s58,s61\nv_xor_b32 v9,0x130,v6\nv_xor_b32 v6,0x10000,v6\nM v[76:79],a[220:223],a[156:159],v[76:79]\ns_cbranch_vccnz Lcs45_%=\nLcr46_%=:\nv_exp_f32 v64,v64\n" \
"M v[84:87],a[220:223],a[172:175],v[84:87]\ns_lshl_b32 s58,s58,2\nv_exp_f32 v65,v65\nM v[92:95],a[220:223],a[188:191],v[92:95]\ns_waitcnt lgkmcnt(1)\ns_add_u32 s58,s58,s60\nds_read_b128 a[220:223],v14 offset:23552\nC v64,v64,v65\n" \
"M v[128:131],a[192:195],v[96:99],v[128:131]\ns_lshl_b32 s58,s58,15\nv_exp_f32 v66,v66\nM v[160:163],a[192:195],v[104:107],v[160:163]\ns_add_u32 s28,s58,50331648\nv_exp_f32 v67,v67\nM v[192:195],a[192:195],v[112:115],v[192:195]\ns_add_u32 s29,s58,52428800\n" \
"v_exp_f32 v68,v68\nM v[224:227],a[192:195],v[120:123],v[224:227]\ns_add_u32 s30,s58,54525952\nv_exp_f32 v69,v69\nM v[132:135],a[196:199],v[96:99],v[132:135]\ns_add_u32 s31,s58,56623104\nv_exp_f32 v70,v70\nM v[164:167],a[196:199],v[104:107],v[164:167]\n" \
"s_lshr_b32 s58,%2,1\nv_exp_f32 v71,v71\nM v[196:199],a[196:199],v[112:115],v[196:199]\ns_waitcnt vmcnt(19) lgkmcnt(0)\nv_exp_f32 v72,v72\nM v[228:231],a[196:199],v[120:123],v[228:231]\ns_lshl_b32 s58,s58,18\ns_barrier\n" \
"ds_read_b128 a[192:195],v13 offset:49152\nM v[136:139],a[200:203],v[96:99],v[136:139]\nbuffer_load_dwordx4 v22,s[40:43],s45 offen offset:0 sc1 lds\nM v[168:171],a[200:203],v[104:107],v[168:171]\ns_add_u32 s47,s28,0\nv_exp_f32 v73,v73\nM v[200:203],a[200:203],v[112:115],v[200:203]\nv_exp_f32 v74,v74\n" \
"M v[232:235],a[200:203],v[120:123],v[232:235]\nv_exp_f32 v75,v75\nM v[140:143],a[204:207],v[96:99],v[140:143]\nbuffer_load_dwordx4 v22,s[40:43],s45 offen offset:1024 sc1 lds\nM v[172:175],a[204:207],v[104:107],v[172:175]\nv_exp_f32 v76,v76\nM v[204:207],a[204:207],v[112:115],v[204:207]\nv_exp_f32 v77,v77\n" \
"M v[236:239],a[204:207],v[120:123],v[236:239]\nv_exp_f32 v78,v78\nM v[144:147],a[208:211],v[96:99],v[144:147]\nbuffer_load_dwordx4 v22,s[40:43],s45 offen offset:2048 sc1 lds\nM v[176:179],a[208:211],v[104:107],v[176:179]\nds_read_b128 a[196:199],v13 offset:51200\nC v65,v66,v67\nM v[208:211],a[208:211],v[112:115],v[208:211]\n" \
"v_exp_f32 v79,v79\nM v[240:243],a[208:211],v[120:123],v[240:243]\nv_exp_f32 v80,v80\nM v[148:151],a[212:215],v[96:99],v[148:151]\nbuffer_load_dwordx4 v22,s[40:43],s45 offen offset:3072 sc1 lds\nM v[180:183],a[212:215],v[104:107],v[180:183]\ns_add_u32 m0,s22,16384\nv_exp_f32 v81,v81\n" \
"M v[212:215],a[212:215],v[112:115],v[212:215]\ns_add_u32 s52,s52,1\nds_read_b128 a[200:203],v13 offset:53248\nC v66,v68,v69\nM v[244:247],a[212:215],v[120:123],v[244:247]\ns_add_u32 s45,s45,s49\nv_exp_f32 v82,v82\nM v[152:155],a[216:219],v[96:99],v[152:155]\n" \
"buffer_load_dwordx4 v22,s[40:43],s48 offen offset:0 sc1 lds\nM v[184:187],a[216:219],v[104:107],v[184:187]\nv_exp_f32 v83,v83\nM v[216:219],a[216:219],v[112:115],v[216:219]\nds_read_b128 a[204:207],v13 offset:55296\nC v67,v70,v71\nM v[248:251],a[216:219],v[120:123],v[248:251]\nv_exp_f32 v84,v84\n" \
"M v[156:159],a[220:223],v[96:99],v[156:159]\nbuffer_load_dwordx4 v22,s[40:43],s48 offen offset:1024 sc1 lds\nM v[188:191],a[220:223],v[104:107],v[188:191]\ns_waitcnt lgkmcnt(1)\nv_exp_f32 v85,v85\nM v[220:223],a[220:223],v[112:115],v[220:223]\ns_cmp_eq_u32 s52,s56\nC v72,v72,v73\n" \
"C v73,v74,v75\nM v[252:255],a[220:223],v[120:123],v[252:255]\ns_cbranch_scc1 LEV_%=\nLEVR10_%=:\nds_read_b128 a[208:211],v13 offset:50176\nC v74,v76,v77\nM v[96:99],a[192:195],a[128:131],v[32:35]\nbuffer_load_dwordx4 v22,s[40:43],s48 offen offset:2048 sc1 lds\n" \
"M v[104:107],a[192:195],a[144:147],v[36:39]\ns_lshl_b32 s95,%1,2\nv_exp_f32 v86,v86\nM v[16:19],a[224:227],v[64:67],v[24:27]\ns_add_u32 s95,s95,%2\nv_exp_f32 v87,v87\nM v[112:115],a[192:195],a[160:163],v[40:43]\ns_lshl_b32 s95,s95,5\n" \
"C v75,v78,v79\nC v80,v80,v81\nM v[120:123],a[192:195],a[176:179],v[44:47]\nbuffer_load_dwordx4 v22,s[40:43],s48 offen offset:3072 sc1 lds\nM v[96:99],a[196:199],a[132:135],v[96:99]\ns_add_u32 m0,s22,49152\nds_read_b128 a[212:215],v13 offset:52224\nC v81,v82,v83\n" \
"M v[104:107],a[196:199],a[148:151],v[104:107]\ns_add_u32 s48,s48,s49\nv_exp_f32 v88,v88\nM v[16:19],a[228:231],v[72:75],v[16:19]\ns_add_u32 s95,s95,94437376\nv_exp_f32 v89,v89\nM v[112:115],a[196:199],a[164:167],v[112:115]\nv_exp_f32 v90,v90\n" \
"M v[120:123],a[196:199],a[180:183],v[120:123]\nds_read_b128 a[216:219],v13 offset:54272\nC v82,v84,v85\nM v[96:99],a[200:203],a[136:139],v[96:99]\nv_exp_f32 v91,v91\nM v[104:107],a[200:203],a[152:155],v[104:107]\nds_read_b128 a[220:223],v13 offset:56320\nC v83,v86,v87\n" \
"M v[112:115],a[200:203],a[168:171],v[112:115]\nv_exp_f32 v92,v92\nM v[120:123],a[200:203],a[184:187],v[120:123]\ns_waitcnt lgkmcnt(2)\nv_exp_f32 v93,v93\nM v[96:99],a[204:207],a[140:143],v[96:99]\nv_exp_f32 v94,v94\nM v[104:107],a[204:207],a[156:159],v[104:107]\n" \
"C v88,v88,v89\nC v89,v90,v91\nM v[112:115],a[204:207],a[172:175],v[112:115]\nv_exp_f32 v95,v95\nM v[120:123],a[204:207],a[188:191],v[120:123]\nC v90,v92,v93\nv_xor_b32 v14,0x10000,v14\nM v[16:19],a[232:235],v[80:83],v[16:19]\n" \
"ds_read_b128 a[192:195],v14 offset:0\nC v91,v94,v95\nM v[100:103],a[208:211],a[128:131],v[32:35]\nbuffer_store_dwordx4 v[48:51],v4,s[36:39],s72 offen offset:0 sc1\nM v[108:111],a[208:211],a[144:147],v[36:39]\nds_read_b128 a[196:199],v14 offset:1024\nds_read_b128 a[200:203],v14 offset:2048\nM v[116:119],a[208:211],a[160:163],v[40:43]\n" \
"ds_read_b128 a[204:207],v14 offset:3072\nv_xor_b32 v13,0x10000,v13\nM v[124:127],a[208:211],a[176:179],v[44:47]\nds_read_b128 a[208:211],v14 offset:4096\nv_xor_b32 v7,0x10000,v7\nM v[100:103],a[212:215],a[132:135],v[100:103]\nv_xor_b32 v8,0x10000,v8\nv_xor_b32 v9,0x10000,v9\n" \
"M v[108:111],a[212:215],a[148:151],v[108:111]\ns_waitcnt lgkmcnt(4)\nv_add_u32 v2,s58,v22\nv_bfe_u32 v52,v4,7,2\nM v[116:119],a[212:215],a[164:167],v[116:119]\ns_lshl_b32 s58,s61,2\nv_mov_b32 v53,s11\nM v[124:127],a[212:215],a[180:183],v[124:127]\n" \
"s_add_u32 s58,s58,s60\nds_read_b128 a[212:215],v14 offset:5120\nM v[16:19],a[236:239],v[88:91],v[16:19]\ns_lshl_b32 s58,s58,6\nM v[100:103],a[216:219],a[136:139],v[100:103]\ns_add_u32 s58,s58,94388224\nv_lshl_add_u32 v5,v52,11,s58\nv_bfe_u32 v52,v4,4,3\n" \
"M v[108:111],a[216:219],a[152:155],v[108:111]\ns_lshl_b32 s58,%2,13\nv_max3_f32 v15,v16,v17,v18\nv_max_f32 v15,v15,v19\nM v[116:119],a[216:219],a[168:171],v[116:119]\ns_add_u32 s58,s58,0x20000\nv_cmp_nge_f32 vcc,0x53800000,v15\nv_lshl_add_u32 v5,v52,3,v5\n" \
"M v[124:127],a[216:219],a[184:187],v[124:127]\nds_read_b128 a[216:219],v14 offset:6144\nv_mov_b32 v52,s10\nM v[100:103],a[220:223],a[140:143],v[100:103]\nv_add_u32 v12,s58,v4\nv_xor_b32 v57,0x4000,v12\nM v[108:111],a[220:223],a[156:159],v[108:111]\ns_cbranch_vccnz Lcs47_%=\n" \
"Lcr48_%=:\nv_exp_f32 v96,v96\nM v[116:119],a[220:223],a[172:175],v[116:119]\ns_lshl_b32 s58,s7,1\nv_exp_f32 v97,v97\nM v[124:127],a[220:223],a[188:191],v[124:127]\ns_sub_u32 s58,15,s58\nds_read_b128 a[220:223],v14 offset:7168\n" \
"C v96,v96,v97\nM v[128:131],a[192:195],v[64:67],v[128:131]\ns_lshl_b32 s59,s58,20\nv_exp_f32 v98,v98\nM v[160:163],a[192:195],v[72:75],v[160:163]\ns_add_u32 s51,s68,s59\nv_exp_f32 v99,v99\nM v[192:195],a[192:195],v[80:83],v[192:195]\n" \
"s_lshl_b32 s59,s54,6\nv_exp_f32 v100,v100\nM v[224:227],a[192:195],v[88:91],v[224:227]\ns_waitcnt lgkmcnt(1)\ns_add_u32 s51,s51,s59\nv_exp_f32 v101,v101\nM v[132:135],a[196:199],v[64:67],v[132:135]\ns_add_u32 s62,s51,0\n" \
"v_exp_f32 v102,v102\nM v[164:167],a[196:199],v[72:75],v[164:167]\ns_lshl_b32 s59,s58,10\nv_exp_f32 v103,v103\nM v[196:199],a[196:199],v[80:83],v[196:199]\ns_waitcnt vmcnt(13) lgkmcnt(0)\nv_exp_f32 v104,v104\nM v[228:231],a[196:199],v[88:91],v[228:231]\n" \
"s_add_u32 s50,s69,s59\ns_barrier\nds_read_b128 a[192:195],v13 offset:32768\nM v[136:139],a[200:203],v[64:67],v[136:139]\nbuffer_load_dwordx4 v1,s[84:87],s46 offen offset:0 lds\nM v[168:171],a[200:203],v[72:75],v[168:171]\ns_add_u32 m0,s22,50048\nv_exp_f32 v105,v105\n" \
"M v[200:203],a[200:203],v[80:83],v[200:203]\ns_lshr_b32 s59,s54,4\nv_exp_f32 v106,v106\nM v[232:235],a[200:203],v[88:91],v[232:235]\ns_add_u32 s50,s50,s59\nv_exp_f32 v107,v107\nM v[140:143],a[204:207],v[64:67],v[140:143]\nbuffer_load_dwordx4 v1,s[84:87],s46 offen offset:128 lds\n" \
"M v[172:175],a[204:207],v[72:75],v[172:175]\ns_add_u32 m0,s22,51200\nv_exp_f32 v108,v108\nM v[204:207],a[204:207],v[80:83],v[204:207]\ns_mov_b32 s46,327680\nv_exp_f32 v109,v109\nM v[236:239],a[204:207],v[88:91],v[236:239]\nds_read_b128 a[196:199],v13 offset:34816\n" \
"C v97,v98,v99\nM v[144:147],a[208:211],v[64:67],v[144:147]\nbuffer_load_dwordx4 v1,s[84:87],s46 offen offset:0 lds\nM v[176:179],a[208:211],v[72:75],v[176:179]\ns_add_u32 m0,s22,52096\nv_exp_f32 v110,v110\nM v[208:211],a[208:211],v[80:83],v[208:211]\nv_exp_f32 v111,v111\n" \
"M v[240:243],a[208:211],v[88:91],v[240:243]\nds_read_b128 a[200:203],v13 offset:36864\nC v98,v100,v101\nM v[148:151],a[212:215],v[64:67],v[148:151]\nbuffer_load_dwordx4 v1,s[84:87],s46 offen offset:128 lds\nM v[180:183],a[212:215],v[72:75],v[180:183]\ns_add_u32 m0,s22,65536\nv_exp_f32 v112,v112\n" \
"M v[212:215],a[212:215],v[80:83],v[212:215]\ns_mov_b32 s46,393216\nv_exp_f32 v113,v113\nM v[244:247],a[212:215],v[88:91],v[244:247]\nv_exp_f32 v114,v114\nM v[152:155],a[216:219],v[64:67],v[152:155]\nbuffer_load_dwordx4 v22,s[40:43],s45 offen offset:0 sc1 lds\nM v[184:187],a[216:219],v[72:75],v[184:187]\n" \
"ds_read_b128 a[204:207],v13 offset:38912\nC v99,v102,v103\nM v[216:219],a[216:219],v[80:83],v[216:219]\nv_exp_f32 v115,v115\nM v[248:251],a[216:219],v[88:91],v[248:251]\nv_exp_f32 v116,v116\nM v[156:159],a[220:223],v[64:67],v[156:159]\nbuffer_load_dwordx4 v22,s[40:43],s45 offen offset:1024 sc1 lds\n" \
"M v[188:191],a[220:223],v[72:75],v[188:191]\nv_exp_f32 v117,v117\nM v[220:223],a[220:223],v[80:83],v[220:223]\nv_exp_f32 v118,v118\nM v[252:255],a[220:223],v[88:91],v[252:255]\ns_waitcnt lgkmcnt(0)\nds_read_b128 a[208:211],v13 offset:33792\nC v104,v104,v105\n" \
"M v[64:67],a[192:195],a[128:131],v[32:35]\nbuffer_load_dwordx4 v22,s[40:43],s45 offen offset:2048 sc1 lds\nM v[72:75],a[192:195],a[144:147],v[36:39]\nds_read_b128 a[212:215],v13 offset:35840\nC v105,v106,v107\nM v[24:27],a[224:227],v[96:99],v[16:19]\nv_exp_f32 v119,v119\nM v[80:83],a[192:195],a[160:163],v[40:43]\n" \
"v_exp_f32 v120,v120\nM v[88:91],a[192:195],a[176:179],v[44:47]\nbuffer_load_dwordx4 v22,s[40:43],s45 offen offset:3072 sc1 lds\nM v[64:67],a[196:199],a[132:135],v[64:67]\ns_add_u32 m0,s22,98304\nC v106,v108,v109\nC v107,v110,v111\nM v[72:75],a[196:199],a[148:151],v[72:75]\n" \
"s_add_u32 s45,s45,s49\nds_read_b128 a[216:219],v13 offset:37888\nC v112,v112,v113\nM v[24:27],a[228:231],v[104:107],v[24:27]\nv_exp_f32 v121,v121\nM v[80:83],a[196:199],a[164:167],v[80:83]\nv_exp_f32 v122,v122\nM v[88:91],a[196:199],a[180:183],v[88:91]\n" \
"v_exp_f32 v123,v123\nM v[64:67],a[200:203],a[136:139],v[64:67]\nv_exp_f32 v124,v124\nM v[72:75],a[200:203],a[152:155],v[72:75]\nv_exp_f32 v125,v125\nM v[80:83],a[200:203],a[168:171],v[80:83]\nds_read_b128 a[220:223],v13 offset:39936\nC v113,v114,v115\n" \
"M v[88:91],a[200:203],a[184:187],v[88:91]\nv_exp_f32 v126,v126\nM v[64:67],a[204:207],a[140:143],v[64:67]\nds_read_b128 a[192:195],v14 offset:16384\nC v114,v116,v117\nM v[72:75],a[204:207],a[156:159],v[72:75]\nC v115,v118,v119\nC v120,v120,v121\n" \
"M v[80:83],a[204:207],a[172:175],v[80:83]\nv_exp_f32 v127,v127\nM v[88:91],a[204:207],a[188:191],v[88:91]\nds_read_b128 a[196:199],v14 offset:17408\nds_read_b128 a[200:203],v14 offset:18432\nM v[24:27],a[232:235],v[112:115],v[24:27]\ns_waitcnt lgkmcnt(3)\nds_read_b64_tr_b16 a[0:1],v6 offset:49152\n" \
"C v121,v122,v123\nM v[68:71],a[208:211],a[128:131],v[32:35]\nbuffer_store_dwordx4 v[48:51],v4,s[36:39],s72 offen offset:1024 sc1\nM v[76:79],a[208:211],a[144:147],v[36:39]\nds_read_b64_tr_b16 a[2:3],v7 offset:49152\nds_read_b64_tr_b16 a[4:5],v8 offset:49152\nM v[84:87],a[208:211],a[160:163],v[40:43]\nds_read_b64_tr_b16 a[6:7],v9 offset:49152\n" \
"C v122,v124,v125\nM v[92:95],a[208:211],a[176:179],v[44:47]\nds_read_b64_tr_b16 a[8:9],v6 offset:51200\nC v123,v126,v127\nM v[68:71],a[212:215],a[132:135],v[68:71]\nds_read_b64_tr_b16 a[10:11],v7 offset:51200\nds_read_b64_tr_b16 a[12:13],v8 offset:51200\nM v[76:79],a[212:215],a[148:151],v[76:79]\n" \
"ds_read_b64_tr_b16 a[14:15],v9 offset:51200\nv_xor_b32 v13,0x10000,v13\nM v[84:87],a[212:215],a[164:167],v[84:87]\nds_read_b128 a[204:207],v14 offset:19456\nv_xor_b32 v6,0x10000,v6\nM v[92:95],a[212:215],a[180:183],v[92:95]\nds_read_b128 a[208:211],v14 offset:20480\nv_xor_b32 v7,0x10000,v7\n" \
"M v[24:27],a[236:239],v[120:123],v[24:27]\nds_read_b128 a[212:215],v14 offset:21504\nv_xor_b32 v8,0x10000,v8\nM v[68:71],a[216:219],a[136:139],v[68:71]\nv_xor_b32 v9,0x10000,v9\nM v[76:79],a[216:219],a[152:155],v[76:79]\nv_max3_f32 v15,v24,v25,v26\nv_max_f32 v15,v15,v27\n" \
"M v[84:87],a[216:219],a[168:171],v[84:87]\nv_cmp_nge_f32 vcc,0x53800000,v15\nM v[92:95],a[216:219],a[184:187],v[92:95]\nds_read_b128 a[216:219],v14 offset:22528\nM v[68:71],a[220:223],a[140:143],v[68:71]\nM v[76:79],a[220:223],a[156:159],v[76:79]\ns_cbranch_vccnz Lcs49_%=\nLcr50_%=:\n" \
"v_exp_f32 v64,v64\nM v[84:87],a[220:223],a[172:175],v[84:87]\nv_exp_f32 v65,v65\nM v[92:95],a[220:223],a[188:191],v[92:95]\ns_waitcnt lgkmcnt(1)\nds_read_b128 a[220:223],v14 offset:23552\nC v64,v64,v65\nM v[128:131],a[192:195],v[96:99],v[128:131]\n" \
"v_exp_f32 v66,v66\nM v[160:163],a[192:195],v[104:107],v[160:163]\nv_exp_f32 v67,v67\nM v[192:195],a[192:195],v[112:115],v[192:195]\nv_exp_f32 v68,v68\nM v[224:227],a[192:195],v[120:123],v[224:227]\nv_exp_f32 v69,v69\nM v[132:135],a[196:199],v[96:99],v[132:135]\n" \
"v_exp_f32 v70,v70\nM v[164:167],a[196:199],v[104:107],v[164:167]\nv_exp_f32 v71,v71\nM v[196:199],a[196:199],v[112:115],v[196:199]\ns_waitcnt vmcnt(10) lgkmcnt(0)\nv_exp_f32 v72,v72\nM v[228:231],a[196:199],v[120:123],v[228:231]\ns_barrier\n" \
"ds_read_b128 a[192:195],v13 offset:32768\nM v[136:139],a[200:203],v[96:99],v[136:139]\nbuffer_load_dwordx4 v22,s[40:43],s48 offen offset:0 sc1 lds\nM v[168:171],a[200:203],v[104:107],v[168:171]\nv_exp_f32 v73,v73\nM v[200:203],a[200:203],v[112:115],v[200:203]\nv_exp_f32 v74,v74\nM v[232:235],a[200:203],v[120:123],v[232:235]\n" \
"v_exp_f32 v75,v75\nM v[140:143],a[204:207],v[96:99],v[140:143]\nbuffer_load_dwordx4 v22,s[40:43],s48 offen offset:1024 sc1 lds\nM v[172:175],a[204:207],v[104:107],v[172:175]\nLG18_%=:\ns_cbranch_execz LG22_%=\nv_exp_f32 v76,v76\nM v[204:207],a[204:207],v[112:115],v[204:207]\n" \
"v_exp_f32 v77,v77\nM v[236:239],a[204:207],v[120:123],v[236:239]\nv_exp_f32 v78,v78\nM v[144:147],a[208:211],v[96:99],v[144:147]\nbuffer_load_dwordx4 v22,s[40:43],s48 offen offset:2048 sc1 lds\nM v[176:179],a[208:211],v[104:107],v[176:179]\nds_read_b128 a[196:199],v13 offset:34816\nC v65,v66,v67\n" \
"M v[208:211],a[208:211],v[112:115],v[208:211]\nv_exp_f32 v79,v79\nM v[240:243],a[208:211],v[120:123],v[240:243]\nv_exp_f32 v80,v80\nM v[148:151],a[212:215],v[96:99],v[148:151]\nbuffer_load_dwordx4 v22,s[40:43],s48 offen offset:3072 sc1 lds\nM v[180:183],a[212:215],v[104:107],v[180:183]\ns_add_u32 m0,s22,81920\n" \
"v_exp_f32 v81,v81\nM v[212:215],a[212:215],v[112:115],v[212:215]\ns_add_u32 s52,s52,1\nds_read_b128 a[200:203],v13 offset:36864\nC v66,v68,v69\nM v[244:247],a[212:215],v[120:123],v[244:247]\ns_add_u32 s48,s48,s49\nv_exp_f32 v82,v82\n" \
"M v[152:155],a[216:219],v[96:99],v[152:155]\nbuffer_load_dwordx4 v1,s[84:87],s46 offen offset:0 lds\nM v[184:187],a[216:219],v[104:107],v[184:187]\ns_add_u32 m0,s22,82816\nds_read_b128 a[204:207],v13 offset:38912\nC v67,v70,v71\nM v[216:219],a[216:219],v[112:115],v[216:219]\nv_exp_f32 v83,v83\n" \
"M v[248:251],a[216:219],v[120:123],v[248:251]\ns_waitcnt lgkmcnt(1)\nv_exp_f32 v84,v84\nM v[156:159],a[220:223],v[96:99],v[156:159]\nbuffer_load_dwordx4 v1,s[84:87],s46 offen offset:128 lds\nM v[188:191],a[220:223],v[104:107],v[188:191]\ns_add_u32 m0,s22,83968\nv_exp_f32 v85,v85\n" \
"M v[220:223],a[220:223],v[112:115],v[220:223]\ns_cmp_eq_u32 s52,s56\nds_read_b128 a[208:211],v13 offset:33792\nC v72,v72,v73\nM v[252:255],a[220:223],v[120:123],v[252:255]\ns_cbranch_scc1 LEV_%=\nLEVR11_%=:\nv_exp_f32 v86,v86\n" \
"M v[96:99],a[192:195],a[128:131],v[32:35]\ns_mov_b32 s46,458752\nv_exp_f32 v87,v87\nM v[104:107],a[192:195],a[144:147],v[36:39]\nbuffer_load_dwordx4 v1,s[84:87],s46 offen offset:0 lds\nM v[16:19],a[224:227],v[64:67],v[24:27]\ns_add_u32 m0,s22,84864\nv_exp_f32 v88,v88\n" \
"M v[112:115],a[192:195],a[160:163],v[40:43]\nds_read_b128 a[212:215],v13 offset:35840\nC v73,v74,v75\nM v[120:123],a[192:195],a[176:179],v[44:47]\nv_exp_f32 v89,v89\nM v[96:99],a[196:199],a[132:135],v[96:99]\nC v74,v76,v77\nC v75,v78,v79\n" \
"M v[104:107],a[196:199],a[148:151],v[104:107]\nbuffer_load_dwordx4 v1,s[84:87],s46 offen offset:128 lds\nM v[16:19],a[228:231],v[72:75],v[16:19]\ns_add_u32 m0,s22,114688\nv_exp_f32 v90,v90\nM v[112:115],a[196:199],a[164:167],v[112:115]\ns_mov_b32 s46,524288\nds_read_b128 a[216:219],v13 offset:37888\n" \
"C v80,v80,v81\nM v[120:123],a[196:199],a[180:183],v[120:123]\nv_exp_f32 v91,v91\nM v[96:99],a[200:203],a[136:139],v[96:99]\nbuffer_load_dwordx4 v22,s[40:43],s45 offen offset:0 sc1 lds\nM v[104:107],a[200:203],a[152:155],v[104:107]\nds_read_b128 a[220:223],v13 offset:39936\nC v81,v82,v83\n" \
"M v[112:115],a[200:203],a[168:171],v[112:115]\nv_exp_f32 v92,v92\nM v[120:123],a[200:203],a[184:187],v[120:123]\ns_waitcnt lgkmcnt(1)\nv_exp_f32 v93,v93\nM v[96:99],a[204:207],a[140:143],v[96:99]\nbuffer_load_dwordx4 v22,s[40:43],s45 offen offset:1024 sc1 lds\nM v[104:107],a[204:207],a[156:159],v[104:107]\n" \
"C v82,v84,v85\nC v83,v86,v87\nM v[112:115],a[204:207],a[172:175],v[112:115]\nv_exp_f32 v94,v94\nM v[120:123],a[204:207],a[188:191],v[120:123]\nC v88,v88,v89\nC v89,v90,v91\nM v[16:19],a[232:235],v[80:83],v[16:19]\n" \
"buffer_load_dwordx4 v22,s[40:43],s45 offen offset:2048 sc1 lds\nM v[100:103],a[208:211],a[128:131],v[32:35]\nds_read_b64_tr_b16 a[16:17],v6 offset:0\nds_read_b64_tr_b16 a[18:19],v7 offset:0\nM v[108:111],a[208:211],a[144:147],v[36:39]\nds_read_b64_tr_b16 a[20:21],v8 offset:0\nC v90,v92,v93\nM v[116:119],a[208:211],a[160:163],v[40:43]\n" \
"ds_read_b64_tr_b16 a[22:23],v9 offset:0\nv_xor_b32 v14,0x10000,v14\nM v[124:127],a[208:211],a[176:179],v[44:47]\nbuffer_load_dwordx4 v22,s[40:43],s45 offen offset:3072 sc1 lds\nM v[100:103],a[212:215],a[132:135],v[100:103]\ns_add_u32 m0,s22,0\nds_read_b128 a[192:195],v14 offset:16384\nds_read_b128 a[196:199],v14 offset:17408\n" \
"M v[108:111],a[212:215],a[148:151],v[108:111]\ns_add_u32 s45,s45,s49\nv_exp_f32 v95,v95\nM v[116:119],a[212:215],a[164:167],v[116:119]\nds_read_b128 a[200:203],v14 offset:18432\nC v91,v94,v95\nM v[124:127],a[212:215],a[180:183],v[124:127]\nbuffer_store_dwordx4 v[48:51],v4,s[36:39],s72 offen offset:2048 sc1\n" \
"M v[16:19],a[236:239],v[88:91],v[16:19]\nds_read_b64_tr_b16 a[24:25],v6 offset:2048\nds_read_b64_tr_b16 a[26:27],v7 offset:2048\nM v[100:103],a[216:219],a[136:139],v[100:103]\nds_read_b64_tr_b16 a[28:29],v8 offset:2048\nM v[108:111],a[216:219],a[152:155],v[108:111]\nds_read_b64_tr_b16 a[30:31],v9 offset:2048\nds_read_b128 a[204:207],v14 offset:19456\n" \
"M v[116:119],a[216:219],a[168:171],v[116:119]\nds_read_b128 a[208:211],v14 offset:20480\nv_max3_f32 v15,v16,v17,v18\nM v[124:127],a[216:219],a[184:187],v[124:127]\ns_waitcnt lgkmcnt(6)\nv_max_f32 v15,v15,v19\nv_cmp_nge_f32 vcc,0x53800000,v15\nM v[100:103],a[220:223],a[140:143],v[100:103]\n" \
"ds_read_b128 a[212:215],v14 offset:21504\nds_read_b128 a[216:219],v14 offset:22528\nM v[108:111],a[220:223],a[156:159],v[108:111]\ns_cbranch_vccnz Lcs51_%=\nLcr52_%=:\nM v[116:119],a[220:223],a[172:175],v[116:119]\ns_cmp_eq_u32 s52,s55\nM v[124:127],a[220:223],a[188:191],v[124:127]\n" \
"s_cbranch_scc1 LH1_%=\nLH1R12_%=:\nds_read_b128 a[220:223],v14 offset:23552\nv_xor_b32 v13,0x10000,v13\nM v[128:131],a[192:195],v[64:67],v[128:131]\nv_exp_f32 v96,v96\nM v[160:163],a[192:195],v[72:75],v[160:163]\nv_exp_f32 v97,v97\n" \
"M v[192:195],a[192:195],v[80:83],v[192:195]\nv_exp_f32 v98,v98\nM v[224:227],a[192:195],v[88:91],v[224:227]\nv_exp_f32 v99,v99\nM v[132:135],a[196:199],v[64:67],v[132:135]\nv_exp_f32 v100,v100\nM v[164:167],a[196:199],v[72:75],v[164:167]\nv_exp_f32 v101,v101\n" \
"M v[196:199],a[196:199],v[80:83],v[196:199]\ns_waitcnt vmcnt(9) lgkmcnt(0)\nv_exp_f32 v102,v102\nM v[228:231],a[196:199],v[88:91],v[228:231]\ns_barrier\nds_read_b128 a[192:195],v13 offset:0\nM v[136:139],a[200:203],v[64:67],v[136:139]\nbuffer_load_dwordx4 v22,s[40:43],s48 offen offset:0 sc1 lds\n" \
"M v[168:171],a[200:203],v[72:75],v[168:171]\nv_exp_f32 v103,v103\nM v[200:203],a[200:203],v[80:83],v[200:203]\nv_exp_f32 v104,v104\nM v[232:235],a[200:203],v[88:91],v[232:235]\nv_exp_f32 v105,v105\nM v[140:143],a[204:207],v[64:67],v[140:143]\nbuffer_load_dwordx4 v22,s[40:43],s48 offen offset:1024 sc1 lds\n" \
"M v[172:175],a[204:207],v[72:75],v[172:175]\nv_exp_f32 v106,v106\nM v[204:207],a[204:207],v[80:83],v[204:207]\nv_exp_f32 v107,v107\nM v[236:239],a[204:207],v[88:91],v[236:239]\nv_exp_f32 v108,v108\nM v[144:147],a[208:211],v[64:67],v[144:147]\nbuffer_load_dwordx4 v22,s[40:43],s48 offen offset:2048 sc1 lds\n" \
"M v[176:179],a[208:211],v[72:75],v[176:179]\nds_read_b128 a[196:199],v13 offset:2048\nC v96,v96,v97\nM v[208:211],a[208:211],v[80:83],v[208:211]\nv_exp_f32 v109,v109\nM v[240:243],a[208:211],v[88:91],v[240:243]\nv_exp_f32 v110,v110\nM v[148:151],a[212:215],v[64:67],v[148:151]\n" \
"buffer_load_dwordx4 v22,s[40:43],s48 offen offset:3072 sc1 lds\nM v[180:183],a[212:215],v[72:75],v[180:183]\ns_add_u32 m0,s22,32768\nv_exp_f32 v111,v111\nM v[212:215],a[212:215],v[80:83],v[212:215]\ns_add_u32 s48,s48,s49\nds_read_b128 a[200:203],v13 offset:4096\nC v97,v98,v99\n" \
"M v[244:247],a[212:215],v[88:91],v[244:247]\nv_exp_f32 v112,v112\nM v[152:155],a[216:219],v[64:67],v[152:155]\nbuffer_load_dwordx4 v1,s[84:87],s46 offen offset:0 lds\nM v[184:187],a[216:219],v[72:75],v[184:187]\ns_add_u32 m0,s22,33664\nds_read_b128 a[204:207],v13 offset:6144\nC v98,v100,v101\n" \
"M v[216:219],a[216:219],v[80:83],v[216:219]\nv_exp_f32 v113,v113\nM v[248:251],a[216:219],v[88:91],v[248:251]\nv_exp_f32 v114,v114\nM v[156:159],a[220:223],v[64:67],v[156:159]\nbuffer_load_dwordx4 v1,s[84:87],s46 offen offset:128 lds\nM v[188:191],a[220:223],v[72:75],v[188:191]\ns_add_u32 m0,s22,34816\n" \
"v_exp_f32 v115,v115\nM v[220:223],a[220:223],v[80:83],v[220:223]\ns_mov_b32 s46,589824\nv_exp_f32 v116,v116\nM v[252:255],a[220:223],v[88:91],v[252:255]\ns_waitcnt lgkmcnt(0)\nds_read_b128 a[208:211],v13 offset:1024\nC v99,v102,v103\n" \
"M v[64:67],a[192:195],a[128:131],v[32:35]\nbuffer_load_dwordx4 v1,s[84:87],s46 offen offset:0 lds\nM v[72:75],a[192:195],a[144:147],v[36:39]\ns_add_u32 m0,s22,35712\nv_exp_f32 v117,v117\nM v[24:27],a[224:227],v[96:99],v[16:19]\nC v104,v104,v105\nC v105,v106,v107\n" \
"M v[80:83],a[192:195],a[160:163],v[40:43]\nds_read_b128 a[212:215],v13 offset:3072\nC v106,v108,v109\nM v[88:91],a[192:195],a[176:179],v[44:47]\nbuffer_load_dwordx4 v1,s[84:87],s46 offen offset:128 lds\nM v[64:67],a[196:199],a[132:135],v[64:67]\ns_add_u32 m0,s22,16384\nC v107,v110,v111\n" \
"C v112,v112,v113\nM v[72:75],a[196:199],a[148:151],v[72:75]\ns_mov_b32 s46,655360\nv_exp_f32 v118,v118\nM v[24:27],a[228:231],v[104:107],v[24:27]\nv_exp_f32 v119,v119\nM v[80:83],a[196:199],a[164:167],v[80:83]\nbuffer_load_dwordx4 v22,s[40:43],s45 offen offset:0 sc1 lds\n" \
"M v[88:91],a[196:199],a[180:183],v[88:91]\nds_read_b128 a[216:219],v13 offset:5120\nC v113,v114,v115\nM v[64:67],a[200:203],a[136:139],v[64:67]\nv_exp_f32 v120,v120\nM v[72:75],a[200:203],a[152:155],v[72:75]\nv_exp_f32 v121,v121\nM v[80:83],a[200:203],a[168:171],v[80:83]\n" \
"buffer_load_dwordx4 v22,s[40:43],s45 offen offset:1024 sc1 lds\nM v[88:91],a[200:203],a[184:187],v[88:91]\nds_read_b128 a[220:223],v13 offset:7168\nC v114,v116,v117\nM v[64:67],a[204:207],a[140:143],v[64:67]\nv_exp_f32 v122,v122\nM v[72:75],a[204:207],a[156:159],v[72:75]\nv_exp_f32 v123,v123\n" \
"M v[80:83],a[204:207],a[172:175],v[80:83]\nC v115,v118,v119\nC v120,v120,v121\nM v[88:91],a[204:207],a[188:191],v[88:91]\nbuffer_load_dwordx4 v22,s[40:43],s45 offen offset:2048 sc1 lds\nM v[24:27],a[232:235],v[112:115],v[24:27]\ns_waitcnt lgkmcnt(0)\nds_read_b64_tr_b16 a[32:33],v6 offset:49152\n" \
"ds_read_b64_tr_b16 a[34:35],v7 offset:49152\nM v[68:71],a[208:211],a[128:131],v[32:35]\nds_read_b64_tr_b16 a[36:37],v8 offset:49152\nC v121,v122,v123\nM v[76:79],a[208:211],a[144:147],v[36:39]\nds_read_b64_tr_b16 a[38:39],v9 offset:49152\nv_xor_b32 v14,0x10000,v14\nM v[84:87],a[208:211],a[160:163],v[40:43]\n" \
"v_exp_f32 v124,v124\nM v[92:95],a[208:211],a[176:179],v[44:47]\nv_exp_f32 v125,v125\nM v[68:71],a[212:215],a[132:135],v[68:71]\nv_exp_f32 v126,v126\nM v[76:79],a[212:215],a[148:151],v[76:79]\nv_exp_f32 v127,v127\nM v[84:87],a[212:215],a[164:167],v[84:87]\n" \
"ds_read_b128 a[192:195],v14 offset:32768\nC v122,v124,v125\nC v123,v126,v127\nM v[92:95],a[212:215],a[180:183],v[92:95]\nbuffer_load_dwordx4 v22,s[40:43],s45 offen offset:3072 sc1 lds\nM v[24:27],a[236:239],v[120:123],v[24:27]\ns_add_u32 m0,s22,49152\nds_read_b128 a[196:199],v14 offset:33792\n" \
"ds_read_b128 a[200:203],v14 offset:34816\nM v[68:71],a[216:219],a[136:139],v[68:71]\ns_add_u32 s45,s45,s49\nds_read_b64_tr_b16 a[40:41],v6 offset:51200\nv_xor_b32 v6,0x10000,v6\nM v[76:79],a[216:219],a[152:155],v[76:79]\nds_read_b64_tr_b16 a[42:43],v7 offset:51200\nv_max3_f32 v15,v24,v25,v26\n" \
"M v[84:87],a[216:219],a[168:171],v[84:87]\nbuffer_store_dwordx4 v[48:51],v4,s[36:39],s72 offen offset:3072 sc1\nM v[92:95],a[216:219],a[184:187],v[92:95]\nv_max_f32 v15,v15,v27\nv_cmp_nge_f32 vcc,0x53800000,v15\nM v[68:71],a[220:223],a[140:143],v[68:71]\nds_read_b64_tr_b16 a[44:45],v8 offset:51200\nds_read_b64_tr_b16 a[46:47],v9 offset:51200\n" \
"M v[76:79],a[220:223],a[156:159],v[76:79]\ns_cbranch_vccnz Lcs53_%=\nLcr54_%=:\nds_read_b128 a[204:207],v14 offset:35840\nv_xor_b32 v7,0x10000,v7\nM v[84:87],a[220:223],a[172:175],v[84:87]\nds_read_b128 a[208:211],v14 offset:36864\nv_xor_b32 v8,0x10000,v8\n" \
"M v[92:95],a[220:223],a[188:191],v[92:95]\ns_waitcnt lgkmcnt(4)\nds_read_b128 a[212:215],v14 offset:37888\nv_xor_b32 v9,0x10000,v9\nM v[128:131],a[192:195],v[96:99],v[128:131]\nds_read_b128 a[216:219],v14 offset:38912\nds_read_b128 a[220:223],v14 offset:39936\nM v[160:163],a[192:195],v[104:107],v[160:163]\n" \
"v_exp_f32 v64,v64\nM v[192:195],a[192:195],v[112:115],v[192:195]\nv_exp_f32 v65,v65\nM v[224:227],a[192:195],v[120:123],v[224:227]\nv_exp_f32 v66,v66\nM v[132:135],a[196:199],v[96:99],v[132:135]\nv_exp_f32 v67,v67\nM v[164:167],a[196:199],v[104:107],v[164:167]\n" \
"v_exp_f32 v68,v68\nM v[196:199],a[196:199],v[112:115],v[196:199]\ns_waitcnt vmcnt(9) lgkmcnt(0)\nv_exp_f32 v69,v69\nM v[228:231],a[196:199],v[120:123],v[228:231]\ns_barrier\nds_read_b128 a[192:195],v13 offset:49152\nM v[136:139],a[200:203],v[96:99],v[136:139]\n" \
"buffer_load_dwordx4 v22,s[40:43],s48 offen offset:0 sc1 lds\nM v[168:171],a[200:203],v[104:107],v[168:171]\nv_exp_f32 v70,v70\nM v[200:203],a[200:203],v[112:115],v[200:203]\nv_exp_f32 v71,v71\nM v[232:235],a[200:203],v[120:123],v[232:235]\nv_exp_f32 v72,v72\nM v[140:143],a[204:207],v[96:99],v[140:143]\n" \
"buffer_load_dwordx4 v22,s[40:43],s48 offen offset:1024 sc1 lds\nM v[172:175],a[204:207],v[104:107],v[172:175]\nv_exp_f32 v73,v73\nM v[204:207],a[204:207],v[112:115],v[204:207]\nv_exp_f32 v74,v74\nM v[236:239],a[204:207],v[120:123],v[236:239]\nv_exp_f32 v75,v75\nM v[144:147],a[208:211],v[96:99],v[144:147]\n" \
"buffer_load_dwordx4 v22,s[40:43],s48 offen offset:2048 sc1 lds\nM v[176:179],a[208:211],v[104:107],v[176:179]\nds_read_b128 a[196:199],v13 offset:51200\nC v64,v64,v65\nM v[208:211],a[208:211],v[112:115],v[208:211]\nv_exp_f32 v76,v76\nM v[240:243],a[208:211],v[120:123],v[240:243]\nv_exp_f32 v77,v77\n" \
"M v[148:151],a[212:215],v[96:99],v[148:151]\nbuffer_load_dwordx4 v22,s[40:43],s48 offen offset:3072 sc1 lds\nM v[180:183],a[212:215],v[104:107],v[180:183]\ns_add_u32 m0,s22,65536\nv_exp_f32 v78,v78\nM v[212:215],a[212:215],v[112:115],v[212:215]\ns_add_u32 s52,s52,1\nds_read_b128 a[200:203],v13 offset:53248\n" \
"C v65,v66,v67\nM v[244:247],a[212:215],v[120:123],v[244:247]\ns_add_u32 s48,s48,s49\nv_exp_f32 v79,v79\nM v[152:155],a[216:219],v[96:99],v[152:155]\nbuffer_load_dwordx4 v1,s[84:87],s46 offen offset:0 lds\nM v[184:187],a[216:219],v[104:107],v[184:187]\ns_add_u32 m0,s22,66432\n" \
"ds_read_b128 a[204:207],v13 offset:55296\nC v66,v68,v69\nM v[216:219],a[216:219],v[112:115],v[216:219]\nv_exp_f32 v80,v80\nM v[248:251],a[216:219],v[120:123],v[248:251]\ns_waitcnt lgkmcnt(1)\nv_exp_f32 v81,v81\nM v[156:159],a[220:223],v[96:99],v[156:159]\n" \
"buffer_load_dwordx4 v1,s[84:87],s46 offen offset:128 lds\nM v[188:191],a[220:223],v[104:107],v[188:191]\ns_add_u32 m0,s22,67584\nv_exp_f32 v82,v82\nM v[220:223],a[220:223],v[112:115],v[220:223]\ns_cmp_eq_u32 s52,s56\nds_read_b128 a[208:211],v13 offset:50176\nC v67,v70,v71\n" \
"M v[252:255],a[220:223],v[120:123],v[252:255]\ns_cbranch_scc1 LEV_%=\nLEVR12_%=:\nv_exp_f32 v83,v83\nM v[96:99],a[192:195],a[128:131],v[32:35]\ns_mov_b32 s46,720896\nv_exp_f32 v84,v84\nM v[104:107],a[192:195],a[144:147],v[36:39]\n" \
"buffer_load_dwordx4 v1,s[84:87],s46 offen offset:0 lds\nM v[16:19],a[224:227],v[64:67],v[24:27]\ns_add_u32 m0,s22,68480\nv_exp_f32 v85,v85\nM v[112:115],a[192:195],a[160:163],v[40:43]\nds_read_b128 a[212:215],v13 offset:52224\nC v72,v72,v73\nM v[120:123],a[192:195],a[176:179],v[44:47]\n" \
"v_exp_f32 v86,v86\nM v[96:99],a[196:199],a[132:135],v[96:99]\nC v73,v74,v75\nC v74,v76,v77\nC v75,v78,v79\nM v[104:107],a[196:199],a[148:151],v[104:107]\nbuffer_load_dwordx4 v1,s[84:87],s46 offen offset:128 lds\nM v[16:19],a[228:231],v[72:75],v[16:19]\n" \
"s_add_u32 m0,s22,98304\nv_exp_f32 v87,v87\nM v[112:115],a[196:199],a[164:167],v[112:115]\ns_mov_b32 s46,786432\nds_read_b128 a[216:219],v13 offset:54272\nC v80,v80,v81\nM v[120:123],a[196:199],a[180:183],v[120:123]\nv_exp_f32 v88,v88\n" \
"M v[96:99],a[200:203],a[136:139],v[96:99]\nbuffer_load_dwordx4 v22,s[40:43],s45 offen offset:0 sc1 lds\nM v[104:107],a[200:203],a[152:155],v[104:107]\nds_read_b128 a[220:223],v13 offset:56320\nC v81,v82,v83\nM v[112:115],a[200:203],a[168:171],v[112:115]\nv_exp_f32 v89,v89\nM v[120:123],a[200:203],a[184:187],v[120:123]\n" \
"s_waitcnt lgkmcnt(1)\nv_exp_f32 v90,v90\nM v[96:99],a[204:207],a[140:143],v[96:99]\nbuffer_load_dwordx4 v22,s[40:43],s45 offen offset:1024 sc1 lds\nM v[104:107],a[204:207],a[156:159],v[104:107]\nv_exp_f32 v91,v91\nM v[112:115],a[204:207],a[172:175],v[112:115]\nC v82,v84,v85\n" \
"C v83,v86,v87\nM v[120:123],a[204:207],a[188:191],v[120:123]\nv_exp_f32 v92,v92\nM v[16:19],a[232:235],v[80:83],v[16:19]\nbuffer_load_dwordx4 v22,s[40:43],s45 offen offset:2048 sc1 lds\nM v[100:103],a[208:211],a[128:131],v[32:35]\nds_read_b64_tr_b16 a[48:49],v6 offset:16384\nds_read_b64_tr_b16 a[50:51],v7 offset:16384\n" \
"M v[108:111],a[208:211],a[144:147],v[36:39]\nds_read_b64_tr_b16 a[52:53],v8 offset:16384\nC v88,v88,v89\nM v[116:119],a[208:211],a[160:163],v[40:43]\nds_read_b64_tr_b16 a[54:55],v9 offset:16384\nC v89,v90,v91\nM v[124:127],a[208:211],a[176:179],v[44:47]\nv_exp_f32 v93,v93\n" \
"M v[100:103],a[212:215],a[132:135],v[100:103]\nv_exp_f32 v94,v94\nM v[108:111],a[212:215],a[148:151],v[108:111]\nv_exp_f32 v95,v95\nv_xor_b32 v14,0x10000,v14\nM v[116:119],a[212:215],a[164:167],v[116:119]\nds_read_b128 a[192:195],v14 offset:0\nC v90,v92,v93\n" \
"C v91,v94,v95\nM v[124:127],a[212:215],a[180:183],v[124:127]\nbuffer_load_dwordx4 v22,s[40:43],s45 offen offset:3072 sc1 lds\nM v[16:19],a[236:239],v[88:91],v[16:19]\ns_add_u32 m0,s22,81920\nds_read_b128 a[196:199],v14 offset:1024\nds_read_b128 a[200:203],v14 offset:2048\nM v[100:103],a[216:219],a[136:139],v[100:103]\n" \
"s_add_u32 s45,s45,s49\nds_read_b64_tr_b16 a[56:57],v6 offset:18432\nv_xor_b32 v13,0x10000,v13\nM v[108:111],a[216:219],a[152:155],v[108:111]\nds_read_b64_tr_b16 a[58:59],v7 offset:18432\nv_max3_f32 v15,v16,v17,v18\nM v[116:119],a[216:219],a[168:171],v[116:119]\nds_read_b64_tr_b16 a[60:61],v8 offset:18432\n" \
"v_max_f32 v15,v15,v19\nM v[124:127],a[216:219],a[184:187],v[124:127]\ns_waitcnt lgkmcnt(5)\nds_read_b64_tr_b16 a[62:63],v9 offset:18432\nv_cmp_nge_f32 vcc,0x53800000,v15\nM v[100:103],a[220:223],a[140:143],v[100:103]\nds_read_b128 a[204:207],v14 offset:3072\nds_read_b128 a[208:211],v14 offset:4096\n" \
"M v[108:111],a[220:223],a[156:159],v[108:111]\ns_cbranch_vccnz Lcs55_%=\nLcr56_%=:\nds_read_b128 a[212:215],v14 offset:5120\nv_xor_b32 v6,0x10000,v6\nM v[116:119],a[220:223],a[172:175],v[116:119]\nds_read_b128 a[216:219],v14 offset:6144\nv_xor_b32 v7,0x10000,v7\n" \
"M v[124:127],a[220:223],a[188:191],v[124:127]\nds_read_b128 a[220:223],v14 offset:7168\nv_xor_b32 v8,0x10000,v8\nM v[128:131],a[192:195],v[64:67],v[128:131]\nv_exp_f32 v96,v96\nM v[160:163],a[192:195],v[72:75],v[160:163]\nv_exp_f32 v97,v97\nM v[192:195],a[192:195],v[80:83],v[192:195]\n" \
"v_exp_f32 v98,v98\nM v[224:227],a[192:195],v[88:91],v[224:227]\ns_waitcnt lgkmcnt(1)\nv_exp_f32 v99,v99\nM v[132:135],a[196:199],v[64:67],v[132:135]\nv_exp_f32 v100,v100\nM v[164:167],a[196:199],v[72:75],v[164:167]\nv_exp_f32 v101,v101\n" \
"M v[196:199],a[196:199],v[80:83],v[196:199]\ns_waitcnt vmcnt(8) lgkmcnt(0)\nv_exp_f32 v102,v102\nM v[228:231],a[196:199],v[88:91],v[228:231]\ns_barrier\nds_read_b128 a[192:195],v13 offset:16384\nM v[136:139],a[200:203],v[64:67],v[136:139]\nbuffer_load_dwordx4 v22,s[40:43],s48 offen offset:0 sc1 lds\n" \
"M v[168:171],a[200:203],v[72:75],v[168:171]\nv_exp_f32 v103,v103\nM v[200:203],a[200:203],v[80:83],v[200:203]\nv_exp_f32 v104,v104\nM v[232:235],a[200:203],v[88:91],v[232:235]\nv_exp_f32 v105,v105\nM v[140:143],a[204:207],v[64:67],v[140:143]\nbuffer_load_dwordx4 v22,s[40:43],s48 offen offset:1024 sc1 lds\n" \
"M v[172:175],a[204:207],v[72:75],v[172:175]\nv_exp_f32 v106,v106\nM v[204:207],a[204:207],v[80:83],v[204:207]\nv_exp_f32 v107,v107\nM v[236:239],a[204:207],v[88:91],v[236:239]\nv_exp_f32 v108,v108\nM v[144:147],a[208:211],v[64:67],v[144:147]\nbuffer_load_dwordx4 v22,s[40:43],s48 offen offset:2048 sc1 lds\n" \
"M v[176:179],a[208:211],v[72:75],v[176:179]\nds_read_b128 a[196:199],v13 offset:18432\nC v96,v96,v97\nM v[208:211],a[208:211],v[80:83],v[208:211]\nv_exp_f32 v109,v109\nM v[240:243],a[208:211],v[88:91],v[240:243]\nv_exp_f32 v110,v110\nM v[148:151],a[212:215],v[64:67],v[148:151]\n" \
"buffer_load_dwordx4 v22,s[40:43],s48 offen offset:3072 sc1 lds\nM v[180:183],a[212:215],v[72:75],v[180:183]\ns_add_u32 m0,s22,114688\nds_read_b128 a[200:203],v13 offset:20480\nC v97,v98,v99\nM v[212:215],a[212:215],v[80:83],v[212:215]\ns_add_u32 s48,s48,s49\nv_exp_f32 v111,v111\n" \
"M v[244:247],a[212:215],v[88:91],v[244:247]\nv_exp_f32 v112,v112\nM v[152:155],a[216:219],v[64:67],v[152:155]\nbuffer_load_dwordx4 v1,s[84:87],s46 offen offset:0 lds\nM v[184:187],a[216:219],v[72:75],v[184:187]\ns_add_u32 m0,s22,115584\nv_exp_f32 v113,v113\nM v[216:219],a[216:219],v[80:83],v[216:219]\n" \
"ds_read_b128 a[204:207],v13 offset:22528\nC v98,v100,v101\nM v[248:251],a[216:219],v[88:91],v[248:251]\nv_exp_f32 v114,v114\nM v[156:159],a[220:223],v[64:67],v[156:159]\nbuffer_load_dwordx4 v1,s[84:87],s46 offen offset:128 lds\nM v[188:191],a[220:223],v[72:75],v[188:191]\ns_add_u32 m0,s22,116736\n" \
"v_exp_f32 v115,v115\nM v[220:223],a[220:223],v[80:83],v[220:223]\ns_mov_b32 s46,851968\nv_exp_f32 v116,v116\nM v[252:255],a[220:223],v[88:91],v[252:255]\ns_waitcnt lgkmcnt(0)\nv_exp_f32 v117,v117\nM v[64:67],a[192:195],a[128:131],v[32:35]\n" \
"ds_read_b128 a[208:211],v13 offset:17408\nC v99,v102,v103\nM v[72:75],a[192:195],a[144:147],v[36:39]\nLG19_%=:\ns_cbranch_execz LG23_%=\nbuffer_load_dwordx2 v[10:11],v5,s[36:39],0 offen sc1\nM v[24:27],a[224:227],v[96:99],v[16:19]\nv_exp_f32 v118,v118\n" \
"M v[80:83],a[192:195],a[160:163],v[40:43]\nds_read_b128 a[212:215],v13 offset:19456\nC v104,v104,v105\nM v[88:91],a[192:195],a[176:179],v[44:47]\nv_exp_f32 v119,v119\nM v[64:67],a[196:199],a[132:135],v[64:67]\nC v105,v106,v107\nC v106,v108,v109\n" \
"C v107,v110,v111\nM v[72:75],a[196:199],a[148:151],v[72:75]\nbuffer_load_dwordx4 v1,s[84:87],s46 offen offset:0 lds\nM v[24:27],a[228:231],v[104:107],v[24:27]\ns_add_u32 m0,s22,117632\nds_read_b128 a[216:219],v13 offset:21504\nC v112,v112,v113\nM v[80:83],a[196:199],a[164:167],v[80:83]\n" \
"v_exp_f32 v120,v120\nM v[88:91],a[196:199],a[180:183],v[88:91]\nv_exp_f32 v121,v121\nM v[64:67],a[200:203],a[136:139],v[64:67]\nbuffer_load_dwordx4 v1,s[84:87],s46 offen offset:128 lds\nM v[72:75],a[200:203],a[152:155],v[72:75]\ns_add_u32 m0,s22,0\nv_exp_f32 v122,v122\n" \
"M v[80:83],a[200:203],a[168:171],v[80:83]\ns_mov_b32 s46,917504\nds_read_b128 a[220:223],v13 offset:23552\nC v113,v114,v115\nM v[88:91],a[200:203],a[184:187],v[88:91]\nv_exp_f32 v123,v123\nM v[64:67],a[204:207],a[140:143],v[64:67]\nbuffer_load_dwordx4 v22,s[40:43],s45 offen offset:0 sc1 lds\n" \
"M v[72:75],a[204:207],a[156:159],v[72:75]\ns_waitcnt vmcnt(22)\nds_read_b128 a[192:195],v14 offset:49152\nC v114,v116,v117\nM v[80:83],a[204:207],a[172:175],v[80:83]\nC v115,v118,v119\nC v120,v120,v121\nM v[88:91],a[204:207],a[188:191],v[88:91]\n" \
"s_waitcnt lgkmcnt(1)\nds_read_b128 a[196:199],v14 offset:50176\nds_read_b128 a[200:203],v14 offset:51200\nM v[24:27],a[232:235],v[112:115],v[24:27]\nbuffer_store_dwordx2 v[52:53],v56,s[36:39],s95 offen offset:24 sc1\nM v[68:71],a[208:211],a[128:131],v[32:35]\nds_read_b64_tr_b16 a[64:65],v6 offset:32768\nds_read_b64_tr_b16 a[66:67],v7 offset:32768\n" \
"M v[76:79],a[208:211],a[144:147],v[36:39]\nds_read_b64_tr_b16 a[68:69],v8 offset:32768\nC v121,v122,v123\nM v[84:87],a[208:211],a[160:163],v[40:43]\nv_exp_f32 v124,v124\nM v[92:95],a[208:211],a[176:179],v[44:47]\nv_exp_f32 v125,v125\nM v[68:71],a[212:215],a[132:135],v[68:71]\n" \
"v_exp_f32 v126,v126\nM v[76:79],a[212:215],a[148:151],v[76:79]\nv_exp_f32 v127,v127\nM v[84:87],a[212:215],a[164:167],v[84:87]\nC v122,v124,v125\nC v123,v126,v127\nM v[92:95],a[212:215],a[180:183],v[92:95]\nbuffer_load_dwordx4 v22,s[40:43],s45 offen offset:1024 sc1 lds\n" \
"M v[24:27],a[236:239],v[120:123],v[24:27]\nds_read_b64_tr_b16 a[72:73],v6 offset:34816\nds_read_b64_tr_b16 a[74:75],v7 offset:34816\nM v[68:71],a[216:219],a[136:139],v[68:71]\nds_read_b64_tr_b16 a[76:77],v8 offset:34816\nv_xor_b32 v9,0x10000,v9\nM v[76:79],a[216:219],a[152:155],v[76:79]\nds_read_b64_tr_b16 a[70:71],v9 offset:32768\n" \
"ds_read_b64_tr_b16 a[78:79],v9 offset:34816\nM v[84:87],a[216:219],a[168:171],v[84:87]\nbuffer_load_dwordx4 v22,s[40:43],s45 offen offset:2048 sc1 lds\nM v[92:95],a[216:219],a[184:187],v[92:95]\ns_waitcnt vmcnt(6)\nv_max3_f32 v15,v24,v25,v26\nv_max_f32 v15,v15,v27\nv_cmp_nge_f32 vcc,0x53800000,v15\n" \
"M v[68:71],a[220:223],a[140:143],v[68:71]\ns_waitcnt lgkmcnt(3)\nds_read_b128 a[204:207],v14 offset:52224\nds_read_b128 a[208:211],v14 offset:53248\nM v[76:79],a[220:223],a[156:159],v[76:79]\ns_cbranch_vccnz Lcs57_%=\nLcr58_%=:\nds_read_b128 a[212:215],v14 offset:54272\n" \
"v_cmp_gt_u64 s[98:99],s[14:15],v[10:11]\nM v[84:87],a[220:223],a[172:175],v[84:87]\nbuffer_load_dwordx4 v22,s[40:43],s45 offen offset:3072 sc1 lds\nM v[92:95],a[220:223],a[188:191],v[92:95]\ns_add_u32 m0,s22,32768\nds_read_b128 a[216:219],v14 offset:55296\nds_read_b128 a[220:223],v14 offset:56320\nM v[128:131],a[192:195],v[96:99],v[128:131]\n" \
"s_add_u32 s45,s45,s49\nv_cmp_le_u64 s[18:19],s[16:17],v[10:11]\nv_xor_b32 v13,0x10000,v13\nM v[160:163],a[192:195],v[104:107],v[160:163]\ns_or_b64 s[98:99],s[98:99],s[18:19]\ns_and_b32 s98,s98,0xffff\ns_cbranch_scc1 LPE_%=\nLPER_%=:\n" \
"v_exp_f32 v64,v64\nM v[192:195],a[192:195],v[112:115],v[192:195]\nv_exp_f32 v65,v65\nM v[224:227],a[192:195],v[120:123],v[224:227]\nv_exp_f32 v66,v66\nM v[132:135],a[196:199],v[96:99],v[132:135]\nv_exp_f32 v67,v67\nM v[164:167],a[196:199],v[104:107],v[164:167]\n" \
"v_exp_f32 v68,v68\nM v[196:199],a[196:199],v[112:115],v[196:199]\ns_waitcnt vmcnt(10) lgkmcnt(0)\nv_exp_f32 v69,v69\nM v[228:231],a[196:199],v[120:123],v[228:231]\ns_barrier\nds_read_b128 a[192:195],v13 offset:32768\nM v[136:139],a[200:203],v[96:99],v[136:139]\n" \
"buffer_load_dwordx4 v22,s[40:43],s48 offen offset:0 sc1 lds\nM v[168:171],a[200:203],v[104:107],v[168:171]\nv_exp_f32 v70,v70\nM v[200:203],a[200:203],v[112:115],v[200:203]\nv_exp_f32 v71,v71\nM v[232:235],a[200:203],v[120:123],v[232:235]\nv_exp_f32 v72,v72\nM v[140:143],a[204:207],v[96:99],v[140:143]\n" \
"buffer_load_dwordx4 v22,s[40:43],s48 offen offset:1024 sc1 lds\nM v[172:175],a[204:207],v[104:107],v[172:175]\nv_exp_f32 v73,v73\nM v[204:207],a[204:207],v[112:115],v[204:207]\nv_exp_f32 v74,v74\nM v[236:239],a[204:207],v[120:123],v[236:239]\nv_exp_f32 v75,v75\nM v[144:147],a[208:211],v[96:99],v[144:147]\n" \
"buffer_load_dwordx4 v22,s[40:43],s48 offen offset:2048 sc1 lds\nM v[176:179],a[208:211],v[104:107],v[176:179]\nds_read_b128 a[196:199],v13 offset:34816\nC v64,v64,v65\nM v[208:211],a[208:211],v[112:115],v[208:211]\nv_exp_f32 v76,v76\nM v[240:243],a[208:211],v[120:123],v[240:243]\nv_exp_f32 v77,v77\n" \
"M v[148:151],a[212:215],v[96:99],v[148:151]\nbuffer_load_dwordx4 v22,s[40:43],s48 offen offset:3072 sc1 lds\nM v[180:183],a[212:215],v[104:107],v[180:183]\ns_add_u32 m0,s22,16384\nv_exp_f32 v78,v78\nM v[212:215],a[212:215],v[112:115],v[212:215]\ns_add_u32 s52,s52,1\nds_read_b128 a[200:203],v13 offset:36864\n" \
"C v65,v66,v67\nM v[244:247],a[212:215],v[120:123],v[244:247]\ns_add_u32 s48,s48,s49\nv_exp_f32 v79,v79\nM v[152:155],a[216:219],v[96:99],v[152:155]\nbuffer_load_dwordx4 v1,s[84:87],s46 offen offset:0 lds\nM v[184:187],a[216:219],v[104:107],v[184:187]\ns_add_u32 m0,s22,17280\n" \
"ds_read_b128 a[204:207],v13 offset:38912\nC v66,v68,v69\nM v[216:219],a[216:219],v[112:115],v[216:219]\nv_exp_f32 v80,v80\nM v[248:251],a[216:219],v[120:123],v[248:251]\ns_waitcnt lgkmcnt(1)\nv_exp_f32 v81,v81\nM v[156:159],a[220:223],v[96:99],v[156:159]\n" \
"buffer_load_dwordx4 v1,s[84:87],s46 offen offset:128 lds\nM v[188:191],a[220:223],v[104:107],v[188:191]\ns_add_u32 m0,s22,18432\nv_exp_f32 v82,v82\nM v[220:223],a[220:223],v[112:115],v[220:223]\ns_cmp_eq_u32 s52,s56\nds_read_b128 a[208:211],v13 offset:33792\nC v67,v70,v71\n" \
"M v[252:255],a[220:223],v[120:123],v[252:255]\ns_cbranch_scc1 LEV_%=\nLEVR13_%=:\nv_exp_f32 v83,v83\nM v[96:99],a[192:195],a[128:131],v[32:35]\ns_mov_b32 s46,983040\nv_exp_f32 v84,v84\nM v[104:107],a[192:195],a[144:147],v[36:39]\n" \
"buffer_load_dwordx4 v1,s[84:87],s46 offen offset:0 lds\nM v[16:19],a[224:227],v[64:67],v[24:27]\ns_add_u32 m0,s22,19328\nv_exp_f32 v85,v85\nM v[112:115],a[192:195],a[160:163],v[40:43]\nds_read_b128 a[212:215],v13 offset:35840\nC v72,v72,v73\nM v[120:123],a[192:195],a[176:179],v[44:47]\n" \
"v_exp_f32 v86,v86\nM v[96:99],a[196:199],a[132:135],v[96:99]\nC v73,v74,v75\nC v74,v76,v77\nC v75,v78,v79\nM v[104:107],a[196:199],a[148:151],v[104:107]\nbuffer_load_dwordx4 v1,s[84:87],s46 offen offset:128 lds\nM v[16:19],a[228:231],v[72:75],v[16:19]\n" \
"s_add_u32 m0,s22,49152\nv_exp_f32 v87,v87\nM v[112:115],a[196:199],a[164:167],v[112:115]\ns_mov_b32 s46,1048576\nds_read_b128 a[216:219],v13 offset:37888\nC v80,v80,v81\nM v[120:123],a[196:199],a[180:183],v[120:123]\nv_exp_f32 v88,v88\n" \
"M v[96:99],a[200:203],a[136:139],v[96:99]\nbuffer_load_dwordx4 v22,s[40:43],s45 offen offset:0 sc1 lds\nM v[104:107],a[200:203],a[152:155],v[104:107]\nds_read_b128 a[220:223],v13 offset:39936\nC v81,v82,v83\nM v[112:115],a[200:203],a[168:171],v[112:115]\nv_exp_f32 v89,v89\nM v[120:123],a[200:203],a[184:187],v[120:123]\n" \
"s_waitcnt lgkmcnt(1)\nv_exp_f32 v90,v90\nM v[96:99],a[204:207],a[140:143],v[96:99]\nbuffer_load_dwordx4 v22,s[40:43],s45 offen offset:1024 sc1 lds\nM v[104:107],a[204:207],a[156:159],v[104:107]\nv_exp_f32 v91,v91\nM v[112:115],a[204:207],a[172:175],v[112:115]\nC v82,v84,v85\n" \
"C v83,v86,v87\nM v[120:123],a[204:207],a[188:191],v[120:123]\nv_exp_f32 v92,v92\nM v[16:19],a[232:235],v[80:83],v[16:19]\nbuffer_load_dwordx4 v22,s[40:43],s45 offen offset:2048 sc1 lds\nM v[100:103],a[208:211],a[128:131],v[32:35]\nv_exp_f32 v93,v93\nM v[108:111],a[208:211],a[144:147],v[36:39]\n" \
"v_exp_f32 v94,v94\nM v[116:119],a[208:211],a[160:163],v[40:43]\nC v88,v88,v89\nC v89,v90,v91\nM v[124:127],a[208:211],a[176:179],v[44:47]\nbuffer_load_dwordx4 v22,s[40:43],s45 offen offset:3072 sc1 lds\nM v[100:103],a[212:215],a[132:135],v[100:103]\ns_add_u32 m0,s22,65536\n" \
"v_exp_f32 v95,v95\nM v[108:111],a[212:215],a[148:151],v[108:111]\nv_xor_b32 v14,0x10000,v14\nC v90,v92,v93\nM v[116:119],a[212:215],a[164:167],v[116:119]\nds_read_b128 a[192:195],v14 offset:16384\nC v91,v94,v95\nM v[124:127],a[212:215],a[180:183],v[124:127]\n" \
"ds_read_b128 a[196:199],v14 offset:17408\nds_read_b128 a[200:203],v14 offset:18432\nM v[16:19],a[236:239],v[88:91],v[16:19]\nds_read_b128 a[204:207],v14 offset:19456\nv_xor_b32 v6,0x10000,v6\nM v[100:103],a[216:219],a[136:139],v[100:103]\nds_read_b64_tr_b16 a[80:81],v6 offset:0\nv_xor_b32 v7,0x10000,v7\n" \
"M v[108:111],a[216:219],a[152:155],v[108:111]\nds_read_b64_tr_b16 a[82:83],v7 offset:0\nv_xor_b32 v8,0x10000,v8\nM v[116:119],a[216:219],a[168:171],v[116:119]\nds_read_b64_tr_b16 a[84:85],v8 offset:0\nv_xor_b32 v9,0x10000,v9\nM v[124:127],a[216:219],a[184:187],v[124:127]\ns_waitcnt lgkmcnt(4)\n" \
"v_max3_f32 v15,v16,v17,v18\nv_max_f32 v15,v15,v19\nv_cmp_nge_f32 vcc,0x53800000,v15\nM v[100:103],a[220:223],a[140:143],v[100:103]\nds_read_b64_tr_b16 a[86:87],v9 offset:0\nds_read_b64_tr_b16 a[88:89],v6 offset:2048\nM v[108:111],a[220:223],a[156:159],v[108:111]\ns_cbranch_vccnz Lcs59_%=\n" \
"Lcr60_%=:\nds_read_b64_tr_b16 a[90:91],v7 offset:2048\nM v[116:119],a[220:223],a[172:175],v[116:119]\ns_cmp_eq_u32 s52,s55\nds_read_b64_tr_b16 a[92:93],v8 offset:2048\nds_read_b64_tr_b16 a[94:95],v9 offset:2048\nM v[124:127],a[220:223],a[188:191],v[124:127]\ns_cbranch_scc1 LH1_%=\n" \
"LH1R14_%=:\nds_read_b128 a[208:211],v14 offset:20480\nv_xor_b32 v13,0x10000,v13\nM v[128:131],a[192:195],v[64:67],v[128:131]\nds_read_b128 a[212:215],v14 offset:21504\nds_read_b128 a[216:219],v14 offset:22528\nds_read_b128 a[220:223],v14 offset:23552\nM v[160:163],a[192:195],v[72:75],v[160:163]\n" \
"v_exp_f32 v96,v96\nM v[192:195],a[192:195],v[80:83],v[192:195]\nv_exp_f32 v97,v97\nM v[224:227],a[192:195],v[88:91],v[224:227]\nv_exp_f32 v98,v98\nM v[132:135],a[196:199],v[64:67],v[132:135]\nv_exp_f32 v99,v99\nM v[164:167],a[196:199],v[72:75],v[164:167]\n" \
"v_exp_f32 v100,v100\nM v[196:199],a[196:199],v[80:83],v[196:199]\ns_waitcnt vmcnt(8) lgkmcnt(0)\nv_exp_f32 v101,v101\nM v[228:231],a[196:199],v[88:91],v[228:231]\ns_barrier\nds_read_b128 a[192:195],v13 offset:0\nM v[136:139],a[200:203],v[64:67],v[136:139]\n" \
"buffer_load_dwordx4 v22,s[40:43],s48 offen offset:0 sc1 lds\nM v[168:171],a[200:203],v[72:75],v[168:171]\nv_exp_f32 v102,v102\nM v[200:203],a[200:203],v[80:83],v[200:203]\nv_exp_f32 v103,v103\nM v[232:235],a[200:203],v[88:91],v[232:235]\nv_exp_f32 v104,v104\nM v[140:143],a[204:207],v[64:67],v[140:143]\n" \
"buffer_load_dwordx4 v22,s[40:43],s48 offen offset:1024 sc1 lds\nM v[172:175],a[204:207],v[72:75],v[172:175]\nv_exp_f32 v105,v105\nM v[204:207],a[204:207],v[80:83],v[204:207]\nv_exp_f32 v106,v106\nM v[236:239],a[204:207],v[88:91],v[236:239]\nv_exp_f32 v107,v107\nM v[144:147],a[208:211],v[64:67],v[144:147]\n" \
"buffer_load_dwordx4 v22,s[40:43],s48 offen offset:2048 sc1 lds\nM v[176:179],a[208:211],v[72:75],v[176:179]\nds_read_b128 a[196:199],v13 offset:2048\nC v96,v96,v97\nM v[208:211],a[208:211],v[80:83],v[208:211]\nv_exp_f32 v108,v108\nM v[240:243],a[208:211],v[88:91],v[240:243]\nv_exp_f32 v109,v109\n" \
"M v[148:151],a[212:215],v[64:67],v[148:151]\nbuffer_load_dwordx4 v22,s[40:43],s48 offen offset:3072 sc1 lds\nM v[180:183],a[212:215],v[72:75],v[180:183]\ns_add_u32 m0,s22,98304\nv_exp_f32 v110,v110\nM v[212:215],a[212:215],v[80:83],v[212:215]\ns_add_u32 s48,s48,s49\nds_read_b128 a[200:203],v13 offset:4096\n" \
"C v97,v98,v99\nM v[244:247],a[212:215],v[88:91],v[244:247]\nv_exp_f32 v111,v111\nM v[152:155],a[216:219],v[64:67],v[152:155]\nbuffer_load_dwordx4 v22,s[40:43],s48 offen offset:0 sc1 lds\nM v[184:187],a[216:219],v[72:75],v[184:187]\nds_read_b128 a[204:207],v13 offset:6144\nC v98,v100,v101\n" \
"M v[216:219],a[216:219],v[80:83],v[216:219]\nv_exp_f32 v112,v112\nM v[248:251],a[216:219],v[88:91],v[248:251]\nv_exp_f32 v113,v113\nM v[156:159],a[220:223],v[64:67],v[156:159]\nbuffer_load_dwordx4 v22,s[40:43],s48 offen offset:1024 sc1 lds\nM v[188:191],a[220:223],v[72:75],v[188:191]\nv_exp_f32 v114,v114\n" \
"M v[220:223],a[220:223],v[80:83],v[220:223]\nv_exp_f32 v115,v115\nM v[252:255],a[220:223],v[88:91],v[252:255]\ns_waitcnt lgkmcnt(0)\nds_read_b128 a[208:211],v13 offset:1024\nC v99,v102,v103\nM v[64:67],a[192:195],a[128:131],v[32:35]\nbuffer_load_dwordx4 v22,s[40:43],s48 offen offset:2048 sc1 lds\n" \
"M v[72:75],a[192:195],a[144:147],v[36:39]\nv_exp_f32 v116,v116\nM v[24:27],a[224:227],v[96:99],v[16:19]\nv_exp_f32 v117,v117\nM v[80:83],a[192:195],a[160:163],v[40:43]\nC v104,v104,v105\nC v105,v106,v107\nM v[88:91],a[192:195],a[176:179],v[44:47]\n" \
"buffer_load_dwordx4 v22,s[40:43],s48 offen offset:3072 sc1 lds\nM v[64:67],a[196:199],a[132:135],v[64:67]\ns_add_u32 m0,s22,81920\nC v106,v108,v109\nC v107,v110,v111\nM v[72:75],a[196:199],a[148:151],v[72:75]\nds_read_b128 a[212:215],v13 offset:3072\nC v112,v112,v113\n" \
"M v[24:27],a[228:231],v[104:107],v[24:27]\nv_exp_f32 v118,v118\nM v[80:83],a[196:199],a[164:167],v[80:83]\nbuffer_load_dwordx4 v2,s[36:39],s47 offen offset:0 sc1 lds\nM v[88:91],a[196:199],a[180:183],v[88:91]\nds_read_b128 a[216:219],v13 offset:5120\nC v113,v114,v115\nM v[64:67],a[200:203],a[136:139],v[64:67]\n" \
"v_exp_f32 v119,v119\nM v[72:75],a[200:203],a[152:155],v[72:75]\nv_exp_f32 v120,v120\nM v[80:83],a[200:203],a[168:171],v[80:83]\nbuffer_load_dwordx4 v2,s[36:39],s47 offen offset:1024 sc1 lds\nM v[88:91],a[200:203],a[184:187],v[88:91]\nds_read_b128 a[220:223],v13 offset:7168\nC v114,v116,v117\n" \
"M v[64:67],a[204:207],a[140:143],v[64:67]\nv_exp_f32 v121,v121\nM v[72:75],a[204:207],a[156:159],v[72:75]\nv_exp_f32 v122,v122\nM v[80:83],a[204:207],a[172:175],v[80:83]\nC v115,v118,v119\nC v120,v120,v121\nM v[88:91],a[204:207],a[188:191],v[88:91]\n" \
"buffer_load_dwordx4 v2,s[36:39],s47 offen offset:2048 sc1 lds\nM v[24:27],a[232:235],v[112:115],v[24:27]\ns_waitcnt lgkmcnt(0)\nds_read_b64_tr_b16 a[96:97],v6 offset:49152\nds_read_b64_tr_b16 a[98:99],v7 offset:49152\nM v[68:71],a[208:211],a[128:131],v[32:35]\nds_read_b64_tr_b16 a[100:101],v8 offset:49152\nv_xor_b32 v14,0x10000,v14\n" \
"M v[76:79],a[208:211],a[144:147],v[36:39]\nv_exp_f32 v123,v123\nM v[84:87],a[208:211],a[160:163],v[40:43]\nv_exp_f32 v124,v124\nM v[92:95],a[208:211],a[176:179],v[44:47]\nv_exp_f32 v125,v125\nM v[68:71],a[212:215],a[132:135],v[68:71]\nv_exp_f32 v126,v126\n" \
"M v[76:79],a[212:215],a[148:151],v[76:79]\nv_exp_f32 v127,v127\nM v[84:87],a[212:215],a[164:167],v[84:87]\nds_read_b128 a[192:195],v14 offset:32768\nC v121,v122,v123\nC v122,v124,v125\nC v123,v126,v127\nM v[92:95],a[212:215],a[180:183],v[92:95]\n" \
"buffer_load_dwordx4 v2,s[36:39],s47 offen offset:3072 sc1 lds\nM v[24:27],a[236:239],v[120:123],v[24:27]\ns_add_u32 m0,s22,114688\nds_read_b128 a[196:199],v14 offset:33792\nds_read_b128 a[200:203],v14 offset:34816\nM v[68:71],a[216:219],a[136:139],v[68:71]\ns_add_u32 s47,s28,8192\nds_read_b64_tr_b16 a[102:103],v9 offset:49152\n" \
"M v[76:79],a[216:219],a[152:155],v[76:79]\nds_read_b64_tr_b16 a[104:105],v6 offset:51200\nv_max3_f32 v15,v24,v25,v26\nM v[84:87],a[216:219],a[168:171],v[84:87]\nds_read_b64_tr_b16 a[106:107],v7 offset:51200\nv_max_f32 v15,v15,v27\nM v[92:95],a[216:219],a[184:187],v[92:95]\nds_read_b64_tr_b16 a[108:109],v8 offset:51200\n" \
"v_cmp_nge_f32 vcc,0x53800000,v15\nM v[68:71],a[220:223],a[140:143],v[68:71]\nds_read_b64_tr_b16 a[110:111],v9 offset:51200\nds_read_b128 a[204:207],v14 offset:35840\nM v[76:79],a[220:223],a[156:159],v[76:79]\ns_cbranch_vccnz Lcs61_%=\nLcr62_%=:\nds_read_b128 a[208:211],v14 offset:36864\n" \
"v_xor_b32 v6,0x10000,v6\nM v[84:87],a[220:223],a[172:175],v[84:87]\nds_read_b128 a[212:215],v14 offset:37888\nv_xor_b32 v7,0x10000,v7\nM v[92:95],a[220:223],a[188:191],v[92:95]\ns_waitcnt lgkmcnt(5)\nds_read_b128 a[216:219],v14 offset:38912\nv_xor_b32 v8,0x10000,v8\n" \
"M v[128:131],a[192:195],v[96:99],v[128:131]\nds_read_b128 a[220:223],v14 offset:39936\nv_xor_b32 v9,0x10000,v9\nM v[160:163],a[192:195],v[104:107],v[160:163]\nv_exp_f32 v64,v64\nM v[192:195],a[192:195],v[112:115],v[192:195]\nv_exp_f32 v65,v65\nM v[224:227],a[192:195],v[120:123],v[224:227]\n" \
"v_exp_f32 v66,v66\nM v[132:135],a[196:199],v[96:99],v[132:135]\nv_exp_f32 v67,v67\nM v[164:167],a[196:199],v[104:107],v[164:167]\nv_exp_f32 v68,v68\nM v[196:199],a[196:199],v[112:115],v[196:199]\ns_waitcnt vmcnt(8) lgkmcnt(0)\nv_exp_f32 v69,v69\n" \
"M v[228:231],a[196:199],v[120:123],v[228:231]\ns_barrier\nds_read_b128 a[192:195],v13 offset:49152\nM v[136:139],a[200:203],v[96:99],v[136:139]\nbuffer_load_dwordx4 v2,s[36:39],s47 offen offset:0 sc1 lds\nM v[168:171],a[200:203],v[104:107],v[168:171]\nv_exp_f32 v70,v70\nM v[200:203],a[200:203],v[112:115],v[200:203]\n" \
"v_exp_f32 v71,v71\nM v[232:235],a[200:203],v[120:123],v[232:235]\nv_exp_f32 v72,v72\nM v[140:143],a[204:207],v[96:99],v[140:143]\nbuffer_load_dwordx4 v2,s[36:39],s47 offen offset:1024 sc1 lds\nM v[172:175],a[204:207],v[104:107],v[172:175]\nv_exp_f32 v73,v73\nM v[204:207],a[204:207],v[112:115],v[204:207]\n" \
"v_exp_f32 v74,v74\nM v[236:239],a[204:207],v[120:123],v[236:239]\nv_exp_f32 v75,v75\nM v[144:147],a[208:211],v[96:99],v[144:147]\nbuffer_load_dwordx4 v2,s[36:39],s47 offen offset:2048 sc1 lds\nM v[176:179],a[208:211],v[104:107],v[176:179]\nds_read_b128 a[196:199],v13 offset:51200\nC v64,v64,v65\n" \
"M v[208:211],a[208:211],v[112:115],v[208:211]\nv_exp_f32 v76,v76\nM v[240:243],a[208:211],v[120:123],v[240:243]\nv_exp_f32 v77,v77\nM v[148:151],a[212:215],v[96:99],v[148:151]\nbuffer_load_dwordx4 v2,s[36:39],s47 offen offset:3072 sc1 lds\nM v[180:183],a[212:215],v[104:107],v[180:183]\ns_add_u32 m0,s22,0\n" \
"ds_read_b128 a[200:203],v13 offset:53248\nC v65,v66,v67\nM v[212:215],a[212:215],v[112:115],v[212:215]\ns_add_u32 s52,s52,1\nv_exp_f32 v78,v78\nM v[244:247],a[212:215],v[120:123],v[244:247]\ns_add_u32 s47,s29,0\nv_exp_f32 v79,v79\n" \
"M v[152:155],a[216:219],v[96:99],v[152:155]\nbuffer_load_dwordx4 v1,s[84:87],s46 offen offset:0 lds\nM v[184:187],a[216:219],v[104:107],v[184:187]\ns_add_u32 m0,s22,896\nds_read_b128 a[204:207],v13 offset:55296\nC v66,v68,v69\nM v[216:219],a[216:219],v[112:115],v[216:219]\nv_exp_f32 v80,v80\n" \
"M v[248:251],a[216:219],v[120:123],v[248:251]\ns_waitcnt lgkmcnt(1)\nv_exp_f32 v81,v81\nM v[156:159],a[220:223],v[96:99],v[156:159]\nbuffer_load_dwordx4 v1,s[84:87],s46 offen offset:128 lds\nM v[188:191],a[220:223],v[104:107],v[188:191]\ns_add_u32 m0,s22,2048\nv_exp_f32 v82,v82\n" \
"M v[220:223],a[220:223],v[112:115],v[220:223]\ns_cmp_eq_u32 s52,s56\nds_read_b128 a[208:211],v13 offset:50176\nC v67,v70,v71\nM v[252:255],a[220:223],v[120:123],v[252:255]\ns_cbranch_scc1 LEV_%=\nLEVR14_%=:\nv_exp_f32 v83,v83\n" \
"M v[96:99],a[192:195],a[128:131],v[32:35]\nbuffer_load_dwordx2 v[10:11],v5,s[36:39],0 offen sc1\nM v[104:107],a[192:195],a[144:147],v[36:39]\ns_mov_b32 s46,1114112\nv_exp_f32 v84,v84\nM v[16:19],a[224:227],v[64:67],v[24:27]\nv_exp_f32 v85,v85\nM v[112:115],a[192:195],a[160:163],v[40:43]\n" \
"ds_read_b128 a[212:215],v13 offset:52224\nC v72,v72,v73\nM v[120:123],a[192:195],a[176:179],v[44:47]\nbuffer_load_dwordx4 v1,s[84:87],s46 offen offset:0 lds\nM v[96:99],a[196:199],a[132:135],v[96:99]\ns_add_u32 m0,s22,2944\nC v73,v74,v75\nC v74,v76,v77\n" \
"C v75,v78,v79\nM v[104:107],a[196:199],a[148:151],v[104:107]\nv_exp_f32 v86,v86\nM v[16:19],a[228:231],v[72:75],v[16:19]\nv_exp_f32 v87,v87\nM v[112:115],a[196:199],a[164:167],v[112:115]\nbuffer_load_dwordx4 v1,s[84:87],s46 offen offset:128 lds\nM v[120:123],a[196:199],a[180:183],v[120:123]\n" \
"s_add_u32 m0,s22,32768\nds_read_b128 a[216:219],v13 offset:54272\nC v80,v80,v81\nM v[96:99],a[200:203],a[136:139],v[96:99]\ns_mov_b32 s46,1179648\nv_exp_f32 v88,v88\nM v[104:107],a[200:203],a[152:155],v[104:107]\nds_read_b128 a[220:223],v13 offset:56320\n" \
"C v81,v82,v83\nM v[112:115],a[200:203],a[168:171],v[112:115]\nbuffer_load_dwordx4 v2,s[36:39],s47 offen offset:0 sc1 lds\nM v[120:123],a[200:203],a[184:187],v[120:123]\ns_waitcnt lgkmcnt(2)\nv_exp_f32 v89,v89\nM v[96:99],a[204:207],a[140:143],v[96:99]\nv_exp_f32 v90,v90\n" \
"M v[104:107],a[204:207],a[156:159],v[104:107]\nv_exp_f32 v91,v91\nM v[112:115],a[204:207],a[172:175],v[112:115]\nC v82,v84,v85\nC v83,v86,v87\nM v[120:123],a[204:207],a[188:191],v[120:123]\nbuffer_load_dwordx4 v2,s[36:39],s47 offen offset:1024 sc1 lds\nM v[16:19],a[232:235],v[80:83],v[16:19]\n" \
"ds_read_b64_tr_b16 a[112:113],v6 offset:16384\nds_read_b64_tr_b16 a[114:115],v7 offset:16384\nM v[100:103],a[208:211],a[128:131],v[32:35]\nLG20_%=:\ns_cbranch_execz LG24_%=\nds_read_b64_tr_b16 a[116:117],v8 offset:16384\nC v88,v88,v89\nM v[108:111],a[208:211],a[144:147],v[36:39]\n" \
"ds_read_b64_tr_b16 a[118:119],v9 offset:16384\nC v89,v90,v91\nM v[116:119],a[208:211],a[160:163],v[40:43]\nv_exp_f32 v92,v92\nM v[124:127],a[208:211],a[176:179],v[44:47]\nv_exp_f32 v93,v93\nM v[100:103],a[212:215],a[132:135],v[100:103]\nv_exp_f32 v94,v94\n" \
"M v[108:111],a[212:215],a[148:151],v[108:111]\nv_exp_f32 v95,v95\nv_xor_b32 v14,0x10000,v14\nM v[116:119],a[212:215],a[164:167],v[116:119]\nds_read_b128 a[192:195],v14 offset:0\nC v90,v92,v93\nC v91,v94,v95\nM v[124:127],a[212:215],a[180:183],v[124:127]\n" \
"buffer_load_dwordx4 v2,s[36:39],s47 offen offset:2048 sc1 lds\nM v[16:19],a[236:239],v[88:91],v[16:19]\ns_waitcnt lgkmcnt(1)\nds_read_b128 a[196:199],v14 offset:1024\nds_read_b128 a[200:203],v14 offset:2048\nM v[100:103],a[216:219],a[136:139],v[100:103]\nds_read_b64_tr_b16 a[120:121],v6 offset:18432\nv_add_u32 v20,0x10000,v4\n" \
"M v[108:111],a[216:219],a[152:155],v[108:111]\nds_read_b64_tr_b16 a[122:123],v7 offset:18432\nv_max3_f32 v15,v16,v17,v18\nM v[116:119],a[216:219],a[168:171],v[116:119]\nbuffer_load_dwordx4 v2,s[36:39],s47 offen offset:3072 sc1 lds\nM v[124:127],a[216:219],a[184:187],v[124:127]\ns_waitcnt vmcnt(6)\nv_max_f32 v15,v15,v19\n" \
"v_cmp_nge_f32 vcc,0x53800000,v15\nM v[100:103],a[220:223],a[140:143],v[100:103]\ns_add_u32 m0,s22,16384\nds_read_b64_tr_b16 a[124:125],v8 offset:18432\nds_read_b64_tr_b16 a[126:127],v9 offset:18432\nM v[108:111],a[220:223],a[156:159],v[108:111]\ns_cbranch_vccnz Lcs63_%=\nLcr64_%=:\n" \
"ds_read_b128 a[204:207],v14 offset:3072\nv_cmp_gt_u64 s[98:99],s[14:15],v[10:11]\nM v[116:119],a[220:223],a[172:175],v[116:119]\ns_add_u32 s47,s29,8192\nds_read_b128 a[208:211],v14 offset:4096\nv_cmp_le_u64 s[18:19],s[16:17],v[10:11]\nM v[124:127],a[220:223],a[188:191],v[124:127]\ns_or_b64 s[98:99],s[98:99],s[18:19]\n" \
"s_and_b32 s98,s98,0xff0000\ns_cbranch_scc1 LPF_%=\nLPFR_%=:\ns_waitcnt lgkmcnt(4)\nds_read_b128 a[212:215],v14 offset:5120\nv_add_u32 v23,0x20000,v4\nM v[128:131],a[192:195],v[64:67],v[128:131]\nds_read_b128 a[216:219],v14 offset:6144\n" \
"ds_read_b128 a[220:223],v14 offset:7168\nM v[160:163],a[192:195],v[72:75],v[160:163]\nv_exp_f32 v96,v96\nM v[192:195],a[192:195],v[80:83],v[192:195]\nv_exp_f32 v97,v97\nM v[224:227],a[192:195],v[88:91],v[224:227]\nv_exp_f32 v98,v98\nM v[132:135],a[196:199],v[64:67],v[132:135]\n" \
"v_exp_f32 v99,v99\nM v[164:167],a[196:199],v[72:75],v[164:167]\nv_exp_f32 v100,v100\nM v[196:199],a[196:199],v[80:83],v[196:199]\ns_waitcnt vmcnt(13) lgkmcnt(0)\nv_exp_f32 v101,v101\nM v[228:231],a[196:199],v[88:91],v[228:231]\ns_barrier\n" \
"ds_read_b128 v[76:79],v20 offset:16384\nM v[136:139],a[200:203],v[64:67],v[136:139]\nbuffer_load_dwordx4 v1,s[84:87],s46 offen offset:0 lds\nM v[168:171],a[200:203],v[72:75],v[168:171]\ns_add_u32 m0,s22,17280\nv_exp_f32 v102,v102\nM v[200:203],a[200:203],v[80:83],v[200:203]\nv_exp_f32 v103,v103\n" \
"M v[232:235],a[200:203],v[88:91],v[232:235]\nv_exp_f32 v104,v104\nM v[140:143],a[204:207],v[64:67],v[140:143]\nbuffer_load_dwordx4 v1,s[84:87],s46 offen offset:128 lds\nM v[172:175],a[204:207],v[72:75],v[172:175]\ns_add_u32 m0,s22,18432\nv_exp_f32 v105,v105\nM v[204:207],a[204:207],v[80:83],v[204:207]\n" \
"s_mov_b32 s46,1245184\nv_exp_f32 v106,v106\nM v[236:239],a[204:207],v[88:91],v[236:239]\nv_exp_f32 v107,v107\nM v[144:147],a[208:211],v[64:67],v[144:147]\nbuffer_load_dwordx4 v1,s[84:87],s46 offen offset:0 lds\nM v[176:179],a[208:211],v[72:75],v[176:179]\ns_add_u32 m0,s22,19328\n" \
"ds_read_b128 v[84:87],v20 offset:24576\nC v96,v96,v97\nM v[208:211],a[208:211],v[80:83],v[208:211]\nv_exp_f32 v108,v108\nM v[240:243],a[208:211],v[88:91],v[240:243]\nds_read_b128 v[92:95],v20 offset:17408\nC v97,v98,v99\nM v[148:151],a[212:215],v[64:67],v[148:151]\n" \
"buffer_load_dwordx4 v1,s[84:87],s46 offen offset:128 lds\nM v[180:183],a[212:215],v[72:75],v[180:183]\ns_add_u32 m0,s22,49152\nv_exp_f32 v109,v109\nM v[212:215],a[212:215],v[80:83],v[212:215]\ns_mov_b32 s46,1310720\nv_exp_f32 v110,v110\nM v[244:247],a[212:215],v[88:91],v[244:247]\n" \
"v_exp_f32 v111,v111\nM v[152:155],a[216:219],v[64:67],v[152:155]\nbuffer_load_dwordx4 v2,s[36:39],s47 offen offset:0 sc1 lds\nM v[184:187],a[216:219],v[72:75],v[184:187]\nv_exp_f32 v112,v112\nM v[216:219],a[216:219],v[80:83],v[216:219]\nv_exp_f32 v113,v113\nM v[248:251],a[216:219],v[88:91],v[248:251]\n" \
"v_exp_f32 v114,v114\nM v[156:159],a[220:223],v[64:67],v[156:159]\nbuffer_load_dwordx4 v2,s[36:39],s47 offen offset:1024 sc1 lds\nM v[188:191],a[220:223],v[72:75],v[188:191]\nds_read_b128 v[72:75],v20 offset:25600\nC v104,v104,v105\nM v[220:223],a[220:223],v[80:83],v[220:223]\nds_read_b128 v[80:83],v20 offset:18432\n" \
"C v98,v100,v101\nM v[252:255],a[220:223],v[88:91],v[252:255]\ns_waitcnt lgkmcnt(2)\nds_read_b128 v[88:91],v20 offset:26624\nC v99,v102,v103\nM v[48:51],a[0:3],v[76:79],0\nbuffer_load_dwordx4 v2,s[36:39],s47 offen offset:2048 sc1 lds\nM v[52:55],a[4:7],v[76:79],0\n" \
"ds_read_b128 v[76:79],v20 offset:19456\nC v105,v106,v107\nM v[64:67],a[0:3],v[84:87],0\nv_exp_f32 v115,v115\nM v[68:71],a[4:7],v[84:87],0\ns_waitcnt lgkmcnt(2)\nds_read_b128 v[84:87],v20 offset:27648\nC v106,v108,v109\n" \
"M v[24:27],a[224:227],v[96:99],v[16:19]\nbuffer_load_dwordx4 v2,s[36:39],s47 offen offset:3072 sc1 lds\nM v[48:51],a[8:11],v[92:95],v[48:51]\ns_add_u32 m0,s22,65536\nv_exp_f32 v116,v116\nM v[52:55],a[12:15],v[92:95],v[52:55]\ns_add_u32 s47,s30,0\nds_read_b128 v[92:95],v20 offset:20480\n" \
"C v107,v110,v111\nM v[64:67],a[8:11],v[72:75],v[64:67]\nv_exp_f32 v117,v117\nM v[68:71],a[12:15],v[72:75],v[68:71]\nbuffer_load_dwordx4 v2,s[36:39],s47 offen offset:0 sc1 lds\nM v[24:27],a[228:231],v[104:107],v[24:27]\nds_read_b128 v[72:75],v20 offset:28672\nC v112,v112,v113\n" \
"M v[48:51],a[16:19],v[80:83],v[48:51]\nv_exp_f32 v118,v118\nM v[52:55],a[20:23],v[80:83],v[52:55]\ns_waitcnt lgkmcnt(1)\nds_read_b128 v[80:83],v20 offset:21504\nC v113,v114,v115\nM v[64:67],a[16:19],v[88:91],v[64:67]\nbuffer_load_dwordx4 v2,s[36:39],s47 offen offset:1024 sc1 lds\n" \
"M v[68:71],a[20:23],v[88:91],v[68:71]\nds_read_b128 v[88:91],v20 offset:29696\nC v114,v116,v117\nM v[48:51],a[24:27],v[76:79],v[48:51]\nds_read_b128 a[192:195],v14 offset:32768\nds_read_b128 a[196:199],v14 offset:33792\nM v[52:55],a[28:31],v[76:79],v[52:55]\nds_read_b128 v[76:79],v20 offset:22528\n" \
"M v[64:67],a[24:27],v[84:87],v[64:67]\nbuffer_load_dwordx4 v2,s[36:39],s47 offen offset:2048 sc1 lds\nM v[68:71],a[28:31],v[84:87],v[68:71]\nv_exp_f32 v119,v119\nM v[48:51],a[32:35],v[92:95],v[48:51]\nds_read_b128 v[84:87],v20 offset:30720\nC v115,v118,v119\nM v[52:55],a[36:39],v[92:95],v[52:55]\n" \
"s_waitcnt lgkmcnt(2)\nds_read_b128 v[92:95],v20 offset:23552\nds_read_b128 a[200:203],v14 offset:34816\nM v[24:27],a[232:235],v[112:115],v[24:27]\nbuffer_load_dwordx4 v2,s[36:39],s47 offen offset:3072 sc1 lds\nM v[64:67],a[32:35],v[72:75],v[64:67]\ns_add_u32 m0,s22,98304\nv_exp_f32 v120,v120\n" \
"M v[68:71],a[36:39],v[72:75],v[68:71]\ns_add_u32 s47,s30,8192\nv_exp_f32 v121,v121\nM v[48:51],a[40:43],v[80:83],v[48:51]\nv_exp_f32 v122,v122\nM v[52:55],a[44:47],v[80:83],v[52:55]\nv_exp_f32 v123,v123\nM v[64:67],a[40:43],v[88:91],v[64:67]\n" \
"v_exp_f32 v124,v124\nM v[68:71],a[44:47],v[88:91],v[68:71]\ns_waitcnt lgkmcnt(0)\nv_exp_f32 v125,v125\nM v[48:51],a[48:51],v[76:79],v[48:51]\nds_read_b128 v[72:75],v20 offset:31744\nv_exp_f32 v126,v126\nM v[52:55],a[52:55],v[76:79],v[52:55]\n" \
"ds_read_b128 a[204:207],v14 offset:35840\nv_exp_f32 v127,v127\nM v[64:67],a[48:51],v[84:87],v[64:67]\nds_read_b128 a[208:211],v14 offset:36864\nC v120,v120,v121\nC v121,v122,v123\nC v122,v124,v125\nC v123,v126,v127\n" \
"M v[68:71],a[52:55],v[84:87],v[68:71]\nds_read_b128 a[212:215],v14 offset:37888\nds_read_b128 a[216:219],v14 offset:38912\nM v[24:27],a[236:239],v[120:123],v[24:27]\nds_read_b128 a[220:223],v14 offset:39936\nM v[48:51],a[56:59],v[92:95],v[48:51]\nM v[52:55],a[60:63],v[92:95],v[52:55]\ns_waitcnt lgkmcnt(4)\n" \
"v_max3_f32 v15,v24,v25,v26\nv_max_f32 v15,v15,v27\nv_cmp_nge_f32 vcc,0x53800000,v15\nM v[64:67],a[56:59],v[72:75],v[64:67]\nM v[68:71],a[60:63],v[72:75],v[68:71]\ns_cbranch_vccnz Lcs65_%=\nLcr66_%=:\nds_write_b64 v57,v[26:27] offset:8\n" \
"ds_write2_b32 v57,v40,v44 offset1:1\nM v[192:195],a[192:195],v[112:115],v[192:195]\nM v[196:199],a[196:199],v[112:115],v[196:199]\nM v[200:203],a[200:203],v[112:115],v[200:203]\nM v[204:207],a[204:207],v[112:115],v[204:207]\ns_waitcnt lgkmcnt(2)\nM v[208:211],a[208:211],v[112:115],v[208:211]\ns_waitcnt vmcnt(21) lgkmcnt(0)\n" \
"M v[212:215],a[212:215],v[112:115],v[212:215]\ns_barrier\nds_read_b128 v[28:31],v12 offset:0\nM v[216:219],a[216:219],v[112:115],v[216:219]\nbuffer_load_dwordx4 v1,s[84:87],s46 offen offset:0 lds\nM v[220:223],a[220:223],v[112:115],v[220:223]\ns_add_u32 m0,s22,99200\nds_write_b128 v12,v[192:195] offset:0\n" \
"M v[224:227],a[192:195],v[120:123],v[224:227]\nM v[228:231],a[196:199],v[120:123],v[228:231]\ns_waitcnt lgkmcnt(1)\nM v[232:235],a[200:203],v[120:123],v[232:235]\nbuffer_load_dwordx4 v1,s[84:87],s46 offen offset:128 lds\nM v[236:239],a[204:207],v[120:123],v[236:239]\ns_add_u32 m0,s22,100352\nds_write_b128 v12,v[196:199] offset:1024\n" \
"M v[240:243],a[208:211],v[120:123],v[240:243]\ns_mov_b32 s46,1376256\nv_min_f32 v15,v32,v28\nv_sub_f32 v16,v15,v32\nM v[244:247],a[212:215],v[120:123],v[244:247]\nv_exp_f32 v16,v16\nM v[248:251],a[216:219],v[120:123],v[248:251]\nbuffer_load_dwordx4 v1,s[84:87],s46 offen offset:0 lds\n" \
"M v[252:255],a[220:223],v[120:123],v[252:255]\ns_add_u32 m0,s22,101248\nv_sub_f32 v18,v15,v28\nv_mul_f32 v15,v24,v16\nM v[128:131],a[192:195],v[96:99],v[128:131]\nds_write_b128 v12,v[200:203] offset:2048\nM v[132:135],a[196:199],v[96:99],v[132:135]\nv_exp_f32 v18,v18\n" \
"M v[136:139],a[200:203],v[96:99],v[136:139]\nbuffer_load_dwordx4 v1,s[84:87],s46 offen offset:128 lds\nM v[140:143],a[204:207],v[96:99],v[140:143]\ns_add_u32 m0,s22,81920\nv_fmac_f32 v15,v30,v18\nv_mov_b32 v28,s10\nM v[144:147],a[208:211],v[96:99],v[144:147]\ns_mov_b32 s46,1441792\n" \
"ds_write_b128 v12,v[204:207] offset:3072\nM v[148:151],a[212:215],v[96:99],v[148:151]\nv_rcp_f32 v15,v15\nM v[152:155],a[216:219],v[96:99],v[152:155]\nbuffer_load_dwordx4 v2,s[36:39],s47 offen offset:0 sc1 lds\nM v[156:159],a[220:223],v[96:99],v[156:159]\ns_add_u32 s52,s52,1\nv_mul_f32 v16,v16,v15\n" \
"v_mul_f32 v18,v18,v15\nM v[160:163],a[192:195],v[104:107],v[160:163]\nds_read_b128 v[80:83],v20 offset:49152\nv_min_f32 v15,v36,v29\nM v[164:167],a[196:199],v[104:107],v[164:167]\nv_mul_f32 v128,v16,v128\nv_mul_f32 v129,v16,v129\nM v[168:171],a[200:203],v[104:107],v[168:171]\n" \
"buffer_load_dwordx4 v2,s[36:39],s47 offen offset:1024 sc1 lds\nM v[172:175],a[204:207],v[104:107],v[172:175]\nds_read_b128 v[88:91],v20 offset:57344\nv_mul_f32 v130,v16,v130\nM v[176:179],a[208:211],v[104:107],v[176:179]\nds_write_b128 v12,v[208:211] offset:4096\nM v[180:183],a[212:215],v[104:107],v[180:183]\ns_waitcnt lgkmcnt(2)\n" \
"ds_read_b128 v[76:79],v20 offset:50176\nv_mul_f32 v131,v16,v131\nM v[184:187],a[216:219],v[104:107],v[184:187]\ns_cmp_eq_u32 s52,s56\nv_mul_f32 v132,v16,v132\nv_mul_f32 v133,v16,v133\nM v[188:191],a[220:223],v[104:107],v[188:191]\ns_cbranch_scc1 LEV_%=\n" \
"LEVR15_%=:\nds_read_b128 v[84:87],v20 offset:58368\nv_mul_f32 v134,v16,v134\nM v[48:51],a[64:67],v[80:83],v[48:51]\nbuffer_load_dwordx4 v2,s[36:39],s47 offen offset:2048 sc1 lds\nM v[52:55],a[68:71],v[80:83],v[52:55]\ns_waitcnt lgkmcnt(2)\nds_read_b128 v[92:95],v20 offset:51200\n" \
"v_mul_f32 v135,v16,v135\nM v[64:67],a[64:67],v[88:91],v[64:67]\nds_write_b128 v12,v[212:215] offset:5120\nM v[68:71],a[68:71],v[88:91],v[68:71]\ns_waitcnt lgkmcnt(3)\nds_read_b128 v[72:75],v20 offset:59392\nv_mul_f32 v136,v16,v136\nM v[48:51],a[72:75],v[76:79],v[48:51]\n" \
"buffer_load_dwordx4 v2,s[36:39],s47 offen offset:3072 sc1 lds\nM v[52:55],a[76:79],v[76:79],v[52:55]\ns_waitcnt lgkmcnt(3)\ns_add_u32 m0,s22,114688\nds_read_b128 v[80:83],v20 offset:52224\nv_mul_f32 v137,v16,v137\nM v[64:67],a[72:75],v[84:87],v[64:67]\ns_add_u32 s47,s31,0\n" \
"ds_read_b128 v[88:91],v20 offset:60416\nv_mul_f32 v138,v16,v138\nM v[68:71],a[76:79],v[84:87],v[68:71]\ns_waitcnt lgkmcnt(3)\nv_mul_f32 v139,v16,v139\nv_mul_f32 v140,v16,v140\nM v[48:51],a[80:83],v[92:95],v[48:51]\nds_write_b128 v12,v[216:219] offset:6144\n" \
"M v[52:55],a[84:87],v[92:95],v[52:55]\ns_waitcnt lgkmcnt(3)\nds_read_b128 v[76:79],v20 offset:53248\nv_mul_f32 v141,v16,v141\nM v[64:67],a[80:83],v[72:75],v[64:67]\nv_mul_f32 v142,v16,v142\nv_mul_f32 v143,v16,v143\nM v[68:71],a[84:87],v[72:75],v[68:71]\n" \
"s_waitcnt lgkmcnt(2)\nds_write_b128 v12,v[220:223] offset:7168\nds_read_b128 v[84:87],v20 offset:61440\nM v[48:51],a[88:91],v[80:83],v[48:51]\nv_sub_f32 v17,v15,v36\nv_mul_f32 v144,v16,v144\nM v[52:55],a[92:95],v[80:83],v[52:55]\nds_read_b128 v[92:95],v20 offset:54272\n" \
"v_mul_f32 v145,v16,v145\nM v[64:67],a[88:91],v[88:91],v[64:67]\nds_read_b128 v[72:75],v20 offset:62464\nv_mul_f32 v146,v16,v146\nM v[68:71],a[92:95],v[88:91],v[68:71]\ns_waitcnt lgkmcnt(4)\nds_read_b128 v[80:83],v20 offset:55296\nv_mul_f32 v147,v16,v147\n" \
"M v[48:51],a[96:99],v[76:79],v[48:51]\ns_waitcnt lgkmcnt(3)\nds_read_b128 v[88:91],v20 offset:63488\nv_mul_f32 v148,v16,v148\nM v[52:55],a[100:103],v[76:79],v[52:55]\ns_waitcnt vmcnt(20) lgkmcnt(5)\nds_read_b128 v[76:79],v20 offset:56320\nv_mul_f32 v149,v16,v149\n" \
"M v[64:67],a[96:99],v[84:87],v[64:67]\ns_barrier\nds_read_b128 v[96:99],v57 offset:0\nM v[68:71],a[100:103],v[84:87],v[68:71]\ns_waitcnt lgkmcnt(4)\nds_read_b128 v[100:103],v57 offset:1024\nv_mul_f32 v150,v16,v150\nM v[48:51],a[104:107],v[92:95],v[48:51]\n" \
"ds_read_b128 v[104:107],v57 offset:2048\nv_mul_f32 v151,v16,v151\nM v[52:55],a[108:111],v[92:95],v[52:55]\nds_read_b128 v[108:111],v57 offset:3072\nv_mul_f32 v152,v16,v152\nM v[64:67],a[104:107],v[72:75],v[64:67]\nds_read_b128 v[112:115],v57 offset:4096\nv_mul_f32 v153,v16,v153\n" \
"M v[68:71],a[108:111],v[72:75],v[68:71]\ns_waitcnt lgkmcnt(4)\nds_read_b128 v[84:87],v20 offset:64512\nv_mul_f32 v154,v16,v154\nM v[48:51],a[112:115],v[80:83],v[48:51]\nds_read_b128 v[116:119],v57 offset:5120\nv_mul_f32 v155,v16,v155\nM v[52:55],a[116:119],v[80:83],v[52:55]\n" \
"ds_read_b128 v[92:95],v4 offset:32768\nv_fmac_f32 v128,v96,v18\nM v[64:67],a[112:115],v[88:91],v[64:67]\nds_read_b128 v[120:123],v57 offset:6144\nv_fmac_f32 v129,v97,v18\nM v[68:71],a[116:119],v[88:91],v[68:71]\nds_read_b128 v[72:75],v4 offset:40960\nv_fmac_f32 v130,v98,v18\n" \
"M v[48:51],a[120:123],v[76:79],v[48:51]\nds_read_b128 v[124:127],v57 offset:7168\nv_fmac_f32 v131,v99,v18\nM v[52:55],a[124:127],v[76:79],v[52:55]\ns_waitcnt lgkmcnt(4)\nds_read_b128 v[80:83],v4 offset:33792\nv_fmac_f32 v132,v100,v18\nM v[64:67],a[120:123],v[84:87],v[64:67]\n" \
"ds_read_b128 v[88:91],v4 offset:41984\nv_fmac_f32 v133,v101,v18\nM v[68:71],a[124:127],v[84:87],v[68:71]\ns_waitcnt lgkmcnt(4)\nv_fmac_f32 v134,v102,v18\nv_fmac_f32 v135,v103,v18\nM v[32:35],a[0:3],v[92:95],0\nds_write_b128 v57,v[224:227] offset:0\n" \
"M v[36:39],a[4:7],v[92:95],0\ns_waitcnt lgkmcnt(3)\nds_read_b128 v[76:79],v4 offset:34816\nv_fmac_f32 v136,v104,v18\nM v[40:43],a[0:3],v[72:75],0\nv_fmac_f32 v137,v105,v18\nv_fmac_f32 v138,v106,v18\nM v[44:47],a[4:7],v[72:75],0\n" \
"s_waitcnt lgkmcnt(2)\nds_read_b128 v[84:87],v4 offset:43008\nv_fmac_f32 v139,v107,v18\nM v[32:35],a[8:11],v[80:83],v[32:35]\nds_read_b128 v[92:95],v4 offset:35840\nv_fmac_f32 v140,v108,v18\nM v[36:39],a[12:15],v[80:83],v[36:39]\nC v128,v128,v129\n" \
"C v129,v130,v131\nM v[40:43],a[8:11],v[88:91],v[40:43]\nds_write_b128 v57,v[228:231] offset:1024\nM v[44:47],a[12:15],v[88:91],v[44:47]\ns_waitcnt lgkmcnt(3)\nds_read_b128 v[72:75],v4 offset:44032\nC v130,v132,v133\nM v[32:35],a[16:19],v[76:79],v[32:35]\n" \
"C v131,v134,v135\nv_fmac_f32 v141,v109,v18\nM v[36:39],a[20:23],v[76:79],v[36:39]\ns_waitcnt lgkmcnt(2)\nds_read_b128 v[80:83],v4 offset:36864\nv_fmac_f32 v142,v110,v18\nM v[40:43],a[16:19],v[84:87],v[40:43]\nbuffer_store_dwordx4 v[128:131],v4,s[36:39],s62 offen offset:0 sc1\n" \
"M v[44:47],a[20:23],v[84:87],v[44:47]\nds_read_b64_tr_b16 a[128:129],v6 offset:0\nds_read_b64_tr_b16 a[130:131],v7 offset:0\nds_read_b64_tr_b16 a[132:133],v8 offset:0\nds_read_b64_tr_b16 a[134:135],v9 offset:0\nds_read_b64_tr_b16 a[136:137],v6 offset:2048\nds_read_b64_tr_b16 a[138:139],v7 offset:2048\nds_read_b64_tr_b16 a[140:141],v8 offset:2048\n" \
"ds_read_b64_tr_b16 a[142:143],v9 offset:2048\nds_read_b128 v[88:91],v4 offset:45056\nM v[32:35],a[24:27],v[92:95],v[32:35]\nv_fmac_f32 v143,v111,v18\nC v132,v136,v137\nM v[36:39],a[28:31],v[92:95],v[36:39]\ns_waitcnt lgkmcnt(10)\nds_read_b128 v[76:79],v4 offset:37888\n" \
"C v133,v138,v139\nM v[40:43],a[24:27],v[72:75],v[40:43]\nbuffer_load_dwordx2 v[10:11],v5,s[36:39],0 offen sc1\nM v[44:47],a[28:31],v[72:75],v[44:47]\ns_waitcnt lgkmcnt(10)\nds_read_b128 v[84:87],v4 offset:46080\nC v134,v140,v141\nM v[32:35],a[32:35],v[80:83],v[32:35]\n" \
"s_waitcnt lgkmcnt(2)\nds_write_b128 v57,v[232:235] offset:2048\nM v[36:39],a[36:39],v[80:83],v[36:39]\ns_waitcnt vmcnt(14) lgkmcnt(4)\nds_read_b128 v[92:95],v4 offset:38912\nC v135,v142,v143\nM v[40:43],a[32:35],v[88:91],v[40:43]\ns_barrier\n" \
"v_fmac_f32 v144,v112,v18\nM v[44:47],a[36:39],v[88:91],v[44:47]\ns_waitcnt lgkmcnt(3)\nds_read_b128 v[72:75],v4 offset:47104\nv_fmac_f32 v145,v113,v18\nM v[32:35],a[40:43],v[76:79],v[32:35]\nbuffer_store_dwordx4 v[132:135],v4,s[36:39],s62 offen offset:1024 sc1\nM v[36:39],a[44:47],v[76:79],v[36:39]\n" \
"s_waitcnt lgkmcnt(2)\nds_read_b128 v[80:83],v4 offset:39936\nv_fmac_f32 v146,v114,v18\nM v[40:43],a[40:43],v[84:87],v[40:43]\nds_write_b128 v57,v[236:239] offset:3072\nM v[44:47],a[44:47],v[84:87],v[44:47]\ns_waitcnt lgkmcnt(3)\nds_read_b128 v[88:91],v4 offset:48128\n" \
"v_fmac_f32 v147,v115,v18\nM v[32:35],a[48:51],v[92:95],v[32:35]\nbuffer_load_dwordx4 v1,s[84:87],s46 offen offset:0 lds\nM v[36:39],a[52:55],v[92:95],v[36:39]\ns_waitcnt lgkmcnt(3)\ns_add_u32 m0,s22,115584\nds_read_b128 v[76:79],v4 offset:49152\nv_fmac_f32 v148,v116,v18\n" \
"M v[40:43],a[48:51],v[72:75],v[40:43]\nds_read_b128 v[84:87],v4 offset:57344\nv_fmac_f32 v149,v117,v18\nM v[44:47],a[52:55],v[72:75],v[44:47]\ns_waitcnt lgkmcnt(3)\nv_fmac_f32 v150,v118,v18\nv_fmac_f32 v151,v119,v18\nM v[32:35],a[56:59],v[80:83],v[32:35]\n" \
"buffer_load_dwordx4 v1,s[84:87],s46 offen offset:128 lds\nM v[36:39],a[60:63],v[80:83],v[36:39]\ns_waitcnt lgkmcnt(2)\ns_add_u32 m0,s22,116736\nds_read_b128 v[92:95],v4 offset:50176\nC v136,v144,v145\nM v[40:43],a[56:59],v[88:91],v[40:43]\ns_mov_b32 s46,1507328\n" \
"ds_write_b128 v57,v[240:243] offset:4096\nM v[44:47],a[60:63],v[88:91],v[44:47]\ns_waitcnt lgkmcnt(2)\nds_read_b128 v[72:75],v4 offset:58368\nC v137,v146,v147\nM v[32:35],a[64:67],v[76:79],v[32:35]\nbuffer_load_dwordx4 v1,s[84:87],s46 offen offset:0 lds\nM v[36:39],a[68:71],v[76:79],v[36:39]\n" \
"s_add_u32 m0,s22,117632\nds_read_b128 v[80:83],v4 offset:51200\nC v138,v148,v149\nM v[40:43],a[64:67],v[84:87],v[40:43]\nds_read_b128 v[88:91],v4 offset:59392\nC v139,v150,v151\nM v[44:47],a[68:71],v[84:87],v[44:47]\ns_waitcnt lgkmcnt(3)\n" \
"v_mul_f32 v156,v16,v156\nv_mul_f32 v157,v16,v157\nM v[32:35],a[72:75],v[92:95],v[32:35]\nbuffer_store_dwordx4 v[136:139],v4,s[36:39],s62 offen offset:2048 sc1\nM v[36:39],a[76:79],v[92:95],v[36:39]\ns_waitcnt lgkmcnt(2)\nds_read_b128 v[76:79],v4 offset:52224\nv_mul_f32 v158,v16,v158\n" \
"M v[40:43],a[72:75],v[72:75],v[40:43]\nds_write_b128 v57,v[244:247] offset:5120\nM v[44:47],a[76:79],v[72:75],v[44:47]\ns_waitcnt lgkmcnt(2)\nds_read_b128 v[84:87],v4 offset:60416\nv_mul_f32 v159,v16,v159\nM v[32:35],a[80:83],v[80:83],v[32:35]\nbuffer_load_dwordx4 v1,s[84:87],s46 offen offset:128 lds\n" \
"M v[36:39],a[84:87],v[80:83],v[36:39]\ns_add_u32 m0,s22,0\nds_read_b128 v[92:95],v4 offset:53248\nv_fmac_f32 v152,v120,v18\nM v[40:43],a[80:83],v[88:91],v[40:43]\ns_mov_b32 s46,1572864\nds_read_b64_tr_b16 a[144:145],v6 offset:16384\nv_fmac_f32 v153,v121,v18\n" \
"M v[44:47],a[84:87],v[88:91],v[44:47]\ns_waitcnt lgkmcnt(3)\nds_write_b128 v57,v[248:251] offset:6144\nds_write_b128 v57,v[252:255] offset:7168\nds_read_b64_tr_b16 a[146:147],v7 offset:16384\nds_read_b64_tr_b16 a[148:149],v8 offset:16384\nds_read_b64_tr_b16 a[150:151],v9 offset:16384\nds_read_b64_tr_b16 a[152:153],v6 offset:18432\n" \
"ds_read_b64_tr_b16 a[154:155],v7 offset:18432\nds_read_b64_tr_b16 a[156:157],v8 offset:18432\nds_read_b64_tr_b16 a[158:159],v9 offset:18432\nds_read_b128 v[72:75],v4 offset:61440\nM v[32:35],a[88:91],v[76:79],v[32:35]\ns_waitcnt vmcnt(6)\nv_cmp_gt_u64 s[98:99],s[14:15],v[10:11]\nv_cmp_le_u64 s[18:19],s[16:17],v[10:11]\n" \
"M v[36:39],a[92:95],v[76:79],v[36:39]\ns_or_b64 s[98:99],s[98:99],s[18:19]\ns_and_b32 s98,s98,0xff000000\ns_cbranch_scc1 LPM_%=\nLPMR_%=:\ns_waitcnt lgkmcnt(12)\nds_read_b128 v[80:83],v4 offset:54272\nv_fmac_f32 v154,v122,v18\n" \
"M v[40:43],a[88:91],v[84:87],v[40:43]\nds_read_b128 v[88:91],v4 offset:62464\nv_fmac_f32 v155,v123,v18\nM v[44:47],a[92:95],v[84:87],v[44:47]\ns_waitcnt lgkmcnt(12)\nds_read_b128 v[76:79],v4 offset:55296\nv_fmac_f32 v156,v124,v18\nM v[32:35],a[96:99],v[92:95],v[32:35]\n" \
"s_waitcnt lgkmcnt(3)\nds_read_b128 v[84:87],v4 offset:63488\nv_fmac_f32 v157,v125,v18\nM v[36:39],a[100:103],v[92:95],v[36:39]\nLG21_%=:\ns_cbranch_execz LG25_%=\ns_waitcnt vmcnt(16) lgkmcnt(5)\nds_read_b128 v[92:95],v4 offset:56320\n" \
"v_fmac_f32 v158,v126,v18\nM v[40:43],a[96:99],v[72:75],v[40:43]\ns_barrier\nds_read_b128 v[96:99],v12 offset:0\nM v[44:47],a[100:103],v[72:75],v[44:47]\ns_waitcnt lgkmcnt(4)\nbuffer_load_dwordx4 v2,s[36:39],s47 offen offset:0 sc1 lds\nM v[32:35],a[104:107],v[80:83],v[32:35]\n" \
"ds_read_b128 v[100:103],v12 offset:1024\nds_read_b128 v[104:107],v12 offset:2048\nM v[36:39],a[108:111],v[80:83],v[36:39]\nds_read_b128 v[108:111],v12 offset:3072\nv_sub_f32 v19,v15,v29\nM v[40:43],a[104:107],v[88:91],v[40:43]\ns_waitcnt lgkmcnt(4)\nds_read_b128 v[72:75],v4 offset:64512\n" \
"v_fmac_f32 v159,v127,v18\nM v[44:47],a[108:111],v[88:91],v[44:47]\nbuffer_load_dwordx4 v2,s[36:39],s47 offen offset:1024 sc1 lds\nM v[32:35],a[112:115],v[76:79],v[32:35]\nds_read_b128 v[80:83],v20 offset:0\nds_read_b128 v[88:91],v20 offset:8192\nM v[36:39],a[116:119],v[76:79],v[36:39]\nds_read_b128 v[76:79],v20 offset:1024\n" \
"C v140,v152,v153\nM v[40:43],a[112:115],v[84:87],v[40:43]\nds_read_b128 v[112:115],v12 offset:4096\nC v141,v154,v155\nM v[44:47],a[116:119],v[84:87],v[44:47]\nbuffer_load_dwordx4 v2,s[36:39],s47 offen offset:2048 sc1 lds\nM v[32:35],a[120:123],v[92:95],v[32:35]\nds_read_b128 v[84:87],v20 offset:9216\n" \
"ds_read_b128 v[116:119],v12 offset:5120\nM v[36:39],a[124:127],v[92:95],v[36:39]\ns_waitcnt lgkmcnt(4)\nds_read_b128 v[92:95],v20 offset:2048\nC v142,v156,v157\nM v[40:43],a[120:123],v[72:75],v[40:43]\nds_read_b128 v[120:123],v12 offset:6144\nC v143,v158,v159\n" \
"M v[44:47],a[124:127],v[72:75],v[44:47]\nbuffer_store_dwordx4 v[140:143],v4,s[36:39],s62 offen offset:3072 sc1\nM v[192:195],a[0:3],v[80:83],0\ns_add_u32 s62,s51,262144\nds_read_b128 v[72:75],v20 offset:10240\nds_read_b128 v[124:127],v12 offset:7168\nM v[196:199],a[4:7],v[80:83],0\ns_lshl_b32 s59,s61,20\n" \
"ds_read_b128 v[80:83],v20 offset:3072\nv_xor_b32 v6,0x10000,v6\nM v[200:203],a[0:3],v[88:91],0\ns_lshl_b32 s58,s60,15\nv_exp_f32 v17,v17\nM v[204:207],a[4:7],v[88:91],0\ns_waitcnt lgkmcnt(3)\nbuffer_load_dwordx4 v2,s[36:39],s47 offen offset:3072 sc1 lds\n" \
"M v[192:195],a[8:11],v[76:79],v[192:195]\ns_add_u32 m0,s22,32768\nds_read_b128 v[88:91],v20 offset:11264\nv_mul_f32 v15,v25,v17\nM v[196:199],a[12:15],v[76:79],v[196:199]\ns_add_u32 s47,s31,8192\nds_read_b128 v[76:79],v20 offset:4096\nv_xor_b32 v7,0x10000,v7\n" \
"M v[200:203],a[8:11],v[84:87],v[200:203]\ns_add_u32 s94,s59,s58\nv_exp_f32 v19,v19\nM v[204:207],a[12:15],v[84:87],v[204:207]\nbuffer_load_dwordx4 v1,s[84:87],s46 offen offset:0 lds\nM v[192:195],a[16:19],v[92:95],v[192:195]\ns_add_u32 m0,s22,33664\nds_read_b128 v[84:87],v20 offset:12288\n" \
"v_fmac_f32 v15,v31,v19\nM v[196:199],a[20:23],v[92:95],v[196:199]\ns_waitcnt lgkmcnt(2)\ns_add_u32 s58,s60,1\nds_read_b128 v[92:95],v20 offset:5120\nv_xor_b32 v8,0x10000,v8\nM v[200:203],a[16:19],v[72:75],v[200:203]\ns_and_b32 s58,s58,3\n" \
"v_rcp_f32 v15,v15\nM v[204:207],a[20:23],v[72:75],v[204:207]\nbuffer_load_dwordx4 v1,s[84:87],s46 offen offset:128 lds\nM v[192:195],a[24:27],v[80:83],v[192:195]\ns_mov_b32 s46,1638400\ns_add_u32 m0,s22,34816\nds_read_b128 v[72:75],v20 offset:13312\nv_mul_f32 v17,v17,v15\n" \
"M v[196:199],a[28:31],v[80:83],v[196:199]\ns_and_b32 s59,s58,2\nds_read_b128 v[80:83],v20 offset:6144\nv_mul_f32 v19,v19,v15\nM v[200:203],a[24:27],v[88:91],v[200:203]\ns_add_u32 s59,s59,s58\nv_mul_f32 v160,v17,v160\nv_mul_f32 v161,v17,v161\n" \
"M v[204:207],a[28:31],v[88:91],v[204:207]\ns_waitcnt lgkmcnt(2)\nbuffer_load_dwordx4 v1,s[84:87],s46 offen offset:0 lds\nM v[192:195],a[32:35],v[76:79],v[192:195]\ns_add_u32 m0,s22,35712\nds_read_b128 v[88:91],v20 offset:14336\nv_mul_f32 v162,v17,v162\nM v[196:199],a[36:39],v[76:79],v[196:199]\n" \
"s_waitcnt vmcnt(16) lgkmcnt(8)\nds_read_b128 v[76:79],v20 offset:7168\nv_mul_f32 v163,v17,v163\nM v[200:203],a[32:35],v[84:87],v[200:203]\ns_add_u32 s94,s94,58720256\ns_barrier\nv_mul_f32 v164,v17,v164\nM v[204:207],a[36:39],v[84:87],v[204:207]\n" \
"buffer_load_dwordx4 v1,s[84:87],s46 offen offset:128 lds\nM v[192:195],a[40:43],v[92:95],v[192:195]\ns_add_u32 m0,s22,16384\nds_read_b128 v[84:87],v20 offset:15360\nv_mul_f32 v165,v17,v165\nM v[196:199],a[44:47],v[92:95],v[196:199]\ns_waitcnt lgkmcnt(2)\ns_mov_b32 s46,1703936\n" \
"ds_read_b128 v[92:95],v20 offset:16384\nv_mul_f32 v166,v17,v166\nM v[200:203],a[40:43],v[72:75],v[200:203]\ns_lshl_b32 s59,s59,17\nv_mul_f32 v167,v17,v167\nv_mul_f32 v168,v17,v168\nM v[204:207],a[44:47],v[72:75],v[204:207]\nbuffer_load_dwordx4 v2,s[36:39],s47 offen offset:0 sc1 lds\n" \
"M v[192:195],a[48:51],v[80:83],v[192:195]\ns_add_u32 s32,s94,s59\nds_read_b128 v[72:75],v20 offset:24576\nv_mul_f32 v169,v17,v169\nM v[196:199],a[52:55],v[80:83],v[196:199]\ns_lshl_b32 s58,s58,1\nds_read_b128 v[80:83],v20 offset:17408\nv_mul_f32 v170,v17,v170\n" \
"M v[200:203],a[48:51],v[88:91],v[200:203]\ns_and_b32 s59,%1,0xf9\nv_mul_f32 v171,v17,v171\nv_mul_f32 v172,v17,v172\nM v[204:207],a[52:55],v[88:91],v[204:207]\ns_waitcnt lgkmcnt(2)\nbuffer_load_dwordx4 v2,s[36:39],s47 offen offset:1024 sc1 lds\nM v[192:195],a[56:59],v[76:79],v[192:195]\n" \
"s_or_b32 s58,s58,s59\nds_read_b128 v[88:91],v20 offset:25600\nv_mul_f32 v173,v17,v173\nM v[196:199],a[60:63],v[76:79],v[196:199]\ns_lshl_b32 s58,s58,2\nds_read_b128 v[76:79],v20 offset:18432\nv_mul_f32 v174,v17,v174\nM v[200:203],a[56:59],v[84:87],v[200:203]\n" \
"s_add_u32 s58,s58,%2\nv_mul_f32 v175,v17,v175\nv_fmac_f32 v160,v96,v19\nM v[204:207],a[60:63],v[84:87],v[204:207]\nbuffer_load_dwordx4 v2,s[36:39],s47 offen offset:2048 sc1 lds\nM v[192:195],a[64:67],v[92:95],v[192:195]\ns_mul_i32 s58,s58,12288\nds_read_b128 v[84:87],v20 offset:26624\n" \
"ds_read_b64_tr_b16 a[160:161],v6 offset:32768\nM v[196:199],a[68:71],v[92:95],v[196:199]\ns_waitcnt lgkmcnt(3)\ns_add_u32 s44,s58,109058048\nds_read_b128 v[92:95],v20 offset:19456\nv_fmac_f32 v161,v97,v19\nM v[200:203],a[64:67],v[72:75],v[200:203]\ns_add_u32 s58,s60,2\n" \
"ds_read_b64_tr_b16 a[162:163],v7 offset:32768\nv_fmac_f32 v162,v98,v19\nM v[204:207],a[68:71],v[72:75],v[204:207]\nbuffer_load_dwordx4 v2,s[36:39],s47 offen offset:3072 sc1 lds\nM v[192:195],a[72:75],v[80:83],v[192:195]\ns_add_u32 m0,s22,131072\nds_read_b128 v[72:75],v20 offset:27648\nds_read_b64_tr_b16 a[164:165],v8 offset:32768\n" \
"M v[196:199],a[76:79],v[80:83],v[196:199]\ns_add_u32 s47,s28,16384\nds_read_b128 v[80:83],v20 offset:20480\nv_fmac_f32 v163,v99,v19\nM v[200:203],a[72:75],v[88:91],v[200:203]\ns_and_b32 s58,s58,3\nds_read_b64_tr_b16 a[168:169],v6 offset:34816\nv_fmac_f32 v164,v100,v19\n" \
"M v[204:207],a[76:79],v[88:91],v[204:207]\ns_waitcnt lgkmcnt(4)\nbuffer_load_dwordx4 v1,s[84:87],s46 offen offset:0 lds\nM v[192:195],a[80:83],v[76:79],v[192:195]\ns_add_u32 m0,s22,131968\nds_read_b64_tr_b16 a[170:171],v7 offset:34816\nds_read_b64_tr_b16 a[172:173],v8 offset:34816\nM v[196:199],a[84:87],v[76:79],v[196:199]\n" \
"s_and_b32 s59,s58,2\nds_read_b128 v[88:91],v20 offset:28672\nv_fmac_f32 v165,v101,v19\nM v[200:203],a[80:83],v[84:87],v[200:203]\ns_add_u32 s59,s59,s58\nds_read_b128 v[76:79],v20 offset:21504\nv_xor_b32 v9,0x10000,v9\nM v[204:207],a[84:87],v[84:87],v[204:207]\n" \
"buffer_load_dwordx4 v1,s[84:87],s46 offen offset:128 lds\nds_read_b64_tr_b16 a[166:167],v9 offset:32768\nds_read_b64_tr_b16 a[174:175],v9 offset:34816\nM v[192:195],a[88:91],v[92:95],v[192:195]\ns_mov_b32 s46,1769472\ns_add_u32 m0,s22,133120\nds_read_b128 v[84:87],v20 offset:29696\nv_fmac_f32 v166,v102,v19\n" \
"M v[196:199],a[92:95],v[92:95],v[196:199]\ns_waitcnt lgkmcnt(5)\ns_lshl_b32 s59,s59,17\nds_read_b128 v[92:95],v20 offset:22528\nv_fmac_f32 v167,v103,v19\nM v[200:203],a[88:91],v[72:75],v[200:203]\ns_add_u32 s33,s94,s59\nv_fmac_f32 v168,v104,v19\n" \
"C v160,v160,v161\nM v[204:207],a[92:95],v[72:75],v[204:207]\nbuffer_load_dwordx4 v1,s[84:87],s46 offen offset:0 lds\nM v[192:195],a[96:99],v[80:83],v[192:195]\ns_add_u32 m0,s22,134016\nds_read_b128 v[72:75],v20 offset:30720\nC v161,v162,v163\nM v[196:199],a[100:103],v[80:83],v[196:199]\n" \
"s_waitcnt vmcnt(11) lgkmcnt(3)\nds_read_b128 v[80:83],v20 offset:23552\nC v162,v164,v165\nM v[200:203],a[96:99],v[88:91],v[200:203]\ns_lshl_b32 s58,s58,1\ns_barrier\nC v163,v166,v167\nM v[204:207],a[100:103],v[88:91],v[204:207]\n" \
"buffer_store_dwordx4 v[160:163],v4,s[36:39],s62 offen offset:0 sc1\nM v[192:195],a[104:107],v[76:79],v[192:195]\ns_and_b32 s59,%1,0xf9\nds_read_b128 v[88:91],v20 offset:31744\nv_fmac_f32 v169,v105,v19\nM v[196:199],a[108:111],v[76:79],v[196:199]\ns_waitcnt lgkmcnt(2)\ns_or_b32 s58,s58,s59\n" \
"ds_read_b128 v[76:79],v4 offset:0\nv_fmac_f32 v170,v106,v19\nM v[200:203],a[104:107],v[84:87],v[200:203]\ns_lshl_b32 s58,s58,2\nv_fmac_f32 v171,v107,v19\nv_fmac_f32 v172,v108,v19\nM v[204:207],a[108:111],v[84:87],v[204:207]\nbuffer_load_dwordx4 v1,s[84:87],s46 offen offset:128 lds\n" \
"M v[192:195],a[112:115],v[92:95],v[192:195]\ns_add_u32 m0,s22,147456\nds_read_b128 v[84:87],v4 offset:8192\nv_fmac_f32 v173,v109,v19\nM v[196:199],a[116:119],v[92:95],v[196:199]\ns_mov_b32 s46,1835008\nds_read_b128 v[92:95],v4 offset:1024\nv_fmac_f32 v174,v110,v19\n" \
"M v[200:203],a[112:115],v[72:75],v[200:203]\ns_add_u32 s58,s58,%2\nv_fmac_f32 v175,v111,v19\nC v164,v168,v169\nM v[204:207],a[116:119],v[72:75],v[204:207]\ns_waitcnt lgkmcnt(2)\nbuffer_load_dwordx4 v2,s[36:39],s47 offen offset:0 sc1 lds\nM v[192:195],a[120:123],v[80:83],v[192:195]\n" \
"s_mul_i32 s58,s58,12288\nds_read_b128 v[72:75],v4 offset:9216\nC v165,v170,v171\nM v[196:199],a[124:127],v[80:83],v[196:199]\ns_add_u32 s45,s58,109060096\nds_read_b128 v[80:83],v4 offset:2048\nC v166,v172,v173\nM v[200:203],a[120:123],v[88:91],v[200:203]\n" \
"s_add_u32 s58,s60,3\nds_read_b64_tr_b16 a[176:177],v6 offset:49152\nC v167,v174,v175\nM v[204:207],a[124:127],v[88:91],v[204:207]\nbuffer_store_dwordx4 v[164:167],v4,s[36:39],s62 offen offset:1024 sc1\nM v[208:211],a[0:3],v[76:79],0\ns_and_b32 s58,s58,3\nds_read_b128 v[88:91],v4 offset:10240\n" \
"ds_read_b64_tr_b16 a[178:179],v7 offset:49152\nM v[212:215],a[4:7],v[76:79],0\ns_waitcnt lgkmcnt(4)\ns_and_b32 s59,s58,2\nds_read_b128 v[76:79],v4 offset:3072\nv_mul_f32 v176,v17,v176\nM v[216:219],a[0:3],v[84:87],0\ns_add_u32 s59,s59,s58\n" \
"ds_read_b64_tr_b16 a[180:181],v8 offset:49152\nv_mul_f32 v177,v17,v177\nM v[220:223],a[4:7],v[84:87],0\nbuffer_load_dwordx4 v2,s[36:39],s47 offen offset:1024 sc1 lds\nM v[208:211],a[8:11],v[92:95],v[208:211]\ns_lshl_b32 s59,s59,17\nds_read_b128 v[84:87],v4 offset:11264\nds_read_b64_tr_b16 a[182:183],v9 offset:49152\n" \
"M v[212:215],a[12:15],v[92:95],v[212:215]\ns_add_u32 s34,s94,s59\nds_read_b128 v[92:95],v4 offset:4096\nv_mul_f32 v178,v17,v178\nM v[216:219],a[8:11],v[72:75],v[216:219]\ns_lshl_b32 s58,s58,1\nds_read_b64_tr_b16 a[184:185],v6 offset:51200\nv_mul_f32 v179,v17,v179\n" \
"M v[220:223],a[12:15],v[72:75],v[220:223]\ns_waitcnt lgkmcnt(4)\nbuffer_load_dwordx4 v2,s[36:39],s47 offen offset:2048 sc1 lds\nM v[208:211],a[16:19],v[80:83],v[208:211]\ns_and_b32 s59,%1,0xf9\nds_read_b64_tr_b16 a[186:187],v7 offset:51200\nds_read_b64_tr_b16 a[188:189],v8 offset:51200\nM v[212:215],a[20:23],v[80:83],v[212:215]\n" \
"s_or_b32 s58,s58,s59\nds_read_b64_tr_b16 a[190:191],v9 offset:51200\nv_mul_f32 v180,v17,v180\nM v[216:219],a[16:19],v[88:91],v[216:219]\ns_lshl_b32 s58,s58,2\nds_read_b128 v[72:75],v4 offset:12288\nv_mul_f32 v181,v17,v181\nM v[220:223],a[20:23],v[88:91],v[220:223]\n" \
"buffer_load_dwordx4 v2,s[36:39],s47 offen offset:3072 sc1 lds\nM v[208:211],a[24:27],v[76:79],v[208:211]\ns_add_u32 m0,s22,49152\ns_add_u32 s47,s29,16384\nds_read_b128 v[80:83],v4 offset:5120\nds_read_b128 v[88:91],v4 offset:13312\nM v[212:215],a[28:31],v[76:79],v[212:215]\ns_waitcnt lgkmcnt(4)\n" \
"s_add_u32 s58,s58,%2\nds_read_b128 v[76:79],v4 offset:6144\nv_mul_f32 v182,v17,v182\nM v[216:219],a[24:27],v[84:87],v[216:219]\ns_mul_i32 s58,s58,12288\nv_mul_f32 v183,v17,v183\nv_fmac_f32 v176,v112,v19\nM v[220:223],a[28:31],v[84:87],v[220:223]\n" \
"buffer_load_dwordx4 v2,s[36:39],s47 offen offset:0 sc1 lds\nM v[208:211],a[32:35],v[92:95],v[208:211]\ns_add_u32 s48,s58,109062144\nds_read_b128 v[84:87],v4 offset:14336\nv_fmac_f32 v177,v113,v19\nM v[212:215],a[36:39],v[92:95],v[212:215]\ns_waitcnt vmcnt(11) lgkmcnt(5)\ns_waitcnt lgkmcnt(2)\n" \
"ds_read_b128 v[92:95],v4 offset:7168\nv_fmac_f32 v178,v114,v19\nM v[216:219],a[32:35],v[72:75],v[216:219]\ns_add_u32 s58,s60,4\ns_barrier\nv_fmac_f32 v179,v115,v19\nM v[220:223],a[36:39],v[72:75],v[220:223]\nbuffer_load_dwordx4 v2,s[36:39],s47 offen offset:1024 sc1 lds\n" \
"M v[208:211],a[40:43],v[80:83],v[208:211]\ns_and_b32 s58,s58,3\nds_read_b128 v[72:75],v4 offset:15360\nv_fmac_f32 v180,v116,v19\nM v[212:215],a[44:47],v[80:83],v[212:215]\ns_and_b32 s59,s58,2\nds_read_b128 v[80:83],v4 offset:16384\nv_fmac_f32 v181,v117,v19\n" \
"M v[216:219],a[40:43],v[88:91],v[216:219]\ns_add_u32 s59,s59,s58\nv_fmac_f32 v182,v118,v19\nv_fmac_f32 v183,v119,v19\nM v[220:223],a[44:47],v[88:91],v[220:223]\ns_waitcnt lgkmcnt(2)\nbuffer_load_dwordx4 v2,s[36:39],s47 offen offset:2048 sc1 lds\nM v[208:211],a[48:51],v[76:79],v[208:211]\n" \
"s_and_b32 s58,%1,0xf9\nds_read_b128 v[88:91],v4 offset:24576\nC v168,v176,v177\nM v[212:215],a[52:55],v[76:79],v[212:215]\ns_lshl_b32 s59,s59,17\nds_read_b128 v[76:79],v4 offset:17408\nC v169,v178,v179\nM v[216:219],a[48:51],v[84:87],v[216:219]\n" \
"s_lshl_b32 s58,s58,2\nC v170,v180,v181\nC v171,v182,v183\nM v[220:223],a[52:55],v[84:87],v[220:223]\nbuffer_store_dwordx4 v[168:171],v4,s[36:39],s62 offen offset:2048 sc1\nM v[208:211],a[56:59],v[92:95],v[208:211]\ns_add_u32 s35,s94,s59\nds_read_b128 v[84:87],v4 offset:25600\n" \
"v_mul_f32 v184,v17,v184\nM v[212:215],a[60:63],v[92:95],v[212:215]\ns_waitcnt lgkmcnt(2)\ns_add_u32 s58,s58,%2\nds_read_b128 v[92:95],v4 offset:18432\nv_mul_f32 v185,v17,v185\nM v[216:219],a[56:59],v[72:75],v[216:219]\ns_sub_u32 s59,3,s60\n" \
"v_mul_f32 v186,v17,v186\nv_mul_f32 v187,v17,v187\nM v[220:223],a[60:63],v[72:75],v[220:223]\nbuffer_load_dwordx4 v2,s[36:39],s47 offen offset:3072 sc1 lds\nM v[208:211],a[64:67],v[80:83],v[208:211]\ns_add_u32 m0,s22,65536\nds_read_b128 v[72:75],v4 offset:26624\nv_mul_f32 v188,v17,v188\n" \
"M v[212:215],a[68:71],v[80:83],v[212:215]\ns_add_u32 s47,s30,16384\nds_read_b128 v[80:83],v4 offset:19456\nv_mul_f32 v189,v17,v189\nM v[216:219],a[64:67],v[88:91],v[216:219]\ns_lshl_b32 s95,s58,5\nv_mul_f32 v190,v17,v190\nv_mul_f32 v191,v17,v191\n" \
"M v[220:223],a[68:71],v[88:91],v[220:223]\ns_waitcnt lgkmcnt(2)\nbuffer_load_dwordx4 v1,s[84:87],s46 offen offset:0 lds\nM v[208:211],a[72:75],v[76:79],v[208:211]\ns_add_u32 m0,s22,66432\nds_read_b128 v[88:91],v4 offset:27648\nv_fmac_f32 v184,v120,v19\nM v[212:215],a[76:79],v[76:79],v[212:215]\n" \
"s_and_b32 s59,s59,3\nds_read_b128 v[76:79],v4 offset:20480\nv_fmac_f32 v185,v121,v19\nM v[216:219],a[72:75],v[84:87],v[216:219]\ns_mul_i32 s58,s58,12288\nv_fmac_f32 v186,v122,v19\nv_fmac_f32 v187,v123,v19\nM v[220:223],a[76:79],v[84:87],v[220:223]\n" \
"buffer_load_dwordx4 v1,s[84:87],s46 offen offset:128 lds\nM v[208:211],a[80:83],v[92:95],v[208:211]\ns_add_u32 m0,s22,67584\ns_mov_b32 s46,1900544\nds_read_b128 v[84:87],v4 offset:28672\nv_fmac_f32 v188,v124,v19\nM v[212:215],a[84:87],v[92:95],v[212:215]\ns_waitcnt lgkmcnt(2)\n" \
"s_lshl_b32 s59,s59,11\nds_read_b128 v[92:95],v4 offset:21504\nv_fmac_f32 v189,v125,v19\nM v[216:219],a[80:83],v[72:75],v[216:219]\ns_add_u32 s59,s59,s58\nv_xor_b32 v6,0x10000,v6\nv_xor_b32 v7,0x10000,v7\nv_xor_b32 v8,0x10000,v8\n" \
"v_xor_b32 v9,0x10000,v9\nM v[220:223],a[84:87],v[72:75],v[220:223]\nbuffer_load_dwordx4 v1,s[84:87],s46 offen offset:0 lds\nds_read_b64_tr_b16 a[192:193],v6 offset:32768\nds_read_b64_tr_b16 a[194:195],v7 offset:32768\nds_read_b64_tr_b16 a[196:197],v8 offset:32768\nds_read_b64_tr_b16 a[198:199],v9 offset:32768\nds_read_b64_tr_b16 a[200:201],v6 offset:34816\n" \
"ds_read_b64_tr_b16 a[202:203],v7 offset:34816\nds_read_b64_tr_b16 a[204:205],v8 offset:34816\nds_read_b64_tr_b16 a[206:207],v9 offset:34816\nM v[208:211],a[88:91],v[80:83],v[208:211]\ns_add_u32 m0,s22,68480\nv_fmac_f32 v190,v126,v19\nv_fmac_f32 v191,v127,v19\nM v[212:215],a[92:95],v[80:83],v[212:215]\n" \
"s_add_u32 s59,s59,109051904\nds_read_b128 v[72:75],v4 offset:29696\nC v172,v184,v185\nM v[216:219],a[88:91],v[88:91],v[216:219]\ns_cmp_eq_u32 s60,0\ns_cselect_b32 s40,0x40000000,s59\nds_read_b128 v[80:83],v4 offset:22528\nC v173,v186,v187\n" \
"M v[220:223],a[92:95],v[88:91],v[220:223]\ns_waitcnt lgkmcnt(10)\nbuffer_load_dwordx4 v1,s[84:87],s46 offen offset:128 lds\nM v[208:211],a[96:99],v[76:79],v[208:211]\ns_add_u32 m0,s22,98304\ns_mov_b32 s46,1966080\nds_read_b128 v[88:91],v4 offset:30720\nC v174,v188,v189\n" \
"M v[212:215],a[100:103],v[76:79],v[212:215]\ns_waitcnt vmcnt(4) lgkmcnt(3)\nds_read_b128 v[76:79],v4 offset:23552\nC v175,v190,v191\nM v[216:219],a[96:99],v[84:87],v[216:219]\ns_sub_u32 s59,4,s60\ns_barrier\nv_xor_b32 v6,0x20000,v6\n" \
"M v[220:223],a[100:103],v[84:87],v[220:223]\nbuffer_store_dwordx4 v[172:175],v4,s[36:39],s62 offen offset:3072 sc1\nM v[208:211],a[104:107],v[92:95],v[208:211]\ns_and_b32 s59,s59,3\nds_read_b128 v[84:87],v4 offset:31744\nv_xor_b32 v7,0x20000,v7\nM v[212:215],a[108:111],v[92:95],v[212:215]\ns_waitcnt lgkmcnt(2)\n" \
"s_lshl_b32 s59,s59,11\nds_read_b128 v[92:95],v23 offset:16384\nv_xor_b32 v8,0x20000,v8\nM v[216:219],a[104:107],v[72:75],v[216:219]\ns_add_u32 s59,s59,s58\nds_read_b64_tr_b16 a[208:209],v6 offset:0\nv_xor_b32 v9,0x20000,v9\nM v[220:223],a[108:111],v[72:75],v[220:223]\n" \
"buffer_load_dwordx4 v1,s[84:87],s46 offen offset:0 lds\nM v[208:211],a[112:115],v[80:83],v[208:211]\ns_add_u32 m0,s22,99200\nds_read_b128 v[72:75],v23 offset:24576\nds_read_b64_tr_b16 a[210:211],v7 offset:0\nM v[212:215],a[116:119],v[80:83],v[212:215]\ns_add_u32 s59,s59,109150208\nds_read_b128 v[80:83],v23 offset:17408\n" \
"v_mov_b32 v29,s11\nM v[216:219],a[112:115],v[88:91],v[216:219]\ns_cmp_eq_u32 s60,1\ns_cselect_b32 s41,0x40000000,s59\nds_read_b64_tr_b16 a[212:213],v8 offset:0\nv_and_b32 v30,0xf0,v4\nM v[220:223],a[116:119],v[88:91],v[220:223]\ns_waitcnt lgkmcnt(4)\n" \
"buffer_load_dwordx4 v1,s[84:87],s46 offen offset:128 lds\nM v[208:211],a[120:123],v[76:79],v[208:211]\ns_add_u32 m0,s22,100352\ns_mov_b32 s46,2031616\nds_read_b128 v[88:91],v23 offset:25600\nds_read_b64_tr_b16 a[214:215],v9 offset:0\nM v[212:215],a[124:127],v[76:79],v[212:215]\ns_sub_u32 s59,5,s60\n" \
"ds_read_b128 v[76:79],v23 offset:18432\nv_lshrrev_b32 v15,2,v30\nM v[216:219],a[120:123],v[84:87],v[216:219]\ns_and_b32 s59,s59,3\nds_read_b64_tr_b16 a[216:217],v6 offset:2048\nv_xor_b32 v6,0x30000,v6\nM v[220:223],a[124:127],v[84:87],v[220:223]\nbuffer_load_dwordx4 v1,s[84:87],s46 offen offset:0 lds\n" \
"M v[48:51],a[128:131],v[92:95],v[48:51]\ns_add_u32 m0,s22,101248\nds_read_b128 v[84:87],v23 offset:26624\nds_read_b64_tr_b16 a[218:219],v7 offset:2048\nM v[52:55],a[132:135],v[92:95],v[52:55]\ns_waitcnt lgkmcnt(4)\ns_lshl_b32 s59,s59,11\nds_read_b128 v[92:95],v23 offset:19456\n" \
"v_xor_b32 v7,0x30000,v7\nM v[64:67],a[128:131],v[72:75],v[64:67]\ns_add_u32 s59,s59,s58\nds_read_b64_tr_b16 a[220:221],v8 offset:2048\nv_xor_b32 v8,0x30000,v8\nM v[68:71],a[132:135],v[72:75],v[68:71]\nbuffer_load_dwordx4 v1,s[84:87],s46 offen offset:128 lds\nM v[48:51],a[136:139],v[80:83],v[48:51]\n" \
"s_add_u32 m0,s22,81920\nds_read_b128 v[72:75],v23 offset:27648\nds_read_b64_tr_b16 a[222:223],v9 offset:2048\nM v[52:55],a[140:143],v[80:83],v[52:55]\ns_add_u32 s59,s59,109248512\nds_read_b128 v[80:83],v23 offset:20480\nv_xor_b32 v9,0x30000,v9\nM v[64:67],a[136:139],v[88:91],v[64:67]\n" \
"s_cmp_eq_u32 s60,2\ns_cselect_b32 s42,0x40000000,s59\nv_lshlrev_b32 v13,7,v30\nv_lshlrev_b32 v14,3,v30\nM v[68:71],a[140:143],v[88:91],v[68:71]\ns_waitcnt lgkmcnt(3)\nbuffer_load_dwordx4 v2,s[36:39],s47 offen offset:0 sc1 lds\nM v[48:51],a[144:147],v[76:79],v[48:51]\n" \
"s_sub_u32 s59,6,s60\nds_read_b128 v[88:91],v23 offset:28672\nv_bfe_u32 v30,v4,8,1\nM v[52:55],a[148:151],v[76:79],v[52:55]\ns_and_b32 s59,s59,3\nds_read_b128 v[76:79],v23 offset:21504\nv_lshl_add_u32 v13,v30,5,v13\nM v[64:67],a[144:147],v[84:87],v[64:67]\n" \
"s_lshl_b32 s59,s59,11\nv_lshl_add_u32 v14,v30,4,v14\nv_bfe_u32 v30,v4,7,2\nM v[68:71],a[148:151],v[84:87],v[68:71]\nbuffer_load_dwordx4 v2,s[36:39],s47 offen offset:1024 sc1 lds\nM v[48:51],a[152:155],v[92:95],v[48:51]\ns_add_u32 s59,s59,s58\nds_read_b128 v[84:87],v23 offset:29696\n" \
"v_lshrrev_b32 v31,9,v4\nM v[52:55],a[156:159],v[92:95],v[52:55]\ns_waitcnt lgkmcnt(2)\ns_add_u32 s59,s59,109346816\nds_read_b128 v[92:95],v23 offset:22528\nv_lshl_add_u32 v13,v31,4,v13\nM v[64:67],a[152:155],v[72:75],v[64:67]\ns_and_b32 s58,s61,6\n" \
"v_lshl_add_u32 v14,v31,5,v14\nv_lshrrev_b32 v190,4,v4\nM v[68:71],a[156:159],v[72:75],v[68:71]\nbuffer_load_dwordx4 v2,s[36:39],s47 offen offset:2048 sc1 lds\nM v[48:51],a[160:163],v[80:83],v[48:51]\ns_cmp_eq_u32 s60,3\ns_cselect_b32 s43,0x40000000,s59\nds_read_b128 v[72:75],v23 offset:30720\n" \
"M v[52:55],a[164:167],v[80:83],v[52:55]\ns_add_u32 s58,s58,s61\nds_read_b128 v[80:83],v23 offset:23552\nM v[64:67],a[160:163],v[88:91],v[64:67]\ns_lshl_b32 s59,s60,4\nM v[68:71],a[164:167],v[88:91],v[68:71]\ns_waitcnt lgkmcnt(2)\nbuffer_load_dwordx4 v2,s[36:39],s47 offen offset:3072 sc1 lds\n" \
"M v[48:51],a[168:171],v[76:79],v[48:51]\ns_add_u32 m0,s22,114688\ns_add_u32 s47,s31,16384\nds_read_b128 v[88:91],v23 offset:31744\nM v[52:55],a[172:175],v[76:79],v[52:55]\nLG22_%=:\ns_cbranch_execz LHR2_%=\ns_add_u32 s92,s58,s59\n" \
"ds_read_b128 v[76:79],v4 offset:49152\nM v[64:67],a[168:171],v[84:87],v[64:67]\ns_and_b32 s58,s60,2\nM v[68:71],a[172:175],v[84:87],v[68:71]\nbuffer_load_dwordx4 v2,s[36:39],s47 offen offset:0 sc1 lds\nM v[48:51],a[176:179],v[92:95],v[48:51]\ns_add_u32 s58,s58,s60\nds_read_b128 v[84:87],v4 offset:57344\n" \
"M v[52:55],a[180:183],v[92:95],v[52:55]\ns_waitcnt lgkmcnt(2)\ns_lshl_b32 s59,s61,3\nds_read_b128 v[92:95],v4 offset:50176\nM v[64:67],a[176:179],v[72:75],v[64:67]\ns_lshr_b32 s63,%1,5\nM v[68:71],a[180:183],v[72:75],v[68:71]\nbuffer_load_dwordx4 v2,s[36:39],s47 offen offset:1024 sc1 lds\n" \
"M v[48:51],a[184:187],v[80:83],v[48:51]\ns_add_u32 s58,s58,s59\nds_read_b128 v[72:75],v4 offset:58368\nM v[52:55],a[188:191],v[80:83],v[52:55]\ns_lshl_b32 s63,s63,2\nds_read_b128 v[80:83],v4 offset:51200\nM v[64:67],a[184:187],v[88:91],v[64:67]\ns_add_u32 s93,s58,64\n" \
"M v[68:71],a[188:191],v[88:91],v[68:71]\ns_waitcnt lgkmcnt(2)\nbuffer_load_dwordx4 v2,s[36:39],s47 offen offset:2048 sc1 lds\nM v[32:35],a[128:131],v[76:79],v[32:35]\ns_add_u32 s63,s63,%2\nds_read_b128 v[88:91],v4 offset:59392\nM v[36:39],a[132:135],v[76:79],v[36:39]\ns_lshl_b32 s58,s92,15\n" \
"ds_read_b128 v[76:79],v4 offset:52224\nM v[40:43],a[128:131],v[84:87],v[40:43]\ns_lshl_b32 s59,s63,6\nM v[44:47],a[132:135],v[84:87],v[44:47]\nbuffer_load_dwordx4 v2,s[36:39],s47 offen offset:3072 sc1 lds\nM v[32:35],a[136:139],v[92:95],v[32:35]\ns_add_u32 m0,s22,0\nds_read_b128 v[84:87],v4 offset:60416\n" \
"M v[36:39],a[140:143],v[92:95],v[36:39]\ns_waitcnt lgkmcnt(2)\ns_add_u32 s47,s28,24576\nds_read_b128 v[92:95],v4 offset:53248\nM v[40:43],a[136:139],v[72:75],v[40:43]\ns_add_u32 s80,s58,s59\nM v[44:47],a[140:143],v[72:75],v[44:47]\nbuffer_load_dwordx4 v2,s[36:39],s47 offen offset:0 sc1 lds\n" \
"M v[32:35],a[144:147],v[80:83],v[32:35]\ns_and_b32 s58,s58,0x3e0000\nds_read_b128 v[72:75],v4 offset:61440\nM v[36:39],a[148:151],v[80:83],v[36:39]\ns_lshl_b32 s59,s63,12\nds_read_b128 v[80:83],v4 offset:54272\nM v[40:43],a[144:147],v[88:91],v[40:43]\ns_add_u32 s58,s58,s59\n" \
"M v[44:47],a[148:151],v[88:91],v[44:47]\ns_waitcnt lgkmcnt(2)\nbuffer_load_dwordx4 v2,s[36:39],s47 offen offset:1024 sc1 lds\nM v[32:35],a[152:155],v[76:79],v[32:35]\ns_and_b32 s59,s92,1\nds_read_b128 v[88:91],v4 offset:62464\nM v[36:39],a[156:159],v[76:79],v[36:39]\ns_lshl_b32 s59,s59,6\n" \
"ds_read_b128 v[76:79],v4 offset:55296\nM v[40:43],a[152:155],v[84:87],v[40:43]\ns_add_u32 s58,s58,s59\nM v[44:47],a[156:159],v[84:87],v[44:47]\nbuffer_load_dwordx4 v2,s[36:39],s47 offen offset:2048 sc1 lds\nM v[32:35],a[160:163],v[92:95],v[32:35]\ns_waitcnt vmcnt(15)\nds_read_b128 v[84:87],v4 offset:63488\n" \
"M v[36:39],a[164:167],v[92:95],v[36:39]\ns_waitcnt vmcnt(3) lgkmcnt(14)\ns_waitcnt lgkmcnt(2)\nds_read_b128 v[92:95],v4 offset:56320\nM v[40:43],a[160:163],v[72:75],v[40:43]\ns_add_u32 s82,s58,67108864\ns_barrier\nds_read_b64_tr_b16 a[224:225],v6 offset:0\n" \
"M v[44:47],a[164:167],v[72:75],v[44:47]\nbuffer_store_dwordx2 v[28:29],v56,s[36:39],s50 offen offset:0 sc1\nM v[32:35],a[168:171],v[80:83],v[32:35]\ns_lshl_b32 s58,s93,15\nds_read_b128 v[72:75],v4 offset:64512\nds_read_b64_tr_b16 a[226:227],v7 offset:0\nM v[36:39],a[172:175],v[80:83],v[36:39]\ns_lshl_b32 s59,s63,6\n" \
"ds_read_b128 v[80:83],v20 offset:16384\nM v[40:43],a[168:171],v[88:91],v[40:43]\ns_add_u32 s81,s58,s59\nds_read_b64_tr_b16 a[228:229],v8 offset:0\nM v[44:47],a[172:175],v[88:91],v[44:47]\ns_waitcnt lgkmcnt(4)\nbuffer_load_dwordx4 v2,s[36:39],s47 offen offset:3072 sc1 lds\nM v[32:35],a[176:179],v[76:79],v[32:35]\n" \
"s_add_u32 m0,s22,32768\ns_add_u32 s47,s29,24576\nds_read_b128 v[88:91],v20 offset:24576\nds_read_b64_tr_b16 a[230:231],v9 offset:0\nM v[36:39],a[180:183],v[76:79],v[36:39]\ns_and_b32 s58,s58,0x3e0000\nds_read_b128 v[76:79],v20 offset:17408\nM v[40:43],a[176:179],v[84:87],v[40:43]\n" \
"s_lshl_b32 s59,s63,12\nds_read_b64_tr_b16 a[232:233],v6 offset:2048\nM v[44:47],a[180:183],v[84:87],v[44:47]\nbuffer_load_dwordx4 v2,s[36:39],s47 offen offset:0 sc1 lds\nM v[32:35],a[184:187],v[92:95],v[32:35]\ns_add_u32 s58,s58,s59\nds_read_b128 v[84:87],v20 offset:25600\nds_read_b64_tr_b16 a[234:235],v7 offset:2048\n" \
"M v[36:39],a[188:191],v[92:95],v[36:39]\ns_waitcnt lgkmcnt(4)\ns_and_b32 s59,s93,1\nds_read_b128 v[92:95],v20 offset:18432\nM v[40:43],a[184:187],v[72:75],v[40:43]\ns_lshl_b32 s59,s59,6\nds_read_b64_tr_b16 a[236:237],v8 offset:2048\nM v[44:47],a[188:191],v[72:75],v[44:47]\n" \
"buffer_load_dwordx4 v2,s[36:39],s47 offen offset:1024 sc1 lds\nM v[192:195],a[128:131],v[80:83],v[192:195]\ns_add_u32 s58,s58,s59\nds_read_b128 v[72:75],v20 offset:26624\nds_read_b64_tr_b16 a[238:239],v9 offset:2048\nM v[196:199],a[132:135],v[80:83],v[196:199]\ns_add_u32 s83,s58,67108864\nds_read_b128 v[80:83],v20 offset:19456\n" \
"M v[200:203],a[128:131],v[88:91],v[200:203]\ns_lshl_b32 s58,s61,10\nds_read_b64_tr_b16 a[240:241],v6 offset:32768\nM v[204:207],a[132:135],v[88:91],v[204:207]\ns_waitcnt lgkmcnt(4)\nbuffer_load_dwordx4 v2,s[36:39],s47 offen offset:2048 sc1 lds\nM v[192:195],a[136:139],v[76:79],v[192:195]\ns_lshl_b32 s59,s60,6\n" \
"ds_read_b128 v[88:91],v20 offset:27648\nds_read_b64_tr_b16 a[242:243],v7 offset:32768\nM v[196:199],a[140:143],v[76:79],v[196:199]\ns_add_u32 s58,s58,s59\nds_read_b128 v[76:79],v20 offset:20480\nv_mov_b32 v31,0x3f80\nM v[200:203],a[136:139],v[84:87],v[200:203]\ns_add_u32 s58,s58,94396416\n" \
"ds_read_b64_tr_b16 a[244:245],v8 offset:32768\nv_lshl_add_u32 v58,v30,8,s58\nM v[204:207],a[140:143],v[84:87],v[204:207]\nbuffer_load_dwordx4 v2,s[36:39],s47 offen offset:3072 sc1 lds\nM v[192:195],a[144:147],v[92:95],v[192:195]\ns_add_u32 m0,s22,16384\nds_read_b128 v[84:87],v20 offset:28672\nds_read_b64_tr_b16 a[246:247],v9 offset:32768\n" \
"M v[196:199],a[148:151],v[92:95],v[196:199]\ns_waitcnt lgkmcnt(4)\ns_add_u32 s47,s30,24576\nds_read_b128 v[92:95],v20 offset:21504\nv_bfe_u32 v30,v4,4,3\nM v[200:203],a[144:147],v[72:75],v[200:203]\ns_add_u32 s95,s95,94437376\nds_read_b64_tr_b16 a[248:249],v6 offset:34816\n" \
"v_lshl_add_u32 v58,v30,3,v58\nM v[204:207],a[148:151],v[72:75],v[204:207]\ns_add_u32 s58,s95,24\nds_read_b128 v[72:75],v20 offset:29696\nv_and_b32 v30,0x30,v4\nM v[192:195],a[152:155],v[80:83],v[192:195]\nbuffer_load_dwordx4 v2,s[36:39],s47 offen offset:0 sc1 lds\nM v[196:199],a[156:159],v[80:83],v[196:199]\n" \
"s_mov_b32 s59,0x30000300\nds_read_b128 v[80:83],v20 offset:22528\nds_read_b64_tr_b16 a[250:251],v7 offset:34816\nM v[200:203],a[152:155],v[88:91],v[200:203]\ns_lshl_b32 s92,s92,6\nds_read_b64_tr_b16 a[252:253],v8 offset:34816\ns_mov_b32 exec_lo,0\nv_lshl_add_u32 v58,v30,4,s58\n" \
"s_mov_b32 exec_lo,-1\ns_nop 1\nM v[204:207],a[156:159],v[88:91],v[204:207]\ns_waitcnt lgkmcnt(4)\ns_mov_b32 s58,0x300003\nds_read_b128 v[88:91],v20 offset:30720\nv_and_b32 v30,16,v4\nM v[192:195],a[160:163],v[76:79],v[192:195]\n" \
"buffer_load_dwordx4 v2,s[36:39],s47 offen offset:1024 sc1 lds\nM v[196:199],a[164:167],v[76:79],v[196:199]\ns_add_u32 s92,s92,71303168\nds_read_b128 v[76:79],v20 offset:23552\nds_read_b64_tr_b16 a[254:255],v9 offset:34816\nM v[200:203],a[160:163],v[84:87],v[200:203]\ns_lshl_b32 s93,s93,6\nv_lshlrev_b32 v30,v30,v31\n" \
"v_cndmask_b32 v186,0,v30,s[58:59]\nM v[204:207],a[164:167],v[84:87],v[204:207]\ns_mov_b32 s58,0xc0000c\nds_read_b128 v[84:87],v20 offset:31744\nv_mov_b32 v188,0\nM v[192:195],a[168:171],v[92:95],v[192:195]\nbuffer_load_dword v16,v15,s[36:39],s92 offen offset:0 sc1\nM v[196:199],a[172:175],v[92:95],v[196:199]\n" \
"s_waitcnt lgkmcnt(3)\ns_mov_b32 s59,0xc0000c00\nds_read_b128 v[92:95],v20 offset:49152\nv_cndmask_b32 v187,0,v30,s[58:59]\nM v[200:203],a[168:171],v[72:75],v[200:203]\ns_mov_b32 s58,0x3000003\nv_mov_b32 v189,0\nv_mov_b32 v184,0\n" \
"M v[204:207],a[172:175],v[72:75],v[204:207]\ns_mov_b32 s59,0x0\nds_read_b128 v[72:75],v20 offset:57344\nv_cndmask_b32 v224,0,v30,s[58:59]\nM v[192:195],a[176:179],v[80:83],v[192:195]\nbuffer_load_dwordx4 v2,s[36:39],s47 offen offset:2048 sc1 lds\nM v[196:199],a[180:183],v[80:83],v[196:199]\ns_mov_b32 s58,0xc00000c\n" \
"ds_read_b128 v[80:83],v20 offset:50176\nv_mov_b32 v185,0\nM v[200:203],a[176:179],v[88:91],v[200:203]\ns_mov_b32 s59,0x0\nv_cndmask_b32 v225,0,v30,s[58:59]\nM v[204:207],a[180:183],v[88:91],v[204:207]\ns_waitcnt lgkmcnt(2)\ns_mov_b32 s58,0x30000030\n" \
"ds_read_b128 v[88:91],v20 offset:58368\nM v[192:195],a[184:187],v[76:79],v[192:195]\nbuffer_load_dwordx4 v2,s[36:39],s47 offen offset:3072 sc1 lds\nM v[196:199],a[188:191],v[76:79],v[196:199]\ns_add_u32 m0,s22,131072\nds_read_b128 v[76:79],v20 offset:51200\nM v[200:203],a[184:187],v[84:87],v[200:203]\ns_add_u32 s47,s31,24576\n" \
"M v[204:207],a[188:191],v[84:87],v[204:207]\ns_mov_b32 s59,0x0\nds_read_b128 v[84:87],v20 offset:59392\nv_cndmask_b32 v226,0,v30,s[58:59]\nM v[208:211],a[128:131],v[92:95],v[208:211]\nbuffer_load_dwordx4 v2,s[36:39],s47 offen offset:0 sc1 lds\nM v[212:215],a[132:135],v[92:95],v[212:215]\ns_waitcnt lgkmcnt(2)\n" \
"s_mov_b32 s58,0xc00000c0\nds_read_b128 v[92:95],v20 offset:52224\nM v[216:219],a[128:131],v[72:75],v[216:219]\ns_mov_b32 s59,0x0\nv_cndmask_b32 v227,0,v30,s[58:59]\nM v[220:223],a[132:135],v[72:75],v[220:223]\ns_mov_b32 s58,0x0\nds_read_b128 v[72:75],v20 offset:60416\n" \
"M v[208:211],a[136:139],v[80:83],v[208:211]\nbuffer_load_dwordx4 v2,s[36:39],s47 offen offset:1024 sc1 lds\nM v[212:215],a[140:143],v[80:83],v[212:215]\ns_mov_b32 s59,0x3000003\nds_read_b128 v[80:83],v20 offset:53248\nv_cndmask_b32 v228,0,v30,s[58:59]\nM v[216:219],a[136:139],v[88:91],v[216:219]\ns_mov_b32 s58,0x0\n" \
"M v[220:223],a[140:143],v[88:91],v[220:223]\ns_waitcnt lgkmcnt(2)\ns_mov_b32 s59,0xc00000c\nds_read_b128 v[88:91],v20 offset:61440\nv_cndmask_b32 v229,0,v30,s[58:59]\nM v[208:211],a[144:147],v[76:79],v[208:211]\nbuffer_load_dwordx4 v2,s[36:39],s47 offen offset:2048 sc1 lds\nM v[212:215],a[148:151],v[76:79],v[212:215]\n" \
"s_mov_b32 s58,0x0\nds_read_b128 v[76:79],v20 offset:54272\nM v[216:219],a[144:147],v[84:87],v[216:219]\ns_mov_b32 s59,0x30000030\nv_cndmask_b32 v230,0,v30,s[58:59]\nM v[220:223],a[148:151],v[84:87],v[220:223]\ns_mov_b32 s58,0x0\nds_read_b128 v[84:87],v20 offset:62464\n" \
"M v[208:211],a[152:155],v[92:95],v[208:211]\nbuffer_load_dword v17,v15,s[36:39],s92 offen offset:128 sc1\nM v[212:215],a[156:159],v[92:95],v[212:215]\ns_waitcnt lgkmcnt(2)\ns_mov_b32 s59,0xc00000c0\nds_read_b128 v[92:95],v20 offset:55296\nv_cndmask_b32 v231,0,v30,s[58:59]\nM v[216:219],a[152:155],v[72:75],v[216:219]\n" \
"s_add_u32 s58,s80,0\nv_add_u32 v31,s58,v13\nM v[220:223],a[156:159],v[72:75],v[220:223]\ns_add_u32 s58,s80,65536\nds_read_b128 v[72:75],v20 offset:63488\nM v[208:211],a[160:163],v[80:83],v[208:211]\nbuffer_load_dwordx4 v2,s[36:39],s47 offen offset:3072 sc1 lds\nM v[212:215],a[164:167],v[80:83],v[212:215]\n" \
"s_waitcnt vmcnt(10) lgkmcnt(14)\nds_read_b128 v[80:83],v20 offset:56320\nM v[216:219],a[160:163],v[88:91],v[216:219]\ns_add_u32 s93,s93,71303168\ns_barrier\nM v[220:223],a[164:167],v[88:91],v[220:223]\ns_waitcnt lgkmcnt(2)\ns_add_u32 m0,s22,147456\n" \
"ds_read_b128 v[88:91],v20 offset:64512\nM v[208:211],a[168:171],v[76:79],v[208:211]\nglobal_load_dwordx4 v[160:163],v31,s[12:13]\nM v[212:215],a[172:175],v[76:79],v[212:215]\ns_add_u32 s47,s32,0\nds_read_b128 v[76:79],v4 offset:0\nv_add_u32 v31,s58,v13\nM v[216:219],a[168:171],v[84:87],v[216:219]\n" \
"s_add_u32 s58,s81,0\nM v[220:223],a[172:175],v[84:87],v[220:223]\ns_sub_u32 s49,s72,8192\nds_read_b128 v[84:87],v4 offset:8192\nM v[208:211],a[176:179],v[92:95],v[208:211]\nglobal_load_dwordx4 v[164:167],v31,s[12:13]\nM v[212:215],a[180:183],v[92:95],v[212:215]\ns_waitcnt lgkmcnt(2)\n" \
"s_add_u32 s63,s49,0\nds_read_b128 v[92:95],v4 offset:1024\nv_add_u32 v31,s58,v13\nM v[216:219],a[176:179],v[72:75],v[216:219]\ns_add_u32 s58,s81,65536\nM v[220:223],a[180:183],v[72:75],v[220:223]\ns_add_u32 s59,s80,65536\nds_read_b128 v[72:75],v4 offset:9216\n" \
"M v[208:211],a[184:187],v[80:83],v[208:211]\nglobal_load_dwordx4 v[168:171],v31,s[12:13]\nM v[212:215],a[188:191],v[80:83],v[212:215]\ns_waitcnt lgkmcnt(2)\nds_read_b128 v[80:83],v4 offset:2048\nv_add_u32 v31,s58,v13\nM v[216:219],a[184:187],v[88:91],v[216:219]\ns_add_u32 s58,s60,1\n" \
"v_add_u32 v191,s58,v190\nv_and_b32 v191,3,v191\nM v[220:223],a[188:191],v[88:91],v[220:223]\ns_lshl_b32 s58,s60,8\nds_read_b128 v[88:91],v4 offset:10240\nv_lshlrev_b32 v191,8,v191\nM v[48:51],a[192:195],v[76:79],v[48:51]\nbuffer_load_dwordx4 v[96:99],v14,s[36:39],s82 offen offset:0 sc1\n" \
"M v[52:55],a[196:199],v[76:79],v[52:55]\ns_add_u32 s58,s58,s95\nds_read_b128 v[76:79],v4 offset:3072\nv_lshl_add_u32 v191,v190,3,v191\nM v[64:67],a[192:195],v[84:87],v[64:67]\nv_add_u32 v190,1,v190\nv_lshrrev_b32 v190,2,v190\nM v[68:71],a[196:199],v[84:87],v[68:71]\n" \
"s_waitcnt lgkmcnt(2)\nds_read_b128 v[84:87],v4 offset:11264\nv_lshl_add_u32 v191,v190,27,v191\nM v[48:51],a[200:203],v[92:95],v[48:51]\nbuffer_load_dword v18,v15,s[36:39],s93 offen offset:0 sc1\nM v[52:55],a[204:207],v[92:95],v[52:55]\nds_read_b128 v[92:95],v4 offset:4096\nv_bfe_u32 v190,v4,4,2\n" \
"M v[64:67],a[200:203],v[72:75],v[64:67]\nv_lshl_add_u32 v190,v190,3,s58\nM v[68:71],a[204:207],v[72:75],v[68:71]\ns_add_u32 s58,s80,0\nds_read_b128 v[72:75],v4 offset:12288\nM v[48:51],a[208:211],v[80:83],v[48:51]\nbuffer_load_dwordx4 v[100:103],v14,s[36:39],s82 offen offset:2048 sc1\nM v[52:55],a[212:215],v[80:83],v[52:55]\n" \
"s_waitcnt lgkmcnt(2)\nds_read_b128 v[80:83],v4 offset:5120\nM v[64:67],a[208:211],v[88:91],v[64:67]\nM v[68:71],a[212:215],v[88:91],v[68:71]\nds_read_b128 v[88:91],v4 offset:13312\nM v[48:51],a[216:219],v[76:79],v[48:51]\nbuffer_load_dwordx4 v[104:107],v14,s[36:39],s83 offen offset:0 sc1\nM v[52:55],a[220:223],v[76:79],v[52:55]\n" \
"ds_read_b128 v[76:79],v4 offset:6144\nM v[64:67],a[216:219],v[84:87],v[64:67]\nM v[68:71],a[220:223],v[84:87],v[68:71]\ns_waitcnt lgkmcnt(2)\nds_read_b128 v[84:87],v4 offset:14336\nM v[48:51],a[224:227],v[92:95],v[48:51]\nbuffer_load_dwordx2 v[10:11],v58,s[36:39],0 offen sc1\nM v[52:55],a[228:231],v[92:95],v[52:55]\n" \
"ds_read_b128 v[92:95],v4 offset:7168\nM v[64:67],a[224:227],v[72:75],v[64:67]\nM v[68:71],a[228:231],v[72:75],v[68:71]\nds_read_b128 v[72:75],v4 offset:15360\nM v[48:51],a[232:235],v[80:83],v[48:51]\nbuffer_load_dwordx4 v[108:111],v14,s[36:39],s83 offen offset:2048 sc1\nM v[52:55],a[236:239],v[80:83],v[52:55]\ns_waitcnt lgkmcnt(2)\n" \
"ds_read_b128 v[80:83],v4 offset:32768\nM v[64:67],a[232:235],v[88:91],v[64:67]\nM v[68:71],a[236:239],v[88:91],v[68:71]\nds_read_b128 v[88:91],v4 offset:40960\nM v[48:51],a[240:243],v[76:79],v[48:51]\nglobal_load_dwordx4 v[172:175],v31,s[12:13]\nM v[52:55],a[244:247],v[76:79],v[52:55]\nds_read_b128 v[76:79],v4 offset:33792\n" \
"M v[64:67],a[240:243],v[84:87],v[64:67]\nM v[68:71],a[244:247],v[84:87],v[68:71]\ns_waitcnt lgkmcnt(2)\nds_read_b128 v[84:87],v4 offset:41984\nM v[48:51],a[248:251],v[92:95],v[48:51]\nbuffer_load_dword v19,v15,s[36:39],s93 offen offset:128 sc1\nM v[52:55],a[252:255],v[92:95],v[52:55]\nds_read_b128 v[92:95],v4 offset:34816\n" \
"M v[64:67],a[248:251],v[72:75],v[64:67]\nC v48,v48,v49\nC v49,v50,v51\nM v[68:71],a[252:255],v[72:75],v[68:71]\nds_read_b128 v[72:75],v4 offset:43008\nC v50,v52,v53\nM v[32:35],a[192:195],v[80:83],v[32:35]\ns_waitcnt lgkmcnt(2)\n" \
"C v51,v54,v55\nC v64,v64,v65\nM v[36:39],a[196:199],v[80:83],v[36:39]\nbuffer_store_dwordx4 v[48:51],v4,s[36:39],s40 offen offset:0 sc1\nM v[40:43],a[192:195],v[88:91],v[40:43]\nds_read_b128 v[80:83],v4 offset:35840\nC v65,v66,v67\nM v[44:47],a[196:199],v[88:91],v[44:47]\n" \
"ds_read_b128 v[88:91],v4 offset:44032\nC v66,v68,v69\nM v[32:35],a[200:203],v[76:79],v[32:35]\nC v67,v70,v71\nv_cndmask_b32 v120,v120,v48,s[96:97]\nM v[36:39],a[204:207],v[76:79],v[36:39]\nds_read_b128 v[76:79],v4 offset:36864\nv_cndmask_b32 v121,v121,v49,s[96:97]\n" \
"M v[40:43],a[200:203],v[84:87],v[40:43]\nv_cndmask_b32 v122,v122,v50,s[96:97]\nv_cndmask_b32 v123,v123,v51,s[96:97]\nM v[44:47],a[204:207],v[84:87],v[44:47]\ns_waitcnt lgkmcnt(2)\nds_read_b128 v[84:87],v4 offset:45056\nv_cndmask_b32 v124,v124,v64,s[96:97]\nM v[32:35],a[208:211],v[92:95],v[32:35]\n" \
"v_cndmask_b32 v125,v125,v65,s[96:97]\nv_cndmask_b32 v126,v126,v66,s[96:97]\nM v[36:39],a[212:215],v[92:95],v[36:39]\nbuffer_store_dwordx4 v[64:67],v4,s[36:39],s40 offen offset:1024 sc1\nM v[40:43],a[208:211],v[72:75],v[40:43]\nds_read_b128 v[92:95],v4 offset:37888\nv_cndmask_b32 v127,v127,v67,s[96:97]\nM v[44:47],a[212:215],v[72:75],v[44:47]\n" \
"s_cmp_eq_u32 s60,1\ns_cselect_b64 s[96:97],-1,0\nds_read_b128 v[72:75],v4 offset:46080\nM v[32:35],a[216:219],v[80:83],v[32:35]\nM v[36:39],a[220:223],v[80:83],v[36:39]\ns_waitcnt lgkmcnt(2)\nds_read_b128 v[80:83],v4 offset:38912\nM v[40:43],a[216:219],v[88:91],v[40:43]\n" \
"M v[44:47],a[220:223],v[88:91],v[44:47]\nds_read_b128 v[88:91],v4 offset:47104\nM v[32:35],a[224:227],v[76:79],v[32:35]\nM v[36:39],a[228:231],v[76:79],v[36:39]\ns_waitcnt vmcnt(13) lgkmcnt(14)\nds_read_b128 v[76:79],v4 offset:39936\nM v[40:43],a[224:227],v[84:87],v[40:43]\ns_barrier\n" \
"M v[44:47],a[228:231],v[84:87],v[44:47]\ns_waitcnt lgkmcnt(2)\nds_read_b128 v[84:87],v4 offset:48128\nM v[32:35],a[232:235],v[92:95],v[32:35]\nM v[36:39],a[236:239],v[92:95],v[36:39]\nds_read_b128 v[92:95],v4 offset:16384\nM v[40:43],a[232:235],v[72:75],v[40:43]\nM v[44:47],a[236:239],v[72:75],v[44:47]\n" \
"s_waitcnt lgkmcnt(2)\nds_read_b128 v[72:75],v4 offset:24576\nM v[32:35],a[240:243],v[80:83],v[32:35]\ns_waitcnt vmcnt(5)\nv_cmp_gt_u64 s[98:99],s[14:15],v[10:11]\nv_cmp_le_u64 s[18:19],s[16:17],v[10:11]\nM v[36:39],a[244:247],v[80:83],v[36:39]\ns_or_b64 s[98:99],s[98:99],s[18:19]\n" \
"s_cbranch_scc1 LPL_%=\nLPLR_%=:\nbuffer_load_dwordx4 v2,s[36:39],s47 offen offset:0 sc1 lds\nM v[40:43],a[240:243],v[88:91],v[40:43]\nds_read_b128 v[80:83],v4 offset:17408\nM v[44:47],a[244:247],v[88:91],v[44:47]\nds_read_b128 v[88:91],v4 offset:25600\nM v[32:35],a[248:251],v[76:79],v[32:35]\n" \
"s_waitcnt lgkmcnt(2)\nM v[36:39],a[252:255],v[76:79],v[36:39]\nbuffer_load_dwordx4 v2,s[36:39],s47 offen offset:1024 sc1 lds\nM v[40:43],a[248:251],v[84:87],v[40:43]\nds_read_b128 v[76:79],v4 offset:18432\nC v32,v32,v33\nM v[44:47],a[252:255],v[84:87],v[44:47]\nds_read_b128 v[84:87],v4 offset:26624\n" \
"C v33,v34,v35\nM v[192:195],a[192:195],v[92:95],v[192:195]\nC v34,v36,v37\nC v35,v38,v39\nM v[196:199],a[196:199],v[92:95],v[196:199]\nbuffer_load_dwordx4 v2,s[36:39],s47 offen offset:2048 sc1 lds\nM v[200:203],a[192:195],v[72:75],v[200:203]\nds_read_b128 v[92:95],v4 offset:19456\n" \
"C v40,v40,v41\nM v[204:207],a[196:199],v[72:75],v[204:207]\ns_waitcnt lgkmcnt(2)\nds_read_b128 v[72:75],v4 offset:27648\nC v41,v42,v43\nM v[192:195],a[200:203],v[80:83],v[192:195]\ns_waitcnt vmcnt(5)\nC v42,v44,v45\n" \
"C v43,v46,v47\nM v[196:199],a[204:207],v[80:83],v[196:199]\nbuffer_load_dwordx4 v2,s[36:39],s47 offen offset:3072 sc1 lds\nM v[200:203],a[200:203],v[88:91],v[200:203]\ns_add_u32 s47,s32,8192\ns_add_u32 m0,s22,49152\nds_read_b128 v[80:83],v4 offset:20480\nv_cndmask_b32 v120,v120,v32,s[96:97]\n" \
"M v[204:207],a[204:207],v[88:91],v[204:207]\nds_read_b128 v[88:91],v4 offset:28672\nv_cndmask_b32 v121,v121,v33,s[96:97]\nM v[192:195],a[208:211],v[76:79],v[192:195]\ns_waitcnt lgkmcnt(2)\nv_cndmask_b32 v122,v122,v34,s[96:97]\nv_cndmask_b32 v123,v123,v35,s[96:97]\nM v[196:199],a[212:215],v[76:79],v[196:199]\n" \
"buffer_load_dwordx4 v2,s[36:39],s47 offen offset:0 sc1 lds\nM v[200:203],a[208:211],v[84:87],v[200:203]\nds_read_b128 v[76:79],v4 offset:21504\nv_cndmask_b32 v124,v124,v40,s[96:97]\nM v[204:207],a[212:215],v[84:87],v[204:207]\nds_read_b128 v[84:87],v4 offset:29696\nv_cndmask_b32 v125,v125,v41,s[96:97]\nM v[192:195],a[216:219],v[92:95],v[192:195]\n" \
"v_cndmask_b32 v126,v126,v42,s[96:97]\nv_cndmask_b32 v127,v127,v43,s[96:97]\nM v[196:199],a[220:223],v[92:95],v[196:199]\nbuffer_load_dwordx4 v2,s[36:39],s47 offen offset:1024 sc1 lds\nM v[200:203],a[216:219],v[72:75],v[200:203]\ns_cmp_eq_u32 s60,2\ns_cselect_b64 s[96:97],-1,0\nds_read_b128 v[92:95],v4 offset:22528\n" \
"v_sub_f32 v24,1.0,v16\nM v[204:207],a[220:223],v[72:75],v[204:207]\ns_waitcnt lgkmcnt(2)\nds_read_b128 v[72:75],v4 offset:30720\nC v24,v24,v24\nM v[192:195],a[224:227],v[80:83],v[192:195]\nv_permlane16_swap_b32 v160,v162\nM v[196:199],a[228:231],v[80:83],v[196:199]\n" \
"buffer_load_dwordx4 v2,s[36:39],s47 offen offset:2048 sc1 lds\nM v[200:203],a[224:227],v[88:91],v[200:203]\nds_read_b128 v[80:83],v4 offset:23552\nv_lshlrev_b32 v30,16,v160\nM v[204:207],a[228:231],v[88:91],v[204:207]\nds_read_b128 v[88:91],v4 offset:31744\nv_and_b32 v160,0xffff0000,v160\nM v[192:195],a[232:235],v[76:79],v[192:195]\n" \
"s_waitcnt lgkmcnt(2)\nv_permlane16_swap_b32 v161,v163\nM v[196:199],a[236:239],v[76:79],v[196:199]\nbuffer_load_dwordx4 v2,s[36:39],s47 offen offset:3072 sc1 lds\nM v[200:203],a[232:235],v[84:87],v[200:203]\ns_add_u32 m0,s22,65536\nds_read_b128 v[76:79],v23 offset:0\nv_and_b32 v24,0xffff0000,v24\n" \
"M v[204:207],a[236:239],v[84:87],v[204:207]\ns_add_u32 s47,s32,16384\nds_read_b128 v[84:87],v23 offset:8192\nv_sub_f32 v25,1.0,v17\nM v[192:195],a[240:243],v[92:95],v[192:195]\nv_permlane16_swap_b32 v164,v166\nM v[196:199],a[244:247],v[92:95],v[196:199]\nbuffer_load_dwordx4 v2,s[36:39],s47 offen offset:0 sc1 lds\n" \
"M v[200:203],a[240:243],v[72:75],v[200:203]\nLG23_%=:\ns_cbranch_execz LHR2_%=\nds_read_b128 v[92:95],v23 offset:1024\nC v25,v25,v25\nM v[204:207],a[244:247],v[72:75],v[204:207]\ns_waitcnt lgkmcnt(2)\nds_read_b128 v[72:75],v23 offset:9216\n" \
"v_and_b32 v25,0xffff0000,v25\nM v[192:195],a[248:251],v[80:83],v[192:195]\nv_permlane16_swap_b32 v165,v167\nM v[196:199],a[252:255],v[80:83],v[196:199]\nbuffer_load_dwordx4 v2,s[36:39],s47 offen offset:1024 sc1 lds\nM v[200:203],a[248:251],v[88:91],v[200:203]\nds_read_b128 v[80:83],v23 offset:2048\nC v192,v192,v193\n" \
"M v[204:207],a[252:255],v[88:91],v[204:207]\nds_read_b128 v[88:91],v23 offset:10240\nC v193,v194,v195\nM v[128:131],v[96:99],v[224:227],0\nC v194,v196,v197\nC v195,v198,v199\nM v[132:135],v[100:103],v[224:227],0\nbuffer_load_dwordx4 v2,s[36:39],s47 offen offset:2048 sc1 lds\n" \
"M v[136:139],v[96:99],v[228:231],0\nC v200,v200,v201\nC v201,v202,v203\nM v[140:143],v[100:103],v[228:231],0\nC v202,v204,v205\nC v203,v206,v207\nM v[144:147],v[104:107],v[224:227],0\nv_cndmask_b32 v120,v120,v192,s[96:97]\n" \
"v_cndmask_b32 v121,v121,v193,s[96:97]\nM v[148:151],v[108:111],v[224:227],0\nbuffer_load_dwordx4 v2,s[36:39],s47 offen offset:3072 sc1 lds\nM v[152:155],v[104:107],v[228:231],0\ns_add_u32 m0,s22,98304\nv_cndmask_b32 v122,v122,v194,s[96:97]\nv_cndmask_b32 v123,v123,v195,s[96:97]\nM v[156:159],v[108:111],v[228:231],0\n" \
"s_add_u32 s47,s32,24576\nv_cndmask_b32 v124,v124,v200,s[96:97]\nv_cndmask_b32 v125,v125,v201,s[96:97]\nM v[208:211],a[192:195],v[76:79],v[208:211]\ns_waitcnt lgkmcnt(0)\nv_cndmask_b32 v126,v126,v202,s[96:97]\nv_cndmask_b32 v127,v127,v203,s[96:97]\nM v[212:215],a[196:199],v[76:79],v[212:215]\n" \
"buffer_load_dwordx4 v2,s[36:39],s47 offen offset:0 sc1 lds\nM v[216:219],a[192:195],v[84:87],v[216:219]\ns_cmp_eq_u32 s60,3\ns_cselect_b64 s[96:97],-1,0\nds_read_b128 v[76:79],v23 offset:3072\nv_mul_f32 v128,v128,v30\nM v[220:223],a[196:199],v[84:87],v[220:223]\nds_read_b128 v[84:87],v23 offset:11264\n" \
"v_lshlrev_b32 v30,16,v161\nM v[208:211],a[200:203],v[92:95],v[208:211]\nv_mul_f32 v130,v130,v30\nv_lshlrev_b32 v30,16,v162\nM v[212:215],a[204:207],v[92:95],v[212:215]\nbuffer_load_dwordx4 v2,s[36:39],s47 offen offset:1024 sc1 lds\nM v[216:219],a[200:203],v[72:75],v[216:219]\nds_read_b128 v[92:95],v23 offset:4096\n" \
"v_mul_f32 v132,v132,v30\nM v[220:223],a[204:207],v[72:75],v[220:223]\nds_read_b128 v[72:75],v23 offset:12288\nv_lshlrev_b32 v30,16,v163\nM v[208:211],a[208:211],v[80:83],v[208:211]\nv_mul_f32 v134,v134,v30\nv_lshlrev_b32 v30,16,v164\nM v[212:215],a[212:215],v[80:83],v[212:215]\n" \
"buffer_load_dwordx4 v2,s[36:39],s47 offen offset:2048 sc1 lds\nM v[216:219],a[208:211],v[88:91],v[216:219]\nds_read_b128 v[80:83],v23 offset:5120\nv_mul_f32 v136,v136,v30\nM v[220:223],a[212:215],v[88:91],v[220:223]\ns_waitcnt lgkmcnt(2)\nds_read_b128 v[88:91],v23 offset:13312\nv_lshlrev_b32 v30,16,v165\n" \
"M v[208:211],a[216:219],v[76:79],v[208:211]\nv_mul_f32 v138,v138,v30\nv_lshlrev_b32 v30,16,v166\nM v[212:215],a[220:223],v[76:79],v[212:215]\nbuffer_load_dwordx4 v2,s[36:39],s47 offen offset:3072 sc1 lds\nM v[216:219],a[216:219],v[84:87],v[216:219]\ns_add_u32 m0,s22,81920\nds_read_b128 v[76:79],v23 offset:6144\n" \
"v_mul_f32 v140,v140,v30\nM v[220:223],a[220:223],v[84:87],v[220:223]\ns_add_u32 s47,s33,0\nds_read_b128 v[84:87],v23 offset:14336\nv_lshlrev_b32 v30,16,v167\nM v[208:211],a[224:227],v[92:95],v[208:211]\ns_waitcnt lgkmcnt(2)\nv_mul_f32 v142,v142,v30\n" \
"v_and_b32 v161,0xffff0000,v161\nM v[212:215],a[228:231],v[92:95],v[212:215]\ns_waitcnt vmcnt(8) lgkmcnt(14)\nds_read_b128 v[92:95],v23 offset:7168\nv_and_b32 v162,0xffff0000,v162\nM v[216:219],a[224:227],v[72:75],v[216:219]\ns_barrier\nv_mul_f32 v129,v129,v160\n" \
"M v[220:223],a[228:231],v[72:75],v[220:223]\nbuffer_load_dwordx4 v2,s[36:39],s47 offen offset:0 sc1 lds\nM v[208:211],a[232:235],v[80:83],v[208:211]\nds_read_b128 v[72:75],v23 offset:15360\nv_mul_f32 v133,v133,v162\nM v[212:215],a[236:239],v[80:83],v[212:215]\nds_read_b128 v[80:83],v23 offset:16384\nv_and_b32 v163,0xffff0000,v163\n" \
"M v[216:219],a[232:235],v[88:91],v[216:219]\ns_waitcnt lgkmcnt(2)\nv_permlane16_swap_b32 v168,v170\nM v[220:223],a[236:239],v[88:91],v[220:223]\nbuffer_load_dwordx4 v2,s[36:39],s47 offen offset:1024 sc1 lds\nM v[208:211],a[240:243],v[76:79],v[208:211]\nds_read_b128 v[88:91],v23 offset:24576\nv_lshlrev_b32 v30,16,v168\n" \
"M v[212:215],a[244:247],v[76:79],v[212:215]\nds_read_b128 v[76:79],v23 offset:17408\nv_mul_f32 v144,v144,v30\nM v[216:219],a[240:243],v[84:87],v[216:219]\nv_permlane16_swap_b32 v169,v171\nM v[220:223],a[244:247],v[84:87],v[220:223]\nbuffer_load_dwordx4 v2,s[36:39],s47 offen offset:2048 sc1 lds\nM v[208:211],a[248:251],v[92:95],v[208:211]\n" \
"ds_read_b128 v[84:87],v23 offset:25600\nv_mul_f32 v131,v131,v161\nM v[212:215],a[252:255],v[92:95],v[212:215]\ns_waitcnt lgkmcnt(2)\nds_read_b128 v[92:95],v23 offset:18432\nv_lshlrev_b32 v30,16,v169\nM v[216:219],a[248:251],v[72:75],v[216:219]\nC v208,v208,v209\n" \
"C v209,v210,v211\nM v[220:223],a[252:255],v[72:75],v[220:223]\nbuffer_load_dwordx4 v2,s[36:39],s47 offen offset:3072 sc1 lds\nM v[224:227],a[0:3],v[80:83],0\ns_add_u32 m0,s22,114688\nds_read_b128 v[72:75],v23 offset:26624\nC v210,v212,v213\nM v[228:231],a[4:7],v[80:83],0\n" \
"s_add_u32 s47,s33,8192\nds_read_b128 v[80:83],v23 offset:19456\nC v211,v214,v215\nM v[232:235],a[0:3],v[88:91],0\ns_waitcnt lgkmcnt(2)\nC v216,v216,v217\nC v217,v218,v219\nM v[236:239],a[4:7],v[88:91],0\n" \
"buffer_load_dwordx4 v2,s[36:39],s47 offen offset:0 sc1 lds\nM v[224:227],a[8:11],v[76:79],v[224:227]\nds_read_b128 v[88:91],v23 offset:27648\nC v218,v220,v221\nM v[228:231],a[12:15],v[76:79],v[228:231]\nds_read_b128 v[76:79],v23 offset:20480\nC v219,v222,v223\nM v[232:235],a[8:11],v[84:87],v[232:235]\n" \
"v_cndmask_b32 v120,v120,v208,s[96:97]\nv_cndmask_b32 v121,v121,v209,s[96:97]\nM v[236:239],a[12:15],v[84:87],v[236:239]\nbuffer_load_dwordx4 v2,s[36:39],s47 offen offset:1024 sc1 lds\nM v[224:227],a[16:19],v[92:95],v[224:227]\nds_read_b128 v[84:87],v23 offset:28672\nv_cndmask_b32 v122,v122,v210,s[96:97]\nM v[228:231],a[20:23],v[92:95],v[228:231]\n" \
"s_waitcnt lgkmcnt(2)\nds_read_b128 v[92:95],v23 offset:21504\nv_cndmask_b32 v123,v123,v211,s[96:97]\nM v[232:235],a[16:19],v[72:75],v[232:235]\nv_cndmask_b32 v124,v124,v216,s[96:97]\nv_cndmask_b32 v125,v125,v217,s[96:97]\nM v[236:239],a[20:23],v[72:75],v[236:239]\nbuffer_load_dwordx4 v2,s[36:39],s47 offen offset:2048 sc1 lds\n" \
"M v[224:227],a[24:27],v[80:83],v[224:227]\nds_read_b128 v[72:75],v23 offset:29696\nv_cndmask_b32 v126,v126,v218,s[96:97]\nM v[228:231],a[28:31],v[80:83],v[228:231]\nds_read_b128 v[80:83],v23 offset:22528\nv_cndmask_b32 v127,v127,v219,s[96:97]\nM v[232:235],a[24:27],v[88:91],v[232:235]\ns_waitcnt lgkmcnt(2)\n" \
"C v129,v128,v129\nv_mul_f32 v135,v135,v163\nM v[236:239],a[28:31],v[88:91],v[236:239]\nbuffer_load_dwordx4 v2,s[36:39],s47 offen offset:3072 sc1 lds\nM v[224:227],a[32:35],v[76:79],v[224:227]\ns_add_u32 m0,s22,0\nds_read_b128 v[88:91],v23 offset:30720\nC v133,v132,v133\n" \
"M v[228:231],a[36:39],v[76:79],v[228:231]\ns_add_u32 s47,s33,16384\nds_read_b128 v[76:79],v23 offset:23552\nv_mul_f32 v146,v146,v30\nM v[232:235],a[32:35],v[84:87],v[232:235]\nC v131,v130,v131\nC v135,v134,v135\nM v[236:239],a[36:39],v[84:87],v[236:239]\n" \
"buffer_load_dwordx4 v2,s[36:39],s47 offen offset:0 sc1 lds\nM v[224:227],a[40:43],v[92:95],v[224:227]\nds_read_b128 v[84:87],v23 offset:31744\nv_lshlrev_b32 v128,16,v129\nM v[228:231],a[44:47],v[92:95],v[228:231]\ns_waitcnt lgkmcnt(2)\nds_read_b128 v[92:95],v4 offset:49152\nv_lshlrev_b32 v132,16,v133\n" \
"M v[232:235],a[40:43],v[72:75],v[232:235]\nv_lshlrev_b32 v30,16,v170\nv_lshlrev_b32 v130,16,v131\nM v[236:239],a[44:47],v[72:75],v[236:239]\nbuffer_load_dwordx4 v2,s[36:39],s47 offen offset:1024 sc1 lds\nM v[224:227],a[48:51],v[80:83],v[224:227]\nds_read_b128 v[72:75],v4 offset:57344\nv_lshlrev_b32 v134,16,v135\n" \
"M v[228:231],a[52:55],v[80:83],v[228:231]\nds_read_b128 v[80:83],v4 offset:50176\nv_and_b32 v129,0xffff0000,v129\nM v[232:235],a[48:51],v[88:91],v[232:235]\ns_waitcnt lgkmcnt(2)\nv_mul_f32 v148,v148,v30\nv_and_b32 v133,0xffff0000,v133\nM v[236:239],a[52:55],v[88:91],v[236:239]\n" \
"buffer_load_dwordx4 v2,s[36:39],s47 offen offset:2048 sc1 lds\nM v[224:227],a[56:59],v[76:79],v[224:227]\nds_read_b128 v[88:91],v4 offset:58368\nv_and_b32 v131,0xffff0000,v131\nM v[228:231],a[60:63],v[76:79],v[228:231]\nds_read_b128 v[76:79],v4 offset:51200\nv_and_b32 v135,0xffff0000,v135\nM v[232:235],a[56:59],v[84:87],v[232:235]\n" \
"s_waitcnt lgkmcnt(2)\nv_and_b32 v164,0xffff0000,v164\nv_and_b32 v165,0xffff0000,v165\nM v[236:239],a[60:63],v[84:87],v[236:239]\nbuffer_load_dwordx4 v2,s[36:39],s47 offen offset:3072 sc1 lds\nM v[224:227],a[64:67],v[92:95],v[224:227]\ns_add_u32 m0,s22,32768\nds_read_b128 v[84:87],v4 offset:59392\n" \
"v_and_b32 v166,0xffff0000,v166\nM v[228:231],a[68:71],v[92:95],v[228:231]\ns_add_u32 s47,s33,24576\nds_read_b128 v[92:95],v4 offset:52224\nv_and_b32 v167,0xffff0000,v167\nM v[232:235],a[64:67],v[72:75],v[232:235]\nv_mul_f32 v128,v16,v128\nv_mul_f32 v129,v16,v129\n" \
"M v[236:239],a[68:71],v[72:75],v[236:239]\nbuffer_load_dwordx4 v2,s[36:39],s47 offen offset:0 sc1 lds\nM v[224:227],a[72:75],v[80:83],v[224:227]\nds_read_b128 v[72:75],v4 offset:60416\nv_lshlrev_b32 v30,16,v171\nM v[228:231],a[76:79],v[80:83],v[228:231]\ns_waitcnt lgkmcnt(2)\nds_read_b128 v[80:83],v4 offset:53248\n" \
"v_mul_f32 v132,v16,v132\nM v[232:235],a[72:75],v[88:91],v[232:235]\nv_mul_f32 v133,v16,v133\nv_mul_f32 v130,v16,v130\nM v[236:239],a[76:79],v[88:91],v[236:239]\nbuffer_load_dwordx4 v2,s[36:39],s47 offen offset:1024 sc1 lds\nM v[224:227],a[80:83],v[76:79],v[224:227]\nds_read_b128 v[88:91],v4 offset:61440\n" \
"v_mul_f32 v131,v16,v131\nM v[228:231],a[84:87],v[76:79],v[228:231]\nds_read_b128 v[76:79],v4 offset:54272\nv_mul_f32 v134,v16,v134\nM v[232:235],a[80:83],v[84:87],v[232:235]\ns_waitcnt lgkmcnt(2)\nv_mul_f32 v135,v16,v135\nv_mul_f32 v137,v137,v164\n" \
"M v[236:239],a[84:87],v[84:87],v[236:239]\nbuffer_load_dwordx4 v2,s[36:39],s47 offen offset:2048 sc1 lds\nM v[224:227],a[88:91],v[92:95],v[224:227]\nds_read_b128 v[84:87],v4 offset:62464\nv_mul_f32 v139,v139,v165\nM v[228:231],a[92:95],v[92:95],v[228:231]\nds_read_b128 v[92:95],v4 offset:55296\nv_mul_f32 v141,v141,v166\n" \
"M v[232:235],a[88:91],v[72:75],v[232:235]\ns_waitcnt lgkmcnt(2)\nv_mul_f32 v143,v143,v167\nC v129,v128,v129\nM v[236:239],a[92:95],v[72:75],v[236:239]\nbuffer_load_dwordx4 v2,s[36:39],s47 offen offset:3072 sc1 lds\nM v[224:227],a[96:99],v[80:83],v[224:227]\ns_add_u32 m0,s22,16384\n" \
"ds_read_b128 v[72:75],v4 offset:63488\nv_mul_f32 v150,v150,v30\nM v[228:231],a[100:103],v[80:83],v[228:231]\ns_waitcnt vmcnt(16) lgkmcnt(14)\nds_read_b128 v[80:83],v4 offset:56320\nC v133,v132,v133\nM v[232:235],a[96:99],v[88:91],v[232:235]\ns_add_u32 s47,s34,0\n" \
"s_barrier\nC v131,v130,v131\nM v[236:239],a[100:103],v[88:91],v[236:239]\nds_read_b128 v[88:91],v4 offset:64512\nC v135,v134,v135\nM v[224:227],a[104:107],v[76:79],v[224:227]\nbuffer_store_dwordx4 v[32:35],v4,s[36:39],s41 offen offset:0 sc1\nM v[228:231],a[108:111],v[76:79],v[228:231]\n" \
"s_waitcnt lgkmcnt(2)\nds_read_b128 v[76:79],v20 offset:0\nC v137,v136,v137\nM v[232:235],a[104:107],v[84:87],v[232:235]\nv_permlane16_swap_b32 v172,v174\nM v[236:239],a[108:111],v[84:87],v[236:239]\nds_read_b128 v[84:87],v20 offset:8192\nC v139,v138,v139\n" \
"M v[224:227],a[112:115],v[92:95],v[224:227]\nC v141,v140,v141\nC v143,v142,v143\nM v[228:231],a[116:119],v[92:95],v[228:231]\nds_read_b128 v[92:95],v20 offset:1024\nv_lshlrev_b32 v30,16,v172\nM v[232:235],a[112:115],v[72:75],v[232:235]\nv_lshlrev_b32 v128,16,v129\n" \
"v_lshlrev_b32 v132,16,v133\nM v[236:239],a[116:119],v[72:75],v[236:239]\ns_waitcnt lgkmcnt(2)\nds_read_b128 v[72:75],v20 offset:9216\nv_lshlrev_b32 v130,16,v131\nM v[224:227],a[120:123],v[80:83],v[224:227]\nv_lshlrev_b32 v134,16,v135\nv_lshlrev_b32 v136,16,v137\n" \
"M v[228:231],a[124:127],v[80:83],v[228:231]\nds_read_b128 v[80:83],v20 offset:2048\nv_lshlrev_b32 v138,16,v139\nM v[232:235],a[120:123],v[88:91],v[232:235]\nv_lshlrev_b32 v140,16,v141\nv_lshlrev_b32 v142,16,v143\nM v[236:239],a[124:127],v[88:91],v[236:239]\nds_read_b128 v[88:91],v20 offset:10240\n" \
"v_mul_f32 v152,v152,v30\nM v[224:227],a[128:131],v[76:79],v[224:227]\ns_waitcnt lgkmcnt(2)\nv_permlane16_swap_b32 v173,v175\nM v[228:231],a[132:135],v[76:79],v[228:231]\nbuffer_store_dwordx4 v[40:43],v4,s[36:39],s41 offen offset:1024 sc1\nM v[232:235],a[128:131],v[84:87],v[232:235]\nds_read_b128 v[76:79],v20 offset:3072\n" \
"v_and_b32 v129,0xffff0000,v129\nM v[236:239],a[132:135],v[84:87],v[236:239]\nds_read_b128 v[84:87],v20 offset:11264\nv_and_b32 v133,0xffff0000,v133\nM v[224:227],a[136:139],v[92:95],v[224:227]\nv_and_b32 v131,0xffff0000,v131\nv_and_b32 v135,0xffff0000,v135\nM v[228:231],a[140:143],v[92:95],v[228:231]\n" \
"ds_read_b128 v[92:95],v20 offset:4096\nv_and_b32 v137,0xffff0000,v137\nM v[232:235],a[136:139],v[72:75],v[232:235]\nv_and_b32 v139,0xffff0000,v139\nv_and_b32 v141,0xffff0000,v141\nM v[236:239],a[140:143],v[72:75],v[236:239]\ns_waitcnt lgkmcnt(2)\nds_read_b128 v[72:75],v20 offset:12288\n" \
"v_and_b32 v143,0xffff0000,v143\nM v[224:227],a[144:147],v[80:83],v[224:227]\nv_and_b32 v168,0xffff0000,v168\nv_lshlrev_b32 v30,16,v173\nM v[228:231],a[148:151],v[80:83],v[228:231]\nds_read_b128 v[80:83],v20 offset:5120\nv_and_b32 v169,0xffff0000,v169\nM v[232:235],a[144:147],v[88:91],v[232:235]\n" \
"v_mul_f32 v136,v17,v136\nv_mul_f32 v137,v17,v137\nM v[236:239],a[148:151],v[88:91],v[236:239]\nds_read_b128 v[88:91],v20 offset:13312\nv_and_b32 v170,0xffff0000,v170\nM v[224:227],a[152:155],v[76:79],v[224:227]\nv_and_b32 v171,0xffff0000,v171\nv_mul_f32 v138,v17,v138\n" \
"M v[228:231],a[156:159],v[76:79],v[228:231]\ns_waitcnt lgkmcnt(2)\nds_read_b128 v[76:79],v20 offset:6144\nv_mul_f32 v139,v17,v139\nM v[232:235],a[152:155],v[84:87],v[232:235]\nbuffer_store_dwordx4 v[192:195],v4,s[36:39],s42 offen offset:0 sc1\nM v[236:239],a[156:159],v[84:87],v[236:239]\nds_read_b128 v[84:87],v20 offset:14336\n" \
"v_mul_f32 v140,v17,v140\nM v[224:227],a[160:163],v[92:95],v[224:227]\nv_mul_f32 v141,v17,v141\nv_mul_f32 v142,v17,v142\nM v[228:231],a[164:167],v[92:95],v[228:231]\nds_read_b128 v[92:95],v20 offset:7168\nv_mul_f32 v143,v17,v143\nM v[232:235],a[160:163],v[72:75],v[232:235]\n" \
"v_mul_f32 v145,v145,v168\nv_mul_f32 v154,v154,v30\nM v[236:239],a[164:167],v[72:75],v[236:239]\ns_waitcnt lgkmcnt(2)\nds_read_b128 v[72:75],v20 offset:15360\nv_mul_f32 v147,v147,v169\nM v[224:227],a[168:171],v[80:83],v[224:227]\nC v137,v136,v137\n" \
"v_mul_f32 v149,v149,v170\nM v[228:231],a[172:175],v[80:83],v[228:231]\nds_read_b128 v[80:83],v20 offset:32768\nv_mul_f32 v151,v151,v171\nM v[232:235],a[168:171],v[88:91],v[232:235]\nC v139,v138,v139\nC v141,v140,v141\nM v[236:239],a[172:175],v[88:91],v[236:239]\n" \
"ds_read_b128 v[88:91],v20 offset:40960\nC v143,v142,v143\nM v[224:227],a[176:179],v[76:79],v[224:227]\nC v145,v144,v145\nv_lshlrev_b32 v30,16,v174\nM v[228:231],a[180:183],v[76:79],v[228:231]\ns_waitcnt lgkmcnt(2)\nds_read_b128 v[76:79],v20 offset:33792\n" \
"C v147,v146,v147\nM v[232:235],a[176:179],v[84:87],v[232:235]\nC v149,v148,v149\nv_lshlrev_b32 v136,16,v137\nM v[236:239],a[180:183],v[84:87],v[236:239]\nbuffer_store_dwordx4 v[200:203],v4,s[36:39],s42 offen offset:1024 sc1\nM v[224:227],a[184:187],v[92:95],v[224:227]\nds_read_b128 v[84:87],v20 offset:41984\n" \
"C v151,v150,v151\nM v[228:231],a[188:191],v[92:95],v[228:231]\nds_read_b128 v[92:95],v20 offset:34816\nv_lshlrev_b32 v138,16,v139\nM v[232:235],a[184:187],v[72:75],v[232:235]\nv_lshlrev_b32 v140,16,v141\nv_lshlrev_b32 v142,16,v143\nM v[236:239],a[188:191],v[72:75],v[236:239]\n" \
"s_waitcnt lgkmcnt(2)\nds_read_b128 v[72:75],v20 offset:43008\nv_lshlrev_b32 v144,16,v145\nM v[224:227],a[192:195],v[80:83],v[224:227]\nv_sub_f32 v26,1.0,v18\nv_mul_f32 v156,v156,v30\nM v[228:231],a[196:199],v[80:83],v[228:231]\nds_read_b128 v[80:83],v20 offset:35840\n" \
"v_lshlrev_b32 v148,16,v149\nM v[232:235],a[192:195],v[88:91],v[232:235]\nv_sub_f32 v27,1.0,v19\nv_lshlrev_b32 v146,16,v147\nM v[236:239],a[196:199],v[88:91],v[236:239]\nds_read_b128 v[88:91],v20 offset:44032\nv_lshlrev_b32 v150,16,v151\nM v[224:227],a[200:203],v[76:79],v[224:227]\n" \
"v_and_b32 v137,0xffff0000,v137\nv_and_b32 v139,0xffff0000,v139\nM v[228:231],a[204:207],v[76:79],v[228:231]\ns_waitcnt lgkmcnt(2)\nds_read_b128 v[76:79],v20 offset:36864\nv_and_b32 v141,0xffff0000,v141\nM v[232:235],a[200:203],v[84:87],v[232:235]\nv_and_b32 v143,0xffff0000,v143\n" \
"v_and_b32 v145,0xffff0000,v145\nM v[236:239],a[204:207],v[84:87],v[236:239]\nds_read_b128 v[84:87],v20 offset:45056\nv_lshlrev_b32 v30,16,v175\nM v[224:227],a[208:211],v[92:95],v[224:227]\nbuffer_store_dwordx4 v[208:211],v4,s[36:39],s43 offen offset:0 sc1\nM v[228:231],a[212:215],v[92:95],v[228:231]\nds_read_b128 v[92:95],v20 offset:37888\n" \
"C v26,v26,v26\nM v[232:235],a[208:211],v[72:75],v[232:235]\nv_and_b32 v149,0xffff0000,v149\nC v27,v27,v27\nM v[236:239],a[212:215],v[72:75],v[236:239]\ns_waitcnt lgkmcnt(2)\nds_read_b128 v[72:75],v20 offset:46080\nv_and_b32 v147,0xffff0000,v147\n" \
"M v[224:227],a[216:219],v[80:83],v[224:227]\nv_and_b32 v151,0xffff0000,v151\nv_and_b32 v172,0xffff0000,v172\nM v[228:231],a[220:223],v[80:83],v[228:231]\nds_read_b128 v[80:83],v20 offset:38912\nv_and_b32 v174,0xffff0000,v174\nM v[232:235],a[216:219],v[88:91],v[232:235]\nv_and_b32 v173,0xffff0000,v173\n" \
"v_mul_f32 v144,v18,v144\nM v[236:239],a[220:223],v[88:91],v[236:239]\nds_read_b128 v[88:91],v20 offset:47104\nv_and_b32 v175,0xffff0000,v175\nM v[224:227],a[224:227],v[76:79],v[224:227]\ns_waitcnt lgkmcnt(2)\nv_mul_f32 v145,v18,v145\nv_mul_f32 v148,v18,v148\n" \
"M v[228:231],a[228:231],v[76:79],v[228:231]\ns_waitcnt vmcnt(13) lgkmcnt(14)\nds_read_b128 v[76:79],v20 offset:39936\nv_and_b32 v26,0xffff0000,v26\nM v[232:235],a[224:227],v[84:87],v[232:235]\ns_barrier\nv_mul_f32 v149,v18,v149\nM v[236:239],a[228:231],v[84:87],v[236:239]\n" \
"ds_read_b128 v[84:87],v20 offset:48128\nv_and_b32 v27,0xffff0000,v27\nM v[224:227],a[232:235],v[92:95],v[224:227]\nv_mul_f32 v146,v18,v146\nv_mul_f32 v147,v18,v147\nM v[228:231],a[236:239],v[92:95],v[228:231]\nbuffer_store_dwordx4 v[216:219],v4,s[36:39],s43 offen offset:1024 sc1\nM v[232:235],a[232:235],v[72:75],v[232:235]\n" \
"ds_read_b128 v[92:95],v20 offset:16384\nv_mul_f32 v150,v18,v150\nM v[236:239],a[236:239],v[72:75],v[236:239]\ns_waitcnt lgkmcnt(2)\nds_read_b128 v[72:75],v20 offset:24576\nv_mul_f32 v151,v18,v151\nM v[224:227],a[240:243],v[80:83],v[224:227]\nv_mul_f32 v153,v153,v172\n" \
"v_mul_f32 v157,v157,v174\nM v[228:231],a[244:247],v[80:83],v[228:231]\nds_read_b128 v[80:83],v20 offset:17408\nv_mul_f32 v155,v155,v173\nM v[232:235],a[240:243],v[88:91],v[232:235]\nv_mul_f32 v158,v158,v30\nv_mul_f32 v159,v159,v175\nM v[236:239],a[244:247],v[88:91],v[236:239]\n" \
"ds_read_b128 v[88:91],v20 offset:25600\nC v145,v144,v145\nM v[224:227],a[248:251],v[76:79],v[224:227]\nC v149,v148,v149\nC v147,v146,v147\nM v[228:231],a[252:255],v[76:79],v[228:231]\ns_waitcnt lgkmcnt(2)\nds_read_b128 v[76:79],v20 offset:18432\n" \
"C v151,v150,v151\nM v[232:235],a[248:251],v[84:87],v[232:235]\nC v224,v224,v225\nC v225,v226,v227\nM v[236:239],a[252:255],v[84:87],v[236:239]\nds_read_b128 v[84:87],v20 offset:26624\nC v226,v228,v229\nM v[240:243],a[0:3],v[92:95],0\n" \
"C v227,v230,v231\nC v232,v232,v233\nM v[244:247],a[4:7],v[92:95],0\nds_read_b128 v[92:95],v20 offset:19456\nC v233,v234,v235\nM v[248:251],a[0:3],v[72:75],0\nbuffer_store_dwordx4 v[224:227],v4,s[36:39],s44 offen offset:0 sc1\nM v[252:255],a[4:7],v[72:75],0\n" \
"s_waitcnt lgkmcnt(2)\nds_read_b128 v[72:75],v20 offset:27648\nC v234,v236,v237\nM v[240:243],a[8:11],v[80:83],v[240:243]\nC v235,v238,v239\nC v153,v152,v153\nM v[244:247],a[12:15],v[80:83],v[244:247]\nds_read_b128 v[80:83],v20 offset:20480\n" \
"C v157,v156,v157\nM v[248:251],a[8:11],v[88:91],v[248:251]\nC v155,v154,v155\nC v159,v158,v159\nM v[252:255],a[12:15],v[88:91],v[252:255]\nds_read_b128 v[88:91],v20 offset:28672\nv_lshlrev_b32 v144,16,v145\nM v[240:243],a[16:19],v[76:79],v[240:243]\n" \
"v_lshlrev_b32 v148,16,v149\nv_lshlrev_b32 v146,16,v147\nM v[244:247],a[20:23],v[76:79],v[244:247]\ns_waitcnt lgkmcnt(2)\nds_read_b128 v[76:79],v20 offset:21504\nv_lshlrev_b32 v150,16,v151\nM v[248:251],a[16:19],v[84:87],v[248:251]\nv_lshlrev_b32 v152,16,v153\n" \
"v_lshlrev_b32 v156,16,v157\nM v[252:255],a[20:23],v[84:87],v[252:255]\nds_read_b128 v[84:87],v20 offset:29696\nv_lshlrev_b32 v154,16,v155\nM v[240:243],a[24:27],v[92:95],v[240:243]\nv_lshlrev_b32 v158,16,v159\nv_and_b32 v145,0xffff0000,v145\nM v[244:247],a[28:31],v[92:95],v[244:247]\n" \
"ds_read_b128 v[92:95],v20 offset:22528\nv_and_b32 v149,0xffff0000,v149\nM v[248:251],a[24:27],v[72:75],v[248:251]\ns_waitcnt lgkmcnt(2)\nv_and_b32 v147,0xffff0000,v147\nv_and_b32 v151,0xffff0000,v151\nM v[252:255],a[28:31],v[72:75],v[252:255]\nbuffer_store_dwordx4 v[232:235],v4,s[36:39],s44 offen offset:1024 sc1\n" \
"M v[240:243],a[32:35],v[80:83],v[240:243]\nds_read_b128 v[72:75],v20 offset:30720\nv_and_b32 v153,0xffff0000,v153\nM v[244:247],a[36:39],v[80:83],v[244:247]\nLG24_%=:\ns_cbranch_execz LHR2_%=\nds_read_b128 v[80:83],v20 offset:23552\nv_and_b32 v157,0xffff0000,v157\n" \
"M v[248:251],a[32:35],v[88:91],v[248:251]\nv_and_b32 v155,0xffff0000,v155\nv_and_b32 v159,0xffff0000,v159\nM v[252:255],a[36:39],v[88:91],v[252:255]\nds_read_b128 v[88:91],v20 offset:31744\nv_mul_f32 v152,v19,v152\nM v[240:243],a[40:43],v[76:79],v[240:243]\nv_mul_f32 v153,v19,v153\n" \
"v_mul_f32 v156,v19,v156\nM v[244:247],a[44:47],v[76:79],v[244:247]\ns_waitcnt lgkmcnt(2)\nds_read_b128 v[76:79],v20 offset:49152\nv_mul_f32 v157,v19,v157\nM v[248:251],a[40:43],v[84:87],v[248:251]\nv_mul_f32 v154,v19,v154\nv_mul_f32 v155,v19,v155\n" \
"M v[252:255],a[44:47],v[84:87],v[252:255]\nds_read_b128 v[84:87],v20 offset:57344\nv_mul_f32 v158,v19,v158\nM v[240:243],a[48:51],v[92:95],v[240:243]\nv_mul_f32 v159,v19,v159\nC v153,v152,v153\nM v[244:247],a[52:55],v[92:95],v[244:247]\nds_read_b128 v[92:95],v20 offset:50176\n" \
"C v157,v156,v157\nM v[248:251],a[48:51],v[72:75],v[248:251]\nC v155,v154,v155\nC v159,v158,v159\nM v[252:255],a[52:55],v[72:75],v[252:255]\ns_waitcnt lgkmcnt(2)\nds_read_b128 v[72:75],v20 offset:58368\nv_lshlrev_b32 v152,16,v153\n" \
"M v[240:243],a[56:59],v[80:83],v[240:243]\nbuffer_load_dwordx4 v2,s[36:39],s47 offen offset:0 sc1 lds\nM v[244:247],a[60:63],v[80:83],v[244:247]\nds_read_b128 v[80:83],v20 offset:51200\nv_lshlrev_b32 v156,16,v157\nM v[248:251],a[56:59],v[88:91],v[248:251]\nv_lshlrev_b32 v154,16,v155\nv_lshlrev_b32 v158,16,v159\n" \
"M v[252:255],a[60:63],v[88:91],v[252:255]\nds_read_b128 v[88:91],v20 offset:59392\nv_and_b32 v153,0xffff0000,v153\nM v[240:243],a[64:67],v[76:79],v[240:243]\nbuffer_load_dwordx4 v2,s[36:39],s47 offen offset:1024 sc1 lds\nM v[244:247],a[68:71],v[76:79],v[244:247]\ns_waitcnt lgkmcnt(2)\nds_read_b128 v[76:79],v20 offset:52224\n" \
"v_and_b32 v157,0xffff0000,v157\nM v[248:251],a[64:67],v[84:87],v[248:251]\nv_and_b32 v155,0xffff0000,v155\nv_and_b32 v159,0xffff0000,v159\nM v[252:255],a[68:71],v[84:87],v[252:255]\nds_read_b128 v[84:87],v20 offset:60416\nM v[240:243],a[72:75],v[92:95],v[240:243]\nM v[244:247],a[76:79],v[92:95],v[244:247]\n" \
"buffer_load_dwordx4 v2,s[36:39],s47 offen offset:2048 sc1 lds\nM v[248:251],a[72:75],v[72:75],v[248:251]\nds_read_b128 v[92:95],v20 offset:53248\nM v[252:255],a[76:79],v[72:75],v[252:255]\ns_waitcnt lgkmcnt(2)\nds_read_b128 v[72:75],v20 offset:61440\nM v[240:243],a[80:83],v[80:83],v[240:243]\nM v[244:247],a[84:87],v[80:83],v[244:247]\n" \
"buffer_load_dwordx4 v2,s[36:39],s47 offen offset:3072 sc1 lds\nM v[248:251],a[80:83],v[88:91],v[248:251]\ns_add_u32 m0,s22,131072\nds_read_b128 v[80:83],v20 offset:54272\nM v[252:255],a[84:87],v[88:91],v[252:255]\ns_add_u32 s47,s34,8192\nds_read_b128 v[88:91],v20 offset:62464\nM v[240:243],a[88:91],v[76:79],v[240:243]\n" \
"M v[244:247],a[92:95],v[76:79],v[244:247]\ns_waitcnt lgkmcnt(2)\nds_read_b128 v[76:79],v20 offset:55296\nM v[248:251],a[88:91],v[84:87],v[248:251]\nbuffer_load_dwordx4 v2,s[36:39],s47 offen offset:0 sc1 lds\nM v[252:255],a[92:95],v[84:87],v[252:255]\nds_read_b128 v[84:87],v20 offset:63488\nM v[240:243],a[96:99],v[92:95],v[240:243]\n" \
"s_waitcnt lgkmcnt(2)\nM v[244:247],a[100:103],v[92:95],v[244:247]\ns_waitcnt vmcnt(13) lgkmcnt(14)\nds_read_b128 v[92:95],v20 offset:56320\nM v[248:251],a[96:99],v[72:75],v[248:251]\ns_barrier\nM v[252:255],a[100:103],v[72:75],v[252:255]\nbuffer_load_dwordx4 v2,s[36:39],s47 offen offset:1024 sc1 lds\n" \
"M v[240:243],a[104:107],v[80:83],v[240:243]\nds_read_b128 v[72:75],v20 offset:64512\nM v[244:247],a[108:111],v[80:83],v[244:247]\nds_read_b128 v[80:83],v4 offset:0\nM v[248:251],a[104:107],v[88:91],v[248:251]\ns_waitcnt lgkmcnt(2)\nM v[252:255],a[108:111],v[88:91],v[252:255]\nbuffer_load_dwordx4 v2,s[36:39],s47 offen offset:2048 sc1 lds\n" \
"M v[240:243],a[112:115],v[76:79],v[240:243]\nds_read_b128 v[88:91],v4 offset:8192\nM v[244:247],a[116:119],v[76:79],v[244:247]\nds_read_b128 v[76:79],v4 offset:1024\nM v[248:251],a[112:115],v[84:87],v[248:251]\ns_waitcnt lgkmcnt(2)\nM v[252:255],a[116:119],v[84:87],v[252:255]\nbuffer_load_dwordx4 v2,s[36:39],s47 offen offset:3072 sc1 lds\n" \
"M v[240:243],a[120:123],v[92:95],v[240:243]\ns_add_u32 m0,s22,147456\nds_read_b128 v[84:87],v4 offset:9216\nM v[244:247],a[124:127],v[92:95],v[244:247]\ns_add_u32 s47,s34,16384\nds_read_b128 v[92:95],v4 offset:2048\nM v[248:251],a[120:123],v[72:75],v[248:251]\nM v[252:255],a[124:127],v[72:75],v[252:255]\n" \
"ds_read_b128 v[72:75],v4 offset:10240\nM v[240:243],a[128:131],v[80:83],v[240:243]\nbuffer_load_dwordx4 v2,s[36:39],s47 offen offset:0 sc1 lds\nM v[244:247],a[132:135],v[80:83],v[244:247]\ns_waitcnt lgkmcnt(2)\nds_read_b128 v[80:83],v4 offset:3072\nM v[248:251],a[128:131],v[88:91],v[248:251]\nM v[252:255],a[132:135],v[88:91],v[252:255]\n" \
"ds_read_b128 v[88:91],v4 offset:11264\nM v[240:243],a[136:139],v[76:79],v[240:243]\nbuffer_load_dwordx4 v2,s[36:39],s47 offen offset:1024 sc1 lds\nM v[244:247],a[140:143],v[76:79],v[244:247]\nds_read_b128 v[76:79],v4 offset:4096\nM v[248:251],a[136:139],v[84:87],v[248:251]\nM v[252:255],a[140:143],v[84:87],v[252:255]\ns_waitcnt lgkmcnt(2)\n" \
"ds_read_b128 v[84:87],v4 offset:12288\nM v[240:243],a[144:147],v[92:95],v[240:243]\nM v[244:247],a[148:151],v[92:95],v[244:247]\nbuffer_load_dwordx4 v2,s[36:39],s47 offen offset:2048 sc1 lds\nM v[248:251],a[144:147],v[72:75],v[248:251]\nds_read_b128 v[92:95],v4 offset:5120\nM v[252:255],a[148:151],v[72:75],v[252:255]\nds_read_b128 v[72:75],v4 offset:13312\n" \
"M v[240:243],a[152:155],v[80:83],v[240:243]\ns_waitcnt lgkmcnt(2)\nM v[244:247],a[156:159],v[80:83],v[244:247]\nbuffer_load_dwordx4 v2,s[36:39],s47 offen offset:3072 sc1 lds\nM v[248:251],a[152:155],v[88:91],v[248:251]\ns_add_u32 m0,s22,49152\nds_read_b128 v[80:83],v4 offset:6144\nM v[252:255],a[156:159],v[88:91],v[252:255]\n" \
"s_add_u32 s47,s34,24576\nds_read_b128 v[88:91],v4 offset:14336\nM v[240:243],a[160:163],v[76:79],v[240:243]\nM v[244:247],a[164:167],v[76:79],v[244:247]\nds_read_b128 v[76:79],v4 offset:7168\nM v[248:251],a[160:163],v[84:87],v[248:251]\nbuffer_load_dwordx4 v2,s[36:39],s47 offen offset:0 sc1 lds\nM v[252:255],a[164:167],v[84:87],v[252:255]\n" \
"s_waitcnt lgkmcnt(2)\nds_read_b128 v[84:87],v4 offset:15360\nM v[240:243],a[168:171],v[92:95],v[240:243]\nM v[244:247],a[172:175],v[92:95],v[244:247]\ns_waitcnt vmcnt(13)\nds_read_b128 v[92:95],v4 offset:32768\nM v[248:251],a[168:171],v[72:75],v[248:251]\nbuffer_store_dwordx2 v[28:29],v191,s[36:39],s95 offen offset:0 sc1\n" \
"M v[252:255],a[172:175],v[72:75],v[252:255]\nds_read_b128 v[72:75],v4 offset:40960\nM v[240:243],a[176:179],v[80:83],v[240:243]\nM v[244:247],a[180:183],v[80:83],v[244:247]\ns_waitcnt lgkmcnt(2)\nds_read_b128 v[80:83],v4 offset:33792\nM v[248:251],a[176:179],v[88:91],v[248:251]\nbuffer_load_dwordx4 v2,s[36:39],s47 offen offset:1024 sc1 lds\n" \
"M v[252:255],a[180:183],v[88:91],v[252:255]\nds_read_b128 v[88:91],v4 offset:41984\nM v[240:243],a[184:187],v[76:79],v[240:243]\nM v[244:247],a[188:191],v[76:79],v[244:247]\nds_read_b128 v[76:79],v4 offset:34816\nM v[248:251],a[184:187],v[84:87],v[248:251]\nbuffer_load_dwordx4 v2,s[36:39],s47 offen offset:2048 sc1 lds\nM v[252:255],a[188:191],v[84:87],v[252:255]\n" \
"s_waitcnt lgkmcnt(2)\nds_read_b128 v[84:87],v4 offset:43008\nM v[240:243],a[192:195],v[92:95],v[240:243]\nM v[244:247],a[196:199],v[92:95],v[244:247]\nds_read_b128 v[92:95],v4 offset:35840\nM v[248:251],a[192:195],v[72:75],v[248:251]\nbuffer_load_dwordx4 v2,s[36:39],s47 offen offset:3072 sc1 lds\nM v[252:255],a[196:199],v[72:75],v[252:255]\n" \
"s_add_u32 m0,s22,65536\nds_read_b128 v[72:75],v4 offset:44032\nM v[240:243],a[200:203],v[80:83],v[240:243]\ns_add_u32 s47,s35,0\nM v[244:247],a[204:207],v[80:83],v[244:247]\ns_waitcnt lgkmcnt(2)\nds_read_b128 v[80:83],v4 offset:36864\nM v[248:251],a[200:203],v[88:91],v[248:251]\n" \
"buffer_load_dwordx4 v2,s[36:39],s47 offen offset:0 sc1 lds\nM v[252:255],a[204:207],v[88:91],v[252:255]\nds_read_b128 v[88:91],v4 offset:45056\nM v[240:243],a[208:211],v[76:79],v[240:243]\nM v[244:247],a[212:215],v[76:79],v[244:247]\nds_read_b128 v[76:79],v4 offset:37888\nM v[248:251],a[208:211],v[84:87],v[248:251]\nbuffer_load_dwordx4 v2,s[36:39],s47 offen offset:1024 sc1 lds\n" \
"M v[252:255],a[212:215],v[84:87],v[252:255]\ns_waitcnt lgkmcnt(2)\nds_read_b128 v[84:87],v4 offset:46080\nM v[240:243],a[216:219],v[92:95],v[240:243]\nM v[244:247],a[220:223],v[92:95],v[244:247]\nds_read_b128 v[92:95],v4 offset:38912\nM v[248:251],a[216:219],v[72:75],v[248:251]\nM v[252:255],a[220:223],v[72:75],v[252:255]\n" \
"buffer_load_dwordx4 v2,s[36:39],s47 offen offset:2048 sc1 lds\nM v[240:243],a[224:227],v[80:83],v[240:243]\ns_waitcnt lgkmcnt(1)\nds_read_b128 v[72:75],v4 offset:47104\nM v[244:247],a[228:231],v[80:83],v[244:247]\ns_waitcnt vmcnt(12) lgkmcnt(14)\nds_read_b128 v[80:83],v4 offset:39936\nM v[248:251],a[224:227],v[88:91],v[248:251]\n" \
"s_barrier\nM v[252:255],a[228:231],v[88:91],v[252:255]\nbuffer_load_dwordx4 v2,s[36:39],s47 offen offset:3072 sc1 lds\nM v[240:243],a[232:235],v[76:79],v[240:243]\ns_add_u32 m0,s22,98304\nds_read_b128 v[88:91],v4 offset:48128\nM v[244:247],a[236:239],v[76:79],v[244:247]\ns_add_u32 s47,s35,8192\n" \
"ds_read_b128 v[76:79],v4 offset:16384\nM v[248:251],a[232:235],v[84:87],v[248:251]\nM v[252:255],a[236:239],v[84:87],v[252:255]\ns_waitcnt lgkmcnt(2)\nds_read_b128 v[84:87],v4 offset:24576\nM v[240:243],a[240:243],v[92:95],v[240:243]\nbuffer_load_dwordx4 v2,s[36:39],s47 offen offset:0 sc1 lds\nM v[244:247],a[244:247],v[92:95],v[244:247]\n" \
"ds_read_b128 v[92:95],v4 offset:17408\nM v[248:251],a[240:243],v[72:75],v[248:251]\nM v[252:255],a[244:247],v[72:75],v[252:255]\nds_read_b128 v[72:75],v4 offset:25600\nM v[240:243],a[248:251],v[80:83],v[240:243]\nbuffer_load_dwordx4 v2,s[36:39],s47 offen offset:1024 sc1 lds\nM v[244:247],a[252:255],v[80:83],v[244:247]\ns_waitcnt lgkmcnt(2)\n" \
"ds_read_b128 v[80:83],v4 offset:18432\nM v[248:251],a[248:251],v[88:91],v[248:251]\nC v240,v240,v241\nC v241,v242,v243\nM v[252:255],a[252:255],v[88:91],v[252:255]\nds_read_b128 v[88:91],v4 offset:26624\nC v242,v244,v245\nM v[192:195],a[0:3],v[76:79],0\n" \
"C v243,v246,v247\nC v248,v248,v249\nM v[196:199],a[4:7],v[76:79],0\nbuffer_store_dwordx4 v[240:243],v4,s[36:39],s45 offen offset:0 sc1\nM v[200:203],a[0:3],v[84:87],0\nds_read_b128 v[76:79],v4 offset:19456\nC v249,v250,v251\nM v[204:207],a[4:7],v[84:87],0\n" \
"s_waitcnt lgkmcnt(2)\nds_read_b128 v[84:87],v4 offset:27648\nC v250,v252,v253\nM v[192:195],a[8:11],v[92:95],v[192:195]\nC v251,v254,v255\nM v[196:199],a[12:15],v[92:95],v[196:199]\nds_read_b128 v[92:95],v4 offset:20480\nM v[200:203],a[8:11],v[72:75],v[200:203]\n" \
"buffer_store_dwordx4 v[248:251],v4,s[36:39],s45 offen offset:1024 sc1\nM v[204:207],a[12:15],v[72:75],v[204:207]\nds_read_b128 v[72:75],v4 offset:28672\nM v[192:195],a[16:19],v[80:83],v[192:195]\nM v[196:199],a[20:23],v[80:83],v[196:199]\ns_waitcnt lgkmcnt(2)\nds_read_b128 v[80:83],v4 offset:21504\nM v[200:203],a[16:19],v[88:91],v[200:203]\n" \
"M v[204:207],a[20:23],v[88:91],v[204:207]\nds_read_b128 v[88:91],v4 offset:29696\nM v[192:195],a[24:27],v[76:79],v[192:195]\nM v[196:199],a[28:31],v[76:79],v[196:199]\nds_read_b128 v[76:79],v4 offset:22528\nM v[200:203],a[24:27],v[84:87],v[200:203]\nM v[204:207],a[28:31],v[84:87],v[204:207]\ns_waitcnt lgkmcnt(2)\n" \
"ds_read_b128 v[84:87],v4 offset:30720\nM v[192:195],a[32:35],v[92:95],v[192:195]\nM v[196:199],a[36:39],v[92:95],v[196:199]\nds_read_b128 v[92:95],v4 offset:23552\nM v[200:203],a[32:35],v[72:75],v[200:203]\nM v[204:207],a[36:39],v[72:75],v[204:207]\nbuffer_load_dwordx4 v2,s[36:39],s47 offen offset:2048 sc1 lds\nM v[192:195],a[40:43],v[80:83],v[192:195]\n" \
"ds_read_b128 v[72:75],v4 offset:31744\nM v[196:199],a[44:47],v[80:83],v[196:199]\ns_waitcnt lgkmcnt(2)\nds_read_b128 v[80:83],v23 offset:0\nM v[200:203],a[40:43],v[88:91],v[200:203]\nM v[204:207],a[44:47],v[88:91],v[204:207]\nbuffer_load_dwordx2 v[10:11],v190,s[36:39],0 offen sc1\nM v[192:195],a[48:51],v[76:79],v[192:195]\n" \
"ds_read_b128 v[88:91],v23 offset:8192\nM v[196:199],a[52:55],v[76:79],v[196:199]\nds_read_b128 v[76:79],v23 offset:1024\nM v[200:203],a[48:51],v[84:87],v[200:203]\ns_waitcnt lgkmcnt(2)\nM v[204:207],a[52:55],v[84:87],v[204:207]\nbuffer_load_dwordx4 v2,s[36:39],s47 offen offset:3072 sc1 lds\nM v[192:195],a[56:59],v[92:95],v[192:195]\n" \
"s_add_u32 m0,s22,81920\nds_read_b128 v[84:87],v23 offset:9216\nM v[196:199],a[60:63],v[92:95],v[196:199]\ns_add_u32 s47,s35,16384\nds_read_b128 v[92:95],v23 offset:2048\nM v[200:203],a[56:59],v[72:75],v[200:203]\nM v[204:207],a[60:63],v[72:75],v[204:207]\nbuffer_load_dwordx4 v2,s[36:39],s47 offen offset:0 sc1 lds\n" \
"M v[192:195],a[64:67],v[80:83],v[192:195]\nds_read_b128 v[72:75],v23 offset:10240\nM v[196:199],a[68:71],v[80:83],v[196:199]\ns_waitcnt lgkmcnt(2)\nds_read_b128 v[80:83],v23 offset:3072\nM v[200:203],a[64:67],v[88:91],v[200:203]\nM v[204:207],a[68:71],v[88:91],v[204:207]\nbuffer_load_dwordx4 v2,s[36:39],s47 offen offset:1024 sc1 lds\n" \
"M v[192:195],a[72:75],v[76:79],v[192:195]\nds_read_b128 v[88:91],v23 offset:11264\nM v[196:199],a[76:79],v[76:79],v[196:199]\nds_read_b128 v[76:79],v23 offset:4096\nM v[200:203],a[72:75],v[84:87],v[200:203]\ns_waitcnt lgkmcnt(2)\nM v[204:207],a[76:79],v[84:87],v[204:207]\nbuffer_load_dwordx4 v2,s[36:39],s47 offen offset:2048 sc1 lds\n" \
"M v[192:195],a[80:83],v[92:95],v[192:195]\nds_read_b128 v[84:87],v23 offset:12288\nM v[196:199],a[84:87],v[92:95],v[196:199]\nds_read_b128 v[92:95],v23 offset:5120\nM v[200:203],a[80:83],v[72:75],v[200:203]\ns_waitcnt lgkmcnt(2)\nM v[204:207],a[84:87],v[72:75],v[204:207]\nbuffer_load_dwordx4 v2,s[36:39],s47 offen offset:3072 sc1 lds\n" \
"M v[192:195],a[88:91],v[80:83],v[192:195]\ns_add_u32 m0,s22,114688\nds_read_b128 v[72:75],v23 offset:13312\nM v[196:199],a[92:95],v[80:83],v[196:199]\ns_add_u32 s47,s35,24576\nds_read_b128 v[80:83],v23 offset:6144\nM v[200:203],a[88:91],v[88:91],v[200:203]\nM v[204:207],a[92:95],v[88:91],v[204:207]\n" \
"s_waitcnt lgkmcnt(2)\nds_read_b128 v[88:91],v23 offset:14336\nM v[192:195],a[96:99],v[76:79],v[192:195]\nbuffer_load_dwordx4 v2,s[36:39],s47 offen offset:0 sc1 lds\nM v[196:199],a[100:103],v[76:79],v[196:199]\ns_waitcnt vmcnt(16) lgkmcnt(14)\nds_read_b128 v[76:79],v23 offset:7168\nM v[200:203],a[96:99],v[84:87],v[200:203]\n" \
"s_barrier\nM v[204:207],a[100:103],v[84:87],v[204:207]\nds_read_b128 v[84:87],v23 offset:15360\nM v[192:195],a[104:107],v[92:95],v[192:195]\nbuffer_load_dwordx4 v2,s[36:39],s47 offen offset:1024 sc1 lds\nM v[196:199],a[108:111],v[92:95],v[196:199]\ns_waitcnt lgkmcnt(2)\nds_read_b128 v[92:95],v23 offset:16384\n" \
"M v[200:203],a[104:107],v[72:75],v[200:203]\nM v[204:207],a[108:111],v[72:75],v[204:207]\nds_read_b128 v[72:75],v23 offset:24576\nM v[192:195],a[112:115],v[80:83],v[192:195]\nM v[196:199],a[116:119],v[80:83],v[196:199]\nbuffer_load_dwordx4 v2,s[36:39],s47 offen offset:2048 sc1 lds\nM v[200:203],a[112:115],v[88:91],v[200:203]\ns_waitcnt lgkmcnt(1)\n" \
"ds_read_b128 v[80:83],v23 offset:17408\nM v[204:207],a[116:119],v[88:91],v[204:207]\ns_waitcnt vmcnt(8)\nds_read_b128 v[88:91],v23 offset:25600\nv_cmp_gt_u64 s[98:99],s[14:15],v[10:11]\nM v[192:195],a[120:123],v[76:79],v[192:195]\nv_cmp_le_u64 s[18:19],s[16:17],v[10:11]\nM v[196:199],a[124:127],v[76:79],v[196:199]\n" \
"buffer_load_dwordx4 v2,s[36:39],s47 offen offset:3072 sc1 lds\nM v[200:203],a[120:123],v[84:87],v[200:203]\ns_or_b64 s[98:99],s[98:99],s[18:19]\ns_and_b32 s98,s98,0x7\ns_cbranch_scc1 LPP_%=\nLPPR_%=:\nds_read_b128 v[76:79],v23 offset:18432\nM v[204:207],a[124:127],v[84:87],v[204:207]\n" \
"ds_read_b128 v[84:87],v23 offset:26624\nM v[192:195],a[128:131],v[92:95],v[192:195]\nM v[196:199],a[132:135],v[92:95],v[196:199]\ns_waitcnt lgkmcnt(2)\nds_read_b128 v[92:95],v23 offset:19456\nM v[200:203],a[128:131],v[72:75],v[200:203]\nbuffer_load_dwordx4 v[96:99],v4,s[36:39],s63 offen offset:0 sc1\nM v[204:207],a[132:135],v[72:75],v[204:207]\n" \
"ds_read_b128 v[72:75],v23 offset:27648\nM v[192:195],a[136:139],v[80:83],v[192:195]\nM v[196:199],a[140:143],v[80:83],v[196:199]\nds_read_b128 v[80:83],v23 offset:20480\nM v[200:203],a[136:139],v[88:91],v[200:203]\nbuffer_load_dwordx4 v[100:103],v4,s[36:39],s63 offen offset:1024 sc1\nM v[204:207],a[140:143],v[88:91],v[204:207]\ns_waitcnt lgkmcnt(2)\n" \
"ds_read_b128 v[88:91],v23 offset:28672\nM v[192:195],a[144:147],v[76:79],v[192:195]\nM v[196:199],a[148:151],v[76:79],v[196:199]\nds_read_b128 v[76:79],v23 offset:21504\nM v[200:203],a[144:147],v[84:87],v[200:203]\nM v[204:207],a[148:151],v[84:87],v[204:207]\nbuffer_load_dwordx4 v[104:107],v4,s[36:39],s63 offen offset:2048 sc1\nM v[192:195],a[152:155],v[92:95],v[192:195]\n" \
"ds_read_b128 v[84:87],v23 offset:29696\nM v[196:199],a[156:159],v[92:95],v[196:199]\ns_waitcnt lgkmcnt(2)\nds_read_b128 v[92:95],v23 offset:22528\nM v[200:203],a[152:155],v[72:75],v[200:203]\nM v[204:207],a[156:159],v[72:75],v[204:207]\nbuffer_load_dwordx4 v[108:111],v4,s[36:39],s63 offen offset:3072 sc1\nM v[192:195],a[160:163],v[80:83],v[192:195]\n" \
"s_add_u32 s63,s49,4096\nds_read_b128 v[72:75],v23 offset:30720\nM v[196:199],a[164:167],v[80:83],v[196:199]\nds_read_b128 v[80:83],v23 offset:23552\nM v[200:203],a[160:163],v[88:91],v[200:203]\nM v[204:207],a[164:167],v[88:91],v[204:207]\ns_waitcnt lgkmcnt(2)\nds_read_b128 v[88:91],v23 offset:31744\n" \
"M v[192:195],a[168:171],v[76:79],v[192:195]\nbuffer_load_dwordx4 v[112:115],v4,s[36:39],s63 offen offset:0 sc1\nM v[196:199],a[172:175],v[76:79],v[196:199]\nds_read_b128 v[76:79],v4 offset:49152\nM v[200:203],a[168:171],v[84:87],v[200:203]\nM v[204:207],a[172:175],v[84:87],v[204:207]\nds_read_b128 v[84:87],v4 offset:57344\nM v[192:195],a[176:179],v[92:95],v[192:195]\n" \
"buffer_load_dwordx4 v[116:119],v4,s[36:39],s63 offen offset:1024 sc1\nM v[196:199],a[180:183],v[92:95],v[196:199]\ns_waitcnt lgkmcnt(2)\nds_read_b128 v[92:95],v4 offset:50176\nM v[200:203],a[176:179],v[72:75],v[200:203]\nM v[204:207],a[180:183],v[72:75],v[204:207]\nds_read_b128 v[72:75],v4 offset:58368\nM v[192:195],a[184:187],v[80:83],v[192:195]\n" \
"M v[196:199],a[188:191],v[80:83],v[196:199]\nbuffer_load_dwordx4 v[176:179],v4,s[36:39],s63 offen offset:2048 sc1\nM v[200:203],a[184:187],v[88:91],v[200:203]\nds_read_b128 v[80:83],v4 offset:51200\nM v[204:207],a[188:191],v[88:91],v[204:207]\ns_waitcnt lgkmcnt(2)\nds_read_b128 v[88:91],v4 offset:59392\nM v[192:195],a[192:195],v[76:79],v[192:195]\n" \
"M v[196:199],a[196:199],v[76:79],v[196:199]\nbuffer_load_dwordx4 v[180:183],v4,s[36:39],s63 offen offset:3072 sc1\nM v[200:203],a[192:195],v[84:87],v[200:203]\nds_read_b128 v[76:79],v4 offset:52224\nM v[204:207],a[196:199],v[84:87],v[204:207]\nds_read_b128 v[84:87],v4 offset:60416\nM v[192:195],a[200:203],v[92:95],v[192:195]\nM v[196:199],a[204:207],v[92:95],v[196:199]\n" \
"s_waitcnt lgkmcnt(2)\nds_read_b128 v[92:95],v4 offset:53248\nM v[200:203],a[200:203],v[72:75],v[200:203]\nM v[204:207],a[204:207],v[72:75],v[204:207]\nds_read_b128 v[72:75],v4 offset:61440\nM v[192:195],a[208:211],v[80:83],v[192:195]\nM v[196:199],a[212:215],v[80:83],v[196:199]\nds_read_b128 v[80:83],v4 offset:54272\n" \
"M v[200:203],a[208:211],v[88:91],v[200:203]\nM v[204:207],a[212:215],v[88:91],v[204:207]\ns_waitcnt lgkmcnt(2)\nds_read_b128 v[88:91],v4 offset:62464\nM v[192:195],a[216:219],v[76:79],v[192:195]\nM v[196:199],a[220:223],v[76:79],v[196:199]\nds_read_b128 v[76:79],v4 offset:55296\nM v[200:203],a[216:219],v[84:87],v[200:203]\n" \
"M v[204:207],a[220:223],v[84:87],v[204:207]\nds_read_b128 v[84:87],v4 offset:63488\nM v[192:195],a[224:227],v[92:95],v[192:195]\ns_waitcnt lgkmcnt(2)\nM v[196:199],a[228:231],v[92:95],v[196:199]\ns_waitcnt vmcnt(16) lgkmcnt(14)\nds_read_b128 v[92:95],v4 offset:56320\nM v[200:203],a[224:227],v[72:75],v[200:203]\n" \
"s_barrier\nM v[204:207],a[228:231],v[72:75],v[204:207]\nds_read_b128 v[72:75],v4 offset:64512\nM v[192:195],a[232:235],v[80:83],v[192:195]\nM v[196:199],a[236:239],v[80:83],v[196:199]\nds_read_b128 v[80:83],v20 offset:0\nM v[200:203],a[232:235],v[88:91],v[200:203]\nM v[204:207],a[236:239],v[88:91],v[204:207]\n" \
"s_waitcnt lgkmcnt(2)\nds_read_b128 v[88:91],v20 offset:8192\nM v[192:195],a[240:243],v[76:79],v[192:195]\nM v[196:199],a[244:247],v[76:79],v[196:199]\nds_read_b128 v[76:79],v20 offset:1024\nM v[200:203],a[240:243],v[84:87],v[200:203]\nM v[204:207],a[244:247],v[84:87],v[204:207]\nds_read_b128 v[84:87],v20 offset:9216\n" \
"M v[192:195],a[248:251],v[92:95],v[192:195]\nM v[196:199],a[252:255],v[92:95],v[196:199]\ns_waitcnt lgkmcnt(2)\nds_read_b128 v[92:95],v20 offset:2048\nM v[200:203],a[248:251],v[72:75],v[200:203]\nC v192,v192,v193\nC v193,v194,v195\nM v[204:207],a[252:255],v[72:75],v[204:207]\n" \
"ds_read_b128 v[72:75],v20 offset:10240\nC v194,v196,v197\nM v[208:211],a[0:3],v[80:83],0\nC v195,v198,v199\nC v200,v200,v201\nM v[212:215],a[4:7],v[80:83],0\nbuffer_store_dwordx4 v[192:195],v4,s[36:39],s48 offen offset:0 sc1\nM v[216:219],a[0:3],v[88:91],0\n" \
"ds_read_b128 v[80:83],v20 offset:3072\nC v201,v202,v203\nM v[220:223],a[4:7],v[88:91],0\ns_waitcnt lgkmcnt(2)\nds_read_b128 v[88:91],v20 offset:11264\nC v202,v204,v205\nM v[208:211],a[8:11],v[76:79],v[208:211]\nC v203,v206,v207\n" \
"M v[212:215],a[12:15],v[76:79],v[212:215]\ns_waitcnt vmcnt(1)\nds_read_b128 v[76:79],v20 offset:4096\nM v[216:219],a[8:11],v[84:87],v[216:219]\nbuffer_store_dwordx4 v[200:203],v4,s[36:39],s48 offen offset:1024 sc1\nM v[220:223],a[12:15],v[84:87],v[220:223]\nds_read_b128 v[84:87],v20 offset:12288\nM v[208:211],a[16:19],v[92:95],v[208:211]\n" \
"M v[212:215],a[20:23],v[92:95],v[212:215]\ns_waitcnt lgkmcnt(2)\nds_read_b128 v[92:95],v20 offset:5120\nM v[216:219],a[16:19],v[72:75],v[216:219]\nM v[220:223],a[20:23],v[72:75],v[220:223]\nds_read_b128 v[72:75],v20 offset:13312\nM v[208:211],a[24:27],v[80:83],v[208:211]\nM v[212:215],a[28:31],v[80:83],v[212:215]\n" \
"ds_read_b128 v[80:83],v20 offset:6144\nM v[216:219],a[24:27],v[88:91],v[216:219]\nM v[220:223],a[28:31],v[88:91],v[220:223]\ns_waitcnt lgkmcnt(2)\nds_read_b128 v[88:91],v20 offset:14336\nM v[208:211],a[32:35],v[76:79],v[208:211]\nM v[212:215],a[36:39],v[76:79],v[212:215]\nds_read_b128 v[76:79],v20 offset:7168\n" \
"M v[216:219],a[32:35],v[84:87],v[216:219]\nM v[220:223],a[36:39],v[84:87],v[220:223]\nLG25_%=:\ns_cbranch_execz LHR2_%=\nds_read_b128 v[84:87],v20 offset:15360\nM v[208:211],a[40:43],v[92:95],v[208:211]\nM v[212:215],a[44:47],v[92:95],v[212:215]\ns_waitcnt lgkmcnt(2)\n" \
"ds_read_b128 v[92:95],v20 offset:32768\nM v[216:219],a[40:43],v[72:75],v[216:219]\nM v[220:223],a[44:47],v[72:75],v[220:223]\nds_read_b128 v[72:75],v20 offset:40960\nM v[208:211],a[48:51],v[80:83],v[208:211]\nM v[212:215],a[52:55],v[80:83],v[212:215]\nds_read_b128 v[80:83],v20 offset:33792\nM v[216:219],a[48:51],v[88:91],v[216:219]\n" \
"M v[220:223],a[52:55],v[88:91],v[220:223]\ns_waitcnt lgkmcnt(2)\nds_read_b128 v[88:91],v20 offset:41984\nM v[208:211],a[56:59],v[76:79],v[208:211]\nM v[212:215],a[60:63],v[76:79],v[212:215]\nds_read_b128 v[76:79],v20 offset:34816\nM v[216:219],a[56:59],v[84:87],v[216:219]\nM v[220:223],a[60:63],v[84:87],v[220:223]\n" \
"ds_read_b128 v[84:87],v20 offset:43008\nM v[48:51],v[186:189],v[120:123],0\nM v[52:55],v[184:187],v[120:123],0\nM v[64:67],v[186:189],v[124:127],0\nM v[68:71],v[184:187],v[124:127],0\nM v[48:51],v[186:189],v[96:99],v[48:51]\nM v[52:55],v[184:187],v[96:99],v[52:55]\nM v[64:67],v[186:189],v[100:103],v[64:67]\n" \
"M v[68:71],v[184:187],v[100:103],v[68:71]\nM v[48:51],v[186:189],v[104:107],v[48:51]\nM v[52:55],v[184:187],v[104:107],v[52:55]\nM v[64:67],v[186:189],v[108:111],v[64:67]\nM v[68:71],v[184:187],v[108:111],v[68:71]\nM v[48:51],v[186:189],v[112:115],v[48:51]\nM v[52:55],v[184:187],v[112:115],v[52:55]\nM v[64:67],v[186:189],v[116:119],v[64:67]\n" \
"C v160,v48,v49\nC v161,v50,v51\nM v[68:71],v[184:187],v[116:119],v[68:71]\nC v162,v52,v53\nC v163,v54,v55\nM v[208:211],v[186:189],v[176:179],v[208:211]\nv_lshlrev_b32 v48,16,v160\nv_and_b32 v49,0xffff0000,v160\n" \
"M v[212:215],v[184:187],v[176:179],v[212:215]\nv_lshlrev_b32 v52,16,v162\nv_and_b32 v53,0xffff0000,v162\nM v[216:219],v[186:189],v[180:183],v[216:219]\nv_lshlrev_b32 v50,16,v161\nv_and_b32 v51,0xffff0000,v161\nM v[220:223],v[184:187],v[180:183],v[220:223]\nv_lshlrev_b32 v54,16,v163\n" \
"v_and_b32 v55,0xffff0000,v163\nM v[208:211],a[64:67],v[92:95],v[208:211]\nv_mul_f32 v48,v24,v48\nv_mul_f32 v49,v24,v49\nM v[212:215],a[68:71],v[92:95],v[212:215]\ns_waitcnt lgkmcnt(0)\nds_read_b128 v[92:95],v20 offset:35840\nv_mul_f32 v52,v24,v52\n" \
"M v[216:219],a[64:67],v[72:75],v[216:219]\nv_mul_f32 v53,v24,v53\nv_mul_f32 v50,v24,v50\nM v[220:223],a[68:71],v[72:75],v[220:223]\nds_read_b128 v[72:75],v20 offset:44032\nv_mul_f32 v51,v24,v51\nM v[208:211],a[72:75],v[80:83],v[208:211]\nv_mul_f32 v54,v24,v54\n" \
"v_mul_f32 v55,v24,v55\nM v[212:215],a[76:79],v[80:83],v[212:215]\nds_read_b128 v[80:83],v20 offset:36864\nC v160,v48,v49\nM v[216:219],a[72:75],v[88:91],v[216:219]\nC v162,v52,v53\nC v161,v50,v51\nM v[220:223],a[76:79],v[88:91],v[220:223]\n" \
"ds_read_b128 v[88:91],v20 offset:45056\nC v163,v54,v55\nM v[208:211],a[80:83],v[76:79],v[208:211]\nv_lshlrev_b32 v48,16,v160\nv_lshlrev_b32 v52,16,v162\nM v[212:215],a[84:87],v[76:79],v[212:215]\nds_read_b128 v[76:79],v20 offset:37888\nv_and_b32 v49,0xffff0000,v160\n" \
"M v[216:219],a[80:83],v[84:87],v[216:219]\ns_waitcnt lgkmcnt(2)\nv_lshlrev_b32 v50,16,v161\nv_and_b32 v53,0xffff0000,v162\nM v[220:223],a[84:87],v[84:87],v[220:223]\ns_add_u32 s63,s49,8192\nds_read_b128 v[84:87],v20 offset:46080\nv_and_b32 v51,0xffff0000,v161\n" \
"M v[208:211],a[88:91],v[92:95],v[208:211]\nbuffer_load_dwordx4 v[32:35],v4,s[36:39],s63 offen offset:0 sc1\nM v[212:215],a[92:95],v[92:95],v[212:215]\nds_read_b128 v[92:95],v20 offset:38912\nv_lshlrev_b32 v54,16,v163\nM v[216:219],a[88:91],v[72:75],v[216:219]\nv_and_b32 v55,0xffff0000,v163\nC v164,v64,v65\n" \
"M v[220:223],a[92:95],v[72:75],v[220:223]\ns_waitcnt lgkmcnt(2)\nds_read_b128 v[72:75],v20 offset:47104\nC v165,v66,v67\nM v[208:211],a[96:99],v[80:83],v[208:211]\nbuffer_load_dwordx4 v[36:39],v4,s[36:39],s63 offen offset:1024 sc1\nM v[212:215],a[100:103],v[80:83],v[212:215]\ns_waitcnt vmcnt(12) lgkmcnt(14)\n" \
"ds_read_b128 v[80:83],v20 offset:39936\nC v166,v68,v69\nM v[216:219],a[96:99],v[88:91],v[216:219]\ns_barrier\nv_add_f32 v48,v48,v128\nM v[220:223],a[100:103],v[88:91],v[220:223]\nds_read_b128 v[88:91],v20 offset:48128\nC v167,v70,v71\n" \
"M v[208:211],a[104:107],v[76:79],v[208:211]\nv_add_f32 v52,v52,v132\nv_add_f32 v49,v49,v129\nM v[212:215],a[108:111],v[76:79],v[212:215]\ns_waitcnt lgkmcnt(2)\nds_read_b128 v[76:79],v20 offset:16384\nv_add_f32 v50,v50,v130\nM v[216:219],a[104:107],v[84:87],v[216:219]\n" \
"v_add_f32 v53,v53,v133\nv_add_f32 v51,v51,v131\nM v[220:223],a[108:111],v[84:87],v[220:223]\nds_read_b128 v[84:87],v20 offset:24576\nv_add_f32 v54,v54,v134\nM v[208:211],a[112:115],v[92:95],v[208:211]\nv_add_f32 v55,v55,v135\nv_lshlrev_b32 v64,16,v164\n" \
"M v[212:215],a[116:119],v[92:95],v[212:215]\nds_read_b128 v[92:95],v20 offset:17408\nv_lshlrev_b32 v66,16,v165\nM v[216:219],a[112:115],v[72:75],v[216:219]\nv_and_b32 v65,0xffff0000,v164\nv_lshlrev_b32 v68,16,v166\nM v[220:223],a[116:119],v[72:75],v[220:223]\ns_waitcnt lgkmcnt(2)\n" \
"ds_read_b128 v[72:75],v20 offset:25600\nv_and_b32 v67,0xffff0000,v165\nM v[208:211],a[120:123],v[80:83],v[208:211]\nv_and_b32 v69,0xffff0000,v166\nv_lshlrev_b32 v70,16,v167\nM v[212:215],a[124:127],v[80:83],v[212:215]\nds_read_b128 v[80:83],v20 offset:18432\nC v160,v48,v49\n" \
"M v[216:219],a[120:123],v[88:91],v[216:219]\nC v162,v52,v53\nC v161,v50,v51\nM v[220:223],a[124:127],v[88:91],v[220:223]\nds_read_b128 v[88:91],v20 offset:26624\nv_and_b32 v71,0xffff0000,v167\nM v[208:211],a[128:131],v[76:79],v[208:211]\nC v163,v54,v55\n" \
"v_mul_f32 v64,v25,v64\nM v[212:215],a[132:135],v[76:79],v[212:215]\ns_waitcnt lgkmcnt(2)\nds_read_b128 v[76:79],v20 offset:19456\nv_mul_f32 v66,v25,v66\nM v[216:219],a[128:131],v[84:87],v[216:219]\nv_mul_f32 v65,v25,v65\nv_mul_f32 v67,v25,v67\n" \
"M v[220:223],a[132:135],v[84:87],v[220:223]\nds_read_b128 v[84:87],v20 offset:27648\nv_mul_f32 v68,v25,v68\nM v[208:211],a[136:139],v[92:95],v[208:211]\nv_mul_f32 v69,v25,v69\nv_mul_f32 v70,v25,v70\nM v[212:215],a[140:143],v[92:95],v[212:215]\nds_read_b128 v[92:95],v20 offset:20480\n" \
"v_mul_f32 v71,v25,v71\nM v[216:219],a[136:139],v[72:75],v[216:219]\nv_permlane16_swap_b32 v160,v162\nM v[220:223],a[140:143],v[72:75],v[220:223]\ns_waitcnt lgkmcnt(2)\nds_read_b128 v[72:75],v20 offset:28672\nC v164,v64,v65\nM v[208:211],a[144:147],v[80:83],v[208:211]\n" \
"v_permlane16_swap_b32 v161,v163\nM v[212:215],a[148:151],v[80:83],v[212:215]\nbuffer_store_dwordx4 v[160:163],v13,s[88:91],s58 offen offset:0\nM v[216:219],a[144:147],v[88:91],v[216:219]\ns_add_u32 s58,s81,0\nds_read_b128 v[80:83],v20 offset:21504\nC v165,v66,v67\nM v[220:223],a[148:151],v[88:91],v[220:223]\n" \
"ds_read_b128 v[88:91],v20 offset:29696\nC v166,v68,v69\nM v[208:211],a[152:155],v[76:79],v[208:211]\nC v167,v70,v71\nv_lshlrev_b32 v64,16,v164\nM v[212:215],a[156:159],v[76:79],v[212:215]\ns_waitcnt lgkmcnt(2)\nds_read_b128 v[76:79],v20 offset:22528\n" \
"v_lshlrev_b32 v66,16,v165\nM v[216:219],a[152:155],v[84:87],v[216:219]\nv_and_b32 v65,0xffff0000,v164\nv_lshlrev_b32 v68,16,v166\nM v[220:223],a[156:159],v[84:87],v[220:223]\nds_read_b128 v[84:87],v20 offset:30720\nv_and_b32 v67,0xffff0000,v165\nM v[208:211],a[160:163],v[92:95],v[208:211]\n" \
"v_and_b32 v69,0xffff0000,v166\nv_lshlrev_b32 v70,16,v167\nM v[212:215],a[164:167],v[92:95],v[212:215]\ns_add_u32 s63,s49,8192\nds_read_b128 v[92:95],v20 offset:23552\nv_and_b32 v71,0xffff0000,v167\nM v[216:219],a[160:163],v[72:75],v[216:219]\nbuffer_load_dwordx4 v[40:43],v4,s[36:39],s63 offen offset:2048 sc1\n" \
"M v[220:223],a[164:167],v[72:75],v[220:223]\ns_waitcnt lgkmcnt(2)\nds_read_b128 v[72:75],v20 offset:31744\nv_add_f32 v64,v64,v136\nM v[208:211],a[168:171],v[80:83],v[208:211]\nv_add_f32 v66,v66,v138\nv_add_f32 v65,v65,v137\nM v[212:215],a[172:175],v[80:83],v[212:215]\n" \
"ds_read_b128 v[80:83],v20 offset:49152\nv_add_f32 v68,v68,v140\nM v[216:219],a[168:171],v[88:91],v[216:219]\nbuffer_load_dwordx4 v[44:47],v4,s[36:39],s63 offen offset:3072 sc1\nM v[220:223],a[172:175],v[88:91],v[220:223]\nds_read_b128 v[88:91],v20 offset:57344\nv_add_f32 v67,v67,v139\nM v[208:211],a[176:179],v[76:79],v[208:211]\n" \
"s_waitcnt vmcnt(3)\nv_max_u32 v10,v32,v33\nv_max3_u32 v10,v10,v34,v35\nM v[212:215],a[180:183],v[76:79],v[212:215]\ns_waitcnt lgkmcnt(2)\nds_read_b128 v[76:79],v20 offset:50176\nv_max3_u32 v10,v10,v36,v37\nM v[216:219],a[176:179],v[84:87],v[216:219]\n" \
"v_max3_u32 v10,v10,v38,v39\nv_cmp_eq_u32 vcc,-1,v10\nM v[220:223],a[180:183],v[84:87],v[220:223]\nds_read_b128 v[84:87],v20 offset:58368\nv_add_f32 v69,v69,v141\nM v[208:211],a[184:187],v[92:95],v[208:211]\ns_cbranch_vccnz LIN_%=\nLINR_%=:\n" \
"v_add_f32 v70,v70,v142\nv_add_f32 v71,v71,v143\nM v[212:215],a[188:191],v[92:95],v[212:215]\nds_read_b128 v[92:95],v20 offset:51200\nC v164,v64,v65\nM v[216:219],a[184:187],v[72:75],v[216:219]\nC v165,v66,v67\nC v166,v68,v69\n" \
"M v[220:223],a[188:191],v[72:75],v[220:223]\ns_waitcnt lgkmcnt(2)\nds_read_b128 v[72:75],v20 offset:59392\nC v167,v70,v71\nM v[208:211],a[192:195],v[80:83],v[208:211]\nv_permlane16_swap_b32 v164,v166\nM v[212:215],a[196:199],v[80:83],v[212:215]\nds_read_b128 v[80:83],v20 offset:52224\n" \
"M v[216:219],a[192:195],v[88:91],v[216:219]\nv_permlane16_swap_b32 v165,v167\nM v[220:223],a[196:199],v[88:91],v[220:223]\nbuffer_store_dwordx4 v[164:167],v13,s[88:91],s59 offen offset:0\nM v[208:211],a[200:203],v[76:79],v[208:211]\ns_add_u32 s59,s81,65536\nds_read_b128 v[88:91],v20 offset:60416\nM v[212:215],a[204:207],v[76:79],v[212:215]\n" \
"s_waitcnt lgkmcnt(2)\nds_read_b128 v[76:79],v20 offset:53248\nM v[216:219],a[200:203],v[84:87],v[216:219]\nM v[220:223],a[204:207],v[84:87],v[220:223]\nds_read_b128 v[84:87],v20 offset:61440\nM v[208:211],a[208:211],v[92:95],v[208:211]\nM v[212:215],a[212:215],v[92:95],v[212:215]\nds_read_b128 v[92:95],v20 offset:54272\n" \
"M v[216:219],a[208:211],v[72:75],v[216:219]\nM v[220:223],a[212:215],v[72:75],v[220:223]\ns_waitcnt lgkmcnt(2)\nds_read_b128 v[72:75],v20 offset:62464\nM v[208:211],a[216:219],v[80:83],v[208:211]\nM v[212:215],a[220:223],v[80:83],v[212:215]\nds_read_b128 v[80:83],v20 offset:55296\nM v[216:219],a[216:219],v[88:91],v[216:219]\n" \
"M v[220:223],a[220:223],v[88:91],v[220:223]\nds_read_b128 v[88:91],v20 offset:63488\nM v[208:211],a[224:227],v[76:79],v[208:211]\nM v[212:215],a[228:231],v[76:79],v[212:215]\ns_waitcnt lgkmcnt(2)\nds_read_b128 v[76:79],v20 offset:56320\nM v[216:219],a[224:227],v[84:87],v[216:219]\nM v[220:223],a[228:231],v[84:87],v[220:223]\n" \
"ds_read_b128 v[84:87],v20 offset:64512\nM v[208:211],a[232:235],v[92:95],v[208:211]\nM v[212:215],a[236:239],v[92:95],v[212:215]\nM v[216:219],a[232:235],v[72:75],v[216:219]\nM v[220:223],a[236:239],v[72:75],v[220:223]\ns_waitcnt lgkmcnt(1)\nM v[208:211],a[240:243],v[80:83],v[208:211]\nM v[212:215],a[244:247],v[80:83],v[212:215]\n" \
"M v[216:219],a[240:243],v[88:91],v[216:219]\nM v[220:223],a[244:247],v[88:91],v[220:223]\nM v[208:211],a[248:251],v[76:79],v[208:211]\nM v[212:215],a[252:255],v[76:79],v[212:215]\ns_waitcnt lgkmcnt(0)\nM v[216:219],a[248:251],v[84:87],v[216:219]\ns_waitcnt vmcnt(1)\nv_max_u32 v10,v40,v41\n" \
"v_max3_u32 v10,v10,v42,v43\nM v[220:223],a[252:255],v[84:87],v[220:223]\nv_max3_u32 v10,v10,v44,v45\nv_max3_u32 v10,v10,v46,v47\nM v[208:211],v[186:189],v[32:35],v[208:211]\nv_cmp_eq_u32 vcc,-1,v10\nM v[212:215],v[184:187],v[32:35],v[212:215]\nM v[216:219],v[186:189],v[36:39],v[216:219]\n" \
"s_cbranch_vccnz LIO_%=\nLIOR_%=:\nM v[220:223],v[184:187],v[36:39],v[220:223]\nM v[208:211],v[186:189],v[40:43],v[208:211]\nM v[212:215],v[184:187],v[40:43],v[212:215]\nM v[216:219],v[186:189],v[44:47],v[216:219]\nC v160,v208,v209\nC v161,v210,v211\n" \
"M v[220:223],v[184:187],v[44:47],v[220:223]\nC v162,v212,v213\nC v163,v214,v215\nv_lshlrev_b32 v208,16,v160\nv_lshlrev_b32 v210,16,v161\nv_lshlrev_b32 v212,16,v162\nv_lshlrev_b32 v214,16,v163\nv_and_b32 v209,0xffff0000,v160\n" \
"v_and_b32 v211,0xffff0000,v161\nv_and_b32 v213,0xffff0000,v162\nv_and_b32 v215,0xffff0000,v163\nv_pk_mul_f32 v[208:209],v[208:209],v[26:27] op_sel_hi:[1,0]\nv_pk_mul_f32 v[210:211],v[210:211],v[26:27] op_sel_hi:[1,0]\nv_pk_mul_f32 v[212:213],v[212:213],v[26:27] op_sel_hi:[1,0]\nv_pk_mul_f32 v[214:215],v[214:215],v[26:27] op_sel_hi:[1,0]\nC v160,v208,v209\n" \
"C v161,v210,v211\nC v162,v212,v213\nC v163,v214,v215\nv_lshlrev_b32 v208,16,v160\nv_lshlrev_b32 v210,16,v161\nv_lshlrev_b32 v212,16,v162\nv_lshlrev_b32 v214,16,v163\nv_and_b32 v209,0xffff0000,v160\n" \
"v_and_b32 v211,0xffff0000,v161\nv_and_b32 v213,0xffff0000,v162\nv_and_b32 v215,0xffff0000,v163\nv_pk_add_f32 v[208:209],v[208:209],v[144:145]\nv_pk_add_f32 v[210:211],v[210:211],v[146:147]\nv_pk_add_f32 v[212:213],v[212:213],v[148:149]\nv_pk_add_f32 v[214:215],v[214:215],v[150:151]\nC v160,v208,v209\n" \
"C v161,v210,v211\nC v162,v212,v213\nC v163,v214,v215\ns_nop 0\nv_permlane16_swap_b32 v160,v162\nv_permlane16_swap_b32 v161,v163\nbuffer_store_dwordx4 v[160:163],v13,s[88:91],s58 offen offset:0\nC v164,v216,v217\n" \
"C v165,v218,v219\nC v166,v220,v221\nC v167,v222,v223\nv_lshlrev_b32 v216,16,v164\nv_lshlrev_b32 v218,16,v165\nv_lshlrev_b32 v220,16,v166\nv_lshlrev_b32 v222,16,v167\nv_and_b32 v217,0xffff0000,v164\n" \
"v_and_b32 v219,0xffff0000,v165\nv_and_b32 v221,0xffff0000,v166\nv_and_b32 v223,0xffff0000,v167\nv_pk_mul_f32 v[216:217],v[216:217],v[26:27] op_sel:[0,1] op_sel_hi:[1,1]\nv_pk_mul_f32 v[218:219],v[218:219],v[26:27] op_sel:[0,1] op_sel_hi:[1,1]\nv_pk_mul_f32 v[220:221],v[220:221],v[26:27] op_sel:[0,1] op_sel_hi:[1,1]\nv_pk_mul_f32 v[222:223],v[222:223],v[26:27] op_sel:[0,1] op_sel_hi:[1,1]\nC v164,v216,v217\n" \
"C v165,v218,v219\nC v166,v220,v221\nC v167,v222,v223\nv_lshlrev_b32 v216,16,v164\nv_lshlrev_b32 v218,16,v165\nv_lshlrev_b32 v220,16,v166\nv_lshlrev_b32 v222,16,v167\nv_and_b32 v217,0xffff0000,v164\n" \
"v_and_b32 v219,0xffff0000,v165\nv_and_b32 v221,0xffff0000,v166\nv_and_b32 v223,0xffff0000,v167\nv_pk_add_f32 v[216:217],v[216:217],v[152:153]\nv_pk_add_f32 v[218:219],v[218:219],v[154:155]\nv_pk_add_f32 v[220:221],v[220:221],v[156:157]\nv_pk_add_f32 v[222:223],v[222:223],v[158:159]\nC v164,v216,v217\n" \
"C v165,v218,v219\nC v166,v220,v221\nC v167,v222,v223\ns_nop 0\nv_permlane16_swap_b32 v164,v166\nv_permlane16_swap_b32 v165,v167\nbuffer_store_dwordx4 v[164:167],v13,s[88:91],s59 offen offset:0\ns_branch LEND_%=\n" \
".p2align 6\nLG26_%=:\ns_cbranch_execz LG14_%=\nLH1_%=:\nds_read_b128 a[128:131],v21 offset:8192\nds_read_b128 a[132:135],v21 offset:9216\nds_read_b128 a[136:139],v21 offset:10240\nds_read_b128 a[140:143],v21 offset:11264\n" \
"ds_read_b128 a[144:147],v21 offset:12288\nds_read_b128 a[148:151],v21 offset:13312\nds_read_b128 a[152:155],v21 offset:14336\nds_read_b128 a[156:159],v21 offset:15360\nds_read_b128 a[160:163],v21 offset:0\nds_read_b128 a[164:167],v21 offset:1024\nds_read_b128 a[168:171],v21 offset:2048\nds_read_b128 a[172:175],v21 offset:3072\n" \
"ds_read_b128 a[176:179],v21 offset:4096\nds_read_b128 a[180:183],v21 offset:5120\nds_read_b128 a[184:187],v21 offset:6144\nds_read_b128 a[188:191],v21 offset:7168\nv_max3_f32 v60,v96,v97,v98\nv_max3_f32 v61,v112,v113,v114\nv_max3_f32 v60,v60,v99,v100\nv_max3_f32 v61,v61,v115,v116\n" \
"v_max3_f32 v60,v60,v101,v102\nv_max3_f32 v61,v61,v117,v118\nv_max3_f32 v60,v60,v103,v104\nv_max3_f32 v61,v61,v119,v120\nv_max3_f32 v60,v60,v105,v106\nv_max3_f32 v61,v61,v121,v122\nv_max3_f32 v60,v60,v107,v108\nv_max3_f32 v61,v61,v123,v124\n" \
"v_max3_f32 v60,v60,v109,v110\nv_max3_f32 v61,v61,v125,v126\nv_max3_f32 v60,v60,v111,v127\nv_xor_b32 v13,0x2000,v13\nv_max_f32 v60,v60,v61\ns_mov_b32 s75,1\nv_cmp_nge_f32 vcc,0x41f00000,v60\nv_pk_mov_b32 v[32:33],v[28:29],v[28:29] op_sel:[0,0]\n" \
"v_pk_mov_b32 v[34:35],v[28:29],v[28:29] op_sel:[0,0]\nv_pk_mov_b32 v[36:37],v[28:29],v[28:29] op_sel:[1,1]\nv_pk_mov_b32 v[38:39],v[28:29],v[28:29] op_sel:[1,1]\nv_pk_mov_b32 v[40:41],v[30:31],v[30:31] op_sel:[0,0]\nv_pk_mov_b32 v[42:43],v[30:31],v[30:31] op_sel:[0,0]\nv_pk_mov_b32 v[44:45],v[30:31],v[30:31] op_sel:[1,1]\nv_pk_mov_b32 v[46:47],v[30:31],v[30:31] op_sel:[1,1]\ns_cbranch_vccz LH1X_%=\n" \
"v_max3_f32 v60,v96,v97,v98\nv_max3_f32 v61,v99,v100,v101\nv_max3_f32 v60,v60,v102,v103\nv_max3_f32 v60,v60,v61,0\nv_mov_b32 v61,v60\ns_nop 1\nv_permlane32_swap_b32 v60,v61\nv_max_f32 v60,v60,v61\n" \
"v_mov_b32 v61,v60\ns_nop 1\nv_permlane16_swap_b32 v60,v61\nv_max_f32 v60,v60,v61\nv_exp_f32 v62,-v60\nv_sub_f32 v96,v96,v60\nv_sub_f32 v97,v97,v60\nv_sub_f32 v98,v98,v60\n" \
"v_sub_f32 v99,v99,v60\nv_sub_f32 v100,v100,v60\nv_sub_f32 v101,v101,v60\nv_sub_f32 v102,v102,v60\nv_sub_f32 v103,v103,v60\nv_mul_f32 v16,v16,v62\nv_mul_f32 v128,v128,v62\nv_mul_f32 v129,v129,v62\n" \
"v_mul_f32 v130,v130,v62\nv_mul_f32 v131,v131,v62\nv_mul_f32 v132,v132,v62\nv_mul_f32 v133,v133,v62\nv_mul_f32 v134,v134,v62\nv_mul_f32 v135,v135,v62\nv_mul_f32 v136,v136,v62\nv_mul_f32 v137,v137,v62\n" \
"v_mul_f32 v138,v138,v62\nv_mul_f32 v139,v139,v62\nv_mul_f32 v140,v140,v62\nv_mul_f32 v141,v141,v62\nv_mul_f32 v142,v142,v62\nv_mul_f32 v143,v143,v62\nv_mul_f32 v144,v144,v62\nv_mul_f32 v145,v145,v62\n" \
"v_mul_f32 v146,v146,v62\nv_mul_f32 v147,v147,v62\nv_mul_f32 v148,v148,v62\nv_mul_f32 v149,v149,v62\nv_mul_f32 v150,v150,v62\nv_mul_f32 v151,v151,v62\nv_mul_f32 v152,v152,v62\nv_mul_f32 v153,v153,v62\n" \
"v_mul_f32 v154,v154,v62\nv_mul_f32 v155,v155,v62\nv_mul_f32 v156,v156,v62\nv_mul_f32 v157,v157,v62\nv_mul_f32 v158,v158,v62\nv_mul_f32 v159,v159,v62\nv_lshlrev_b32 v61,16,v64\nv_and_b32 v63,0xffff0000,v64\n" \
"v_mul_f32 v61,v61,v62\nv_mul_f32 v63,v63,v62\nC v64,v61,v63\nv_lshlrev_b32 v61,16,v65\nv_and_b32 v63,0xffff0000,v65\nv_mul_f32 v61,v61,v62\nv_mul_f32 v63,v63,v62\nC v65,v61,v63\n" \
"v_lshlrev_b32 v61,16,v66\nv_and_b32 v63,0xffff0000,v66\nv_mul_f32 v61,v61,v62\nv_mul_f32 v63,v63,v62\nC v66,v61,v63\nv_lshlrev_b32 v61,16,v67\nv_and_b32 v63,0xffff0000,v67\nv_mul_f32 v61,v61,v62\n" \
"v_mul_f32 v63,v63,v62\nC v67,v61,v63\nv_max3_f32 v60,v104,v105,v106\nv_max3_f32 v61,v107,v108,v109\nv_max3_f32 v60,v60,v110,v111\nv_max3_f32 v60,v60,v61,0\nv_mov_b32 v61,v60\ns_nop 1\n" \
"v_permlane32_swap_b32 v60,v61\nv_max_f32 v60,v60,v61\nv_mov_b32 v61,v60\ns_nop 1\nv_permlane16_swap_b32 v60,v61\nv_max_f32 v60,v60,v61\nv_exp_f32 v62,-v60\nv_sub_f32 v104,v104,v60\n" \
"v_sub_f32 v105,v105,v60\nv_sub_f32 v106,v106,v60\nv_sub_f32 v107,v107,v60\nv_sub_f32 v108,v108,v60\nv_sub_f32 v109,v109,v60\nv_sub_f32 v110,v110,v60\nv_sub_f32 v111,v111,v60\nv_mul_f32 v17,v17,v62\n" \
"v_mul_f32 v160,v160,v62\nv_mul_f32 v161,v161,v62\nv_mul_f32 v162,v162,v62\nv_mul_f32 v163,v163,v62\nv_mul_f32 v164,v164,v62\nv_mul_f32 v165,v165,v62\nv_mul_f32 v166,v166,v62\nv_mul_f32 v167,v167,v62\n" \
"v_mul_f32 v168,v168,v62\nv_mul_f32 v169,v169,v62\nv_mul_f32 v170,v170,v62\nv_mul_f32 v171,v171,v62\nv_mul_f32 v172,v172,v62\nv_mul_f32 v173,v173,v62\nv_mul_f32 v174,v174,v62\nv_mul_f32 v175,v175,v62\n" \
"v_mul_f32 v176,v176,v62\nv_mul_f32 v177,v177,v62\nv_mul_f32 v178,v178,v62\nv_mul_f32 v179,v179,v62\nv_mul_f32 v180,v180,v62\nv_mul_f32 v181,v181,v62\nv_mul_f32 v182,v182,v62\nv_mul_f32 v183,v183,v62\n" \
"v_mul_f32 v184,v184,v62\nv_mul_f32 v185,v185,v62\nv_mul_f32 v186,v186,v62\nv_mul_f32 v187,v187,v62\nv_mul_f32 v188,v188,v62\nv_mul_f32 v189,v189,v62\nv_mul_f32 v190,v190,v62\nv_mul_f32 v191,v191,v62\n" \
"v_lshlrev_b32 v61,16,v72\nv_and_b32 v63,0xffff0000,v72\nv_mul_f32 v61,v61,v62\nv_mul_f32 v63,v63,v62\nC v72,v61,v63\nv_lshlrev_b32 v61,16,v73\nv_and_b32 v63,0xffff0000,v73\nv_mul_f32 v61,v61,v62\n" \
"v_mul_f32 v63,v63,v62\nC v73,v61,v63\nv_lshlrev_b32 v61,16,v74\nv_and_b32 v63,0xffff0000,v74\nv_mul_f32 v61,v61,v62\nv_mul_f32 v63,v63,v62\nC v74,v61,v63\nv_lshlrev_b32 v61,16,v75\n" \
"v_and_b32 v63,0xffff0000,v75\nv_mul_f32 v61,v61,v62\nv_mul_f32 v63,v63,v62\nC v75,v61,v63\nv_max3_f32 v60,v112,v113,v114\nv_max3_f32 v61,v115,v116,v117\nv_max3_f32 v60,v60,v118,v119\nv_max3_f32 v60,v60,v61,0\n" \
"v_mov_b32 v61,v60\ns_nop 1\nv_permlane32_swap_b32 v60,v61\nv_max_f32 v60,v60,v61\nv_mov_b32 v61,v60\ns_nop 1\nv_permlane16_swap_b32 v60,v61\nv_max_f32 v60,v60,v61\n" \
"v_exp_f32 v62,-v60\nv_sub_f32 v112,v112,v60\nv_sub_f32 v113,v113,v60\nv_sub_f32 v114,v114,v60\nv_sub_f32 v115,v115,v60\nv_sub_f32 v116,v116,v60\nv_sub_f32 v117,v117,v60\nv_sub_f32 v118,v118,v60\n" \
"v_sub_f32 v119,v119,v60\nv_mul_f32 v18,v18,v62\nv_mul_f32 v192,v192,v62\nv_mul_f32 v193,v193,v62\nv_mul_f32 v194,v194,v62\nv_mul_f32 v195,v195,v62\nv_mul_f32 v196,v196,v62\nv_mul_f32 v197,v197,v62\n" \
"v_mul_f32 v198,v198,v62\nv_mul_f32 v199,v199,v62\nv_mul_f32 v200,v200,v62\nv_mul_f32 v201,v201,v62\nv_mul_f32 v202,v202,v62\nv_mul_f32 v203,v203,v62\nv_mul_f32 v204,v204,v62\nv_mul_f32 v205,v205,v62\n" \
"v_mul_f32 v206,v206,v62\nv_mul_f32 v207,v207,v62\nv_mul_f32 v208,v208,v62\nv_mul_f32 v209,v209,v62\nv_mul_f32 v210,v210,v62\nv_mul_f32 v211,v211,v62\nv_mul_f32 v212,v212,v62\nv_mul_f32 v213,v213,v62\n" \
"v_mul_f32 v214,v214,v62\nv_mul_f32 v215,v215,v62\nv_mul_f32 v216,v216,v62\nv_mul_f32 v217,v217,v62\nv_mul_f32 v218,v218,v62\nv_mul_f32 v219,v219,v62\nv_mul_f32 v220,v220,v62\nv_mul_f32 v221,v221,v62\n" \
"v_mul_f32 v222,v222,v62\nv_mul_f32 v223,v223,v62\nv_lshlrev_b32 v61,16,v80\nv_and_b32 v63,0xffff0000,v80\nv_mul_f32 v61,v61,v62\nv_mul_f32 v63,v63,v62\nC v80,v61,v63\nv_lshlrev_b32 v61,16,v81\n" \
"v_and_b32 v63,0xffff0000,v81\nv_mul_f32 v61,v61,v62\nv_mul_f32 v63,v63,v62\nC v81,v61,v63\nv_lshlrev_b32 v61,16,v82\nv_and_b32 v63,0xffff0000,v82\nv_mul_f32 v61,v61,v62\nv_mul_f32 v63,v63,v62\n" \
"C v82,v61,v63\nv_lshlrev_b32 v61,16,v83\nv_and_b32 v63,0xffff0000,v83\nv_mul_f32 v61,v61,v62\nv_mul_f32 v63,v63,v62\nC v83,v61,v63\nv_max3_f32 v60,v120,v121,v122\nv_max3_f32 v61,v123,v124,v125\n" \
"v_max3_f32 v60,v60,v126,v127\nv_max3_f32 v60,v60,v61,0\nv_mov_b32 v61,v60\ns_nop 1\nv_permlane32_swap_b32 v60,v61\nv_max_f32 v60,v60,v61\nv_mov_b32 v61,v60\ns_nop 1\n" \
"v_permlane16_swap_b32 v60,v61\nv_max_f32 v60,v60,v61\nv_exp_f32 v62,-v60\nv_sub_f32 v120,v120,v60\nv_sub_f32 v121,v121,v60\nv_sub_f32 v122,v122,v60\nv_sub_f32 v123,v123,v60\nv_sub_f32 v124,v124,v60\n" \
"v_sub_f32 v125,v125,v60\nv_sub_f32 v126,v126,v60\nv_sub_f32 v127,v127,v60\nv_mul_f32 v19,v19,v62\nv_mul_f32 v224,v224,v62\nv_mul_f32 v225,v225,v62\nv_mul_f32 v226,v226,v62\nv_mul_f32 v227,v227,v62\n" \
"v_mul_f32 v228,v228,v62\nv_mul_f32 v229,v229,v62\nv_mul_f32 v230,v230,v62\nv_mul_f32 v231,v231,v62\nv_mul_f32 v232,v232,v62\nv_mul_f32 v233,v233,v62\nv_mul_f32 v234,v234,v62\nv_mul_f32 v235,v235,v62\n" \
"v_mul_f32 v236,v236,v62\nv_mul_f32 v237,v237,v62\nv_mul_f32 v238,v238,v62\nv_mul_f32 v239,v239,v62\nv_mul_f32 v240,v240,v62\nv_mul_f32 v241,v241,v62\nv_mul_f32 v242,v242,v62\nv_mul_f32 v243,v243,v62\n" \
"v_mul_f32 v244,v244,v62\nv_mul_f32 v245,v245,v62\nv_mul_f32 v246,v246,v62\nv_mul_f32 v247,v247,v62\nv_mul_f32 v248,v248,v62\nv_mul_f32 v249,v249,v62\nv_mul_f32 v250,v250,v62\nv_mul_f32 v251,v251,v62\n" \
"v_mul_f32 v252,v252,v62\nv_mul_f32 v253,v253,v62\nv_mul_f32 v254,v254,v62\nv_mul_f32 v255,v255,v62\nv_lshlrev_b32 v61,16,v88\nv_and_b32 v63,0xffff0000,v88\nv_mul_f32 v61,v61,v62\nv_mul_f32 v63,v63,v62\n" \
"C v88,v61,v63\nv_lshlrev_b32 v61,16,v89\nv_and_b32 v63,0xffff0000,v89\nv_mul_f32 v61,v61,v62\nv_mul_f32 v63,v63,v62\nC v89,v61,v63\nv_lshlrev_b32 v61,16,v90\nv_and_b32 v63,0xffff0000,v90\n" \
"v_mul_f32 v61,v61,v62\nv_mul_f32 v63,v63,v62\nC v90,v61,v63\nv_lshlrev_b32 v61,16,v91\nv_and_b32 v63,0xffff0000,v91\nv_mul_f32 v61,v61,v62\nv_mul_f32 v63,v63,v62\nC v91,v61,v63\n" \
"LH1X_%=:\ns_nop 1\ns_cmp_eq_u32 s52,-1\ns_cbranch_scc1 LH1R0_%=\ns_cmp_eq_u32 s52,0\ns_cbranch_scc1 LH1R1_%=\ns_cmp_eq_u32 s52,8\ns_cbranch_scc1 LH1R9_%=\n" \
"s_cmp_eq_u32 s52,9\ns_cbranch_scc1 LH1R10_%=\ns_cmp_eq_u32 s52,11\ns_cbranch_scc1 LH1R12_%=\ns_cmp_eq_u32 s52,13\ns_cbranch_scc1 LH1R14_%=\ns_branch LH1R2_%=\n.p2align 6\n" \
"LG27_%=:\ns_cbranch_execz LG15_%=\nLEV_%=:\ns_cmp_eq_u32 s52,s4\ns_cbranch_scc0 LEV3_%=\nv_rcp_f32 v60,v24\nv_rcp_f32 v61,v25\nv_rcp_f32 v62,v26\n" \
"v_rcp_f32 v63,v27\nv_xor_b32 v14,0x2000,v14\ns_mov_b32 s75,0\ns_mov_b32 s77,s68\nv_mov_b32 v24,0\nv_mov_b32 v25,0\nv_mov_b32 v26,0\nv_mov_b32 v27,0\n" \
"v_pk_mul_f32 v[128:129],v[128:129],v[60:61] op_sel_hi:[1,0]\nv_pk_mul_f32 v[130:131],v[130:131],v[60:61] op_sel_hi:[1,0]\nv_pk_mul_f32 v[132:133],v[132:133],v[60:61] op_sel_hi:[1,0]\nv_pk_mul_f32 v[134:135],v[134:135],v[60:61] op_sel_hi:[1,0]\nv_pk_mul_f32 v[136:137],v[136:137],v[60:61] op_sel_hi:[1,0]\nv_pk_mul_f32 v[138:139],v[138:139],v[60:61] op_sel_hi:[1,0]\nv_pk_mul_f32 v[140:141],v[140:141],v[60:61] op_sel_hi:[1,0]\nv_pk_mul_f32 v[142:143],v[142:143],v[60:61] op_sel_hi:[1,0]\n" \
"v_pk_mul_f32 v[144:145],v[144:145],v[60:61] op_sel_hi:[1,0]\nv_pk_mul_f32 v[146:147],v[146:147],v[60:61] op_sel_hi:[1,0]\nv_pk_mul_f32 v[148:149],v[148:149],v[60:61] op_sel_hi:[1,0]\nv_pk_mul_f32 v[150:151],v[150:151],v[60:61] op_sel_hi:[1,0]\nv_pk_mul_f32 v[152:153],v[152:153],v[60:61] op_sel_hi:[1,0]\nv_pk_mul_f32 v[154:155],v[154:155],v[60:61] op_sel_hi:[1,0]\nv_pk_mul_f32 v[156:157],v[156:157],v[60:61] op_sel_hi:[1,0]\nv_pk_mul_f32 v[158:159],v[158:159],v[60:61] op_sel_hi:[1,0]\n" \
"v_pk_mul_f32 v[160:161],v[160:161],v[60:61] op_sel:[0,1] op_sel_hi:[1,1]\nv_pk_mul_f32 v[162:163],v[162:163],v[60:61] op_sel:[0,1] op_sel_hi:[1,1]\nv_pk_mul_f32 v[164:165],v[164:165],v[60:61] op_sel:[0,1] op_sel_hi:[1,1]\nv_pk_mul_f32 v[166:167],v[166:167],v[60:61] op_sel:[0,1] op_sel_hi:[1,1]\nv_pk_mul_f32 v[168:169],v[168:169],v[60:61] op_sel:[0,1] op_sel_hi:[1,1]\nv_pk_mul_f32 v[170:171],v[170:171],v[60:61] op_sel:[0,1] op_sel_hi:[1,1]\nv_pk_mul_f32 v[172:173],v[172:173],v[60:61] op_sel:[0,1] op_sel_hi:[1,1]\nv_pk_mul_f32 v[174:175],v[174:175],v[60:61] op_sel:[0,1] op_sel_hi:[1,1]\n" \
"v_pk_mul_f32 v[176:177],v[176:177],v[60:61] op_sel:[0,1] op_sel_hi:[1,1]\nv_pk_mul_f32 v[178:179],v[178:179],v[60:61] op_sel:[0,1] op_sel_hi:[1,1]\nv_pk_mul_f32 v[180:181],v[180:181],v[60:61] op_sel:[0,1] op_sel_hi:[1,1]\nv_pk_mul_f32 v[182:183],v[182:183],v[60:61] op_sel:[0,1] op_sel_hi:[1,1]\nv_pk_mul_f32 v[184:185],v[184:185],v[60:61] op_sel:[0,1] op_sel_hi:[1,1]\nv_pk_mul_f32 v[186:187],v[186:187],v[60:61] op_sel:[0,1] op_sel_hi:[1,1]\nv_pk_mul_f32 v[188:189],v[188:189],v[60:61] op_sel:[0,1] op_sel_hi:[1,1]\nv_pk_mul_f32 v[190:191],v[190:191],v[60:61] op_sel:[0,1] op_sel_hi:[1,1]\n" \
"v_pk_mul_f32 v[192:193],v[192:193],v[62:63] op_sel_hi:[1,0]\nv_pk_mul_f32 v[194:195],v[194:195],v[62:63] op_sel_hi:[1,0]\nv_pk_mul_f32 v[196:197],v[196:197],v[62:63] op_sel_hi:[1,0]\nv_pk_mul_f32 v[198:199],v[198:199],v[62:63] op_sel_hi:[1,0]\nv_pk_mul_f32 v[200:201],v[200:201],v[62:63] op_sel_hi:[1,0]\nv_pk_mul_f32 v[202:203],v[202:203],v[62:63] op_sel_hi:[1,0]\nv_pk_mul_f32 v[204:205],v[204:205],v[62:63] op_sel_hi:[1,0]\nv_pk_mul_f32 v[206:207],v[206:207],v[62:63] op_sel_hi:[1,0]\n" \
"v_pk_mul_f32 v[208:209],v[208:209],v[62:63] op_sel_hi:[1,0]\nv_pk_mul_f32 v[210:211],v[210:211],v[62:63] op_sel_hi:[1,0]\nv_pk_mul_f32 v[212:213],v[212:213],v[62:63] op_sel_hi:[1,0]\nv_pk_mul_f32 v[214:215],v[214:215],v[62:63] op_sel_hi:[1,0]\nv_pk_mul_f32 v[216:217],v[216:217],v[62:63] op_sel_hi:[1,0]\nv_pk_mul_f32 v[218:219],v[218:219],v[62:63] op_sel_hi:[1,0]\nv_pk_mul_f32 v[220:221],v[220:221],v[62:63] op_sel_hi:[1,0]\nv_pk_mul_f32 v[222:223],v[222:223],v[62:63] op_sel_hi:[1,0]\n" \
"v_pk_mul_f32 v[224:225],v[224:225],v[62:63] op_sel:[0,1] op_sel_hi:[1,1]\nv_pk_mul_f32 v[226:227],v[226:227],v[62:63] op_sel:[0,1] op_sel_hi:[1,1]\nv_pk_mul_f32 v[228:229],v[228:229],v[62:63] op_sel:[0,1] op_sel_hi:[1,1]\nv_pk_mul_f32 v[230:231],v[230:231],v[62:63] op_sel:[0,1] op_sel_hi:[1,1]\nv_pk_mul_f32 v[232:233],v[232:233],v[62:63] op_sel:[0,1] op_sel_hi:[1,1]\nv_pk_mul_f32 v[234:235],v[234:235],v[62:63] op_sel:[0,1] op_sel_hi:[1,1]\nv_pk_mul_f32 v[236:237],v[236:237],v[62:63] op_sel:[0,1] op_sel_hi:[1,1]\nv_pk_mul_f32 v[238:239],v[238:239],v[62:63] op_sel:[0,1] op_sel_hi:[1,1]\n" \
"v_pk_mul_f32 v[240:241],v[240:241],v[62:63] op_sel:[0,1] op_sel_hi:[1,1]\nv_pk_mul_f32 v[242:243],v[242:243],v[62:63] op_sel:[0,1] op_sel_hi:[1,1]\nv_pk_mul_f32 v[244:245],v[244:245],v[62:63] op_sel:[0,1] op_sel_hi:[1,1]\nv_pk_mul_f32 v[246:247],v[246:247],v[62:63] op_sel:[0,1] op_sel_hi:[1,1]\nv_pk_mul_f32 v[248:249],v[248:249],v[62:63] op_sel:[0,1] op_sel_hi:[1,1]\nv_pk_mul_f32 v[250:251],v[250:251],v[62:63] op_sel:[0,1] op_sel_hi:[1,1]\nv_pk_mul_f32 v[252:253],v[252:253],v[62:63] op_sel:[0,1] op_sel_hi:[1,1]\nv_pk_mul_f32 v[254:255],v[254:255],v[62:63] op_sel:[0,1] op_sel_hi:[1,1]\n" \
"C v128,v128,v129\nC v129,v130,v131\nC v130,v132,v133\nC v131,v134,v135\nbuffer_store_dwordx4 v[128:131],v4,s[36:39],s77 offen offset:0 sc1\nC v136,v136,v137\nC v137,v138,v139\nC v138,v140,v141\n" \
"C v139,v142,v143\nbuffer_store_dwordx4 v[136:139],v4,s[36:39],s77 offen offset:1024 sc1\ns_nop 1\nv_mfma_f32_32x32x16_bf16 v[128:143],v[24:27],v[24:27],0\nC v144,v144,v145\nC v145,v146,v147\nC v146,v148,v149\nC v147,v150,v151\n" \
"buffer_store_dwordx4 v[144:147],v4,s[36:39],s77 offen offset:2048 sc1\nC v152,v152,v153\nC v153,v154,v155\nC v154,v156,v157\nC v155,v158,v159\nbuffer_store_dwordx4 v[152:155],v4,s[36:39],s77 offen offset:3072 sc1\ns_add_u32 s77,s77,262144\ns_nop 0\n" \
"v_mfma_f32_32x32x16_bf16 v[144:159],v[24:27],v[24:27],0\nC v160,v160,v161\nC v161,v162,v163\nC v162,v164,v165\nC v163,v166,v167\nbuffer_store_dwordx4 v[160:163],v4,s[36:39],s77 offen offset:0 sc1\nC v168,v168,v169\nC v169,v170,v171\n" \
"C v170,v172,v173\nC v171,v174,v175\nbuffer_store_dwordx4 v[168:171],v4,s[36:39],s77 offen offset:1024 sc1\ns_nop 1\nv_mfma_f32_32x32x16_bf16 v[160:175],v[24:27],v[24:27],0\nC v176,v176,v177\nC v177,v178,v179\nC v178,v180,v181\n" \
"C v179,v182,v183\nbuffer_store_dwordx4 v[176:179],v4,s[36:39],s77 offen offset:2048 sc1\nC v184,v184,v185\nC v185,v186,v187\nC v186,v188,v189\nC v187,v190,v191\nbuffer_store_dwordx4 v[184:187],v4,s[36:39],s77 offen offset:3072 sc1\ns_add_u32 s77,s77,262144\n" \
"s_nop 0\nv_mfma_f32_32x32x16_bf16 v[176:191],v[24:27],v[24:27],0\nC v192,v192,v193\nC v193,v194,v195\nC v194,v196,v197\nC v195,v198,v199\nbuffer_store_dwordx4 v[192:195],v4,s[36:39],s77 offen offset:0 sc1\nC v200,v200,v201\n" \
"C v201,v202,v203\nC v202,v204,v205\nC v203,v206,v207\nbuffer_store_dwordx4 v[200:203],v4,s[36:39],s77 offen offset:1024 sc1\ns_nop 1\nv_mfma_f32_32x32x16_bf16 v[192:207],v[24:27],v[24:27],0\nC v208,v208,v209\nC v209,v210,v211\n" \
"C v210,v212,v213\nC v211,v214,v215\nbuffer_store_dwordx4 v[208:211],v4,s[36:39],s77 offen offset:2048 sc1\nC v216,v216,v217\nC v217,v218,v219\nC v218,v220,v221\nC v219,v222,v223\nbuffer_store_dwordx4 v[216:219],v4,s[36:39],s77 offen offset:3072 sc1\n" \
"s_add_u32 s77,s77,262144\ns_nop 0\nv_mfma_f32_32x32x16_bf16 v[208:223],v[24:27],v[24:27],0\nC v224,v224,v225\nC v225,v226,v227\nC v226,v228,v229\nC v227,v230,v231\nbuffer_store_dwordx4 v[224:227],v4,s[36:39],s77 offen offset:0 sc1\n" \
"C v232,v232,v233\nC v233,v234,v235\nC v234,v236,v237\nC v235,v238,v239\nbuffer_store_dwordx4 v[232:235],v4,s[36:39],s77 offen offset:1024 sc1\ns_nop 1\nv_mfma_f32_32x32x16_bf16 v[224:239],v[24:27],v[24:27],0\nC v240,v240,v241\n" \
"C v241,v242,v243\nC v242,v244,v245\nC v243,v246,v247\nbuffer_store_dwordx4 v[240:243],v4,s[36:39],s77 offen offset:2048 sc1\nC v248,v248,v249\nC v249,v250,v251\nC v250,v252,v253\nC v251,v254,v255\n" \
"buffer_store_dwordx4 v[248:251],v4,s[36:39],s77 offen offset:3072 sc1\ns_add_u32 s77,s77,262144\ns_nop 0\nv_mfma_f32_32x32x16_bf16 v[240:255],v[24:27],v[24:27],0\ns_add_u32 s56,s4,1\ns_branch LEV4_%=\nLEV3_%=:\ns_mov_b32 s56,s4\n" \
"s_cmp_lt_u32 s52,s4\ns_cbranch_scc1 LEV4_%=\ns_mov_b32 s56,50\ns_add_u32 s77,s4,1\ns_cmp_eq_u32 s52,s77\ns_cbranch_scc0 LEV4_%=\nv_lshrrev_b32 v62,5,v4\nv_mov_b32 v60,s10\n" \
"v_lshlrev_b32 v62,27,v62\nv_mov_b32 v61,s11\nv_lshl_add_u32 v62,v4,5,v62\ns_waitcnt vmcnt(17)\nbuffer_store_dwordx2 v[60:61],v62,s[36:39],s69 offen offset:0 sc1\nLEV4_%=:\ns_cmp_lt_u32 s52,8\ns_cbranch_scc0 LEV5_%=\n" \
"s_min_u32 s56,s56,8\nLEV5_%=:\ns_cmp_eq_u32 s52,8\ns_cbranch_scc0 LEV6_%=\ns_mov_b64 exec,0\ns_cmp_eq_u32 %2,0\ns_cbranch_scc1 LG18_%=\ns_cmp_eq_u32 %2,1\n" \
"s_cbranch_scc1 LG19_%=\ns_cmp_eq_u32 %2,2\ns_cbranch_scc1 LG20_%=\ns_cmp_eq_u32 %2,3\ns_cbranch_scc1 LG21_%=\nLHR2_%=:\ns_mov_b64 exec,-1\nLEV6_%=:\n" \
"s_cmp_eq_u32 s52,0\ns_cbranch_scc1 LEVR0_%=\ns_cmp_eq_u32 s52,8\ns_cbranch_scc1 LEVR8_%=\ns_cmp_eq_u32 s52,9\ns_cbranch_scc1 LEVR9_%=\ns_cmp_eq_u32 s52,10\ns_cbranch_scc1 LEVR10_%=\n" \
"s_cmp_eq_u32 s52,11\ns_cbranch_scc1 LEVR11_%=\ns_cmp_eq_u32 s52,12\ns_cbranch_scc1 LEVR12_%=\ns_cmp_eq_u32 s52,13\ns_cbranch_scc1 LEVR13_%=\ns_cmp_eq_u32 s52,14\ns_cbranch_scc1 LEVR14_%=\n" \
"s_cmp_eq_u32 s52,15\ns_cbranch_scc1 LEVR15_%=\ns_branch LEVR1_%=\nLcs1_%=:\ns_add_u32 s9,s5,2\ns_mov_b32 s8,3\ns_mov_b32 s64,0\ns_branch LSLOW1_%=\n" \
"Lcs3_%=:\ns_add_u32 s9,s5,1\ns_mov_b32 s8,2\ns_mov_b32 s64,0\ns_branch LSLOW0_%=\nLcs5_%=:\ns_add_u32 s9,s5,0\ns_mov_b32 s8,1\n" \
"s_mov_b32 s64,1\ns_branch LSLOW1_%=\nLcs7_%=:\ns_lshl_b32 s9,s52,1\ns_add_u32 s9,s9,0\ns_sub_u32 s8,59,s9\ns_cmp_ge_u32 s9,s70\ns_cselect_b32 s9,s8,s9\n" \
"s_mov_b32 s8,0\ns_mov_b32 s64,1\ns_branch LSLOW0_%=\nLcs9_%=:\ns_lshl_b32 s9,s52,1\ns_add_u32 s9,s9,1\ns_sub_u32 s8,59,s9\ns_cmp_ge_u32 s9,s70\n" \
"s_cselect_b32 s9,s8,s9\ns_mov_b32 s8,0\ns_mov_b32 s64,2\ns_branch LSLOW1_%=\nLcs11_%=:\ns_lshl_b32 s9,s52,1\ns_add_u32 s9,s9,0\ns_sub_u32 s8,59,s9\n" \
"s_cmp_ge_u32 s9,s70\ns_cselect_b32 s9,s8,s9\ns_mov_b32 s8,0\ns_mov_b32 s64,2\ns_branch LSLOW0_%=\nLcs13_%=:\ns_lshl_b32 s9,s52,1\ns_add_u32 s9,s9,1\n" \
"s_sub_u32 s8,59,s9\ns_cmp_ge_u32 s9,s70\ns_cselect_b32 s9,s8,s9\ns_mov_b32 s8,0\ns_mov_b32 s64,3\ns_branch LSLOW1_%=\nLcs39_%=:\ns_lshl_b32 s9,s52,1\n" \
"s_add_u32 s9,s9,0\ns_sub_u32 s8,59,s9\ns_cmp_ge_u32 s9,s70\ns_cselect_b32 s9,s8,s9\ns_mov_b32 s8,0\ns_mov_b32 s64,3\ns_branch LSLOW0_%=\nLcs41_%=:\n" \
"s_lshl_b32 s9,s52,1\ns_add_u32 s9,s9,1\ns_sub_u32 s8,59,s9\ns_cmp_ge_u32 s9,s70\ns_cselect_b32 s9,s8,s9\ns_mov_b32 s8,0\ns_mov_b32 s64,4\ns_branch LSLOW1_%=\n" \
"Lcs43_%=:\ns_lshl_b32 s9,s52,1\ns_add_u32 s9,s9,0\ns_sub_u32 s8,59,s9\ns_cmp_ge_u32 s9,s70\ns_cselect_b32 s9,s8,s9\ns_mov_b32 s8,0\ns_mov_b32 s64,4\n" \
"s_branch LSLOW0_%=\nLcs45_%=:\ns_lshl_b32 s9,s52,1\ns_add_u32 s9,s9,1\ns_sub_u32 s8,59,s9\ns_cmp_ge_u32 s9,s70\ns_cselect_b32 s9,s8,s9\ns_mov_b32 s8,0\n" \
"s_mov_b32 s64,5\ns_branch LSLOW1_%=\nLcs47_%=:\ns_lshl_b32 s9,s52,1\ns_add_u32 s9,s9,0\ns_sub_u32 s8,59,s9\ns_cmp_ge_u32 s9,s70\ns_cselect_b32 s9,s8,s9\n" \
"s_mov_b32 s8,0\ns_mov_b32 s64,5\ns_branch LSLOW0_%=\nLcs49_%=:\ns_lshl_b32 s9,s52,1\ns_add_u32 s9,s9,1\ns_sub_u32 s8,59,s9\ns_cmp_ge_u32 s9,s70\n" \
"s_cselect_b32 s9,s8,s9\ns_mov_b32 s8,0\ns_mov_b32 s64,6\ns_branch LSLOW1_%=\nLcs51_%=:\ns_lshl_b32 s9,s52,1\ns_add_u32 s9,s9,0\ns_sub_u32 s8,59,s9\n" \
"s_cmp_ge_u32 s9,s70\ns_cselect_b32 s9,s8,s9\ns_mov_b32 s8,0\ns_mov_b32 s64,6\ns_branch LSLOW0_%=\nLcs53_%=:\ns_lshl_b32 s9,s52,1\ns_add_u32 s9,s9,1\n" \
"s_sub_u32 s8,59,s9\ns_cmp_ge_u32 s9,s70\ns_cselect_b32 s9,s8,s9\ns_mov_b32 s8,0\ns_mov_b32 s64,7\ns_branch LSLOW1_%=\nLcs55_%=:\ns_lshl_b32 s9,s52,1\n" \
"s_add_u32 s9,s9,0\ns_sub_u32 s8,59,s9\ns_cmp_ge_u32 s9,s70\ns_cselect_b32 s9,s8,s9\ns_mov_b32 s8,0\ns_mov_b32 s64,7\ns_branch LSLOW0_%=\nLcs57_%=:\n" \
"s_lshl_b32 s9,s52,1\ns_add_u32 s9,s9,1\ns_sub_u32 s8,59,s9\ns_cmp_ge_u32 s9,s70\ns_cselect_b32 s9,s8,s9\ns_mov_b32 s8,0\ns_mov_b32 s64,8\ns_branch LSLOW1_%=\n" \
"Lcs59_%=:\ns_lshl_b32 s9,s52,1\ns_add_u32 s9,s9,0\ns_sub_u32 s8,59,s9\ns_cmp_ge_u32 s9,s70\ns_cselect_b32 s9,s8,s9\ns_mov_b32 s8,0\ns_mov_b32 s64,8\n" \
"s_branch LSLOW0_%=\nLcs61_%=:\ns_lshl_b32 s9,s52,1\ns_add_u32 s9,s9,1\ns_sub_u32 s8,59,s9\ns_cmp_ge_u32 s9,s70\ns_cselect_b32 s9,s8,s9\ns_mov_b32 s8,0\n" \
"s_mov_b32 s64,9\ns_branch LSLOW1_%=\nLcs63_%=:\ns_lshl_b32 s9,s52,1\ns_add_u32 s9,s9,0\ns_sub_u32 s8,59,s9\ns_cmp_ge_u32 s9,s70\ns_cselect_b32 s9,s8,s9\n" \
"s_mov_b32 s8,0\ns_mov_b32 s64,9\ns_branch LSLOW0_%=\nLcs65_%=:\ns_lshl_b32 s9,s52,1\ns_add_u32 s9,s9,1\ns_sub_u32 s8,59,s9\ns_cmp_ge_u32 s9,s70\n" \
"s_cselect_b32 s9,s8,s9\ns_mov_b32 s8,0\ns_mov_b32 s64,74\ns_branch LSLOW1_%=\nLSLOW0_%=:\ns_lshl_b32 s9,s9,14\ns_add_u32 s9,s9,s71\ns_nop 7\n" \
"s_waitcnt lgkmcnt(0)\nbuffer_load_dwordx4 a[240:243],v4,s[36:39],s9 offen offset:0 sc1\nbuffer_load_dwordx4 a[244:247],v4,s[36:39],s9 offen offset:2048 sc1\ns_add_u32 s9,s9,4096\nbuffer_load_dwordx4 a[248:251],v4,s[36:39],s9 offen offset:0 sc1\nbuffer_load_dwordx4 a[252:255],v4,s[36:39],s9 offen offset:2048 sc1\ns_sub_u32 s9,s9,3072\ns_waitcnt vmcnt(0)\n" \
"s_cmp_gt_u32 s8,0\ns_cbranch_scc1 Lk67_%=\nM v[64:67],a[240:243],a[128:131],v[32:35]\nM v[64:67],a[244:247],a[132:135],v[64:67]\nM v[64:67],a[248:251],a[136:139],v[64:67]\nM v[64:67],a[252:255],a[140:143],v[64:67]\nLk67_%=:\ns_cmp_gt_u32 s8,1\n" \
"s_cbranch_scc1 Lk68_%=\nM v[72:75],a[240:243],a[144:147],v[36:39]\nM v[72:75],a[244:247],a[148:151],v[72:75]\nM v[72:75],a[248:251],a[152:155],v[72:75]\nM v[72:75],a[252:255],a[156:159],v[72:75]\nLk68_%=:\ns_cmp_gt_u32 s8,2\ns_cbranch_scc1 Lk69_%=\n" \
"M v[80:83],a[240:243],a[160:163],v[40:43]\nM v[80:83],a[244:247],a[164:167],v[80:83]\nM v[80:83],a[248:251],a[168:171],v[80:83]\nM v[80:83],a[252:255],a[172:175],v[80:83]\nLk69_%=:\ns_cmp_gt_u32 s8,3\ns_cbranch_scc1 Lk70_%=\nM v[88:91],a[240:243],a[176:179],v[44:47]\n" \
"M v[88:91],a[244:247],a[180:183],v[88:91]\nM v[88:91],a[248:251],a[184:187],v[88:91]\nM v[88:91],a[252:255],a[188:191],v[88:91]\nLk70_%=:\ns_nop 7\nbuffer_load_dwordx4 a[240:243],v4,s[36:39],s9 offen offset:0 sc1\nbuffer_load_dwordx4 a[244:247],v4,s[36:39],s9 offen offset:2048 sc1\ns_add_u32 s9,s9,4096\n" \
"buffer_load_dwordx4 a[248:251],v4,s[36:39],s9 offen offset:0 sc1\nbuffer_load_dwordx4 a[252:255],v4,s[36:39],s9 offen offset:2048 sc1\ns_sub_u32 s9,s9,3072\ns_waitcnt vmcnt(0)\ns_cmp_gt_u32 s8,0\ns_cbranch_scc1 Lk71_%=\nM v[68:71],a[240:243],a[128:131],v[32:35]\nM v[68:71],a[244:247],a[132:135],v[68:71]\n" \
"M v[68:71],a[248:251],a[136:139],v[68:71]\nM v[68:71],a[252:255],a[140:143],v[68:71]\nLk71_%=:\ns_cmp_gt_u32 s8,1\ns_cbranch_scc1 Lk72_%=\nM v[76:79],a[240:243],a[144:147],v[36:39]\nM v[76:79],a[244:247],a[148:151],v[76:79]\nM v[76:79],a[248:251],a[152:155],v[76:79]\n" \
"M v[76:79],a[252:255],a[156:159],v[76:79]\nLk72_%=:\ns_cmp_gt_u32 s8,2\ns_cbranch_scc1 Lk73_%=\nM v[84:87],a[240:243],a[160:163],v[40:43]\nM v[84:87],a[244:247],a[164:167],v[84:87]\nM v[84:87],a[248:251],a[168:171],v[84:87]\nM v[84:87],a[252:255],a[172:175],v[84:87]\n" \
"Lk73_%=:\ns_cmp_gt_u32 s8,3\ns_cbranch_scc1 Lk74_%=\nM v[92:95],a[240:243],a[176:179],v[44:47]\nM v[92:95],a[244:247],a[180:183],v[92:95]\nM v[92:95],a[248:251],a[184:187],v[92:95]\nM v[92:95],a[252:255],a[188:191],v[92:95]\nLk74_%=:\n" \
"s_nop 7\ns_cmp_gt_u32 s8,0\ns_cbranch_scc0 Lk76_%=\nv_mov_b32 v16,v24\ns_cmp_eq_u32 s8,1\ns_cbranch_scc0 Lk75_%=\nM v[16:19],a[224:227],v[64:67],v[16:19]\ns_nop 7\n" \
"s_branch Lk75_%=\nLk76_%=:\nv_max3_f32 v62,v64,v65,v66\nv_max3_f32 v63,v67,v68,v69\nv_max3_f32 v62,v62,v70,v71\nv_max3_f32 v62,v62,v63,0\nv_mov_b32 v63,v62\ns_nop 1\n" \
"v_permlane32_swap_b32 v62,v63\nv_max_f32 v62,v62,v63\nv_mov_b32 v63,v62\ns_nop 1\nv_permlane16_swap_b32 v62,v63\nv_max_f32 v62,v62,v63\nv_exp_f32 v63,-v62\nv_sub_f32 v64,v64,v62\n" \
"v_sub_f32 v65,v65,v62\nv_sub_f32 v66,v66,v62\nv_sub_f32 v67,v67,v62\nv_sub_f32 v68,v68,v62\nv_sub_f32 v69,v69,v62\nv_sub_f32 v70,v70,v62\nv_sub_f32 v71,v71,v62\nv_exp_f32 v64,v64\n" \
"v_exp_f32 v65,v65\nv_exp_f32 v66,v66\nv_exp_f32 v67,v67\nv_exp_f32 v68,v68\nv_exp_f32 v69,v69\nv_exp_f32 v70,v70\nv_exp_f32 v71,v71\nv_mul_f32 v16,v24,v63\n" \
"C v64,v64,v65\nC v65,v66,v67\nC v66,v68,v69\nC v67,v70,v71\nv_sub_f32 v32,v32,v62\nv_sub_f32 v33,v33,v62\nv_sub_f32 v34,v34,v62\nv_sub_f32 v35,v35,v62\n" \
"M v[16:19],a[224:227],v[64:67],v[16:19]\nv_mul_f32 v128,v128,v63\nv_mul_f32 v129,v129,v63\nv_mul_f32 v130,v130,v63\nv_mul_f32 v131,v131,v63\nv_mul_f32 v132,v132,v63\nv_mul_f32 v133,v133,v63\nv_mul_f32 v134,v134,v63\n" \
"v_mul_f32 v135,v135,v63\nv_mul_f32 v136,v136,v63\nv_mul_f32 v137,v137,v63\nv_mul_f32 v138,v138,v63\nv_mul_f32 v139,v139,v63\nv_mul_f32 v140,v140,v63\nv_mul_f32 v141,v141,v63\nv_mul_f32 v142,v142,v63\n" \
"v_mul_f32 v143,v143,v63\nv_mul_f32 v144,v144,v63\nv_mul_f32 v145,v145,v63\nv_mul_f32 v146,v146,v63\nv_mul_f32 v147,v147,v63\nv_mul_f32 v148,v148,v63\nv_mul_f32 v149,v149,v63\nv_mul_f32 v150,v150,v63\n" \
"v_mul_f32 v151,v151,v63\nv_mul_f32 v152,v152,v63\nv_mul_f32 v153,v153,v63\nv_mul_f32 v154,v154,v63\nv_mul_f32 v155,v155,v63\nv_mul_f32 v156,v156,v63\nv_mul_f32 v157,v157,v63\nv_mul_f32 v158,v158,v63\n" \
"v_mul_f32 v159,v159,v63\ns_bitcmp1_b32 s64,6\ns_cbranch_scc1 Lk75_%=\nv_sub_f32 v96,v96,v62\nv_sub_f32 v97,v97,v62\nv_sub_f32 v98,v98,v62\nv_sub_f32 v99,v99,v62\nv_sub_f32 v100,v100,v62\n" \
"v_sub_f32 v101,v101,v62\nv_sub_f32 v102,v102,v62\nv_sub_f32 v103,v103,v62\nLk75_%=:\ns_cmp_gt_u32 s8,1\ns_cbranch_scc0 Lk78_%=\nv_mov_b32 v17,v25\ns_cmp_eq_u32 s8,2\n" \
"s_cbranch_scc0 Lk77_%=\nM v[16:19],a[228:231],v[72:75],v[16:19]\ns_nop 7\ns_branch Lk77_%=\nLk78_%=:\nv_max3_f32 v62,v72,v73,v74\nv_max3_f32 v63,v75,v76,v77\nv_max3_f32 v62,v62,v78,v79\n" \
"v_max3_f32 v62,v62,v63,0\nv_mov_b32 v63,v62\ns_nop 1\nv_permlane32_swap_b32 v62,v63\nv_max_f32 v62,v62,v63\nv_mov_b32 v63,v62\ns_nop 1\nv_permlane16_swap_b32 v62,v63\n" \
"v_max_f32 v62,v62,v63\nv_exp_f32 v63,-v62\nv_sub_f32 v72,v72,v62\nv_sub_f32 v73,v73,v62\nv_sub_f32 v74,v74,v62\nv_sub_f32 v75,v75,v62\nv_sub_f32 v76,v76,v62\nv_sub_f32 v77,v77,v62\n" \
"v_sub_f32 v78,v78,v62\nv_sub_f32 v79,v79,v62\nv_exp_f32 v72,v72\nv_exp_f32 v73,v73\nv_exp_f32 v74,v74\nv_exp_f32 v75,v75\nv_exp_f32 v76,v76\nv_exp_f32 v77,v77\n" \
"v_exp_f32 v78,v78\nv_exp_f32 v79,v79\nv_mul_f32 v17,v25,v63\nC v72,v72,v73\nC v73,v74,v75\nC v74,v76,v77\nC v75,v78,v79\nv_sub_f32 v36,v36,v62\n" \
"v_sub_f32 v37,v37,v62\nv_sub_f32 v38,v38,v62\nv_sub_f32 v39,v39,v62\nM v[16:19],a[228:231],v[72:75],v[16:19]\nv_mul_f32 v160,v160,v63\nv_mul_f32 v161,v161,v63\nv_mul_f32 v162,v162,v63\nv_mul_f32 v163,v163,v63\n" \
"v_mul_f32 v164,v164,v63\nv_mul_f32 v165,v165,v63\nv_mul_f32 v166,v166,v63\nv_mul_f32 v167,v167,v63\nv_mul_f32 v168,v168,v63\nv_mul_f32 v169,v169,v63\nv_mul_f32 v170,v170,v63\nv_mul_f32 v171,v171,v63\n" \
"v_mul_f32 v172,v172,v63\nv_mul_f32 v173,v173,v63\nv_mul_f32 v174,v174,v63\nv_mul_f32 v175,v175,v63\nv_mul_f32 v176,v176,v63\nv_mul_f32 v177,v177,v63\nv_mul_f32 v178,v178,v63\nv_mul_f32 v179,v179,v63\n" \
"v_mul_f32 v180,v180,v63\nv_mul_f32 v181,v181,v63\nv_mul_f32 v182,v182,v63\nv_mul_f32 v183,v183,v63\nv_mul_f32 v184,v184,v63\nv_mul_f32 v185,v185,v63\nv_mul_f32 v186,v186,v63\nv_mul_f32 v187,v187,v63\n" \
"v_mul_f32 v188,v188,v63\nv_mul_f32 v189,v189,v63\nv_mul_f32 v190,v190,v63\nv_mul_f32 v191,v191,v63\ns_bitcmp1_b32 s64,6\ns_cbranch_scc1 Lk77_%=\nv_sub_f32 v104,v104,v62\nv_sub_f32 v105,v105,v62\n" \
"v_sub_f32 v106,v106,v62\nv_sub_f32 v107,v107,v62\nv_sub_f32 v108,v108,v62\nv_sub_f32 v109,v109,v62\nv_sub_f32 v110,v110,v62\nv_sub_f32 v111,v111,v62\nLk77_%=:\ns_cmp_gt_u32 s8,2\n" \
"s_cbranch_scc0 Lk80_%=\nv_mov_b32 v18,v26\ns_cmp_eq_u32 s8,3\ns_cbranch_scc0 Lk79_%=\nM v[16:19],a[232:235],v[80:83],v[16:19]\ns_nop 7\ns_branch Lk79_%=\nLk80_%=:\n" \
"v_max3_f32 v62,v80,v81,v82\nv_max3_f32 v63,v83,v84,v85\nv_max3_f32 v62,v62,v86,v87\nv_max3_f32 v62,v62,v63,0\nv_mov_b32 v63,v62\ns_nop 1\nv_permlane32_swap_b32 v62,v63\nv_max_f32 v62,v62,v63\n" \
"v_mov_b32 v63,v62\ns_nop 1\nv_permlane16_swap_b32 v62,v63\nv_max_f32 v62,v62,v63\nv_exp_f32 v63,-v62\nv_sub_f32 v80,v80,v62\nv_sub_f32 v81,v81,v62\nv_sub_f32 v82,v82,v62\n" \
"v_sub_f32 v83,v83,v62\nv_sub_f32 v84,v84,v62\nv_sub_f32 v85,v85,v62\nv_sub_f32 v86,v86,v62\nv_sub_f32 v87,v87,v62\nv_exp_f32 v80,v80\nv_exp_f32 v81,v81\nv_exp_f32 v82,v82\n" \
"v_exp_f32 v83,v83\nv_exp_f32 v84,v84\nv_exp_f32 v85,v85\nv_exp_f32 v86,v86\nv_exp_f32 v87,v87\nv_mul_f32 v18,v26,v63\nC v80,v80,v81\nC v81,v82,v83\n" \
"C v82,v84,v85\nC v83,v86,v87\nv_sub_f32 v40,v40,v62\nv_sub_f32 v41,v41,v62\nv_sub_f32 v42,v42,v62\nv_sub_f32 v43,v43,v62\nM v[16:19],a[232:235],v[80:83],v[16:19]\nv_mul_f32 v192,v192,v63\n" \
"v_mul_f32 v193,v193,v63\nv_mul_f32 v194,v194,v63\nv_mul_f32 v195,v195,v63\nv_mul_f32 v196,v196,v63\nv_mul_f32 v197,v197,v63\nv_mul_f32 v198,v198,v63\nv_mul_f32 v199,v199,v63\nv_mul_f32 v200,v200,v63\n" \
"v_mul_f32 v201,v201,v63\nv_mul_f32 v202,v202,v63\nv_mul_f32 v203,v203,v63\nv_mul_f32 v204,v204,v63\nv_mul_f32 v205,v205,v63\nv_mul_f32 v206,v206,v63\nv_mul_f32 v207,v207,v63\nv_mul_f32 v208,v208,v63\n" \
"v_mul_f32 v209,v209,v63\nv_mul_f32 v210,v210,v63\nv_mul_f32 v211,v211,v63\nv_mul_f32 v212,v212,v63\nv_mul_f32 v213,v213,v63\nv_mul_f32 v214,v214,v63\nv_mul_f32 v215,v215,v63\nv_mul_f32 v216,v216,v63\n" \
"v_mul_f32 v217,v217,v63\nv_mul_f32 v218,v218,v63\nv_mul_f32 v219,v219,v63\nv_mul_f32 v220,v220,v63\nv_mul_f32 v221,v221,v63\nv_mul_f32 v222,v222,v63\nv_mul_f32 v223,v223,v63\ns_bitcmp1_b32 s64,6\n" \
"s_cbranch_scc1 Lk79_%=\nv_sub_f32 v112,v112,v62\nv_sub_f32 v113,v113,v62\nv_sub_f32 v114,v114,v62\nv_sub_f32 v115,v115,v62\nv_sub_f32 v116,v116,v62\nv_sub_f32 v117,v117,v62\nv_sub_f32 v118,v118,v62\n" \
"v_sub_f32 v119,v119,v62\nLk79_%=:\ns_cmp_gt_u32 s8,3\ns_cbranch_scc0 Lk82_%=\nv_mov_b32 v19,v27\ns_cmp_eq_u32 s8,4\ns_cbranch_scc0 Lk81_%=\nM v[16:19],a[236:239],v[88:91],v[16:19]\n" \
"s_nop 7\ns_branch Lk81_%=\nLk82_%=:\nv_max3_f32 v62,v88,v89,v90\nv_max3_f32 v63,v91,v92,v93\nv_max3_f32 v62,v62,v94,v95\nv_max3_f32 v62,v62,v63,0\nv_mov_b32 v63,v62\n" \
"s_nop 1\nv_permlane32_swap_b32 v62,v63\nv_max_f32 v62,v62,v63\nv_mov_b32 v63,v62\ns_nop 1\nv_permlane16_swap_b32 v62,v63\nv_max_f32 v62,v62,v63\nv_exp_f32 v63,-v62\n" \
"v_sub_f32 v88,v88,v62\nv_sub_f32 v89,v89,v62\nv_sub_f32 v90,v90,v62\nv_sub_f32 v91,v91,v62\nv_sub_f32 v92,v92,v62\nv_sub_f32 v93,v93,v62\nv_sub_f32 v94,v94,v62\nv_sub_f32 v95,v95,v62\n" \
"v_exp_f32 v88,v88\nv_exp_f32 v89,v89\nv_exp_f32 v90,v90\nv_exp_f32 v91,v91\nv_exp_f32 v92,v92\nv_exp_f32 v93,v93\nv_exp_f32 v94,v94\nv_exp_f32 v95,v95\n" \
"v_mul_f32 v19,v27,v63\nC v88,v88,v89\nC v89,v90,v91\nC v90,v92,v93\nC v91,v94,v95\nv_sub_f32 v44,v44,v62\nv_sub_f32 v45,v45,v62\nv_sub_f32 v46,v46,v62\n" \
"v_sub_f32 v47,v47,v62\nM v[16:19],a[236:239],v[88:91],v[16:19]\nv_mul_f32 v224,v224,v63\nv_mul_f32 v225,v225,v63\nv_mul_f32 v226,v226,v63\nv_mul_f32 v227,v227,v63\nv_mul_f32 v228,v228,v63\nv_mul_f32 v229,v229,v63\n" \
"v_mul_f32 v230,v230,v63\nv_mul_f32 v231,v231,v63\nv_mul_f32 v232,v232,v63\nv_mul_f32 v233,v233,v63\nv_mul_f32 v234,v234,v63\nv_mul_f32 v235,v235,v63\nv_mul_f32 v236,v236,v63\nv_mul_f32 v237,v237,v63\n" \
"v_mul_f32 v238,v238,v63\nv_mul_f32 v239,v239,v63\nv_mul_f32 v240,v240,v63\nv_mul_f32 v241,v241,v63\nv_mul_f32 v242,v242,v63\nv_mul_f32 v243,v243,v63\nv_mul_f32 v244,v244,v63\nv_mul_f32 v245,v245,v63\n" \
"v_mul_f32 v246,v246,v63\nv_mul_f32 v247,v247,v63\nv_mul_f32 v248,v248,v63\nv_mul_f32 v249,v249,v63\nv_mul_f32 v250,v250,v63\nv_mul_f32 v251,v251,v63\nv_mul_f32 v252,v252,v63\nv_mul_f32 v253,v253,v63\n" \
"v_mul_f32 v254,v254,v63\nv_mul_f32 v255,v255,v63\ns_bitcmp1_b32 s64,6\ns_cbranch_scc1 Lk81_%=\nv_sub_f32 v120,v120,v62\nv_sub_f32 v121,v121,v62\nv_sub_f32 v122,v122,v62\nv_sub_f32 v123,v123,v62\n" \
"v_sub_f32 v124,v124,v62\nv_sub_f32 v125,v125,v62\nv_sub_f32 v126,v126,v62\nv_sub_f32 v127,v127,v62\nLk81_%=:\ns_nop 7\nLSR0_%=:\ns_cmp_eq_u32 s64,0\n" \
"s_cbranch_scc1 Lcr4_%=\ns_cmp_eq_u32 s64,1\ns_cbranch_scc1 Lcr8_%=\ns_cmp_eq_u32 s64,2\ns_cbranch_scc1 Lcr12_%=\ns_cmp_eq_u32 s64,3\ns_cbranch_scc1 Lcr40_%=\ns_cmp_eq_u32 s64,4\n" \
"s_cbranch_scc1 Lcr44_%=\ns_cmp_eq_u32 s64,5\ns_cbranch_scc1 Lcr48_%=\ns_cmp_eq_u32 s64,6\ns_cbranch_scc1 Lcr52_%=\ns_cmp_eq_u32 s64,7\ns_cbranch_scc1 Lcr56_%=\ns_cmp_eq_u32 s64,8\n" \
"s_cbranch_scc1 Lcr60_%=\ns_branch Lcr64_%=\nLSLOW1_%=:\ns_cmp_eq_u32 s75,1\ns_cbranch_scc1 LSR1_%=\ns_lshl_b32 s9,s9,14\ns_add_u32 s9,s9,s71\ns_nop 7\n" \
"s_waitcnt lgkmcnt(0)\nbuffer_load_dwordx4 a[240:243],v4,s[36:39],s9 offen offset:0 sc1\nbuffer_load_dwordx4 a[244:247],v4,s[36:39],s9 offen offset:2048 sc1\ns_add_u32 s9,s9,4096\nbuffer_load_dwordx4 a[248:251],v4,s[36:39],s9 offen offset:0 sc1\nbuffer_load_dwordx4 a[252:255],v4,s[36:39],s9 offen offset:2048 sc1\ns_sub_u32 s9,s9,3072\ns_waitcnt vmcnt(0)\n" \
"s_cmp_gt_u32 s8,0\ns_cbranch_scc1 Lk83_%=\nM v[96:99],a[240:243],a[128:131],v[32:35]\nM v[96:99],a[244:247],a[132:135],v[96:99]\nM v[96:99],a[248:251],a[136:139],v[96:99]\nM v[96:99],a[252:255],a[140:143],v[96:99]\nLk83_%=:\ns_cmp_gt_u32 s8,1\n" \
"s_cbranch_scc1 Lk84_%=\nM v[104:107],a[240:243],a[144:147],v[36:39]\nM v[104:107],a[244:247],a[148:151],v[104:107]\nM v[104:107],a[248:251],a[152:155],v[104:107]\nM v[104:107],a[252:255],a[156:159],v[104:107]\nLk84_%=:\ns_cmp_gt_u32 s8,2\ns_cbranch_scc1 Lk85_%=\n" \
"M v[112:115],a[240:243],a[160:163],v[40:43]\nM v[112:115],a[244:247],a[164:167],v[112:115]\nM v[112:115],a[248:251],a[168:171],v[112:115]\nM v[112:115],a[252:255],a[172:175],v[112:115]\nLk85_%=:\ns_cmp_gt_u32 s8,3\ns_cbranch_scc1 Lk86_%=\nM v[120:123],a[240:243],a[176:179],v[44:47]\n" \
"M v[120:123],a[244:247],a[180:183],v[120:123]\nM v[120:123],a[248:251],a[184:187],v[120:123]\nM v[120:123],a[252:255],a[188:191],v[120:123]\nLk86_%=:\ns_nop 7\nbuffer_load_dwordx4 a[240:243],v4,s[36:39],s9 offen offset:0 sc1\nbuffer_load_dwordx4 a[244:247],v4,s[36:39],s9 offen offset:2048 sc1\ns_add_u32 s9,s9,4096\n" \
"buffer_load_dwordx4 a[248:251],v4,s[36:39],s9 offen offset:0 sc1\nbuffer_load_dwordx4 a[252:255],v4,s[36:39],s9 offen offset:2048 sc1\ns_sub_u32 s9,s9,3072\ns_waitcnt vmcnt(0)\ns_cmp_gt_u32 s8,0\ns_cbranch_scc1 Lk87_%=\nM v[100:103],a[240:243],a[128:131],v[32:35]\nM v[100:103],a[244:247],a[132:135],v[100:103]\n" \
"M v[100:103],a[248:251],a[136:139],v[100:103]\nM v[100:103],a[252:255],a[140:143],v[100:103]\nLk87_%=:\ns_cmp_gt_u32 s8,1\ns_cbranch_scc1 Lk88_%=\nM v[108:111],a[240:243],a[144:147],v[36:39]\nM v[108:111],a[244:247],a[148:151],v[108:111]\nM v[108:111],a[248:251],a[152:155],v[108:111]\n" \
"M v[108:111],a[252:255],a[156:159],v[108:111]\nLk88_%=:\ns_cmp_gt_u32 s8,2\ns_cbranch_scc1 Lk89_%=\nM v[116:119],a[240:243],a[160:163],v[40:43]\nM v[116:119],a[244:247],a[164:167],v[116:119]\nM v[116:119],a[248:251],a[168:171],v[116:119]\nM v[116:119],a[252:255],a[172:175],v[116:119]\n" \
"Lk89_%=:\ns_cmp_gt_u32 s8,3\ns_cbranch_scc1 Lk90_%=\nM v[124:127],a[240:243],a[176:179],v[44:47]\nM v[124:127],a[244:247],a[180:183],v[124:127]\nM v[124:127],a[248:251],a[184:187],v[124:127]\nM v[124:127],a[252:255],a[188:191],v[124:127]\nLk90_%=:\n" \
"s_nop 7\ns_cmp_gt_u32 s8,0\ns_cbranch_scc0 Lk92_%=\nv_mov_b32 v24,v16\ns_cmp_eq_u32 s8,1\ns_cbranch_scc0 Lk91_%=\nM v[24:27],a[224:227],v[96:99],v[24:27]\ns_nop 7\n" \
"s_branch Lk91_%=\nLk92_%=:\nv_max3_f32 v62,v96,v97,v98\nv_max3_f32 v63,v99,v100,v101\nv_max3_f32 v62,v62,v102,v103\nv_max3_f32 v62,v62,v63,0\nv_mov_b32 v63,v62\ns_nop 1\n" \
"v_permlane32_swap_b32 v62,v63\nv_max_f32 v62,v62,v63\nv_mov_b32 v63,v62\ns_nop 1\nv_permlane16_swap_b32 v62,v63\nv_max_f32 v62,v62,v63\nv_exp_f32 v63,-v62\nv_sub_f32 v96,v96,v62\n" \
"v_sub_f32 v97,v97,v62\nv_sub_f32 v98,v98,v62\nv_sub_f32 v99,v99,v62\nv_sub_f32 v100,v100,v62\nv_sub_f32 v101,v101,v62\nv_sub_f32 v102,v102,v62\nv_sub_f32 v103,v103,v62\nv_exp_f32 v96,v96\n" \
"v_exp_f32 v97,v97\nv_exp_f32 v98,v98\nv_exp_f32 v99,v99\nv_exp_f32 v100,v100\nv_exp_f32 v101,v101\nv_exp_f32 v102,v102\nv_exp_f32 v103,v103\nv_mul_f32 v24,v16,v63\n" \
"C v96,v96,v97\nC v97,v98,v99\nC v98,v100,v101\nC v99,v102,v103\nv_sub_f32 v32,v32,v62\nv_sub_f32 v33,v33,v62\nv_sub_f32 v34,v34,v62\nv_sub_f32 v35,v35,v62\n" \
"M v[24:27],a[224:227],v[96:99],v[24:27]\nv_mul_f32 v128,v128,v63\nv_mul_f32 v129,v129,v63\nv_mul_f32 v130,v130,v63\nv_mul_f32 v131,v131,v63\nv_mul_f32 v132,v132,v63\nv_mul_f32 v133,v133,v63\nv_mul_f32 v134,v134,v63\n" \
"v_mul_f32 v135,v135,v63\nv_mul_f32 v136,v136,v63\nv_mul_f32 v137,v137,v63\nv_mul_f32 v138,v138,v63\nv_mul_f32 v139,v139,v63\nv_mul_f32 v140,v140,v63\nv_mul_f32 v141,v141,v63\nv_mul_f32 v142,v142,v63\n" \
"v_mul_f32 v143,v143,v63\nv_mul_f32 v144,v144,v63\nv_mul_f32 v145,v145,v63\nv_mul_f32 v146,v146,v63\nv_mul_f32 v147,v147,v63\nv_mul_f32 v148,v148,v63\nv_mul_f32 v149,v149,v63\nv_mul_f32 v150,v150,v63\n" \
"v_mul_f32 v151,v151,v63\nv_mul_f32 v152,v152,v63\nv_mul_f32 v153,v153,v63\nv_mul_f32 v154,v154,v63\nv_mul_f32 v155,v155,v63\nv_mul_f32 v156,v156,v63\nv_mul_f32 v157,v157,v63\nv_mul_f32 v158,v158,v63\n" \
"v_mul_f32 v159,v159,v63\ns_bitcmp1_b32 s64,6\ns_cbranch_scc1 Lk91_%=\nv_sub_f32 v64,v64,v62\nv_sub_f32 v65,v65,v62\nv_sub_f32 v66,v66,v62\nv_sub_f32 v67,v67,v62\nv_sub_f32 v68,v68,v62\n" \
"v_sub_f32 v69,v69,v62\nv_sub_f32 v70,v70,v62\nv_sub_f32 v71,v71,v62\nLk91_%=:\ns_cmp_gt_u32 s8,1\ns_cbranch_scc0 Lk94_%=\nv_mov_b32 v25,v17\ns_cmp_eq_u32 s8,2\n" \
"s_cbranch_scc0 Lk93_%=\nM v[24:27],a[228:231],v[104:107],v[24:27]\ns_nop 7\ns_branch Lk93_%=\nLk94_%=:\nv_max3_f32 v62,v104,v105,v106\nv_max3_f32 v63,v107,v108,v109\nv_max3_f32 v62,v62,v110,v111\n" \
"v_max3_f32 v62,v62,v63,0\nv_mov_b32 v63,v62\ns_nop 1\nv_permlane32_swap_b32 v62,v63\nv_max_f32 v62,v62,v63\nv_mov_b32 v63,v62\ns_nop 1\nv_permlane16_swap_b32 v62,v63\n" \
"v_max_f32 v62,v62,v63\nv_exp_f32 v63,-v62\nv_sub_f32 v104,v104,v62\nv_sub_f32 v105,v105,v62\nv_sub_f32 v106,v106,v62\nv_sub_f32 v107,v107,v62\nv_sub_f32 v108,v108,v62\nv_sub_f32 v109,v109,v62\n" \
"v_sub_f32 v110,v110,v62\nv_sub_f32 v111,v111,v62\nv_exp_f32 v104,v104\nv_exp_f32 v105,v105\nv_exp_f32 v106,v106\nv_exp_f32 v107,v107\nv_exp_f32 v108,v108\nv_exp_f32 v109,v109\n" \
"v_exp_f32 v110,v110\nv_exp_f32 v111,v111\nv_mul_f32 v25,v17,v63\nC v104,v104,v105\nC v105,v106,v107\nC v106,v108,v109\nC v107,v110,v111\nv_sub_f32 v36,v36,v62\n" \
"v_sub_f32 v37,v37,v62\nv_sub_f32 v38,v38,v62\nv_sub_f32 v39,v39,v62\nM v[24:27],a[228:231],v[104:107],v[24:27]\nv_mul_f32 v160,v160,v63\nv_mul_f32 v161,v161,v63\nv_mul_f32 v162,v162,v63\nv_mul_f32 v163,v163,v63\n" \
"v_mul_f32 v164,v164,v63\nv_mul_f32 v165,v165,v63\nv_mul_f32 v166,v166,v63\nv_mul_f32 v167,v167,v63\nv_mul_f32 v168,v168,v63\nv_mul_f32 v169,v169,v63\nv_mul_f32 v170,v170,v63\nv_mul_f32 v171,v171,v63\n" \
"v_mul_f32 v172,v172,v63\nv_mul_f32 v173,v173,v63\nv_mul_f32 v174,v174,v63\nv_mul_f32 v175,v175,v63\nv_mul_f32 v176,v176,v63\nv_mul_f32 v177,v177,v63\nv_mul_f32 v178,v178,v63\nv_mul_f32 v179,v179,v63\n" \
"v_mul_f32 v180,v180,v63\nv_mul_f32 v181,v181,v63\nv_mul_f32 v182,v182,v63\nv_mul_f32 v183,v183,v63\nv_mul_f32 v184,v184,v63\nv_mul_f32 v185,v185,v63\nv_mul_f32 v186,v186,v63\nv_mul_f32 v187,v187,v63\n" \
"v_mul_f32 v188,v188,v63\nv_mul_f32 v189,v189,v63\nv_mul_f32 v190,v190,v63\nv_mul_f32 v191,v191,v63\ns_bitcmp1_b32 s64,6\ns_cbranch_scc1 Lk93_%=\nv_sub_f32 v72,v72,v62\nv_sub_f32 v73,v73,v62\n" \
"v_sub_f32 v74,v74,v62\nv_sub_f32 v75,v75,v62\nv_sub_f32 v76,v76,v62\nv_sub_f32 v77,v77,v62\nv_sub_f32 v78,v78,v62\nv_sub_f32 v79,v79,v62\nLk93_%=:\ns_cmp_gt_u32 s8,2\n" \
"s_cbranch_scc0 Lk96_%=\nv_mov_b32 v26,v18\ns_cmp_eq_u32 s8,3\ns_cbranch_scc0 Lk95_%=\nM v[24:27],a[232:235],v[112:115],v[24:27]\ns_nop 7\ns_branch Lk95_%=\nLk96_%=:\n" \
"v_max3_f32 v62,v112,v113,v114\nv_max3_f32 v63,v115,v116,v117\nv_max3_f32 v62,v62,v118,v119\nv_max3_f32 v62,v62,v63,0\nv_mov_b32 v63,v62\ns_nop 1\nv_permlane32_swap_b32 v62,v63\nv_max_f32 v62,v62,v63\n" \
"v_mov_b32 v63,v62\ns_nop 1\nv_permlane16_swap_b32 v62,v63\nv_max_f32 v62,v62,v63\nv_exp_f32 v63,-v62\nv_sub_f32 v112,v112,v62\nv_sub_f32 v113,v113,v62\nv_sub_f32 v114,v114,v62\n" \
"v_sub_f32 v115,v115,v62\nv_sub_f32 v116,v116,v62\nv_sub_f32 v117,v117,v62\nv_sub_f32 v118,v118,v62\nv_sub_f32 v119,v119,v62\nv_exp_f32 v112,v112\nv_exp_f32 v113,v113\nv_exp_f32 v114,v114\n" \
"v_exp_f32 v115,v115\nv_exp_f32 v116,v116\nv_exp_f32 v117,v117\nv_exp_f32 v118,v118\nv_exp_f32 v119,v119\nv_mul_f32 v26,v18,v63\nC v112,v112,v113\nC v113,v114,v115\n" \
"C v114,v116,v117\nC v115,v118,v119\nv_sub_f32 v40,v40,v62\nv_sub_f32 v41,v41,v62\nv_sub_f32 v42,v42,v62\nv_sub_f32 v43,v43,v62\nM v[24:27],a[232:235],v[112:115],v[24:27]\nv_mul_f32 v192,v192,v63\n" \
"v_mul_f32 v193,v193,v63\nv_mul_f32 v194,v194,v63\nv_mul_f32 v195,v195,v63\nv_mul_f32 v196,v196,v63\nv_mul_f32 v197,v197,v63\nv_mul_f32 v198,v198,v63\nv_mul_f32 v199,v199,v63\nv_mul_f32 v200,v200,v63\n" \
"v_mul_f32 v201,v201,v63\nv_mul_f32 v202,v202,v63\nv_mul_f32 v203,v203,v63\nv_mul_f32 v204,v204,v63\nv_mul_f32 v205,v205,v63\nv_mul_f32 v206,v206,v63\nv_mul_f32 v207,v207,v63\nv_mul_f32 v208,v208,v63\n" \
"v_mul_f32 v209,v209,v63\nv_mul_f32 v210,v210,v63\nv_mul_f32 v211,v211,v63\nv_mul_f32 v212,v212,v63\nv_mul_f32 v213,v213,v63\nv_mul_f32 v214,v214,v63\nv_mul_f32 v215,v215,v63\nv_mul_f32 v216,v216,v63\n" \
"v_mul_f32 v217,v217,v63\nv_mul_f32 v218,v218,v63\nv_mul_f32 v219,v219,v63\nv_mul_f32 v220,v220,v63\nv_mul_f32 v221,v221,v63\nv_mul_f32 v222,v222,v63\nv_mul_f32 v223,v223,v63\ns_bitcmp1_b32 s64,6\n" \
"s_cbranch_scc1 Lk95_%=\nv_sub_f32 v80,v80,v62\nv_sub_f32 v81,v81,v62\nv_sub_f32 v82,v82,v62\nv_sub_f32 v83,v83,v62\nv_sub_f32 v84,v84,v62\nv_sub_f32 v85,v85,v62\nv_sub_f32 v86,v86,v62\n" \
"v_sub_f32 v87,v87,v62\nLk95_%=:\ns_cmp_gt_u32 s8,3\ns_cbranch_scc0 Lk98_%=\nv_mov_b32 v27,v19\ns_cmp_eq_u32 s8,4\ns_cbranch_scc0 Lk97_%=\nM v[24:27],a[236:239],v[120:123],v[24:27]\n" \
"s_nop 7\ns_branch Lk97_%=\nLk98_%=:\nv_max3_f32 v62,v120,v121,v122\nv_max3_f32 v63,v123,v124,v125\nv_max3_f32 v62,v62,v126,v127\nv_max3_f32 v62,v62,v63,0\nv_mov_b32 v63,v62\n" \
"s_nop 1\nv_permlane32_swap_b32 v62,v63\nv_max_f32 v62,v62,v63\nv_mov_b32 v63,v62\ns_nop 1\nv_permlane16_swap_b32 v62,v63\nv_max_f32 v62,v62,v63\nv_exp_f32 v63,-v62\n" \
"v_sub_f32 v120,v120,v62\nv_sub_f32 v121,v121,v62\nv_sub_f32 v122,v122,v62\nv_sub_f32 v123,v123,v62\nv_sub_f32 v124,v124,v62\nv_sub_f32 v125,v125,v62\nv_sub_f32 v126,v126,v62\nv_sub_f32 v127,v127,v62\n" \
"v_exp_f32 v120,v120\nv_exp_f32 v121,v121\nv_exp_f32 v122,v122\nv_exp_f32 v123,v123\nv_exp_f32 v124,v124\nv_exp_f32 v125,v125\nv_exp_f32 v126,v126\nv_exp_f32 v127,v127\n" \
"v_mul_f32 v27,v19,v63\nC v120,v120,v121\nC v121,v122,v123\nC v122,v124,v125\nC v123,v126,v127\nv_sub_f32 v44,v44,v62\nv_sub_f32 v45,v45,v62\nv_sub_f32 v46,v46,v62\n" \
"v_sub_f32 v47,v47,v62\nM v[24:27],a[236:239],v[120:123],v[24:27]\nv_mul_f32 v224,v224,v63\nv_mul_f32 v225,v225,v63\nv_mul_f32 v226,v226,v63\nv_mul_f32 v227,v227,v63\nv_mul_f32 v228,v228,v63\nv_mul_f32 v229,v229,v63\n" \
"v_mul_f32 v230,v230,v63\nv_mul_f32 v231,v231,v63\nv_mul_f32 v232,v232,v63\nv_mul_f32 v233,v233,v63\nv_mul_f32 v234,v234,v63\nv_mul_f32 v235,v235,v63\nv_mul_f32 v236,v236,v63\nv_mul_f32 v237,v237,v63\n" \
"v_mul_f32 v238,v238,v63\nv_mul_f32 v239,v239,v63\nv_mul_f32 v240,v240,v63\nv_mul_f32 v241,v241,v63\nv_mul_f32 v242,v242,v63\nv_mul_f32 v243,v243,v63\nv_mul_f32 v244,v244,v63\nv_mul_f32 v245,v245,v63\n" \
"v_mul_f32 v246,v246,v63\nv_mul_f32 v247,v247,v63\nv_mul_f32 v248,v248,v63\nv_mul_f32 v249,v249,v63\nv_mul_f32 v250,v250,v63\nv_mul_f32 v251,v251,v63\nv_mul_f32 v252,v252,v63\nv_mul_f32 v253,v253,v63\n" \
"v_mul_f32 v254,v254,v63\nv_mul_f32 v255,v255,v63\ns_bitcmp1_b32 s64,6\ns_cbranch_scc1 Lk97_%=\nv_sub_f32 v88,v88,v62\nv_sub_f32 v89,v89,v62\nv_sub_f32 v90,v90,v62\nv_sub_f32 v91,v91,v62\n" \
"v_sub_f32 v92,v92,v62\nv_sub_f32 v93,v93,v62\nv_sub_f32 v94,v94,v62\nv_sub_f32 v95,v95,v62\nLk97_%=:\ns_nop 7\nLSR1_%=:\ns_cmp_eq_u32 s64,0\n" \
"s_cbranch_scc1 Lcr2_%=\ns_cmp_eq_u32 s64,1\ns_cbranch_scc1 Lcr6_%=\ns_cmp_eq_u32 s64,2\ns_cbranch_scc1 Lcr10_%=\ns_cmp_eq_u32 s64,3\ns_cbranch_scc1 Lcr14_%=\ns_cmp_eq_u32 s64,4\n" \
"s_cbranch_scc1 Lcr42_%=\ns_cmp_eq_u32 s64,5\ns_cbranch_scc1 Lcr46_%=\ns_cmp_eq_u32 s64,6\ns_cbranch_scc1 Lcr50_%=\ns_cmp_eq_u32 s64,7\ns_cbranch_scc1 Lcr54_%=\ns_cmp_eq_u32 s64,8\n" \
"s_cbranch_scc1 Lcr58_%=\ns_cmp_eq_u32 s64,9\ns_cbranch_scc1 Lcr62_%=\ns_branch Lcr66_%=\nLPK_%=:\ns_mov_b32 s25,0\nLPKL_%=:\ns_sleep 2\n" \
"buffer_load_dwordx2 v[58:59],v57,s[36:39],0 offen sc1\ns_add_u32 s25,s25,1\ns_waitcnt vmcnt(0)\nv_cmp_gt_u64 s[98:99],s[14:15],v[58:59]\nv_cmp_le_u64 s[18:19],s[16:17],v[58:59]\ns_cmp_gt_u32 s25,262144\ns_cbranch_scc1 LPKR_%=\ns_or_b64 s[98:99],s[98:99],s[18:19]\n" \
"s_cbranch_scc1 LPKL_%=\ns_branch LPKR_%=\nLPZ_%=:\ns_mov_b32 s25,0\nLPZL_%=:\ns_sleep 2\nbuffer_load_dwordx2 v[10:11],v5,s[36:39],0 offen sc1\ns_add_u32 s25,s25,1\n" \
"s_waitcnt vmcnt(0)\nv_cmp_gt_u64 s[98:99],s[14:15],v[10:11]\nv_cmp_le_u64 s[18:19],s[16:17],v[10:11]\ns_cmp_gt_u32 s25,262144\ns_cbranch_scc1 LPZR_%=\ns_or_b64 s[98:99],s[98:99],s[18:19]\ns_cbranch_scc1 LPZL_%=\ns_branch LPZR_%=\n" \
"LPE_%=:\ns_mov_b32 s25,0\nLPEL_%=:\ns_sleep 2\nbuffer_load_dwordx2 v[10:11],v5,s[36:39],0 offen sc1\ns_add_u32 s25,s25,1\ns_waitcnt vmcnt(0)\nv_cmp_gt_u64 s[98:99],s[14:15],v[10:11]\n" \
"v_cmp_le_u64 s[18:19],s[16:17],v[10:11]\ns_cmp_gt_u32 s25,262144\ns_cbranch_scc1 LPER_%=\ns_or_b64 s[98:99],s[98:99],s[18:19]\ns_and_b32 s98,s98,0xffff\ns_cbranch_scc1 LPEL_%=\ns_branch LPER_%=\nLPF_%=:\n" \
"s_mov_b32 s25,0\nLPFL_%=:\ns_sleep 2\nbuffer_load_dwordx2 v[10:11],v5,s[36:39],0 offen sc1\ns_add_u32 s25,s25,1\ns_waitcnt vmcnt(0)\nv_cmp_gt_u64 s[98:99],s[14:15],v[10:11]\nv_cmp_le_u64 s[18:19],s[16:17],v[10:11]\n" \
"s_cmp_gt_u32 s25,262144\ns_cbranch_scc1 LPFR_%=\ns_or_b64 s[98:99],s[98:99],s[18:19]\ns_and_b32 s98,s98,0xff0000\ns_cbranch_scc1 LPFL_%=\ns_branch LPFR_%=\nLPM_%=:\ns_mov_b32 s25,0\n" \
"LPML_%=:\ns_sleep 2\nbuffer_load_dwordx2 v[10:11],v5,s[36:39],0 offen sc1\ns_add_u32 s25,s25,1\ns_waitcnt vmcnt(0)\nv_cmp_gt_u64 s[98:99],s[14:15],v[10:11]\nv_cmp_le_u64 s[18:19],s[16:17],v[10:11]\ns_cmp_gt_u32 s25,262144\n" \
"s_cbranch_scc1 LPMR_%=\ns_or_b64 s[98:99],s[98:99],s[18:19]\ns_and_b32 s98,s98,0xff000000\ns_cbranch_scc1 LPML_%=\ns_branch LPMR_%=\nLPL_%=:\ns_mov_b32 s25,0\nLPLL_%=:\n" \
"s_sleep 2\nbuffer_load_dwordx2 v[10:11],v58,s[36:39],0 offen sc1\ns_add_u32 s25,s25,1\ns_waitcnt vmcnt(0)\nv_cmp_gt_u64 s[98:99],s[14:15],v[10:11]\nv_cmp_le_u64 s[18:19],s[16:17],v[10:11]\ns_cmp_gt_u32 s25,262144\ns_cbranch_scc1 LPLR_%=\n" \
"s_or_b64 s[98:99],s[98:99],s[18:19]\ns_cbranch_scc1 LPLL_%=\ns_branch LPLR_%=\nLPP_%=:\ns_mov_b32 s25,0\nLPPL_%=:\ns_sleep 2\nbuffer_load_dwordx2 v[10:11],v190,s[36:39],0 offen sc1\n" \
"s_add_u32 s25,s25,1\ns_waitcnt vmcnt(0)\nv_cmp_gt_u64 s[98:99],s[14:15],v[10:11]\nv_cmp_le_u64 s[18:19],s[16:17],v[10:11]\ns_cmp_gt_u32 s25,262144\ns_cbranch_scc1 LPPR_%=\ns_or_b64 s[98:99],s[98:99],s[18:19]\ns_and_b32 s98,s98,0x7\n" \
"s_cbranch_scc1 LPPL_%=\ns_branch LPPR_%=\nLIN_%=:\ns_mov_b32 s25,0\nLINL_%=:\ns_sleep 1\ns_add_u32 s57,s49,8192\nbuffer_load_dwordx4 v[32:35],v4,s[36:39],s57 offen offset:0 sc1\n" \
"buffer_load_dwordx4 v[36:39],v4,s[36:39],s57 offen offset:1024 sc1\ns_add_u32 s25,s25,1\ns_waitcnt vmcnt(0)\nv_max_u32 v10,v32,v33\nv_max3_u32 v10,v10,v34,v35\nv_max3_u32 v10,v10,v36,v37\nv_max3_u32 v10,v10,v38,v39\nv_cmp_eq_u32 vcc,-1,v10\n" \
"s_cmp_gt_u32 s25,262144\ns_cbranch_scc1 LINR_%=\ns_cbranch_vccnz LINL_%=\ns_branch LINR_%=\nLIO_%=:\ns_mov_b32 s25,0\nLIOL_%=:\ns_sleep 1\n" \
"s_add_u32 s57,s49,8192\nbuffer_load_dwordx4 v[40:43],v4,s[36:39],s57 offen offset:2048 sc1\nbuffer_load_dwordx4 v[44:47],v4,s[36:39],s57 offen offset:3072 sc1\ns_add_u32 s25,s25,1\ns_waitcnt vmcnt(0)\nv_max_u32 v10,v40,v41\nv_max3_u32 v10,v10,v42,v43\nv_max3_u32 v10,v10,v44,v45\n" \
"v_max3_u32 v10,v10,v46,v47\nv_cmp_eq_u32 vcc,-1,v10\ns_cmp_gt_u32 s25,262144\ns_cbranch_scc1 LIOR_%=\ns_cbranch_vccnz LIOL_%=\ns_branch LIOR_%=\nLPUK_%=:\ns_mov_b32 s25,0\n" \
"LPUKL_%=:\ns_sleep 2\nbuffer_load_dwordx2 v[58:59],v57,s[36:39],0 offen sc1\ns_add_u32 s25,s25,1\ns_waitcnt vmcnt(0)\nv_cmp_gt_u64 vcc,s[14:15],v[58:59]\nv_cmp_le_u64 s[18:19],s[16:17],v[58:59]\ns_cmp_gt_u32 s25,262144\n" \
"s_cbranch_scc1 LPUKR_%=\ns_or_b64 vcc,vcc,s[18:19]\ns_cbranch_scc1 LPUKL_%=\ns_branch LPUKR_%=\nLPUV_%=:\ns_mov_b32 s25,0\nLPUVL_%=:\ns_sleep 2\n" \
"buffer_load_dwordx2 v[58:59],v57,s[36:39],0 offen sc1\ns_add_u32 s25,s25,1\ns_waitcnt vmcnt(0)\nv_cmp_gt_u64 vcc,s[14:15],v[58:59]\nv_cmp_le_u64 s[18:19],s[16:17],v[58:59]\ns_cmp_gt_u32 s25,262144\ns_cbranch_scc1 LPUVR_%=\ns_or_b64 vcc,vcc,s[18:19]\n" \
"s_cbranch_scc1 LPUVL_%=\ns_branch LPUVR_%=\nLEND_%=:\ns_waitcnt vmcnt(0) lgkmcnt(0)\n.purgem M\n.purgem C\n" \
""
constexpr int Q_LDS = 163840;
__global__ __launch_bounds__(256, 1) void fused_kernel(const void* x, const void* k, const void* v, const void* shk, const void* ssmk, const void* ssmD, const void* ckv,
                                                       const void* wq, const void* wuk, const void* wuv, const void* wo, const void* gw, void* out, void* ws) {
    const int w = __builtin_amdgcn_readfirstlane(threadIdx.x >> 6);
    const unsigned long long ka = (unsigned long long)__builtin_amdgcn_kernarg_segment_ptr();
    asm volatile(QKV_ASM_TEXT :: "s"(ka), "s"(blockIdx.x), "s"(w) : QKV_ASM_CLOBBERS);
}

extern "C" void kernel_launch(void* const* d_in, const int* in_sizes, int n_in, void* d_out, int out_size, void* d_ws, size_t ws_size,
                              hipStream_t stream) {
    (void)hipFuncSetAttribute((const void*)fused_kernel, hipFuncAttributeMaxDynamicSharedMemorySize, Q_LDS);
    fused_kernel<<<256, 256, Q_LDS, stream>>>(d_in[0], d_in[1], d_in[2], d_in[3], d_in[4], d_in[5], d_in[7], d_in[8], d_in[9], d_in[10], d_in[11], d_in[12], d_out, d_ws);
}
```

```cpp
#include <hip/hip_runtime.h>
#include <stdint.h>
#define QKV_ASM_CLOBBERS "v0", "v1", "v2", "v3", "v4", "v5", "v6", "v7", "v8", "v9", "v10", "v11", "v12", "v13", "v14", "v15", "v16", "v17", "v18", "v19", "v20", "v21", "v22", "v23", "v24", "v25", "v26", "v27", "v28", "v29", "v30", "v31", "v32", "v33", "v34", "v35", "v36", "v37", "v38", "v39", "v40", "v41", "v42", "v43", "v44", "v45", "v46", "v47", "v48", "v49", "v50", "v51", "v52", "v53", "v54", "v55", "v56", "v57", "v58", "v59", "v60", "v61", "v62", "v63", "v64", "v65", "v66", "v67", "v68", "v69", "v70", "v71", "v72", "v73", "v74", "v75", "v76", "v77", "v78", "v79", "v80", "v81", "v82", "v83", "v84", "v85", "v86", "v87", "v88", "v89", "v90", "v91", "v92", "v93", "v94", "v95", "v96", "v97", "v98", "v99", "v100", "v101", "v102", "v103", "v104", "v105", "v106", "v107", "v108", "v109", "v110", "v111", "v112", "v113", "v114", "v115", "v116", "v117", "v118", "v119", "v120", "v121", "v122", "v123", "v124", "v125", "v126", "v127", "v128", "v129", "v130", "v131", "v132", "v133", "v134", "v135", "v136", "v137", "v138", "v139", "v140", "v141", "v142", "v143", "v144", "v145", "v146", "v147", "v148", "v149", "v150", "v151", "v152", "v153", "v154", "v155", "v156", "v157", "v158", "v159", "v160", "v161", "v162", "v163", "v164", "v165", "v166", "v167", "v168", "v169", "v170", "v171", "v172", "v173", "v174", "v175", "v176", "v177", "v178", "v179", "v180", "v181", "v182", "v183", "v184", "v185", "v186", "v187", "v188", "v189", "v190", "v191", "v192", "v193", "v194", "v195", "v196", "v197", "v198", "v199", "v200", "v201", "v202", "v203", "v204", "v205", "v206", "v207", "v208", "v209", "v210", "v211", "v212", "v213", "v214", "v215", "v216", "v217", "v218", "v219", "v220", "v221", "v222", "v223", "v224", "v225", "v226", "v227", "v228", "v229", "v230", "v231", "v232", "v233", "v234", "v235", "v236", "v237", "v238", "v239", "v240", "v241", "v242", "v243", "v244", "v245", "v246", "v247", "v248", "v249", "v250", "v251", "v252", "v253", "v254", "v255", "a0", "a1", "a2", "a3", "a4", "a5", "a6", "a7", "a8", "a9", "a10", "a11", "a12", "a13", "a14", "a15", "a16", "a17", "a18", "a19", "a20", "a21", "a22", "a23", "a24", "a25", "a26", "a27", "a28", "a29", "a30", "a31", "a32", "a33", "a34", "a35", "a36", "a37", "a38", "a39", "a40", "a41", "a42", "a43", "a44", "a45", "a46", "a47", "a48", "a49", "a50", "a51", "a52", "a53", "a54", "a55", "a56", "a57", "a58", "a59", "a60", "a61", "a62", "a63", "a64", "a65", "a66", "a67", "a68", "a69", "a70", "a71", "a72", "a73", "a74", "a75", "a76", "a77", "a78", "a79", "a80", "a81", "a82", "a83", "a84", "a85", "a86", "a87", "a88", "a89", "a90", "a91", "a92", "a93", "a94", "a95", "a96", "a97", "a98", "a99", "a100", "a101", "a102", "a103", "a104", "a105", "a106", "a107", "a108", "a109", "a110", "a111", "a112", "a113", "a114", "a115", "a116", "a117", "a118", "a119", "a120", "a121", "a122", "a123", "a124", "a125", "a126", "a127", "a128", "a129", "a130", "a131", "a132", "a133", "a134", "a135", "a136", "a137", "a138", "a139", "a140", "a141", "a142", "a143", "a144", "a145", "a146", "a147", "a148", "a149", "a150", "a151", "a152", "a153", "a154", "a155", "a156", "a157", "a158", "a159", "a160", "a161", "a162", "a163", "a164", "a165", "a166", "a167", "a168", "a169", "a170", "a171", "a172", "a173", "a174", "a175", "a176", "a177", "a178", "a179", "a180", "a181", "a182", "a183", "a184", "a185", "a186", "a187", "a188", "a189", "a190", "a191", "a192", "a193", "a194", "a195", "a196", "a197", "a198", "a199", "a200", "a201", "a202", "a203", "a204", "a205", "a206", "a207", "a208", "a209", "a210", "a211", "a212", "a213", "a214", "a215", "a216", "a217", "a218", "a219", "a220", "a221", "a222", "a223", "a224", "a225", "a226", "a227", "a228", "a229", "a230", "a231", "a232", "a233", "a234", "a235", "a236", "a237", "a238", "a239", "a240", "a241", "a242", "a243", "a244", "a245", "a246", "a247", "a248", "a249", "a250", "a251", "a252", "a253", "a254", "a255", "s4", "s5", "s6", "s7", "s8", "s9", "s10", "s11", "s12", "s13", "s14", "s15", "s16", "s17", "s18", "s19", "s20", "s21", "s22", "s23", "s24", "s25", "s26", "s27", "s28", "s29", "s30", "s31", "s32", "s33", "s34", "s35", "s36", "s37", "s38", "s39", "s40", "s41", "s42", "s43", "s44", "s45", "s46", "s47", "s48", "s49", "s50", "s51", "s52", "s53", "s54", "s55", "s56", "s57", "s58", "s59", "s60", "s61", "s62", "s63", "s64", "s65", "s66", "s67", "s68", "s69", "s70", "s71", "s72", "s73", "s74", "s75", "s76", "s77", "s78", "s79", "s80", "s81", "s82", "s83", "s84", "s85", "s86", "s87", "s88", "s89", "s90", "s91", "s92", "s93", "s94", "s95", "s96", "s97", "s98", "s99", "s100", "s101", "vcc", "scc", "memory"
#define QKV_ASM_TEXT \
".macro M d,a,b,c\nv_mfma_f32_16x16x32_bf16 \\d,\\a,\\b,\\c\n.endm\n.macro C d,a,b\nv_cvt_pk_bf16_f32 \\d,\\a,\\b\n.endm\ns_memrealtime s[18:19]\ns_load_dwordx16 s[64:79],%0,0x0\n" \
"s_load_dwordx8 s[80:87],%0,0x40\ns_load_dwordx4 s[88:91],%0,0x60\nv_mbcnt_lo_u32_b32 v23,-1,0\nv_mbcnt_hi_u32_b32 v23,-1,v23\ns_bfe_u32 s7,%1,0x30003\ns_and_b32 s6,%1,7\ns_lshl_b32 s6,s6,2\ns_lshr_b32 s60,%1,6\n" \
"s_add_u32 s6,s6,s60\nv_lshlrev_b32 v4,4,v23\ns_getpc_b64 s[40:41]\nLCTBEG_%=:\ns_add_u32 s40,s40,LBEG_%=-LCTBEG_%=\ns_addc_u32 s41,s41,0\ns_mov_b64 s[100:101],s[40:41]\ns_and_b32 s41,s41,0xffff\n" \
"s_mov_b32 s42,LEND_%=-LBEG_%=\ns_mov_b32 s43,0x00020000\ns_lshr_b32 s9,%1,3\ns_lshl_b32 s9,s9,2\ns_add_u32 s9,s9,%2\ns_and_b32 s9,s9,31\ns_lshl_b32 s9,s9,13\nv_lshlrev_b32 v64,3,v4\n" \
"buffer_load_dword v64,v64,s[40:43],s9 offen\ns_lshl_b32 s60,%2,2\nv_lshrrev_b32 v24,4,v23\nv_add_u32 v25,s60,v24\nv_xor_b32 v25,v25,v23\nv_and_b32 v25,7,v25\ns_lshl_b32 s61,%2,3\nv_lshrrev_b32 v26,3,v23\n" \
"v_add_u32 v26,s61,v26\nv_lshlrev_b32 v26,10,v26\nv_lshl_add_u32 v13,v25,4,v26\nv_add_u32 v63,v13,v26\ns_lshl_b32 s58,s7,17\ns_sub_u32 s59,1966080,s58\nv_add_u32 v17,s59,v13\nv_add_u32 v13,s58,v13\n" \
"v_add_u32 v14,0x8000,v13\nv_add_u32 v18,0x8000,v17\nv_add_u32 v15,0x8000,v14\nv_add_u32 v19,0x8000,v18\nv_add_u32 v16,0x8000,v15\nv_add_u32 v20,0x8000,v19\nv_bfe_u32 v25,v23,2,3\nv_add_u32 v25,s61,v25\n" \
"v_lshlrev_b32 v25,13,v25\ns_lshl_b32 s61,%2,1\nv_and_b32 v26,1,v24\nv_add_u32 v26,s61,v26\nv_xor_b32 v26,v26,v23\nv_and_b32 v26,3,v26\nv_lshrrev_b32 v27,5,v23\nv_lshl_add_u32 v26,v27,2,v26\n" \
"v_lshl_add_u32 v21,v26,4,v25\ns_lshl_b32 s50,%2,10\ns_add_u32 s50,s50,65536\ns_lshl_b32 s22,%2,12\ns_lshl_b32 s51,%2,11\ns_mov_b32 s31,0x00020000\ns_mov_b32 s35,0x00020000\ns_mov_b32 s95,0x00020000\n" \
"s_mov_b32 s30,2097152\ns_lshl_b32 s98,s6,8\ns_sub_u32 s34,4194304,s98\ns_mov_b32 s94,s34\ns_sub_u32 s49,8388608,s98\ns_cmp_lt_u32 %2,2\ns_cselect_b32 s57,s58,s59\ns_lshr_b32 s57,s57,1\n" \
"s_lshl_b32 s60,s6,20\ns_add_u32 s57,s57,s60\ns_and_b32 s54,%2,1\ns_lshl_b32 s60,s54,13\ns_add_u32 s57,s57,s60\ns_add_u32 s57,s57,16777216\ns_bitcmp1_b32 %2,0\ns_cselect_b32 s20,1.0,0x3e0293ee\n" \
"s_lshl_b32 s54,s54,11\ns_lshl_b32 s24,s6,3\ns_add_u32 s24,s24,s7\ns_lshl_b32 s24,s24,3\ns_lshl_b32 s60,%2,27\ns_add_u32 s24,s24,s60\ns_add_u32 s24,s24,94375936\ns_and_b32 s8,%1,7\n" \
"s_lshl_b32 s23,s8,8\ns_add_u32 s23,s23,s60\ns_and_b32 s60,%1,0xf8\ns_add_u32 s23,s23,s60\ns_add_u32 s23,s23,94380032\ns_lshl_b32 s8,s8,5\ns_lshr_b32 s58,%1,3\ns_add_u32 s8,s8,s58\n" \
"v_lshrrev_b32 v23,4,v4\nv_and_b32 v24,15,v23\nv_lshrrev_b32 v25,4,v23\ns_lshr_b32 s58,%2,1\ns_lshl_b32 s58,s58,14\ns_add_u32 s58,s58,65536\nv_lshrrev_b32 v26,1,v24\nv_xor_b32 v26,v26,v25\n" \
"v_lshlrev_b32 v26,4,v26\nv_lshl_add_u32 v26,v24,7,v26\ns_waitcnt lgkmcnt(0)\ns_mov_b64 s[28:29],s[76:77]\ns_and_b32 s29,s29,0xffff\ns_add_u32 s32,s80,s98\ns_addc_u32 s33,s81,0\ns_and_b32 s33,s33,0xffff\n" \
"s_add_u32 s92,s82,s98\ns_addc_u32 s93,s83,0\ns_and_b32 s93,s93,0xffff\ns_mov_b32 s47,0\ns_add_u32 m0,s22,0\nv_add_u32 v5,s58,v26\nbuffer_load_dwordx4 v21,s[32:35],s47 offen offset:0 lds\nv_xor_b32 v6,64,v5\n" \
"v_add_u32 v7,0x10000,v5\nv_add_u32 v8,0x10000,v6\nv_lshrrev_b32 v26,2,v24\ns_mov_b32 s46,0\ns_add_u32 m0,s50,65536\nv_lshlrev_b32 v26,6,v26\nbuffer_load_dwordx4 v13,s[28:31],s46 offen lds\nv_and_b32 v27,1,v23\n" \
"v_lshl_add_u32 v26,v27,3,v26\nv_bfe_u32 v27,v23,1,1\nv_lshlrev_b32 v28,1,v25\ns_add_u32 m0,s22,896\nv_xor_b32 v27,v27,v28\nbuffer_load_dwordx4 v21,s[32:35],s47 offen offset:128 lds\nv_and_b32 v27,3,v27\nv_lshl_add_u32 v26,v27,4,v26\n" \
"v_lshl_add_u32 v9,v25,12,v26\nv_add_u32 v9,s54,v9\ns_add_u32 m0,s50,69632\nv_lshl_add_u32 v62,v25,11,s54\nbuffer_load_dwordx4 v14,s[28:31],s46 offen lds\nv_xor_b32 v10,0x110,v9\nv_xor_b32 v11,32,v9\nv_xor_b32 v12,0x130,v9\n" \
"s_and_b32 s62,%1,7\ns_add_u32 m0,s22,2048\ns_lshl_b32 s62,s62,2\nbuffer_load_dwordx4 v21,s[92:95],s47 offen offset:0 lds\ns_add_u32 s62,s62,%2\ns_lshl_b32 s62,s62,5\ns_and_b32 s63,%1,0xf8\nv_bfe_u32 v24,v4,4,3\n" \
"s_add_u32 m0,s50,73728\nv_lshrrev_b32 v23,7,v4\nbuffer_load_dwordx4 v15,s[28:31],s46 offen lds\nv_lshl_add_u32 v24,v24,2,s62\nv_add_u32 v25,s63,v23\nv_lshlrev_b32 v26,14,v25\nv_lshl_add_u32 v59,v24,1,v26\ns_add_u32 m0,s22,2944\n" \
"v_add_u32 v54,0xffffe800,v59\nbuffer_load_dwordx4 v21,s[92:95],s47 offen offset:128 lds\nv_add_u32 v55,0xfffff800,v59\nv_lshlrev_b32 v60,12,v24\nv_lshl_add_u32 v60,v25,4,v60\nv_lshlrev_b32 v27,3,v24\ns_add_u32 m0,s50,77824\ns_lshl_b32 s62,%1,2\n" \
"buffer_load_dwordx4 v16,s[28:31],s46 offen lds\ns_add_u32 s62,s62,%2\ns_lshl_b32 s63,s62,12\nv_add_u32 v26,s63,v4\ns_lshl_b32 s62,s62,3\ns_mov_b32 s46,0\ns_add_u32 m0,s50,81920\ns_add_u32 s62,s62,71303168\n" \
"buffer_load_dwordx4 v17,s[28:31],s46 offen lds\nv_and_b32 v61,4,v23\nv_add_u32 v61,s62,v61\nv_lshlrev_b32 v2,3,v4\nv_and_b32 v1,0x380,v2\ns_add_u32 m0,s50,86016\nv_lshrrev_b32 v23,7,v4\nbuffer_load_dwordx4 v18,s[28:31],s46 offen lds\n" \
"v_lshl_add_u32 v1,v23,13,v1\ns_lshr_b32 s58,%1,3\ns_lshl_b32 s58,s58,2\ns_add_u32 s58,s58,%2\ns_add_u32 m0,s50,90112\ns_and_b32 s60,%1,7\nbuffer_load_dwordx4 v19,s[28:31],s46 offen lds\ns_lshl_b32 s60,s60,10\n" \
"s_lshl_b32 s59,s58,14\ns_add_u32 s59,s59,s60\ns_mov_b64 s[4:5],s[90:91]\ns_add_u32 m0,s50,94208\ns_lshl_b32 s99,s58,11\nbuffer_load_dwordx4 v20,s[28:31],s46 offen lds\ns_lshl_b32 s96,s60,8\ns_add_u32 s99,s99,s96\n" \
"s_add_u32 s96,s76,s99\ns_addc_u32 s97,s77,0\ns_add_u32 s76,s78,s59\ns_addc_u32 s77,s79,0\ns_mov_b32 s47,262144\ns_add_u32 m0,s22,16384\ns_and_b32 s59,s59,1048575\nbuffer_load_dwordx4 v21,s[32:35],s47 offen offset:0 lds\n" \
"s_cmp_lt_u32 s58,64\ns_cselect_b64 s[84:85],s[80:81],s[82:83]\ns_add_u32 s84,s84,s59\ns_addc_u32 s85,s85,0\ns_lshl_b32 s59,s8,2\ns_add_u32 s59,s59,%2\ns_add_u32 m0,s22,17280\ns_lshl_b32 s59,s59,12\n" \
"buffer_load_dwordx4 v21,s[32:35],s47 offen offset:128 lds\ns_add_u32 s88,s72,s59\ns_addc_u32 s89,s73,0\ns_add_u32 m0,s22,18432\ns_nop 0\nbuffer_load_dwordx4 v21,s[92:95],s47 offen offset:0 lds\ns_add_u32 m0,s22,19328\ns_nop 0\n" \
"buffer_load_dwordx4 v21,s[92:95],s47 offen offset:128 lds\ns_mov_b64 exec,0xff\nglobal_load_dword v0,v1,s[84:85]\ns_mov_b64 exec,-1\ns_mov_b64 exec,0xff00\nglobal_load_dword v0,v1,s[84:85]\ns_mov_b64 exec,-1\ns_mov_b64 exec,0xff\n" \
"global_load_dword v0,v2,s[96:97]\ns_mov_b64 exec,-1\ns_mov_b64 exec,0xff00\nglobal_load_dword v0,v2,s[96:97]\ns_mov_b64 exec,-1\ns_mov_b64 exec,0\ns_cmp_eq_u32 %2,0\ns_cbranch_scc1 LG0_%=\n" \
"s_cmp_eq_u32 %2,1\ns_cbranch_scc1 LG1_%=\ns_cmp_eq_u32 %2,2\ns_cbranch_scc1 LG2_%=\ns_cmp_eq_u32 %2,3\ns_cbranch_scc1 LG3_%=\nLHR0_%=:\ns_mov_b64 exec,-1\n" \
"s_waitcnt vmcnt(8)\ns_barrier\ns_branch LBEG_%=\n.p2align 12\nLBEG_%=:\ns_bitcmp1_b32 %2,0\ns_cbranch_scc1 LAV_%=\nds_read_b64_tr_b16 a[32:33],v9 offset:0\n" \
"ds_read_b64_tr_b16 a[34:35],v10 offset:0\nds_read_b64_tr_b16 a[36:37],v11 offset:0\nds_read_b64_tr_b16 a[38:39],v12 offset:0\nds_read_b128 a[24:27],v7 offset:12288\nds_read_b128 a[28:31],v7 offset:14336\ns_waitcnt lgkmcnt(0)\nM v[128:131],a[32:35],a[24:27],0\ns_add_u32 m0,s22,32768\n" \
"s_mov_b32 s47,524288\ns_mov_b32 s46,128\nds_read_b64_tr_b16 a[40:41],v9 offset:512\nds_read_b64_tr_b16 a[42:43],v10 offset:512\nds_read_b64_tr_b16 a[44:45],v11 offset:512\nds_read_b64_tr_b16 a[46:47],v12 offset:512\nds_read_b64_tr_b16 a[48:49],v9 offset:1024\nds_read_b64_tr_b16 a[50:51],v10 offset:1024\n" \
"ds_read_b64_tr_b16 a[52:53],v11 offset:1024\nds_read_b64_tr_b16 a[54:55],v12 offset:1024\nM v[132:135],a[36:39],a[24:27],0\nbuffer_load_dwordx4 v21,s[32:35],s47 offen offset:0 lds\nds_read_b64_tr_b16 a[56:57],v9 offset:1536\nds_read_b64_tr_b16 a[58:59],v10 offset:1536\nds_read_b64_tr_b16 a[60:61],v11 offset:1536\nds_read_b64_tr_b16 a[62:63],v12 offset:1536\n" \
"M v[140:143],a[36:39],a[28:31],0\ns_add_u32 m0,s22,33664\nM v[136:139],a[32:35],a[28:31],0\ns_waitcnt lgkmcnt(8)\ns_mov_b64 s[40:41],s[66:67]\nds_read_b128 a[16:19],v7 offset:8192\nM v[152:155],a[40:43],a[28:31],0\ns_and_b32 s41,s41,0xffff\n" \
"ds_read_b128 a[20:23],v7 offset:10240\nM v[156:159],a[44:47],a[28:31],0\nbuffer_load_dwordx4 v21,s[32:35],s47 offen offset:128 lds\nM v[148:151],a[44:47],a[24:27],0\ns_add_u32 m0,s22,34816\nds_read_b128 a[0:3],v7 offset:0\nM v[144:147],a[40:43],a[24:27],0\ns_waitcnt lgkmcnt(3)\n" \
"s_mov_b32 s42,4194304\nds_read_b128 a[4:7],v7 offset:2048\nM v[160:163],a[48:51],a[24:27],0\ns_mov_b32 s43,0x00020000\nds_read_b128 a[8:11],v7 offset:4096\nM v[164:167],a[52:55],a[24:27],0\nbuffer_load_dwordx4 v21,s[92:95],s47 offen offset:0 lds\nM v[172:175],a[52:55],a[28:31],0\n" \
"s_add_u32 m0,s22,35712\nM v[168:171],a[48:51],a[28:31],0\ns_mov_b64 s[80:81],s[68:69]\nds_read_b128 a[12:15],v7 offset:6144\nM v[184:187],a[56:59],a[28:31],0\ns_mov_b32 s52,0\nds_read_b128 a[88:91],v8 offset:12288\nM v[188:191],a[60:63],a[28:31],0\n" \
"buffer_load_dwordx4 v21,s[92:95],s47 offen offset:128 lds\nM v[180:183],a[60:63],a[24:27],0\ns_add_u32 m0,s50,0\nds_read_b128 a[92:95],v8 offset:14336\nM v[176:179],a[56:59],a[24:27],0\ns_waitcnt lgkmcnt(3)\ns_mov_b32 s47,786432\nds_read_b128 a[80:83],v8 offset:8192\n" \
"M v[64:67],a[32:35],a[16:19],0\ns_waitcnt vmcnt(8) lgkmcnt(9)\nds_read_b128 a[84:87],v8 offset:10240\nM v[68:71],a[36:39],a[16:19],0\nbuffer_load_dwordx4 v13,s[28:31],s46 offen lds\ns_barrier\nM v[76:79],a[36:39],a[20:23],0\ns_add_u32 m0,s50,4096\n" \
"M v[72:75],a[32:35],a[20:23],0\ns_and_b32 s81,s81,0xffff\nds_read_b64_tr_b16 a[96:97],v9 offset:16384\nds_read_b64_tr_b16 a[98:99],v10 offset:16384\nM v[88:91],a[40:43],a[20:23],0\ns_mov_b32 s82,4194304\nds_read_b64_tr_b16 a[100:101],v11 offset:16384\nM v[92:95],a[44:47],a[20:23],0\n" \
"buffer_load_dwordx4 v14,s[28:31],s46 offen lds\nM v[84:87],a[44:47],a[16:19],0\ns_add_u32 m0,s50,8192\nds_read_b64_tr_b16 a[102:103],v12 offset:16384\nM v[80:83],a[40:43],a[16:19],0\ns_mov_b32 s83,0x00020000\nds_read_b64_tr_b16 a[104:105],v9 offset:16896\nM v[96:99],a[48:51],a[16:19],0\n" \
"s_lshl_b32 s60,s7,18\nds_read_b64_tr_b16 a[106:107],v10 offset:16896\nM v[100:103],a[52:55],a[16:19],0\nbuffer_load_dwordx4 v15,s[28:31],s46 offen lds\nM v[108:111],a[52:55],a[20:23],0\ns_add_u32 m0,s50,12288\nds_read_b64_tr_b16 a[108:109],v11 offset:16896\nM v[104:107],a[48:51],a[20:23],0\n" \
"s_sub_u32 s61,3932160,s60\nds_read_b64_tr_b16 a[110:111],v12 offset:16896\nM v[120:123],a[56:59],a[20:23],0\ns_add_u32 s44,s78,s98\ns_addc_u32 s45,s79,0\nds_read_b64_tr_b16 a[112:113],v9 offset:17408\nds_read_b64_tr_b16 a[114:115],v10 offset:17408\nM v[124:127],a[60:63],a[20:23],0\n" \
"buffer_load_dwordx4 v16,s[28:31],s46 offen lds\nM v[116:119],a[60:63],a[16:19],0\ns_add_u32 m0,s50,16384\ns_mov_b32 s46,128\nM v[112:115],a[56:59],a[16:19],0\ns_and_b32 s45,s45,0xffff\nds_read_b64_tr_b16 a[116:117],v11 offset:17408\nds_read_b64_tr_b16 a[118:119],v12 offset:17408\n" \
"M v[192:195],a[32:35],a[0:3],0\ns_mov_b64 s[36:37],s[90:91]\nds_read_b64_tr_b16 a[120:121],v9 offset:17920\nM v[196:199],a[36:39],a[0:3],0\nbuffer_load_dwordx4 v17,s[28:31],s46 offen lds\nM v[204:207],a[36:39],a[4:7],0\ns_add_u32 m0,s50,20480\nds_read_b64_tr_b16 a[122:123],v10 offset:17920\n" \
"M v[200:203],a[32:35],a[4:7],0\ns_and_b32 s37,s37,0xffff\nds_read_b64_tr_b16 a[124:125],v11 offset:17920\nM v[216:219],a[40:43],a[4:7],0\ns_mov_b32 s38,134217728\nds_read_b64_tr_b16 a[126:127],v12 offset:17920\nM v[220:223],a[44:47],a[4:7],0\nbuffer_load_dwordx4 v18,s[28:31],s46 offen lds\n" \
"M v[212:215],a[44:47],a[0:3],0\ns_add_u32 m0,s50,24576\nds_read_b128 a[64:67],v8 offset:0\nM v[208:211],a[40:43],a[0:3],0\ns_mov_b32 s39,0x00020000\nds_read_b128 a[68:71],v8 offset:2048\nM v[224:227],a[48:51],a[0:3],0\ns_add_u32 s96,s57,32768\n" \
"ds_read_b128 a[72:75],v8 offset:4096\nM v[228:231],a[52:55],a[0:3],0\nbuffer_load_dwordx4 v19,s[28:31],s46 offen lds\nM v[236:239],a[52:55],a[4:7],0\ns_add_u32 m0,s50,28672\nM v[232:235],a[48:51],a[4:7],0\ns_lshr_b32 s62,%1,3\nds_read_b128 a[76:79],v8 offset:6144\n" \
"M v[248:251],a[56:59],a[4:7],0\ns_lshl_b32 s62,s62,2\nM v[252:255],a[60:63],a[4:7],0\nbuffer_load_dwordx4 v20,s[28:31],s46 offen lds\nM v[244:247],a[60:63],a[0:3],0\ns_add_u32 m0,s22,49152\nM v[240:243],a[56:59],a[0:3],0\ns_waitcnt lgkmcnt(1)\n" \
"s_mov_b32 s46,256\nM a[128:131],a[32:35],a[8:11],0\ns_add_u32 s62,s62,%2\nM a[132:135],a[36:39],a[8:11],0\nbuffer_load_dwordx4 v21,s[32:35],s47 offen offset:0 lds\nM a[140:143],a[36:39],a[12:15],0\ns_add_u32 m0,s22,50048\nM a[136:139],a[32:35],a[12:15],0\n" \
"s_lshl_b32 s62,s62,10\nM a[152:155],a[40:43],a[12:15],0\ns_min_u32 s62,s62,LEND_%=-LBEG_%=-1024\nM a[156:159],a[44:47],a[12:15],0\nbuffer_load_dwordx4 v21,s[32:35],s47 offen offset:128 lds\nM a[148:151],a[44:47],a[8:11],0\ns_add_u32 m0,s22,51200\nM a[144:147],a[40:43],a[8:11],0\n" \
"s_add_u32 s100,s100,s62\ns_addc_u32 s101,s101,0\nM a[160:163],a[48:51],a[8:11],0\ns_lshl_b32 s62,s8,2\nM a[164:167],a[52:55],a[8:11],0\nbuffer_load_dwordx4 v21,s[92:95],s47 offen offset:0 lds\nM a[172:175],a[52:55],a[12:15],0\ns_add_u32 m0,s22,52096\n" \
"M a[168:171],a[48:51],a[12:15],0\ns_add_u32 s62,s62,%2\nM a[184:187],a[56:59],a[12:15],0\ns_mov_b64 s[10:11],s[72:73]\nM a[188:191],a[60:63],a[12:15],0\nbuffer_load_dwordx4 v21,s[92:95],s47 offen offset:128 lds\nM a[180:183],a[60:63],a[8:11],0\ns_add_u32 m0,s50,32768\n" \
"M a[176:179],a[56:59],a[8:11],0\ns_mov_b32 s47,1048576\nM v[128:131],a[96:99],a[88:91],v[128:131]\ns_lshl_b32 s63,s62,12\nM v[132:135],a[100:103],a[88:91],v[132:135]\nbuffer_load_dwordx4 v13,s[28:31],s46 offen lds\nM v[140:143],a[100:103],a[92:95],v[140:143]\ns_add_u32 m0,s50,36864\n" \
"M v[136:139],a[96:99],a[92:95],v[136:139]\ns_add_u32 s68,s10,s63\ns_addc_u32 s69,s11,0\nM v[152:155],a[104:107],a[92:95],v[152:155]\ns_and_b32 s69,s69,0xffff\nM v[156:159],a[108:111],a[92:95],v[156:159]\nbuffer_load_dwordx4 v14,s[28:31],s46 offen lds\nM v[148:151],a[108:111],a[88:91],v[148:151]\n" \
"s_add_u32 m0,s50,40960\nM v[144:147],a[104:107],a[88:91],v[144:147]\ns_lshl_b32 s62,s62,1\nM v[160:163],a[112:115],a[88:91],v[160:163]\ns_mov_b64 s[12:13],s[74:75]\nM v[164:167],a[116:119],a[88:91],v[164:167]\nbuffer_load_dwordx4 v15,s[28:31],s46 offen lds\nM v[172:175],a[116:119],a[92:95],v[172:175]\n" \
"s_add_u32 m0,s50,45056\nM v[168:171],a[112:115],a[92:95],v[168:171]\ns_sub_u32 s14,s18,2000\ns_subb_u32 s15,s19,0\nM v[184:187],a[120:123],a[92:95],v[184:187]\ns_add_u32 s16,s14,16777216\ns_addc_u32 s17,s15,0\nM v[188:191],a[124:127],a[92:95],v[188:191]\n" \
"buffer_load_dwordx4 v16,s[28:31],s46 offen lds\nM v[180:183],a[124:127],a[88:91],v[180:183]\ns_add_u32 m0,s50,49152\nM v[176:179],a[120:123],a[88:91],v[176:179]\ns_mov_b32 s46,256\nM v[64:67],a[96:99],a[80:83],v[64:67]\ns_lshr_b32 s21,s63,5\nM v[68:71],a[100:103],a[80:83],v[68:71]\n" \
"buffer_load_dwordx4 v17,s[28:31],s46 offen lds\nM v[76:79],a[100:103],a[84:87],v[76:79]\ns_add_u32 m0,s50,53248\nM v[72:75],a[96:99],a[84:87],v[72:75]\ns_mov_b64 s[4:5],s[90:91]\nM v[88:91],a[104:107],a[84:87],v[88:91]\ns_waitcnt vmcnt(9) lgkmcnt(0)\nM v[92:95],a[108:111],a[84:87],v[92:95]\n" \
"s_add_u32 s63,s63,79691776\ns_barrier\nds_read_b64_tr_b16 a[32:33],v9 offset:32768\nM v[84:87],a[108:111],a[80:83],v[84:87]\nbuffer_load_dwordx4 v18,s[28:31],s46 offen lds\nM v[80:83],a[104:107],a[80:83],v[80:83]\ns_add_u32 m0,s50,57344\nds_read_b64_tr_b16 a[34:35],v10 offset:32768\n" \
"M v[96:99],a[112:115],a[80:83],v[96:99]\ns_add_u32 s72,s4,s63\ns_addc_u32 s73,s5,0\nds_read_b64_tr_b16 a[36:37],v11 offset:32768\nds_read_b64_tr_b16 a[38:39],v12 offset:32768\nM v[100:103],a[116:119],a[80:83],v[100:103]\ns_mov_b32 s63,65536\nds_read_b128 a[24:27],v5 offset:12288\n" \
"M v[108:111],a[116:119],a[84:87],v[108:111]\nbuffer_load_dwordx4 v19,s[28:31],s46 offen lds\nM v[104:107],a[112:115],a[84:87],v[104:107]\ns_add_u32 m0,s50,61440\nM v[120:123],a[120:123],a[84:87],v[120:123]\ns_cmp_lt_u32 %2,2\ns_cselect_b32 s63,32768,s63\nds_read_b128 a[28:31],v5 offset:14336\n" \
"M v[124:127],a[124:127],a[84:87],v[124:127]\ns_lshl_b32 s56,%2,14\nds_read_b64_tr_b16 a[40:41],v9 offset:33280\nds_read_b64_tr_b16 a[42:43],v10 offset:33280\nM v[116:119],a[124:127],a[80:83],v[116:119]\nbuffer_load_dwordx4 v20,s[28:31],s46 offen lds\nM v[112:115],a[120:123],a[80:83],v[112:115]\ns_add_u32 m0,s22,0\n" \
"M v[192:195],a[96:99],a[64:67],v[192:195]\ns_mov_b32 s46,384\nds_read_b64_tr_b16 a[44:45],v11 offset:33280\nds_read_b64_tr_b16 a[46:47],v12 offset:33280\nM v[196:199],a[100:103],a[64:67],v[196:199]\ns_add_u32 s56,s56,s63\nds_read_b64_tr_b16 a[48:49],v9 offset:33792\nM v[204:207],a[100:103],a[68:71],v[204:207]\n" \
"buffer_load_dwordx4 v21,s[32:35],s47 offen offset:0 lds\nM v[200:203],a[96:99],a[68:71],v[200:203]\ns_add_u32 m0,s22,896\nds_read_b64_tr_b16 a[50:51],v10 offset:33792\nM v[216:219],a[104:107],a[68:71],v[216:219]\ns_and_b32 s73,s73,0xffff\nds_read_b64_tr_b16 a[52:53],v11 offset:33792\nM v[220:223],a[108:111],a[68:71],v[220:223]\n" \
"s_mov_b32 s74,4096\nds_read_b64_tr_b16 a[54:55],v12 offset:33792\nM v[212:215],a[108:111],a[64:67],v[212:215]\nbuffer_load_dwordx4 v21,s[32:35],s47 offen offset:128 lds\nM v[208:211],a[104:107],a[64:67],v[208:211]\ns_add_u32 m0,s22,2048\nds_read_b64_tr_b16 a[56:57],v9 offset:34304\nM v[224:227],a[112:115],a[64:67],v[224:227]\n" \
"s_mov_b32 s75,0x00020000\nds_read_b64_tr_b16 a[58:59],v10 offset:34304\nM v[228:231],a[116:119],a[64:67],v[228:231]\ns_add_u32 s84,s84,1048576\ns_addc_u32 s85,s85,0\nds_read_b64_tr_b16 a[60:61],v11 offset:34304\nds_read_b64_tr_b16 a[62:63],v12 offset:34304\nM v[236:239],a[116:119],a[68:71],v[236:239]\n" \
"buffer_load_dwordx4 v21,s[92:95],s47 offen offset:0 lds\nM v[232:235],a[112:115],a[68:71],v[232:235]\ns_add_u32 m0,s22,2944\nM v[248:251],a[120:123],a[68:71],v[248:251]\ns_add_u32 s21,s21,67108864\nds_read_b128 a[16:19],v5 offset:8192\nM v[252:255],a[124:127],a[68:71],v[252:255]\ns_mov_b32 s53,s22\n" \
"ds_read_b128 a[20:23],v5 offset:10240\nM v[244:247],a[124:127],a[64:67],v[244:247]\ns_mov_b64 exec,0xff\nglobal_load_dword v0,v1,s[84:85]\ns_mov_b64 exec,-1\nM v[240:243],a[120:123],a[64:67],v[240:243]\nds_read_b128 a[0:3],v5 offset:0\nds_read_b128 a[4:7],v5 offset:2048\n" \
"M a[128:131],a[96:99],a[72:75],a[128:131]\ns_mov_b64 exec,0xff00\nglobal_load_dword v0,v1,s[84:85]\ns_mov_b64 exec,-1\nM a[132:135],a[100:103],a[72:75],a[132:135]\ns_add_u32 s84,s84,1048576\ns_addc_u32 s85,s85,0\nds_read_b128 a[8:11],v5 offset:4096\n" \
"ds_read_b128 a[12:15],v5 offset:6144\nM a[140:143],a[100:103],a[76:79],a[140:143]\nglobal_load_dwordx4 v[46:49],v4,s[86:87] offset:0\nM a[136:139],a[96:99],a[76:79],a[136:139]\nds_read_b128 a[88:91],v6 offset:12288\nds_read_b128 a[92:95],v6 offset:14336\nM a[152:155],a[104:107],a[76:79],a[152:155]\nds_read_b128 a[64:67],v6 offset:0\n" \
"M a[156:159],a[108:111],a[76:79],a[156:159]\nds_read_b128 a[68:71],v6 offset:2048\nM a[148:151],a[108:111],a[72:75],a[148:151]\nglobal_load_dwordx4 v[50:53],v4,s[86:87] offset:1024\nM a[144:147],a[104:107],a[72:75],a[144:147]\nds_read_b128 a[80:83],v6 offset:8192\nds_read_b128 a[84:87],v6 offset:10240\nM a[160:163],a[112:115],a[72:75],a[160:163]\n" \
"M a[164:167],a[116:119],a[72:75],a[164:167]\nM a[172:175],a[116:119],a[76:79],a[172:175]\nglobal_load_dwordx4 v[30:33],v26,s[64:65] offset:0\nM a[168:171],a[112:115],a[76:79],a[168:171]\nM a[184:187],a[120:123],a[76:79],a[184:187]\nM a[188:191],a[124:127],a[76:79],a[188:191]\nM a[180:183],a[124:127],a[72:75],a[180:183]\nglobal_load_dwordx4 v[34:37],v26,s[64:65] offset:1024\n" \
"M a[176:179],a[120:123],a[72:75],a[176:179]\ns_waitcnt lgkmcnt(0)\nds_read_b128 a[72:75],v6 offset:4096\nds_read_b128 a[76:79],v6 offset:6144\nM v[128:131],a[32:35],a[24:27],v[128:131]\nM v[132:135],a[36:39],a[24:27],v[132:135]\nM v[140:143],a[36:39],a[28:31],v[140:143]\nglobal_load_dwordx4 v[38:41],v26,s[64:65] offset:2048\n" \
"M v[136:139],a[32:35],a[28:31],v[136:139]\nM v[152:155],a[40:43],a[28:31],v[152:155]\nM v[156:159],a[44:47],a[28:31],v[156:159]\nM v[148:151],a[44:47],a[24:27],v[148:151]\nglobal_load_dwordx4 v[42:45],v26,s[64:65] offset:3072\nM v[144:147],a[40:43],a[24:27],v[144:147]\nM v[160:163],a[48:51],a[24:27],v[160:163]\nM v[164:167],a[52:55],a[24:27],v[164:167]\n" \
"M v[172:175],a[52:55],a[28:31],v[172:175]\nbuffer_load_dwordx4 v21,s[92:95],s47 offen offset:128 lds\nM v[168:171],a[48:51],a[28:31],v[168:171]\ns_add_u32 m0,s22,16384\nM v[184:187],a[56:59],a[28:31],v[184:187]\ns_waitcnt vmcnt(20) lgkmcnt(14)\nM v[188:191],a[60:63],a[28:31],v[188:191]\ns_mov_b32 s47,1310720\n" \
"s_barrier\nds_read_b64_tr_b16 a[96:97],v9 offset:49152\nM v[180:183],a[60:63],a[24:27],v[180:183]\nbuffer_load_dwordx4 v21,s[32:35],s47 offen offset:0 lds\nM v[176:179],a[56:59],a[24:27],v[176:179]\ns_add_u32 m0,s22,17280\nds_read_b64_tr_b16 a[98:99],v10 offset:49152\nM v[64:67],a[32:35],a[16:19],v[64:67]\n" \
"ds_read_b64_tr_b16 a[100:101],v11 offset:49152\nds_read_b64_tr_b16 a[102:103],v12 offset:49152\nM v[68:71],a[36:39],a[16:19],v[68:71]\nds_read_b64_tr_b16 a[104:105],v9 offset:49664\nM v[76:79],a[36:39],a[20:23],v[76:79]\nbuffer_load_dwordx4 v21,s[32:35],s47 offen offset:128 lds\nM v[72:75],a[32:35],a[20:23],v[72:75]\ns_add_u32 m0,s22,18432\n" \
"ds_read_b64_tr_b16 a[106:107],v10 offset:49664\nM v[88:91],a[40:43],a[20:23],v[88:91]\nds_read_b64_tr_b16 a[108:109],v11 offset:49664\nM v[92:95],a[44:47],a[20:23],v[92:95]\nds_read_b64_tr_b16 a[110:111],v12 offset:49664\nM v[84:87],a[44:47],a[16:19],v[84:87]\nbuffer_load_dwordx4 v21,s[92:95],s47 offen offset:0 lds\nM v[80:83],a[40:43],a[16:19],v[80:83]\n" \
"s_add_u32 m0,s22,19328\nds_read_b64_tr_b16 a[112:113],v9 offset:50176\nM v[96:99],a[48:51],a[16:19],v[96:99]\nds_read_b64_tr_b16 a[114:115],v10 offset:50176\nM v[100:103],a[52:55],a[16:19],v[100:103]\nds_read_b64_tr_b16 a[116:117],v11 offset:50176\nds_read_b64_tr_b16 a[118:119],v12 offset:50176\nM v[108:111],a[52:55],a[20:23],v[108:111]\n" \
"buffer_load_dwordx4 v21,s[92:95],s47 offen offset:128 lds\nM v[104:107],a[48:51],a[20:23],v[104:107]\ns_add_u32 m0,s50,65536\nM v[120:123],a[56:59],a[20:23],v[120:123]\ns_mov_b32 s47,1572864\nds_read_b64_tr_b16 a[120:121],v9 offset:50688\nds_read_b64_tr_b16 a[122:123],v10 offset:50688\nM v[124:127],a[60:63],a[20:23],v[124:127]\n" \
"ds_read_b64_tr_b16 a[124:125],v11 offset:50688\nM v[116:119],a[60:63],a[16:19],v[116:119]\nds_read_b64_tr_b16 a[126:127],v12 offset:50688\nM v[112:115],a[56:59],a[16:19],v[112:115]\nM v[192:195],a[32:35],a[0:3],v[192:195]\nM v[196:199],a[36:39],a[0:3],v[196:199]\nM v[204:207],a[36:39],a[4:7],v[204:207]\nM v[200:203],a[32:35],a[4:7],v[200:203]\n" \
"M v[216:219],a[40:43],a[4:7],v[216:219]\nbuffer_load_dwordx4 v13,s[28:31],s46 offen lds\nM v[220:223],a[44:47],a[4:7],v[220:223]\ns_add_u32 m0,s50,69632\nM v[212:215],a[44:47],a[0:3],v[212:215]\nM v[208:211],a[40:43],a[0:3],v[208:211]\nM v[224:227],a[48:51],a[0:3],v[224:227]\nbuffer_load_dwordx4 v14,s[28:31],s46 offen lds\n" \
"M v[228:231],a[52:55],a[0:3],v[228:231]\ns_add_u32 m0,s50,73728\nM v[236:239],a[52:55],a[4:7],v[236:239]\nM v[232:235],a[48:51],a[4:7],v[232:235]\nM v[248:251],a[56:59],a[4:7],v[248:251]\nbuffer_load_dwordx4 v15,s[28:31],s46 offen lds\nM v[252:255],a[60:63],a[4:7],v[252:255]\ns_add_u32 m0,s50,77824\n" \
"M v[244:247],a[60:63],a[0:3],v[244:247]\nM v[240:243],a[56:59],a[0:3],v[240:243]\nM a[128:131],a[32:35],a[8:11],a[128:131]\nbuffer_load_dwordx4 v16,s[28:31],s46 offen lds\nM a[132:135],a[36:39],a[8:11],a[132:135]\ns_add_u32 m0,s50,81920\nM a[140:143],a[36:39],a[12:15],a[140:143]\ns_mov_b32 s46,384\n" \
"M a[136:139],a[32:35],a[12:15],a[136:139]\nM a[152:155],a[40:43],a[12:15],a[152:155]\nbuffer_load_dwordx4 v17,s[28:31],s46 offen lds\nM a[156:159],a[44:47],a[12:15],a[156:159]\ns_add_u32 m0,s50,86016\nM a[148:151],a[44:47],a[8:11],a[148:151]\nM a[144:147],a[40:43],a[8:11],a[144:147]\nM a[160:163],a[48:51],a[8:11],a[160:163]\n" \
"buffer_load_dwordx4 v18,s[28:31],s46 offen lds\nM a[164:167],a[52:55],a[8:11],a[164:167]\ns_add_u32 m0,s50,90112\nM a[172:175],a[52:55],a[12:15],a[172:175]\nM a[168:171],a[48:51],a[12:15],a[168:171]\nM a[184:187],a[56:59],a[12:15],a[184:187]\nbuffer_load_dwordx4 v19,s[28:31],s46 offen lds\nM a[188:191],a[60:63],a[12:15],a[188:191]\n" \
"s_add_u32 m0,s50,94208\nM a[180:183],a[60:63],a[8:11],a[180:183]\nM a[176:179],a[56:59],a[8:11],a[176:179]\ns_waitcnt lgkmcnt(0)\nM v[128:131],a[96:99],a[88:91],v[128:131]\nbuffer_load_dwordx4 v20,s[28:31],s46 offen lds\nM v[132:135],a[100:103],a[88:91],v[132:135]\ns_add_u32 m0,s22,32768\n" \
"M v[140:143],a[100:103],a[92:95],v[140:143]\ns_waitcnt vmcnt(12) lgkmcnt(0)\nM v[136:139],a[96:99],a[92:95],v[136:139]\ns_mov_b32 s46,512\ns_barrier\nds_read_b64_tr_b16 a[32:33],v9 offset:0\nM v[152:155],a[104:107],a[92:95],v[152:155]\nbuffer_load_dwordx4 v21,s[32:35],s47 offen offset:0 lds\n" \
"M v[156:159],a[108:111],a[92:95],v[156:159]\ns_add_u32 m0,s22,33664\nds_read_b64_tr_b16 a[34:35],v10 offset:0\nM v[148:151],a[108:111],a[88:91],v[148:151]\nds_read_b64_tr_b16 a[36:37],v11 offset:0\nds_read_b64_tr_b16 a[38:39],v12 offset:0\nM v[144:147],a[104:107],a[88:91],v[144:147]\nds_read_b64_tr_b16 a[40:41],v9 offset:512\n" \
"M v[160:163],a[112:115],a[88:91],v[160:163]\nbuffer_load_dwordx4 v21,s[32:35],s47 offen offset:128 lds\nM v[164:167],a[116:119],a[88:91],v[164:167]\ns_add_u32 m0,s22,34816\nds_read_b64_tr_b16 a[42:43],v10 offset:512\nM v[172:175],a[116:119],a[92:95],v[172:175]\nds_read_b64_tr_b16 a[44:45],v11 offset:512\nM v[168:171],a[112:115],a[92:95],v[168:171]\n" \
"ds_read_b64_tr_b16 a[46:47],v12 offset:512\nM v[184:187],a[120:123],a[92:95],v[184:187]\nbuffer_load_dwordx4 v21,s[92:95],s47 offen offset:0 lds\nM v[188:191],a[124:127],a[92:95],v[188:191]\ns_add_u32 m0,s22,35712\nds_read_b64_tr_b16 a[48:49],v9 offset:1024\nM v[180:183],a[124:127],a[88:91],v[180:183]\nds_read_b64_tr_b16 a[50:51],v10 offset:1024\n" \
"M v[176:179],a[120:123],a[88:91],v[176:179]\nds_read_b64_tr_b16 a[52:53],v11 offset:1024\nds_read_b64_tr_b16 a[54:55],v12 offset:1024\nM v[64:67],a[96:99],a[80:83],v[64:67]\nbuffer_load_dwordx4 v21,s[92:95],s47 offen offset:128 lds\nM v[68:71],a[100:103],a[80:83],v[68:71]\ns_add_u32 m0,s22,49152\nM v[76:79],a[100:103],a[84:87],v[76:79]\n" \
"s_mov_b32 s47,1835008\nds_read_b64_tr_b16 a[56:57],v9 offset:1536\nds_read_b64_tr_b16 a[58:59],v10 offset:1536\nM v[72:75],a[96:99],a[84:87],v[72:75]\nds_read_b64_tr_b16 a[60:61],v11 offset:1536\nM v[88:91],a[104:107],a[84:87],v[88:91]\nbuffer_load_dwordx4 v21,s[32:35],s47 offen offset:0 lds\nM v[92:95],a[108:111],a[84:87],v[92:95]\n" \
"s_add_u32 m0,s22,50048\nds_read_b64_tr_b16 a[62:63],v12 offset:1536\nM v[84:87],a[108:111],a[80:83],v[84:87]\nds_read_b128 a[24:27],v5 offset:45056\nM v[80:83],a[104:107],a[80:83],v[80:83]\nds_read_b128 a[28:31],v5 offset:47104\nM v[96:99],a[112:115],a[80:83],v[96:99]\nbuffer_load_dwordx4 v21,s[32:35],s47 offen offset:128 lds\n" \
"M v[100:103],a[116:119],a[80:83],v[100:103]\ns_add_u32 m0,s22,51200\nM v[108:111],a[116:119],a[84:87],v[108:111]\nds_read_b128 a[16:19],v5 offset:40960\nM v[104:107],a[112:115],a[84:87],v[104:107]\nds_read_b128 a[20:23],v5 offset:43008\nM v[120:123],a[120:123],a[84:87],v[120:123]\nbuffer_load_dwordx4 v21,s[92:95],s47 offen offset:0 lds\n" \
"M v[124:127],a[124:127],a[84:87],v[124:127]\ns_add_u32 m0,s22,52096\nds_read_b128 a[0:3],v5 offset:32768\nM v[116:119],a[124:127],a[80:83],v[116:119]\nds_read_b128 a[4:7],v5 offset:34816\nM v[112:115],a[120:123],a[80:83],v[112:115]\nds_read_b128 a[8:11],v5 offset:36864\nM v[192:195],a[96:99],a[64:67],v[192:195]\n" \
"LG0_%=:\ns_cbranch_execz LG4_%=\nbuffer_load_dwordx4 v21,s[92:95],s47 offen offset:128 lds\nM v[196:199],a[100:103],a[64:67],v[196:199]\ns_add_u32 m0,s50,0\nM v[204:207],a[100:103],a[68:71],v[204:207]\ns_mov_b32 s47,2097152\nds_read_b128 a[12:15],v5 offset:38912\n" \
"M v[200:203],a[96:99],a[68:71],v[200:203]\nds_read_b128 a[80:83],v6 offset:40960\nM v[216:219],a[104:107],a[68:71],v[216:219]\nbuffer_load_dwordx4 v13,s[28:31],s46 offen lds\nM v[220:223],a[108:111],a[68:71],v[220:223]\ns_add_u32 m0,s50,4096\nds_read_b128 a[84:87],v6 offset:43008\nM v[212:215],a[108:111],a[64:67],v[212:215]\n" \
"ds_read_b128 a[88:91],v6 offset:45056\nM v[208:211],a[104:107],a[64:67],v[208:211]\nds_read_b128 a[92:95],v6 offset:47104\nM v[224:227],a[112:115],a[64:67],v[224:227]\nbuffer_load_dwordx4 v14,s[28:31],s46 offen lds\nM v[228:231],a[116:119],a[64:67],v[228:231]\ns_add_u32 m0,s50,8192\nM v[236:239],a[116:119],a[68:71],v[236:239]\n" \
"M v[232:235],a[112:115],a[68:71],v[232:235]\nM v[248:251],a[120:123],a[68:71],v[248:251]\nM v[252:255],a[124:127],a[68:71],v[252:255]\nbuffer_load_dwordx4 v15,s[28:31],s46 offen lds\nM v[244:247],a[124:127],a[64:67],v[244:247]\ns_add_u32 m0,s50,12288\nM v[240:243],a[120:123],a[64:67],v[240:243]\nds_read_b128 a[64:67],v6 offset:32768\n" \
"ds_read_b128 a[68:71],v6 offset:34816\nM a[128:131],a[96:99],a[72:75],a[128:131]\nM a[132:135],a[100:103],a[72:75],a[132:135]\nbuffer_load_dwordx4 v16,s[28:31],s46 offen lds\nM a[140:143],a[100:103],a[76:79],a[140:143]\ns_add_u32 m0,s50,16384\nM a[136:139],a[96:99],a[76:79],a[136:139]\ns_mov_b32 s46,512\n" \
"M a[152:155],a[104:107],a[76:79],a[152:155]\nM a[156:159],a[108:111],a[76:79],a[156:159]\nM a[148:151],a[108:111],a[72:75],a[148:151]\ns_waitcnt vmcnt(20) lgkmcnt(14)\nM a[144:147],a[104:107],a[72:75],a[144:147]\ns_barrier\nds_read_b64_tr_b16 a[96:97],v9 offset:16384\nM a[160:163],a[112:115],a[72:75],a[160:163]\n" \
"buffer_load_dwordx4 v17,s[28:31],s46 offen lds\nM a[164:167],a[116:119],a[72:75],a[164:167]\ns_add_u32 m0,s50,20480\nds_read_b64_tr_b16 a[98:99],v10 offset:16384\nM a[172:175],a[116:119],a[76:79],a[172:175]\nds_read_b64_tr_b16 a[100:101],v11 offset:16384\nds_read_b64_tr_b16 a[102:103],v12 offset:16384\nM a[168:171],a[112:115],a[76:79],a[168:171]\n" \
"ds_read_b64_tr_b16 a[104:105],v9 offset:16896\nM a[184:187],a[120:123],a[76:79],a[184:187]\nbuffer_load_dwordx4 v18,s[28:31],s46 offen lds\nM a[188:191],a[124:127],a[76:79],a[188:191]\ns_add_u32 m0,s50,24576\nds_read_b64_tr_b16 a[106:107],v10 offset:16896\nM a[180:183],a[124:127],a[72:75],a[180:183]\nds_read_b64_tr_b16 a[108:109],v11 offset:16896\n" \
"M a[176:179],a[120:123],a[72:75],a[176:179]\ns_waitcnt lgkmcnt(5)\nds_read_b128 a[72:75],v6 offset:36864\nM v[128:131],a[32:35],a[24:27],v[128:131]\nbuffer_load_dwordx4 v19,s[28:31],s46 offen lds\nM v[132:135],a[36:39],a[24:27],v[132:135]\ns_add_u32 m0,s50,28672\nds_read_b128 a[76:79],v6 offset:38912\n" \
"M v[140:143],a[36:39],a[28:31],v[140:143]\nds_read_b64_tr_b16 a[110:111],v12 offset:16896\nM v[136:139],a[32:35],a[28:31],v[136:139]\nds_read_b64_tr_b16 a[112:113],v9 offset:17408\nM v[152:155],a[40:43],a[28:31],v[152:155]\nbuffer_load_dwordx4 v20,s[28:31],s46 offen lds\nM v[156:159],a[44:47],a[28:31],v[156:159]\ns_add_u32 m0,s22,0\n" \
"ds_read_b64_tr_b16 a[114:115],v10 offset:17408\nM v[148:151],a[44:47],a[24:27],v[148:151]\ns_mov_b32 s46,640\nds_read_b64_tr_b16 a[116:117],v11 offset:17408\nM v[144:147],a[40:43],a[24:27],v[144:147]\nds_read_b64_tr_b16 a[118:119],v12 offset:17408\nM v[160:163],a[48:51],a[24:27],v[160:163]\nbuffer_load_dwordx4 v21,s[32:35],s47 offen offset:0 lds\n" \
"M v[164:167],a[52:55],a[24:27],v[164:167]\ns_add_u32 m0,s22,896\nds_read_b64_tr_b16 a[120:121],v9 offset:17920\nM v[172:175],a[52:55],a[28:31],v[172:175]\nds_read_b64_tr_b16 a[122:123],v10 offset:17920\nM v[168:171],a[48:51],a[28:31],v[168:171]\nds_read_b64_tr_b16 a[124:125],v11 offset:17920\nds_read_b64_tr_b16 a[126:127],v12 offset:17920\n" \
"M v[184:187],a[56:59],a[28:31],v[184:187]\nbuffer_load_dwordx4 v21,s[32:35],s47 offen offset:128 lds\nM v[188:191],a[60:63],a[28:31],v[188:191]\ns_add_u32 m0,s22,2048\nM v[180:183],a[60:63],a[24:27],v[180:183]\nM v[176:179],a[56:59],a[24:27],v[176:179]\nM v[64:67],a[32:35],a[16:19],v[64:67]\nM v[68:71],a[36:39],a[16:19],v[68:71]\n" \
"buffer_load_dwordx4 v21,s[92:95],s47 offen offset:0 lds\nM v[76:79],a[36:39],a[20:23],v[76:79]\ns_add_u32 m0,s22,2944\nM v[72:75],a[32:35],a[20:23],v[72:75]\nM v[88:91],a[40:43],a[20:23],v[88:91]\nM v[92:95],a[44:47],a[20:23],v[92:95]\nbuffer_load_dwordx4 v21,s[92:95],s47 offen offset:128 lds\nM v[84:87],a[44:47],a[16:19],v[84:87]\n" \
"s_add_u32 m0,s22,16384\nM v[80:83],a[40:43],a[16:19],v[80:83]\ns_mov_b32 s47,2359296\nM v[96:99],a[48:51],a[16:19],v[96:99]\nM v[100:103],a[52:55],a[16:19],v[100:103]\ns_mov_b64 exec,0xff\nglobal_load_dword v0,v1,s[84:85]\ns_mov_b64 exec,-1\n" \
"M v[108:111],a[52:55],a[20:23],v[108:111]\nM v[104:107],a[48:51],a[20:23],v[104:107]\ns_mov_b64 exec,0xff00\nglobal_load_dword v0,v1,s[84:85]\ns_mov_b64 exec,-1\nM v[120:123],a[56:59],a[20:23],v[120:123]\ns_add_u32 s84,s84,1048576\ns_addc_u32 s85,s85,0\n" \
"M v[124:127],a[60:63],a[20:23],v[124:127]\nglobal_load_dwordx4 a[230:233],v27,s[70:71] offset:0\nM v[116:119],a[60:63],a[16:19],v[116:119]\nM v[112:115],a[56:59],a[16:19],v[112:115]\nM v[192:195],a[32:35],a[0:3],v[192:195]\nM v[196:199],a[36:39],a[0:3],v[196:199]\nglobal_load_dwordx4 a[234:237],v27,s[70:71] offset:16\nM v[204:207],a[36:39],a[4:7],v[204:207]\n" \
"s_mov_b32 s70,4096\nM v[200:203],a[32:35],a[4:7],v[200:203]\ns_mov_b32 s71,0x00020000\nM v[216:219],a[40:43],a[4:7],v[216:219]\nM v[220:223],a[44:47],a[4:7],v[220:223]\nbuffer_load_dwordx2 a[192:193],v54,s[40:43],0 offen offset:0 nt\nM v[212:215],a[44:47],a[0:3],v[212:215]\nM v[208:211],a[40:43],a[0:3],v[208:211]\n" \
"M v[224:227],a[48:51],a[0:3],v[224:227]\nM v[228:231],a[52:55],a[0:3],v[228:231]\nbuffer_load_dwordx2 a[194:195],v54,s[40:43],0 offen offset:2048 nt\nM v[236:239],a[52:55],a[4:7],v[236:239]\nM v[232:235],a[48:51],a[4:7],v[232:235]\nM v[248:251],a[56:59],a[4:7],v[248:251]\nM v[252:255],a[60:63],a[4:7],v[252:255]\nbuffer_load_dwordx2 a[196:197],v55,s[40:43],0 offen offset:0 nt\n" \
"M v[244:247],a[60:63],a[0:3],v[244:247]\nM v[240:243],a[56:59],a[0:3],v[240:243]\nM a[128:131],a[32:35],a[8:11],a[128:131]\ns_waitcnt vmcnt(40)\nv_lshlrev_b32 v25,16,v46\nv_lshlrev_b32 v26,16,v30\nM a[132:135],a[36:39],a[8:11],a[132:135]\nbuffer_load_dwordx2 a[198:199],v59,s[40:43],s52 offen offset:0 nt\n" \
"M a[140:143],a[36:39],a[12:15],a[140:143]\nv_and_b32 v46,0xffff0000,v46\nv_mul_f32 v23,v26,v25\nM a[136:139],a[32:35],a[12:15],a[136:139]\nv_and_b32 v30,0xffff0000,v30\nv_lshlrev_b32 v26,16,v38\nM a[152:155],a[40:43],a[12:15],a[152:155]\nv_fmac_f32 v23,v30,v46\n" \
"v_mul_f32 v24,v26,v25\nM a[156:159],a[44:47],a[12:15],a[156:159]\nbuffer_load_dwordx2 a[214:215],v59,s[80:83],s52 offen offset:0 nt\nM a[148:151],a[44:47],a[8:11],a[148:151]\ns_waitcnt vmcnt(25) lgkmcnt(0)\nv_lshlrev_b32 v25,16,v47\nv_lshlrev_b32 v26,16,v31\nM a[144:147],a[40:43],a[8:11],a[144:147]\n" \
"s_barrier\nds_read_b64_tr_b16 a[32:33],v9 offset:32768\nM a[160:163],a[48:51],a[8:11],a[160:163]\nds_read_b64_tr_b16 a[34:35],v10 offset:32768\nds_read_b64_tr_b16 a[36:37],v11 offset:32768\nM a[164:167],a[52:55],a[8:11],a[164:167]\nbuffer_load_dwordx2 a[200:201],v59,s[40:43],s52 offen offset:2048 nt\nM a[172:175],a[52:55],a[12:15],a[172:175]\n" \
"ds_read_b64_tr_b16 a[38:39],v12 offset:32768\nds_read_b64_tr_b16 a[40:41],v9 offset:33280\nM a[168:171],a[48:51],a[12:15],a[168:171]\nds_read_b64_tr_b16 a[42:43],v10 offset:33280\nv_and_b32 v38,0xffff0000,v38\nM a[184:187],a[56:59],a[12:15],a[184:187]\nds_read_b64_tr_b16 a[44:45],v11 offset:33280\nds_read_b64_tr_b16 a[46:47],v12 offset:33280\n" \
"M a[188:191],a[60:63],a[12:15],a[188:191]\nbuffer_load_dwordx2 a[216:217],v59,s[80:83],s52 offen offset:2048 nt\nM a[180:183],a[60:63],a[8:11],a[180:183]\ns_mov_b32 s52,4096\nds_read_b64_tr_b16 a[48:49],v9 offset:33792\nds_read_b64_tr_b16 a[50:51],v10 offset:33792\nM a[176:179],a[56:59],a[8:11],a[176:179]\nds_read_b64_tr_b16 a[52:53],v11 offset:33792\n" \
"v_fmac_f32 v23,v26,v25\nM v[128:131],a[96:99],a[88:91],v[128:131]\nds_read_b64_tr_b16 a[54:55],v12 offset:33792\nds_read_b64_tr_b16 a[56:57],v9 offset:34304\nM v[132:135],a[100:103],a[88:91],v[132:135]\nbuffer_load_dwordx2 a[202:203],v59,s[40:43],s52 offen offset:0 nt\nM v[140:143],a[100:103],a[92:95],v[140:143]\nds_read_b64_tr_b16 a[58:59],v10 offset:34304\n" \
"ds_read_b64_tr_b16 a[60:61],v11 offset:34304\nM v[136:139],a[96:99],a[92:95],v[136:139]\nds_read_b64_tr_b16 a[62:63],v12 offset:34304\nv_fmac_f32 v24,v38,v46\nM v[152:155],a[104:107],a[92:95],v[152:155]\nds_read_b128 a[24:27],v7 offset:12288\nv_and_b32 v47,0xffff0000,v47\nM v[156:159],a[108:111],a[92:95],v[156:159]\n" \
"buffer_load_dwordx2 a[218:219],v59,s[80:83],s52 offen offset:0 nt\nM v[148:151],a[108:111],a[88:91],v[148:151]\nds_read_b128 a[28:31],v7 offset:14336\nds_read_b128 a[16:19],v7 offset:8192\nM v[144:147],a[104:107],a[88:91],v[144:147]\nds_read_b128 a[20:23],v7 offset:10240\nv_and_b32 v31,0xffff0000,v31\nM v[160:163],a[112:115],a[88:91],v[160:163]\n" \
"ds_read_b128 a[0:3],v7 offset:0\nv_lshlrev_b32 v26,16,v39\nM v[164:167],a[116:119],a[88:91],v[164:167]\nbuffer_load_dwordx2 a[204:205],v59,s[40:43],s52 offen offset:2048 nt\nM v[172:175],a[116:119],a[92:95],v[172:175]\nds_read_b128 a[4:7],v7 offset:2048\nds_read_b128 a[8:11],v7 offset:4096\nM v[168:171],a[112:115],a[92:95],v[168:171]\n" \
"ds_read_b128 a[12:15],v7 offset:6144\nv_fmac_f32 v23,v31,v47\nM v[184:187],a[120:123],a[92:95],v[184:187]\nv_fmac_f32 v24,v26,v25\nv_lshlrev_b32 v25,16,v48\nM v[188:191],a[124:127],a[92:95],v[188:191]\nbuffer_load_dwordx2 a[220:221],v59,s[80:83],s52 offen offset:2048 nt\nM v[180:183],a[124:127],a[88:91],v[180:183]\n" \
"s_mov_b32 s52,8192\nv_lshlrev_b32 v26,16,v32\nv_and_b32 v39,0xffff0000,v39\nM v[176:179],a[120:123],a[88:91],v[176:179]\nds_read_b128 a[88:91],v8 offset:12288\nds_read_b128 a[92:95],v8 offset:14336\nM v[64:67],a[96:99],a[80:83],v[64:67]\nv_fmac_f32 v23,v26,v25\n" \
"v_fmac_f32 v24,v39,v47\nM v[68:71],a[100:103],a[80:83],v[68:71]\nv_and_b32 v48,0xffff0000,v48\nv_and_b32 v32,0xffff0000,v32\nM v[76:79],a[100:103],a[84:87],v[76:79]\nv_lshlrev_b32 v26,16,v40\nv_fmac_f32 v23,v32,v48\nM v[72:75],a[96:99],a[84:87],v[72:75]\n" \
"v_fmac_f32 v24,v26,v25\nv_lshlrev_b32 v25,16,v49\nM v[88:91],a[104:107],a[84:87],v[88:91]\nv_lshlrev_b32 v26,16,v33\nv_and_b32 v40,0xffff0000,v40\nM v[92:95],a[108:111],a[84:87],v[92:95]\nv_fmac_f32 v23,v26,v25\nv_fmac_f32 v24,v40,v48\n" \
"M v[84:87],a[108:111],a[80:83],v[84:87]\nv_and_b32 v49,0xffff0000,v49\nv_and_b32 v33,0xffff0000,v33\nM v[80:83],a[104:107],a[80:83],v[80:83]\nv_lshlrev_b32 v26,16,v41\nv_fmac_f32 v23,v33,v49\nM v[96:99],a[112:115],a[80:83],v[96:99]\nv_fmac_f32 v24,v26,v25\n" \
"v_lshlrev_b32 v25,16,v50\nM v[100:103],a[116:119],a[80:83],v[100:103]\nv_lshlrev_b32 v26,16,v34\nv_and_b32 v41,0xffff0000,v41\nM v[108:111],a[116:119],a[84:87],v[108:111]\nv_fmac_f32 v23,v26,v25\nv_fmac_f32 v24,v41,v49\nM v[104:107],a[112:115],a[84:87],v[104:107]\n" \
"v_and_b32 v50,0xffff0000,v50\nv_and_b32 v34,0xffff0000,v34\nM v[120:123],a[120:123],a[84:87],v[120:123]\nv_lshlrev_b32 v26,16,v42\nv_fmac_f32 v23,v34,v50\nM v[124:127],a[124:127],a[84:87],v[124:127]\nv_fmac_f32 v24,v26,v25\nv_lshlrev_b32 v25,16,v51\n" \
"M v[116:119],a[124:127],a[80:83],v[116:119]\nv_lshlrev_b32 v26,16,v35\nv_and_b32 v42,0xffff0000,v42\nM v[112:115],a[120:123],a[80:83],v[112:115]\nds_read_b128 a[80:83],v8 offset:8192\nds_read_b128 a[84:87],v8 offset:10240\nM v[192:195],a[96:99],a[64:67],v[192:195]\nv_fmac_f32 v23,v26,v25\n" \
"v_fmac_f32 v24,v42,v50\nM v[196:199],a[100:103],a[64:67],v[196:199]\nv_and_b32 v51,0xffff0000,v51\nv_and_b32 v35,0xffff0000,v35\nM v[204:207],a[100:103],a[68:71],v[204:207]\nv_lshlrev_b32 v26,16,v43\nv_fmac_f32 v23,v35,v51\nM v[200:203],a[96:99],a[68:71],v[200:203]\n" \
"v_fmac_f32 v24,v26,v25\nv_lshlrev_b32 v25,16,v52\nM v[216:219],a[104:107],a[68:71],v[216:219]\nv_lshlrev_b32 v26,16,v36\nv_and_b32 v43,0xffff0000,v43\nM v[220:223],a[108:111],a[68:71],v[220:223]\nv_fmac_f32 v23,v26,v25\nv_fmac_f32 v24,v43,v51\n" \
"M v[212:215],a[108:111],a[64:67],v[212:215]\nv_and_b32 v52,0xffff0000,v52\nv_and_b32 v36,0xffff0000,v36\nM v[208:211],a[104:107],a[64:67],v[208:211]\nv_lshlrev_b32 v26,16,v44\nv_fmac_f32 v23,v36,v52\nM v[224:227],a[112:115],a[64:67],v[224:227]\nv_fmac_f32 v24,v26,v25\n" \
"v_lshlrev_b32 v25,16,v53\nM v[228:231],a[116:119],a[64:67],v[228:231]\nv_lshlrev_b32 v26,16,v37\nv_and_b32 v44,0xffff0000,v44\nM v[236:239],a[116:119],a[68:71],v[236:239]\nv_fmac_f32 v23,v26,v25\nv_fmac_f32 v24,v44,v52\nM v[232:235],a[112:115],a[68:71],v[232:235]\n" \
"v_lshlrev_b32 v26,16,v45\nv_and_b32 v53,0xffff0000,v53\nM v[248:251],a[120:123],a[68:71],v[248:251]\nv_and_b32 v45,0xffff0000,v45\nv_fmac_f32 v24,v26,v25\nM v[252:255],a[124:127],a[68:71],v[252:255]\nv_fmac_f32 v24,v45,v53\nv_and_b32 v37,0xffff0000,v37\n" \
"M v[244:247],a[124:127],a[64:67],v[244:247]\nv_fmac_f32 v23,v37,v53\nM v[240:243],a[120:123],a[64:67],v[240:243]\nds_read_b128 a[64:67],v8 offset:0\nds_read_b128 a[68:71],v8 offset:2048\nM a[128:131],a[96:99],a[72:75],a[128:131]\nv_permlane32_swap_b32 v23,v24\nM a[132:135],a[100:103],a[72:75],a[132:135]\n" \
"v_add_f32 v23,v23,v24\nv_mov_b32 v24,v23\nM a[140:143],a[100:103],a[76:79],a[140:143]\ns_nop 0\nv_permlane16_swap_b32 v23,v24\nM a[136:139],a[96:99],a[76:79],a[136:139]\nv_add_f32 v23,v23,v24\ns_nop 1\n" \
"v_add_f32 v23,v23,v23 row_ror:8 row_mask:0xf bank_mask:0xf\nM a[152:155],a[104:107],a[76:79],a[152:155]\ns_nop 0\nv_add_f32 v23,v23,v23 row_ror:4 row_mask:0xf bank_mask:0xf\ns_nop 1\nv_add_f32 v23,v23,v23 row_ror:2 row_mask:0xf bank_mask:0xf\nM a[156:159],a[108:111],a[76:79],a[156:159]\ns_nop 0\n" \
"v_add_f32 v23,v23,v23 row_ror:1 row_mask:0xf bank_mask:0xf\nC v23,v23,v23\nM a[148:151],a[108:111],a[72:75],a[148:151]\ns_waitcnt vmcnt(27) lgkmcnt(14)\nv_and_b32 v23,0xffff0000,v23\nv_mul_f32 v23,0xbfb8aa3b,v23\nM a[144:147],a[104:107],a[72:75],a[144:147]\ns_barrier\n" \
"ds_read_b64_tr_b16 a[96:97],v9 offset:49152\nM a[160:163],a[112:115],a[72:75],a[160:163]\nds_read_b64_tr_b16 a[98:99],v10 offset:49152\nds_read_b64_tr_b16 a[100:101],v11 offset:49152\nM a[164:167],a[116:119],a[72:75],a[164:167]\nds_read_b64_tr_b16 a[102:103],v12 offset:49152\nv_add_u32 v24,992,v4\nM a[172:175],a[116:119],a[76:79],a[172:175]\n" \
"ds_read_b64_tr_b16 a[104:105],v9 offset:49664\nds_read_b64_tr_b16 a[106:107],v10 offset:49664\nM a[168:171],a[112:115],a[76:79],a[168:171]\nds_read_b64_tr_b16 a[108:109],v11 offset:49664\nM a[184:187],a[120:123],a[76:79],a[184:187]\nds_read_b64_tr_b16 a[110:111],v12 offset:49664\nM a[188:191],a[124:127],a[76:79],a[188:191]\nds_read_b64_tr_b16 a[112:113],v9 offset:50176\n" \
"ds_read_b64_tr_b16 a[114:115],v10 offset:50176\nM a[180:183],a[124:127],a[72:75],a[180:183]\nds_read_b64_tr_b16 a[116:117],v11 offset:50176\nM a[176:179],a[120:123],a[72:75],a[176:179]\ns_waitcnt lgkmcnt(7)\nds_read_b128 a[72:75],v8 offset:4096\nM v[128:131],a[32:35],a[24:27],v[128:131]\nds_read_b128 a[76:79],v8 offset:6144\n" \
"M v[132:135],a[36:39],a[24:27],v[132:135]\nbuffer_load_dwordx4 v21,s[32:35],s47 offen offset:0 lds\nM v[140:143],a[36:39],a[28:31],v[140:143]\ns_add_u32 m0,s22,17280\nds_read_b64_tr_b16 a[118:119],v12 offset:50176\nM v[136:139],a[32:35],a[28:31],v[136:139]\nds_read_b64_tr_b16 a[120:121],v9 offset:50688\nM v[152:155],a[40:43],a[28:31],v[152:155]\n" \
"ds_read_b64_tr_b16 a[122:123],v10 offset:50688\nM v[156:159],a[44:47],a[28:31],v[156:159]\nbuffer_load_dwordx4 v21,s[32:35],s47 offen offset:128 lds\nM v[148:151],a[44:47],a[24:27],v[148:151]\ns_add_u32 m0,s22,18432\nds_read_b64_tr_b16 a[124:125],v11 offset:50688\nM v[144:147],a[40:43],a[24:27],v[144:147]\nds_read_b64_tr_b16 a[126:127],v12 offset:50688\n" \
"M v[160:163],a[48:51],a[24:27],v[160:163]\nv_exp_f32 v23,v23\nM v[164:167],a[52:55],a[24:27],v[164:167]\nbuffer_load_dwordx4 v21,s[92:95],s47 offen offset:0 lds\nM v[172:175],a[52:55],a[28:31],v[172:175]\ns_add_u32 m0,s22,19328\nv_add_f32 v23,1.0,v23\nM v[168:171],a[48:51],a[28:31],v[168:171]\n" \
"v_rcp_f32 v23,v23\nM v[184:187],a[56:59],a[28:31],v[184:187]\nC v23,v23,v23\nv_and_b32 v23,0xffff0000,v23\nM v[188:191],a[60:63],a[28:31],v[188:191]\nbuffer_load_dwordx4 v21,s[92:95],s47 offen offset:128 lds\nM v[180:183],a[60:63],a[24:27],v[180:183]\ns_add_u32 m0,s50,32768\n" \
"M v[176:179],a[56:59],a[24:27],v[176:179]\ns_mov_b32 s47,2621440\nM v[64:67],a[32:35],a[16:19],v[64:67]\nM v[68:71],a[36:39],a[16:19],v[68:71]\nbuffer_load_dwordx4 v13,s[28:31],s46 offen lds\nM v[76:79],a[36:39],a[20:23],v[76:79]\ns_add_u32 m0,s50,36864\nM v[72:75],a[32:35],a[20:23],v[72:75]\n" \
"M v[88:91],a[40:43],a[20:23],v[88:91]\nM v[92:95],a[44:47],a[20:23],v[92:95]\nbuffer_load_dwordx4 v14,s[28:31],s46 offen lds\nM v[84:87],a[44:47],a[16:19],v[84:87]\ns_add_u32 m0,s50,40960\nM v[80:83],a[40:43],a[16:19],v[80:83]\nM v[96:99],a[48:51],a[16:19],v[96:99]\nM v[100:103],a[52:55],a[16:19],v[100:103]\n" \
"buffer_load_dwordx4 v15,s[28:31],s46 offen lds\nM v[108:111],a[52:55],a[20:23],v[108:111]\ns_add_u32 m0,s50,45056\nM v[104:107],a[48:51],a[20:23],v[104:107]\nM v[120:123],a[56:59],a[20:23],v[120:123]\nM v[124:127],a[60:63],a[20:23],v[124:127]\nbuffer_load_dwordx4 v16,s[28:31],s46 offen lds\nM v[116:119],a[60:63],a[16:19],v[116:119]\n" \
"s_add_u32 m0,s50,49152\nM v[112:115],a[56:59],a[16:19],v[112:115]\ns_mov_b32 s46,640\nM v[192:195],a[32:35],a[0:3],v[192:195]\nM v[196:199],a[36:39],a[0:3],v[196:199]\nbuffer_load_dwordx4 v17,s[28:31],s46 offen lds\nM v[204:207],a[36:39],a[4:7],v[204:207]\ns_add_u32 m0,s50,53248\n" \
"M v[200:203],a[32:35],a[4:7],v[200:203]\nM v[216:219],a[40:43],a[4:7],v[216:219]\nM v[220:223],a[44:47],a[4:7],v[220:223]\nbuffer_load_dwordx4 v18,s[28:31],s46 offen lds\nM v[212:215],a[44:47],a[0:3],v[212:215]\ns_add_u32 m0,s50,57344\nM v[208:211],a[40:43],a[0:3],v[208:211]\nM v[224:227],a[48:51],a[0:3],v[224:227]\n" \
"M v[228:231],a[52:55],a[0:3],v[228:231]\nbuffer_load_dwordx4 v19,s[28:31],s46 offen lds\nM v[236:239],a[52:55],a[4:7],v[236:239]\ns_add_u32 m0,s50,61440\nM v[232:235],a[48:51],a[4:7],v[232:235]\nM v[248:251],a[56:59],a[4:7],v[248:251]\nM v[252:255],a[60:63],a[4:7],v[252:255]\nbuffer_load_dwordx4 v20,s[28:31],s46 offen lds\n" \
"M v[244:247],a[60:63],a[0:3],v[244:247]\ns_add_u32 m0,s22,32768\nM v[240:243],a[56:59],a[0:3],v[240:243]\ns_mov_b32 s46,768\nM a[128:131],a[32:35],a[8:11],a[128:131]\nM a[132:135],a[36:39],a[8:11],a[132:135]\nbuffer_load_dwordx4 v21,s[32:35],s47 offen offset:0 lds\nM a[140:143],a[36:39],a[12:15],a[140:143]\n" \
"s_add_u32 m0,s22,33664\nM a[136:139],a[32:35],a[12:15],a[136:139]\nM a[152:155],a[40:43],a[12:15],a[152:155]\nM a[156:159],a[44:47],a[12:15],a[156:159]\nbuffer_load_dwordx4 v21,s[32:35],s47 offen offset:128 lds\nM a[148:151],a[44:47],a[8:11],a[148:151]\ns_waitcnt vmcnt(29) lgkmcnt(0)\nM a[144:147],a[40:43],a[8:11],a[144:147]\n" \
"s_add_u32 m0,s22,34816\ns_barrier\nds_read_b64_tr_b16 a[32:33],v9 offset:0\nM a[160:163],a[48:51],a[8:11],a[160:163]\nds_read_b64_tr_b16 a[34:35],v10 offset:0\nds_read_b64_tr_b16 a[36:37],v11 offset:0\nM a[164:167],a[52:55],a[8:11],a[164:167]\nbuffer_load_dwordx4 v21,s[92:95],s47 offen offset:0 lds\n" \
"M a[172:175],a[52:55],a[12:15],a[172:175]\ns_add_u32 m0,s22,35712\nds_read_b64_tr_b16 a[38:39],v12 offset:0\nM a[168:171],a[48:51],a[12:15],a[168:171]\nds_read_b64_tr_b16 a[40:41],v9 offset:512\nM a[184:187],a[56:59],a[12:15],a[184:187]\nds_read_b64_tr_b16 a[42:43],v10 offset:512\nM a[188:191],a[60:63],a[12:15],a[188:191]\n" \
"buffer_load_dwordx4 v21,s[92:95],s47 offen offset:128 lds\nM a[180:183],a[60:63],a[8:11],a[180:183]\ns_add_u32 m0,s22,49152\nds_read_b64_tr_b16 a[44:45],v11 offset:512\nM a[176:179],a[56:59],a[8:11],a[176:179]\ns_mov_b32 s47,2883584\nds_read_b64_tr_b16 a[46:47],v12 offset:512\nM v[128:131],a[96:99],a[88:91],v[128:131]\n" \
"ds_read_b64_tr_b16 a[48:49],v9 offset:1024\nds_read_b64_tr_b16 a[50:51],v10 offset:1024\nM v[132:135],a[100:103],a[88:91],v[132:135]\nbuffer_load_dwordx4 v21,s[32:35],s47 offen offset:0 lds\nM v[140:143],a[100:103],a[92:95],v[140:143]\ns_add_u32 m0,s22,50048\nM v[136:139],a[96:99],a[92:95],v[136:139]\nds_read_b64_tr_b16 a[52:53],v11 offset:1024\n" \
"ds_read_b64_tr_b16 a[54:55],v12 offset:1024\nM v[152:155],a[104:107],a[92:95],v[152:155]\nds_read_b64_tr_b16 a[56:57],v9 offset:1536\nM v[156:159],a[108:111],a[92:95],v[156:159]\nbuffer_load_dwordx4 v21,s[32:35],s47 offen offset:128 lds\nM v[148:151],a[108:111],a[88:91],v[148:151]\ns_add_u32 m0,s22,51200\nds_read_b64_tr_b16 a[58:59],v10 offset:1536\n" \
"M v[144:147],a[104:107],a[88:91],v[144:147]\nds_read_b64_tr_b16 a[60:61],v11 offset:1536\nM v[160:163],a[112:115],a[88:91],v[160:163]\nds_read_b64_tr_b16 a[62:63],v12 offset:1536\nM v[164:167],a[116:119],a[88:91],v[164:167]\nbuffer_load_dwordx4 v21,s[92:95],s47 offen offset:0 lds\nM v[172:175],a[116:119],a[92:95],v[172:175]\ns_add_u32 m0,s22,52096\n" \
"ds_read_b128 a[24:27],v5 offset:12288\nM v[168:171],a[112:115],a[92:95],v[168:171]\nds_read_b128 a[28:31],v5 offset:14336\nM v[184:187],a[120:123],a[92:95],v[184:187]\nds_read_b128 a[16:19],v5 offset:8192\nM v[188:191],a[124:127],a[92:95],v[188:191]\nbuffer_store_dword v23,v61,s[36:39],0 offen sc1\nM v[180:183],a[124:127],a[88:91],v[180:183]\n" \
"ds_read_b128 a[20:23],v5 offset:10240\nds_read_b128 a[0:3],v5 offset:0\nM v[176:179],a[120:123],a[88:91],v[176:179]\nbuffer_load_dwordx4 v21,s[92:95],s47 offen offset:128 lds\nM v[64:67],a[96:99],a[80:83],v[64:67]\ns_add_u32 m0,s50,65536\nv_add_u32 v23,-32,v4\nM v[68:71],a[100:103],a[80:83],v[68:71]\n" \
"s_mov_b32 s47,3145728\nds_read_b128 a[4:7],v5 offset:2048\nM v[76:79],a[100:103],a[84:87],v[76:79]\nds_read_b128 a[8:11],v5 offset:4096\nM v[72:75],a[96:99],a[84:87],v[72:75]\nLG1_%=:\ns_cbranch_execz LG5_%=\nds_read_b128 a[12:15],v5 offset:6144\n" \
"M v[88:91],a[104:107],a[84:87],v[88:91]\nbuffer_load_dwordx4 v13,s[28:31],s46 offen lds\nM v[92:95],a[108:111],a[84:87],v[92:95]\ns_add_u32 m0,s50,69632\nds_read_b128 a[88:91],v6 offset:12288\nM v[84:87],a[108:111],a[80:83],v[84:87]\nds_read_b128 a[92:95],v6 offset:14336\nM v[80:83],a[104:107],a[80:83],v[80:83]\n" \
"M v[96:99],a[112:115],a[80:83],v[96:99]\nbuffer_load_dwordx4 v14,s[28:31],s46 offen lds\nM v[100:103],a[116:119],a[80:83],v[100:103]\ns_add_u32 m0,s50,73728\nM v[108:111],a[116:119],a[84:87],v[108:111]\nM v[104:107],a[112:115],a[84:87],v[104:107]\nM v[120:123],a[120:123],a[84:87],v[120:123]\nM v[124:127],a[124:127],a[84:87],v[124:127]\n" \
"buffer_load_dwordx4 v15,s[28:31],s46 offen lds\nM v[116:119],a[124:127],a[80:83],v[116:119]\ns_add_u32 m0,s50,77824\nM v[112:115],a[120:123],a[80:83],v[112:115]\nds_read_b128 a[80:83],v6 offset:8192\nds_read_b128 a[84:87],v6 offset:10240\nM v[192:195],a[96:99],a[64:67],v[192:195]\nM v[196:199],a[100:103],a[64:67],v[196:199]\n" \
"buffer_load_dwordx4 v16,s[28:31],s46 offen lds\nM v[204:207],a[100:103],a[68:71],v[204:207]\ns_add_u32 m0,s50,81920\nM v[200:203],a[96:99],a[68:71],v[200:203]\ns_mov_b32 s46,768\nM v[216:219],a[104:107],a[68:71],v[216:219]\nM v[220:223],a[108:111],a[68:71],v[220:223]\nM v[212:215],a[108:111],a[64:67],v[212:215]\n" \
"buffer_load_dwordx2 a[206:207],v59,s[40:43],s52 offen offset:0 nt\nM v[208:211],a[104:107],a[64:67],v[208:211]\nM v[224:227],a[112:115],a[64:67],v[224:227]\nM v[228:231],a[116:119],a[64:67],v[228:231]\nM v[236:239],a[116:119],a[68:71],v[236:239]\nbuffer_load_dwordx2 a[222:223],v59,s[80:83],s52 offen offset:0 nt\nM v[232:235],a[112:115],a[68:71],v[232:235]\nM v[248:251],a[120:123],a[68:71],v[248:251]\n" \
"M v[252:255],a[124:127],a[68:71],v[252:255]\nM v[244:247],a[124:127],a[64:67],v[244:247]\nbuffer_load_dwordx2 a[208:209],v59,s[40:43],s52 offen offset:2048 nt\nM v[240:243],a[120:123],a[64:67],v[240:243]\nds_read_b128 a[64:67],v6 offset:0\nds_read_b128 a[68:71],v6 offset:2048\nM a[128:131],a[96:99],a[72:75],a[128:131]\nM a[132:135],a[100:103],a[72:75],a[132:135]\n" \
"M a[140:143],a[100:103],a[76:79],a[140:143]\nbuffer_load_dwordx2 a[224:225],v59,s[80:83],s52 offen offset:2048 nt\nM a[136:139],a[96:99],a[76:79],a[136:139]\ns_mov_b32 s52,12288\nM a[152:155],a[104:107],a[76:79],a[152:155]\nM a[156:159],a[108:111],a[76:79],a[156:159]\nM a[148:151],a[108:111],a[72:75],a[148:151]\ns_waitcnt vmcnt(25) lgkmcnt(14)\n" \
"M a[144:147],a[104:107],a[72:75],a[144:147]\ns_waitcnt vmcnt(29)\ns_barrier\nds_read_b64_tr_b16 a[96:97],v9 offset:16384\nM a[160:163],a[112:115],a[72:75],a[160:163]\nbuffer_load_dwordx2 a[210:211],v59,s[40:43],s52 offen offset:0 nt\nM a[164:167],a[116:119],a[72:75],a[164:167]\nds_read_b64_tr_b16 a[98:99],v10 offset:16384\n" \
"ds_read_b64_tr_b16 a[100:101],v11 offset:16384\nM a[172:175],a[116:119],a[76:79],a[172:175]\nds_read_b64_tr_b16 a[102:103],v12 offset:16384\nv_accvgpr_read_b32 v45,a231\nM a[168:171],a[112:115],a[76:79],a[168:171]\nds_read_b64_tr_b16 a[104:105],v9 offset:16896\nds_read_b64_tr_b16 a[106:107],v10 offset:16896\nM a[184:187],a[120:123],a[76:79],a[184:187]\n" \
"buffer_load_dwordx2 a[226:227],v59,s[80:83],s52 offen offset:0 nt\nM a[188:191],a[124:127],a[76:79],a[188:191]\nds_read_b64_tr_b16 a[108:109],v11 offset:16896\nds_read_b64_tr_b16 a[110:111],v12 offset:16896\nM a[180:183],a[124:127],a[72:75],a[180:183]\nds_read_b64_tr_b16 a[112:113],v9 offset:17408\nv_accvgpr_read_b32 v49,a233\nM a[176:179],a[120:123],a[72:75],a[176:179]\n" \
"s_waitcnt lgkmcnt(6)\nds_read_b128 a[72:75],v6 offset:4096\nv_lshlrev_b32 v44,16,v45\nM v[128:131],a[32:35],a[24:27],v[128:131]\nbuffer_load_dwordx2 a[212:213],v59,s[40:43],s52 offen offset:2048 nt\nM v[132:135],a[36:39],a[24:27],v[132:135]\nds_read_b128 a[76:79],v6 offset:6144\nds_read_b64_tr_b16 a[114:115],v10 offset:17408\n" \
"M v[140:143],a[36:39],a[28:31],v[140:143]\nds_read_b64_tr_b16 a[116:117],v11 offset:17408\nv_accvgpr_read_b32 v36,a192\nM v[136:139],a[32:35],a[28:31],v[136:139]\nds_read_b64_tr_b16 a[118:119],v12 offset:17408\nv_accvgpr_read_b32 v43,a230\nM v[152:155],a[40:43],a[28:31],v[152:155]\nbuffer_load_dwordx2 a[228:229],v59,s[80:83],s52 offen offset:2048 nt\n" \
"M v[156:159],a[44:47],a[28:31],v[156:159]\nds_read_b64_tr_b16 a[120:121],v9 offset:17920\nds_read_b64_tr_b16 a[122:123],v10 offset:17920\nM v[148:151],a[44:47],a[24:27],v[148:151]\nds_read_b64_tr_b16 a[124:125],v11 offset:17920\nv_accvgpr_read_b32 v37,a194\nM v[144:147],a[40:43],a[24:27],v[144:147]\nds_read_b64_tr_b16 a[126:127],v12 offset:17920\n" \
"v_lshlrev_b32 v48,16,v49\nM v[160:163],a[48:51],a[24:27],v[160:163]\ns_mov_b64 exec,0xff\nglobal_load_dword v0,v1,s[84:85]\ns_mov_b64 exec,-1\nM v[164:167],a[52:55],a[24:27],v[164:167]\nv_lshlrev_b32 v30,16,v36\nv_and_b32 v45,0xffff0000,v45\n" \
"M v[172:175],a[52:55],a[28:31],v[172:175]\ns_mov_b64 exec,0xff00\nglobal_load_dword v0,v1,s[84:85]\ns_mov_b64 exec,-1\nM v[168:171],a[48:51],a[28:31],v[168:171]\nv_accvgpr_read_b32 v47,a232\nv_lshlrev_b32 v31,16,v37\nM v[184:187],a[56:59],a[28:31],v[184:187]\n" \
"s_mov_b64 exec,0xff\nglobal_load_dword v0,v1,s[76:77]\ns_mov_b64 exec,-1\nM v[188:191],a[60:63],a[28:31],v[188:191]\nv_accvgpr_read_b32 v38,a196\nv_lshlrev_b32 v42,16,v43\nM v[180:183],a[60:63],a[24:27],v[180:183]\ns_mov_b64 exec,0xff00\n" \
"global_load_dword v0,v1,s[76:77]\ns_mov_b64 exec,-1\nM v[176:179],a[56:59],a[24:27],v[176:179]\ns_add_u32 s76,s76,2097152\ns_addc_u32 s77,s77,0\nv_and_b32 v49,0xffff0000,v49\nv_and_b32 v36,0xffff0000,v36\nM v[64:67],a[32:35],a[16:19],v[64:67]\n" \
"v_accvgpr_read_b32 v39,a198\nv_mul_f32 v25,v30,v45\nM v[68:71],a[36:39],a[16:19],v[68:71]\nv_lshlrev_b32 v46,16,v47\nv_and_b32 v37,0xffff0000,v37\nM v[76:79],a[36:39],a[20:23],v[76:79]\nv_lshlrev_b32 v32,16,v38\nv_and_b32 v43,0xffff0000,v43\n" \
"M v[72:75],a[32:35],a[20:23],v[72:75]\nv_mul_f32 v26,v36,v49\nv_lshlrev_b32 v33,16,v39\nM v[88:91],a[40:43],a[20:23],v[88:91]\nv_fmac_f32 v25,v31,v44\nv_and_b32 v47,0xffff0000,v47\nM v[92:95],a[44:47],a[20:23],v[92:95]\nv_and_b32 v38,0xffff0000,v38\n" \
"v_fmac_f32 v26,v37,v48\nM v[84:87],a[44:47],a[16:19],v[84:87]\nv_and_b32 v39,0xffff0000,v39\nv_fmac_f32 v25,v32,v43\nM v[80:83],a[40:43],a[16:19],v[80:83]\nv_fmac_f32 v26,v38,v47\nv_fmac_f32 v25,v33,v42\nM v[96:99],a[48:51],a[16:19],v[96:99]\n" \
"v_fmac_f32 v26,v39,v46\nv_accvgpr_read_b32 v28,a214\nM v[100:103],a[52:55],a[16:19],v[100:103]\nC v26,v25,v26\nv_lshlrev_b32 v27,16,v28\nM v[108:111],a[52:55],a[20:23],v[108:111]\nv_lshlrev_b32 v25,16,v26\nv_and_b32 v28,0xffff0000,v28\n" \
"M v[104:107],a[48:51],a[20:23],v[104:107]\nv_and_b32 v26,0xffff0000,v26\nv_mul_f32 v29,v25,v27\nM v[120:123],a[56:59],a[20:23],v[120:123]\nv_mul_f32 v58,v26,v28\nv_accvgpr_read_b32 v40,a200\nM v[124:127],a[60:63],a[20:23],v[124:127]\nv_mul_f32 v25,v31,v45\n" \
"v_mul_f32 v26,v37,v49\nM v[116:119],a[60:63],a[16:19],v[116:119]\nv_lshlrev_b32 v34,16,v40\nv_fmac_f32 v25,v32,v44\nM v[112:115],a[56:59],a[16:19],v[112:115]\nv_fmac_f32 v26,v38,v48\nv_fmac_f32 v25,v33,v43\nM v[192:195],a[32:35],a[0:3],v[192:195]\n" \
"v_and_b32 v40,0xffff0000,v40\nv_fmac_f32 v26,v39,v47\nM v[196:199],a[36:39],a[0:3],v[196:199]\nv_fmac_f32 v25,v34,v42\nv_fmac_f32 v26,v40,v46\nM v[204:207],a[36:39],a[4:7],v[204:207]\nv_accvgpr_read_b32 v28,a216\nC v26,v25,v26\n" \
"M v[200:203],a[32:35],a[4:7],v[200:203]\nv_lshlrev_b32 v27,16,v28\nv_lshlrev_b32 v25,16,v26\nM v[216:219],a[40:43],a[4:7],v[216:219]\nv_and_b32 v28,0xffff0000,v28\nv_and_b32 v26,0xffff0000,v26\nM v[220:223],a[44:47],a[4:7],v[220:223]\nv_mul_f32 v25,v25,v27\n" \
"v_mul_f32 v26,v26,v28\nM v[212:215],a[44:47],a[0:3],v[212:215]\nC v53,v25,v29\nC v57,v26,v58\nM v[208:211],a[40:43],a[0:3],v[208:211]\nv_accvgpr_read_b32 v41,a202\nv_lshlrev_b32 v35,16,v41\nM v[224:227],a[48:51],a[0:3],v[224:227]\n" \
"v_mul_f32 v25,v32,v45\nv_mul_f32 v26,v38,v49\nM v[228:231],a[52:55],a[0:3],v[228:231]\nv_fmac_f32 v25,v33,v44\nv_and_b32 v41,0xffff0000,v41\nM v[236:239],a[52:55],a[4:7],v[236:239]\nv_fmac_f32 v26,v39,v48\nv_fmac_f32 v25,v34,v43\n" \
"M v[232:235],a[48:51],a[4:7],v[232:235]\nv_fmac_f32 v26,v40,v47\nv_fmac_f32 v25,v35,v42\nM v[248:251],a[56:59],a[4:7],v[248:251]\nv_fmac_f32 v26,v41,v46\nv_accvgpr_read_b32 v28,a218\nM v[252:255],a[60:63],a[4:7],v[252:255]\nC v26,v25,v26\n" \
"v_lshlrev_b32 v27,16,v28\nM v[244:247],a[60:63],a[0:3],v[244:247]\nv_lshlrev_b32 v25,16,v26\nv_and_b32 v28,0xffff0000,v28\nM v[240:243],a[56:59],a[0:3],v[240:243]\nv_and_b32 v26,0xffff0000,v26\nv_mul_f32 v29,v25,v27\nM a[128:131],a[32:35],a[8:11],a[128:131]\n" \
"v_mul_f32 v58,v26,v28\nv_accvgpr_read_b32 v36,a204\nM a[132:135],a[36:39],a[8:11],a[132:135]\nv_mul_f32 v25,v33,v45\nv_lshlrev_b32 v30,16,v36\nM a[140:143],a[36:39],a[12:15],a[140:143]\nv_mul_f32 v26,v39,v49\nv_fmac_f32 v25,v34,v44\n" \
"M a[136:139],a[32:35],a[12:15],a[136:139]\nv_fmac_f32 v26,v40,v48\nv_and_b32 v36,0xffff0000,v36\nM a[152:155],a[40:43],a[12:15],a[152:155]\nbuffer_load_dwordx4 v17,s[28:31],s46 offen lds\nM a[156:159],a[44:47],a[12:15],a[156:159]\ns_add_u32 m0,s50,86016\nv_fmac_f32 v25,v35,v43\n" \
"v_fmac_f32 v26,v41,v47\nM a[148:151],a[44:47],a[8:11],a[148:151]\ns_waitcnt vmcnt(22) lgkmcnt(0)\nv_fmac_f32 v25,v30,v42\nv_fmac_f32 v26,v36,v46\nM a[144:147],a[40:43],a[8:11],a[144:147]\ns_barrier\nds_read_b64_tr_b16 a[32:33],v9 offset:32768\n" \
"M a[160:163],a[48:51],a[8:11],a[160:163]\nbuffer_load_dwordx4 v18,s[28:31],s46 offen lds\nM a[164:167],a[52:55],a[8:11],a[164:167]\ns_add_u32 m0,s50,90112\nds_read_b64_tr_b16 a[34:35],v10 offset:32768\nv_accvgpr_read_b32 v28,a220\nM a[172:175],a[52:55],a[12:15],a[172:175]\nds_read_b64_tr_b16 a[36:37],v11 offset:32768\n" \
"ds_read_b64_tr_b16 a[38:39],v12 offset:32768\nM a[168:171],a[48:51],a[12:15],a[168:171]\nds_read_b64_tr_b16 a[40:41],v9 offset:33280\nC v26,v25,v26\nM a[184:187],a[56:59],a[12:15],a[184:187]\nbuffer_load_dwordx4 v19,s[28:31],s46 offen lds\nM a[188:191],a[60:63],a[12:15],a[188:191]\ns_add_u32 m0,s50,94208\n" \
"ds_read_b64_tr_b16 a[42:43],v10 offset:33280\nv_lshlrev_b32 v27,16,v28\nM a[180:183],a[60:63],a[8:11],a[180:183]\nds_read_b64_tr_b16 a[44:45],v11 offset:33280\nv_lshlrev_b32 v25,16,v26\nM a[176:179],a[56:59],a[8:11],a[176:179]\nds_read_b64_tr_b16 a[46:47],v12 offset:33280\nv_and_b32 v28,0xffff0000,v28\n" \
"M v[128:131],a[96:99],a[88:91],v[128:131]\nbuffer_load_dwordx4 v20,s[28:31],s46 offen lds\nM v[132:135],a[100:103],a[88:91],v[132:135]\ns_add_u32 m0,s22,0\nds_read_b64_tr_b16 a[48:49],v9 offset:33792\nv_and_b32 v26,0xffff0000,v26\nM v[140:143],a[100:103],a[92:95],v[140:143]\ns_mov_b32 s46,896\n" \
"ds_read_b64_tr_b16 a[50:51],v10 offset:33792\nv_mul_f32 v25,v25,v27\nM v[136:139],a[96:99],a[92:95],v[136:139]\nds_read_b64_tr_b16 a[52:53],v11 offset:33792\nds_read_b64_tr_b16 a[54:55],v12 offset:33792\nM v[152:155],a[104:107],a[92:95],v[152:155]\nbuffer_load_dwordx4 v21,s[32:35],s47 offen offset:0 lds\nM v[156:159],a[108:111],a[92:95],v[156:159]\n" \
"s_add_u32 m0,s22,896\nv_mul_f32 v26,v26,v28\nC v52,v25,v29\nM v[148:151],a[108:111],a[88:91],v[148:151]\nds_read_b64_tr_b16 a[56:57],v9 offset:34304\nds_read_b64_tr_b16 a[58:59],v10 offset:34304\nM v[144:147],a[104:107],a[88:91],v[144:147]\nds_read_b64_tr_b16 a[60:61],v11 offset:34304\n" \
"C v56,v26,v58\nM v[160:163],a[112:115],a[88:91],v[160:163]\nbuffer_load_dwordx4 v21,s[32:35],s47 offen offset:128 lds\nM v[164:167],a[116:119],a[88:91],v[164:167]\ns_add_u32 m0,s22,2048\nds_read_b64_tr_b16 a[62:63],v12 offset:34304\nv_mov_b32 v59,s19\nM v[172:175],a[116:119],a[92:95],v[172:175]\n" \
"ds_read_b128 a[24:27],v5 offset:45056\nM v[168:171],a[112:115],a[92:95],v[168:171]\nds_read_b128 a[28:31],v5 offset:47104\nM v[184:187],a[120:123],a[92:95],v[184:187]\nbuffer_load_dwordx4 v21,s[92:95],s47 offen offset:0 lds\nM v[188:191],a[124:127],a[92:95],v[188:191]\ns_add_u32 m0,s22,2944\nM v[180:183],a[124:127],a[88:91],v[180:183]\n" \
"ds_read_b128 a[16:19],v5 offset:40960\nM v[176:179],a[120:123],a[88:91],v[176:179]\nds_read_b128 a[20:23],v5 offset:43008\nM v[64:67],a[96:99],a[80:83],v[64:67]\nbuffer_load_dwordx4 v21,s[92:95],s47 offen offset:128 lds\nM v[68:71],a[100:103],a[80:83],v[68:71]\ns_add_u32 m0,s22,16384\nds_read_b128 a[0:3],v5 offset:32768\n" \
"M v[76:79],a[100:103],a[84:87],v[76:79]\ns_mov_b32 s47,3407872\nds_read_b128 a[4:7],v5 offset:34816\nM v[72:75],a[96:99],a[84:87],v[72:75]\nds_read_b128 a[8:11],v5 offset:36864\nM v[88:91],a[104:107],a[84:87],v[88:91]\nbuffer_load_dwordx4 v21,s[32:35],s47 offen offset:0 lds\nM v[92:95],a[108:111],a[84:87],v[92:95]\n" \
"s_add_u32 m0,s22,17280\nM v[84:87],a[108:111],a[80:83],v[84:87]\nds_read_b128 a[12:15],v5 offset:38912\nM v[80:83],a[104:107],a[80:83],v[80:83]\nds_read_b128 a[88:91],v6 offset:45056\nM v[96:99],a[112:115],a[80:83],v[96:99]\nbuffer_load_dwordx4 v21,s[32:35],s47 offen offset:128 lds\nM v[100:103],a[116:119],a[80:83],v[100:103]\n" \
"s_add_u32 m0,s22,18432\nds_read_b128 a[92:95],v6 offset:47104\nM v[108:111],a[116:119],a[84:87],v[108:111]\nM v[104:107],a[112:115],a[84:87],v[104:107]\nM v[120:123],a[120:123],a[84:87],v[120:123]\nbuffer_load_dwordx4 v21,s[92:95],s47 offen offset:0 lds\nM v[124:127],a[124:127],a[84:87],v[124:127]\ns_add_u32 m0,s22,19328\n" \
"M v[116:119],a[124:127],a[80:83],v[116:119]\nM v[112:115],a[120:123],a[80:83],v[112:115]\nds_read_b128 a[80:83],v6 offset:40960\nds_read_b128 a[84:87],v6 offset:43008\nM v[192:195],a[96:99],a[64:67],v[192:195]\nbuffer_load_dwordx4 v21,s[92:95],s47 offen offset:128 lds\nM v[196:199],a[100:103],a[64:67],v[196:199]\ns_add_u32 m0,s50,0\n" \
"M v[204:207],a[100:103],a[68:71],v[204:207]\ns_mov_b32 s47,3670016\nM v[200:203],a[96:99],a[68:71],v[200:203]\nM v[216:219],a[104:107],a[68:71],v[216:219]\nbuffer_load_dwordx4 v13,s[28:31],s46 offen lds\nM v[220:223],a[108:111],a[68:71],v[220:223]\ns_add_u32 m0,s50,4096\nv_add_u32 v13,s60,v63\n" \
"M v[212:215],a[108:111],a[64:67],v[212:215]\nM v[208:211],a[104:107],a[64:67],v[208:211]\nM v[224:227],a[112:115],a[64:67],v[224:227]\nbuffer_load_dwordx4 v14,s[28:31],s46 offen lds\nM v[228:231],a[116:119],a[64:67],v[228:231]\ns_add_u32 m0,s50,8192\nv_add_u32 v14,0x10000,v13\nM v[236:239],a[116:119],a[68:71],v[236:239]\n" \
"M v[232:235],a[112:115],a[68:71],v[232:235]\nM v[248:251],a[120:123],a[68:71],v[248:251]\nbuffer_load_dwordx4 v15,s[28:31],s46 offen lds\nM v[252:255],a[124:127],a[68:71],v[252:255]\ns_add_u32 m0,s50,12288\nv_add_u32 v15,0x10000,v14\nM v[244:247],a[124:127],a[64:67],v[244:247]\nM v[240:243],a[120:123],a[64:67],v[240:243]\n" \
"ds_read_b128 a[64:67],v6 offset:32768\nds_read_b128 a[68:71],v6 offset:34816\nM a[128:131],a[96:99],a[72:75],a[128:131]\nbuffer_load_dwordx4 v16,s[28:31],s46 offen lds\nM a[132:135],a[100:103],a[72:75],a[132:135]\ns_add_u32 m0,s50,16384\nv_add_u32 v16,0x10000,v15\nM a[140:143],a[100:103],a[76:79],a[140:143]\n" \
"s_mov_b32 s46,896\nM a[136:139],a[96:99],a[76:79],a[136:139]\nM a[152:155],a[104:107],a[76:79],a[152:155]\nbuffer_load_dwordx4 v17,s[28:31],s46 offen lds\nM a[156:159],a[108:111],a[76:79],a[156:159]\ns_add_u32 m0,s50,20480\nv_add_u32 v17,s61,v63\nv_mov_b32 v63,0x3f803f80\n" \
"M a[148:151],a[108:111],a[72:75],a[148:151]\ns_waitcnt vmcnt(33) lgkmcnt(14)\nM a[144:147],a[104:107],a[72:75],a[144:147]\ns_barrier\nds_read_b64_tr_b16 a[96:97],v9 offset:49152\nM a[160:163],a[112:115],a[72:75],a[160:163]\nds_read_b64_tr_b16 a[98:99],v10 offset:49152\nds_read_b64_tr_b16 a[100:101],v11 offset:49152\n" \
"M a[164:167],a[116:119],a[72:75],a[164:167]\nbuffer_load_dwordx4 v18,s[28:31],s46 offen lds\nM a[172:175],a[116:119],a[76:79],a[172:175]\ns_add_u32 m0,s50,24576\nds_read_b64_tr_b16 a[102:103],v12 offset:49152\nv_add_u32 v18,0x10000,v17\nM a[168:171],a[112:115],a[76:79],a[168:171]\nds_read_b64_tr_b16 a[104:105],v9 offset:49664\n" \
"M a[184:187],a[120:123],a[76:79],a[184:187]\nds_read_b64_tr_b16 a[106:107],v10 offset:49664\nM a[188:191],a[124:127],a[76:79],a[188:191]\nbuffer_load_dwordx4 v19,s[28:31],s46 offen lds\nM a[180:183],a[124:127],a[72:75],a[180:183]\ns_add_u32 m0,s50,28672\nds_read_b64_tr_b16 a[108:109],v11 offset:49664\nv_add_u32 v19,0x10000,v18\n" \
"M a[176:179],a[120:123],a[72:75],a[176:179]\ns_waitcnt lgkmcnt(4)\nds_read_b128 a[72:75],v6 offset:36864\nM v[128:131],a[32:35],a[24:27],v[128:131]\nds_read_b128 a[76:79],v6 offset:38912\nM v[132:135],a[36:39],a[24:27],v[132:135]\nds_read_b64_tr_b16 a[110:111],v12 offset:49664\nM v[140:143],a[36:39],a[28:31],v[140:143]\n" \
"buffer_load_dwordx4 v20,s[28:31],s46 offen lds\nM v[136:139],a[32:35],a[28:31],v[136:139]\ns_add_u32 m0,s22,32768\nds_read_b64_tr_b16 a[112:113],v9 offset:50176\nv_add_u32 v20,0x10000,v19\nM v[152:155],a[40:43],a[28:31],v[152:155]\ns_mov_b64 s[28:29],s[64:65]\nds_read_b64_tr_b16 a[114:115],v10 offset:50176\n" \
"M v[156:159],a[44:47],a[28:31],v[156:159]\ns_and_b32 s29,s29,0xffff\nds_read_b64_tr_b16 a[116:117],v11 offset:50176\nds_read_b64_tr_b16 a[118:119],v12 offset:50176\nM v[148:151],a[44:47],a[24:27],v[148:151]\nbuffer_load_dwordx4 v21,s[32:35],s47 offen offset:0 lds\nM v[144:147],a[40:43],a[24:27],v[144:147]\ns_add_u32 m0,s22,33664\n" \
"M v[160:163],a[48:51],a[24:27],v[160:163]\ns_mov_b32 s30,4194304\nds_read_b64_tr_b16 a[120:121],v9 offset:50688\nds_read_b64_tr_b16 a[122:123],v10 offset:50688\nM v[164:167],a[52:55],a[24:27],v[164:167]\ns_mov_b32 s46,0\nds_read_b64_tr_b16 a[124:125],v11 offset:50688\nM v[172:175],a[52:55],a[28:31],v[172:175]\n" \
"ds_read_b64_tr_b16 a[126:127],v12 offset:50688\nM v[168:171],a[48:51],a[28:31],v[168:171]\nbuffer_load_dwordx4 v21,s[32:35],s47 offen offset:128 lds\nM v[184:187],a[56:59],a[28:31],v[184:187]\ns_add_u32 m0,s22,34816\nM v[188:191],a[60:63],a[28:31],v[188:191]\nM v[180:183],a[60:63],a[24:27],v[180:183]\nM v[176:179],a[56:59],a[24:27],v[176:179]\n" \
"buffer_load_dwordx4 v21,s[92:95],s47 offen offset:0 lds\nM v[64:67],a[32:35],a[16:19],v[64:67]\ns_add_u32 m0,s22,35712\nM v[68:71],a[36:39],a[16:19],v[68:71]\nM v[76:79],a[36:39],a[20:23],v[76:79]\nM v[72:75],a[32:35],a[20:23],v[72:75]\nM v[88:91],a[40:43],a[20:23],v[88:91]\nbuffer_load_dwordx4 v21,s[92:95],s47 offen offset:128 lds\n" \
"M v[92:95],a[44:47],a[20:23],v[92:95]\ns_add_u32 m0,s22,49152\nM v[84:87],a[44:47],a[16:19],v[84:87]\ns_mov_b32 s47,3932160\nM v[80:83],a[40:43],a[16:19],v[80:83]\nM v[96:99],a[48:51],a[16:19],v[96:99]\nM v[100:103],a[52:55],a[16:19],v[100:103]\nM v[108:111],a[52:55],a[20:23],v[108:111]\n" \
"M v[104:107],a[48:51],a[20:23],v[104:107]\nM v[120:123],a[56:59],a[20:23],v[120:123]\nM v[124:127],a[60:63],a[20:23],v[124:127]\nM v[116:119],a[60:63],a[16:19],v[116:119]\nM v[112:115],a[56:59],a[16:19],v[112:115]\nM v[192:195],a[32:35],a[0:3],v[192:195]\ns_mov_b64 exec,0xff\nglobal_load_dword v0,v2,s[88:89]\n" \
"s_mov_b64 exec,-1\nM v[196:199],a[36:39],a[0:3],v[196:199]\nM v[204:207],a[36:39],a[4:7],v[204:207]\ns_mov_b64 exec,0xff00\nglobal_load_dword v0,v2,s[88:89]\ns_mov_b64 exec,-1\nM v[200:203],a[32:35],a[4:7],v[200:203]\ns_add_u32 s88,s88,2048\n" \
"s_addc_u32 s89,s89,0\nM v[216:219],a[40:43],a[4:7],v[216:219]\ns_mov_b64 exec,0xff\nglobal_load_dword v0,v2,s[88:89]\ns_mov_b64 exec,-1\nM v[220:223],a[44:47],a[4:7],v[220:223]\nM v[212:215],a[44:47],a[0:3],v[212:215]\ns_mov_b64 exec,0xff00\n" \
"global_load_dword v0,v2,s[88:89]\ns_mov_b64 exec,-1\nM v[208:211],a[40:43],a[0:3],v[208:211]\nM v[224:227],a[48:51],a[0:3],v[224:227]\ns_mov_b64 exec,0xff\nglobal_load_dword v0,v1,s[76:77]\ns_mov_b64 exec,-1\nM v[228:231],a[52:55],a[0:3],v[228:231]\n" \
"M v[236:239],a[52:55],a[4:7],v[236:239]\ns_mov_b64 exec,0xff00\nglobal_load_dword v0,v1,s[76:77]\ns_mov_b64 exec,-1\nM v[232:235],a[48:51],a[4:7],v[232:235]\ns_add_u32 s76,s76,2097152\ns_addc_u32 s77,s77,0\nM v[248:251],a[56:59],a[4:7],v[248:251]\n" \
"M v[252:255],a[60:63],a[4:7],v[252:255]\nM v[244:247],a[60:63],a[0:3],v[244:247]\ns_waitcnt vmcnt(34)\nv_accvgpr_read_b32 v37,a206\nv_mul_f32 v25,v34,v45\nM v[240:243],a[56:59],a[0:3],v[240:243]\ns_mov_b32 s98,79691776\nv_mul_f32 v26,v40,v49\n" \
"v_lshlrev_b32 v31,16,v37\nM a[128:131],a[32:35],a[8:11],a[128:131]\ns_mov_b32 s99,79695872\nv_fmac_f32 v25,v35,v44\nv_fmac_f32 v26,v41,v48\nM a[132:135],a[36:39],a[8:11],a[132:135]\nv_and_b32 v37,0xffff0000,v37\nv_fmac_f32 v25,v30,v43\n" \
"M a[140:143],a[36:39],a[12:15],a[140:143]\nv_fmac_f32 v26,v36,v47\nv_fmac_f32 v25,v31,v42\nM a[136:139],a[32:35],a[12:15],a[136:139]\nv_fmac_f32 v26,v37,v46\nv_accvgpr_read_b32 v28,a222\nM a[152:155],a[40:43],a[12:15],a[152:155]\nC v26,v25,v26\n" \
"v_lshlrev_b32 v27,16,v28\nM a[156:159],a[44:47],a[12:15],a[156:159]\nv_lshlrev_b32 v25,16,v26\nv_and_b32 v28,0xffff0000,v28\nM a[148:151],a[44:47],a[8:11],a[148:151]\ns_waitcnt vmcnt(22) lgkmcnt(0)\nv_and_b32 v26,0xffff0000,v26\nv_mul_f32 v29,v25,v27\n" \
"M a[144:147],a[40:43],a[8:11],a[144:147]\ns_barrier\nv_mul_f32 v58,v26,v28\nM a[160:163],a[48:51],a[8:11],a[160:163]\nv_accvgpr_read_b32 v38,a208\nv_mul_f32 v25,v35,v45\nM a[164:167],a[52:55],a[8:11],a[164:167]\nv_lshlrev_b32 v32,16,v38\n" \
"v_mul_f32 v26,v41,v49\nM a[172:175],a[52:55],a[12:15],a[172:175]\nv_fmac_f32 v25,v30,v44\nv_and_b32 v38,0xffff0000,v38\nM a[168:171],a[48:51],a[12:15],a[168:171]\nLG2_%=:\ns_cbranch_execz LG6_%=\nv_fmac_f32 v26,v36,v48\n" \
"v_fmac_f32 v25,v31,v43\nM a[184:187],a[56:59],a[12:15],a[184:187]\nds_read_b64_tr_b16 a[32:33],v9 offset:0\nds_read_b64_tr_b16 a[34:35],v10 offset:0\nM a[188:191],a[60:63],a[12:15],a[188:191]\nds_read_b64_tr_b16 a[36:37],v11 offset:0\nv_fmac_f32 v26,v37,v47\nM a[180:183],a[60:63],a[8:11],a[180:183]\n" \
"ds_read_b64_tr_b16 a[38:39],v12 offset:0\nds_read_b64_tr_b16 a[40:41],v9 offset:512\nM a[176:179],a[56:59],a[8:11],a[176:179]\nds_read_b64_tr_b16 a[42:43],v10 offset:512\nv_fmac_f32 v25,v32,v42\nM v[128:131],a[96:99],a[88:91],v[128:131]\nv_fmac_f32 v26,v38,v46\nv_accvgpr_read_b32 v28,a224\n" \
"M v[132:135],a[100:103],a[88:91],v[132:135]\nds_read_b64_tr_b16 a[44:45],v11 offset:512\nC v26,v25,v26\nM v[140:143],a[100:103],a[92:95],v[140:143]\nds_read_b64_tr_b16 a[46:47],v12 offset:512\nv_lshlrev_b32 v27,16,v28\nM v[136:139],a[96:99],a[92:95],v[136:139]\nds_read_b64_tr_b16 a[48:49],v9 offset:1024\n" \
"ds_read_b64_tr_b16 a[50:51],v10 offset:1024\nM v[152:155],a[104:107],a[92:95],v[152:155]\nds_read_b64_tr_b16 a[52:53],v11 offset:1024\nv_lshlrev_b32 v25,16,v26\nM v[156:159],a[108:111],a[92:95],v[156:159]\nds_read_b64_tr_b16 a[54:55],v12 offset:1024\nds_read_b64_tr_b16 a[56:57],v9 offset:1536\nM v[148:151],a[108:111],a[88:91],v[148:151]\n" \
"ds_read_b64_tr_b16 a[58:59],v10 offset:1536\nv_and_b32 v28,0xffff0000,v28\nM v[144:147],a[104:107],a[88:91],v[144:147]\nds_read_b64_tr_b16 a[60:61],v11 offset:1536\nv_and_b32 v26,0xffff0000,v26\nM v[160:163],a[112:115],a[88:91],v[160:163]\nds_read_b64_tr_b16 a[62:63],v12 offset:1536\nv_mul_f32 v25,v25,v27\n" \
"M v[164:167],a[116:119],a[88:91],v[164:167]\nds_read_b128 a[24:27],v7 offset:12288\nv_mul_f32 v26,v26,v28\nM v[172:175],a[116:119],a[92:95],v[172:175]\nds_read_b128 a[28:31],v7 offset:14336\nC v51,v25,v29\nM v[168:171],a[112:115],a[92:95],v[168:171]\nC v55,v26,v58\n" \
"v_accvgpr_read_b32 v39,a210\nM v[184:187],a[120:123],a[92:95],v[184:187]\nv_mul_f32 v25,v30,v45\nv_mul_f32 v26,v36,v49\nM v[188:191],a[124:127],a[92:95],v[188:191]\nv_lshlrev_b32 v33,16,v39\nv_fmac_f32 v25,v31,v44\nM v[180:183],a[124:127],a[88:91],v[180:183]\n" \
"v_fmac_f32 v26,v37,v48\nv_and_b32 v39,0xffff0000,v39\nM v[176:179],a[120:123],a[88:91],v[176:179]\nv_fmac_f32 v25,v32,v43\nv_fmac_f32 v26,v38,v47\nM v[64:67],a[96:99],a[80:83],v[64:67]\nv_fmac_f32 v25,v33,v42\nv_fmac_f32 v26,v39,v46\n" \
"M v[68:71],a[100:103],a[80:83],v[68:71]\nv_accvgpr_read_b32 v28,a226\nC v26,v25,v26\nM v[76:79],a[100:103],a[84:87],v[76:79]\nv_lshlrev_b32 v27,16,v28\nv_lshlrev_b32 v25,16,v26\nM v[72:75],a[96:99],a[84:87],v[72:75]\nv_and_b32 v28,0xffff0000,v28\n" \
"v_and_b32 v26,0xffff0000,v26\nM v[88:91],a[104:107],a[84:87],v[88:91]\nv_mul_f32 v29,v25,v27\nv_mul_f32 v58,v26,v28\nM v[92:95],a[108:111],a[84:87],v[92:95]\nv_accvgpr_read_b32 v40,a212\nv_lshlrev_b32 v34,16,v40\nM v[84:87],a[108:111],a[80:83],v[84:87]\n" \
"v_mul_f32 v25,v31,v45\nv_mul_f32 v26,v37,v49\nM v[80:83],a[104:107],a[80:83],v[80:83]\nds_read_b128 a[16:19],v7 offset:8192\nv_fmac_f32 v25,v32,v44\nM v[96:99],a[112:115],a[80:83],v[96:99]\nds_read_b128 a[20:23],v7 offset:10240\nv_and_b32 v40,0xffff0000,v40\n" \
"M v[100:103],a[116:119],a[80:83],v[100:103]\nv_fmac_f32 v26,v38,v48\nv_fmac_f32 v25,v33,v43\nM v[108:111],a[116:119],a[84:87],v[108:111]\nv_fmac_f32 v26,v39,v47\nv_fmac_f32 v25,v34,v42\nM v[104:107],a[112:115],a[84:87],v[104:107]\nv_fmac_f32 v26,v40,v46\n" \
"v_accvgpr_read_b32 v28,a228\nM v[120:123],a[120:123],a[84:87],v[120:123]\nC v26,v25,v26\nv_lshlrev_b32 v27,16,v28\nM v[124:127],a[124:127],a[84:87],v[124:127]\nv_lshlrev_b32 v25,16,v26\nv_mul_f32 v25,v25,v27\nM v[116:119],a[124:127],a[80:83],v[116:119]\n" \
"C v50,v25,v29\nv_accvgpr_read_b32 v45,a235\nM v[112:115],a[120:123],a[80:83],v[112:115]\nv_and_b32 v28,0xffff0000,v28\nv_and_b32 v26,0xffff0000,v26\nM v[192:195],a[96:99],a[64:67],v[192:195]\nv_lshlrev_b32 v44,16,v45\nv_accvgpr_read_b32 v49,a237\n" \
"M v[196:199],a[100:103],a[64:67],v[196:199]\nds_read_b128 a[0:3],v7 offset:0\nv_accvgpr_read_b32 v36,a193\nM v[204:207],a[100:103],a[68:71],v[204:207]\nds_read_b128 a[4:7],v7 offset:2048\nv_accvgpr_read_b32 v37,a195\nM v[200:203],a[96:99],a[68:71],v[200:203]\nv_mul_f32 v26,v26,v28\n" \
"v_accvgpr_read_b32 v43,a234\nM v[216:219],a[104:107],a[68:71],v[216:219]\nv_accvgpr_read_b32 v47,a236\nv_lshlrev_b32 v48,16,v49\nM v[220:223],a[108:111],a[68:71],v[220:223]\nv_and_b32 v45,0xffff0000,v45\nv_lshlrev_b32 v30,16,v36\nM v[212:215],a[108:111],a[64:67],v[212:215]\n" \
"buffer_store_dwordx4 v[50:53],v60,s[36:39],s98 offen offset:0 sc1\nM v[208:211],a[104:107],a[64:67],v[208:211]\ns_mov_b32 s98,79699968\nv_lshlrev_b32 v31,16,v37\nv_accvgpr_read_b32 v38,a197\nM v[224:227],a[112:115],a[64:67],v[224:227]\nC v54,v26,v58\nv_accvgpr_read_b32 v39,a199\n" \
"M v[228:231],a[116:119],a[64:67],v[228:231]\nv_lshlrev_b32 v42,16,v43\nv_and_b32 v49,0xffff0000,v49\nM v[236:239],a[116:119],a[68:71],v[236:239]\nv_lshlrev_b32 v46,16,v47\nv_and_b32 v36,0xffff0000,v36\nM v[232:235],a[112:115],a[68:71],v[232:235]\nv_lshlrev_b32 v32,16,v38\n" \
"v_mul_f32 v25,v30,v45\nM v[248:251],a[120:123],a[68:71],v[248:251]\nds_read_b128 a[8:11],v7 offset:4096\nds_read_b128 a[12:15],v7 offset:6144\nM v[252:255],a[124:127],a[68:71],v[252:255]\nv_and_b32 v37,0xffff0000,v37\nv_lshlrev_b32 v33,16,v39\nM v[244:247],a[124:127],a[64:67],v[244:247]\n" \
"v_and_b32 v43,0xffff0000,v43\nv_mul_f32 v26,v36,v49\nM v[240:243],a[120:123],a[64:67],v[240:243]\nds_read_b128 a[64:67],v8 offset:0\nds_read_b128 a[68:71],v8 offset:2048\nM a[128:131],a[96:99],a[72:75],a[128:131]\nv_and_b32 v47,0xffff0000,v47\nv_and_b32 v38,0xffff0000,v38\n" \
"M a[132:135],a[100:103],a[72:75],a[132:135]\nds_read_b128 a[80:83],v8 offset:8192\nv_fmac_f32 v25,v31,v44\nM a[140:143],a[100:103],a[76:79],a[140:143]\nv_and_b32 v39,0xffff0000,v39\nv_fmac_f32 v26,v37,v48\nM a[136:139],a[96:99],a[76:79],a[136:139]\nv_fmac_f32 v25,v32,v43\n" \
"v_fmac_f32 v26,v38,v47\nM a[152:155],a[104:107],a[76:79],a[152:155]\nv_fmac_f32 v25,v33,v42\nv_fmac_f32 v26,v39,v46\nM a[156:159],a[108:111],a[76:79],a[156:159]\nds_read_b128 a[84:87],v8 offset:10240\nv_accvgpr_read_b32 v28,a215\nM a[148:151],a[108:111],a[72:75],a[148:151]\n" \
"s_waitcnt vmcnt(19) lgkmcnt(12)\nds_read_b128 a[88:91],v8 offset:12288\nds_read_b128 a[92:95],v8 offset:14336\nM a[144:147],a[104:107],a[72:75],a[144:147]\ns_barrier\nds_read_b64_tr_b16 a[96:97],v9 offset:16384\nM a[160:163],a[112:115],a[72:75],a[160:163]\nds_read_b64_tr_b16 a[98:99],v10 offset:16384\n" \
"C v26,v25,v26\nM a[164:167],a[116:119],a[72:75],a[164:167]\nds_read_b64_tr_b16 a[100:101],v11 offset:16384\nv_lshlrev_b32 v27,16,v28\nM a[172:175],a[116:119],a[76:79],a[172:175]\nds_read_b64_tr_b16 a[102:103],v12 offset:16384\nds_read_b64_tr_b16 a[104:105],v9 offset:16896\nM a[168:171],a[112:115],a[76:79],a[168:171]\n" \
"ds_read_b64_tr_b16 a[106:107],v10 offset:16896\nv_lshlrev_b32 v25,16,v26\nM a[184:187],a[120:123],a[76:79],a[184:187]\nds_read_b64_tr_b16 a[108:109],v11 offset:16896\nv_and_b32 v28,0xffff0000,v28\nM a[188:191],a[124:127],a[76:79],a[188:191]\ns_waitcnt lgkmcnt(6)\nds_read_b64_tr_b16 a[110:111],v12 offset:16896\n" \
"ds_read_b64_tr_b16 a[112:113],v9 offset:17408\nM a[180:183],a[124:127],a[72:75],a[180:183]\nbuffer_store_dwordx4 v[54:57],v60,s[36:39],s99 offen offset:0 sc1\nM a[176:179],a[120:123],a[72:75],a[176:179]\ns_mov_b32 s99,79704064\nds_read_b128 a[72:75],v8 offset:4096\nds_read_b128 a[76:79],v8 offset:6144\nM v[128:131],a[32:35],a[24:27],v[128:131]\n" \
"ds_read_b64_tr_b16 a[114:115],v10 offset:17408\nv_and_b32 v26,0xffff0000,v26\nM v[132:135],a[36:39],a[24:27],v[132:135]\nds_read_b64_tr_b16 a[116:117],v11 offset:17408\nv_mul_f32 v29,v25,v27\nM v[140:143],a[36:39],a[28:31],v[140:143]\nds_read_b64_tr_b16 a[118:119],v12 offset:17408\nv_mul_f32 v58,v26,v28\n" \
"M v[136:139],a[32:35],a[28:31],v[136:139]\nds_read_b64_tr_b16 a[120:121],v9 offset:17920\nds_read_b64_tr_b16 a[122:123],v10 offset:17920\nM v[152:155],a[40:43],a[28:31],v[152:155]\nds_read_b64_tr_b16 a[124:125],v11 offset:17920\nv_accvgpr_read_b32 v40,a201\nM v[156:159],a[44:47],a[28:31],v[156:159]\nds_read_b64_tr_b16 a[126:127],v12 offset:17920\n" \
"v_lshlrev_b32 v34,16,v40\nM v[148:151],a[44:47],a[24:27],v[148:151]\nv_mul_f32 v25,v31,v45\nv_mul_f32 v26,v37,v49\nM v[144:147],a[40:43],a[24:27],v[144:147]\nv_fmac_f32 v25,v32,v44\nv_and_b32 v40,0xffff0000,v40\nM v[160:163],a[48:51],a[24:27],v[160:163]\n" \
"v_fmac_f32 v26,v38,v48\nv_fmac_f32 v25,v33,v43\nM v[164:167],a[52:55],a[24:27],v[164:167]\nv_fmac_f32 v26,v39,v47\nv_fmac_f32 v25,v34,v42\nM v[172:175],a[52:55],a[28:31],v[172:175]\nv_fmac_f32 v26,v40,v46\nv_accvgpr_read_b32 v28,a217\n" \
"M v[168:171],a[48:51],a[28:31],v[168:171]\nC v26,v25,v26\nv_lshlrev_b32 v27,16,v28\nM v[184:187],a[56:59],a[28:31],v[184:187]\nv_lshlrev_b32 v25,16,v26\nv_and_b32 v28,0xffff0000,v28\nM v[188:191],a[60:63],a[28:31],v[188:191]\nv_and_b32 v26,0xffff0000,v26\n" \
"v_mul_f32 v25,v25,v27\nM v[180:183],a[60:63],a[24:27],v[180:183]\nv_mul_f32 v26,v26,v28\nC v53,v25,v29\nM v[176:179],a[56:59],a[24:27],v[176:179]\nC v57,v26,v58\nv_accvgpr_read_b32 v41,a203\nM v[64:67],a[32:35],a[16:19],v[64:67]\n" \
"v_mul_f32 v25,v32,v45\nv_lshlrev_b32 v35,16,v41\nM v[68:71],a[36:39],a[16:19],v[68:71]\nv_mul_f32 v26,v38,v49\nv_fmac_f32 v25,v33,v44\nM v[76:79],a[36:39],a[20:23],v[76:79]\nv_fmac_f32 v26,v39,v48\nv_and_b32 v41,0xffff0000,v41\n" \
"M v[72:75],a[32:35],a[20:23],v[72:75]\nv_fmac_f32 v25,v34,v43\nv_fmac_f32 v26,v40,v47\nM v[88:91],a[40:43],a[20:23],v[88:91]\nv_fmac_f32 v25,v35,v42\nv_fmac_f32 v26,v41,v46\nM v[92:95],a[44:47],a[20:23],v[92:95]\nv_accvgpr_read_b32 v28,a219\n" \
"C v26,v25,v26\nM v[84:87],a[44:47],a[16:19],v[84:87]\ns_mov_b64 exec,0xff\nglobal_load_dword v0,v2,s[100:101]\ns_mov_b64 exec,-1\nM v[80:83],a[40:43],a[16:19],v[80:83]\nv_lshlrev_b32 v27,16,v28\nv_lshlrev_b32 v25,16,v26\n" \
"M v[96:99],a[48:51],a[16:19],v[96:99]\nbuffer_load_dwordx4 v21,s[32:35],s47 offen offset:0 lds\nM v[100:103],a[52:55],a[16:19],v[100:103]\ns_add_u32 m0,s22,50048\nv_and_b32 v28,0xffff0000,v28\nv_and_b32 v26,0xffff0000,v26\nM v[108:111],a[52:55],a[20:23],v[108:111]\nv_mul_f32 v29,v25,v27\n" \
"v_mul_f32 v58,v26,v28\nM v[104:107],a[48:51],a[20:23],v[104:107]\nv_accvgpr_read_b32 v36,a205\nv_lshlrev_b32 v30,16,v36\nM v[120:123],a[56:59],a[20:23],v[120:123]\nbuffer_load_dwordx4 v21,s[32:35],s47 offen offset:128 lds\nM v[124:127],a[60:63],a[20:23],v[124:127]\ns_add_u32 m0,s22,51200\n" \
"v_mul_f32 v25,v33,v45\nv_mul_f32 v26,v39,v49\nM v[116:119],a[60:63],a[16:19],v[116:119]\ns_mov_b64 s[32:33],s[44:45]\nv_fmac_f32 v25,v34,v44\nv_and_b32 v36,0xffff0000,v36\nM v[112:115],a[56:59],a[16:19],v[112:115]\ns_mov_b32 s34,s49\n" \
"v_fmac_f32 v26,v40,v48\nv_fmac_f32 v25,v35,v43\nM v[192:195],a[32:35],a[0:3],v[192:195]\nbuffer_load_dwordx4 v21,s[92:95],s47 offen offset:0 lds\nM v[196:199],a[36:39],a[0:3],v[196:199]\ns_add_u32 m0,s22,52096\nv_fmac_f32 v26,v41,v47\nv_fmac_f32 v25,v30,v42\n" \
"M v[204:207],a[36:39],a[4:7],v[204:207]\nv_fmac_f32 v26,v36,v46\nv_accvgpr_read_b32 v28,a221\nM v[200:203],a[32:35],a[4:7],v[200:203]\nC v26,v25,v26\nv_lshlrev_b32 v27,16,v28\nM v[216:219],a[40:43],a[4:7],v[216:219]\nbuffer_load_dwordx4 v21,s[92:95],s47 offen offset:128 lds\n" \
"M v[220:223],a[44:47],a[4:7],v[220:223]\ns_add_u32 m0,s50,32768\nv_lshlrev_b32 v25,16,v26\nv_and_b32 v28,0xffff0000,v28\nM v[212:215],a[44:47],a[0:3],v[212:215]\ns_mov_b32 s47,0\nv_and_b32 v26,0xffff0000,v26\nv_mul_f32 v25,v25,v27\n" \
"M v[208:211],a[40:43],a[0:3],v[208:211]\nv_mul_f32 v26,v26,v28\nC v52,v25,v29\nM v[224:227],a[48:51],a[0:3],v[224:227]\nbuffer_load_dwordx4 v13,s[28:31],s46 offen lds\nM v[228:231],a[52:55],a[0:3],v[228:231]\ns_add_u32 m0,s50,36864\nC v56,v26,v58\n" \
"v_accvgpr_read_b32 v37,a207\nM v[236:239],a[52:55],a[4:7],v[236:239]\nv_mul_f32 v25,v34,v45\nv_lshlrev_b32 v31,16,v37\nM v[232:235],a[48:51],a[4:7],v[232:235]\nv_mul_f32 v26,v40,v49\nv_fmac_f32 v25,v35,v44\nM v[248:251],a[56:59],a[4:7],v[248:251]\n" \
"buffer_load_dwordx4 v14,s[28:31],s46 offen lds\nM v[252:255],a[60:63],a[4:7],v[252:255]\ns_add_u32 m0,s50,40960\nv_fmac_f32 v26,v41,v48\nv_and_b32 v37,0xffff0000,v37\nM v[244:247],a[60:63],a[0:3],v[244:247]\nv_fmac_f32 v25,v30,v43\nv_fmac_f32 v26,v36,v47\n" \
"M v[240:243],a[56:59],a[0:3],v[240:243]\nv_fmac_f32 v25,v31,v42\nv_fmac_f32 v26,v37,v46\nM a[128:131],a[32:35],a[8:11],a[128:131]\nbuffer_load_dwordx4 v15,s[28:31],s46 offen lds\nM a[132:135],a[36:39],a[8:11],a[132:135]\ns_add_u32 m0,s50,45056\nv_accvgpr_read_b32 v28,a223\n" \
"C v26,v25,v26\nM a[140:143],a[36:39],a[12:15],a[140:143]\nv_lshlrev_b32 v27,16,v28\nv_lshlrev_b32 v25,16,v26\nM a[136:139],a[32:35],a[12:15],a[136:139]\nv_and_b32 v28,0xffff0000,v28\nv_and_b32 v26,0xffff0000,v26\nM a[152:155],a[40:43],a[12:15],a[152:155]\n" \
"buffer_load_dwordx4 v16,s[28:31],s46 offen lds\nM a[156:159],a[44:47],a[12:15],a[156:159]\ns_add_u32 m0,s50,49152\nv_mul_f32 v29,v25,v27\nv_mul_f32 v58,v26,v28\nM a[148:151],a[44:47],a[8:11],a[148:151]\ns_waitcnt vmcnt(17) lgkmcnt(0)\nv_accvgpr_read_b32 v38,a209\n" \
"v_mul_f32 v25,v35,v45\nM a[144:147],a[40:43],a[8:11],a[144:147]\ns_mov_b32 s46,0\ns_barrier\nds_read_b64_tr_b16 a[32:33],v9 offset:32768\nM a[160:163],a[48:51],a[8:11],a[160:163]\nbuffer_load_dwordx4 v17,s[28:31],s46 offen lds\nM a[164:167],a[52:55],a[8:11],a[164:167]\n" \
"s_add_u32 m0,s50,53248\nds_read_b64_tr_b16 a[34:35],v10 offset:32768\nv_mul_f32 v26,v41,v49\nM a[172:175],a[52:55],a[12:15],a[172:175]\nds_read_b64_tr_b16 a[36:37],v11 offset:32768\nds_read_b64_tr_b16 a[38:39],v12 offset:32768\nM a[168:171],a[48:51],a[12:15],a[168:171]\nds_read_b64_tr_b16 a[40:41],v9 offset:33280\n" \
"v_lshlrev_b32 v32,16,v38\nM a[184:187],a[56:59],a[12:15],a[184:187]\nbuffer_load_dwordx4 v18,s[28:31],s46 offen lds\nM a[188:191],a[60:63],a[12:15],a[188:191]\ns_add_u32 m0,s50,57344\nds_read_b64_tr_b16 a[42:43],v10 offset:33280\nv_fmac_f32 v25,v30,v44\nM a[180:183],a[60:63],a[8:11],a[180:183]\n" \
"ds_read_b64_tr_b16 a[44:45],v11 offset:33280\nv_fmac_f32 v26,v36,v48\nM a[176:179],a[56:59],a[8:11],a[176:179]\nds_read_b64_tr_b16 a[46:47],v12 offset:33280\nv_and_b32 v38,0xffff0000,v38\nM v[128:131],a[96:99],a[88:91],v[128:131]\nbuffer_load_dwordx4 v19,s[28:31],s46 offen lds\nM v[132:135],a[100:103],a[88:91],v[132:135]\n" \
"s_add_u32 m0,s50,61440\nds_read_b64_tr_b16 a[48:49],v9 offset:33792\nv_fmac_f32 v25,v31,v43\nM v[140:143],a[100:103],a[92:95],v[140:143]\nds_read_b64_tr_b16 a[50:51],v10 offset:33792\nv_fmac_f32 v26,v37,v47\nM v[136:139],a[96:99],a[92:95],v[136:139]\nds_read_b64_tr_b16 a[52:53],v11 offset:33792\n" \
"ds_read_b64_tr_b16 a[54:55],v12 offset:33792\nM v[152:155],a[104:107],a[92:95],v[152:155]\nbuffer_load_dwordx4 v20,s[28:31],s46 offen lds\nM v[156:159],a[108:111],a[92:95],v[156:159]\ns_add_u32 m0,s51,0\nv_fmac_f32 v25,v32,v42\nv_fmac_f32 v26,v38,v46\nM v[148:151],a[108:111],a[88:91],v[148:151]\n" \
"s_mov_b32 s46,128\nds_read_b64_tr_b16 a[56:57],v9 offset:34304\nds_read_b64_tr_b16 a[58:59],v10 offset:34304\nM v[144:147],a[104:107],a[88:91],v[144:147]\nds_read_b64_tr_b16 a[60:61],v11 offset:34304\nv_accvgpr_read_b32 v28,a225\nM v[160:163],a[112:115],a[88:91],v[160:163]\nbuffer_load_dwordx4 v21,s[32:35],s47 offen offset:0 lds\n" \
"M v[164:167],a[116:119],a[88:91],v[164:167]\ns_add_u32 m0,s51,896\nds_read_b64_tr_b16 a[62:63],v12 offset:34304\nC v26,v25,v26\nM v[172:175],a[116:119],a[92:95],v[172:175]\nds_read_b128 a[24:27],v5 offset:12288\nv_lshlrev_b32 v27,16,v28\nM v[168:171],a[112:115],a[92:95],v[168:171]\n" \
"ds_read_b128 a[28:31],v5 offset:14336\nv_lshlrev_b32 v25,16,v26\nM v[184:187],a[120:123],a[92:95],v[184:187]\nbuffer_load_dwordx4 v21,s[32:35],s47 offen offset:128 lds\nM v[188:191],a[124:127],a[92:95],v[188:191]\ns_add_u32 m0,s51,8192\nv_and_b32 v28,0xffff0000,v28\nv_and_b32 v26,0xffff0000,v26\n" \
"M v[180:183],a[124:127],a[88:91],v[180:183]\ns_mov_b32 s47,262144\nv_mul_f32 v25,v25,v27\nv_mul_f32 v26,v26,v28\nM v[176:179],a[120:123],a[88:91],v[176:179]\nC v51,v25,v29\nC v55,v26,v58\nM v[64:67],a[96:99],a[80:83],v[64:67]\n" \
"buffer_load_dwordx4 v21,s[32:35],s47 offen offset:0 lds\nM v[68:71],a[100:103],a[80:83],v[68:71]\ns_add_u32 m0,s51,9088\nv_accvgpr_read_b32 v39,a211\nv_mul_f32 v25,v30,v45\nM v[76:79],a[100:103],a[84:87],v[76:79]\nv_lshlrev_b32 v33,16,v39\nv_mul_f32 v26,v36,v49\n" \
"M v[72:75],a[96:99],a[84:87],v[72:75]\nv_fmac_f32 v25,v31,v44\nv_fmac_f32 v26,v37,v48\nM v[88:91],a[104:107],a[84:87],v[88:91]\nbuffer_load_dwordx4 v21,s[32:35],s47 offen offset:128 lds\nM v[92:95],a[108:111],a[84:87],v[92:95]\ns_add_u32 m0,s51,16384\nv_and_b32 v39,0xffff0000,v39\n" \
"v_fmac_f32 v25,v32,v43\nM v[84:87],a[108:111],a[80:83],v[84:87]\ns_mov_b32 s47,524288\nv_fmac_f32 v26,v38,v47\nv_fmac_f32 v25,v33,v42\nM v[80:83],a[104:107],a[80:83],v[80:83]\nv_fmac_f32 v26,v39,v46\nv_accvgpr_read_b32 v28,a227\n" \
"M v[96:99],a[112:115],a[80:83],v[96:99]\nbuffer_load_dwordx4 v21,s[32:35],s47 offen offset:0 lds\nM v[100:103],a[116:119],a[80:83],v[100:103]\ns_add_u32 m0,s51,17280\nC v26,v25,v26\nv_lshlrev_b32 v27,16,v28\nM v[108:111],a[116:119],a[84:87],v[108:111]\nds_read_b128 a[16:19],v5 offset:8192\n" \
"v_lshlrev_b32 v25,16,v26\nM v[104:107],a[112:115],a[84:87],v[104:107]\nds_read_b128 a[20:23],v5 offset:10240\nv_and_b32 v28,0xffff0000,v28\nM v[120:123],a[120:123],a[84:87],v[120:123]\nbuffer_load_dwordx4 v21,s[32:35],s47 offen offset:128 lds\nM v[124:127],a[124:127],a[84:87],v[124:127]\ns_add_u32 m0,s51,24576\n" \
"v_and_b32 v26,0xffff0000,v26\nv_mul_f32 v29,v25,v27\nM v[116:119],a[124:127],a[80:83],v[116:119]\ns_mov_b32 s47,786432\nv_mul_f32 v58,v26,v28\nv_accvgpr_read_b32 v40,a213\nM v[112:115],a[120:123],a[80:83],v[112:115]\nv_lshlrev_b32 v34,16,v40\n" \
"v_mul_f32 v25,v31,v45\nM v[192:195],a[96:99],a[64:67],v[192:195]\ns_mov_b64 exec,0xff\nglobal_load_dword v0,v1,s[76:77]\ns_mov_b64 exec,-1\nM v[196:199],a[100:103],a[64:67],v[196:199]\nv_mul_f32 v26,v37,v49\nv_fmac_f32 v25,v32,v44\n" \
"M v[204:207],a[100:103],a[68:71],v[204:207]\ns_mov_b64 exec,0xff00\nglobal_load_dword v0,v1,s[76:77]\ns_mov_b64 exec,-1\nM v[200:203],a[96:99],a[68:71],v[200:203]\ns_add_u32 s76,s76,2097152\ns_addc_u32 s77,s77,0\nv_and_b32 v40,0xffff0000,v40\n" \
"v_fmac_f32 v26,v38,v48\nM v[216:219],a[104:107],a[68:71],v[216:219]\ns_mov_b64 exec,0xff\nglobal_load_dword v0,v1,s[76:77]\ns_mov_b64 exec,-1\nM v[220:223],a[108:111],a[68:71],v[220:223]\nv_fmac_f32 v25,v33,v43\nv_fmac_f32 v26,v39,v47\n" \
"M v[212:215],a[108:111],a[64:67],v[212:215]\ns_mov_b64 exec,0xff00\nglobal_load_dword v0,v1,s[76:77]\ns_mov_b64 exec,-1\nM v[208:211],a[104:107],a[64:67],v[208:211]\ns_mov_b32 s76,0xf149f2ca\nv_fmac_f32 v25,v34,v42\nv_fmac_f32 v26,v40,v46\n" \
"M v[224:227],a[112:115],a[64:67],v[224:227]\nv_accvgpr_read_b32 v28,a229\nC v26,v25,v26\nM v[228:231],a[116:119],a[64:67],v[228:231]\nv_lshlrev_b32 v27,16,v28\nv_lshlrev_b32 v25,16,v26\nM v[236:239],a[116:119],a[68:71],v[236:239]\nv_mul_f32 v25,v25,v27\n" \
"C v50,v25,v29\nM v[232:235],a[112:115],a[68:71],v[232:235]\nds_read_b128 a[0:3],v5 offset:0\nds_read_b128 a[4:7],v5 offset:2048\nM v[248:251],a[120:123],a[68:71],v[248:251]\nds_read_b128 a[8:11],v5 offset:4096\nv_and_b32 v28,0xffff0000,v28\nM v[252:255],a[124:127],a[68:71],v[252:255]\n" \
"ds_read_b128 a[12:15],v5 offset:6144\nv_and_b32 v26,0xffff0000,v26\nM v[244:247],a[124:127],a[64:67],v[244:247]\nv_mul_f32 v26,v26,v28\nC v54,v26,v58\nM v[240:243],a[120:123],a[64:67],v[240:243]\nds_read_b128 a[64:67],v6 offset:0\nds_read_b128 a[68:71],v6 offset:2048\n" \
"M a[128:131],a[96:99],a[72:75],a[128:131]\nds_read_b128 a[80:83],v6 offset:8192\nv_add_u32 v5,s54,v5\nM a[132:135],a[100:103],a[72:75],a[132:135]\nds_read_b128 a[84:87],v6 offset:10240\nv_add_u32 v7,s54,v7\nM a[140:143],a[100:103],a[76:79],a[140:143]\nds_read_b128 a[88:91],v6 offset:12288\n" \
"v_add_u32 v8,s54,v8\nM a[136:139],a[96:99],a[76:79],a[136:139]\nds_read_b128 a[92:95],v6 offset:14336\nv_mov_b32 v58,s18\nM a[152:155],a[104:107],a[76:79],a[152:155]\nv_add_u32 v25,-16,v4\nv_mov_b32 v29,s62\nM a[156:159],a[108:111],a[76:79],a[156:159]\n" \
"s_and_b32 s62,%1,7\nv_mov_b32 v28,0\nv_add_u32 v26,-512,v4\nM a[148:151],a[108:111],a[72:75],a[148:151]\ns_waitcnt vmcnt(18) lgkmcnt(14)\nv_add_u32 v27,512,v4\nM a[144:147],a[104:107],a[72:75],a[144:147]\ns_lshl_b32 s62,s62,8\n" \
"s_barrier\nds_read_b64_tr_b16 a[96:97],v9 offset:49152\nM a[160:163],a[112:115],a[72:75],a[160:163]\ns_add_u32 s62,s62,94380032\nds_read_b64_tr_b16 a[98:99],v10 offset:49152\nds_read_b64_tr_b16 a[100:101],v11 offset:49152\nM a[164:167],a[116:119],a[72:75],a[164:167]\nds_read_b64_tr_b16 a[102:103],v12 offset:49152\n" \
"M a[172:175],a[116:119],a[76:79],a[172:175]\nds_read_b64_tr_b16 a[104:105],v9 offset:49664\nds_read_b64_tr_b16 a[106:107],v10 offset:49664\nM a[168:171],a[112:115],a[76:79],a[168:171]\nLG3_%=:\ns_cbranch_execz LG7_%=\nds_read_b64_tr_b16 a[108:109],v11 offset:49664\nM a[184:187],a[120:123],a[76:79],a[184:187]\n" \
"ds_read_b64_tr_b16 a[110:111],v12 offset:49664\nM a[188:191],a[124:127],a[76:79],a[188:191]\nds_read_b64_tr_b16 a[112:113],v9 offset:50176\nds_read_b64_tr_b16 a[114:115],v10 offset:50176\nM a[180:183],a[124:127],a[72:75],a[180:183]\nds_read_b64_tr_b16 a[116:117],v11 offset:50176\nM a[176:179],a[120:123],a[72:75],a[176:179]\ns_waitcnt lgkmcnt(7)\n" \
"ds_read_b128 a[72:75],v6 offset:4096\nM v[128:131],a[32:35],a[24:27],v[128:131]\nds_read_b128 a[76:79],v6 offset:6144\nv_add_u32 v6,s54,v6\nM v[132:135],a[36:39],a[24:27],v[132:135]\nds_read_b64_tr_b16 a[118:119],v12 offset:50176\nM v[140:143],a[36:39],a[28:31],v[140:143]\nds_read_b64_tr_b16 a[120:121],v9 offset:50688\n" \
"ds_read_b64_tr_b16 a[122:123],v10 offset:50688\nM v[136:139],a[32:35],a[28:31],v[136:139]\nds_read_b64_tr_b16 a[124:125],v11 offset:50688\nv_sub_u32 v9,v9,v62\nM v[152:155],a[40:43],a[28:31],v[152:155]\nds_read_b64_tr_b16 a[126:127],v12 offset:50688\nv_sub_u32 v10,v10,v62\nM v[156:159],a[44:47],a[28:31],v[156:159]\n" \
"v_sub_u32 v11,v11,v62\nv_sub_u32 v12,v12,v62\nM v[148:151],a[44:47],a[24:27],v[148:151]\nM v[144:147],a[40:43],a[24:27],v[144:147]\nM v[160:163],a[48:51],a[24:27],v[160:163]\nM v[164:167],a[52:55],a[24:27],v[164:167]\nM v[172:175],a[52:55],a[28:31],v[172:175]\nM v[168:171],a[48:51],a[28:31],v[168:171]\n" \
"M v[184:187],a[56:59],a[28:31],v[184:187]\nM v[188:191],a[60:63],a[28:31],v[188:191]\nM v[180:183],a[60:63],a[24:27],v[180:183]\nbuffer_load_dwordx4 v21,s[32:35],s47 offen offset:0 lds\nM v[176:179],a[56:59],a[24:27],v[176:179]\ns_add_u32 m0,s51,25472\nM v[64:67],a[32:35],a[16:19],v[64:67]\nM v[68:71],a[36:39],a[16:19],v[68:71]\n" \
"M v[76:79],a[36:39],a[20:23],v[76:79]\nbuffer_store_dwordx4 v[50:53],v60,s[36:39],s98 offen offset:0 sc1\nM v[72:75],a[32:35],a[20:23],v[72:75]\nM v[88:91],a[40:43],a[20:23],v[88:91]\nM v[92:95],a[44:47],a[20:23],v[92:95]\nM v[84:87],a[44:47],a[16:19],v[84:87]\nM v[80:83],a[40:43],a[16:19],v[80:83]\nM v[96:99],a[48:51],a[16:19],v[96:99]\n" \
"M v[100:103],a[52:55],a[16:19],v[100:103]\nM v[108:111],a[52:55],a[20:23],v[108:111]\nM v[104:107],a[48:51],a[20:23],v[104:107]\nM v[120:123],a[56:59],a[20:23],v[120:123]\nM v[124:127],a[60:63],a[20:23],v[124:127]\nM v[116:119],a[60:63],a[16:19],v[116:119]\nM v[112:115],a[56:59],a[16:19],v[112:115]\nM v[192:195],a[32:35],a[0:3],v[192:195]\n" \
"M v[196:199],a[36:39],a[0:3],v[196:199]\nM v[204:207],a[36:39],a[4:7],v[204:207]\nM v[200:203],a[32:35],a[4:7],v[200:203]\nM v[216:219],a[40:43],a[4:7],v[216:219]\nM v[220:223],a[44:47],a[4:7],v[220:223]\nM v[212:215],a[44:47],a[0:3],v[212:215]\nM v[208:211],a[40:43],a[0:3],v[208:211]\nM v[224:227],a[48:51],a[0:3],v[224:227]\n" \
"M v[228:231],a[52:55],a[0:3],v[228:231]\nM v[236:239],a[52:55],a[4:7],v[236:239]\nbuffer_store_dwordx4 v[54:57],v60,s[36:39],s99 offen offset:0 sc1\nM v[232:235],a[48:51],a[4:7],v[232:235]\ns_nop 0\nv_lshlrev_b32 v56,23,v4\nv_bfe_u32 v57,v4,4,5\nM v[248:251],a[56:59],a[4:7],v[248:251]\n" \
"v_lshl_add_u32 v57,v57,3,s62\nv_add_u32 v55,s56,v4\nM v[252:255],a[60:63],a[4:7],v[252:255]\nM v[244:247],a[60:63],a[0:3],v[244:247]\nM v[240:243],a[56:59],a[0:3],v[240:243]\nM a[128:131],a[32:35],a[8:11],a[128:131]\nM a[132:135],a[36:39],a[8:11],a[132:135]\nM a[140:143],a[36:39],a[12:15],a[140:143]\n" \
"M a[136:139],a[32:35],a[12:15],a[136:139]\nM a[152:155],a[40:43],a[12:15],a[152:155]\nM a[156:159],a[44:47],a[12:15],a[156:159]\nM a[148:151],a[44:47],a[8:11],a[148:151]\ns_waitcnt vmcnt(9) lgkmcnt(0)\nM a[144:147],a[40:43],a[8:11],a[144:147]\ns_barrier\nds_read_b64_tr_b16 a[0:1],v9 offset:0\n" \
"M a[160:163],a[48:51],a[8:11],a[160:163]\nds_read_b64_tr_b16 a[2:3],v10 offset:0\nds_read_b64_tr_b16 a[4:5],v11 offset:0\nM a[164:167],a[52:55],a[8:11],a[164:167]\nds_read_b128 a[44:47],v5 offset:45056\nM a[172:175],a[52:55],a[12:15],a[172:175]\nds_read_b64_tr_b16 a[6:7],v12 offset:0\nM a[168:171],a[48:51],a[12:15],a[168:171]\n" \
"ds_read_b64_tr_b16 a[16:17],v9 offset:1024\nds_read_b64_tr_b16 a[18:19],v10 offset:1024\nM a[184:187],a[56:59],a[12:15],a[184:187]\nds_read_b64_tr_b16 a[20:21],v11 offset:1024\nM a[188:191],a[60:63],a[12:15],a[188:191]\nds_read_b64_tr_b16 a[22:23],v12 offset:1024\nM a[180:183],a[60:63],a[8:11],a[180:183]\nds_read_b64_tr_b16 a[24:25],v9 offset:1536\n" \
"ds_read_b64_tr_b16 a[26:27],v10 offset:1536\nM a[176:179],a[56:59],a[8:11],a[176:179]\nds_read_b64_tr_b16 a[8:9],v9 offset:512\nM v[128:131],a[96:99],a[88:91],v[128:131]\nds_read_b64_tr_b16 a[10:11],v10 offset:512\nM v[132:135],a[100:103],a[88:91],v[132:135]\nds_read_b64_tr_b16 a[12:13],v11 offset:512\nds_read_b64_tr_b16 a[14:15],v12 offset:512\n" \
"M v[140:143],a[100:103],a[92:95],v[140:143]\nbuffer_load_dwordx4 v21,s[32:35],s47 offen offset:128 lds\nM v[136:139],a[96:99],a[92:95],v[136:139]\ns_add_u32 m0,s50,65536\nv_mul_f32 v128,s20,v128\nv_mul_f32 v129,s20,v129\nM v[152:155],a[104:107],a[92:95],v[152:155]\ns_mov_b32 s47,1048576\n" \
"v_mul_f32 v130,s20,v130\nv_mul_f32 v131,s20,v131\nM v[156:159],a[108:111],a[92:95],v[156:159]\nv_mul_f32 v132,s20,v132\nv_mul_f32 v133,s20,v133\nM v[148:151],a[108:111],a[88:91],v[148:151]\nbuffer_load_dwordx4 v13,s[28:31],s46 offen lds\nM v[144:147],a[104:107],a[88:91],v[144:147]\n" \
"s_add_u32 m0,s50,69632\nv_mul_f32 v134,s20,v134\nv_mul_f32 v135,s20,v135\nM v[160:163],a[112:115],a[88:91],v[160:163]\nv_mul_f32 v136,s20,v136\nv_mul_f32 v137,s20,v137\nM v[164:167],a[116:119],a[88:91],v[164:167]\nv_mul_f32 v138,s20,v138\n" \
"v_mul_f32 v139,s20,v139\nM v[172:175],a[116:119],a[92:95],v[172:175]\nbuffer_load_dwordx4 v14,s[28:31],s46 offen lds\nM v[168:171],a[112:115],a[92:95],v[168:171]\ns_add_u32 m0,s50,73728\nds_read_b64_tr_b16 a[28:29],v11 offset:1536\nv_mul_f32 v140,s20,v140\nM v[184:187],a[120:123],a[92:95],v[184:187]\n" \
"ds_read_b64_tr_b16 a[30:31],v12 offset:1536\nds_read_b128 a[40:43],v5 offset:40960\nM v[188:191],a[124:127],a[92:95],v[188:191]\nv_mul_f32 v141,s20,v141\nv_mul_f32 v142,s20,v142\nM v[180:183],a[124:127],a[88:91],v[180:183]\nbuffer_load_dwordx4 v15,s[28:31],s46 offen lds\nM v[176:179],a[120:123],a[88:91],v[176:179]\n" \
"s_add_u32 m0,s50,77824\nv_mul_f32 v143,s20,v143\nv_mul_f32 v144,s20,v144\nM v[64:67],a[96:99],a[80:83],v[64:67]\nv_mul_f32 v145,s20,v145\nv_mul_f32 v146,s20,v146\nM v[68:71],a[100:103],a[80:83],v[68:71]\nds_read_b128 a[32:35],v5 offset:32768\n" \
"v_mul_f32 v147,s20,v147\nM v[76:79],a[100:103],a[84:87],v[76:79]\nbuffer_load_dwordx4 v16,s[28:31],s46 offen lds\nM v[72:75],a[96:99],a[84:87],v[72:75]\ns_add_u32 m0,s50,81920\ns_mov_b32 s46,128\nv_mul_f32 v64,s20,v64\nv_mul_f32 v65,s20,v65\n" \
"M v[88:91],a[104:107],a[84:87],v[88:91]\nv_mul_f32 v66,s20,v66\nv_mul_f32 v67,s20,v67\nM v[92:95],a[108:111],a[84:87],v[92:95]\nC v64,v64,v65\nC v65,v66,v67\nM v[84:87],a[108:111],a[80:83],v[84:87]\nbuffer_load_dwordx4 v17,s[28:31],s46 offen lds\n" \
"M v[80:83],a[104:107],a[80:83],v[80:83]\ns_add_u32 m0,s50,86016\nv_mul_f32 v68,s20,v68\nv_mul_f32 v69,s20,v69\nM v[96:99],a[112:115],a[80:83],v[96:99]\nv_mul_f32 v148,s20,v148\nv_mul_f32 v149,s20,v149\nM v[100:103],a[116:119],a[80:83],v[100:103]\n" \
"v_mul_f32 v150,s20,v150\nv_mul_f32 v151,s20,v151\nM v[108:111],a[116:119],a[84:87],v[108:111]\nbuffer_load_dwordx4 v18,s[28:31],s46 offen lds\nM v[104:107],a[112:115],a[84:87],v[104:107]\ns_add_u32 m0,s50,90112\nv_mul_f32 v70,s20,v70\nv_mul_f32 v152,s20,v152\n" \
"M v[120:123],a[120:123],a[84:87],v[120:123]\nv_mul_f32 v71,s20,v71\nv_mul_f32 v153,s20,v153\nM v[124:127],a[124:127],a[84:87],v[124:127]\nv_mul_f32 v154,s20,v154\nv_mul_f32 v155,s20,v155\nM v[116:119],a[124:127],a[80:83],v[116:119]\nbuffer_load_dwordx4 v19,s[28:31],s46 offen lds\n" \
"M v[112:115],a[120:123],a[80:83],v[112:115]\ns_add_u32 m0,s50,94208\nv_mul_f32 v156,s20,v156\nv_mul_f32 v157,s20,v157\nM v[192:195],a[96:99],a[64:67],v[192:195]\nv_mul_f32 v158,s20,v158\nv_mul_f32 v159,s20,v159\nM v[196:199],a[100:103],a[64:67],v[196:199]\n" \
"v_mul_f32 v160,s20,v160\nv_mul_f32 v161,s20,v161\nM v[204:207],a[100:103],a[68:71],v[204:207]\nbuffer_load_dwordx4 v20,s[28:31],s46 offen lds\nM v[200:203],a[96:99],a[68:71],v[200:203]\ns_add_u32 m0,s51,32768\nds_read_b128 a[36:39],v5 offset:36864\nv_mul_f32 v162,s20,v162\n" \
"M v[216:219],a[104:107],a[68:71],v[216:219]\ns_mov_b32 s46,256\nv_mul_f32 v163,s20,v163\nC v66,v68,v69\nM v[220:223],a[108:111],a[68:71],v[220:223]\nv_mul_f32 v72,s20,v72\nv_mul_f32 v164,s20,v164\nM v[212:215],a[108:111],a[64:67],v[212:215]\n" \
"buffer_load_dwordx4 v21,s[32:35],s47 offen offset:0 lds\nM v[208:211],a[104:107],a[64:67],v[208:211]\ns_add_u32 m0,s51,33664\nv_mul_f32 v73,s20,v73\nC v30,v128,v129\nM v[224:227],a[112:115],a[64:67],v[224:227]\nv_mul_f32 v165,s20,v165\nC v31,v130,v131\n" \
"M v[228:231],a[116:119],a[64:67],v[228:231]\nv_mul_f32 v166,s20,v166\nC v32,v132,v133\nM v[236:239],a[116:119],a[68:71],v[236:239]\nbuffer_load_dwordx4 v21,s[32:35],s47 offen offset:128 lds\nM v[232:235],a[112:115],a[68:71],v[232:235]\ns_add_u32 m0,s51,40960\nv_mul_f32 v167,s20,v167\n" \
"C v33,v134,v135\nM v[248:251],a[120:123],a[68:71],v[248:251]\ns_mov_b32 s47,1310720\nv_mul_f32 v168,s20,v168\nC v34,v136,v137\nM v[252:255],a[124:127],a[68:71],v[252:255]\nv_mul_f32 v169,s20,v169\nC v35,v138,v139\n" \
"M v[244:247],a[124:127],a[64:67],v[244:247]\nbuffer_load_dwordx4 v21,s[32:35],s47 offen offset:0 lds\nM v[240:243],a[120:123],a[64:67],v[240:243]\ns_add_u32 m0,s51,41856\nds_read_b64_tr_b16 a[48:49],v9 offset:8192\nv_mul_f32 v170,s20,v170\nM a[128:131],a[96:99],a[72:75],a[128:131]\nds_read_b64_tr_b16 a[50:51],v10 offset:8192\n" \
"ds_read_b128 a[80:83],v6 offset:32768\nM a[132:135],a[100:103],a[72:75],a[132:135]\nds_read_b128 a[84:87],v6 offset:36864\nv_mul_f32 v171,s20,v171\nM a[140:143],a[100:103],a[76:79],a[140:143]\nbuffer_load_dwordx4 v21,s[32:35],s47 offen offset:128 lds\nM a[136:139],a[96:99],a[76:79],a[136:139]\ns_add_u32 m0,s50,0\n" \
"v_mul_f32 v74,s20,v74\nC v36,v140,v141\nM a[152:155],a[104:107],a[76:79],a[152:155]\ns_mov_b32 s47,1572864\nds_read_b64_tr_b16 a[52:53],v11 offset:8192\nv_mul_f32 v75,s20,v75\nM a[156:159],a[108:111],a[76:79],a[156:159]\nds_read_b64_tr_b16 a[54:55],v12 offset:8192\n" \
"ds_read_b64_tr_b16 a[56:57],v9 offset:8704\nM a[148:151],a[108:111],a[72:75],a[148:151]\nbuffer_load_dwordx4 v13,s[28:31],s46 offen lds\nM a[144:147],a[104:107],a[72:75],a[144:147]\ns_add_u32 m0,s50,4096\nC v37,v142,v143\nC v38,v144,v145\nM a[160:163],a[112:115],a[72:75],a[160:163]\n" \
"ds_read_b64_tr_b16 a[58:59],v10 offset:8704\nds_read_b64_tr_b16 a[60:61],v11 offset:8704\nM a[164:167],a[116:119],a[72:75],a[164:167]\nds_read_b64_tr_b16 a[62:63],v12 offset:8704\nC v39,v146,v147\nM a[172:175],a[116:119],a[76:79],a[172:175]\nbuffer_load_dwordx4 v14,s[28:31],s46 offen lds\nM a[168:171],a[112:115],a[76:79],a[168:171]\n" \
"s_add_u32 m0,s50,8192\nds_read_b64_tr_b16 a[64:65],v9 offset:9216\nv_mul_f32 v172,s20,v172\nM a[184:187],a[120:123],a[76:79],a[184:187]\nds_read_b64_tr_b16 a[66:67],v10 offset:9216\nv_mul_f32 v173,s20,v173\nM a[188:191],a[124:127],a[76:79],a[188:191]\ns_waitcnt lgkmcnt(5)\n" \
"ds_read_b64_tr_b16 a[68:69],v11 offset:9216\nv_mul_f32 v174,s20,v174\nM a[180:183],a[124:127],a[72:75],a[180:183]\nbuffer_load_dwordx4 v15,s[28:31],s46 offen lds\nM a[176:179],a[120:123],a[72:75],a[176:179]\ns_branch LC_%=\nLAV_%=:\nds_read_b64_tr_b16 a[32:33],v9 offset:0\n" \
"ds_read_b64_tr_b16 a[34:35],v10 offset:0\nds_read_b64_tr_b16 a[36:37],v11 offset:0\nds_read_b64_tr_b16 a[38:39],v12 offset:0\nds_read_b128 a[24:27],v7 offset:12288\nds_read_b128 a[28:31],v7 offset:14336\ns_waitcnt lgkmcnt(0)\nM v[128:131],a[24:27],a[32:35],0\ns_add_u32 m0,s22,32768\n" \
"s_mov_b32 s47,524288\ns_mov_b32 s46,128\nds_read_b64_tr_b16 a[40:41],v9 offset:512\nds_read_b64_tr_b16 a[42:43],v10 offset:512\nds_read_b64_tr_b16 a[44:45],v11 offset:512\nds_read_b64_tr_b16 a[46:47],v12 offset:512\nds_read_b64_tr_b16 a[48:49],v9 offset:1024\nds_read_b64_tr_b16 a[50:51],v10 offset:1024\n" \
"ds_read_b64_tr_b16 a[52:53],v11 offset:1024\nds_read_b64_tr_b16 a[54:55],v12 offset:1024\nM v[132:135],a[28:31],a[32:35],0\nbuffer_load_dwordx4 v21,s[32:35],s47 offen offset:0 lds\nds_read_b64_tr_b16 a[56:57],v9 offset:1536\nds_read_b64_tr_b16 a[58:59],v10 offset:1536\nds_read_b64_tr_b16 a[60:61],v11 offset:1536\nds_read_b64_tr_b16 a[62:63],v12 offset:1536\n" \
"M v[140:143],a[28:31],a[36:39],0\ns_add_u32 m0,s22,33664\nM v[136:139],a[24:27],a[36:39],0\ns_waitcnt lgkmcnt(8)\ns_mov_b64 s[40:41],s[66:67]\nds_read_b128 a[16:19],v7 offset:8192\nM v[152:155],a[24:27],a[44:47],0\ns_and_b32 s41,s41,0xffff\n" \
"ds_read_b128 a[20:23],v7 offset:10240\nM v[156:159],a[28:31],a[44:47],0\nbuffer_load_dwordx4 v21,s[32:35],s47 offen offset:128 lds\nM v[148:151],a[28:31],a[40:43],0\ns_add_u32 m0,s22,34816\nds_read_b128 a[0:3],v7 offset:0\nM v[144:147],a[24:27],a[40:43],0\ns_waitcnt lgkmcnt(3)\n" \
"s_mov_b32 s42,4194304\nds_read_b128 a[4:7],v7 offset:2048\nM v[160:163],a[24:27],a[48:51],0\ns_mov_b32 s43,0x00020000\nds_read_b128 a[8:11],v7 offset:4096\nM v[164:167],a[28:31],a[48:51],0\nbuffer_load_dwordx4 v21,s[92:95],s47 offen offset:0 lds\nM v[172:175],a[28:31],a[52:55],0\n" \
"s_add_u32 m0,s22,35712\nM v[168:171],a[24:27],a[52:55],0\ns_mov_b64 s[80:81],s[68:69]\nds_read_b128 a[12:15],v7 offset:6144\nM v[184:187],a[24:27],a[60:63],0\ns_mov_b32 s52,0\nds_read_b128 a[88:91],v8 offset:12288\nM v[188:191],a[28:31],a[60:63],0\n" \
"buffer_load_dwordx4 v21,s[92:95],s47 offen offset:128 lds\nM v[180:183],a[28:31],a[56:59],0\ns_add_u32 m0,s50,0\nds_read_b128 a[92:95],v8 offset:14336\nM v[176:179],a[24:27],a[56:59],0\ns_waitcnt lgkmcnt(3)\ns_mov_b32 s47,786432\nds_read_b128 a[80:83],v8 offset:8192\n" \
"M v[64:67],a[16:19],a[32:35],0\ns_waitcnt vmcnt(8) lgkmcnt(9)\nds_read_b128 a[84:87],v8 offset:10240\nM v[68:71],a[20:23],a[32:35],0\nbuffer_load_dwordx4 v13,s[28:31],s46 offen lds\ns_barrier\nM v[76:79],a[20:23],a[36:39],0\ns_add_u32 m0,s50,4096\n" \
"M v[72:75],a[16:19],a[36:39],0\ns_and_b32 s81,s81,0xffff\nds_read_b64_tr_b16 a[96:97],v9 offset:16384\nds_read_b64_tr_b16 a[98:99],v10 offset:16384\nM v[88:91],a[16:19],a[44:47],0\ns_mov_b32 s82,4194304\nds_read_b64_tr_b16 a[100:101],v11 offset:16384\nM v[92:95],a[20:23],a[44:47],0\n" \
"buffer_load_dwordx4 v14,s[28:31],s46 offen lds\nM v[84:87],a[20:23],a[40:43],0\ns_add_u32 m0,s50,8192\nds_read_b64_tr_b16 a[102:103],v12 offset:16384\nM v[80:83],a[16:19],a[40:43],0\ns_mov_b32 s83,0x00020000\nds_read_b64_tr_b16 a[104:105],v9 offset:16896\nM v[96:99],a[16:19],a[48:51],0\n" \
"s_lshl_b32 s60,s7,18\nds_read_b64_tr_b16 a[106:107],v10 offset:16896\nM v[100:103],a[20:23],a[48:51],0\nbuffer_load_dwordx4 v15,s[28:31],s46 offen lds\nM v[108:111],a[20:23],a[52:55],0\ns_add_u32 m0,s50,12288\nds_read_b64_tr_b16 a[108:109],v11 offset:16896\nM v[104:107],a[16:19],a[52:55],0\n" \
"s_sub_u32 s61,3932160,s60\nds_read_b64_tr_b16 a[110:111],v12 offset:16896\nM v[120:123],a[16:19],a[60:63],0\ns_add_u32 s44,s78,s98\ns_addc_u32 s45,s79,0\nds_read_b64_tr_b16 a[112:113],v9 offset:17408\nds_read_b64_tr_b16 a[114:115],v10 offset:17408\nM v[124:127],a[20:23],a[60:63],0\n" \
"buffer_load_dwordx4 v16,s[28:31],s46 offen lds\nM v[116:119],a[20:23],a[56:59],0\ns_add_u32 m0,s50,16384\ns_mov_b32 s46,128\nM v[112:115],a[16:19],a[56:59],0\ns_and_b32 s45,s45,0xffff\nds_read_b64_tr_b16 a[116:117],v11 offset:17408\nds_read_b64_tr_b16 a[118:119],v12 offset:17408\n" \
"M v[192:195],a[0:3],a[32:35],0\ns_mov_b64 s[36:37],s[90:91]\nds_read_b64_tr_b16 a[120:121],v9 offset:17920\nM v[196:199],a[4:7],a[32:35],0\nbuffer_load_dwordx4 v17,s[28:31],s46 offen lds\nM v[204:207],a[4:7],a[36:39],0\ns_add_u32 m0,s50,20480\nds_read_b64_tr_b16 a[122:123],v10 offset:17920\n" \
"M v[200:203],a[0:3],a[36:39],0\ns_and_b32 s37,s37,0xffff\nds_read_b64_tr_b16 a[124:125],v11 offset:17920\nM v[216:219],a[0:3],a[44:47],0\ns_mov_b32 s38,134217728\nds_read_b64_tr_b16 a[126:127],v12 offset:17920\nM v[220:223],a[4:7],a[44:47],0\nbuffer_load_dwordx4 v18,s[28:31],s46 offen lds\n" \
"M v[212:215],a[4:7],a[40:43],0\ns_add_u32 m0,s50,24576\nds_read_b128 a[64:67],v8 offset:0\nM v[208:211],a[0:3],a[40:43],0\ns_mov_b32 s39,0x00020000\nds_read_b128 a[68:71],v8 offset:2048\nM v[224:227],a[0:3],a[48:51],0\ns_add_u32 s96,s57,32768\n" \
"ds_read_b128 a[72:75],v8 offset:4096\nM v[228:231],a[4:7],a[48:51],0\nbuffer_load_dwordx4 v19,s[28:31],s46 offen lds\nM v[236:239],a[4:7],a[52:55],0\ns_add_u32 m0,s50,28672\nM v[232:235],a[0:3],a[52:55],0\ns_lshr_b32 s62,%1,3\nds_read_b128 a[76:79],v8 offset:6144\n" \
"M v[248:251],a[0:3],a[60:63],0\ns_lshl_b32 s62,s62,2\nM v[252:255],a[4:7],a[60:63],0\nbuffer_load_dwordx4 v20,s[28:31],s46 offen lds\nM v[244:247],a[4:7],a[56:59],0\ns_add_u32 m0,s22,49152\nM v[240:243],a[0:3],a[56:59],0\ns_waitcnt lgkmcnt(1)\n" \
"s_mov_b32 s46,256\nM a[128:131],a[8:11],a[32:35],0\ns_add_u32 s62,s62,%2\nM a[132:135],a[12:15],a[32:35],0\nbuffer_load_dwordx4 v21,s[32:35],s47 offen offset:0 lds\nM a[140:143],a[12:15],a[36:39],0\ns_add_u32 m0,s22,50048\nM a[136:139],a[8:11],a[36:39],0\n" \
"s_lshl_b32 s62,s62,10\nM a[152:155],a[8:11],a[44:47],0\ns_min_u32 s62,s62,LEND_%=-LBEG_%=-1024\nM a[156:159],a[12:15],a[44:47],0\nbuffer_load_dwordx4 v21,s[32:35],s47 offen offset:128 lds\nM a[148:151],a[12:15],a[40:43],0\ns_add_u32 m0,s22,51200\nM a[144:147],a[8:11],a[40:43],0\n" \
"s_add_u32 s100,s100,s62\ns_addc_u32 s101,s101,0\nM a[160:163],a[8:11],a[48:51],0\ns_lshl_b32 s62,s8,2\nM a[164:167],a[12:15],a[48:51],0\nbuffer_load_dwordx4 v21,s[92:95],s47 offen offset:0 lds\nM a[172:175],a[12:15],a[52:55],0\ns_add_u32 m0,s22,52096\n" \
"M a[168:171],a[8:11],a[52:55],0\ns_add_u32 s62,s62,%2\nM a[184:187],a[8:11],a[60:63],0\ns_mov_b64 s[10:11],s[72:73]\nM a[188:191],a[12:15],a[60:63],0\nbuffer_load_dwordx4 v21,s[92:95],s47 offen offset:128 lds\nM a[180:183],a[12:15],a[56:59],0\ns_add_u32 m0,s50,32768\n" \
"M a[176:179],a[8:11],a[56:59],0\ns_mov_b32 s47,1048576\nM v[128:131],a[88:91],a[96:99],v[128:131]\ns_lshl_b32 s63,s62,12\nM v[132:135],a[92:95],a[96:99],v[132:135]\nbuffer_load_dwordx4 v13,s[28:31],s46 offen lds\nM v[140:143],a[92:95],a[100:103],v[140:143]\ns_add_u32 m0,s50,36864\n" \
"M v[136:139],a[88:91],a[100:103],v[136:139]\ns_add_u32 s68,s10,s63\ns_addc_u32 s69,s11,0\nM v[152:155],a[88:91],a[108:111],v[152:155]\ns_and_b32 s69,s69,0xffff\nM v[156:159],a[92:95],a[108:111],v[156:159]\nbuffer_load_dwordx4 v14,s[28:31],s46 offen lds\nM v[148:151],a[92:95],a[104:107],v[148:151]\n" \
"s_add_u32 m0,s50,40960\nM v[144:147],a[88:91],a[104:107],v[144:147]\ns_lshl_b32 s62,s62,1\nM v[160:163],a[88:91],a[112:115],v[160:163]\ns_mov_b64 s[12:13],s[74:75]\nM v[164:167],a[92:95],a[112:115],v[164:167]\nbuffer_load_dwordx4 v15,s[28:31],s46 offen lds\nM v[172:175],a[92:95],a[116:119],v[172:175]\n" \
"s_add_u32 m0,s50,45056\nM v[168:171],a[88:91],a[116:119],v[168:171]\ns_sub_u32 s14,s18,2000\ns_subb_u32 s15,s19,0\nM v[184:187],a[88:91],a[124:127],v[184:187]\ns_add_u32 s16,s14,16777216\ns_addc_u32 s17,s15,0\nM v[188:191],a[92:95],a[124:127],v[188:191]\n" \
"buffer_load_dwordx4 v16,s[28:31],s46 offen lds\nM v[180:183],a[92:95],a[120:123],v[180:183]\ns_add_u32 m0,s50,49152\nM v[176:179],a[88:91],a[120:123],v[176:179]\ns_mov_b32 s46,256\nM v[64:67],a[80:83],a[96:99],v[64:67]\ns_lshr_b32 s21,s63,5\nM v[68:71],a[84:87],a[96:99],v[68:71]\n" \
"buffer_load_dwordx4 v17,s[28:31],s46 offen lds\nM v[76:79],a[84:87],a[100:103],v[76:79]\ns_add_u32 m0,s50,53248\nM v[72:75],a[80:83],a[100:103],v[72:75]\ns_mov_b64 s[4:5],s[90:91]\nM v[88:91],a[80:83],a[108:111],v[88:91]\ns_waitcnt vmcnt(9) lgkmcnt(0)\nM v[92:95],a[84:87],a[108:111],v[92:95]\n" \
"s_add_u32 s63,s63,79691776\ns_barrier\nds_read_b64_tr_b16 a[32:33],v9 offset:32768\nM v[84:87],a[84:87],a[104:107],v[84:87]\nbuffer_load_dwordx4 v18,s[28:31],s46 offen lds\nM v[80:83],a[80:83],a[104:107],v[80:83]\ns_add_u32 m0,s50,57344\nds_read_b64_tr_b16 a[34:35],v10 offset:32768\n" \
"M v[96:99],a[80:83],a[112:115],v[96:99]\ns_add_u32 s72,s4,s63\ns_addc_u32 s73,s5,0\nds_read_b64_tr_b16 a[36:37],v11 offset:32768\nds_read_b64_tr_b16 a[38:39],v12 offset:32768\nM v[100:103],a[84:87],a[112:115],v[100:103]\ns_mov_b32 s63,65536\nds_read_b128 a[24:27],v5 offset:12288\n" \
"M v[108:111],a[84:87],a[116:119],v[108:111]\nbuffer_load_dwordx4 v19,s[28:31],s46 offen lds\nM v[104:107],a[80:83],a[116:119],v[104:107]\ns_add_u32 m0,s50,61440\nM v[120:123],a[80:83],a[124:127],v[120:123]\ns_cmp_lt_u32 %2,2\ns_cselect_b32 s63,32768,s63\nds_read_b128 a[28:31],v5 offset:14336\n" \
"M v[124:127],a[84:87],a[124:127],v[124:127]\ns_lshl_b32 s56,%2,14\nds_read_b64_tr_b16 a[40:41],v9 offset:33280\nds_read_b64_tr_b16 a[42:43],v10 offset:33280\nM v[116:119],a[84:87],a[120:123],v[116:119]\nbuffer_load_dwordx4 v20,s[28:31],s46 offen lds\nM v[112:115],a[80:83],a[120:123],v[112:115]\nLG4_%=:\n" \
"s_cbranch_execz LHR0_%=\ns_add_u32 m0,s22,0\nM v[192:195],a[64:67],a[96:99],v[192:195]\ns_mov_b32 s46,384\nds_read_b64_tr_b16 a[44:45],v11 offset:33280\nds_read_b64_tr_b16 a[46:47],v12 offset:33280\nM v[196:199],a[68:71],a[96:99],v[196:199]\ns_add_u32 s56,s56,s63\n" \
"ds_read_b64_tr_b16 a[48:49],v9 offset:33792\nM v[204:207],a[68:71],a[100:103],v[204:207]\nbuffer_load_dwordx4 v21,s[32:35],s47 offen offset:0 lds\nM v[200:203],a[64:67],a[100:103],v[200:203]\ns_add_u32 m0,s22,896\nds_read_b64_tr_b16 a[50:51],v10 offset:33792\nM v[216:219],a[64:67],a[108:111],v[216:219]\ns_and_b32 s73,s73,0xffff\n" \
"ds_read_b64_tr_b16 a[52:53],v11 offset:33792\nM v[220:223],a[68:71],a[108:111],v[220:223]\ns_mov_b32 s74,4096\nds_read_b64_tr_b16 a[54:55],v12 offset:33792\nM v[212:215],a[68:71],a[104:107],v[212:215]\nbuffer_load_dwordx4 v21,s[32:35],s47 offen offset:128 lds\nM v[208:211],a[64:67],a[104:107],v[208:211]\ns_add_u32 m0,s22,2048\n" \
"ds_read_b64_tr_b16 a[56:57],v9 offset:34304\nM v[224:227],a[64:67],a[112:115],v[224:227]\ns_mov_b32 s75,0x00020000\nds_read_b64_tr_b16 a[58:59],v10 offset:34304\nM v[228:231],a[68:71],a[112:115],v[228:231]\ns_add_u32 s84,s84,1048576\ns_addc_u32 s85,s85,0\nds_read_b64_tr_b16 a[60:61],v11 offset:34304\n" \
"ds_read_b64_tr_b16 a[62:63],v12 offset:34304\nM v[236:239],a[68:71],a[116:119],v[236:239]\nbuffer_load_dwordx4 v21,s[92:95],s47 offen offset:0 lds\nM v[232:235],a[64:67],a[116:119],v[232:235]\ns_add_u32 m0,s22,2944\nM v[248:251],a[64:67],a[124:127],v[248:251]\ns_add_u32 s21,s21,67108864\nds_read_b128 a[16:19],v5 offset:8192\n" \
"M v[252:255],a[68:71],a[124:127],v[252:255]\ns_mov_b32 s53,s22\nds_read_b128 a[20:23],v5 offset:10240\nM v[244:247],a[68:71],a[120:123],v[244:247]\ns_mov_b64 exec,0xff\nglobal_load_dword v0,v1,s[84:85]\ns_mov_b64 exec,-1\nM v[240:243],a[64:67],a[120:123],v[240:243]\n" \
"ds_read_b128 a[0:3],v5 offset:0\nds_read_b128 a[4:7],v5 offset:2048\nM a[128:131],a[72:75],a[96:99],a[128:131]\ns_mov_b64 exec,0xff00\nglobal_load_dword v0,v1,s[84:85]\ns_mov_b64 exec,-1\nM a[132:135],a[76:79],a[96:99],a[132:135]\ns_add_u32 s84,s84,1048576\n" \
"s_addc_u32 s85,s85,0\nds_read_b128 a[8:11],v5 offset:4096\nds_read_b128 a[12:15],v5 offset:6144\nM a[140:143],a[76:79],a[100:103],a[140:143]\nglobal_load_dwordx4 v[46:49],v4,s[86:87] offset:0\nM a[136:139],a[72:75],a[100:103],a[136:139]\nds_read_b128 a[88:91],v6 offset:12288\nds_read_b128 a[92:95],v6 offset:14336\n" \
"M a[152:155],a[72:75],a[108:111],a[152:155]\nds_read_b128 a[64:67],v6 offset:0\nM a[156:159],a[76:79],a[108:111],a[156:159]\nds_read_b128 a[68:71],v6 offset:2048\nM a[148:151],a[76:79],a[104:107],a[148:151]\nglobal_load_dwordx4 v[50:53],v4,s[86:87] offset:1024\nM a[144:147],a[72:75],a[104:107],a[144:147]\nds_read_b128 a[80:83],v6 offset:8192\n" \
"ds_read_b128 a[84:87],v6 offset:10240\nM a[160:163],a[72:75],a[112:115],a[160:163]\nM a[164:167],a[76:79],a[112:115],a[164:167]\nM a[172:175],a[76:79],a[116:119],a[172:175]\nglobal_load_dwordx4 v[30:33],v26,s[64:65] offset:0\nM a[168:171],a[72:75],a[116:119],a[168:171]\nM a[184:187],a[72:75],a[124:127],a[184:187]\nM a[188:191],a[76:79],a[124:127],a[188:191]\n" \
"M a[180:183],a[76:79],a[120:123],a[180:183]\nglobal_load_dwordx4 v[34:37],v26,s[64:65] offset:1024\nM a[176:179],a[72:75],a[120:123],a[176:179]\ns_waitcnt lgkmcnt(0)\nds_read_b128 a[72:75],v6 offset:4096\nds_read_b128 a[76:79],v6 offset:6144\nM v[128:131],a[24:27],a[32:35],v[128:131]\nM v[132:135],a[28:31],a[32:35],v[132:135]\n" \
"M v[140:143],a[28:31],a[36:39],v[140:143]\nglobal_load_dwordx4 v[38:41],v26,s[64:65] offset:2048\nM v[136:139],a[24:27],a[36:39],v[136:139]\nM v[152:155],a[24:27],a[44:47],v[152:155]\nM v[156:159],a[28:31],a[44:47],v[156:159]\nM v[148:151],a[28:31],a[40:43],v[148:151]\nglobal_load_dwordx4 v[42:45],v26,s[64:65] offset:3072\nM v[144:147],a[24:27],a[40:43],v[144:147]\n" \
"M v[160:163],a[24:27],a[48:51],v[160:163]\nM v[164:167],a[28:31],a[48:51],v[164:167]\nM v[172:175],a[28:31],a[52:55],v[172:175]\nbuffer_load_dwordx4 v21,s[92:95],s47 offen offset:128 lds\nM v[168:171],a[24:27],a[52:55],v[168:171]\ns_add_u32 m0,s22,16384\nM v[184:187],a[24:27],a[60:63],v[184:187]\ns_waitcnt vmcnt(20) lgkmcnt(14)\n" \
"M v[188:191],a[28:31],a[60:63],v[188:191]\ns_mov_b32 s47,1310720\ns_barrier\nds_read_b64_tr_b16 a[96:97],v9 offset:49152\nM v[180:183],a[28:31],a[56:59],v[180:183]\nbuffer_load_dwordx4 v21,s[32:35],s47 offen offset:0 lds\nM v[176:179],a[24:27],a[56:59],v[176:179]\ns_add_u32 m0,s22,17280\n" \
"ds_read_b64_tr_b16 a[98:99],v10 offset:49152\nM v[64:67],a[16:19],a[32:35],v[64:67]\nds_read_b64_tr_b16 a[100:101],v11 offset:49152\nds_read_b64_tr_b16 a[102:103],v12 offset:49152\nM v[68:71],a[20:23],a[32:35],v[68:71]\nds_read_b64_tr_b16 a[104:105],v9 offset:49664\nM v[76:79],a[20:23],a[36:39],v[76:79]\nbuffer_load_dwordx4 v21,s[32:35],s47 offen offset:128 lds\n" \
"M v[72:75],a[16:19],a[36:39],v[72:75]\ns_add_u32 m0,s22,18432\nds_read_b64_tr_b16 a[106:107],v10 offset:49664\nM v[88:91],a[16:19],a[44:47],v[88:91]\nds_read_b64_tr_b16 a[108:109],v11 offset:49664\nM v[92:95],a[20:23],a[44:47],v[92:95]\nds_read_b64_tr_b16 a[110:111],v12 offset:49664\nM v[84:87],a[20:23],a[40:43],v[84:87]\n" \
"buffer_load_dwordx4 v21,s[92:95],s47 offen offset:0 lds\nM v[80:83],a[16:19],a[40:43],v[80:83]\ns_add_u32 m0,s22,19328\nds_read_b64_tr_b16 a[112:113],v9 offset:50176\nM v[96:99],a[16:19],a[48:51],v[96:99]\nds_read_b64_tr_b16 a[114:115],v10 offset:50176\nM v[100:103],a[20:23],a[48:51],v[100:103]\nds_read_b64_tr_b16 a[116:117],v11 offset:50176\n" \
"ds_read_b64_tr_b16 a[118:119],v12 offset:50176\nM v[108:111],a[20:23],a[52:55],v[108:111]\nbuffer_load_dwordx4 v21,s[92:95],s47 offen offset:128 lds\nM v[104:107],a[16:19],a[52:55],v[104:107]\ns_add_u32 m0,s50,65536\nM v[120:123],a[16:19],a[60:63],v[120:123]\ns_mov_b32 s47,1572864\nds_read_b64_tr_b16 a[120:121],v9 offset:50688\n" \
"ds_read_b64_tr_b16 a[122:123],v10 offset:50688\nM v[124:127],a[20:23],a[60:63],v[124:127]\nds_read_b64_tr_b16 a[124:125],v11 offset:50688\nM v[116:119],a[20:23],a[56:59],v[116:119]\nds_read_b64_tr_b16 a[126:127],v12 offset:50688\nM v[112:115],a[16:19],a[56:59],v[112:115]\nM v[192:195],a[0:3],a[32:35],v[192:195]\nM v[196:199],a[4:7],a[32:35],v[196:199]\n" \
"M v[204:207],a[4:7],a[36:39],v[204:207]\nM v[200:203],a[0:3],a[36:39],v[200:203]\nM v[216:219],a[0:3],a[44:47],v[216:219]\nbuffer_load_dwordx4 v13,s[28:31],s46 offen lds\nM v[220:223],a[4:7],a[44:47],v[220:223]\ns_add_u32 m0,s50,69632\nM v[212:215],a[4:7],a[40:43],v[212:215]\nM v[208:211],a[0:3],a[40:43],v[208:211]\n" \
"M v[224:227],a[0:3],a[48:51],v[224:227]\nbuffer_load_dwordx4 v14,s[28:31],s46 offen lds\nM v[228:231],a[4:7],a[48:51],v[228:231]\ns_add_u32 m0,s50,73728\nM v[236:239],a[4:7],a[52:55],v[236:239]\nM v[232:235],a[0:3],a[52:55],v[232:235]\nM v[248:251],a[0:3],a[60:63],v[248:251]\nbuffer_load_dwordx4 v15,s[28:31],s46 offen lds\n" \
"M v[252:255],a[4:7],a[60:63],v[252:255]\ns_add_u32 m0,s50,77824\nM v[244:247],a[4:7],a[56:59],v[244:247]\nM v[240:243],a[0:3],a[56:59],v[240:243]\nM a[128:131],a[8:11],a[32:35],a[128:131]\nbuffer_load_dwordx4 v16,s[28:31],s46 offen lds\nM a[132:135],a[12:15],a[32:35],a[132:135]\ns_add_u32 m0,s50,81920\n" \
"M a[140:143],a[12:15],a[36:39],a[140:143]\ns_mov_b32 s46,384\nM a[136:139],a[8:11],a[36:39],a[136:139]\nM a[152:155],a[8:11],a[44:47],a[152:155]\nbuffer_load_dwordx4 v17,s[28:31],s46 offen lds\nM a[156:159],a[12:15],a[44:47],a[156:159]\ns_add_u32 m0,s50,86016\nM a[148:151],a[12:15],a[40:43],a[148:151]\n" \
"M a[144:147],a[8:11],a[40:43],a[144:147]\nM a[160:163],a[8:11],a[48:51],a[160:163]\nbuffer_load_dwordx4 v18,s[28:31],s46 offen lds\nM a[164:167],a[12:15],a[48:51],a[164:167]\ns_add_u32 m0,s50,90112\nM a[172:175],a[12:15],a[52:55],a[172:175]\nM a[168:171],a[8:11],a[52:55],a[168:171]\nM a[184:187],a[8:11],a[60:63],a[184:187]\n" \
"buffer_load_dwordx4 v19,s[28:31],s46 offen lds\nM a[188:191],a[12:15],a[60:63],a[188:191]\ns_add_u32 m0,s50,94208\nM a[180:183],a[12:15],a[56:59],a[180:183]\nM a[176:179],a[8:11],a[56:59],a[176:179]\ns_waitcnt lgkmcnt(0)\nM v[128:131],a[88:91],a[96:99],v[128:131]\nbuffer_load_dwordx4 v20,s[28:31],s46 offen lds\n" \
"M v[132:135],a[92:95],a[96:99],v[132:135]\ns_add_u32 m0,s22,32768\nM v[140:143],a[92:95],a[100:103],v[140:143]\ns_waitcnt vmcnt(12) lgkmcnt(0)\nM v[136:139],a[88:91],a[100:103],v[136:139]\ns_mov_b32 s46,512\ns_barrier\nds_read_b64_tr_b16 a[32:33],v9 offset:0\n" \
"M v[152:155],a[88:91],a[108:111],v[152:155]\nbuffer_load_dwordx4 v21,s[32:35],s47 offen offset:0 lds\nM v[156:159],a[92:95],a[108:111],v[156:159]\ns_add_u32 m0,s22,33664\nds_read_b64_tr_b16 a[34:35],v10 offset:0\nM v[148:151],a[92:95],a[104:107],v[148:151]\nds_read_b64_tr_b16 a[36:37],v11 offset:0\nds_read_b64_tr_b16 a[38:39],v12 offset:0\n" \
"M v[144:147],a[88:91],a[104:107],v[144:147]\nds_read_b64_tr_b16 a[40:41],v9 offset:512\nM v[160:163],a[88:91],a[112:115],v[160:163]\nbuffer_load_dwordx4 v21,s[32:35],s47 offen offset:128 lds\nM v[164:167],a[92:95],a[112:115],v[164:167]\ns_add_u32 m0,s22,34816\nds_read_b64_tr_b16 a[42:43],v10 offset:512\nM v[172:175],a[92:95],a[116:119],v[172:175]\n" \
"ds_read_b64_tr_b16 a[44:45],v11 offset:512\nM v[168:171],a[88:91],a[116:119],v[168:171]\nds_read_b64_tr_b16 a[46:47],v12 offset:512\nM v[184:187],a[88:91],a[124:127],v[184:187]\nbuffer_load_dwordx4 v21,s[92:95],s47 offen offset:0 lds\nM v[188:191],a[92:95],a[124:127],v[188:191]\ns_add_u32 m0,s22,35712\nds_read_b64_tr_b16 a[48:49],v9 offset:1024\n" \
"M v[180:183],a[92:95],a[120:123],v[180:183]\nds_read_b64_tr_b16 a[50:51],v10 offset:1024\nM v[176:179],a[88:91],a[120:123],v[176:179]\nds_read_b64_tr_b16 a[52:53],v11 offset:1024\nds_read_b64_tr_b16 a[54:55],v12 offset:1024\nM v[64:67],a[80:83],a[96:99],v[64:67]\nbuffer_load_dwordx4 v21,s[92:95],s47 offen offset:128 lds\nM v[68:71],a[84:87],a[96:99],v[68:71]\n" \
"s_add_u32 m0,s22,49152\nM v[76:79],a[84:87],a[100:103],v[76:79]\ns_mov_b32 s47,1835008\nds_read_b64_tr_b16 a[56:57],v9 offset:1536\nds_read_b64_tr_b16 a[58:59],v10 offset:1536\nM v[72:75],a[80:83],a[100:103],v[72:75]\nds_read_b64_tr_b16 a[60:61],v11 offset:1536\nM v[88:91],a[80:83],a[108:111],v[88:91]\n" \
"buffer_load_dwordx4 v21,s[32:35],s47 offen offset:0 lds\nM v[92:95],a[84:87],a[108:111],v[92:95]\ns_add_u32 m0,s22,50048\nds_read_b64_tr_b16 a[62:63],v12 offset:1536\nM v[84:87],a[84:87],a[104:107],v[84:87]\nds_read_b128 a[24:27],v5 offset:45056\nM v[80:83],a[80:83],a[104:107],v[80:83]\nds_read_b128 a[28:31],v5 offset:47104\n" \
"M v[96:99],a[80:83],a[112:115],v[96:99]\nbuffer_load_dwordx4 v21,s[32:35],s47 offen offset:128 lds\nM v[100:103],a[84:87],a[112:115],v[100:103]\ns_add_u32 m0,s22,51200\nM v[108:111],a[84:87],a[116:119],v[108:111]\nds_read_b128 a[16:19],v5 offset:40960\nM v[104:107],a[80:83],a[116:119],v[104:107]\nds_read_b128 a[20:23],v5 offset:43008\n" \
"M v[120:123],a[80:83],a[124:127],v[120:123]\nbuffer_load_dwordx4 v21,s[92:95],s47 offen offset:0 lds\nM v[124:127],a[84:87],a[124:127],v[124:127]\ns_add_u32 m0,s22,52096\nds_read_b128 a[0:3],v5 offset:32768\nM v[116:119],a[84:87],a[120:123],v[116:119]\nds_read_b128 a[4:7],v5 offset:34816\nM v[112:115],a[80:83],a[120:123],v[112:115]\n" \
"ds_read_b128 a[8:11],v5 offset:36864\nM v[192:195],a[64:67],a[96:99],v[192:195]\nbuffer_load_dwordx4 v21,s[92:95],s47 offen offset:128 lds\nM v[196:199],a[68:71],a[96:99],v[196:199]\ns_add_u32 m0,s50,0\nM v[204:207],a[68:71],a[100:103],v[204:207]\ns_mov_b32 s47,2097152\nds_read_b128 a[12:15],v5 offset:38912\n" \
"M v[200:203],a[64:67],a[100:103],v[200:203]\nds_read_b128 a[80:83],v6 offset:40960\nM v[216:219],a[64:67],a[108:111],v[216:219]\nbuffer_load_dwordx4 v13,s[28:31],s46 offen lds\nM v[220:223],a[68:71],a[108:111],v[220:223]\ns_add_u32 m0,s50,4096\nds_read_b128 a[84:87],v6 offset:43008\nM v[212:215],a[68:71],a[104:107],v[212:215]\n" \
"ds_read_b128 a[88:91],v6 offset:45056\nM v[208:211],a[64:67],a[104:107],v[208:211]\nds_read_b128 a[92:95],v6 offset:47104\nM v[224:227],a[64:67],a[112:115],v[224:227]\nbuffer_load_dwordx4 v14,s[28:31],s46 offen lds\nM v[228:231],a[68:71],a[112:115],v[228:231]\ns_add_u32 m0,s50,8192\nM v[236:239],a[68:71],a[116:119],v[236:239]\n" \
"M v[232:235],a[64:67],a[116:119],v[232:235]\nM v[248:251],a[64:67],a[124:127],v[248:251]\nM v[252:255],a[68:71],a[124:127],v[252:255]\nbuffer_load_dwordx4 v15,s[28:31],s46 offen lds\nM v[244:247],a[68:71],a[120:123],v[244:247]\ns_add_u32 m0,s50,12288\nM v[240:243],a[64:67],a[120:123],v[240:243]\nds_read_b128 a[64:67],v6 offset:32768\n" \
"ds_read_b128 a[68:71],v6 offset:34816\nM a[128:131],a[72:75],a[96:99],a[128:131]\nM a[132:135],a[76:79],a[96:99],a[132:135]\nbuffer_load_dwordx4 v16,s[28:31],s46 offen lds\nM a[140:143],a[76:79],a[100:103],a[140:143]\ns_add_u32 m0,s50,16384\nM a[136:139],a[72:75],a[100:103],a[136:139]\ns_mov_b32 s46,512\n" \
"M a[152:155],a[72:75],a[108:111],a[152:155]\nM a[156:159],a[76:79],a[108:111],a[156:159]\nM a[148:151],a[76:79],a[104:107],a[148:151]\ns_waitcnt vmcnt(20) lgkmcnt(14)\nM a[144:147],a[72:75],a[104:107],a[144:147]\ns_barrier\nds_read_b64_tr_b16 a[96:97],v9 offset:16384\nM a[160:163],a[72:75],a[112:115],a[160:163]\n" \
"buffer_load_dwordx4 v17,s[28:31],s46 offen lds\nM a[164:167],a[76:79],a[112:115],a[164:167]\ns_add_u32 m0,s50,20480\nds_read_b64_tr_b16 a[98:99],v10 offset:16384\nM a[172:175],a[76:79],a[116:119],a[172:175]\nds_read_b64_tr_b16 a[100:101],v11 offset:16384\nds_read_b64_tr_b16 a[102:103],v12 offset:16384\nM a[168:171],a[72:75],a[116:119],a[168:171]\n" \
"ds_read_b64_tr_b16 a[104:105],v9 offset:16896\nM a[184:187],a[72:75],a[124:127],a[184:187]\nbuffer_load_dwordx4 v18,s[28:31],s46 offen lds\nM a[188:191],a[76:79],a[124:127],a[188:191]\ns_add_u32 m0,s50,24576\nds_read_b64_tr_b16 a[106:107],v10 offset:16896\nM a[180:183],a[76:79],a[120:123],a[180:183]\nds_read_b64_tr_b16 a[108:109],v11 offset:16896\n" \
"M a[176:179],a[72:75],a[120:123],a[176:179]\ns_waitcnt lgkmcnt(5)\nds_read_b128 a[72:75],v6 offset:36864\nM v[128:131],a[24:27],a[32:35],v[128:131]\nbuffer_load_dwordx4 v19,s[28:31],s46 offen lds\nM v[132:135],a[28:31],a[32:35],v[132:135]\ns_add_u32 m0,s50,28672\nds_read_b128 a[76:79],v6 offset:38912\n" \
"M v[140:143],a[28:31],a[36:39],v[140:143]\nds_read_b64_tr_b16 a[110:111],v12 offset:16896\nM v[136:139],a[24:27],a[36:39],v[136:139]\nds_read_b64_tr_b16 a[112:113],v9 offset:17408\nM v[152:155],a[24:27],a[44:47],v[152:155]\nbuffer_load_dwordx4 v20,s[28:31],s46 offen lds\nM v[156:159],a[28:31],a[44:47],v[156:159]\ns_add_u32 m0,s22,0\n" \
"ds_read_b64_tr_b16 a[114:115],v10 offset:17408\nM v[148:151],a[28:31],a[40:43],v[148:151]\ns_mov_b32 s46,640\nds_read_b64_tr_b16 a[116:117],v11 offset:17408\nM v[144:147],a[24:27],a[40:43],v[144:147]\nds_read_b64_tr_b16 a[118:119],v12 offset:17408\nM v[160:163],a[24:27],a[48:51],v[160:163]\nbuffer_load_dwordx4 v21,s[32:35],s47 offen offset:0 lds\n" \
"M v[164:167],a[28:31],a[48:51],v[164:167]\ns_add_u32 m0,s22,896\nds_read_b64_tr_b16 a[120:121],v9 offset:17920\nM v[172:175],a[28:31],a[52:55],v[172:175]\nds_read_b64_tr_b16 a[122:123],v10 offset:17920\nM v[168:171],a[24:27],a[52:55],v[168:171]\nds_read_b64_tr_b16 a[124:125],v11 offset:17920\nds_read_b64_tr_b16 a[126:127],v12 offset:17920\n" \
"M v[184:187],a[24:27],a[60:63],v[184:187]\nbuffer_load_dwordx4 v21,s[32:35],s47 offen offset:128 lds\nM v[188:191],a[28:31],a[60:63],v[188:191]\ns_add_u32 m0,s22,2048\nM v[180:183],a[28:31],a[56:59],v[180:183]\nM v[176:179],a[24:27],a[56:59],v[176:179]\nM v[64:67],a[16:19],a[32:35],v[64:67]\nM v[68:71],a[20:23],a[32:35],v[68:71]\n" \
"buffer_load_dwordx4 v21,s[92:95],s47 offen offset:0 lds\nM v[76:79],a[20:23],a[36:39],v[76:79]\ns_add_u32 m0,s22,2944\nM v[72:75],a[16:19],a[36:39],v[72:75]\nM v[88:91],a[16:19],a[44:47],v[88:91]\nM v[92:95],a[20:23],a[44:47],v[92:95]\nbuffer_load_dwordx4 v21,s[92:95],s47 offen offset:128 lds\nM v[84:87],a[20:23],a[40:43],v[84:87]\n" \
"s_add_u32 m0,s22,16384\nM v[80:83],a[16:19],a[40:43],v[80:83]\ns_mov_b32 s47,2359296\nM v[96:99],a[16:19],a[48:51],v[96:99]\nM v[100:103],a[20:23],a[48:51],v[100:103]\ns_mov_b64 exec,0xff\nglobal_load_dword v0,v1,s[84:85]\ns_mov_b64 exec,-1\n" \
"M v[108:111],a[20:23],a[52:55],v[108:111]\nM v[104:107],a[16:19],a[52:55],v[104:107]\ns_mov_b64 exec,0xff00\nglobal_load_dword v0,v1,s[84:85]\ns_mov_b64 exec,-1\nM v[120:123],a[16:19],a[60:63],v[120:123]\ns_add_u32 s84,s84,1048576\ns_addc_u32 s85,s85,0\n" \
"M v[124:127],a[20:23],a[60:63],v[124:127]\nglobal_load_dwordx4 a[230:233],v27,s[70:71] offset:0\nM v[116:119],a[20:23],a[56:59],v[116:119]\nM v[112:115],a[16:19],a[56:59],v[112:115]\nM v[192:195],a[0:3],a[32:35],v[192:195]\nM v[196:199],a[4:7],a[32:35],v[196:199]\nglobal_load_dwordx4 a[234:237],v27,s[70:71] offset:16\nM v[204:207],a[4:7],a[36:39],v[204:207]\n" \
"s_mov_b32 s70,4096\nM v[200:203],a[0:3],a[36:39],v[200:203]\ns_mov_b32 s71,0x00020000\nM v[216:219],a[0:3],a[44:47],v[216:219]\nM v[220:223],a[4:7],a[44:47],v[220:223]\nbuffer_load_dwordx2 a[192:193],v54,s[40:43],0 offen offset:0 nt\nM v[212:215],a[4:7],a[40:43],v[212:215]\nM v[208:211],a[0:3],a[40:43],v[208:211]\n" \
"M v[224:227],a[0:3],a[48:51],v[224:227]\nM v[228:231],a[4:7],a[48:51],v[228:231]\nbuffer_load_dwordx2 a[194:195],v54,s[40:43],0 offen offset:2048 nt\nM v[236:239],a[4:7],a[52:55],v[236:239]\nM v[232:235],a[0:3],a[52:55],v[232:235]\nM v[248:251],a[0:3],a[60:63],v[248:251]\nM v[252:255],a[4:7],a[60:63],v[252:255]\nbuffer_load_dwordx2 a[196:197],v55,s[40:43],0 offen offset:0 nt\n" \
"M v[244:247],a[4:7],a[56:59],v[244:247]\nM v[240:243],a[0:3],a[56:59],v[240:243]\nM a[128:131],a[8:11],a[32:35],a[128:131]\ns_waitcnt vmcnt(40)\nv_lshlrev_b32 v25,16,v46\nv_lshlrev_b32 v26,16,v30\nM a[132:135],a[12:15],a[32:35],a[132:135]\nbuffer_load_dwordx2 a[198:199],v59,s[40:43],s52 offen offset:0 nt\n" \
"M a[140:143],a[12:15],a[36:39],a[140:143]\nv_and_b32 v46,0xffff0000,v46\nv_mul_f32 v23,v26,v25\nM a[136:139],a[8:11],a[36:39],a[136:139]\nv_and_b32 v30,0xffff0000,v30\nv_lshlrev_b32 v26,16,v38\nM a[152:155],a[8:11],a[44:47],a[152:155]\nv_fmac_f32 v23,v30,v46\n" \
"v_mul_f32 v24,v26,v25\nM a[156:159],a[12:15],a[44:47],a[156:159]\nbuffer_load_dwordx2 a[214:215],v59,s[80:83],s52 offen offset:0 nt\nM a[148:151],a[12:15],a[40:43],a[148:151]\ns_waitcnt vmcnt(25) lgkmcnt(0)\nv_lshlrev_b32 v25,16,v47\nv_lshlrev_b32 v26,16,v31\nM a[144:147],a[8:11],a[40:43],a[144:147]\n" \
"s_barrier\nds_read_b64_tr_b16 a[32:33],v9 offset:32768\nM a[160:163],a[8:11],a[48:51],a[160:163]\nds_read_b64_tr_b16 a[34:35],v10 offset:32768\nds_read_b64_tr_b16 a[36:37],v11 offset:32768\nM a[164:167],a[12:15],a[48:51],a[164:167]\nbuffer_load_dwordx2 a[200:201],v59,s[40:43],s52 offen offset:2048 nt\nM a[172:175],a[12:15],a[52:55],a[172:175]\n" \
"ds_read_b64_tr_b16 a[38:39],v12 offset:32768\nds_read_b64_tr_b16 a[40:41],v9 offset:33280\nM a[168:171],a[8:11],a[52:55],a[168:171]\nds_read_b64_tr_b16 a[42:43],v10 offset:33280\nv_and_b32 v38,0xffff0000,v38\nM a[184:187],a[8:11],a[60:63],a[184:187]\nds_read_b64_tr_b16 a[44:45],v11 offset:33280\nds_read_b64_tr_b16 a[46:47],v12 offset:33280\n" \
"M a[188:191],a[12:15],a[60:63],a[188:191]\nbuffer_load_dwordx2 a[216:217],v59,s[80:83],s52 offen offset:2048 nt\nM a[180:183],a[12:15],a[56:59],a[180:183]\ns_mov_b32 s52,4096\nds_read_b64_tr_b16 a[48:49],v9 offset:33792\nds_read_b64_tr_b16 a[50:51],v10 offset:33792\nM a[176:179],a[8:11],a[56:59],a[176:179]\nds_read_b64_tr_b16 a[52:53],v11 offset:33792\n" \
"v_fmac_f32 v23,v26,v25\nM v[128:131],a[88:91],a[96:99],v[128:131]\nds_read_b64_tr_b16 a[54:55],v12 offset:33792\nds_read_b64_tr_b16 a[56:57],v9 offset:34304\nM v[132:135],a[92:95],a[96:99],v[132:135]\nbuffer_load_dwordx2 a[202:203],v59,s[40:43],s52 offen offset:0 nt\nM v[140:143],a[92:95],a[100:103],v[140:143]\nds_read_b64_tr_b16 a[58:59],v10 offset:34304\n" \
"ds_read_b64_tr_b16 a[60:61],v11 offset:34304\nM v[136:139],a[88:91],a[100:103],v[136:139]\nds_read_b64_tr_b16 a[62:63],v12 offset:34304\nv_fmac_f32 v24,v38,v46\nM v[152:155],a[88:91],a[108:111],v[152:155]\nds_read_b128 a[24:27],v7 offset:12288\nv_and_b32 v47,0xffff0000,v47\nM v[156:159],a[92:95],a[108:111],v[156:159]\n" \
"buffer_load_dwordx2 a[218:219],v59,s[80:83],s52 offen offset:0 nt\nM v[148:151],a[92:95],a[104:107],v[148:151]\nds_read_b128 a[28:31],v7 offset:14336\nds_read_b128 a[16:19],v7 offset:8192\nM v[144:147],a[88:91],a[104:107],v[144:147]\nds_read_b128 a[20:23],v7 offset:10240\nv_and_b32 v31,0xffff0000,v31\nM v[160:163],a[88:91],a[112:115],v[160:163]\n" \
"ds_read_b128 a[0:3],v7 offset:0\nv_lshlrev_b32 v26,16,v39\nM v[164:167],a[92:95],a[112:115],v[164:167]\nbuffer_load_dwordx2 a[204:205],v59,s[40:43],s52 offen offset:2048 nt\nM v[172:175],a[92:95],a[116:119],v[172:175]\nds_read_b128 a[4:7],v7 offset:2048\nds_read_b128 a[8:11],v7 offset:4096\nM v[168:171],a[88:91],a[116:119],v[168:171]\n" \
"ds_read_b128 a[12:15],v7 offset:6144\nv_fmac_f32 v23,v31,v47\nM v[184:187],a[88:91],a[124:127],v[184:187]\nv_fmac_f32 v24,v26,v25\nv_lshlrev_b32 v25,16,v48\nM v[188:191],a[92:95],a[124:127],v[188:191]\nbuffer_load_dwordx2 a[220:221],v59,s[80:83],s52 offen offset:2048 nt\nM v[180:183],a[92:95],a[120:123],v[180:183]\n" \
"s_mov_b32 s52,8192\nv_lshlrev_b32 v26,16,v32\nv_and_b32 v39,0xffff0000,v39\nM v[176:179],a[88:91],a[120:123],v[176:179]\nds_read_b128 a[88:91],v8 offset:12288\nds_read_b128 a[92:95],v8 offset:14336\nM v[64:67],a[80:83],a[96:99],v[64:67]\nv_fmac_f32 v23,v26,v25\n" \
"v_fmac_f32 v24,v39,v47\nM v[68:71],a[84:87],a[96:99],v[68:71]\nv_and_b32 v48,0xffff0000,v48\nv_and_b32 v32,0xffff0000,v32\nM v[76:79],a[84:87],a[100:103],v[76:79]\nv_lshlrev_b32 v26,16,v40\nv_fmac_f32 v23,v32,v48\nM v[72:75],a[80:83],a[100:103],v[72:75]\n" \
"v_fmac_f32 v24,v26,v25\nv_lshlrev_b32 v25,16,v49\nM v[88:91],a[80:83],a[108:111],v[88:91]\nv_lshlrev_b32 v26,16,v33\nv_and_b32 v40,0xffff0000,v40\nM v[92:95],a[84:87],a[108:111],v[92:95]\nv_fmac_f32 v23,v26,v25\nv_fmac_f32 v24,v40,v48\n" \
"M v[84:87],a[84:87],a[104:107],v[84:87]\nLG5_%=:\ns_cbranch_execz LHR0_%=\nv_and_b32 v49,0xffff0000,v49\nv_and_b32 v33,0xffff0000,v33\nM v[80:83],a[80:83],a[104:107],v[80:83]\nv_lshlrev_b32 v26,16,v41\nv_fmac_f32 v23,v33,v49\n" \
"M v[96:99],a[80:83],a[112:115],v[96:99]\nv_fmac_f32 v24,v26,v25\nv_lshlrev_b32 v25,16,v50\nM v[100:103],a[84:87],a[112:115],v[100:103]\nv_lshlrev_b32 v26,16,v34\nv_and_b32 v41,0xffff0000,v41\nM v[108:111],a[84:87],a[116:119],v[108:111]\nv_fmac_f32 v23,v26,v25\n" \
"v_fmac_f32 v24,v41,v49\nM v[104:107],a[80:83],a[116:119],v[104:107]\nv_and_b32 v50,0xffff0000,v50\nv_and_b32 v34,0xffff0000,v34\nM v[120:123],a[80:83],a[124:127],v[120:123]\nv_lshlrev_b32 v26,16,v42\nv_fmac_f32 v23,v34,v50\nM v[124:127],a[84:87],a[124:127],v[124:127]\n" \
"v_fmac_f32 v24,v26,v25\nv_lshlrev_b32 v25,16,v51\nM v[116:119],a[84:87],a[120:123],v[116:119]\nv_lshlrev_b32 v26,16,v35\nv_and_b32 v42,0xffff0000,v42\nM v[112:115],a[80:83],a[120:123],v[112:115]\nds_read_b128 a[80:83],v8 offset:8192\nds_read_b128 a[84:87],v8 offset:10240\n" \
"M v[192:195],a[64:67],a[96:99],v[192:195]\nv_fmac_f32 v23,v26,v25\nv_fmac_f32 v24,v42,v50\nM v[196:199],a[68:71],a[96:99],v[196:199]\nv_and_b32 v51,0xffff0000,v51\nv_and_b32 v35,0xffff0000,v35\nM v[204:207],a[68:71],a[100:103],v[204:207]\nv_lshlrev_b32 v26,16,v43\n" \
"v_fmac_f32 v23,v35,v51\nM v[200:203],a[64:67],a[100:103],v[200:203]\nv_fmac_f32 v24,v26,v25\nv_lshlrev_b32 v25,16,v52\nM v[216:219],a[64:67],a[108:111],v[216:219]\nv_lshlrev_b32 v26,16,v36\nv_and_b32 v43,0xffff0000,v43\nM v[220:223],a[68:71],a[108:111],v[220:223]\n" \
"v_fmac_f32 v23,v26,v25\nv_fmac_f32 v24,v43,v51\nM v[212:215],a[68:71],a[104:107],v[212:215]\nv_and_b32 v52,0xffff0000,v52\nv_and_b32 v36,0xffff0000,v36\nM v[208:211],a[64:67],a[104:107],v[208:211]\nv_lshlrev_b32 v26,16,v44\nv_fmac_f32 v23,v36,v52\n" \
"M v[224:227],a[64:67],a[112:115],v[224:227]\nv_fmac_f32 v24,v26,v25\nv_lshlrev_b32 v25,16,v53\nM v[228:231],a[68:71],a[112:115],v[228:231]\nv_lshlrev_b32 v26,16,v37\nv_and_b32 v44,0xffff0000,v44\nM v[236:239],a[68:71],a[116:119],v[236:239]\nv_fmac_f32 v23,v26,v25\n" \
"v_fmac_f32 v24,v44,v52\nM v[232:235],a[64:67],a[116:119],v[232:235]\nv_lshlrev_b32 v26,16,v45\nv_and_b32 v53,0xffff0000,v53\nM v[248:251],a[64:67],a[124:127],v[248:251]\nv_and_b32 v45,0xffff0000,v45\nv_fmac_f32 v24,v26,v25\nM v[252:255],a[68:71],a[124:127],v[252:255]\n" \
"v_fmac_f32 v24,v45,v53\nv_and_b32 v37,0xffff0000,v37\nM v[244:247],a[68:71],a[120:123],v[244:247]\nv_fmac_f32 v23,v37,v53\nM v[240:243],a[64:67],a[120:123],v[240:243]\nds_read_b128 a[64:67],v8 offset:0\nds_read_b128 a[68:71],v8 offset:2048\nM a[128:131],a[72:75],a[96:99],a[128:131]\n" \
"v_permlane32_swap_b32 v23,v24\nM a[132:135],a[76:79],a[96:99],a[132:135]\nv_add_f32 v23,v23,v24\nv_mov_b32 v24,v23\nM a[140:143],a[76:79],a[100:103],a[140:143]\ns_nop 0\nv_permlane16_swap_b32 v23,v24\nM a[136:139],a[72:75],a[100:103],a[136:139]\n" \
"v_add_f32 v23,v23,v24\ns_nop 1\nv_add_f32 v23,v23,v23 row_ror:8 row_mask:0xf bank_mask:0xf\nM a[152:155],a[72:75],a[108:111],a[152:155]\ns_nop 0\nv_add_f32 v23,v23,v23 row_ror:4 row_mask:0xf bank_mask:0xf\ns_nop 1\nv_add_f32 v23,v23,v23 row_ror:2 row_mask:0xf bank_mask:0xf\n" \
"M a[156:159],a[76:79],a[108:111],a[156:159]\ns_nop 0\nv_add_f32 v23,v23,v23 row_ror:1 row_mask:0xf bank_mask:0xf\nC v23,v23,v23\nM a[148:151],a[76:79],a[104:107],a[148:151]\ns_waitcnt vmcnt(27) lgkmcnt(14)\nv_and_b32 v23,0xffff0000,v23\nv_mul_f32 v23,0xbfb8aa3b,v23\n" \
"M a[144:147],a[72:75],a[104:107],a[144:147]\ns_barrier\nds_read_b64_tr_b16 a[96:97],v9 offset:49152\nM a[160:163],a[72:75],a[112:115],a[160:163]\nds_read_b64_tr_b16 a[98:99],v10 offset:49152\nds_read_b64_tr_b16 a[100:101],v11 offset:49152\nM a[164:167],a[76:79],a[112:115],a[164:167]\nds_read_b64_tr_b16 a[102:103],v12 offset:49152\n" \
"v_add_u32 v24,992,v4\nM a[172:175],a[76:79],a[116:119],a[172:175]\nds_read_b64_tr_b16 a[104:105],v9 offset:49664\nds_read_b64_tr_b16 a[106:107],v10 offset:49664\nM a[168:171],a[72:75],a[116:119],a[168:171]\nds_read_b64_tr_b16 a[108:109],v11 offset:49664\nM a[184:187],a[72:75],a[124:127],a[184:187]\nds_read_b64_tr_b16 a[110:111],v12 offset:49664\n" \
"M a[188:191],a[76:79],a[124:127],a[188:191]\nds_read_b64_tr_b16 a[112:113],v9 offset:50176\nds_read_b64_tr_b16 a[114:115],v10 offset:50176\nM a[180:183],a[76:79],a[120:123],a[180:183]\nds_read_b64_tr_b16 a[116:117],v11 offset:50176\nM a[176:179],a[72:75],a[120:123],a[176:179]\ns_waitcnt lgkmcnt(7)\nds_read_b128 a[72:75],v8 offset:4096\n" \
"M v[128:131],a[24:27],a[32:35],v[128:131]\nds_read_b128 a[76:79],v8 offset:6144\nM v[132:135],a[28:31],a[32:35],v[132:135]\nbuffer_load_dwordx4 v21,s[32:35],s47 offen offset:0 lds\nM v[140:143],a[28:31],a[36:39],v[140:143]\ns_add_u32 m0,s22,17280\nds_read_b64_tr_b16 a[118:119],v12 offset:50176\nM v[136:139],a[24:27],a[36:39],v[136:139]\n" \
"ds_read_b64_tr_b16 a[120:121],v9 offset:50688\nM v[152:155],a[24:27],a[44:47],v[152:155]\nds_read_b64_tr_b16 a[122:123],v10 offset:50688\nM v[156:159],a[28:31],a[44:47],v[156:159]\nbuffer_load_dwordx4 v21,s[32:35],s47 offen offset:128 lds\nM v[148:151],a[28:31],a[40:43],v[148:151]\ns_add_u32 m0,s22,18432\nds_read_b64_tr_b16 a[124:125],v11 offset:50688\n" \
"M v[144:147],a[24:27],a[40:43],v[144:147]\nds_read_b64_tr_b16 a[126:127],v12 offset:50688\nM v[160:163],a[24:27],a[48:51],v[160:163]\nv_exp_f32 v23,v23\nM v[164:167],a[28:31],a[48:51],v[164:167]\nbuffer_load_dwordx4 v21,s[92:95],s47 offen offset:0 lds\nM v[172:175],a[28:31],a[52:55],v[172:175]\ns_add_u32 m0,s22,19328\n" \
"v_add_f32 v23,1.0,v23\nM v[168:171],a[24:27],a[52:55],v[168:171]\nv_rcp_f32 v23,v23\nM v[184:187],a[24:27],a[60:63],v[184:187]\nC v23,v23,v23\nv_and_b32 v23,0xffff0000,v23\nM v[188:191],a[28:31],a[60:63],v[188:191]\nbuffer_load_dwordx4 v21,s[92:95],s47 offen offset:128 lds\n" \
"M v[180:183],a[28:31],a[56:59],v[180:183]\ns_add_u32 m0,s50,32768\nM v[176:179],a[24:27],a[56:59],v[176:179]\ns_mov_b32 s47,2621440\nM v[64:67],a[16:19],a[32:35],v[64:67]\nM v[68:71],a[20:23],a[32:35],v[68:71]\nbuffer_load_dwordx4 v13,s[28:31],s46 offen lds\nM v[76:79],a[20:23],a[36:39],v[76:79]\n" \
"s_add_u32 m0,s50,36864\nM v[72:75],a[16:19],a[36:39],v[72:75]\nM v[88:91],a[16:19],a[44:47],v[88:91]\nM v[92:95],a[20:23],a[44:47],v[92:95]\nbuffer_load_dwordx4 v14,s[28:31],s46 offen lds\nM v[84:87],a[20:23],a[40:43],v[84:87]\ns_add_u32 m0,s50,40960\nM v[80:83],a[16:19],a[40:43],v[80:83]\n" \
"M v[96:99],a[16:19],a[48:51],v[96:99]\nM v[100:103],a[20:23],a[48:51],v[100:103]\nbuffer_load_dwordx4 v15,s[28:31],s46 offen lds\nM v[108:111],a[20:23],a[52:55],v[108:111]\ns_add_u32 m0,s50,45056\nM v[104:107],a[16:19],a[52:55],v[104:107]\nM v[120:123],a[16:19],a[60:63],v[120:123]\nM v[124:127],a[20:23],a[60:63],v[124:127]\n" \
"buffer_load_dwordx4 v16,s[28:31],s46 offen lds\nM v[116:119],a[20:23],a[56:59],v[116:119]\ns_add_u32 m0,s50,49152\nM v[112:115],a[16:19],a[56:59],v[112:115]\ns_mov_b32 s46,640\nM v[192:195],a[0:3],a[32:35],v[192:195]\nM v[196:199],a[4:7],a[32:35],v[196:199]\nbuffer_load_dwordx4 v17,s[28:31],s46 offen lds\n" \
"M v[204:207],a[4:7],a[36:39],v[204:207]\ns_add_u32 m0,s50,53248\nM v[200:203],a[0:3],a[36:39],v[200:203]\nM v[216:219],a[0:3],a[44:47],v[216:219]\nM v[220:223],a[4:7],a[44:47],v[220:223]\nbuffer_load_dwordx4 v18,s[28:31],s46 offen lds\nM v[212:215],a[4:7],a[40:43],v[212:215]\ns_add_u32 m0,s50,57344\n" \
"M v[208:211],a[0:3],a[40:43],v[208:211]\nM v[224:227],a[0:3],a[48:51],v[224:227]\nM v[228:231],a[4:7],a[48:51],v[228:231]\nbuffer_load_dwordx4 v19,s[28:31],s46 offen lds\nM v[236:239],a[4:7],a[52:55],v[236:239]\ns_add_u32 m0,s50,61440\nM v[232:235],a[0:3],a[52:55],v[232:235]\nM v[248:251],a[0:3],a[60:63],v[248:251]\n" \
"M v[252:255],a[4:7],a[60:63],v[252:255]\nbuffer_load_dwordx4 v20,s[28:31],s46 offen lds\nM v[244:247],a[4:7],a[56:59],v[244:247]\ns_add_u32 m0,s22,32768\nM v[240:243],a[0:3],a[56:59],v[240:243]\ns_mov_b32 s46,768\nM a[128:131],a[8:11],a[32:35],a[128:131]\nM a[132:135],a[12:15],a[32:35],a[132:135]\n" \
"buffer_load_dwordx4 v21,s[32:35],s47 offen offset:0 lds\nM a[140:143],a[12:15],a[36:39],a[140:143]\ns_add_u32 m0,s22,33664\nM a[136:139],a[8:11],a[36:39],a[136:139]\nM a[152:155],a[8:11],a[44:47],a[152:155]\nM a[156:159],a[12:15],a[44:47],a[156:159]\nbuffer_load_dwordx4 v21,s[32:35],s47 offen offset:128 lds\nM a[148:151],a[12:15],a[40:43],a[148:151]\n" \
"s_waitcnt vmcnt(29) lgkmcnt(0)\nM a[144:147],a[8:11],a[40:43],a[144:147]\ns_add_u32 m0,s22,34816\ns_barrier\nds_read_b64_tr_b16 a[32:33],v9 offset:0\nM a[160:163],a[8:11],a[48:51],a[160:163]\nds_read_b64_tr_b16 a[34:35],v10 offset:0\nds_read_b64_tr_b16 a[36:37],v11 offset:0\n" \
"M a[164:167],a[12:15],a[48:51],a[164:167]\nbuffer_load_dwordx4 v21,s[92:95],s47 offen offset:0 lds\nM a[172:175],a[12:15],a[52:55],a[172:175]\ns_add_u32 m0,s22,35712\nds_read_b64_tr_b16 a[38:39],v12 offset:0\nM a[168:171],a[8:11],a[52:55],a[168:171]\nds_read_b64_tr_b16 a[40:41],v9 offset:512\nM a[184:187],a[8:11],a[60:63],a[184:187]\n" \
"ds_read_b64_tr_b16 a[42:43],v10 offset:512\nM a[188:191],a[12:15],a[60:63],a[188:191]\nbuffer_load_dwordx4 v21,s[92:95],s47 offen offset:128 lds\nM a[180:183],a[12:15],a[56:59],a[180:183]\ns_add_u32 m0,s22,49152\nds_read_b64_tr_b16 a[44:45],v11 offset:512\nM a[176:179],a[8:11],a[56:59],a[176:179]\ns_mov_b32 s47,2883584\n" \
"ds_read_b64_tr_b16 a[46:47],v12 offset:512\nM v[128:131],a[88:91],a[96:99],v[128:131]\nds_read_b64_tr_b16 a[48:49],v9 offset:1024\nds_read_b64_tr_b16 a[50:51],v10 offset:1024\nM v[132:135],a[92:95],a[96:99],v[132:135]\nbuffer_load_dwordx4 v21,s[32:35],s47 offen offset:0 lds\nM v[140:143],a[92:95],a[100:103],v[140:143]\ns_add_u32 m0,s22,50048\n" \
"M v[136:139],a[88:91],a[100:103],v[136:139]\nds_read_b64_tr_b16 a[52:53],v11 offset:1024\nds_read_b64_tr_b16 a[54:55],v12 offset:1024\nM v[152:155],a[88:91],a[108:111],v[152:155]\nds_read_b64_tr_b16 a[56:57],v9 offset:1536\nM v[156:159],a[92:95],a[108:111],v[156:159]\nbuffer_load_dwordx4 v21,s[32:35],s47 offen offset:128 lds\nM v[148:151],a[92:95],a[104:107],v[148:151]\n" \
"s_add_u32 m0,s22,51200\nds_read_b64_tr_b16 a[58:59],v10 offset:1536\nM v[144:147],a[88:91],a[104:107],v[144:147]\nds_read_b64_tr_b16 a[60:61],v11 offset:1536\nM v[160:163],a[88:91],a[112:115],v[160:163]\nds_read_b64_tr_b16 a[62:63],v12 offset:1536\nM v[164:167],a[92:95],a[112:115],v[164:167]\nbuffer_load_dwordx4 v21,s[92:95],s47 offen offset:0 lds\n" \
"M v[172:175],a[92:95],a[116:119],v[172:175]\ns_add_u32 m0,s22,52096\nds_read_b128 a[24:27],v5 offset:12288\nM v[168:171],a[88:91],a[116:119],v[168:171]\nds_read_b128 a[28:31],v5 offset:14336\nM v[184:187],a[88:91],a[124:127],v[184:187]\nds_read_b128 a[16:19],v5 offset:8192\nM v[188:191],a[92:95],a[124:127],v[188:191]\n" \
"buffer_store_dword v23,v61,s[36:39],0 offen sc1\nM v[180:183],a[92:95],a[120:123],v[180:183]\nds_read_b128 a[20:23],v5 offset:10240\nds_read_b128 a[0:3],v5 offset:0\nM v[176:179],a[88:91],a[120:123],v[176:179]\nbuffer_load_dwordx4 v21,s[92:95],s47 offen offset:128 lds\nM v[64:67],a[80:83],a[96:99],v[64:67]\ns_add_u32 m0,s50,65536\n" \
"v_add_u32 v23,-32,v4\nM v[68:71],a[84:87],a[96:99],v[68:71]\ns_mov_b32 s47,3145728\nds_read_b128 a[4:7],v5 offset:2048\nM v[76:79],a[84:87],a[100:103],v[76:79]\nds_read_b128 a[8:11],v5 offset:4096\nM v[72:75],a[80:83],a[100:103],v[72:75]\nds_read_b128 a[12:15],v5 offset:6144\n" \
"M v[88:91],a[80:83],a[108:111],v[88:91]\nbuffer_load_dwordx4 v13,s[28:31],s46 offen lds\nM v[92:95],a[84:87],a[108:111],v[92:95]\ns_add_u32 m0,s50,69632\nds_read_b128 a[88:91],v6 offset:12288\nM v[84:87],a[84:87],a[104:107],v[84:87]\nds_read_b128 a[92:95],v6 offset:14336\nM v[80:83],a[80:83],a[104:107],v[80:83]\n" \
"M v[96:99],a[80:83],a[112:115],v[96:99]\nbuffer_load_dwordx4 v14,s[28:31],s46 offen lds\nM v[100:103],a[84:87],a[112:115],v[100:103]\ns_add_u32 m0,s50,73728\nM v[108:111],a[84:87],a[116:119],v[108:111]\nM v[104:107],a[80:83],a[116:119],v[104:107]\nM v[120:123],a[80:83],a[124:127],v[120:123]\nM v[124:127],a[84:87],a[124:127],v[124:127]\n" \
"buffer_load_dwordx4 v15,s[28:31],s46 offen lds\nM v[116:119],a[84:87],a[120:123],v[116:119]\ns_add_u32 m0,s50,77824\nM v[112:115],a[80:83],a[120:123],v[112:115]\nds_read_b128 a[80:83],v6 offset:8192\nds_read_b128 a[84:87],v6 offset:10240\nM v[192:195],a[64:67],a[96:99],v[192:195]\nM v[196:199],a[68:71],a[96:99],v[196:199]\n" \
"buffer_load_dwordx4 v16,s[28:31],s46 offen lds\nM v[204:207],a[68:71],a[100:103],v[204:207]\ns_add_u32 m0,s50,81920\nM v[200:203],a[64:67],a[100:103],v[200:203]\ns_mov_b32 s46,768\nM v[216:219],a[64:67],a[108:111],v[216:219]\nM v[220:223],a[68:71],a[108:111],v[220:223]\nM v[212:215],a[68:71],a[104:107],v[212:215]\n" \
"buffer_load_dwordx2 a[206:207],v59,s[40:43],s52 offen offset:0 nt\nM v[208:211],a[64:67],a[104:107],v[208:211]\nM v[224:227],a[64:67],a[112:115],v[224:227]\nM v[228:231],a[68:71],a[112:115],v[228:231]\nM v[236:239],a[68:71],a[116:119],v[236:239]\nbuffer_load_dwordx2 a[222:223],v59,s[80:83],s52 offen offset:0 nt\nM v[232:235],a[64:67],a[116:119],v[232:235]\nM v[248:251],a[64:67],a[124:127],v[248:251]\n" \
"M v[252:255],a[68:71],a[124:127],v[252:255]\nM v[244:247],a[68:71],a[120:123],v[244:247]\nbuffer_load_dwordx2 a[208:209],v59,s[40:43],s52 offen offset:2048 nt\nM v[240:243],a[64:67],a[120:123],v[240:243]\nds_read_b128 a[64:67],v6 offset:0\nds_read_b128 a[68:71],v6 offset:2048\nM a[128:131],a[72:75],a[96:99],a[128:131]\nM a[132:135],a[76:79],a[96:99],a[132:135]\n" \
"M a[140:143],a[76:79],a[100:103],a[140:143]\nbuffer_load_dwordx2 a[224:225],v59,s[80:83],s52 offen offset:2048 nt\nM a[136:139],a[72:75],a[100:103],a[136:139]\ns_mov_b32 s52,12288\nM a[152:155],a[72:75],a[108:111],a[152:155]\nM a[156:159],a[76:79],a[108:111],a[156:159]\nM a[148:151],a[76:79],a[104:107],a[148:151]\ns_waitcnt vmcnt(25) lgkmcnt(14)\n" \
"M a[144:147],a[72:75],a[104:107],a[144:147]\ns_waitcnt vmcnt(29)\ns_barrier\nds_read_b64_tr_b16 a[96:97],v9 offset:16384\nM a[160:163],a[72:75],a[112:115],a[160:163]\nbuffer_load_dwordx2 a[210:211],v59,s[40:43],s52 offen offset:0 nt\nM a[164:167],a[76:79],a[112:115],a[164:167]\nds_read_b64_tr_b16 a[98:99],v10 offset:16384\n" \
"ds_read_b64_tr_b16 a[100:101],v11 offset:16384\nM a[172:175],a[76:79],a[116:119],a[172:175]\nds_read_b64_tr_b16 a[102:103],v12 offset:16384\nv_accvgpr_read_b32 v45,a231\nM a[168:171],a[72:75],a[116:119],a[168:171]\nds_read_b64_tr_b16 a[104:105],v9 offset:16896\nds_read_b64_tr_b16 a[106:107],v10 offset:16896\nM a[184:187],a[72:75],a[124:127],a[184:187]\n" \
"buffer_load_dwordx2 a[226:227],v59,s[80:83],s52 offen offset:0 nt\nM a[188:191],a[76:79],a[124:127],a[188:191]\nds_read_b64_tr_b16 a[108:109],v11 offset:16896\nds_read_b64_tr_b16 a[110:111],v12 offset:16896\nM a[180:183],a[76:79],a[120:123],a[180:183]\nds_read_b64_tr_b16 a[112:113],v9 offset:17408\nv_accvgpr_read_b32 v49,a233\nM a[176:179],a[72:75],a[120:123],a[176:179]\n" \
"s_waitcnt lgkmcnt(6)\nds_read_b128 a[72:75],v6 offset:4096\nv_lshlrev_b32 v44,16,v45\nM v[128:131],a[24:27],a[32:35],v[128:131]\nbuffer_load_dwordx2 a[212:213],v59,s[40:43],s52 offen offset:2048 nt\nM v[132:135],a[28:31],a[32:35],v[132:135]\nds_read_b128 a[76:79],v6 offset:6144\nds_read_b64_tr_b16 a[114:115],v10 offset:17408\n" \
"M v[140:143],a[28:31],a[36:39],v[140:143]\nds_read_b64_tr_b16 a[116:117],v11 offset:17408\nv_accvgpr_read_b32 v36,a192\nM v[136:139],a[24:27],a[36:39],v[136:139]\nds_read_b64_tr_b16 a[118:119],v12 offset:17408\nv_accvgpr_read_b32 v43,a230\nM v[152:155],a[24:27],a[44:47],v[152:155]\nbuffer_load_dwordx2 a[228:229],v59,s[80:83],s52 offen offset:2048 nt\n" \
"M v[156:159],a[28:31],a[44:47],v[156:159]\nds_read_b64_tr_b16 a[120:121],v9 offset:17920\nds_read_b64_tr_b16 a[122:123],v10 offset:17920\nM v[148:151],a[28:31],a[40:43],v[148:151]\nds_read_b64_tr_b16 a[124:125],v11 offset:17920\nv_accvgpr_read_b32 v37,a194\nM v[144:147],a[24:27],a[40:43],v[144:147]\nds_read_b64_tr_b16 a[126:127],v12 offset:17920\n" \
"v_lshlrev_b32 v48,16,v49\nM v[160:163],a[24:27],a[48:51],v[160:163]\ns_mov_b64 exec,0xff\nglobal_load_dword v0,v1,s[84:85]\ns_mov_b64 exec,-1\nM v[164:167],a[28:31],a[48:51],v[164:167]\nv_lshlrev_b32 v30,16,v36\nv_and_b32 v45,0xffff0000,v45\n" \
"M v[172:175],a[28:31],a[52:55],v[172:175]\ns_mov_b64 exec,0xff00\nglobal_load_dword v0,v1,s[84:85]\ns_mov_b64 exec,-1\nM v[168:171],a[24:27],a[52:55],v[168:171]\nv_accvgpr_read_b32 v47,a232\nv_lshlrev_b32 v31,16,v37\nM v[184:187],a[24:27],a[60:63],v[184:187]\n" \
"s_mov_b64 exec,0xff\nglobal_load_dword v0,v1,s[76:77]\ns_mov_b64 exec,-1\nM v[188:191],a[28:31],a[60:63],v[188:191]\nv_accvgpr_read_b32 v38,a196\nv_lshlrev_b32 v42,16,v43\nM v[180:183],a[28:31],a[56:59],v[180:183]\ns_mov_b64 exec,0xff00\n" \
"global_load_dword v0,v1,s[76:77]\ns_mov_b64 exec,-1\nM v[176:179],a[24:27],a[56:59],v[176:179]\ns_add_u32 s76,s76,2097152\ns_addc_u32 s77,s77,0\nv_and_b32 v49,0xffff0000,v49\nv_and_b32 v36,0xffff0000,v36\nM v[64:67],a[16:19],a[32:35],v[64:67]\n" \
"v_accvgpr_read_b32 v39,a198\nv_mul_f32 v25,v30,v45\nM v[68:71],a[20:23],a[32:35],v[68:71]\nv_lshlrev_b32 v46,16,v47\nv_and_b32 v37,0xffff0000,v37\nM v[76:79],a[20:23],a[36:39],v[76:79]\nv_lshlrev_b32 v32,16,v38\nv_and_b32 v43,0xffff0000,v43\n" \
"M v[72:75],a[16:19],a[36:39],v[72:75]\nv_mul_f32 v26,v36,v49\nv_lshlrev_b32 v33,16,v39\nM v[88:91],a[16:19],a[44:47],v[88:91]\nv_fmac_f32 v25,v31,v44\nv_and_b32 v47,0xffff0000,v47\nM v[92:95],a[20:23],a[44:47],v[92:95]\nv_and_b32 v38,0xffff0000,v38\n" \
"v_fmac_f32 v26,v37,v48\nM v[84:87],a[20:23],a[40:43],v[84:87]\nv_and_b32 v39,0xffff0000,v39\nv_fmac_f32 v25,v32,v43\nM v[80:83],a[16:19],a[40:43],v[80:83]\nv_fmac_f32 v26,v38,v47\nv_fmac_f32 v25,v33,v42\nM v[96:99],a[16:19],a[48:51],v[96:99]\n" \
"v_fmac_f32 v26,v39,v46\nv_accvgpr_read_b32 v28,a214\nM v[100:103],a[20:23],a[48:51],v[100:103]\nC v26,v25,v26\nv_lshlrev_b32 v27,16,v28\nM v[108:111],a[20:23],a[52:55],v[108:111]\nv_lshlrev_b32 v25,16,v26\nv_and_b32 v28,0xffff0000,v28\n" \
"M v[104:107],a[16:19],a[52:55],v[104:107]\nv_and_b32 v26,0xffff0000,v26\nv_mul_f32 v29,v25,v27\nM v[120:123],a[16:19],a[60:63],v[120:123]\nv_mul_f32 v58,v26,v28\nv_accvgpr_read_b32 v40,a200\nM v[124:127],a[20:23],a[60:63],v[124:127]\nv_mul_f32 v25,v31,v45\n" \
"v_mul_f32 v26,v37,v49\nM v[116:119],a[20:23],a[56:59],v[116:119]\nv_lshlrev_b32 v34,16,v40\nv_fmac_f32 v25,v32,v44\nM v[112:115],a[16:19],a[56:59],v[112:115]\nv_fmac_f32 v26,v38,v48\nv_fmac_f32 v25,v33,v43\nM v[192:195],a[0:3],a[32:35],v[192:195]\n" \
"v_and_b32 v40,0xffff0000,v40\nv_fmac_f32 v26,v39,v47\nM v[196:199],a[4:7],a[32:35],v[196:199]\nv_fmac_f32 v25,v34,v42\nv_fmac_f32 v26,v40,v46\nM v[204:207],a[4:7],a[36:39],v[204:207]\nv_accvgpr_read_b32 v28,a216\nC v26,v25,v26\n" \
"M v[200:203],a[0:3],a[36:39],v[200:203]\nv_lshlrev_b32 v27,16,v28\nv_lshlrev_b32 v25,16,v26\nM v[216:219],a[0:3],a[44:47],v[216:219]\nv_and_b32 v28,0xffff0000,v28\nv_and_b32 v26,0xffff0000,v26\nM v[220:223],a[4:7],a[44:47],v[220:223]\nv_mul_f32 v25,v25,v27\n" \
"v_mul_f32 v26,v26,v28\nM v[212:215],a[4:7],a[40:43],v[212:215]\nC v53,v25,v29\nC v57,v26,v58\nM v[208:211],a[0:3],a[40:43],v[208:211]\nv_accvgpr_read_b32 v41,a202\nv_lshlrev_b32 v35,16,v41\nM v[224:227],a[0:3],a[48:51],v[224:227]\n" \
"v_mul_f32 v25,v32,v45\nv_mul_f32 v26,v38,v49\nM v[228:231],a[4:7],a[48:51],v[228:231]\nv_fmac_f32 v25,v33,v44\nv_and_b32 v41,0xffff0000,v41\nM v[236:239],a[4:7],a[52:55],v[236:239]\nv_fmac_f32 v26,v39,v48\nv_fmac_f32 v25,v34,v43\n" \
"M v[232:235],a[0:3],a[52:55],v[232:235]\nv_fmac_f32 v26,v40,v47\nv_fmac_f32 v25,v35,v42\nM v[248:251],a[0:3],a[60:63],v[248:251]\nv_fmac_f32 v26,v41,v46\nv_accvgpr_read_b32 v28,a218\nM v[252:255],a[4:7],a[60:63],v[252:255]\nC v26,v25,v26\n" \
"v_lshlrev_b32 v27,16,v28\nM v[244:247],a[4:7],a[56:59],v[244:247]\nv_lshlrev_b32 v25,16,v26\nv_and_b32 v28,0xffff0000,v28\nM v[240:243],a[0:3],a[56:59],v[240:243]\nv_and_b32 v26,0xffff0000,v26\nv_mul_f32 v29,v25,v27\nM a[128:131],a[8:11],a[32:35],a[128:131]\n" \
"v_mul_f32 v58,v26,v28\nv_accvgpr_read_b32 v36,a204\nM a[132:135],a[12:15],a[32:35],a[132:135]\nv_mul_f32 v25,v33,v45\nv_lshlrev_b32 v30,16,v36\nM a[140:143],a[12:15],a[36:39],a[140:143]\nv_mul_f32 v26,v39,v49\nv_fmac_f32 v25,v34,v44\n" \
"M a[136:139],a[8:11],a[36:39],a[136:139]\nv_fmac_f32 v26,v40,v48\nv_and_b32 v36,0xffff0000,v36\nM a[152:155],a[8:11],a[44:47],a[152:155]\nbuffer_load_dwordx4 v17,s[28:31],s46 offen lds\nM a[156:159],a[12:15],a[44:47],a[156:159]\ns_add_u32 m0,s50,86016\nv_fmac_f32 v25,v35,v43\n" \
"v_fmac_f32 v26,v41,v47\nM a[148:151],a[12:15],a[40:43],a[148:151]\ns_waitcnt vmcnt(22) lgkmcnt(0)\nv_fmac_f32 v25,v30,v42\nv_fmac_f32 v26,v36,v46\nM a[144:147],a[8:11],a[40:43],a[144:147]\ns_barrier\nds_read_b64_tr_b16 a[32:33],v9 offset:32768\n" \
"M a[160:163],a[8:11],a[48:51],a[160:163]\nbuffer_load_dwordx4 v18,s[28:31],s46 offen lds\nM a[164:167],a[12:15],a[48:51],a[164:167]\ns_add_u32 m0,s50,90112\nds_read_b64_tr_b16 a[34:35],v10 offset:32768\nv_accvgpr_read_b32 v28,a220\nM a[172:175],a[12:15],a[52:55],a[172:175]\nds_read_b64_tr_b16 a[36:37],v11 offset:32768\n" \
"ds_read_b64_tr_b16 a[38:39],v12 offset:32768\nM a[168:171],a[8:11],a[52:55],a[168:171]\nds_read_b64_tr_b16 a[40:41],v9 offset:33280\nC v26,v25,v26\nM a[184:187],a[8:11],a[60:63],a[184:187]\nbuffer_load_dwordx4 v19,s[28:31],s46 offen lds\nM a[188:191],a[12:15],a[60:63],a[188:191]\ns_add_u32 m0,s50,94208\n" \
"ds_read_b64_tr_b16 a[42:43],v10 offset:33280\nv_lshlrev_b32 v27,16,v28\nM a[180:183],a[12:15],a[56:59],a[180:183]\nds_read_b64_tr_b16 a[44:45],v11 offset:33280\nv_lshlrev_b32 v25,16,v26\nM a[176:179],a[8:11],a[56:59],a[176:179]\nLG6_%=:\ns_cbranch_execz LHR0_%=\n" \
"ds_read_b64_tr_b16 a[46:47],v12 offset:33280\nv_and_b32 v28,0xffff0000,v28\nM v[128:131],a[88:91],a[96:99],v[128:131]\nbuffer_load_dwordx4 v20,s[28:31],s46 offen lds\nM v[132:135],a[92:95],a[96:99],v[132:135]\ns_add_u32 m0,s22,0\nds_read_b64_tr_b16 a[48:49],v9 offset:33792\nv_and_b32 v26,0xffff0000,v26\n" \
"M v[140:143],a[92:95],a[100:103],v[140:143]\ns_mov_b32 s46,896\nds_read_b64_tr_b16 a[50:51],v10 offset:33792\nv_mul_f32 v25,v25,v27\nM v[136:139],a[88:91],a[100:103],v[136:139]\nds_read_b64_tr_b16 a[52:53],v11 offset:33792\nds_read_b64_tr_b16 a[54:55],v12 offset:33792\nM v[152:155],a[88:91],a[108:111],v[152:155]\n" \
"buffer_load_dwordx4 v21,s[32:35],s47 offen offset:0 lds\nM v[156:159],a[92:95],a[108:111],v[156:159]\ns_add_u32 m0,s22,896\nv_mul_f32 v26,v26,v28\nC v52,v25,v29\nM v[148:151],a[92:95],a[104:107],v[148:151]\nds_read_b64_tr_b16 a[56:57],v9 offset:34304\nds_read_b64_tr_b16 a[58:59],v10 offset:34304\n" \
"M v[144:147],a[88:91],a[104:107],v[144:147]\nds_read_b64_tr_b16 a[60:61],v11 offset:34304\nC v56,v26,v58\nM v[160:163],a[88:91],a[112:115],v[160:163]\nbuffer_load_dwordx4 v21,s[32:35],s47 offen offset:128 lds\nM v[164:167],a[92:95],a[112:115],v[164:167]\ns_add_u32 m0,s22,2048\nds_read_b64_tr_b16 a[62:63],v12 offset:34304\n" \
"v_mov_b32 v59,s19\nM v[172:175],a[92:95],a[116:119],v[172:175]\nds_read_b128 a[24:27],v5 offset:45056\nM v[168:171],a[88:91],a[116:119],v[168:171]\nds_read_b128 a[28:31],v5 offset:47104\nM v[184:187],a[88:91],a[124:127],v[184:187]\nbuffer_load_dwordx4 v21,s[92:95],s47 offen offset:0 lds\nM v[188:191],a[92:95],a[124:127],v[188:191]\n" \
"s_add_u32 m0,s22,2944\nM v[180:183],a[92:95],a[120:123],v[180:183]\nds_read_b128 a[16:19],v5 offset:40960\nM v[176:179],a[88:91],a[120:123],v[176:179]\nds_read_b128 a[20:23],v5 offset:43008\nM v[64:67],a[80:83],a[96:99],v[64:67]\nbuffer_load_dwordx4 v21,s[92:95],s47 offen offset:128 lds\nM v[68:71],a[84:87],a[96:99],v[68:71]\n" \
"s_add_u32 m0,s22,16384\nds_read_b128 a[0:3],v5 offset:32768\nM v[76:79],a[84:87],a[100:103],v[76:79]\ns_mov_b32 s47,3407872\nds_read_b128 a[4:7],v5 offset:34816\nM v[72:75],a[80:83],a[100:103],v[72:75]\nds_read_b128 a[8:11],v5 offset:36864\nM v[88:91],a[80:83],a[108:111],v[88:91]\n" \
"buffer_load_dwordx4 v21,s[32:35],s47 offen offset:0 lds\nM v[92:95],a[84:87],a[108:111],v[92:95]\ns_add_u32 m0,s22,17280\nM v[84:87],a[84:87],a[104:107],v[84:87]\nds_read_b128 a[12:15],v5 offset:38912\nM v[80:83],a[80:83],a[104:107],v[80:83]\nds_read_b128 a[88:91],v6 offset:45056\nM v[96:99],a[80:83],a[112:115],v[96:99]\n" \
"buffer_load_dwordx4 v21,s[32:35],s47 offen offset:128 lds\nM v[100:103],a[84:87],a[112:115],v[100:103]\ns_add_u32 m0,s22,18432\nds_read_b128 a[92:95],v6 offset:47104\nM v[108:111],a[84:87],a[116:119],v[108:111]\nM v[104:107],a[80:83],a[116:119],v[104:107]\nM v[120:123],a[80:83],a[124:127],v[120:123]\nbuffer_load_dwordx4 v21,s[92:95],s47 offen offset:0 lds\n" \
"M v[124:127],a[84:87],a[124:127],v[124:127]\ns_add_u32 m0,s22,19328\nM v[116:119],a[84:87],a[120:123],v[116:119]\nM v[112:115],a[80:83],a[120:123],v[112:115]\nds_read_b128 a[80:83],v6 offset:40960\nds_read_b128 a[84:87],v6 offset:43008\nM v[192:195],a[64:67],a[96:99],v[192:195]\nbuffer_load_dwordx4 v21,s[92:95],s47 offen offset:128 lds\n" \
"M v[196:199],a[68:71],a[96:99],v[196:199]\ns_add_u32 m0,s50,0\nM v[204:207],a[68:71],a[100:103],v[204:207]\ns_mov_b32 s47,3670016\nM v[200:203],a[64:67],a[100:103],v[200:203]\nM v[216:219],a[64:67],a[108:111],v[216:219]\nbuffer_load_dwordx4 v13,s[28:31],s46 offen lds\nM v[220:223],a[68:71],a[108:111],v[220:223]\n" \
"s_add_u32 m0,s50,4096\nv_add_u32 v13,s60,v63\nM v[212:215],a[68:71],a[104:107],v[212:215]\nM v[208:211],a[64:67],a[104:107],v[208:211]\nM v[224:227],a[64:67],a[112:115],v[224:227]\nbuffer_load_dwordx4 v14,s[28:31],s46 offen lds\nM v[228:231],a[68:71],a[112:115],v[228:231]\ns_add_u32 m0,s50,8192\n" \
"v_add_u32 v14,0x10000,v13\nM v[236:239],a[68:71],a[116:119],v[236:239]\nM v[232:235],a[64:67],a[116:119],v[232:235]\nM v[248:251],a[64:67],a[124:127],v[248:251]\nbuffer_load_dwordx4 v15,s[28:31],s46 offen lds\nM v[252:255],a[68:71],a[124:127],v[252:255]\ns_add_u32 m0,s50,12288\nv_add_u32 v15,0x10000,v14\n" \
"M v[244:247],a[68:71],a[120:123],v[244:247]\nM v[240:243],a[64:67],a[120:123],v[240:243]\nds_read_b128 a[64:67],v6 offset:32768\nds_read_b128 a[68:71],v6 offset:34816\nM a[128:131],a[72:75],a[96:99],a[128:131]\nbuffer_load_dwordx4 v16,s[28:31],s46 offen lds\nM a[132:135],a[76:79],a[96:99],a[132:135]\ns_add_u32 m0,s50,16384\n" \
"v_add_u32 v16,0x10000,v15\nM a[140:143],a[76:79],a[100:103],a[140:143]\ns_mov_b32 s46,896\nM a[136:139],a[72:75],a[100:103],a[136:139]\nM a[152:155],a[72:75],a[108:111],a[152:155]\nbuffer_load_dwordx4 v17,s[28:31],s46 offen lds\nM a[156:159],a[76:79],a[108:111],a[156:159]\ns_add_u32 m0,s50,20480\n" \
"v_add_u32 v17,s61,v63\nv_mov_b32 v63,0x3f803f80\nM a[148:151],a[76:79],a[104:107],a[148:151]\ns_waitcnt vmcnt(33) lgkmcnt(14)\nM a[144:147],a[72:75],a[104:107],a[144:147]\ns_barrier\nds_read_b64_tr_b16 a[96:97],v9 offset:49152\nM a[160:163],a[72:75],a[112:115],a[160:163]\n" \
"ds_read_b64_tr_b16 a[98:99],v10 offset:49152\nds_read_b64_tr_b16 a[100:101],v11 offset:49152\nM a[164:167],a[76:79],a[112:115],a[164:167]\nbuffer_load_dwordx4 v18,s[28:31],s46 offen lds\nM a[172:175],a[76:79],a[116:119],a[172:175]\ns_add_u32 m0,s50,24576\nds_read_b64_tr_b16 a[102:103],v12 offset:49152\nv_add_u32 v18,0x10000,v17\n" \
"M a[168:171],a[72:75],a[116:119],a[168:171]\nds_read_b64_tr_b16 a[104:105],v9 offset:49664\nM a[184:187],a[72:75],a[124:127],a[184:187]\nds_read_b64_tr_b16 a[106:107],v10 offset:49664\nM a[188:191],a[76:79],a[124:127],a[188:191]\nbuffer_load_dwordx4 v19,s[28:31],s46 offen lds\nM a[180:183],a[76:79],a[120:123],a[180:183]\ns_add_u32 m0,s50,28672\n" \
"ds_read_b64_tr_b16 a[108:109],v11 offset:49664\nv_add_u32 v19,0x10000,v18\nM a[176:179],a[72:75],a[120:123],a[176:179]\ns_waitcnt lgkmcnt(4)\nds_read_b128 a[72:75],v6 offset:36864\nM v[128:131],a[24:27],a[32:35],v[128:131]\nds_read_b128 a[76:79],v6 offset:38912\nM v[132:135],a[28:31],a[32:35],v[132:135]\n" \
"ds_read_b64_tr_b16 a[110:111],v12 offset:49664\nM v[140:143],a[28:31],a[36:39],v[140:143]\nbuffer_load_dwordx4 v20,s[28:31],s46 offen lds\nM v[136:139],a[24:27],a[36:39],v[136:139]\ns_add_u32 m0,s22,32768\nds_read_b64_tr_b16 a[112:113],v9 offset:50176\nv_add_u32 v20,0x10000,v19\nM v[152:155],a[24:27],a[44:47],v[152:155]\n" \
"s_mov_b64 s[28:29],s[64:65]\nds_read_b64_tr_b16 a[114:115],v10 offset:50176\nM v[156:159],a[28:31],a[44:47],v[156:159]\ns_and_b32 s29,s29,0xffff\nds_read_b64_tr_b16 a[116:117],v11 offset:50176\nds_read_b64_tr_b16 a[118:119],v12 offset:50176\nM v[148:151],a[28:31],a[40:43],v[148:151]\nbuffer_load_dwordx4 v21,s[32:35],s47 offen offset:0 lds\n" \
"M v[144:147],a[24:27],a[40:43],v[144:147]\ns_add_u32 m0,s22,33664\nM v[160:163],a[24:27],a[48:51],v[160:163]\ns_mov_b32 s30,4194304\nds_read_b64_tr_b16 a[120:121],v9 offset:50688\nds_read_b64_tr_b16 a[122:123],v10 offset:50688\nM v[164:167],a[28:31],a[48:51],v[164:167]\ns_mov_b32 s46,0\n" \
"ds_read_b64_tr_b16 a[124:125],v11 offset:50688\nM v[172:175],a[28:31],a[52:55],v[172:175]\nds_read_b64_tr_b16 a[126:127],v12 offset:50688\nM v[168:171],a[24:27],a[52:55],v[168:171]\nbuffer_load_dwordx4 v21,s[32:35],s47 offen offset:128 lds\nM v[184:187],a[24:27],a[60:63],v[184:187]\ns_add_u32 m0,s22,34816\nM v[188:191],a[28:31],a[60:63],v[188:191]\n" \
"M v[180:183],a[28:31],a[56:59],v[180:183]\nM v[176:179],a[24:27],a[56:59],v[176:179]\nbuffer_load_dwordx4 v21,s[92:95],s47 offen offset:0 lds\nM v[64:67],a[16:19],a[32:35],v[64:67]\ns_add_u32 m0,s22,35712\nM v[68:71],a[20:23],a[32:35],v[68:71]\nM v[76:79],a[20:23],a[36:39],v[76:79]\nM v[72:75],a[16:19],a[36:39],v[72:75]\n" \
"M v[88:91],a[16:19],a[44:47],v[88:91]\nbuffer_load_dwordx4 v21,s[92:95],s47 offen offset:128 lds\nM v[92:95],a[20:23],a[44:47],v[92:95]\ns_add_u32 m0,s22,49152\nM v[84:87],a[20:23],a[40:43],v[84:87]\ns_mov_b32 s47,3932160\nM v[80:83],a[16:19],a[40:43],v[80:83]\nM v[96:99],a[16:19],a[48:51],v[96:99]\n" \
"M v[100:103],a[20:23],a[48:51],v[100:103]\nM v[108:111],a[20:23],a[52:55],v[108:111]\nM v[104:107],a[16:19],a[52:55],v[104:107]\nM v[120:123],a[16:19],a[60:63],v[120:123]\nM v[124:127],a[20:23],a[60:63],v[124:127]\nM v[116:119],a[20:23],a[56:59],v[116:119]\nM v[112:115],a[16:19],a[56:59],v[112:115]\nM v[192:195],a[0:3],a[32:35],v[192:195]\n" \
"s_mov_b64 exec,0xff\nglobal_load_dword v0,v2,s[88:89]\ns_mov_b64 exec,-1\nM v[196:199],a[4:7],a[32:35],v[196:199]\nM v[204:207],a[4:7],a[36:39],v[204:207]\ns_mov_b64 exec,0xff00\nglobal_load_dword v0,v2,s[88:89]\ns_mov_b64 exec,-1\n" \
"M v[200:203],a[0:3],a[36:39],v[200:203]\ns_add_u32 s88,s88,2048\ns_addc_u32 s89,s89,0\nM v[216:219],a[0:3],a[44:47],v[216:219]\ns_mov_b64 exec,0xff\nglobal_load_dword v0,v2,s[88:89]\ns_mov_b64 exec,-1\nM v[220:223],a[4:7],a[44:47],v[220:223]\n" \
"M v[212:215],a[4:7],a[40:43],v[212:215]\ns_mov_b64 exec,0xff00\nglobal_load_dword v0,v2,s[88:89]\ns_mov_b64 exec,-1\nM v[208:211],a[0:3],a[40:43],v[208:211]\nM v[224:227],a[0:3],a[48:51],v[224:227]\ns_mov_b64 exec,0xff\nglobal_load_dword v0,v1,s[76:77]\n" \
"s_mov_b64 exec,-1\nM v[228:231],a[4:7],a[48:51],v[228:231]\nM v[236:239],a[4:7],a[52:55],v[236:239]\ns_mov_b64 exec,0xff00\nglobal_load_dword v0,v1,s[76:77]\ns_mov_b64 exec,-1\nM v[232:235],a[0:3],a[52:55],v[232:235]\ns_add_u32 s76,s76,2097152\n" \
"s_addc_u32 s77,s77,0\nM v[248:251],a[0:3],a[60:63],v[248:251]\nM v[252:255],a[4:7],a[60:63],v[252:255]\nM v[244:247],a[4:7],a[56:59],v[244:247]\ns_waitcnt vmcnt(34)\nv_accvgpr_read_b32 v37,a206\nv_mul_f32 v25,v34,v45\nM v[240:243],a[0:3],a[56:59],v[240:243]\n" \
"s_mov_b32 s98,79691776\nv_mul_f32 v26,v40,v49\nv_lshlrev_b32 v31,16,v37\nM a[128:131],a[8:11],a[32:35],a[128:131]\ns_mov_b32 s99,79695872\nv_fmac_f32 v25,v35,v44\nv_fmac_f32 v26,v41,v48\nM a[132:135],a[12:15],a[32:35],a[132:135]\n" \
"v_and_b32 v37,0xffff0000,v37\nv_fmac_f32 v25,v30,v43\nM a[140:143],a[12:15],a[36:39],a[140:143]\nv_fmac_f32 v26,v36,v47\nv_fmac_f32 v25,v31,v42\nM a[136:139],a[8:11],a[36:39],a[136:139]\nv_fmac_f32 v26,v37,v46\nv_accvgpr_read_b32 v28,a222\n" \
"M a[152:155],a[8:11],a[44:47],a[152:155]\nC v26,v25,v26\nv_lshlrev_b32 v27,16,v28\nM a[156:159],a[12:15],a[44:47],a[156:159]\nv_lshlrev_b32 v25,16,v26\nv_and_b32 v28,0xffff0000,v28\nM a[148:151],a[12:15],a[40:43],a[148:151]\ns_waitcnt vmcnt(22) lgkmcnt(0)\n" \
"v_and_b32 v26,0xffff0000,v26\nv_mul_f32 v29,v25,v27\nM a[144:147],a[8:11],a[40:43],a[144:147]\ns_barrier\nv_mul_f32 v58,v26,v28\nM a[160:163],a[8:11],a[48:51],a[160:163]\nv_accvgpr_read_b32 v38,a208\nv_mul_f32 v25,v35,v45\n" \
"M a[164:167],a[12:15],a[48:51],a[164:167]\nv_lshlrev_b32 v32,16,v38\nv_mul_f32 v26,v41,v49\nM a[172:175],a[12:15],a[52:55],a[172:175]\nv_fmac_f32 v25,v30,v44\nv_and_b32 v38,0xffff0000,v38\nM a[168:171],a[8:11],a[52:55],a[168:171]\nv_fmac_f32 v26,v36,v48\n" \
"v_fmac_f32 v25,v31,v43\nM a[184:187],a[8:11],a[60:63],a[184:187]\nds_read_b64_tr_b16 a[32:33],v9 offset:0\nds_read_b64_tr_b16 a[34:35],v10 offset:0\nM a[188:191],a[12:15],a[60:63],a[188:191]\nds_read_b64_tr_b16 a[36:37],v11 offset:0\nv_fmac_f32 v26,v37,v47\nM a[180:183],a[12:15],a[56:59],a[180:183]\n" \
"ds_read_b64_tr_b16 a[38:39],v12 offset:0\nds_read_b64_tr_b16 a[40:41],v9 offset:512\nM a[176:179],a[8:11],a[56:59],a[176:179]\nds_read_b64_tr_b16 a[42:43],v10 offset:512\nv_fmac_f32 v25,v32,v42\nM v[128:131],a[88:91],a[96:99],v[128:131]\nv_fmac_f32 v26,v38,v46\nv_accvgpr_read_b32 v28,a224\n" \
"M v[132:135],a[92:95],a[96:99],v[132:135]\nds_read_b64_tr_b16 a[44:45],v11 offset:512\nC v26,v25,v26\nM v[140:143],a[92:95],a[100:103],v[140:143]\nds_read_b64_tr_b16 a[46:47],v12 offset:512\nv_lshlrev_b32 v27,16,v28\nM v[136:139],a[88:91],a[100:103],v[136:139]\nds_read_b64_tr_b16 a[48:49],v9 offset:1024\n" \
"ds_read_b64_tr_b16 a[50:51],v10 offset:1024\nM v[152:155],a[88:91],a[108:111],v[152:155]\nds_read_b64_tr_b16 a[52:53],v11 offset:1024\nv_lshlrev_b32 v25,16,v26\nM v[156:159],a[92:95],a[108:111],v[156:159]\nds_read_b64_tr_b16 a[54:55],v12 offset:1024\nds_read_b64_tr_b16 a[56:57],v9 offset:1536\nM v[148:151],a[92:95],a[104:107],v[148:151]\n" \
"ds_read_b64_tr_b16 a[58:59],v10 offset:1536\nv_and_b32 v28,0xffff0000,v28\nM v[144:147],a[88:91],a[104:107],v[144:147]\nds_read_b64_tr_b16 a[60:61],v11 offset:1536\nv_and_b32 v26,0xffff0000,v26\nM v[160:163],a[88:91],a[112:115],v[160:163]\nds_read_b64_tr_b16 a[62:63],v12 offset:1536\nv_mul_f32 v25,v25,v27\n" \
"M v[164:167],a[92:95],a[112:115],v[164:167]\nds_read_b128 a[24:27],v7 offset:12288\nv_mul_f32 v26,v26,v28\nM v[172:175],a[92:95],a[116:119],v[172:175]\nds_read_b128 a[28:31],v7 offset:14336\nC v51,v25,v29\nM v[168:171],a[88:91],a[116:119],v[168:171]\nC v55,v26,v58\n" \
"v_accvgpr_read_b32 v39,a210\nM v[184:187],a[88:91],a[124:127],v[184:187]\nv_mul_f32 v25,v30,v45\nv_mul_f32 v26,v36,v49\nM v[188:191],a[92:95],a[124:127],v[188:191]\nv_lshlrev_b32 v33,16,v39\nv_fmac_f32 v25,v31,v44\nM v[180:183],a[92:95],a[120:123],v[180:183]\n" \
"v_fmac_f32 v26,v37,v48\nv_and_b32 v39,0xffff0000,v39\nM v[176:179],a[88:91],a[120:123],v[176:179]\nv_fmac_f32 v25,v32,v43\nv_fmac_f32 v26,v38,v47\nM v[64:67],a[80:83],a[96:99],v[64:67]\nv_fmac_f32 v25,v33,v42\nv_fmac_f32 v26,v39,v46\n" \
"M v[68:71],a[84:87],a[96:99],v[68:71]\nv_accvgpr_read_b32 v28,a226\nC v26,v25,v26\nM v[76:79],a[84:87],a[100:103],v[76:79]\nv_lshlrev_b32 v27,16,v28\nv_lshlrev_b32 v25,16,v26\nM v[72:75],a[80:83],a[100:103],v[72:75]\nv_and_b32 v28,0xffff0000,v28\n" \
"v_and_b32 v26,0xffff0000,v26\nM v[88:91],a[80:83],a[108:111],v[88:91]\nv_mul_f32 v29,v25,v27\nv_mul_f32 v58,v26,v28\nM v[92:95],a[84:87],a[108:111],v[92:95]\nv_accvgpr_read_b32 v40,a212\nv_lshlrev_b32 v34,16,v40\nM v[84:87],a[84:87],a[104:107],v[84:87]\n" \
"v_mul_f32 v25,v31,v45\nv_mul_f32 v26,v37,v49\nM v[80:83],a[80:83],a[104:107],v[80:83]\nds_read_b128 a[16:19],v7 offset:8192\nv_fmac_f32 v25,v32,v44\nM v[96:99],a[80:83],a[112:115],v[96:99]\nds_read_b128 a[20:23],v7 offset:10240\nv_and_b32 v40,0xffff0000,v40\n" \
"M v[100:103],a[84:87],a[112:115],v[100:103]\nv_fmac_f32 v26,v38,v48\nv_fmac_f32 v25,v33,v43\nM v[108:111],a[84:87],a[116:119],v[108:111]\nv_fmac_f32 v26,v39,v47\nv_fmac_f32 v25,v34,v42\nM v[104:107],a[80:83],a[116:119],v[104:107]\nv_fmac_f32 v26,v40,v46\n" \
"v_accvgpr_read_b32 v28,a228\nM v[120:123],a[80:83],a[124:127],v[120:123]\nC v26,v25,v26\nv_lshlrev_b32 v27,16,v28\nM v[124:127],a[84:87],a[124:127],v[124:127]\nv_lshlrev_b32 v25,16,v26\nv_mul_f32 v25,v25,v27\nM v[116:119],a[84:87],a[120:123],v[116:119]\n" \
"C v50,v25,v29\nv_accvgpr_read_b32 v45,a235\nM v[112:115],a[80:83],a[120:123],v[112:115]\nv_and_b32 v28,0xffff0000,v28\nv_and_b32 v26,0xffff0000,v26\nM v[192:195],a[64:67],a[96:99],v[192:195]\nv_lshlrev_b32 v44,16,v45\nv_accvgpr_read_b32 v49,a237\n" \
"M v[196:199],a[68:71],a[96:99],v[196:199]\nds_read_b128 a[0:3],v7 offset:0\nv_accvgpr_read_b32 v36,a193\nM v[204:207],a[68:71],a[100:103],v[204:207]\nds_read_b128 a[4:7],v7 offset:2048\nv_accvgpr_read_b32 v37,a195\nM v[200:203],a[64:67],a[100:103],v[200:203]\nv_mul_f32 v26,v26,v28\n" \
"v_accvgpr_read_b32 v43,a234\nM v[216:219],a[64:67],a[108:111],v[216:219]\nv_accvgpr_read_b32 v47,a236\nv_lshlrev_b32 v48,16,v49\nM v[220:223],a[68:71],a[108:111],v[220:223]\nv_and_b32 v45,0xffff0000,v45\nv_lshlrev_b32 v30,16,v36\nM v[212:215],a[68:71],a[104:107],v[212:215]\n" \
"buffer_store_dwordx4 v[50:53],v60,s[36:39],s98 offen offset:0 sc1\nM v[208:211],a[64:67],a[104:107],v[208:211]\ns_mov_b32 s98,79699968\nv_lshlrev_b32 v31,16,v37\nv_accvgpr_read_b32 v38,a197\nM v[224:227],a[64:67],a[112:115],v[224:227]\nC v54,v26,v58\nv_accvgpr_read_b32 v39,a199\n" \
"M v[228:231],a[68:71],a[112:115],v[228:231]\nv_lshlrev_b32 v42,16,v43\nv_and_b32 v49,0xffff0000,v49\nM v[236:239],a[68:71],a[116:119],v[236:239]\nv_lshlrev_b32 v46,16,v47\nv_and_b32 v36,0xffff0000,v36\nM v[232:235],a[64:67],a[116:119],v[232:235]\nv_lshlrev_b32 v32,16,v38\n" \
"v_mul_f32 v25,v30,v45\nM v[248:251],a[64:67],a[124:127],v[248:251]\nds_read_b128 a[8:11],v7 offset:4096\nds_read_b128 a[12:15],v7 offset:6144\nM v[252:255],a[68:71],a[124:127],v[252:255]\nv_and_b32 v37,0xffff0000,v37\nv_lshlrev_b32 v33,16,v39\nM v[244:247],a[68:71],a[120:123],v[244:247]\n" \
"v_and_b32 v43,0xffff0000,v43\nv_mul_f32 v26,v36,v49\nM v[240:243],a[64:67],a[120:123],v[240:243]\nds_read_b128 a[64:67],v8 offset:0\nds_read_b128 a[68:71],v8 offset:2048\nM a[128:131],a[72:75],a[96:99],a[128:131]\nv_and_b32 v47,0xffff0000,v47\nv_and_b32 v38,0xffff0000,v38\n" \
"M a[132:135],a[76:79],a[96:99],a[132:135]\nds_read_b128 a[80:83],v8 offset:8192\nv_fmac_f32 v25,v31,v44\nM a[140:143],a[76:79],a[100:103],a[140:143]\nv_and_b32 v39,0xffff0000,v39\nv_fmac_f32 v26,v37,v48\nM a[136:139],a[72:75],a[100:103],a[136:139]\nv_fmac_f32 v25,v32,v43\n" \
"v_fmac_f32 v26,v38,v47\nM a[152:155],a[72:75],a[108:111],a[152:155]\nv_fmac_f32 v25,v33,v42\nv_fmac_f32 v26,v39,v46\nM a[156:159],a[76:79],a[108:111],a[156:159]\nds_read_b128 a[84:87],v8 offset:10240\nv_accvgpr_read_b32 v28,a215\nM a[148:151],a[76:79],a[104:107],a[148:151]\n" \
"s_waitcnt vmcnt(19) lgkmcnt(12)\nds_read_b128 a[88:91],v8 offset:12288\nds_read_b128 a[92:95],v8 offset:14336\nM a[144:147],a[72:75],a[104:107],a[144:147]\ns_barrier\nds_read_b64_tr_b16 a[96:97],v9 offset:16384\nM a[160:163],a[72:75],a[112:115],a[160:163]\nds_read_b64_tr_b16 a[98:99],v10 offset:16384\n" \
"C v26,v25,v26\nM a[164:167],a[76:79],a[112:115],a[164:167]\nds_read_b64_tr_b16 a[100:101],v11 offset:16384\nv_lshlrev_b32 v27,16,v28\nM a[172:175],a[76:79],a[116:119],a[172:175]\nds_read_b64_tr_b16 a[102:103],v12 offset:16384\nds_read_b64_tr_b16 a[104:105],v9 offset:16896\nM a[168:171],a[72:75],a[116:119],a[168:171]\n" \
"ds_read_b64_tr_b16 a[106:107],v10 offset:16896\nv_lshlrev_b32 v25,16,v26\nM a[184:187],a[72:75],a[124:127],a[184:187]\nds_read_b64_tr_b16 a[108:109],v11 offset:16896\nv_and_b32 v28,0xffff0000,v28\nM a[188:191],a[76:79],a[124:127],a[188:191]\ns_waitcnt lgkmcnt(6)\nds_read_b64_tr_b16 a[110:111],v12 offset:16896\n" \
"ds_read_b64_tr_b16 a[112:113],v9 offset:17408\nM a[180:183],a[76:79],a[120:123],a[180:183]\nbuffer_store_dwordx4 v[54:57],v60,s[36:39],s99 offen offset:0 sc1\nM a[176:179],a[72:75],a[120:123],a[176:179]\ns_mov_b32 s99,79704064\nds_read_b128 a[72:75],v8 offset:4096\nds_read_b128 a[76:79],v8 offset:6144\nM v[128:131],a[24:27],a[32:35],v[128:131]\n" \
"ds_read_b64_tr_b16 a[114:115],v10 offset:17408\nv_and_b32 v26,0xffff0000,v26\nM v[132:135],a[28:31],a[32:35],v[132:135]\nds_read_b64_tr_b16 a[116:117],v11 offset:17408\nv_mul_f32 v29,v25,v27\nM v[140:143],a[28:31],a[36:39],v[140:143]\nds_read_b64_tr_b16 a[118:119],v12 offset:17408\nv_mul_f32 v58,v26,v28\n" \
"M v[136:139],a[24:27],a[36:39],v[136:139]\nds_read_b64_tr_b16 a[120:121],v9 offset:17920\nds_read_b64_tr_b16 a[122:123],v10 offset:17920\nM v[152:155],a[24:27],a[44:47],v[152:155]\nds_read_b64_tr_b16 a[124:125],v11 offset:17920\nv_accvgpr_read_b32 v40,a201\nM v[156:159],a[28:31],a[44:47],v[156:159]\nds_read_b64_tr_b16 a[126:127],v12 offset:17920\n" \
"v_lshlrev_b32 v34,16,v40\nM v[148:151],a[28:31],a[40:43],v[148:151]\nv_mul_f32 v25,v31,v45\nv_mul_f32 v26,v37,v49\nM v[144:147],a[24:27],a[40:43],v[144:147]\nv_fmac_f32 v25,v32,v44\nv_and_b32 v40,0xffff0000,v40\nM v[160:163],a[24:27],a[48:51],v[160:163]\n" \
"v_fmac_f32 v26,v38,v48\nv_fmac_f32 v25,v33,v43\nM v[164:167],a[28:31],a[48:51],v[164:167]\nv_fmac_f32 v26,v39,v47\nv_fmac_f32 v25,v34,v42\nM v[172:175],a[28:31],a[52:55],v[172:175]\nv_fmac_f32 v26,v40,v46\nv_accvgpr_read_b32 v28,a217\n" \
"M v[168:171],a[24:27],a[52:55],v[168:171]\nC v26,v25,v26\nv_lshlrev_b32 v27,16,v28\nM v[184:187],a[24:27],a[60:63],v[184:187]\nv_lshlrev_b32 v25,16,v26\nv_and_b32 v28,0xffff0000,v28\nM v[188:191],a[28:31],a[60:63],v[188:191]\nv_and_b32 v26,0xffff0000,v26\n" \
"v_mul_f32 v25,v25,v27\nM v[180:183],a[28:31],a[56:59],v[180:183]\nv_mul_f32 v26,v26,v28\nC v53,v25,v29\nM v[176:179],a[24:27],a[56:59],v[176:179]\nC v57,v26,v58\nv_accvgpr_read_b32 v41,a203\nM v[64:67],a[16:19],a[32:35],v[64:67]\n" \
"v_mul_f32 v25,v32,v45\nv_lshlrev_b32 v35,16,v41\nM v[68:71],a[20:23],a[32:35],v[68:71]\nv_mul_f32 v26,v38,v49\nv_fmac_f32 v25,v33,v44\nM v[76:79],a[20:23],a[36:39],v[76:79]\nv_fmac_f32 v26,v39,v48\nv_and_b32 v41,0xffff0000,v41\n" \
"M v[72:75],a[16:19],a[36:39],v[72:75]\nv_fmac_f32 v25,v34,v43\nv_fmac_f32 v26,v40,v47\nM v[88:91],a[16:19],a[44:47],v[88:91]\nv_fmac_f32 v25,v35,v42\nv_fmac_f32 v26,v41,v46\nM v[92:95],a[20:23],a[44:47],v[92:95]\nv_accvgpr_read_b32 v28,a219\n" \
"C v26,v25,v26\nM v[84:87],a[20:23],a[40:43],v[84:87]\ns_mov_b64 exec,0xff\nglobal_load_dword v0,v2,s[100:101]\ns_mov_b64 exec,-1\nM v[80:83],a[16:19],a[40:43],v[80:83]\nv_lshlrev_b32 v27,16,v28\nv_lshlrev_b32 v25,16,v26\n" \
"M v[96:99],a[16:19],a[48:51],v[96:99]\nbuffer_load_dwordx4 v21,s[32:35],s47 offen offset:0 lds\nM v[100:103],a[20:23],a[48:51],v[100:103]\nLG7_%=:\ns_cbranch_execz LHR0_%=\ns_add_u32 m0,s22,50048\nv_and_b32 v28,0xffff0000,v28\nv_and_b32 v26,0xffff0000,v26\n" \
"M v[108:111],a[20:23],a[52:55],v[108:111]\nv_mul_f32 v29,v25,v27\nv_mul_f32 v58,v26,v28\nM v[104:107],a[16:19],a[52:55],v[104:107]\nv_accvgpr_read_b32 v36,a205\nv_lshlrev_b32 v30,16,v36\nM v[120:123],a[16:19],a[60:63],v[120:123]\nbuffer_load_dwordx4 v21,s[32:35],s47 offen offset:128 lds\n" \
"M v[124:127],a[20:23],a[60:63],v[124:127]\ns_add_u32 m0,s22,51200\nv_mul_f32 v25,v33,v45\nv_mul_f32 v26,v39,v49\nM v[116:119],a[20:23],a[56:59],v[116:119]\ns_mov_b64 s[32:33],s[44:45]\nv_fmac_f32 v25,v34,v44\nv_and_b32 v36,0xffff0000,v36\n" \
"M v[112:115],a[16:19],a[56:59],v[112:115]\ns_mov_b32 s34,s49\nv_fmac_f32 v26,v40,v48\nv_fmac_f32 v25,v35,v43\nM v[192:195],a[0:3],a[32:35],v[192:195]\nbuffer_load_dwordx4 v21,s[92:95],s47 offen offset:0 lds\nM v[196:199],a[4:7],a[32:35],v[196:199]\ns_add_u32 m0,s22,52096\n" \
"v_fmac_f32 v26,v41,v47\nv_fmac_f32 v25,v30,v42\nM v[204:207],a[4:7],a[36:39],v[204:207]\nv_fmac_f32 v26,v36,v46\nv_accvgpr_read_b32 v28,a221\nM v[200:203],a[0:3],a[36:39],v[200:203]\nC v26,v25,v26\nv_lshlrev_b32 v27,16,v28\n" \
"M v[216:219],a[0:3],a[44:47],v[216:219]\nbuffer_load_dwordx4 v21,s[92:95],s47 offen offset:128 lds\nM v[220:223],a[4:7],a[44:47],v[220:223]\ns_add_u32 m0,s50,32768\nv_lshlrev_b32 v25,16,v26\nv_and_b32 v28,0xffff0000,v28\nM v[212:215],a[4:7],a[40:43],v[212:215]\ns_mov_b32 s47,0\n" \
"v_and_b32 v26,0xffff0000,v26\nv_mul_f32 v25,v25,v27\nM v[208:211],a[0:3],a[40:43],v[208:211]\nv_mul_f32 v26,v26,v28\nC v52,v25,v29\nM v[224:227],a[0:3],a[48:51],v[224:227]\nbuffer_load_dwordx4 v13,s[28:31],s46 offen lds\nM v[228:231],a[4:7],a[48:51],v[228:231]\n" \
"s_add_u32 m0,s50,36864\nC v56,v26,v58\nv_accvgpr_read_b32 v37,a207\nM v[236:239],a[4:7],a[52:55],v[236:239]\nv_mul_f32 v25,v34,v45\nv_lshlrev_b32 v31,16,v37\nM v[232:235],a[0:3],a[52:55],v[232:235]\nv_mul_f32 v26,v40,v49\n" \
"v_fmac_f32 v25,v35,v44\nM v[248:251],a[0:3],a[60:63],v[248:251]\nbuffer_load_dwordx4 v14,s[28:31],s46 offen lds\nM v[252:255],a[4:7],a[60:63],v[252:255]\ns_add_u32 m0,s50,40960\nv_fmac_f32 v26,v41,v48\nv_and_b32 v37,0xffff0000,v37\nM v[244:247],a[4:7],a[56:59],v[244:247]\n" \
"v_fmac_f32 v25,v30,v43\nv_fmac_f32 v26,v36,v47\nM v[240:243],a[0:3],a[56:59],v[240:243]\nv_fmac_f32 v25,v31,v42\nv_fmac_f32 v26,v37,v46\nM a[128:131],a[8:11],a[32:35],a[128:131]\nbuffer_load_dwordx4 v15,s[28:31],s46 offen lds\nM a[132:135],a[12:15],a[32:35],a[132:135]\n" \
"s_add_u32 m0,s50,45056\nv_accvgpr_read_b32 v28,a223\nC v26,v25,v26\nM a[140:143],a[12:15],a[36:39],a[140:143]\nv_lshlrev_b32 v27,16,v28\nv_lshlrev_b32 v25,16,v26\nM a[136:139],a[8:11],a[36:39],a[136:139]\nv_and_b32 v28,0xffff0000,v28\n" \
"v_and_b32 v26,0xffff0000,v26\nM a[152:155],a[8:11],a[44:47],a[152:155]\nbuffer_load_dwordx4 v16,s[28:31],s46 offen lds\nM a[156:159],a[12:15],a[44:47],a[156:159]\ns_add_u32 m0,s50,49152\nv_mul_f32 v29,v25,v27\nv_mul_f32 v58,v26,v28\nM a[148:151],a[12:15],a[40:43],a[148:151]\n" \
"s_waitcnt vmcnt(17) lgkmcnt(0)\nv_accvgpr_read_b32 v38,a209\nv_mul_f32 v25,v35,v45\nM a[144:147],a[8:11],a[40:43],a[144:147]\ns_mov_b32 s46,0\ns_barrier\nds_read_b64_tr_b16 a[32:33],v9 offset:32768\nM a[160:163],a[8:11],a[48:51],a[160:163]\n" \
"buffer_load_dwordx4 v17,s[28:31],s46 offen lds\nM a[164:167],a[12:15],a[48:51],a[164:167]\ns_add_u32 m0,s50,53248\nds_read_b64_tr_b16 a[34:35],v10 offset:32768\nv_mul_f32 v26,v41,v49\nM a[172:175],a[12:15],a[52:55],a[172:175]\nds_read_b64_tr_b16 a[36:37],v11 offset:32768\nds_read_b64_tr_b16 a[38:39],v12 offset:32768\n" \
"M a[168:171],a[8:11],a[52:55],a[168:171]\nds_read_b64_tr_b16 a[40:41],v9 offset:33280\nv_lshlrev_b32 v32,16,v38\nM a[184:187],a[8:11],a[60:63],a[184:187]\nbuffer_load_dwordx4 v18,s[28:31],s46 offen lds\nM a[188:191],a[12:15],a[60:63],a[188:191]\ns_add_u32 m0,s50,57344\nds_read_b64_tr_b16 a[42:43],v10 offset:33280\n" \
"v_fmac_f32 v25,v30,v44\nM a[180:183],a[12:15],a[56:59],a[180:183]\nds_read_b64_tr_b16 a[44:45],v11 offset:33280\nv_fmac_f32 v26,v36,v48\nM a[176:179],a[8:11],a[56:59],a[176:179]\nds_read_b64_tr_b16 a[46:47],v12 offset:33280\nv_and_b32 v38,0xffff0000,v38\nM v[128:131],a[88:91],a[96:99],v[128:131]\n" \
"buffer_load_dwordx4 v19,s[28:31],s46 offen lds\nM v[132:135],a[92:95],a[96:99],v[132:135]\ns_add_u32 m0,s50,61440\nds_read_b64_tr_b16 a[48:49],v9 offset:33792\nv_fmac_f32 v25,v31,v43\nM v[140:143],a[92:95],a[100:103],v[140:143]\nds_read_b64_tr_b16 a[50:51],v10 offset:33792\nv_fmac_f32 v26,v37,v47\n" \
"M v[136:139],a[88:91],a[100:103],v[136:139]\nds_read_b64_tr_b16 a[52:53],v11 offset:33792\nds_read_b64_tr_b16 a[54:55],v12 offset:33792\nM v[152:155],a[88:91],a[108:111],v[152:155]\nbuffer_load_dwordx4 v20,s[28:31],s46 offen lds\nM v[156:159],a[92:95],a[108:111],v[156:159]\ns_add_u32 m0,s51,0\nv_fmac_f32 v25,v32,v42\n" \
"v_fmac_f32 v26,v38,v46\nM v[148:151],a[92:95],a[104:107],v[148:151]\ns_mov_b32 s46,128\nds_read_b64_tr_b16 a[56:57],v9 offset:34304\nds_read_b64_tr_b16 a[58:59],v10 offset:34304\nM v[144:147],a[88:91],a[104:107],v[144:147]\nds_read_b64_tr_b16 a[60:61],v11 offset:34304\nv_accvgpr_read_b32 v28,a225\n" \
"M v[160:163],a[88:91],a[112:115],v[160:163]\nbuffer_load_dwordx4 v21,s[32:35],s47 offen offset:0 lds\nM v[164:167],a[92:95],a[112:115],v[164:167]\ns_add_u32 m0,s51,896\nds_read_b64_tr_b16 a[62:63],v12 offset:34304\nC v26,v25,v26\nM v[172:175],a[92:95],a[116:119],v[172:175]\nds_read_b128 a[24:27],v5 offset:12288\n" \
"v_lshlrev_b32 v27,16,v28\nM v[168:171],a[88:91],a[116:119],v[168:171]\nds_read_b128 a[28:31],v5 offset:14336\nv_lshlrev_b32 v25,16,v26\nM v[184:187],a[88:91],a[124:127],v[184:187]\nbuffer_load_dwordx4 v21,s[32:35],s47 offen offset:128 lds\nM v[188:191],a[92:95],a[124:127],v[188:191]\ns_add_u32 m0,s51,8192\n" \
"v_and_b32 v28,0xffff0000,v28\nv_and_b32 v26,0xffff0000,v26\nM v[180:183],a[92:95],a[120:123],v[180:183]\ns_mov_b32 s47,262144\nv_mul_f32 v25,v25,v27\nv_mul_f32 v26,v26,v28\nM v[176:179],a[88:91],a[120:123],v[176:179]\nC v51,v25,v29\n" \
"C v55,v26,v58\nM v[64:67],a[80:83],a[96:99],v[64:67]\nbuffer_load_dwordx4 v21,s[32:35],s47 offen offset:0 lds\nM v[68:71],a[84:87],a[96:99],v[68:71]\ns_add_u32 m0,s51,9088\nv_accvgpr_read_b32 v39,a211\nv_mul_f32 v25,v30,v45\nM v[76:79],a[84:87],a[100:103],v[76:79]\n" \
"v_lshlrev_b32 v33,16,v39\nv_mul_f32 v26,v36,v49\nM v[72:75],a[80:83],a[100:103],v[72:75]\nv_fmac_f32 v25,v31,v44\nv_fmac_f32 v26,v37,v48\nM v[88:91],a[80:83],a[108:111],v[88:91]\nbuffer_load_dwordx4 v21,s[32:35],s47 offen offset:128 lds\nM v[92:95],a[84:87],a[108:111],v[92:95]\n" \
"s_add_u32 m0,s51,16384\nv_and_b32 v39,0xffff0000,v39\nv_fmac_f32 v25,v32,v43\nM v[84:87],a[84:87],a[104:107],v[84:87]\ns_mov_b32 s47,524288\nv_fmac_f32 v26,v38,v47\nv_fmac_f32 v25,v33,v42\nM v[80:83],a[80:83],a[104:107],v[80:83]\n" \
"v_fmac_f32 v26,v39,v46\nv_accvgpr_read_b32 v28,a227\nM v[96:99],a[80:83],a[112:115],v[96:99]\nbuffer_load_dwordx4 v21,s[32:35],s47 offen offset:0 lds\nM v[100:103],a[84:87],a[112:115],v[100:103]\ns_add_u32 m0,s51,17280\nC v26,v25,v26\nv_lshlrev_b32 v27,16,v28\n" \
"M v[108:111],a[84:87],a[116:119],v[108:111]\nds_read_b128 a[16:19],v5 offset:8192\nv_lshlrev_b32 v25,16,v26\nM v[104:107],a[80:83],a[116:119],v[104:107]\nds_read_b128 a[20:23],v5 offset:10240\nv_and_b32 v28,0xffff0000,v28\nM v[120:123],a[80:83],a[124:127],v[120:123]\nbuffer_load_dwordx4 v21,s[32:35],s47 offen offset:128 lds\n" \
"M v[124:127],a[84:87],a[124:127],v[124:127]\ns_add_u32 m0,s51,24576\nv_and_b32 v26,0xffff0000,v26\nv_mul_f32 v29,v25,v27\nM v[116:119],a[84:87],a[120:123],v[116:119]\ns_mov_b32 s47,786432\nv_mul_f32 v58,v26,v28\nv_accvgpr_read_b32 v40,a213\n" \
"M v[112:115],a[80:83],a[120:123],v[112:115]\nv_lshlrev_b32 v34,16,v40\nv_mul_f32 v25,v31,v45\nM v[192:195],a[64:67],a[96:99],v[192:195]\ns_mov_b64 exec,0xff\nglobal_load_dword v0,v1,s[76:77]\ns_mov_b64 exec,-1\nM v[196:199],a[68:71],a[96:99],v[196:199]\n" \
"v_mul_f32 v26,v37,v49\nv_fmac_f32 v25,v32,v44\nM v[204:207],a[68:71],a[100:103],v[204:207]\ns_mov_b64 exec,0xff00\nglobal_load_dword v0,v1,s[76:77]\ns_mov_b64 exec,-1\nM v[200:203],a[64:67],a[100:103],v[200:203]\ns_add_u32 s76,s76,2097152\n" \
"s_addc_u32 s77,s77,0\nv_and_b32 v40,0xffff0000,v40\nv_fmac_f32 v26,v38,v48\nM v[216:219],a[64:67],a[108:111],v[216:219]\ns_mov_b64 exec,0xff\nglobal_load_dword v0,v1,s[76:77]\ns_mov_b64 exec,-1\nM v[220:223],a[68:71],a[108:111],v[220:223]\n" \
"v_fmac_f32 v25,v33,v43\nv_fmac_f32 v26,v39,v47\nM v[212:215],a[68:71],a[104:107],v[212:215]\ns_mov_b64 exec,0xff00\nglobal_load_dword v0,v1,s[76:77]\ns_mov_b64 exec,-1\nM v[208:211],a[64:67],a[104:107],v[208:211]\ns_mov_b32 s76,0xf149f2ca\n" \
"v_fmac_f32 v25,v34,v42\nv_fmac_f32 v26,v40,v46\nM v[224:227],a[64:67],a[112:115],v[224:227]\nv_accvgpr_read_b32 v28,a229\nC v26,v25,v26\nM v[228:231],a[68:71],a[112:115],v[228:231]\nv_lshlrev_b32 v27,16,v28\nv_lshlrev_b32 v25,16,v26\n" \
"M v[236:239],a[68:71],a[116:119],v[236:239]\nv_mul_f32 v25,v25,v27\nC v50,v25,v29\nM v[232:235],a[64:67],a[116:119],v[232:235]\nds_read_b128 a[0:3],v5 offset:0\nds_read_b128 a[4:7],v5 offset:2048\nM v[248:251],a[64:67],a[124:127],v[248:251]\nds_read_b128 a[8:11],v5 offset:4096\n" \
"v_and_b32 v28,0xffff0000,v28\nM v[252:255],a[68:71],a[124:127],v[252:255]\nds_read_b128 a[12:15],v5 offset:6144\nv_and_b32 v26,0xffff0000,v26\nM v[244:247],a[68:71],a[120:123],v[244:247]\nv_mul_f32 v26,v26,v28\nC v54,v26,v58\nM v[240:243],a[64:67],a[120:123],v[240:243]\n" \
"ds_read_b128 a[64:67],v6 offset:0\nds_read_b128 a[68:71],v6 offset:2048\nM a[128:131],a[72:75],a[96:99],a[128:131]\nds_read_b128 a[80:83],v6 offset:8192\nv_add_u32 v5,s54,v5\nM a[132:135],a[76:79],a[96:99],a[132:135]\nds_read_b128 a[84:87],v6 offset:10240\nv_add_u32 v7,s54,v7\n" \
"M a[140:143],a[76:79],a[100:103],a[140:143]\nds_read_b128 a[88:91],v6 offset:12288\nv_add_u32 v8,s54,v8\nM a[136:139],a[72:75],a[100:103],a[136:139]\nds_read_b128 a[92:95],v6 offset:14336\nv_mov_b32 v58,s18\nM a[152:155],a[72:75],a[108:111],a[152:155]\nv_add_u32 v25,-16,v4\n" \
"v_mov_b32 v29,s62\nM a[156:159],a[76:79],a[108:111],a[156:159]\ns_and_b32 s62,%1,7\nv_mov_b32 v28,0\nv_add_u32 v26,-512,v4\nM a[148:151],a[76:79],a[104:107],a[148:151]\ns_waitcnt vmcnt(18) lgkmcnt(14)\nv_add_u32 v27,512,v4\n" \
"M a[144:147],a[72:75],a[104:107],a[144:147]\ns_lshl_b32 s62,s62,8\ns_barrier\nds_read_b64_tr_b16 a[96:97],v9 offset:49152\nM a[160:163],a[72:75],a[112:115],a[160:163]\ns_add_u32 s62,s62,94380032\nds_read_b64_tr_b16 a[98:99],v10 offset:49152\nds_read_b64_tr_b16 a[100:101],v11 offset:49152\n" \
"M a[164:167],a[76:79],a[112:115],a[164:167]\nds_read_b64_tr_b16 a[102:103],v12 offset:49152\nM a[172:175],a[76:79],a[116:119],a[172:175]\nds_read_b64_tr_b16 a[104:105],v9 offset:49664\nds_read_b64_tr_b16 a[106:107],v10 offset:49664\nM a[168:171],a[72:75],a[116:119],a[168:171]\nds_read_b64_tr_b16 a[108:109],v11 offset:49664\nM a[184:187],a[72:75],a[124:127],a[184:187]\n" \
"ds_read_b64_tr_b16 a[110:111],v12 offset:49664\nM a[188:191],a[76:79],a[124:127],a[188:191]\nds_read_b64_tr_b16 a[112:113],v9 offset:50176\nds_read_b64_tr_b16 a[114:115],v10 offset:50176\nM a[180:183],a[76:79],a[120:123],a[180:183]\nds_read_b64_tr_b16 a[116:117],v11 offset:50176\nM a[176:179],a[72:75],a[120:123],a[176:179]\ns_waitcnt lgkmcnt(7)\n" \
"ds_read_b128 a[72:75],v6 offset:4096\nM v[128:131],a[24:27],a[32:35],v[128:131]\nds_read_b128 a[76:79],v6 offset:6144\nv_add_u32 v6,s54,v6\nM v[132:135],a[28:31],a[32:35],v[132:135]\nds_read_b64_tr_b16 a[118:119],v12 offset:50176\nM v[140:143],a[28:31],a[36:39],v[140:143]\nds_read_b64_tr_b16 a[120:121],v9 offset:50688\n" \
"ds_read_b64_tr_b16 a[122:123],v10 offset:50688\nM v[136:139],a[24:27],a[36:39],v[136:139]\nds_read_b64_tr_b16 a[124:125],v11 offset:50688\nv_sub_u32 v9,v9,v62\nM v[152:155],a[24:27],a[44:47],v[152:155]\nds_read_b64_tr_b16 a[126:127],v12 offset:50688\nv_sub_u32 v10,v10,v62\nM v[156:159],a[28:31],a[44:47],v[156:159]\n" \
"v_sub_u32 v11,v11,v62\nv_sub_u32 v12,v12,v62\nM v[148:151],a[28:31],a[40:43],v[148:151]\nM v[144:147],a[24:27],a[40:43],v[144:147]\nM v[160:163],a[24:27],a[48:51],v[160:163]\nM v[164:167],a[28:31],a[48:51],v[164:167]\nM v[172:175],a[28:31],a[52:55],v[172:175]\nM v[168:171],a[24:27],a[52:55],v[168:171]\n" \
"M v[184:187],a[24:27],a[60:63],v[184:187]\nM v[188:191],a[28:31],a[60:63],v[188:191]\nM v[180:183],a[28:31],a[56:59],v[180:183]\nbuffer_load_dwordx4 v21,s[32:35],s47 offen offset:0 lds\nM v[176:179],a[24:27],a[56:59],v[176:179]\ns_add_u32 m0,s51,25472\nM v[64:67],a[16:19],a[32:35],v[64:67]\nM v[68:71],a[20:23],a[32:35],v[68:71]\n" \
"M v[76:79],a[20:23],a[36:39],v[76:79]\nbuffer_store_dwordx4 v[50:53],v60,s[36:39],s98 offen offset:0 sc1\nM v[72:75],a[16:19],a[36:39],v[72:75]\nM v[88:91],a[16:19],a[44:47],v[88:91]\nM v[92:95],a[20:23],a[44:47],v[92:95]\nM v[84:87],a[20:23],a[40:43],v[84:87]\nM v[80:83],a[16:19],a[40:43],v[80:83]\nM v[96:99],a[16:19],a[48:51],v[96:99]\n" \
"M v[100:103],a[20:23],a[48:51],v[100:103]\nM v[108:111],a[20:23],a[52:55],v[108:111]\nM v[104:107],a[16:19],a[52:55],v[104:107]\nM v[120:123],a[16:19],a[60:63],v[120:123]\nM v[124:127],a[20:23],a[60:63],v[124:127]\nM v[116:119],a[20:23],a[56:59],v[116:119]\nM v[112:115],a[16:19],a[56:59],v[112:115]\nM v[192:195],a[0:3],a[32:35],v[192:195]\n" \
"M v[196:199],a[4:7],a[32:35],v[196:199]\nM v[204:207],a[4:7],a[36:39],v[204:207]\nM v[200:203],a[0:3],a[36:39],v[200:203]\nM v[216:219],a[0:3],a[44:47],v[216:219]\nM v[220:223],a[4:7],a[44:47],v[220:223]\nM v[212:215],a[4:7],a[40:43],v[212:215]\nM v[208:211],a[0:3],a[40:43],v[208:211]\nM v[224:227],a[0:3],a[48:51],v[224:227]\n" \
"M v[228:231],a[4:7],a[48:51],v[228:231]\nM v[236:239],a[4:7],a[52:55],v[236:239]\nbuffer_store_dwordx4 v[54:57],v60,s[36:39],s99 offen offset:0 sc1\nM v[232:235],a[0:3],a[52:55],v[232:235]\ns_nop 0\nv_lshlrev_b32 v56,23,v4\nv_bfe_u32 v57,v4,4,5\nM v[248:251],a[0:3],a[60:63],v[248:251]\n" \
"v_lshl_add_u32 v57,v57,3,s62\nv_add_u32 v55,s56,v4\nM v[252:255],a[4:7],a[60:63],v[252:255]\nM v[244:247],a[4:7],a[56:59],v[244:247]\nM v[240:243],a[0:3],a[56:59],v[240:243]\nM a[128:131],a[8:11],a[32:35],a[128:131]\nM a[132:135],a[12:15],a[32:35],a[132:135]\nM a[140:143],a[12:15],a[36:39],a[140:143]\n" \
"M a[136:139],a[8:11],a[36:39],a[136:139]\nM a[152:155],a[8:11],a[44:47],a[152:155]\nM a[156:159],a[12:15],a[44:47],a[156:159]\nM a[148:151],a[12:15],a[40:43],a[148:151]\ns_waitcnt vmcnt(9) lgkmcnt(0)\nM a[144:147],a[8:11],a[40:43],a[144:147]\ns_barrier\nds_read_b64_tr_b16 a[0:1],v9 offset:0\n" \
"M a[160:163],a[8:11],a[48:51],a[160:163]\nds_read_b64_tr_b16 a[2:3],v10 offset:0\nds_read_b64_tr_b16 a[4:5],v11 offset:0\nM a[164:167],a[12:15],a[48:51],a[164:167]\nds_read_b128 a[44:47],v5 offset:45056\nM a[172:175],a[12:15],a[52:55],a[172:175]\nds_read_b64_tr_b16 a[6:7],v12 offset:0\nM a[168:171],a[8:11],a[52:55],a[168:171]\n" \
"ds_read_b64_tr_b16 a[16:17],v9 offset:1024\nds_read_b64_tr_b16 a[18:19],v10 offset:1024\nM a[184:187],a[8:11],a[60:63],a[184:187]\nds_read_b64_tr_b16 a[20:21],v11 offset:1024\nM a[188:191],a[12:15],a[60:63],a[188:191]\nds_read_b64_tr_b16 a[22:23],v12 offset:1024\nM a[180:183],a[12:15],a[56:59],a[180:183]\nds_read_b64_tr_b16 a[24:25],v9 offset:1536\n" \
"ds_read_b64_tr_b16 a[26:27],v10 offset:1536\nM a[176:179],a[8:11],a[56:59],a[176:179]\nds_read_b64_tr_b16 a[8:9],v9 offset:512\nM v[128:131],a[88:91],a[96:99],v[128:131]\nds_read_b64_tr_b16 a[10:11],v10 offset:512\nM v[132:135],a[92:95],a[96:99],v[132:135]\nds_read_b64_tr_b16 a[12:13],v11 offset:512\nds_read_b64_tr_b16 a[14:15],v12 offset:512\n" \
"M v[140:143],a[92:95],a[100:103],v[140:143]\nbuffer_load_dwordx4 v21,s[32:35],s47 offen offset:128 lds\nM v[136:139],a[88:91],a[100:103],v[136:139]\ns_add_u32 m0,s50,65536\nv_mul_f32 v128,s20,v128\nv_mul_f32 v129,s20,v129\nM v[152:155],a[88:91],a[108:111],v[152:155]\ns_mov_b32 s47,1048576\n" \
"v_mul_f32 v130,s20,v130\nv_mul_f32 v131,s20,v131\nM v[156:159],a[92:95],a[108:111],v[156:159]\nv_mul_f32 v132,s20,v132\nv_mul_f32 v133,s20,v133\nM v[148:151],a[92:95],a[104:107],v[148:151]\nbuffer_load_dwordx4 v13,s[28:31],s46 offen lds\nM v[144:147],a[88:91],a[104:107],v[144:147]\n" \
"s_add_u32 m0,s50,69632\nv_mul_f32 v134,s20,v134\nv_mul_f32 v135,s20,v135\nM v[160:163],a[88:91],a[112:115],v[160:163]\nv_mul_f32 v136,s20,v136\nv_mul_f32 v137,s20,v137\nM v[164:167],a[92:95],a[112:115],v[164:167]\nv_mul_f32 v138,s20,v138\n" \
"v_mul_f32 v139,s20,v139\nM v[172:175],a[92:95],a[116:119],v[172:175]\nbuffer_load_dwordx4 v14,s[28:31],s46 offen lds\nM v[168:171],a[88:91],a[116:119],v[168:171]\ns_add_u32 m0,s50,73728\nds_read_b64_tr_b16 a[28:29],v11 offset:1536\nv_mul_f32 v140,s20,v140\nM v[184:187],a[88:91],a[124:127],v[184:187]\n" \
"ds_read_b64_tr_b16 a[30:31],v12 offset:1536\nds_read_b128 a[40:43],v5 offset:40960\nM v[188:191],a[92:95],a[124:127],v[188:191]\nv_mul_f32 v141,s20,v141\nv_mul_f32 v142,s20,v142\nM v[180:183],a[92:95],a[120:123],v[180:183]\nbuffer_load_dwordx4 v15,s[28:31],s46 offen lds\nM v[176:179],a[88:91],a[120:123],v[176:179]\n" \
"s_add_u32 m0,s50,77824\nv_mul_f32 v143,s20,v143\nv_mul_f32 v144,s20,v144\nM v[64:67],a[80:83],a[96:99],v[64:67]\nv_mul_f32 v145,s20,v145\nv_mul_f32 v146,s20,v146\nM v[68:71],a[84:87],a[96:99],v[68:71]\nds_read_b128 a[32:35],v5 offset:32768\n" \
"v_mul_f32 v147,s20,v147\nM v[76:79],a[84:87],a[100:103],v[76:79]\nbuffer_load_dwordx4 v16,s[28:31],s46 offen lds\nM v[72:75],a[80:83],a[100:103],v[72:75]\ns_add_u32 m0,s50,81920\ns_mov_b32 s46,128\nv_mul_f32 v64,s20,v64\nv_mul_f32 v65,s20,v65\n" \
"M v[88:91],a[80:83],a[108:111],v[88:91]\nv_mul_f32 v66,s20,v66\nv_mul_f32 v67,s20,v67\nM v[92:95],a[84:87],a[108:111],v[92:95]\nC v64,v64,v65\nC v65,v66,v67\nM v[84:87],a[84:87],a[104:107],v[84:87]\nbuffer_load_dwordx4 v17,s[28:31],s46 offen lds\n" \
"M v[80:83],a[80:83],a[104:107],v[80:83]\ns_add_u32 m0,s50,86016\nv_mul_f32 v68,s20,v68\nv_mul_f32 v69,s20,v69\nM v[96:99],a[80:83],a[112:115],v[96:99]\nv_mul_f32 v148,s20,v148\nv_mul_f32 v149,s20,v149\nM v[100:103],a[84:87],a[112:115],v[100:103]\n" \
"v_mul_f32 v150,s20,v150\nv_mul_f32 v151,s20,v151\nM v[108:111],a[84:87],a[116:119],v[108:111]\nbuffer_load_dwordx4 v18,s[28:31],s46 offen lds\nM v[104:107],a[80:83],a[116:119],v[104:107]\ns_add_u32 m0,s50,90112\nv_mul_f32 v70,s20,v70\nv_mul_f32 v152,s20,v152\n" \
"M v[120:123],a[80:83],a[124:127],v[120:123]\nv_mul_f32 v71,s20,v71\nv_mul_f32 v153,s20,v153\nM v[124:127],a[84:87],a[124:127],v[124:127]\nv_mul_f32 v154,s20,v154\nv_mul_f32 v155,s20,v155\nM v[116:119],a[84:87],a[120:123],v[116:119]\nbuffer_load_dwordx4 v19,s[28:31],s46 offen lds\n" \
"M v[112:115],a[80:83],a[120:123],v[112:115]\ns_add_u32 m0,s50,94208\nv_mul_f32 v156,s20,v156\nv_mul_f32 v157,s20,v157\nM v[192:195],a[64:67],a[96:99],v[192:195]\nv_mul_f32 v158,s20,v158\nv_mul_f32 v159,s20,v159\nM v[196:199],a[68:71],a[96:99],v[196:199]\n" \
"v_mul_f32 v160,s20,v160\nv_mul_f32 v161,s20,v161\nM v[204:207],a[68:71],a[100:103],v[204:207]\nbuffer_load_dwordx4 v20,s[28:31],s46 offen lds\nM v[200:203],a[64:67],a[100:103],v[200:203]\ns_add_u32 m0,s51,32768\nds_read_b128 a[36:39],v5 offset:36864\nv_mul_f32 v162,s20,v162\n" \
"M v[216:219],a[64:67],a[108:111],v[216:219]\ns_mov_b32 s46,256\nv_mul_f32 v163,s20,v163\nC v66,v68,v69\nM v[220:223],a[68:71],a[108:111],v[220:223]\nv_mul_f32 v72,s20,v72\nv_mul_f32 v164,s20,v164\nM v[212:215],a[68:71],a[104:107],v[212:215]\n" \
"buffer_load_dwordx4 v21,s[32:35],s47 offen offset:0 lds\nM v[208:211],a[64:67],a[104:107],v[208:211]\ns_add_u32 m0,s51,33664\nv_mul_f32 v73,s20,v73\nC v30,v128,v129\nM v[224:227],a[64:67],a[112:115],v[224:227]\nv_mul_f32 v165,s20,v165\nC v31,v130,v131\n" \
"M v[228:231],a[68:71],a[112:115],v[228:231]\nv_mul_f32 v166,s20,v166\nC v32,v132,v133\nM v[236:239],a[68:71],a[116:119],v[236:239]\nbuffer_load_dwordx4 v21,s[32:35],s47 offen offset:128 lds\nM v[232:235],a[64:67],a[116:119],v[232:235]\ns_add_u32 m0,s51,40960\nv_mul_f32 v167,s20,v167\n" \
"C v33,v134,v135\nM v[248:251],a[64:67],a[124:127],v[248:251]\ns_mov_b32 s47,1310720\nv_mul_f32 v168,s20,v168\nC v34,v136,v137\nM v[252:255],a[68:71],a[124:127],v[252:255]\nv_mul_f32 v169,s20,v169\nC v35,v138,v139\n" \
"M v[244:247],a[68:71],a[120:123],v[244:247]\nbuffer_load_dwordx4 v21,s[32:35],s47 offen offset:0 lds\nM v[240:243],a[64:67],a[120:123],v[240:243]\ns_add_u32 m0,s51,41856\nds_read_b64_tr_b16 a[48:49],v9 offset:8192\nv_mul_f32 v170,s20,v170\nM a[128:131],a[72:75],a[96:99],a[128:131]\nds_read_b64_tr_b16 a[50:51],v10 offset:8192\n" \
"ds_read_b128 a[80:83],v6 offset:32768\nM a[132:135],a[76:79],a[96:99],a[132:135]\nds_read_b128 a[84:87],v6 offset:36864\nv_mul_f32 v171,s20,v171\nM a[140:143],a[76:79],a[100:103],a[140:143]\nbuffer_load_dwordx4 v21,s[32:35],s47 offen offset:128 lds\nM a[136:139],a[72:75],a[100:103],a[136:139]\ns_add_u32 m0,s50,0\n" \
"v_mul_f32 v74,s20,v74\nC v36,v140,v141\nM a[152:155],a[72:75],a[108:111],a[152:155]\ns_mov_b32 s47,1572864\nds_read_b64_tr_b16 a[52:53],v11 offset:8192\nv_mul_f32 v75,s20,v75\nM a[156:159],a[76:79],a[108:111],a[156:159]\nds_read_b64_tr_b16 a[54:55],v12 offset:8192\n" \
"ds_read_b64_tr_b16 a[56:57],v9 offset:8704\nM a[148:151],a[76:79],a[104:107],a[148:151]\nbuffer_load_dwordx4 v13,s[28:31],s46 offen lds\nM a[144:147],a[72:75],a[104:107],a[144:147]\ns_nop 0\ns_add_u32 m0,s50,4096\nC v37,v142,v143\nC v38,v144,v145\n" \
"M a[160:163],a[72:75],a[112:115],a[160:163]\nds_read_b64_tr_b16 a[58:59],v10 offset:8704\nds_read_b64_tr_b16 a[60:61],v11 offset:8704\nM a[164:167],a[76:79],a[112:115],a[164:167]\nds_read_b64_tr_b16 a[62:63],v12 offset:8704\nC v39,v146,v147\nM a[172:175],a[76:79],a[116:119],a[172:175]\nbuffer_load_dwordx4 v14,s[28:31],s46 offen lds\n" \
"M a[168:171],a[72:75],a[116:119],a[168:171]\ns_add_u32 m0,s50,8192\nds_read_b64_tr_b16 a[64:65],v9 offset:9216\nv_mul_f32 v172,s20,v172\nM a[184:187],a[72:75],a[124:127],a[184:187]\nds_read_b64_tr_b16 a[66:67],v10 offset:9216\nv_mul_f32 v173,s20,v173\nM a[188:191],a[76:79],a[124:127],a[188:191]\n" \
"s_waitcnt lgkmcnt(5)\nds_read_b64_tr_b16 a[68:69],v11 offset:9216\nv_mul_f32 v174,s20,v174\nM a[180:183],a[76:79],a[120:123],a[180:183]\nbuffer_load_dwordx4 v15,s[28:31],s46 offen lds\nM a[176:179],a[72:75],a[120:123],a[176:179]\nLC_%=:\ns_add_u32 m0,s50,12288\n" \
"ds_read_b64_tr_b16 a[70:71],v12 offset:9216\nC v40,v148,v149\nM a[192:195],a[0:3],a[44:47],0\nds_read_b64_tr_b16 a[72:73],v9 offset:9728\nv_mul_f32 v175,s20,v175\nM a[196:199],a[4:7],a[44:47],0\nds_read_b64_tr_b16 a[74:75],v10 offset:9728\nds_read_b64_tr_b16 a[76:77],v11 offset:9728\n" \
"M a[200:203],a[8:11],a[44:47],0\nbuffer_load_dwordx4 v16,s[28:31],s46 offen lds\nM a[204:207],a[12:15],a[44:47],0\ns_mov_b32 s46,256\nC v41,v150,v151\nC v67,v70,v71\nM a[208:211],a[16:19],a[44:47],0\ns_add_u32 m0,s50,16384\n" \
"ds_read_b64_tr_b16 a[78:79],v12 offset:9728\nds_read_b128 a[88:91],v6 offset:40960\nM a[212:215],a[20:23],a[44:47],0\nds_read_b128 a[92:95],v6 offset:45056\nv_mul_f32 v76,s20,v76\nM a[216:219],a[24:27],a[44:47],0\nbuffer_load_dwordx4 v17,s[28:31],s46 offen lds\nM a[220:223],a[28:31],a[44:47],0\n" \
"s_add_u32 m0,s50,20480\nv_mul_f32 v77,s20,v77\nC v42,v152,v153\nM a[224:227],a[0:3],a[40:43],0\nC v43,v154,v155\nC v44,v156,v157\nM a[228:231],a[4:7],a[40:43],0\nC v45,v158,v159\n" \
"C v46,v160,v161\nM a[232:235],a[8:11],a[40:43],0\nbuffer_load_dwordx4 v18,s[28:31],s46 offen lds\nM a[236:239],a[12:15],a[40:43],0\ns_add_u32 m0,s50,24576\nC v47,v162,v163\nC v68,v72,v73\nM a[240:243],a[16:19],a[40:43],0\n" \
"v_mul_f32 v176,s20,v176\nC v48,v164,v165\nM a[244:247],a[20:23],a[40:43],0\nv_mul_f32 v177,s20,v177\nC v49,v166,v167\nM a[248:251],a[24:27],a[40:43],0\nbuffer_load_dwordx4 v19,s[28:31],s46 offen lds\nM a[252:255],a[28:31],a[40:43],0\n" \
"s_add_u32 m0,s50,28672\nv_mul_f32 v178,s20,v178\nC v50,v168,v169\nM v[128:131],a[0:3],a[32:35],0\nv_mul_f32 v179,s20,v179\nC v51,v170,v171\nM v[132:135],a[4:7],a[32:35],0\nC v69,v74,v75\n" \
"v_mul_f32 v180,s20,v180\nM v[136:139],a[8:11],a[32:35],0\nbuffer_load_dwordx4 v20,s[28:31],s46 offen lds\nM v[140:143],a[12:15],a[32:35],0\ns_add_u32 m0,s51,49152\nv_mul_f32 v181,s20,v181\nC v52,v172,v173\nM v[144:147],a[16:19],a[32:35],0\n" \
"s_mov_b32 s46,384\nC v53,v174,v175\nC v70,v76,v77\nM v[148:151],a[20:23],a[32:35],0\nv_mul_f32 v182,s20,v182\nv_mul_f32 v183,s20,v183\nM v[152:155],a[24:27],a[32:35],0\nbuffer_load_dwordx4 v21,s[32:35],s47 offen offset:0 lds\n" \
"M v[156:159],a[28:31],a[32:35],0\ns_add_u32 m0,s51,50048\nv_mul_f32 v184,s20,v184\nv_mul_f32 v78,s20,v78\nM v[160:163],a[0:3],a[36:39],0\ns_waitcnt vmcnt(13) lgkmcnt(0)\nv_mul_f32 v185,s20,v185\nv_mul_f32 v79,s20,v79\n" \
"M v[164:167],a[4:7],a[36:39],0\ns_barrier\nC v72,v176,v177\nM v[168:171],a[8:11],a[36:39],0\nbuffer_store_dwordx2 v[58:59],v56,s[36:39],s23 offen sc1\nM v[172:175],a[12:15],a[36:39],0\nC v73,v178,v179\nv_mul_f32 v186,s20,v186\n" \
"M v[176:179],a[16:19],a[36:39],0\nC v74,v180,v181\nC v75,v182,v183\nv_mul_f32 v187,s20,v187\nM v[180:183],a[20:23],a[36:39],0\nC v76,v184,v185\nC v77,v186,v187\nM v[184:187],a[24:27],a[36:39],0\n" \
"C v71,v78,v79\nv_mul_f32 v188,s20,v188\nv_mul_f32 v189,s20,v189\nv_mul_f32 v190,s20,v190\nv_mul_f32 v191,s20,v191\nC v78,v188,v189\nC v79,v190,v191\nM v[188:191],a[28:31],a[36:39],0\n" \
"buffer_load_dwordx4 v21,s[32:35],s47 offen offset:128 lds\nM v[128:131],a[48:51],a[80:83],v[128:131]\ns_add_u32 m0,s51,57344\nds_read_b64_tr_b16 a[0:1],v9 offset:16384\nv_mul_f32 v80,s20,v80\nM v[160:163],a[48:51],a[84:87],v[160:163]\ns_mov_b32 s47,1835008\nds_read_b64_tr_b16 a[2:3],v10 offset:16384\n" \
"ds_read_b128 a[32:35],v7 offset:0\nM a[224:227],a[48:51],a[88:91],a[224:227]\nds_read_b128 a[36:39],v7 offset:4096\nv_mul_f32 v81,s20,v81\nM a[192:195],a[48:51],a[92:95],a[192:195]\nbuffer_load_dwordx4 v21,s[32:35],s47 offen offset:0 lds\nM a[196:199],a[52:55],a[92:95],a[196:199]\ns_add_u32 m0,s51,58240\n" \
"v_mul_f32 v112,s20,v112\nv_mul_f32 v82,s20,v82\nM a[228:231],a[52:55],a[88:91],a[228:231]\nds_read_b128 a[40:43],v7 offset:8192\nv_mul_f32 v113,s20,v113\nM v[164:167],a[52:55],a[84:87],v[164:167]\nds_read_b128 a[44:47],v7 offset:12288\nv_mul_f32 v83,s20,v83\n" \
"M v[132:135],a[52:55],a[80:83],v[132:135]\nbuffer_load_dwordx4 v21,s[32:35],s47 offen offset:128 lds\nM v[136:139],a[56:59],a[80:83],v[136:139]\ns_add_u32 m0,s50,32768\nds_read_b64_tr_b16 a[4:5],v11 offset:16384\nC v80,v80,v81\nM v[168:171],a[56:59],a[84:87],v[168:171]\ns_mov_b32 s47,2097152\n" \
"ds_read_b64_tr_b16 a[6:7],v12 offset:16384\nv_mul_f32 v114,s20,v114\nM a[232:235],a[56:59],a[88:91],a[232:235]\nds_read_b64_tr_b16 a[8:9],v9 offset:16896\nv_mul_f32 v115,s20,v115\nM a[200:203],a[56:59],a[92:95],a[200:203]\nbuffer_load_dwordx4 v13,s[28:31],s46 offen lds\nM a[204:207],a[60:63],a[92:95],a[204:207]\n" \
"s_add_u32 m0,s50,36864\nds_read_b64_tr_b16 a[10:11],v10 offset:16896\nC v112,v112,v113\nM a[236:239],a[60:63],a[88:91],a[236:239]\nds_read_b64_tr_b16 a[12:13],v11 offset:16896\nC v81,v82,v83\nM v[172:175],a[60:63],a[84:87],v[172:175]\nds_read_b64_tr_b16 a[14:15],v12 offset:16896\n" \
"ds_read_b64_tr_b16 a[16:17],v9 offset:17408\nM v[140:143],a[60:63],a[80:83],v[140:143]\nbuffer_load_dwordx4 v14,s[28:31],s46 offen lds\nM v[144:147],a[64:67],a[80:83],v[144:147]\ns_add_u32 m0,s50,40960\nv_mul_f32 v84,s20,v84\nv_mul_f32 v85,s20,v85\nM v[176:179],a[64:67],a[84:87],v[176:179]\n" \
"ds_read_b64_tr_b16 a[18:19],v10 offset:17408\nds_read_b64_tr_b16 a[20:21],v11 offset:17408\nM a[240:243],a[64:67],a[88:91],a[240:243]\nds_read_b64_tr_b16 a[22:23],v12 offset:17408\nC v113,v114,v115\nM a[208:211],a[64:67],a[92:95],a[208:211]\nbuffer_load_dwordx4 v15,s[28:31],s46 offen lds\nM a[212:215],a[68:71],a[92:95],a[212:215]\n" \
"s_add_u32 m0,s50,45056\nds_read_b64_tr_b16 a[24:25],v9 offset:17920\nv_mul_f32 v118,s20,v118\nM a[244:247],a[68:71],a[88:91],a[244:247]\nds_read_b64_tr_b16 a[26:27],v10 offset:17920\nv_mul_f32 v119,s20,v119\nM v[180:183],a[68:71],a[84:87],v[180:183]\nds_read_b64_tr_b16 a[28:29],v11 offset:17920\n" \
"v_mul_f32 v86,s20,v86\nM v[148:151],a[68:71],a[80:83],v[148:151]\nbuffer_load_dwordx4 v16,s[28:31],s46 offen lds\nM v[152:155],a[72:75],a[80:83],v[152:155]\ns_add_u32 m0,s50,49152\nds_read_b64_tr_b16 a[30:31],v12 offset:17920\nv_mul_f32 v87,s20,v87\nM v[184:187],a[72:75],a[84:87],v[184:187]\n" \
"s_mov_b32 s46,384\nds_read_b64_tr_b16 a[48:49],v9 offset:24576\nC v82,v84,v85\nM a[248:251],a[72:75],a[88:91],a[248:251]\nds_read_b64_tr_b16 a[50:51],v10 offset:24576\nds_read_b64_tr_b16 a[52:53],v11 offset:24576\nM a[216:219],a[72:75],a[92:95],a[216:219]\nbuffer_load_dwordx4 v17,s[28:31],s46 offen lds\n" \
"M a[220:223],a[76:79],a[92:95],a[220:223]\ns_add_u32 m0,s50,53248\nv_mul_f32 v88,s20,v88\nv_mul_f32 v89,s20,v89\nM a[252:255],a[76:79],a[88:91],a[252:255]\nds_read_b64_tr_b16 a[54:55],v12 offset:24576\nds_read_b64_tr_b16 a[56:57],v9 offset:25088\nM v[188:191],a[76:79],a[84:87],v[188:191]\n" \
"s_waitcnt lgkmcnt(5)\nds_read_b128 a[84:87],v8 offset:4096\nv_mul_f32 v90,s20,v90\nM v[156:159],a[76:79],a[80:83],v[156:159]\nbuffer_load_dwordx4 v18,s[28:31],s46 offen lds\nM v[128:131],a[0:3],a[32:35],v[128:131]\ns_add_u32 m0,s50,57344\nv_mul_f32 v91,s20,v91\n" \
"C v115,v118,v119\nM v[160:163],a[0:3],a[36:39],v[160:163]\nds_read_b128 a[80:83],v8 offset:0\nv_mul_f32 v124,s20,v124\nM a[224:227],a[0:3],a[40:43],a[224:227]\nds_read_b64_tr_b16 a[58:59],v10 offset:25088\nds_read_b64_tr_b16 a[60:61],v11 offset:25088\nM a[192:195],a[0:3],a[44:47],a[192:195]\n" \
"buffer_load_dwordx4 v19,s[28:31],s46 offen lds\nM a[196:199],a[4:7],a[44:47],a[196:199]\ns_add_u32 m0,s50,61440\nC v83,v86,v87\nv_mul_f32 v125,s20,v125\nM a[228:231],a[4:7],a[40:43],a[228:231]\nds_read_b64_tr_b16 a[62:63],v12 offset:25088\nds_read_b64_tr_b16 a[64:65],v9 offset:25600\n" \
"M v[164:167],a[4:7],a[36:39],v[164:167]\nds_read_b64_tr_b16 a[66:67],v10 offset:25600\nv_mul_f32 v92,s20,v92\nM v[132:135],a[4:7],a[32:35],v[132:135]\nbuffer_load_dwordx4 v20,s[28:31],s46 offen lds\nM v[136:139],a[8:11],a[32:35],v[136:139]\ns_add_u32 m0,s51,0\nds_read_b64_tr_b16 a[68:69],v11 offset:25600\n" \
"v_mul_f32 v93,s20,v93\nM v[168:171],a[8:11],a[36:39],v[168:171]\ns_mov_b32 s46,512\nds_read_b64_tr_b16 a[70:71],v12 offset:25600\nC v84,v88,v89\nM a[232:235],a[8:11],a[40:43],a[232:235]\nds_read_b64_tr_b16 a[72:73],v9 offset:26112\nC v85,v90,v91\n" \
"M a[200:203],a[8:11],a[44:47],a[200:203]\nbuffer_load_dwordx4 v21,s[32:35],s47 offen offset:0 lds\nM a[204:207],a[12:15],a[44:47],a[204:207]\ns_add_u32 m0,s51,896\nds_read_b64_tr_b16 a[74:75],v10 offset:26112\nv_mul_f32 v94,s20,v94\nM a[236:239],a[12:15],a[40:43],a[236:239]\nds_read_b64_tr_b16 a[76:77],v11 offset:26112\n" \
"v_mul_f32 v95,s20,v95\nM v[172:175],a[12:15],a[36:39],v[172:175]\nds_read_b64_tr_b16 a[78:79],v12 offset:26112\nds_read_b128 a[88:91],v8 offset:8192\nM v[140:143],a[12:15],a[32:35],v[140:143]\nbuffer_load_dwordx4 v21,s[32:35],s47 offen offset:128 lds\nM v[144:147],a[16:19],a[32:35],v[144:147]\ns_add_u32 m0,s51,8192\n" \
"C v118,v124,v125\nv_mul_f32 v96,s20,v96\nM v[176:179],a[16:19],a[36:39],v[176:179]\ns_mov_b32 s47,2359296\nds_read_b128 a[92:95],v8 offset:12288\nv_mul_f32 v97,s20,v97\nM a[240:243],a[16:19],a[40:43],a[240:243]\nC v86,v92,v93\n" \
"v_mov_b32 v88,v30\nM a[208:211],a[16:19],a[44:47],a[208:211]\nbuffer_load_dwordx4 v21,s[32:35],s47 offen offset:0 lds\nM a[212:215],a[20:23],a[44:47],a[212:215]\ns_add_u32 m0,s51,9088\nv_mov_b32 v89,v31\nv_mov_b32 v90,v32\nM a[244:247],a[20:23],a[40:43],a[244:247]\n" \
"v_mov_b32 v91,v33\nC v87,v94,v95\nM v[180:183],a[20:23],a[36:39],v[180:183]\nv_mov_b32 v124,v50\nv_mul_f32 v98,s20,v98\nM v[148:151],a[20:23],a[32:35],v[148:151]\nbuffer_store_dwordx4 v[64:67],v4,s[36:39],s96 offen offset:0 sc1\nM v[152:155],a[24:27],a[32:35],v[152:155]\n" \
"s_waitcnt vmcnt(17) lgkmcnt(0)\nv_mul_f32 v99,s20,v99\nC v96,v96,v97\nM v[184:187],a[24:27],a[36:39],v[184:187]\ns_barrier\nds_read_b64_tr_b16 a[0:1],v9 offset:32768\nM a[248:251],a[24:27],a[40:43],a[248:251]\nds_read_b64_tr_b16 a[2:3],v10 offset:32768\n" \
"ds_read_b64_tr_b16 a[4:5],v11 offset:32768\nM a[216:219],a[24:27],a[44:47],a[216:219]\nbuffer_load_dwordx4 v21,s[32:35],s47 offen offset:128 lds\nM a[220:223],a[28:31],a[44:47],a[220:223]\ns_add_u32 m0,s50,65536\nds_read_b128 a[44:47],v5 offset:12288\nv_mov_b32 v92,v34\nM a[252:255],a[28:31],a[40:43],a[252:255]\n" \
"s_mov_b32 s47,2621440\nds_read_b128 a[40:43],v5 offset:8192\nv_mov_b32 v93,v35\nM v[188:191],a[28:31],a[36:39],v[188:191]\nds_read_b128 a[36:39],v5 offset:4096\nv_mov_b32 v94,v36\nM v[156:159],a[28:31],a[32:35],v[156:159]\nbuffer_load_dwordx4 v13,s[28:31],s46 offen lds\n" \
"M v[128:131],a[48:51],a[80:83],v[128:131]\ns_add_u32 m0,s50,69632\nv_mov_b32 v95,v37\nC v97,v98,v99\nM v[160:163],a[48:51],a[84:87],v[160:163]\nds_read_b128 a[32:35],v5 offset:0\nv_mul_f32 v100,s20,v100\nM a[224:227],a[48:51],a[88:91],a[224:227]\n" \
"ds_read_b64_tr_b16 a[6:7],v12 offset:32768\nv_mul_f32 v101,s20,v101\nM a[192:195],a[48:51],a[92:95],a[192:195]\nbuffer_load_dwordx4 v14,s[28:31],s46 offen lds\nM a[196:199],a[52:55],a[92:95],a[196:199]\ns_add_u32 m0,s50,73728\nds_read_b64_tr_b16 a[8:9],v9 offset:33280\nv_mov_b32 v125,v51\n" \
"M a[228:231],a[52:55],a[88:91],a[228:231]\nds_read_b64_tr_b16 a[10:11],v10 offset:33280\nC v98,v100,v101\nM v[164:167],a[52:55],a[84:87],v[164:167]\nds_read_b64_tr_b16 a[12:13],v11 offset:33280\nds_read_b64_tr_b16 a[14:15],v12 offset:33280\nM v[132:135],a[52:55],a[80:83],v[132:135]\nbuffer_load_dwordx4 v15,s[28:31],s46 offen lds\n" \
"M v[136:139],a[56:59],a[80:83],v[136:139]\ns_add_u32 m0,s50,77824\nv_mul_f32 v104,s20,v104\nv_mul_f32 v105,s20,v105\nM v[168:171],a[56:59],a[84:87],v[168:171]\nds_read_b64_tr_b16 a[16:17],v9 offset:33792\nds_read_b64_tr_b16 a[18:19],v10 offset:33792\nM a[232:235],a[56:59],a[88:91],a[232:235]\n" \
"ds_read_b64_tr_b16 a[20:21],v11 offset:33792\nv_mul_f32 v106,s20,v106\nM a[200:203],a[56:59],a[92:95],a[200:203]\nbuffer_load_dwordx4 v16,s[28:31],s46 offen lds\nM a[204:207],a[60:63],a[92:95],a[204:207]\ns_mov_b32 s46,512\nds_read_b64_tr_b16 a[22:23],v12 offset:33792\nv_mul_f32 v107,s20,v107\n" \
"M a[236:239],a[60:63],a[88:91],a[236:239]\ns_add_u32 m0,s50,81920\nds_read_b64_tr_b16 a[24:25],v9 offset:34304\nC v100,v104,v105\nM v[172:175],a[60:63],a[84:87],v[172:175]\nds_read_b64_tr_b16 a[26:27],v10 offset:34304\nC v101,v106,v107\nM v[140:143],a[60:63],a[80:83],v[140:143]\n" \
"buffer_load_dwordx4 v17,s[28:31],s46 offen lds\nM v[144:147],a[64:67],a[80:83],v[144:147]\ns_add_u32 m0,s50,86016\nds_read_b64_tr_b16 a[28:29],v11 offset:34304\nv_mov_b32 v104,v38\nM v[176:179],a[64:67],a[84:87],v[176:179]\nds_read_b64_tr_b16 a[30:31],v12 offset:34304\nv_mov_b32 v105,v39\n" \
"M a[240:243],a[64:67],a[88:91],a[240:243]\nds_read_b64_tr_b16 a[48:49],v9 offset:40960\nds_read_b64_tr_b16 a[50:51],v10 offset:40960\nM a[208:211],a[64:67],a[92:95],a[208:211]\nbuffer_load_dwordx4 v18,s[28:31],s46 offen lds\nM a[212:215],a[68:71],a[92:95],a[212:215]\ns_add_u32 m0,s50,90112\nv_mov_b32 v106,v40\n" \
"v_mov_b32 v107,v41\nM a[244:247],a[68:71],a[88:91],a[244:247]\nds_read_b64_tr_b16 a[52:53],v11 offset:40960\nds_read_b64_tr_b16 a[54:55],v12 offset:40960\nM v[180:183],a[68:71],a[84:87],v[180:183]\nds_read_b64_tr_b16 a[56:57],v9 offset:41472\nv_mul_f32 v126,s20,v126\nM v[148:151],a[68:71],a[80:83],v[148:151]\n" \
"buffer_load_dwordx4 v19,s[28:31],s46 offen lds\nM v[152:155],a[72:75],a[80:83],v[152:155]\ns_add_u32 m0,s50,94208\nds_read_b64_tr_b16 a[58:59],v10 offset:41472\nv_mul_f32 v127,s20,v127\nM v[184:187],a[72:75],a[84:87],v[184:187]\nds_read_b64_tr_b16 a[60:61],v11 offset:41472\nC v119,v126,v127\n" \
"M a[248:251],a[72:75],a[88:91],a[248:251]\nds_read_b64_tr_b16 a[62:63],v12 offset:41472\nv_mul_f32 v102,s20,v102\nM a[216:219],a[72:75],a[92:95],a[216:219]\nbuffer_load_dwordx4 v20,s[28:31],s46 offen lds\nM a[220:223],a[76:79],a[92:95],a[220:223]\ns_add_u32 m0,s51,16384\nds_read_b64_tr_b16 a[64:65],v9 offset:41984\n" \
"v_mul_f32 v103,s20,v103\nM a[252:255],a[76:79],a[88:91],a[252:255]\ns_mov_b32 s46,640\nds_read_b64_tr_b16 a[66:67],v10 offset:41984\nv_mov_b32 v126,v52\nM v[188:191],a[76:79],a[84:87],v[188:191]\ns_waitcnt lgkmcnt(4)\nds_read_b128 a[84:87],v6 offset:4096\n" \
"C v99,v102,v103\nM v[156:159],a[76:79],a[80:83],v[156:159]\nbuffer_load_dwordx4 v21,s[32:35],s47 offen offset:0 lds\nM v[128:131],a[0:3],a[32:35],v[128:131]\ns_add_u32 m0,s51,17280\nds_read_b128 a[80:83],v6 offset:0\nv_mul_f32 v108,s20,v108\nM v[160:163],a[0:3],a[36:39],v[160:163]\n" \
"ds_read_b64_tr_b16 a[68:69],v11 offset:41984\nv_mul_f32 v109,s20,v109\nM a[224:227],a[0:3],a[40:43],a[224:227]\nds_read_b64_tr_b16 a[70:71],v12 offset:41984\nv_mul_f32 v110,s20,v110\nM a[192:195],a[0:3],a[44:47],a[192:195]\nbuffer_load_dwordx4 v21,s[32:35],s47 offen offset:128 lds\nM a[196:199],a[4:7],a[44:47],a[196:199]\n" \
"s_add_u32 m0,s51,24576\nds_read_b64_tr_b16 a[72:73],v9 offset:42496\nv_mul_f32 v111,s20,v111\nM a[228:231],a[4:7],a[40:43],a[228:231]\ns_mov_b32 s47,2883584\nds_read_b64_tr_b16 a[74:75],v10 offset:42496\nC v102,v108,v109\nM v[164:167],a[4:7],a[36:39],v[164:167]\n" \
"ds_read_b64_tr_b16 a[76:77],v11 offset:42496\nds_read_b64_tr_b16 a[78:79],v12 offset:42496\nM v[132:135],a[4:7],a[32:35],v[132:135]\nbuffer_load_dwordx4 v21,s[32:35],s47 offen offset:0 lds\nM v[136:139],a[8:11],a[32:35],v[136:139]\ns_add_u32 m0,s51,25472\nC v103,v110,v111\nv_mov_b32 v108,v42\n" \
"M v[168:171],a[8:11],a[36:39],v[168:171]\nds_read_b128 a[88:91],v6 offset:8192\nv_mov_b32 v109,v43\nM a[232:235],a[8:11],a[40:43],a[232:235]\nds_read_b128 a[92:95],v6 offset:12288\nv_mov_b32 v110,v44\nM a[200:203],a[8:11],a[44:47],a[200:203]\nbuffer_load_dwordx4 v21,s[32:35],s47 offen offset:128 lds\n" \
"M a[204:207],a[12:15],a[44:47],a[204:207]\ns_add_u32 m0,s50,0\nv_mov_b32 v111,v45\nv_mul_f32 v116,s20,v116\nM a[236:239],a[12:15],a[40:43],a[236:239]\ns_mov_b32 s47,3145728\nv_mul_f32 v117,s20,v117\nC v114,v116,v117\n" \
"M v[172:175],a[12:15],a[36:39],v[172:175]\nv_mov_b32 v127,v53\nv_mul_f32 v120,s20,v120\nM v[140:143],a[12:15],a[32:35],v[140:143]\nbuffer_load_dwordx4 v[30:33],v23,s[68:71],0 offen offset:0\nM v[144:147],a[16:19],a[32:35],v[144:147]\nv_mul_f32 v121,s20,v121\nv_mul_f32 v122,s20,v122\n" \
"M v[176:179],a[16:19],a[36:39],v[176:179]\nv_mul_f32 v123,s20,v123\nC v116,v120,v121\nM a[240:243],a[16:19],a[40:43],a[240:243]\nC v117,v122,v123\nv_mov_b32 v120,v46\nM a[208:211],a[16:19],a[44:47],a[208:211]\nv_mov_b32 v121,v47\n" \
"v_mov_b32 v122,v48\nM a[212:215],a[20:23],a[44:47],a[212:215]\nv_mov_b32 v123,v49\nv_mul_f32 v192,s20,v192\nM a[244:247],a[20:23],a[40:43],a[244:247]\nbuffer_load_dword v50,v25,s[68:71],0 offen offset:0\nM v[180:183],a[20:23],a[36:39],v[180:183]\nv_mul_f32 v193,s20,v193\n" \
"v_mul_f32 v194,s20,v194\nM v[148:151],a[20:23],a[32:35],v[148:151]\nv_mul_f32 v195,s20,v195\nC v192,v192,v193\nM v[152:155],a[24:27],a[32:35],v[152:155]\ns_waitcnt vmcnt(19) lgkmcnt(0)\nC v193,v194,v195\nv_mul_f32 v196,s20,v196\n" \
"M v[184:187],a[24:27],a[36:39],v[184:187]\ns_barrier\nds_read_b64_tr_b16 a[0:1],v9 offset:49152\nM a[248:251],a[24:27],a[40:43],a[248:251]\nds_read_b64_tr_b16 a[2:3],v10 offset:49152\nds_read_b64_tr_b16 a[4:5],v11 offset:49152\nM a[216:219],a[24:27],a[44:47],a[216:219]\nbuffer_load_dwordx4 v[34:37],v24,s[68:71],0 offen offset:0\n" \
"M a[220:223],a[28:31],a[44:47],a[220:223]\nds_read_b128 a[44:47],v5 offset:45056\nds_read_b64_tr_b16 a[6:7],v12 offset:49152\nM a[252:255],a[28:31],a[40:43],a[252:255]\nds_read_b128 a[40:43],v5 offset:40960\nv_mul_f32 v197,s20,v197\nM v[188:191],a[28:31],a[36:39],v[188:191]\nds_read_b128 a[36:39],v5 offset:36864\n" \
"v_mul_f32 v198,s20,v198\nM v[156:159],a[28:31],a[32:35],v[156:159]\nds_read_b128 a[32:35],v5 offset:32768\nv_mul_f32 v199,s20,v199\nM v[128:131],a[48:51],a[80:83],v[128:131]\nds_read_b64_tr_b16 a[8:9],v9 offset:49664\nC v194,v196,v197\nM v[160:163],a[48:51],a[84:87],v[160:163]\n" \
"buffer_load_dwordx2 v[58:59],v57,s[36:39],0 offen sc1\nM a[224:227],a[48:51],a[88:91],a[224:227]\nds_read_b64_tr_b16 a[10:11],v10 offset:49664\nds_read_b64_tr_b16 a[12:13],v11 offset:49664\nM a[192:195],a[48:51],a[92:95],a[192:195]\nbuffer_store_dwordx4 v[68:71],v4,s[36:39],s96 offen offset:1024 sc1\nM a[196:199],a[52:55],a[92:95],a[196:199]\nds_read_b64_tr_b16 a[14:15],v12 offset:49664\n" \
"ds_read_b64_tr_b16 a[16:17],v9 offset:50176\nM a[228:231],a[52:55],a[88:91],a[228:231]\nds_read_b64_tr_b16 a[18:19],v10 offset:50176\nv_mul_f32 v200,s20,v200\nM v[164:167],a[52:55],a[84:87],v[164:167]\nds_read_b64_tr_b16 a[20:21],v11 offset:50176\nds_read_b64_tr_b16 a[22:23],v12 offset:50176\nM v[132:135],a[52:55],a[80:83],v[132:135]\n" \
"buffer_load_dword v51,v24,s[68:71],0 offen offset:16\nM v[136:139],a[56:59],a[80:83],v[136:139]\nds_read_b64_tr_b16 a[24:25],v9 offset:50688\nds_read_b64_tr_b16 a[26:27],v10 offset:50688\nM v[168:171],a[56:59],a[84:87],v[168:171]\nds_read_b64_tr_b16 a[28:29],v11 offset:50688\nv_mul_f32 v201,s20,v201\nM a[232:235],a[56:59],a[88:91],a[232:235]\n" \
"ds_read_b64_tr_b16 a[30:31],v12 offset:50688\nds_read_b64_tr_b16 a[48:49],v9 offset:57344\nM a[200:203],a[56:59],a[92:95],a[200:203]\nds_read_b64_tr_b16 a[50:51],v10 offset:57344\nv_mul_f32 v202,s20,v202\nM a[204:207],a[60:63],a[92:95],a[204:207]\nds_read_b64_tr_b16 a[52:53],v11 offset:57344\nv_mul_f32 v203,s20,v203\n" \
"M a[236:239],a[60:63],a[88:91],a[236:239]\nbuffer_load_dwordx4 v[38:41],v24,s[68:71],0 offen offset:1024\nM v[172:175],a[60:63],a[84:87],v[172:175]\nds_read_b64_tr_b16 a[54:55],v12 offset:57344\nds_read_b64_tr_b16 a[56:57],v9 offset:57856\nM v[140:143],a[60:63],a[80:83],v[140:143]\nds_read_b64_tr_b16 a[58:59],v10 offset:57856\nC v195,v198,v199\n" \
"M v[144:147],a[64:67],a[80:83],v[144:147]\ns_nop 0\nds_read_b64_tr_b16 a[60:61],v11 offset:57856\nds_read_b64_tr_b16 a[62:63],v12 offset:57856\nM v[176:179],a[64:67],a[84:87],v[176:179]\nv_mul_f32 v204,s20,v204\nv_mul_f32 v205,s20,v205\nM a[240:243],a[64:67],a[88:91],a[240:243]\n" \
"C v196,v200,v201\nv_mul_f32 v208,s20,v208\nM a[208:211],a[64:67],a[92:95],a[208:211]\nbuffer_load_dwordx4 v13,s[28:31],s46 offen lds\nM a[212:215],a[68:71],a[92:95],a[212:215]\ns_add_u32 m0,s50,4096\nds_read_b64_tr_b16 a[64:65],v9 offset:58368\nv_mul_f32 v209,s20,v209\n" \
"M a[244:247],a[68:71],a[88:91],a[244:247]\nds_read_b64_tr_b16 a[66:67],v10 offset:58368\nv_mul_f32 v210,s20,v210\nM v[180:183],a[68:71],a[84:87],v[180:183]\nC v197,v202,v203\nv_mul_f32 v211,s20,v211\nM v[148:151],a[68:71],a[80:83],v[148:151]\nbuffer_load_dwordx4 v14,s[28:31],s46 offen lds\n" \
"M v[152:155],a[72:75],a[80:83],v[152:155]\ns_add_u32 m0,s50,8192\nds_read_b64_tr_b16 a[68:69],v11 offset:58368\nv_mul_f32 v212,s20,v212\nM v[184:187],a[72:75],a[84:87],v[184:187]\nds_read_b64_tr_b16 a[70:71],v12 offset:58368\nv_mul_f32 v213,s20,v213\nM a[248:251],a[72:75],a[88:91],a[248:251]\n" \
"v_mul_f32 v214,s20,v214\nv_mul_f32 v215,s20,v215\nM a[216:219],a[72:75],a[92:95],a[216:219]\nbuffer_load_dwordx4 v15,s[28:31],s46 offen lds\nM a[220:223],a[76:79],a[92:95],a[220:223]\ns_add_u32 m0,s50,12288\nds_read_b64_tr_b16 a[72:73],v9 offset:58880\nv_mul_f32 v206,s20,v206\n" \
"M a[252:255],a[76:79],a[88:91],a[252:255]\nds_read_b64_tr_b16 a[74:75],v10 offset:58880\nds_read_b128 a[88:91],v6 offset:40960\nM v[188:191],a[76:79],a[84:87],v[188:191]\ns_waitcnt lgkmcnt(4)\nds_read_b128 a[84:87],v6 offset:36864\nv_mul_f32 v207,s20,v207\nM v[156:159],a[76:79],a[80:83],v[156:159]\n" \
"buffer_load_dwordx4 v16,s[28:31],s46 offen lds\nM v[128:131],a[0:3],a[32:35],v[128:131]\ns_add_u32 m0,s50,16384\nC v198,v204,v205\nv_mul_f32 v216,s20,v216\nM v[160:163],a[0:3],a[36:39],v[160:163]\ns_mov_b32 s46,640\nds_read_b128 a[80:83],v6 offset:32768\n" \
"v_mul_f32 v217,s20,v217\nM a[224:227],a[0:3],a[40:43],a[224:227]\nds_read_b64_tr_b16 a[76:77],v11 offset:58880\nv_mul_f32 v218,s20,v218\nM a[192:195],a[0:3],a[44:47],a[192:195]\nbuffer_load_dword v52,v24,s[68:71],0 offen offset:1040\nM a[196:199],a[4:7],a[44:47],a[196:199]\nds_read_b64_tr_b16 a[78:79],v12 offset:58880\n" \
"ds_read_b128 a[92:95],v6 offset:45056\nM a[228:231],a[4:7],a[40:43],a[228:231]\nv_mul_f32 v219,s20,v219\nC v200,v208,v209\nM v[164:167],a[4:7],a[36:39],v[164:167]\nv_mul_f32 v224,s20,v224\nv_mul_f32 v225,s20,v225\nM v[132:135],a[4:7],a[32:35],v[132:135]\n" \
"v_mul_f32 v226,s20,v226\nv_mul_f32 v227,s20,v227\nM v[136:139],a[8:11],a[32:35],v[136:139]\nC v201,v210,v211\nv_mul_f32 v228,s20,v228\nM v[168:171],a[8:11],a[36:39],v[168:171]\nbuffer_load_dwordx4 v17,s[28:31],s46 offen lds\nM a[232:235],a[8:11],a[40:43],a[232:235]\n" \
"s_add_u32 m0,s50,20480\nv_mul_f32 v229,s20,v229\nv_mul_f32 v230,s20,v230\nM a[200:203],a[8:11],a[44:47],a[200:203]\nv_mul_f32 v231,s20,v231\nC v202,v212,v213\nM a[204:207],a[12:15],a[44:47],a[204:207]\nv_mul_f32 v232,s20,v232\n" \
"v_mul_f32 v233,s20,v233\nM a[236:239],a[12:15],a[40:43],a[236:239]\nbuffer_load_dwordx4 v[42:45],v24,s[68:71],0 offen offset:2048\nM v[172:175],a[12:15],a[36:39],v[172:175]\nv_mul_f32 v234,s20,v234\nv_mul_f32 v235,s20,v235\nM v[140:143],a[12:15],a[32:35],v[140:143]\nC v203,v214,v215\n" \
"v_mul_f32 v236,s20,v236\nM v[144:147],a[16:19],a[32:35],v[144:147]\nv_mul_f32 v237,s20,v237\nv_mul_f32 v238,s20,v238\nM v[176:179],a[16:19],a[36:39],v[176:179]\nv_mul_f32 v239,s20,v239\nC v199,v206,v207\nM a[240:243],a[16:19],a[40:43],a[240:243]\n" \
"C v204,v216,v217\nv_mul_f32 v220,s20,v220\nM a[208:211],a[16:19],a[44:47],a[208:211]\ns_waitcnt vmcnt(10)\nbuffer_store_dwordx4 v[80:83],v4,s[36:39],s96 offen offset:2048 sc1\nM a[212:215],a[20:23],a[44:47],a[212:215]\nv_cmp_gt_u64 s[98:99],s[14:15],v[58:59]\nv_cmp_le_u64 s[18:19],s[16:17],v[58:59]\n" \
"M a[244:247],a[20:23],a[40:43],a[244:247]\ns_or_b64 s[98:99],s[98:99],s[18:19]\ns_cbranch_scc1 LPU_%=\nLPUR_%=:\nv_mul_f32 v221,s20,v221\nv_mul_f32 v240,s20,v240\nM v[180:183],a[20:23],a[36:39],v[180:183]\nv_mul_f32 v241,s20,v241\n" \
"v_mul_f32 v242,s20,v242\nM v[148:151],a[20:23],a[32:35],v[148:151]\nbuffer_load_dword v53,v24,s[68:71],0 offen offset:2064\nM v[152:155],a[24:27],a[32:35],v[152:155]\ns_waitcnt vmcnt(20) lgkmcnt(0)\nv_mul_f32 v243,s20,v243\nv_mul_f32 v222,s20,v222\nM v[184:187],a[24:27],a[36:39],v[184:187]\n" \
"s_barrier\nds_read_b64_tr_b16 a[0:1],v9 offset:0\nM a[248:251],a[24:27],a[40:43],a[248:251]\nds_read_b64_tr_b16 a[2:3],v10 offset:0\nds_read_b64_tr_b16 a[4:5],v11 offset:0\nM a[216:219],a[24:27],a[44:47],a[216:219]\nds_read_b64_tr_b16 a[6:7],v12 offset:0\nv_mul_f32 v223,s20,v223\n" \
"M a[220:223],a[28:31],a[44:47],a[220:223]\nds_read_b128 a[44:47],v7 offset:12288\nC v205,v218,v219\nM a[252:255],a[28:31],a[40:43],a[252:255]\nbuffer_load_dwordx4 v[46:49],v24,s[68:71],0 offen offset:3072\nM v[188:191],a[28:31],a[36:39],v[188:191]\nds_read_b128 a[36:39],v7 offset:4096\nds_read_b128 a[40:43],v7 offset:8192\n" \
"M v[156:159],a[28:31],a[32:35],v[156:159]\nds_read_b128 a[32:35],v7 offset:0\nC v208,v224,v225\nM v[128:131],a[48:51],a[80:83],v[128:131]\nds_read_b64_tr_b16 a[8:9],v9 offset:512\nC v209,v226,v227\nM v[160:163],a[48:51],a[84:87],v[160:163]\nds_read_b64_tr_b16 a[10:11],v10 offset:512\n" \
"v_mul_f32 v244,s20,v244\nM a[224:227],a[48:51],a[88:91],a[224:227]\nds_read_b64_tr_b16 a[12:13],v11 offset:512\nv_mul_f32 v245,s20,v245\nM a[192:195],a[48:51],a[92:95],a[192:195]\nbuffer_load_dword v54,v24,s[68:71],0 offen offset:3088\nM a[196:199],a[52:55],a[92:95],a[196:199]\nds_read_b64_tr_b16 a[14:15],v12 offset:512\n" \
"ds_read_b64_tr_b16 a[16:17],v9 offset:1024\nM a[228:231],a[52:55],a[88:91],a[228:231]\nds_read_b64_tr_b16 a[18:19],v10 offset:1024\nv_mul_f32 v246,s20,v246\nM v[164:167],a[52:55],a[84:87],v[164:167]\nds_read_b64_tr_b16 a[20:21],v11 offset:1024\nds_read_b64_tr_b16 a[22:23],v12 offset:1024\nM v[132:135],a[52:55],a[80:83],v[132:135]\n" \
"ds_read_b64_tr_b16 a[24:25],v9 offset:1536\nv_mul_f32 v247,s20,v247\nM v[136:139],a[56:59],a[80:83],v[136:139]\nds_read_b64_tr_b16 a[26:27],v10 offset:1536\nC v210,v228,v229\nM v[168:171],a[56:59],a[84:87],v[168:171]\nglobal_load_short_d16_hi v28,v29,s[12:13]\nM a[232:235],a[56:59],a[88:91],a[232:235]\n" \
"ds_read_b64_tr_b16 a[28:29],v11 offset:1536\nds_read_b64_tr_b16 a[30:31],v12 offset:1536\nM a[200:203],a[56:59],a[92:95],a[200:203]\nds_read_b64_tr_b16 a[48:49],v9 offset:8192\nC v211,v230,v231\nM a[204:207],a[60:63],a[92:95],a[204:207]\nds_read_b64_tr_b16 a[50:51],v10 offset:8192\nds_read_b64_tr_b16 a[52:53],v11 offset:8192\n" \
"M a[236:239],a[60:63],a[88:91],a[236:239]\nds_read_b64_tr_b16 a[54:55],v12 offset:8192\nC v212,v232,v233\nM v[172:175],a[60:63],a[84:87],v[172:175]\nds_read_b64_tr_b16 a[56:57],v9 offset:8704\nC v213,v234,v235\nM v[140:143],a[60:63],a[80:83],v[140:143]\nbuffer_load_dwordx4 v18,s[28:31],s46 offen lds\n" \
"M v[144:147],a[64:67],a[80:83],v[144:147]\ns_add_u32 m0,s50,24576\nds_read_b64_tr_b16 a[58:59],v10 offset:8704\nC v214,v236,v237\nM v[176:179],a[64:67],a[84:87],v[176:179]\nds_read_b64_tr_b16 a[60:61],v11 offset:8704\nC v215,v238,v239\nM a[240:243],a[64:67],a[88:91],a[240:243]\n" \
"ds_read_b64_tr_b16 a[62:63],v12 offset:8704\nC v206,v220,v221\nM a[208:211],a[64:67],a[92:95],a[208:211]\nbuffer_load_dwordx4 v19,s[28:31],s46 offen lds\nM a[212:215],a[68:71],a[92:95],a[212:215]\ns_add_u32 m0,s50,28672\nds_read_b64_tr_b16 a[64:65],v9 offset:9216\nC v216,v240,v241\n" \
"M a[244:247],a[68:71],a[88:91],a[244:247]\nds_read_b64_tr_b16 a[66:67],v10 offset:9216\nC v217,v242,v243\nM v[180:183],a[68:71],a[84:87],v[180:183]\nv_mul_f32 v248,s20,v248\nv_mul_f32 v249,s20,v249\nM v[148:151],a[68:71],a[80:83],v[148:151]\nbuffer_load_dwordx4 v20,s[28:31],s46 offen lds\n" \
"M v[152:155],a[72:75],a[80:83],v[152:155]\ns_add_u32 m0,s56,0\nds_read_b64_tr_b16 a[68:69],v11 offset:9216\nv_mul_f32 v250,s20,v250\nM v[184:187],a[72:75],a[84:87],v[184:187]\ns_mov_b32 s46,768\nds_read_b64_tr_b16 a[70:71],v12 offset:9216\nC v207,v222,v223\n" \
"M a[248:251],a[72:75],a[88:91],a[248:251]\nv_mul_f32 v251,s20,v251\nC v218,v244,v245\nM a[216:219],a[72:75],a[92:95],a[216:219]\nbuffer_load_dwordx4 v23,s[68:71],0 offen offset:0 lds\nM a[220:223],a[76:79],a[92:95],a[220:223]\ns_add_u32 m0,s56,1024\nds_read_b64_tr_b16 a[72:73],v9 offset:9728\n" \
"C v219,v246,v247\nM a[252:255],a[76:79],a[88:91],a[252:255]\nds_read_b64_tr_b16 a[74:75],v10 offset:9728\nds_read_b128 a[88:91],v8 offset:8192\nM v[188:191],a[76:79],a[84:87],v[188:191]\ns_waitcnt lgkmcnt(4)\nds_read_b128 a[84:87],v8 offset:4096\nv_mul_f32 v252,s20,v252\n" \
"M v[156:159],a[76:79],a[80:83],v[156:159]\nbuffer_load_dwordx4 v24,s[68:71],0 offen offset:0 lds\nM v[128:131],a[0:3],a[32:35],v[128:131]\ns_add_u32 m0,s56,1024\nv_mul_f32 v253,s20,v253\nv_mul_f32 v254,s20,v254\nM v[160:163],a[0:3],a[36:39],v[160:163]\nds_read_b128 a[80:83],v8 offset:0\n" \
"v_mul_f32 v255,s20,v255\nM a[224:227],a[0:3],a[40:43],a[224:227]\nds_read_b64_tr_b16 a[76:77],v11 offset:9728\nC v220,v248,v249\nM a[192:195],a[0:3],a[44:47],a[192:195]\nbuffer_load_dwordx4 v24,s[68:71],0 offen offset:1024 lds\nM a[196:199],a[4:7],a[44:47],a[196:199]\ns_add_u32 m0,s56,1024\n" \
"ds_read_b64_tr_b16 a[78:79],v12 offset:9728\nC v221,v250,v251\nM a[228:231],a[4:7],a[40:43],a[228:231]\nds_read_b128 a[92:95],v8 offset:12288\nC v222,v252,v253\nM v[164:167],a[4:7],a[36:39],v[164:167]\nC v223,v254,v255\nM v[132:135],a[4:7],a[32:35],v[132:135]\n" \
"buffer_load_dwordx4 v24,s[68:71],0 offen offset:2048 lds\nM v[136:139],a[8:11],a[32:35],v[136:139]\ns_add_u32 m0,s56,1024\nM v[168:171],a[8:11],a[36:39],v[168:171]\nM a[232:235],a[8:11],a[40:43],a[232:235]\nM a[200:203],a[8:11],a[44:47],a[200:203]\nbuffer_load_dwordx4 v24,s[68:71],0 offen offset:3072 lds\nM a[204:207],a[12:15],a[44:47],a[204:207]\n" \
"s_add_u32 m0,s56,10304\nM a[236:239],a[12:15],a[40:43],a[236:239]\nM v[172:175],a[12:15],a[36:39],v[172:175]\nM v[140:143],a[12:15],a[32:35],v[140:143]\nM v[144:147],a[16:19],a[32:35],v[144:147]\nbuffer_store_dwordx4 v[84:87],v4,s[36:39],s96 offen offset:3072 sc1\nM v[176:179],a[16:19],a[36:39],v[176:179]\ns_add_u32 s96,s57,36864\n" \
"M a[240:243],a[16:19],a[40:43],a[240:243]\nM a[208:211],a[16:19],a[44:47],a[208:211]\nM a[212:215],a[20:23],a[44:47],a[212:215]\ns_mov_b64 exec,0xff\nglobal_load_dword v0,v2,s[100:101]\ns_mov_b64 exec,-1\nM a[244:247],a[20:23],a[40:43],a[244:247]\nM v[180:183],a[20:23],a[36:39],v[180:183]\n" \
"buffer_load_dwordx4 v26,s[72:75],0 offen offset:0 sc1 lds\nM v[148:151],a[20:23],a[32:35],v[148:151]\ns_add_u32 m0,s56,11328\nM v[152:155],a[24:27],a[32:35],v[152:155]\ns_waitcnt vmcnt(8) lgkmcnt(0)\nM v[184:187],a[24:27],a[36:39],v[184:187]\ns_barrier\nds_read_b64_tr_b16 a[0:1],v9 offset:16384\n" \
"M a[248:251],a[24:27],a[40:43],a[248:251]\nbuffer_load_dwordx4 v27,s[72:75],0 offen offset:0 sc1 lds\nM a[216:219],a[24:27],a[44:47],a[216:219]\ns_add_u32 m0,s56,11328\nds_read_b64_tr_b16 a[2:3],v10 offset:16384\nM a[220:223],a[28:31],a[44:47],a[220:223]\nds_read_b64_tr_b16 a[4:5],v11 offset:16384\nds_read_b128 a[44:47],v5 offset:12288\n" \
"M a[252:255],a[28:31],a[40:43],a[252:255]\nds_read_b128 a[40:43],v5 offset:8192\nM v[188:191],a[28:31],a[36:39],v[188:191]\nbuffer_load_dwordx4 v27,s[72:75],0 offen offset:1024 sc1 lds\nM v[156:159],a[28:31],a[32:35],v[156:159]\ns_add_u32 m0,s56,11328\nM v[128:131],a[48:51],a[80:83],v[128:131]\nds_read_b128 a[32:35],v5 offset:0\n" \
"M v[160:163],a[48:51],a[84:87],v[160:163]\nds_read_b128 a[36:39],v5 offset:4096\nM a[224:227],a[48:51],a[88:91],a[224:227]\nds_read_b64_tr_b16 a[6:7],v12 offset:16384\nM a[192:195],a[48:51],a[92:95],a[192:195]\nbuffer_load_dwordx4 v27,s[72:75],0 offen offset:2048 sc1 lds\nM a[196:199],a[52:55],a[92:95],a[196:199]\ns_add_u32 m0,s56,11328\n" \
"ds_read_b64_tr_b16 a[8:9],v9 offset:16896\nM a[228:231],a[52:55],a[88:91],a[228:231]\nds_read_b64_tr_b16 a[10:11],v10 offset:16896\nM v[164:167],a[52:55],a[84:87],v[164:167]\nds_read_b64_tr_b16 a[12:13],v11 offset:16896\nds_read_b64_tr_b16 a[14:15],v12 offset:16896\nM v[132:135],a[52:55],a[80:83],v[132:135]\nbuffer_load_dwordx4 v27,s[72:75],0 offen offset:3072 sc1 lds\n" \
"M v[136:139],a[56:59],a[80:83],v[136:139]\ns_add_u32 m0,s51,0\nM v[168:171],a[56:59],a[84:87],v[168:171]\ns_mov_b32 s74,0\nds_read_b64_tr_b16 a[16:17],v9 offset:17408\nds_read_b64_tr_b16 a[18:19],v10 offset:17408\nM a[232:235],a[56:59],a[88:91],a[232:235]\nds_read_b64_tr_b16 a[20:21],v11 offset:17408\n" \
"M a[200:203],a[56:59],a[92:95],a[200:203]\nds_read_b64_tr_b16 a[22:23],v12 offset:17408\nM a[204:207],a[60:63],a[92:95],a[204:207]\nbuffer_load_dwordx4 v21,s[32:35],s47 offen offset:0 lds\nM a[236:239],a[60:63],a[88:91],a[236:239]\ns_add_u32 m0,s51,896\nds_read_b64_tr_b16 a[24:25],v9 offset:17920\nM v[172:175],a[60:63],a[84:87],v[172:175]\n" \
"ds_read_b64_tr_b16 a[26:27],v10 offset:17920\nM v[140:143],a[60:63],a[80:83],v[140:143]\nds_read_b64_tr_b16 a[28:29],v11 offset:17920\nds_read_b64_tr_b16 a[30:31],v12 offset:17920\nM v[144:147],a[64:67],a[80:83],v[144:147]\nbuffer_load_dwordx4 v21,s[32:35],s47 offen offset:128 lds\nM v[176:179],a[64:67],a[84:87],v[176:179]\ns_add_u32 m0,s51,8192\n" \
"M a[240:243],a[64:67],a[88:91],a[240:243]\ns_mov_b32 s47,3407872\nds_read_b64_tr_b16 a[48:49],v9 offset:24576\nds_read_b64_tr_b16 a[50:51],v10 offset:24576\nM a[208:211],a[64:67],a[92:95],a[208:211]\nds_read_b64_tr_b16 a[52:53],v11 offset:24576\nM a[212:215],a[68:71],a[92:95],a[212:215]\nds_read_b64_tr_b16 a[54:55],v12 offset:24576\n" \
"M a[244:247],a[68:71],a[88:91],a[244:247]\ns_waitcnt vmcnt(17)\nv_alignbyte_b32 v30,v31,v30,2\nv_alignbyte_b32 v31,v32,v31,2\nM v[180:183],a[68:71],a[84:87],v[180:183]\nv_alignbyte_b32 v32,v33,v32,2\nv_alignbyte_b32 v33,v50,v33,2\nM v[148:151],a[68:71],a[80:83],v[148:151]\n" \
"ds_write_b128 v55,v[30:33] offset:5184\nM v[152:155],a[72:75],a[80:83],v[152:155]\nv_alignbyte_b32 v34,v35,v34,2\nv_alignbyte_b32 v35,v36,v35,2\nM v[184:187],a[72:75],a[84:87],v[184:187]\nv_alignbyte_b32 v36,v37,v36,2\nv_alignbyte_b32 v37,v51,v37,2\nM a[248:251],a[72:75],a[88:91],a[248:251]\n" \
"v_lshrrev_b32 v30,4,v4\nv_alignbyte_b32 v46,v47,v46,2\nM a[216:219],a[72:75],a[92:95],a[216:219]\nds_write_b128 v55,v[34:37] offset:6208\nM a[220:223],a[76:79],a[92:95],a[220:223]\nv_and_b32 v31,15,v30\nv_alignbyte_b32 v47,v48,v47,2\nM a[252:255],a[76:79],a[88:91],a[252:255]\n" \
"v_add_u32 v33,1,v31\nv_alignbyte_b32 v48,v49,v48,2\nM v[188:191],a[76:79],a[84:87],v[188:191]\nv_and_b32 v34,1,v33\nv_alignbyte_b32 v49,v54,v49,2\nM v[156:159],a[76:79],a[80:83],v[156:159]\ns_waitcnt lgkmcnt(1)\nv_lshrrev_b32 v32,4,v30\n" \
"v_and_b32 v33,30,v33\nM v[128:131],a[0:3],a[32:35],v[128:131]\nv_mul_u32_u24 v34,5184,v34\nv_lshl_add_u32 v33,v33,1,v34\nM v[160:163],a[0:3],a[36:39],v[160:163]\nds_write_b128 v55,v[46:49] offset:9280\nM a[224:227],a[0:3],a[40:43],a[224:227]\nv_lshl_add_u32 v33,v32,4,v33\n" \
"v_add_u32 v46,s56,v33\nM a[192:195],a[0:3],a[44:47],a[192:195]\nv_lshlrev_b32 v33,1,v31\nv_sub_u32 v33,v33,v32\nM a[196:199],a[4:7],a[44:47],a[196:199]\nds_read_b128 a[84:87],v6 offset:4096\nv_lshlrev_b32 v33,4,v33\nv_add_u32 v33,10320,v33\n" \
"M a[228:231],a[4:7],a[40:43],a[228:231]\nds_read_b128 a[80:83],v6 offset:0\nv_add_u32 v50,s56,v33\nv_bfe_u32 v33,v4,6,3\nM v[164:167],a[4:7],a[36:39],v[164:167]\nds_read_b64_tr_b16 a[56:57],v9 offset:25088\nv_lshlrev_b32 v60,17,v33\nv_and_b32 v33,16,v4\n" \
"M v[132:135],a[4:7],a[32:35],v[132:135]\nv_lshl_add_u32 v60,v33,2,v60\nv_and_b32 v33,32,v4\nv_alignbyte_b32 v38,v39,v38,2\nM v[136:139],a[8:11],a[32:35],v[136:139]\nv_add_u32 v60,v60,v33\nv_lshrrev_b32 v33,9,v4\nv_alignbyte_b32 v39,v40,v39,2\n" \
"M v[168:171],a[8:11],a[36:39],v[168:171]\nv_lshl_add_u32 v60,v33,4,v60\nv_lshl_add_u32 v33,v32,3,v31\nv_alignbyte_b32 v40,v41,v40,2\nM a[232:235],a[8:11],a[40:43],a[232:235]\nv_sub_u32 v33,15,v33\nv_and_b32 v34,16,v4\nv_alignbyte_b32 v41,v52,v41,2\n" \
"M a[200:203],a[8:11],a[44:47],a[200:203]\nv_lshrrev_b32 v33,1,v33\nv_cmp_eq_u32 vcc,0,v33\nv_alignbyte_b32 v42,v43,v42,2\nM a[204:207],a[12:15],a[44:47],a[204:207]\nv_lshrrev_b32 v34,v34,v28\nv_alignbyte_b32 v43,v44,v43,2\nv_alignbyte_b32 v44,v45,v44,2\n" \
"M a[236:239],a[12:15],a[40:43],a[236:239]\nv_cndmask_b32 v52,0,v34,vcc\nv_cmp_eq_u32 vcc,1,v33\nv_alignbyte_b32 v45,v53,v45,2\nM v[172:175],a[12:15],a[36:39],v[172:175]\nds_read_b64_tr_b16 a[58:59],v10 offset:25088\nds_read_b64_tr_b16 a[60:61],v11 offset:25088\nM v[140:143],a[12:15],a[32:35],v[140:143]\n" \
"ds_read_b64_tr_b16 a[62:63],v12 offset:25088\nv_cndmask_b32 v53,0,v34,vcc\nM v[144:147],a[16:19],a[32:35],v[144:147]\nbuffer_store_dwordx4 v[96:99],v4,s[36:39],s96 offen offset:0 sc1\nds_read_b64_tr_b16 a[64:65],v9 offset:25600\nM v[176:179],a[16:19],a[36:39],v[176:179]\nds_read_b64_tr_b16 a[66:67],v10 offset:25600\nds_read_b64_tr_b16 a[68:69],v11 offset:25600\n" \
"M a[240:243],a[16:19],a[40:43],a[240:243]\nds_read_b64_tr_b16 a[70:71],v12 offset:25600\nds_read_b64_tr_b16 a[72:73],v9 offset:26112\nds_read_b64_tr_b16 a[74:75],v10 offset:26112\nds_read_b64_tr_b16 a[76:77],v11 offset:26112\nds_read_b64_tr_b16 a[78:79],v12 offset:26112\nds_read_b128 a[88:91],v6 offset:8192\nds_read_b128 a[92:95],v6 offset:12288\n" \
"M a[208:211],a[16:19],a[44:47],a[208:211]\nv_cmp_eq_u32 vcc,2,v33\nv_add_u32 v47,1024,v46\nM a[212:215],a[20:23],a[44:47],a[212:215]\nv_cndmask_b32 v54,0,v34,vcc\nv_cmp_eq_u32 vcc,3,v33\nM a[244:247],a[20:23],a[40:43],a[244:247]\nds_write_b128 v55,v[38:41] offset:7232\n" \
"M v[180:183],a[20:23],a[36:39],v[180:183]\nv_add_u32 v48,2048,v46\nv_add_u32 v49,3072,v46\nM v[148:151],a[20:23],a[32:35],v[148:151]\nbuffer_load_dwordx4 v21,s[32:35],s47 offen offset:0 lds\nM v[152:155],a[24:27],a[32:35],v[152:155]\ns_waitcnt lgkmcnt(1)\nv_accvgpr_read_b32 v30,a128\n" \
"v_accvgpr_read_b32 v31,a129\nM v[184:187],a[24:27],a[36:39],v[184:187]\ns_add_u32 m0,s51,9088\ns_barrier\nv_accvgpr_read_b32 v32,a130\nM a[248:251],a[24:27],a[40:43],a[248:251]\nds_write_b128 v55,v[42:45] offset:8256\nM a[216:219],a[24:27],a[44:47],a[216:219]\n" \
"v_cndmask_b32 v55,0,v34,vcc\nv_accvgpr_read_b32 v33,a131\nM a[220:223],a[28:31],a[44:47],a[220:223]\nbuffer_load_dwordx4 v21,s[32:35],s47 offen offset:128 lds\nM a[252:255],a[28:31],a[40:43],a[252:255]\ns_add_u32 m0,s50,65536\nv_mul_f32 v30,s20,v30\nv_mul_f32 v31,s20,v31\n" \
"M v[188:191],a[28:31],a[36:39],v[188:191]\ns_mov_b32 s47,3670016\nv_mul_f32 v32,s20,v32\nv_mul_f32 v33,s20,v33\nM v[156:159],a[28:31],a[32:35],v[156:159]\nC v30,v30,v31\nv_accvgpr_read_b32 v34,a132\nM v[128:131],a[48:51],a[80:83],v[128:131]\n" \
"buffer_load_dwordx4 v13,s[28:31],s46 offen lds\nM v[160:163],a[48:51],a[84:87],v[160:163]\ns_waitcnt vmcnt(6) lgkmcnt(0)\nds_read_b128 a[32:35],v50 offset:512\nv_accvgpr_read_b32 v35,a133\nM a[224:227],a[48:51],a[88:91],a[224:227]\ns_add_u32 m0,s50,69632\nds_read_b128 a[36:39],v50 offset:1024\n" \
"v_accvgpr_read_b32 v36,a134\nM a[192:195],a[48:51],a[92:95],a[192:195]\nds_read_b128 a[40:43],v50 offset:1536\nv_accvgpr_read_b32 v37,a135\nM a[196:199],a[52:55],a[92:95],a[196:199]\nds_read_b128 a[44:47],v50 offset:2048\nv_accvgpr_read_b32 v38,a136\nM a[228:231],a[52:55],a[88:91],a[228:231]\n" \
"buffer_store_dwordx4 v[100:103],v4,s[36:39],s96 offen offset:1024 sc1\nM v[164:167],a[52:55],a[84:87],v[164:167]\nds_read_b128 a[48:51],v50 offset:2560\nds_read2_b32 a[0:1],v46 offset0:0 offset1:1\nM v[132:135],a[52:55],a[80:83],v[132:135]\nds_read_b128 a[52:55],v50 offset:3072\nC v31,v32,v33\nM v[136:139],a[56:59],a[80:83],v[136:139]\n" \
"ds_read2_b32 a[2:3],v46 offset0:2 offset1:3\nv_accvgpr_read_b32 v39,a137\nM v[168:171],a[56:59],a[84:87],v[168:171]\nds_read2_b32 a[4:5],v46 offset0:128 offset1:129\nv_mul_f32 v34,s20,v34\nM a[232:235],a[56:59],a[88:91],a[232:235]\nds_read2_b32 a[6:7],v46 offset0:130 offset1:131\nds_read2_b32 a[8:9],v47 offset0:0 offset1:1\n" \
"M a[200:203],a[56:59],a[92:95],a[200:203]\nds_read_b128 a[56:59],v50 offset:3584\nv_mul_f32 v35,s20,v35\nM a[204:207],a[60:63],a[92:95],a[204:207]\nds_read2_b32 a[10:11],v47 offset0:2 offset1:3\nv_mul_f32 v36,s20,v36\nM a[236:239],a[60:63],a[88:91],a[236:239]\nds_read2_b32 a[12:13],v47 offset0:128 offset1:129\n" \
"v_mul_f32 v37,s20,v37\nM v[172:175],a[60:63],a[84:87],v[172:175]\nds_read2_b32 a[14:15],v47 offset0:130 offset1:131\nds_read2_b32 a[16:17],v48 offset0:0 offset1:1\nM v[140:143],a[60:63],a[80:83],v[140:143]\nds_read_b128 a[60:63],v50 offset:4096\nv_accvgpr_read_b32 v40,a138\nM v[144:147],a[64:67],a[80:83],v[144:147]\n" \
"ds_read2_b32 a[18:19],v48 offset0:2 offset1:3\nv_accvgpr_read_b32 v41,a139\nM v[176:179],a[64:67],a[84:87],v[176:179]\nds_read2_b32 a[20:21],v48 offset0:128 offset1:129\nv_mul_f32 v38,s20,v38\nM a[240:243],a[64:67],a[88:91],a[240:243]\nbuffer_store_dwordx4 v[112:115],v4,s[36:39],s96 offen offset:2048 sc1\nM a[208:211],a[64:67],a[92:95],a[208:211]\n" \
"ds_read2_b32 a[22:23],v48 offset0:130 offset1:131\nds_read2_b32 a[24:25],v49 offset0:0 offset1:1\nM a[212:215],a[68:71],a[92:95],a[212:215]\nds_read2_b32 a[26:27],v49 offset0:2 offset1:3\nv_mul_f32 v39,s20,v39\nM a[244:247],a[68:71],a[88:91],a[244:247]\nds_read2_b32 a[28:29],v49 offset0:128 offset1:129\nds_read2_b32 a[30:31],v49 offset0:130 offset1:131\n" \
"M v[180:183],a[68:71],a[84:87],v[180:183]\ns_nop 0\nds_read_b128 a[96:99],v50 offset:448\nC v32,v34,v35\nM v[148:151],a[68:71],a[80:83],v[148:151]\nds_read2_b32 a[64:65],v46 offset0:16 offset1:17\nC v33,v36,v37\nM v[152:155],a[72:75],a[80:83],v[152:155]\n" \
"ds_read2_b32 a[66:67],v46 offset0:18 offset1:19\nv_accvgpr_write_b32 a128,v30\nM v[184:187],a[72:75],a[84:87],v[184:187]\nds_read_b128 a[100:103],v50 offset:960\nv_accvgpr_write_b32 a129,v31\nM a[248:251],a[72:75],a[88:91],a[248:251]\nds_read2_b32 a[68:69],v46 offset0:144 offset1:145\nds_read2_b32 a[70:71],v46 offset0:146 offset1:147\n" \
"M a[216:219],a[72:75],a[92:95],a[216:219]\nds_read_b128 a[104:107],v50 offset:1472\nv_mul_f32 v40,s20,v40\nM a[220:223],a[76:79],a[92:95],a[220:223]\nds_read2_b32 a[72:73],v47 offset0:16 offset1:17\nv_mul_f32 v41,s20,v41\nM a[252:255],a[76:79],a[88:91],a[252:255]\nds_read2_b32 a[74:75],v47 offset0:18 offset1:19\n" \
"C v38,v38,v39\nM v[188:191],a[76:79],a[84:87],v[188:191]\ns_waitcnt lgkmcnt(6)\nds_read_b128 a[108:111],v50 offset:1984\nv_accvgpr_read_b32 v42,a140\nM v[156:159],a[76:79],a[80:83],v[156:159]\nbuffer_store_dwordx4 v[116:119],v4,s[36:39],s96 offen offset:3072 sc1\nM v[224:227],v[52:55],a[32:35],0\n" \
"s_add_u32 s96,s57,0\nds_read2_b32 a[76:77],v47 offset0:144 offset1:145\nds_read2_b32 a[78:79],v47 offset0:146 offset1:147\nM v[228:231],v[52:55],a[36:39],0\nds_read_b128 a[112:115],v50 offset:2496\nv_accvgpr_read_b32 v43,a141\nM v[232:235],v[52:55],a[40:43],0\nds_read2_b32 a[80:81],v48 offset0:16 offset1:17\n" \
"v_accvgpr_read_b32 v44,a142\nM v[236:239],v[52:55],a[44:47],0\nds_read2_b32 a[82:83],v48 offset0:18 offset1:19\nds_read_b128 a[116:119],v50 offset:3008\nM v[240:243],v[52:55],a[48:51],0\nds_read2_b32 a[84:85],v48 offset0:144 offset1:145\nv_accvgpr_read_b32 v45,a143\nM v[244:247],v[52:55],a[52:55],0\n" \
"ds_read2_b32 a[86:87],v48 offset0:146 offset1:147\nv_accvgpr_write_b32 a130,v32\nM v[248:251],v[52:55],a[56:59],0\nds_read_b128 a[120:123],v50 offset:3520\nv_accvgpr_write_b32 a131,v33\nM v[252:255],v[52:55],a[60:63],0\nds_read2_b32 a[88:89],v49 offset0:16 offset1:17\nv_accvgpr_read_b32 v30,a144\n" \
"M v[224:227],a[0:3],a[32:35],v[224:227]\nds_read2_b32 a[90:91],v49 offset0:18 offset1:19\nds_read_b128 a[124:127],v50 offset:4032\nM v[228:231],a[0:3],a[36:39],v[228:231]\nds_read2_b32 a[92:93],v49 offset0:144 offset1:145\nv_accvgpr_read_b32 v31,a145\nM v[228:231],a[4:7],a[32:35],v[228:231]\nds_read2_b32 a[94:95],v49 offset0:146 offset1:147\n" \
"C v39,v40,v41\nM v[232:235],a[0:3],a[40:43],v[232:235]\nbuffer_load_dwordx4 v14,s[28:31],s46 offen lds\nM v[232:235],a[4:7],a[36:39],v[232:235]\ns_add_u32 m0,s50,73728\nv_mul_f32 v42,s20,v42\nv_mul_f32 v43,s20,v43\nM v[232:235],a[8:11],a[32:35],v[232:235]\n" \
"v_mul_f32 v44,s20,v44\nv_mul_f32 v45,s20,v45\nM v[236:239],a[0:3],a[44:47],v[236:239]\nv_accvgpr_read_b32 v32,a146\nv_accvgpr_read_b32 v33,a147\nM v[236:239],a[4:7],a[40:43],v[236:239]\nv_mul_f32 v30,s20,v30\nv_mul_f32 v31,s20,v31\n" \
"M v[236:239],a[8:11],a[36:39],v[236:239]\nbuffer_store_dwordx4 v[192:195],v4,s[36:39],s96 offen offset:0 sc1\nM v[236:239],a[12:15],a[32:35],v[236:239]\nC v40,v42,v43\nC v41,v44,v45\nM v[240:243],a[0:3],a[48:51],v[240:243]\nv_accvgpr_write_b32 a132,v38\nv_accvgpr_write_b32 a133,v39\n" \
"M v[240:243],a[4:7],a[44:47],v[240:243]\nv_mul_f32 v32,s20,v32\nv_mul_f32 v33,s20,v33\nM v[240:243],a[8:11],a[40:43],v[240:243]\nbuffer_load_dwordx4 v15,s[28:31],s46 offen lds\nM v[240:243],a[12:15],a[36:39],v[240:243]\ns_add_u32 m0,s50,77824\nC v30,v30,v31\n" \
"v_accvgpr_write_b32 a134,v40\nM v[240:243],a[16:19],a[32:35],v[240:243]\nv_accvgpr_write_b32 a135,v41\nv_accvgpr_read_b32 v34,a148\nM v[244:247],a[0:3],a[52:55],v[244:247]\nv_accvgpr_read_b32 v35,a149\nv_accvgpr_read_b32 v36,a150\nM v[244:247],a[4:7],a[48:51],v[244:247]\n" \
"buffer_load_dwordx4 v16,s[28:31],s46 offen lds\nM v[244:247],a[8:11],a[44:47],v[244:247]\ns_add_u32 m0,s50,81920\nv_accvgpr_read_b32 v37,a151\nv_accvgpr_read_b32 v38,a152\nM v[244:247],a[12:15],a[40:43],v[244:247]\ns_mov_b32 s46,768\nC v31,v32,v33\n" \
"v_accvgpr_read_b32 v39,a153\nM v[244:247],a[16:19],a[36:39],v[244:247]\nv_mul_f32 v34,s20,v34\nv_mul_f32 v35,s20,v35\nM v[244:247],a[20:23],a[32:35],v[244:247]\nbuffer_load_dwordx4 v17,s[28:31],s46 offen lds\nM v[248:251],a[0:3],a[56:59],v[248:251]\ns_add_u32 m0,s50,86016\n" \
"v_mul_f32 v36,s20,v36\nv_mul_f32 v37,s20,v37\nM v[248:251],a[4:7],a[52:55],v[248:251]\nv_accvgpr_read_b32 v40,a154\nv_accvgpr_read_b32 v41,a155\nM v[248:251],a[8:11],a[48:51],v[248:251]\nv_mul_f32 v38,s20,v38\nv_mul_f32 v39,s20,v39\n" \
"M v[248:251],a[12:15],a[44:47],v[248:251]\nbuffer_load_dwordx4 v18,s[28:31],s46 offen lds\nM v[248:251],a[16:19],a[40:43],v[248:251]\ns_add_u32 m0,s50,90112\nC v32,v34,v35\nC v33,v36,v37\nM v[248:251],a[20:23],a[36:39],v[248:251]\nv_mul_f32 v40,s20,v40\n" \
"v_mul_f32 v41,s20,v41\nM v[248:251],a[24:27],a[32:35],v[248:251]\nC v38,v38,v39\nv_accvgpr_read_b32 v42,a156\nM v[252:255],a[0:3],a[60:63],v[252:255]\nds_read2_b32 a[0:1],v46 offset0:32 offset1:33\nds_read2_b32 a[2:3],v46 offset0:34 offset1:35\nM v[252:255],a[4:7],a[56:59],v[252:255]\n" \
"buffer_store_dwordx4 v[196:199],v4,s[36:39],s96 offen offset:1024 sc1\nM v[252:255],a[8:11],a[52:55],v[252:255]\nds_read2_b32 a[4:5],v46 offset0:160 offset1:161\nds_read2_b32 a[6:7],v46 offset0:162 offset1:163\nM v[252:255],a[12:15],a[48:51],v[252:255]\nds_read2_b32 a[8:9],v47 offset0:32 offset1:33\nv_accvgpr_write_b32 a136,v30\nM v[252:255],a[16:19],a[44:47],v[252:255]\n" \
"ds_read2_b32 a[10:11],v47 offset0:34 offset1:35\nds_read_b128 a[44:47],v50 offset:1920\nM v[252:255],a[20:23],a[40:43],v[252:255]\nbuffer_load_dwordx4 v19,s[28:31],s46 offen lds\nM v[252:255],a[24:27],a[36:39],v[252:255]\ns_add_u32 m0,s50,94208\nv_accvgpr_read_b32 v43,a157\nv_accvgpr_write_b32 a137,v31\n" \
"M v[252:255],a[28:31],a[32:35],v[252:255]\nds_read_b128 a[32:35],v50 offset:384\nv_accvgpr_write_b32 a138,v32\nM v[224:227],a[64:67],a[96:99],v[224:227]\ns_waitcnt lgkmcnt(4)\nds_read_b128 a[36:39],v50 offset:896\nv_accvgpr_write_b32 a139,v33\nM v[228:231],a[64:67],a[100:103],v[228:231]\n" \
"buffer_load_dwordx4 v20,s[28:31],s46 offen lds\nM v[228:231],a[68:71],a[96:99],v[228:231]\ns_add_u32 m0,s51,16384\nds_read_b128 a[40:43],v50 offset:1408\nv_accvgpr_read_b32 v44,a158\nM v[232:235],a[64:67],a[104:107],v[232:235]\ns_mov_b32 s46,896\nds_read2_b32 a[12:13],v47 offset0:160 offset1:161\n" \
"C v39,v40,v41\nM v[232:235],a[68:71],a[100:103],v[232:235]\nds_read2_b32 a[14:15],v47 offset0:162 offset1:163\nv_accvgpr_read_b32 v45,a159\nM v[232:235],a[72:75],a[96:99],v[232:235]\nbuffer_load_dwordx4 v21,s[32:35],s47 offen offset:0 lds\nM v[236:239],a[64:67],a[108:111],v[236:239]\ns_add_u32 m0,s51,17280\n" \
"ds_read_b128 a[48:51],v50 offset:2432\nv_mul_f32 v42,s20,v42\nM v[236:239],a[68:71],a[104:107],v[236:239]\nds_read2_b32 a[16:17],v48 offset0:32 offset1:33\nv_mul_f32 v43,s20,v43\nM v[236:239],a[72:75],a[100:103],v[236:239]\nds_read2_b32 a[18:19],v48 offset0:34 offset1:35\nv_mul_f32 v44,s20,v44\n" \
"M v[236:239],a[76:79],a[96:99],v[236:239]\nbuffer_load_dwordx4 v21,s[32:35],s47 offen offset:128 lds\nM v[240:243],a[64:67],a[112:115],v[240:243]\ns_add_u32 m0,s51,24576\nds_read_b128 a[52:55],v50 offset:2944\nv_mul_f32 v45,s20,v45\nM v[240:243],a[68:71],a[108:111],v[240:243]\ns_mov_b32 s47,3932160\n" \
"ds_read2_b32 a[20:21],v48 offset0:160 offset1:161\nv_accvgpr_read_b32 v30,a160\nM v[240:243],a[72:75],a[104:107],v[240:243]\nds_read2_b32 a[22:23],v48 offset0:162 offset1:163\nv_accvgpr_read_b32 v31,a161\nM v[240:243],a[76:79],a[100:103],v[240:243]\nds_read_b128 a[56:59],v50 offset:3456\nC v40,v42,v43\n" \
"M v[240:243],a[80:83],a[96:99],v[240:243]\nbuffer_store_dwordx4 v[200:203],v4,s[36:39],s96 offen offset:2048 sc1\nM v[244:247],a[64:67],a[116:119],v[244:247]\nds_read2_b32 a[24:25],v49 offset0:32 offset1:33\nds_read2_b32 a[26:27],v49 offset0:34 offset1:35\nM v[244:247],a[68:71],a[112:115],v[244:247]\nds_read_b128 a[60:63],v50 offset:3968\nC v41,v44,v45\n" \
"M v[244:247],a[72:75],a[108:111],v[244:247]\nds_read2_b32 a[28:29],v49 offset0:160 offset1:161\nv_accvgpr_read_b32 v32,a162\nM v[244:247],a[76:79],a[104:107],v[244:247]\nbuffer_load_dwordx4 v21,s[32:35],s47 offen offset:0 lds\nM v[244:247],a[80:83],a[100:103],v[244:247]\ns_add_u32 m0,s51,25472\nds_read2_b32 a[30:31],v49 offset0:162 offset1:163\n" \
"v_accvgpr_read_b32 v33,a163\nM v[244:247],a[84:87],a[96:99],v[244:247]\nv_mul_f32 v30,s20,v30\nv_accvgpr_write_b32 a140,v38\nM v[248:251],a[64:67],a[120:123],v[248:251]\nv_mul_f32 v31,s20,v31\nv_accvgpr_write_b32 a141,v39\nM v[248:251],a[68:71],a[116:119],v[248:251]\n" \
"buffer_load_dwordx4 v21,s[32:35],s47 offen offset:128 lds\nM v[248:251],a[72:75],a[112:115],v[248:251]\ns_add_u32 m0,s50,0\nv_accvgpr_write_b32 a142,v40\nv_accvgpr_write_b32 a143,v41\nM v[248:251],a[76:79],a[108:111],v[248:251]\ns_mov_b32 s47,4194304\nv_mul_f32 v32,s20,v32\n" \
"v_mul_f32 v33,s20,v33\nM v[248:251],a[80:83],a[104:107],v[248:251]\nC v30,v30,v31\nv_accvgpr_read_b32 v34,a164\nM v[248:251],a[84:87],a[100:103],v[248:251]\nbuffer_load_dwordx4 v13,s[28:31],s46 offen lds\nM v[248:251],a[88:91],a[96:99],v[248:251]\ns_add_u32 m0,s50,4096\n" \
"v_accvgpr_read_b32 v35,a165\nv_accvgpr_read_b32 v36,a166\nM v[252:255],a[64:67],a[124:127],v[252:255]\nds_read2_b32 a[64:65],v46 offset0:48 offset1:49\nds_read2_b32 a[66:67],v46 offset0:50 offset1:51\nM v[252:255],a[68:71],a[120:123],v[252:255]\nds_read2_b32 a[68:69],v46 offset0:176 offset1:177\nv_accvgpr_read_b32 v37,a167\n" \
"M v[252:255],a[72:75],a[116:119],v[252:255]\nds_read2_b32 a[70:71],v46 offset0:178 offset1:179\nds_read2_b32 a[72:73],v47 offset0:48 offset1:49\nM v[252:255],a[76:79],a[112:115],v[252:255]\nbuffer_store_dwordx4 v[204:207],v4,s[36:39],s96 offen offset:3072 sc1\nM v[252:255],a[80:83],a[108:111],v[252:255]\ns_add_u32 s96,s57,4096\nds_read2_b32 a[74:75],v47 offset0:50 offset1:51\n" \
"ds_read_b128 a[108:111],v50 offset:1856\nM v[252:255],a[84:87],a[104:107],v[252:255]\nds_read_b128 a[104:107],v50 offset:1344\nC v31,v32,v33\nM v[252:255],a[88:91],a[100:103],v[252:255]\ns_waitcnt lgkmcnt(6)\nds_read_b128 a[100:103],v50 offset:832\nv_mul_f32 v34,s20,v34\n" \
"M v[252:255],a[92:95],a[96:99],v[252:255]\nbuffer_load_dwordx4 v14,s[28:31],s46 offen lds\nM v[224:227],a[0:3],a[32:35],v[224:227]\ns_add_u32 m0,s50,8192\nv_mul_f32 v35,s20,v35\nv_mul_f32 v36,s20,v36\nM v[228:231],a[0:3],a[36:39],v[228:231]\nds_read_b128 a[96:99],v50 offset:320\n" \
"v_mul_f32 v37,s20,v37\nM v[228:231],a[4:7],a[32:35],v[228:231]\nds_read2_b32 a[76:77],v47 offset0:176 offset1:177\nds_read2_b32 a[78:79],v47 offset0:178 offset1:179\nM v[232:235],a[0:3],a[40:43],v[232:235]\nds_read_b128 a[112:115],v50 offset:2368\nv_accvgpr_read_b32 v38,a168\nM v[232:235],a[4:7],a[36:39],v[232:235]\n" \
"buffer_store_dwordx4 v[208:211],v4,s[36:39],s96 offen offset:0 sc1\nM v[232:235],a[8:11],a[32:35],v[232:235]\nds_read2_b32 a[80:81],v48 offset0:48 offset1:49\nds_read2_b32 a[82:83],v48 offset0:50 offset1:51\nM v[236:239],a[0:3],a[44:47],v[236:239]\nds_read_b128 a[116:119],v50 offset:2880\nv_accvgpr_read_b32 v39,a169\nM v[236:239],a[4:7],a[40:43],v[236:239]\n" \
"ds_read2_b32 a[84:85],v48 offset0:176 offset1:177\nC v32,v34,v35\nM v[236:239],a[8:11],a[36:39],v[236:239]\nbuffer_load_dwordx4 v15,s[28:31],s46 offen lds\nM v[236:239],a[12:15],a[32:35],v[236:239]\ns_add_u32 m0,s50,12288\nds_read2_b32 a[86:87],v48 offset0:178 offset1:179\nC v33,v36,v37\n" \
"M v[240:243],a[0:3],a[48:51],v[240:243]\nds_read_b128 a[120:123],v50 offset:3392\nv_accvgpr_read_b32 v40,a170\nM v[240:243],a[4:7],a[44:47],v[240:243]\nds_read2_b32 a[88:89],v49 offset0:48 offset1:49\nv_accvgpr_read_b32 v41,a171\nM v[240:243],a[8:11],a[40:43],v[240:243]\nds_read2_b32 a[90:91],v49 offset0:50 offset1:51\n" \
"ds_read_b128 a[124:127],v50 offset:3904\nM v[240:243],a[12:15],a[36:39],v[240:243]\nbuffer_store_dwordx4 v[212:215],v4,s[36:39],s96 offen offset:1024 sc1\nM v[240:243],a[16:19],a[32:35],v[240:243]\nds_read2_b32 a[92:93],v49 offset0:176 offset1:177\nds_read2_b32 a[94:95],v49 offset0:178 offset1:179\nM v[244:247],a[0:3],a[52:55],v[244:247]\nv_mul_f32 v38,s20,v38\n" \
"v_mul_f32 v39,s20,v39\nM v[244:247],a[4:7],a[48:51],v[244:247]\nv_accvgpr_write_b32 a144,v30\nv_accvgpr_write_b32 a145,v31\nM v[244:247],a[8:11],a[44:47],v[244:247]\nbuffer_load_dwordx4 v16,s[28:31],s46 offen lds\nM v[244:247],a[12:15],a[40:43],v[244:247]\ns_add_u32 m0,s50,16384\n" \
"v_accvgpr_write_b32 a146,v32\nv_accvgpr_write_b32 a147,v33\nM v[244:247],a[16:19],a[36:39],v[244:247]\ns_mov_b32 s46,896\nv_mul_f32 v40,s20,v40\nv_mul_f32 v41,s20,v41\nM v[244:247],a[20:23],a[32:35],v[244:247]\nC v38,v38,v39\n" \
"v_accvgpr_read_b32 v42,a172\nM v[248:251],a[0:3],a[56:59],v[248:251]\nv_accvgpr_read_b32 v43,a173\nv_accvgpr_read_b32 v44,a174\nM v[248:251],a[4:7],a[52:55],v[248:251]\nbuffer_store_dwordx4 v[216:219],v4,s[36:39],s96 offen offset:2048 sc1\nM v[248:251],a[8:11],a[48:51],v[248:251]\nC v39,v40,v41\n" \
"v_accvgpr_read_b32 v45,a175\nM v[248:251],a[12:15],a[44:47],v[248:251]\nv_mul_f32 v42,s20,v42\nv_mul_f32 v43,s20,v43\nM v[248:251],a[16:19],a[40:43],v[248:251]\nv_mul_f32 v44,s20,v44\nv_mul_f32 v45,s20,v45\nM v[248:251],a[20:23],a[36:39],v[248:251]\n" \
"buffer_load_dwordx4 v17,s[28:31],s46 offen lds\nM v[248:251],a[24:27],a[32:35],v[248:251]\ns_add_u32 m0,s50,20480\nC v40,v42,v43\nC v41,v44,v45\nM v[252:255],a[0:3],a[60:63],v[252:255]\nds_read2_b32 a[0:1],v46 offset0:64 offset1:65\nds_read2_b32 a[2:3],v46 offset0:66 offset1:67\n" \
"M v[252:255],a[4:7],a[56:59],v[252:255]\nds_read2_b32 a[4:5],v46 offset0:192 offset1:193\nv_accvgpr_write_b32 a148,v38\nM v[252:255],a[8:11],a[52:55],v[252:255]\nds_read2_b32 a[6:7],v46 offset0:194 offset1:195\nds_read2_b32 a[8:9],v47 offset0:64 offset1:65\nM v[252:255],a[12:15],a[48:51],v[252:255]\nbuffer_store_dwordx4 v[220:223],v4,s[36:39],s96 offen offset:3072 sc1\n" \
"M v[252:255],a[16:19],a[44:47],v[252:255]\ns_add_u32 s96,s57,16384\nds_read2_b32 a[10:11],v47 offset0:66 offset1:67\nds_read_b128 a[44:47],v50 offset:1792\nM v[252:255],a[20:23],a[40:43],v[252:255]\nds_read_b128 a[40:43],v50 offset:1280\nv_accvgpr_write_b32 a149,v39\nM v[252:255],a[24:27],a[36:39],v[252:255]\n" \
"s_waitcnt lgkmcnt(6)\nds_read_b128 a[36:39],v50 offset:768\nv_accvgpr_write_b32 a150,v40\nM v[252:255],a[28:31],a[32:35],v[252:255]\nbuffer_load_dwordx4 v18,s[28:31],s46 offen lds\nM v[224:227],a[64:67],a[96:99],v[224:227]\ns_add_u32 m0,s50,24576\nv_accvgpr_write_b32 a151,v41\n" \
"v_accvgpr_read_b32 v30,a176\nM v[228:231],a[64:67],a[100:103],v[228:231]\nds_read_b128 a[32:35],v50 offset:256\nv_accvgpr_read_b32 v31,a177\nM v[228:231],a[68:71],a[96:99],v[228:231]\nds_read2_b32 a[12:13],v47 offset0:192 offset1:193\nds_read2_b32 a[14:15],v47 offset0:194 offset1:195\nM v[232:235],a[64:67],a[104:107],v[232:235]\n" \
"ds_read_b128 a[48:51],v50 offset:2304\nv_accvgpr_read_b32 v32,a178\nM v[232:235],a[68:71],a[100:103],v[232:235]\nbuffer_store_dwordx4 a[128:131],v4,s[36:39],s96 offen offset:0 sc1\nM v[232:235],a[72:75],a[96:99],v[232:235]\nds_read2_b32 a[16:17],v48 offset0:64 offset1:65\nds_read2_b32 a[18:19],v48 offset0:66 offset1:67\nM v[236:239],a[64:67],a[108:111],v[236:239]\n" \
"ds_read_b128 a[52:55],v50 offset:2816\nv_accvgpr_read_b32 v33,a179\nM v[236:239],a[68:71],a[104:107],v[236:239]\nds_read2_b32 a[20:21],v48 offset0:192 offset1:193\nv_mul_f32 v30,s20,v30\nM v[236:239],a[72:75],a[100:103],v[236:239]\nds_read2_b32 a[22:23],v48 offset0:194 offset1:195\nds_read_b128 a[56:59],v50 offset:3328\n" \
"M v[236:239],a[76:79],a[96:99],v[236:239]\nds_read2_b32 a[24:25],v49 offset0:64 offset1:65\nv_mul_f32 v31,s20,v31\nM v[240:243],a[64:67],a[112:115],v[240:243]\nds_read2_b32 a[26:27],v49 offset0:66 offset1:67\nv_mul_f32 v32,s20,v32\nM v[240:243],a[68:71],a[108:111],v[240:243]\nds_read_b128 a[60:63],v50 offset:3840\n" \
"v_mul_f32 v33,s20,v33\nM v[240:243],a[72:75],a[104:107],v[240:243]\nds_read2_b32 a[28:29],v49 offset0:192 offset1:193\nC v30,v30,v31\nM v[240:243],a[76:79],a[100:103],v[240:243]\nds_read2_b32 a[30:31],v49 offset0:194 offset1:195\nv_accvgpr_read_b32 v34,a180\nM v[240:243],a[80:83],a[96:99],v[240:243]\n" \
"v_accvgpr_read_b32 v35,a181\nv_accvgpr_read_b32 v36,a182\nM v[244:247],a[64:67],a[116:119],v[244:247]\nC v31,v32,v33\nv_accvgpr_read_b32 v37,a183\nM v[244:247],a[68:71],a[112:115],v[244:247]\nv_mul_f32 v34,s20,v34\nv_mul_f32 v35,s20,v35\n" \
"M v[244:247],a[72:75],a[108:111],v[244:247]\nbuffer_store_dwordx4 a[132:135],v4,s[36:39],s96 offen offset:1024 sc1\nM v[244:247],a[76:79],a[104:107],v[244:247]\nv_mul_f32 v36,s20,v36\nv_mul_f32 v37,s20,v37\nM v[244:247],a[80:83],a[100:103],v[244:247]\nC v32,v34,v35\nC v33,v36,v37\n" \
"M v[244:247],a[84:87],a[96:99],v[244:247]\nv_accvgpr_write_b32 a152,v30\nv_accvgpr_write_b32 a153,v31\nM v[248:251],a[64:67],a[120:123],v[248:251]\nv_accvgpr_write_b32 a154,v32\nv_accvgpr_write_b32 a155,v33\nM v[248:251],a[68:71],a[116:119],v[248:251]\nv_accvgpr_read_b32 v38,a184\n" \
"v_accvgpr_read_b32 v39,a185\nM v[248:251],a[72:75],a[112:115],v[248:251]\nv_accvgpr_read_b32 v40,a186\nv_accvgpr_read_b32 v41,a187\nM v[248:251],a[76:79],a[108:111],v[248:251]\nv_mul_f32 v38,s20,v38\nv_mul_f32 v39,s20,v39\nM v[248:251],a[80:83],a[104:107],v[248:251]\n" \
"v_mul_f32 v40,s20,v40\nv_mul_f32 v41,s20,v41\nM v[248:251],a[84:87],a[100:103],v[248:251]\nC v38,v38,v39\nv_accvgpr_read_b32 v42,a188\nM v[248:251],a[88:91],a[96:99],v[248:251]\nv_accvgpr_read_b32 v43,a189\nv_accvgpr_read_b32 v44,a190\n" \
"M v[252:255],a[64:67],a[124:127],v[252:255]\nds_read2_b32 a[64:65],v46 offset0:80 offset1:81\nds_read2_b32 a[66:67],v46 offset0:82 offset1:83\nM v[252:255],a[68:71],a[120:123],v[252:255]\nds_read2_b32 a[68:69],v46 offset0:208 offset1:209\nC v39,v40,v41\nM v[252:255],a[72:75],a[116:119],v[252:255]\nbuffer_store_dwordx4 a[136:139],v4,s[36:39],s96 offen offset:2048 sc1\n" \
"M v[252:255],a[76:79],a[112:115],v[252:255]\nds_read2_b32 a[70:71],v46 offset0:210 offset1:211\nds_read2_b32 a[72:73],v47 offset0:80 offset1:81\nM v[252:255],a[80:83],a[108:111],v[252:255]\nds_read2_b32 a[74:75],v47 offset0:82 offset1:83\nv_accvgpr_read_b32 v45,a191\nM v[252:255],a[84:87],a[104:107],v[252:255]\nds_read_b128 a[104:107],v50 offset:1216\n" \
"v_mul_f32 v42,s20,v42\nM v[252:255],a[88:91],a[100:103],v[252:255]\nds_read_b128 a[100:103],v50 offset:704\nv_mul_f32 v43,s20,v43\nM v[252:255],a[92:95],a[96:99],v[252:255]\ns_waitcnt lgkmcnt(5)\nds_read_b128 a[96:99],v50 offset:192\nv_mul_f32 v44,s20,v44\n" \
"M v[224:227],a[0:3],a[32:35],v[224:227]\nds_read_b128 a[108:111],v50 offset:1728\nv_mul_f32 v45,s20,v45\nM v[228:231],a[0:3],a[36:39],v[228:231]\nds_read2_b32 a[76:77],v47 offset0:208 offset1:209\nds_read2_b32 a[78:79],v47 offset0:210 offset1:211\nM v[228:231],a[4:7],a[32:35],v[228:231]\nds_read_b128 a[112:115],v50 offset:2240\n" \
"C v40,v42,v43\nM v[232:235],a[0:3],a[40:43],v[232:235]\nds_read2_b32 a[80:81],v48 offset0:80 offset1:81\nC v41,v44,v45\nM v[232:235],a[4:7],a[36:39],v[232:235]\nds_read2_b32 a[82:83],v48 offset0:82 offset1:83\nv_accvgpr_write_b32 a156,v38\nM v[232:235],a[8:11],a[32:35],v[232:235]\n" \
"ds_read_b128 a[116:119],v50 offset:2752\nv_accvgpr_write_b32 a157,v39\nM v[236:239],a[0:3],a[44:47],v[236:239]\nds_read2_b32 a[84:85],v48 offset0:208 offset1:209\nds_read2_b32 a[86:87],v48 offset0:210 offset1:211\nM v[236:239],a[4:7],a[40:43],v[236:239]\nbuffer_store_dwordx4 a[140:143],v4,s[36:39],s96 offen offset:3072 sc1\nM v[236:239],a[8:11],a[36:39],v[236:239]\n" \
"s_add_u32 s96,s57,20480\nds_read_b128 a[120:123],v50 offset:3264\nds_read2_b32 a[88:89],v49 offset0:80 offset1:81\nM v[236:239],a[12:15],a[32:35],v[236:239]\nds_read2_b32 a[90:91],v49 offset0:82 offset1:83\nv_accvgpr_write_b32 a158,v40\nM v[240:243],a[0:3],a[48:51],v[240:243]\nds_read_b128 a[124:127],v50 offset:3776\n" \
"v_accvgpr_write_b32 a159,v41\nM v[240:243],a[4:7],a[44:47],v[240:243]\nbuffer_load_dwordx4 v19,s[28:31],s46 offen lds\nM v[240:243],a[8:11],a[40:43],v[240:243]\ns_add_u32 m0,s50,28672\nds_read2_b32 a[92:93],v49 offset0:208 offset1:209\nM v[240:243],a[12:15],a[36:39],v[240:243]\nds_read2_b32 a[94:95],v49 offset0:210 offset1:211\n" \
"M v[240:243],a[16:19],a[32:35],v[240:243]\nM v[244:247],a[0:3],a[52:55],v[244:247]\nM v[244:247],a[4:7],a[48:51],v[244:247]\nbuffer_store_dwordx4 a[144:147],v4,s[36:39],s96 offen offset:0 sc1\nM v[244:247],a[8:11],a[44:47],v[244:247]\nM v[244:247],a[12:15],a[40:43],v[244:247]\nM v[244:247],a[16:19],a[36:39],v[244:247]\nM v[244:247],a[20:23],a[32:35],v[244:247]\n" \
"M v[248:251],a[0:3],a[56:59],v[248:251]\nM v[248:251],a[4:7],a[52:55],v[248:251]\nM v[248:251],a[8:11],a[48:51],v[248:251]\nM v[248:251],a[12:15],a[44:47],v[248:251]\nM v[248:251],a[16:19],a[40:43],v[248:251]\nM v[248:251],a[20:23],a[36:39],v[248:251]\nM v[248:251],a[24:27],a[32:35],v[248:251]\ns_nop 0\n" \
"M v[252:255],a[0:3],a[60:63],v[252:255]\nds_read2_b32 a[0:1],v46 offset0:96 offset1:97\nds_read2_b32 a[2:3],v46 offset0:98 offset1:99\nM v[252:255],a[4:7],a[56:59],v[252:255]\nbuffer_store_dwordx4 a[148:151],v4,s[36:39],s96 offen offset:1024 sc1\nM v[252:255],a[8:11],a[52:55],v[252:255]\nds_read2_b32 a[4:5],v46 offset0:224 offset1:225\nds_read2_b32 a[6:7],v46 offset0:226 offset1:227\n" \
"M v[252:255],a[12:15],a[48:51],v[252:255]\nds_read2_b32 a[8:9],v47 offset0:96 offset1:97\nM v[252:255],a[16:19],a[44:47],v[252:255]\nds_read_b128 a[44:47],v50 offset:1664\nM v[252:255],a[20:23],a[40:43],v[252:255]\nds_read_b128 a[40:43],v50 offset:1152\nM v[252:255],a[24:27],a[36:39],v[252:255]\nds_read_b128 a[36:39],v50 offset:640\n" \
"M v[252:255],a[28:31],a[32:35],v[252:255]\ns_waitcnt lgkmcnt(6)\nds_read_b128 a[32:35],v50 offset:128\nM v[224:227],a[64:67],a[96:99],v[224:227]\nds_read2_b32 a[10:11],v47 offset0:98 offset1:99\nds_read2_b32 a[12:13],v47 offset0:224 offset1:225\nM v[228:231],a[64:67],a[100:103],v[228:231]\nds_read2_b32 a[14:15],v47 offset0:226 offset1:227\n" \
"M v[228:231],a[68:71],a[96:99],v[228:231]\nds_read_b128 a[48:51],v50 offset:2176\nM v[232:235],a[64:67],a[104:107],v[232:235]\nds_read2_b32 a[16:17],v48 offset0:96 offset1:97\nM v[232:235],a[68:71],a[100:103],v[232:235]\nds_read2_b32 a[18:19],v48 offset0:98 offset1:99\nds_read_b128 a[52:55],v50 offset:2688\nM v[232:235],a[72:75],a[96:99],v[232:235]\n" \
"ds_read2_b32 a[20:21],v48 offset0:224 offset1:225\nM v[236:239],a[64:67],a[108:111],v[236:239]\nbuffer_store_dwordx4 a[152:155],v4,s[36:39],s96 offen offset:2048 sc1\nM v[236:239],a[68:71],a[104:107],v[236:239]\nds_read2_b32 a[22:23],v48 offset0:226 offset1:227\nds_read_b128 a[56:59],v50 offset:3200\nM v[236:239],a[72:75],a[100:103],v[236:239]\nds_read2_b32 a[24:25],v49 offset0:96 offset1:97\n" \
"M v[236:239],a[76:79],a[96:99],v[236:239]\nds_read2_b32 a[26:27],v49 offset0:98 offset1:99\nM v[240:243],a[64:67],a[112:115],v[240:243]\nds_read_b128 a[60:63],v50 offset:3712\nM v[240:243],a[68:71],a[108:111],v[240:243]\nds_read2_b32 a[28:29],v49 offset0:224 offset1:225\nds_read2_b32 a[30:31],v49 offset0:226 offset1:227\nM v[240:243],a[72:75],a[104:107],v[240:243]\n" \
"M v[240:243],a[76:79],a[100:103],v[240:243]\nM v[240:243],a[80:83],a[96:99],v[240:243]\nM v[244:247],a[64:67],a[116:119],v[244:247]\nM v[244:247],a[68:71],a[112:115],v[244:247]\nM v[244:247],a[72:75],a[108:111],v[244:247]\nM v[244:247],a[76:79],a[104:107],v[244:247]\nM v[244:247],a[80:83],a[100:103],v[244:247]\nbuffer_store_dwordx4 a[156:159],v4,s[36:39],s96 offen offset:3072 sc1\n" \
"M v[244:247],a[84:87],a[96:99],v[244:247]\ns_add_u32 s96,s57,49152\nM v[248:251],a[64:67],a[120:123],v[248:251]\nM v[248:251],a[68:71],a[116:119],v[248:251]\nM v[248:251],a[72:75],a[112:115],v[248:251]\nM v[248:251],a[76:79],a[108:111],v[248:251]\nM v[248:251],a[80:83],a[104:107],v[248:251]\nM v[248:251],a[84:87],a[100:103],v[248:251]\n" \
"M v[248:251],a[88:91],a[96:99],v[248:251]\nM v[252:255],a[64:67],a[124:127],v[252:255]\nds_read2_b32 a[64:65],v46 offset0:112 offset1:113\nds_read2_b32 a[66:67],v46 offset0:114 offset1:115\nM v[252:255],a[68:71],a[120:123],v[252:255]\nds_read2_b32 a[68:69],v46 offset0:240 offset1:241\nM v[252:255],a[72:75],a[116:119],v[252:255]\nbuffer_load_dwordx4 v20,s[28:31],s46 offen lds\n" \
"M v[252:255],a[76:79],a[112:115],v[252:255]\ns_add_u32 m0,s51,32768\nds_read2_b32 a[70:71],v46 offset0:242 offset1:243\nM v[252:255],a[80:83],a[108:111],v[252:255]\ns_mov_b32 s46,1024\nds_read2_b32 a[72:73],v47 offset0:112 offset1:113\nM v[252:255],a[84:87],a[104:107],v[252:255]\nds_read_b128 a[104:107],v50 offset:1088\n" \
"M v[252:255],a[88:91],a[100:103],v[252:255]\ns_waitcnt lgkmcnt(4)\nds_read_b128 a[100:103],v50 offset:576\nM v[252:255],a[92:95],a[96:99],v[252:255]\nbuffer_store_dwordx4 v[88:91],v4,s[36:39],s96 offen offset:0 sc1\nM v[224:227],a[0:3],a[32:35],v[224:227]\nds_read_b128 a[96:99],v50 offset:64\nds_read2_b32 a[74:75],v47 offset0:114 offset1:115\n" \
"M v[228:231],a[0:3],a[36:39],v[228:231]\nds_read_b128 a[108:111],v50 offset:1600\nM v[228:231],a[4:7],a[32:35],v[228:231]\nds_read2_b32 a[76:77],v47 offset0:240 offset1:241\nM v[232:235],a[0:3],a[40:43],v[232:235]\nds_read2_b32 a[78:79],v47 offset0:242 offset1:243\nM v[232:235],a[4:7],a[36:39],v[232:235]\nds_read_b128 a[112:115],v50 offset:2112\n" \
"M v[232:235],a[8:11],a[32:35],v[232:235]\nds_read2_b32 a[80:81],v48 offset0:112 offset1:113\nds_read2_b32 a[82:83],v48 offset0:114 offset1:115\nM v[236:239],a[0:3],a[44:47],v[236:239]\nds_read_b128 a[116:119],v50 offset:2624\nM v[236:239],a[4:7],a[40:43],v[236:239]\nds_read2_b32 a[84:85],v48 offset0:240 offset1:241\nM v[236:239],a[8:11],a[36:39],v[236:239]\n" \
"ds_read2_b32 a[86:87],v48 offset0:242 offset1:243\nM v[236:239],a[12:15],a[32:35],v[236:239]\nds_read_b128 a[120:123],v50 offset:3136\nM v[240:243],a[0:3],a[48:51],v[240:243]\nds_read2_b32 a[88:89],v49 offset0:112 offset1:113\nds_read2_b32 a[90:91],v49 offset0:114 offset1:115\nM v[240:243],a[4:7],a[44:47],v[240:243]\nds_read_b128 a[124:127],v50 offset:3648\n" \
"M v[240:243],a[8:11],a[40:43],v[240:243]\nds_read2_b32 a[92:93],v49 offset0:240 offset1:241\nM v[240:243],a[12:15],a[36:39],v[240:243]\nds_read2_b32 a[94:95],v49 offset0:242 offset1:243\nM v[240:243],a[16:19],a[32:35],v[240:243]\nM v[244:247],a[0:3],a[52:55],v[244:247]\nM v[244:247],a[4:7],a[48:51],v[244:247]\nM v[244:247],a[8:11],a[44:47],v[244:247]\n" \
"M v[244:247],a[12:15],a[40:43],v[244:247]\nM v[244:247],a[16:19],a[36:39],v[244:247]\nM v[244:247],a[20:23],a[32:35],v[244:247]\nM v[248:251],a[0:3],a[56:59],v[248:251]\nM v[248:251],a[4:7],a[52:55],v[248:251]\nM v[248:251],a[8:11],a[48:51],v[248:251]\nM v[248:251],a[12:15],a[44:47],v[248:251]\nM v[248:251],a[16:19],a[40:43],v[248:251]\n" \
"M v[248:251],a[20:23],a[36:39],v[248:251]\nM v[248:251],a[24:27],a[32:35],v[248:251]\nM v[252:255],a[0:3],a[60:63],v[252:255]\nM v[252:255],a[4:7],a[56:59],v[252:255]\nM v[252:255],a[8:11],a[52:55],v[252:255]\nM v[252:255],a[12:15],a[48:51],v[252:255]\nM v[252:255],a[16:19],a[44:47],v[252:255]\nM v[252:255],a[20:23],a[40:43],v[252:255]\n" \
"M v[252:255],a[24:27],a[36:39],v[252:255]\nM v[252:255],a[28:31],a[32:35],v[252:255]\ns_waitcnt lgkmcnt(0)\nM v[224:227],a[64:67],a[96:99],v[224:227]\ns_waitcnt vmcnt(27) lgkmcnt(0)\nM v[228:231],a[64:67],a[100:103],v[228:231]\ns_barrier\nds_read_b64_tr_b16 a[0:1],v9 offset:0\n" \
"M v[228:231],a[68:71],a[96:99],v[228:231]\nbuffer_load_dwordx4 v21,s[32:35],s47 offen offset:0 lds\nM v[232:235],a[64:67],a[104:107],v[232:235]\ns_add_u32 m0,s51,33664\nC v224,v224,v225\nC v225,v226,v227\nM v[232:235],a[68:71],a[100:103],v[232:235]\nC v226,v228,v229\n" \
"C v227,v230,v231\nM v[232:235],a[72:75],a[96:99],v[232:235]\nv_permlane16_swap_b32 v224,v226\nM v[236:239],a[64:67],a[108:111],v[236:239]\nbuffer_load_dwordx4 v21,s[32:35],s47 offen offset:128 lds\nM v[236:239],a[68:71],a[104:107],v[236:239]\ns_mov_b32 s47,4456448\nds_read_b64_tr_b16 a[2:3],v10 offset:0\n" \
"C v232,v232,v233\nM v[236:239],a[72:75],a[100:103],v[236:239]\ns_add_u32 m0,s51,40960\nv_permlane16_swap_b32 v225,v227\nM v[236:239],a[76:79],a[96:99],v[236:239]\nds_read_b128 a[32:35],v7 offset:0\nds_read_b128 a[36:39],v7 offset:4096\nM v[240:243],a[64:67],a[112:115],v[240:243]\n" \
"buffer_load_dwordx4 v21,s[32:35],s47 offen offset:0 lds\nM v[240:243],a[68:71],a[108:111],v[240:243]\ns_add_u32 m0,s51,41856\nC v233,v234,v235\nC v234,v236,v237\nM v[240:243],a[72:75],a[104:107],v[240:243]\nds_read_b128 a[40:43],v7 offset:8192\nC v235,v238,v239\n" \
"M v[240:243],a[76:79],a[100:103],v[240:243]\nds_read_b64_tr_b16 a[4:5],v11 offset:0\nM v[240:243],a[80:83],a[96:99],v[240:243]\nbuffer_load_dwordx4 v21,s[32:35],s47 offen offset:128 lds\nM v[244:247],a[64:67],a[116:119],v[244:247]\ns_add_u32 m0,s50,32768\nv_permlane16_swap_b32 v232,v234\nM v[244:247],a[68:71],a[112:115],v[244:247]\n" \
"s_mov_b32 s47,4718592\nv_permlane16_swap_b32 v233,v235\nM v[244:247],a[72:75],a[108:111],v[244:247]\nds_read_b128 a[44:47],v7 offset:12288\nds_read_b64_tr_b16 a[6:7],v12 offset:0\nM v[244:247],a[76:79],a[104:107],v[244:247]\nbuffer_load_dwordx4 v13,s[28:31],s46 offen lds\nM v[244:247],a[80:83],a[100:103],v[244:247]\n" \
"s_add_u32 m0,s50,36864\nC v240,v240,v241\nC v241,v242,v243\nM v[244:247],a[84:87],a[96:99],v[244:247]\nds_read_b64_tr_b16 a[8:9],v9 offset:512\nds_read_b64_tr_b16 a[10:11],v10 offset:512\nM v[248:251],a[64:67],a[120:123],v[248:251]\nds_read_b64_tr_b16 a[12:13],v11 offset:512\n" \
"M v[248:251],a[68:71],a[116:119],v[248:251]\nbuffer_load_dwordx4 v14,s[28:31],s46 offen lds\nM v[248:251],a[72:75],a[112:115],v[248:251]\ns_add_u32 m0,s50,40960\nds_read_b64_tr_b16 a[14:15],v12 offset:512\nC v242,v244,v245\nM v[248:251],a[76:79],a[108:111],v[248:251]\nds_read_b64_tr_b16 a[16:17],v9 offset:1024\n" \
"C v243,v246,v247\nM v[248:251],a[80:83],a[104:107],v[248:251]\nds_read_b64_tr_b16 a[18:19],v10 offset:1024\nM v[248:251],a[84:87],a[100:103],v[248:251]\nbuffer_load_dwordx4 v15,s[28:31],s46 offen lds\nM v[248:251],a[88:91],a[96:99],v[248:251]\ns_add_u32 m0,s50,45056\nds_read_b64_tr_b16 a[20:21],v11 offset:1024\n" \
"M v[252:255],a[64:67],a[124:127],v[252:255]\nds_read_b64_tr_b16 a[22:23],v12 offset:1024\nM v[252:255],a[68:71],a[120:123],v[252:255]\nds_read_b64_tr_b16 a[24:25],v9 offset:1536\nds_read_b64_tr_b16 a[26:27],v10 offset:1536\nM v[252:255],a[72:75],a[116:119],v[252:255]\nbuffer_load_dwordx4 v16,s[28:31],s46 offen lds\nM v[252:255],a[76:79],a[112:115],v[252:255]\n" \
"s_mov_b32 s46,1024\nv_permlane16_swap_b32 v240,v242\nM v[252:255],a[80:83],a[108:111],v[252:255]\ns_add_u32 m0,s50,49152\nds_read_b64_tr_b16 a[28:29],v11 offset:1536\nds_read_b64_tr_b16 a[30:31],v12 offset:1536\nM v[252:255],a[84:87],a[104:107],v[252:255]\ns_waitcnt lgkmcnt(5)\n" \
"ds_read_b64_tr_b16 a[48:49],v9 offset:8192\nC v248,v248,v249\nM v[252:255],a[88:91],a[100:103],v[252:255]\nbuffer_load_dwordx4 v17,s[28:31],s46 offen lds\nM v[252:255],a[92:95],a[96:99],v[252:255]\ns_add_u32 m0,s50,53248\nds_read_b64_tr_b16 a[50:51],v10 offset:8192\nC v249,v250,v251\n" \
"M v[128:131],a[0:3],a[32:35],v[128:131]\ns_load_dwordx2 s[84:85],%0,0x50\nds_read_b128 a[80:83],v8 offset:0\nM v[160:163],a[0:3],a[36:39],v[160:163]\ns_load_dwordx2 s[88:89],%0,0x60\nds_read_b128 a[84:87],v8 offset:4096\nC v250,v252,v253\nM a[224:227],a[0:3],a[40:43],a[224:227]\n" \
"buffer_load_dwordx4 v18,s[28:31],s46 offen lds\nM a[192:195],a[0:3],a[44:47],a[192:195]\ns_add_u32 m0,s50,57344\nv_permlane16_swap_b32 v241,v243\nM a[196:199],a[4:7],a[44:47],a[196:199]\ns_load_dwordx2 s[12:13],%0,0x0\nds_read_b64_tr_b16 a[52:53],v11 offset:8192\nds_read_b64_tr_b16 a[54:55],v12 offset:8192\n" \
"M a[228:231],a[4:7],a[40:43],a[228:231]\ns_memrealtime s[10:11]\nds_read_b64_tr_b16 a[56:57],v9 offset:8704\nC v251,v254,v255\nM v[164:167],a[4:7],a[36:39],v[164:167]\nbuffer_load_dwordx4 v19,s[28:31],s46 offen lds\nM v[132:135],a[4:7],a[32:35],v[132:135]\ns_add_u32 m0,s50,61440\n" \
"ds_read_b64_tr_b16 a[58:59],v10 offset:8704\nM v[136:139],a[8:11],a[32:35],v[136:139]\nds_read_b64_tr_b16 a[60:61],v11 offset:8704\nM v[168:171],a[8:11],a[36:39],v[168:171]\nds_read_b64_tr_b16 a[62:63],v12 offset:8704\nM a[232:235],a[8:11],a[40:43],a[232:235]\nbuffer_load_dwordx4 v20,s[28:31],s46 offen lds\nM a[200:203],a[8:11],a[44:47],a[200:203]\n" \
"s_add_u32 m0,s51,49152\nds_read_b64_tr_b16 a[64:65],v9 offset:9216\nM a[204:207],a[12:15],a[44:47],a[204:207]\ns_mov_b32 s46,1152\nds_read_b64_tr_b16 a[66:67],v10 offset:9216\nM a[236:239],a[12:15],a[40:43],a[236:239]\nds_read_b64_tr_b16 a[68:69],v11 offset:9216\nds_read_b64_tr_b16 a[70:71],v12 offset:9216\n" \
"M v[172:175],a[12:15],a[36:39],v[172:175]\nbuffer_load_dwordx4 v21,s[32:35],s47 offen offset:0 lds\nM v[140:143],a[12:15],a[32:35],v[140:143]\ns_add_u32 m0,s51,50048\nv_permlane16_swap_b32 v248,v250\nM v[144:147],a[16:19],a[32:35],v[144:147]\nds_read_b64_tr_b16 a[72:73],v9 offset:9728\nds_read_b64_tr_b16 a[74:75],v10 offset:9728\n" \
"M v[176:179],a[16:19],a[36:39],v[176:179]\nds_read_b64_tr_b16 a[76:77],v11 offset:9728\nv_mov_b32 v58,s10\nM a[240:243],a[16:19],a[40:43],a[240:243]\ns_waitcnt lgkmcnt(5)\nds_read_b64_tr_b16 a[78:79],v12 offset:9728\nds_read_b128 a[88:91],v8 offset:8192\nds_read_b128 a[92:95],v8 offset:12288\n" \
"M a[208:211],a[16:19],a[44:47],a[208:211]\nbuffer_load_dwordx4 v21,s[32:35],s47 offen offset:128 lds\nM a[212:215],a[20:23],a[44:47],a[212:215]\ns_add_u32 m0,s51,57344\nv_permlane16_swap_b32 v249,v251\nM a[244:247],a[20:23],a[40:43],a[244:247]\ns_mov_b32 s47,4980736\nv_mov_b32 v59,s11\n" \
"M v[180:183],a[20:23],a[36:39],v[180:183]\nM v[148:151],a[20:23],a[32:35],v[148:151]\nbuffer_load_dwordx4 v21,s[32:35],s47 offen offset:0 lds\nM v[152:155],a[24:27],a[32:35],v[152:155]\ns_waitcnt vmcnt(16) lgkmcnt(0)\nM v[184:187],a[24:27],a[36:39],v[184:187]\ns_add_u32 m0,s51,58240\ns_barrier\n" \
"ds_read_b64_tr_b16 a[0:1],v9 offset:16384\nM a[248:251],a[24:27],a[40:43],a[248:251]\ns_lshl_b32 s62,s6,20\nds_read_b64_tr_b16 a[2:3],v10 offset:16384\nds_read_b64_tr_b16 a[4:5],v11 offset:16384\nM a[216:219],a[24:27],a[44:47],a[216:219]\nbuffer_store_dwordx4 v[224:227],v60,s[36:39],s21 offen offset:0 sc1\nM a[220:223],a[28:31],a[44:47],a[220:223]\n" \
"s_add_u32 s21,s21,1048576\nds_read_b128 a[44:47],v5 offset:12288\nds_read_b64_tr_b16 a[6:7],v12 offset:16384\nM a[252:255],a[28:31],a[40:43],a[252:255]\ns_add_u32 s62,s62,16777216\nds_read_b128 a[40:43],v5 offset:8192\nv_bfe_u32 v57,v4,4,3\nM v[188:191],a[28:31],a[36:39],v[188:191]\n" \
"s_add_u32 s40,s4,s62\ns_addc_u32 s41,s5,0\nds_read_b128 a[36:39],v5 offset:4096\nv_bfe_u32 v61,v4,4,4\nM v[156:159],a[28:31],a[32:35],v[156:159]\ns_lshl_b32 s62,s7,16\nds_read_b128 a[32:35],v5 offset:0\nv_mov_b32 v24,0\n" \
"M v[128:131],a[48:51],a[80:83],v[128:131]\ns_sub_u32 s63,983040,s62\nds_read_b64_tr_b16 a[8:9],v9 offset:16896\nv_mov_b32 v25,0\nM v[160:163],a[48:51],a[84:87],v[160:163]\ns_cmp_lt_u32 %2,2\ns_cselect_b32 s44,s63,s62\ns_cselect_b32 s42,1048576,s63\n" \
"ds_read_b64_tr_b16 a[10:11],v10 offset:16896\nv_mov_b32 v26,0\nM a[224:227],a[48:51],a[88:91],a[224:227]\ns_and_b32 s63,%2,1\nds_read_b64_tr_b16 a[12:13],v11 offset:16896\nds_read_b64_tr_b16 a[14:15],v12 offset:16896\nM a[192:195],a[48:51],a[92:95],a[192:195]\ns_lshl_b32 s63,s63,12\n" \
"ds_read_b64_tr_b16 a[16:17],v9 offset:17408\nv_add_u32 v22,s63,v4\nM a[196:199],a[52:55],a[92:95],a[196:199]\ns_and_b32 s41,s41,0xffff\nds_read_b64_tr_b16 a[18:19],v10 offset:17408\nv_mov_b32 v27,0\nM a[228:231],a[52:55],a[88:91],a[228:231]\ns_add_u32 s61,s44,32768\n" \
"ds_read_b64_tr_b16 a[20:21],v11 offset:17408\nds_read_b64_tr_b16 a[22:23],v12 offset:17408\nM v[164:167],a[52:55],a[84:87],v[164:167]\ns_lshl_b32 s62,s6,6\nds_read_b64_tr_b16 a[24:25],v9 offset:17920\nM v[132:135],a[52:55],a[80:83],v[132:135]\ns_add_u32 s62,s62,94375936\nds_read_b64_tr_b16 a[26:27],v10 offset:17920\n" \
"v_lshl_add_u32 v57,v57,3,s62\nM v[136:139],a[56:59],a[80:83],v[136:139]\ns_lshl_b32 s62,s7,1\nds_read_b64_tr_b16 a[28:29],v11 offset:17920\nds_read_b64_tr_b16 a[30:31],v12 offset:17920\nM v[168:171],a[56:59],a[84:87],v[168:171]\ns_mov_b32 s4,s62\nds_read_b64_tr_b16 a[48:49],v9 offset:24576\n" \
"M a[232:235],a[56:59],a[88:91],a[232:235]\ns_cmp_lt_u32 %2,2\ns_cselect_b32 s62,s62,50\ns_cselect_b32 s54,8192,0\nds_read_b64_tr_b16 a[50:51],v10 offset:24576\nM a[200:203],a[56:59],a[92:95],a[200:203]\nbuffer_load_dwordx4 v21,s[32:35],s47 offen offset:128 lds\nM a[204:207],a[60:63],a[92:95],a[204:207]\n" \
"s_add_u32 m0,s50,65536\nds_read_b64_tr_b16 a[52:53],v11 offset:24576\nM a[236:239],a[60:63],a[88:91],a[236:239]\ns_mov_b32 s47,5242880\nds_read_b64_tr_b16 a[54:55],v12 offset:24576\nM v[172:175],a[60:63],a[84:87],v[172:175]\ns_sub_u32 s55,s62,1\nds_read_b64_tr_b16 a[56:57],v9 offset:25088\n" \
"ds_read_b64_tr_b16 a[58:59],v10 offset:25088\nM v[140:143],a[60:63],a[80:83],v[140:143]\nbuffer_load_dwordx4 v13,s[28:31],s46 offen lds\nM v[144:147],a[64:67],a[80:83],v[144:147]\ns_add_u32 m0,s50,69632\nM v[176:179],a[64:67],a[84:87],v[176:179]\ns_lshl_b32 s70,s62,1\nds_read_b64_tr_b16 a[60:61],v11 offset:25088\n" \
"ds_read_b64_tr_b16 a[62:63],v12 offset:25088\nM a[240:243],a[64:67],a[88:91],a[240:243]\ns_cmp_eq_u32 s62,0\ns_cselect_b64 s[66:67],-1,0\nM a[208:211],a[64:67],a[92:95],a[208:211]\nbuffer_load_dwordx4 v14,s[28:31],s46 offen lds\nM a[212:215],a[68:71],a[92:95],a[212:215]\ns_add_u32 m0,s50,73728\n" \
"ds_read_b64_tr_b16 a[64:65],v9 offset:25600\nM a[244:247],a[68:71],a[88:91],a[244:247]\ns_lshl_b32 s62,s7,2\nds_read_b64_tr_b16 a[66:67],v10 offset:25600\nM v[180:183],a[68:71],a[84:87],v[180:183]\ns_sub_u32 s63,60,s62\nM v[148:151],a[68:71],a[80:83],v[148:151]\nbuffer_load_dwordx4 v15,s[28:31],s46 offen lds\n" \
"M v[152:155],a[72:75],a[80:83],v[152:155]\ns_add_u32 m0,s50,77824\nds_read_b64_tr_b16 a[68:69],v11 offset:25600\nM v[184:187],a[72:75],a[84:87],v[184:187]\ns_cmp_lt_u32 %2,2\ns_cselect_b32 s5,s62,s63\nds_read_b64_tr_b16 a[70:71],v12 offset:25600\nM a[248:251],a[72:75],a[88:91],a[248:251]\n" \
"s_and_b32 s63,%2,1\nM a[216:219],a[72:75],a[92:95],a[216:219]\nbuffer_load_dwordx4 v16,s[28:31],s46 offen lds\nM a[220:223],a[76:79],a[92:95],a[220:223]\ns_mov_b32 s46,1152\nds_read_b64_tr_b16 a[72:73],v9 offset:26112\nM a[252:255],a[76:79],a[88:91],a[252:255]\ns_add_u32 m0,s50,81920\n" \
"ds_read_b64_tr_b16 a[74:75],v10 offset:26112\nds_read_b128 a[88:91],v6 offset:8192\nM v[188:191],a[76:79],a[84:87],v[188:191]\ns_add_u32 s69,s62,s63\nds_read_b128 a[84:87],v6 offset:4096\nM v[156:159],a[76:79],a[80:83],v[156:159]\ns_waitcnt lgkmcnt(4)\nbuffer_load_dwordx4 v17,s[28:31],s46 offen lds\n" \
"M v[128:131],a[0:3],a[32:35],v[128:131]\ns_add_u32 m0,s50,86016\nM v[160:163],a[0:3],a[36:39],v[160:163]\ns_add_u32 s68,s69,s62\nds_read_b128 a[80:83],v6 offset:0\nM a[224:227],a[0:3],a[40:43],a[224:227]\ns_bfe_u32 s62,%1,0x20001\nds_read_b64_tr_b16 a[76:77],v11 offset:26112\n" \
"M a[192:195],a[0:3],a[44:47],a[192:195]\nbuffer_store_dwordx4 v[232:235],v60,s[36:39],s21 offen offset:0 sc1\nM a[196:199],a[4:7],a[44:47],a[196:199]\ns_add_u32 s21,s21,1048576\nds_read_b64_tr_b16 a[78:79],v12 offset:26112\nds_read_b128 a[92:95],v6 offset:12288\nM a[228:231],a[4:7],a[40:43],a[228:231]\ns_lshr_b32 s63,%1,5\n" \
"M v[164:167],a[4:7],a[36:39],v[164:167]\ns_lshl_b32 s62,s62,21\nM v[132:135],a[4:7],a[32:35],v[132:135]\ns_lshl_b32 s63,s63,8\nM v[136:139],a[8:11],a[32:35],v[136:139]\ns_add_u32 s62,s62,s63\nM v[168:171],a[8:11],a[36:39],v[168:171]\ns_sub_u32 s86,8388608,s62\n" \
"M a[232:235],a[8:11],a[40:43],a[232:235]\ns_add_u32 s84,s84,s62\ns_addc_u32 s85,s85,0\nM a[200:203],a[8:11],a[44:47],a[200:203]\ns_and_b32 s63,%2,1\nM a[204:207],a[12:15],a[44:47],a[204:207]\ns_lshl_b32 s62,s63,4\nM a[236:239],a[12:15],a[40:43],a[236:239]\n" \
"s_and_b32 s63,%2,1\nM v[172:175],a[12:15],a[36:39],v[172:175]\ns_lshl_b32 s63,s63,14\nM v[140:143],a[12:15],a[32:35],v[140:143]\ns_add_u32 s63,s63,0x20000\nM v[144:147],a[16:19],a[32:35],v[144:147]\ns_cmp_lt_u32 %2,2\ns_cselect_b32 s65,0,-4096\n" \
"M v[176:179],a[16:19],a[36:39],v[176:179]\ns_cmp_lt_u32 %2,2\ns_cselect_b32 s49,1,-1\ns_cselect_b32 s45,-1,60\nM a[240:243],a[16:19],a[40:43],a[240:243]\ns_lshl_b32 s45,s45,14\nM a[208:211],a[16:19],a[44:47],a[208:211]\nbuffer_load_dwordx4 v18,s[28:31],s46 offen lds\n" \
"M a[212:215],a[20:23],a[44:47],a[212:215]\ns_add_u32 m0,s50,90112\nM a[244:247],a[20:23],a[40:43],a[244:247]\ns_lshl_b32 s49,s49,14\nM v[180:183],a[20:23],a[36:39],v[180:183]\ns_add_u32 s48,s45,8192\nM v[148:151],a[20:23],a[32:35],v[148:151]\nbuffer_load_dwordx4 v19,s[28:31],s46 offen lds\n" \
"M v[152:155],a[24:27],a[32:35],v[152:155]\ns_waitcnt vmcnt(13) lgkmcnt(0)\nM v[184:187],a[24:27],a[36:39],v[184:187]\ns_add_u32 m0,s50,94208\ns_barrier\nds_read_b64_tr_b16 a[0:1],v9 offset:32768\nM a[248:251],a[24:27],a[40:43],a[248:251]\ns_add_u32 s45,s45,s49\n" \
"ds_read_b64_tr_b16 a[2:3],v10 offset:32768\nds_read_b64_tr_b16 a[4:5],v11 offset:32768\nM a[216:219],a[24:27],a[44:47],a[216:219]\nbuffer_load_dwordx4 v20,s[28:31],s46 offen lds\nM a[220:223],a[28:31],a[44:47],a[220:223]\ns_add_u32 m0,s51,0\nds_read_b128 a[44:47],v5 offset:45056\nM a[252:255],a[28:31],a[40:43],a[252:255]\n" \
"s_mov_b32 s46,1280\nds_read_b128 a[40:43],v5 offset:40960\nM v[188:191],a[28:31],a[36:39],v[188:191]\ns_add_u32 s48,s48,s49\nds_read_b128 a[36:39],v5 offset:36864\nM v[156:159],a[28:31],a[32:35],v[156:159]\nbuffer_load_dwordx4 v21,s[32:35],s47 offen offset:0 lds\nM v[128:131],a[48:51],a[80:83],v[128:131]\n" \
"s_add_u32 m0,s51,896\nM v[160:163],a[48:51],a[84:87],v[160:163]\ns_mov_b32 s52,-1\nds_read_b128 a[32:35],v5 offset:32768\nM a[224:227],a[48:51],a[88:91],a[224:227]\ns_min_u32 s56,s4,8\nds_read_b64_tr_b16 a[6:7],v12 offset:32768\nM a[192:195],a[48:51],a[92:95],a[192:195]\n" \
"buffer_load_dwordx4 v21,s[32:35],s47 offen offset:128 lds\nM a[196:199],a[52:55],a[92:95],a[196:199]\ns_add_u32 m0,s51,8192\nds_read_b64_tr_b16 a[8:9],v9 offset:33280\nM a[228:231],a[52:55],a[88:91],a[228:231]\ns_mov_b32 s47,5505024\nds_read_b64_tr_b16 a[10:11],v10 offset:33280\nM v[164:167],a[52:55],a[84:87],v[164:167]\n" \
"s_mov_b32 s87,0x00020000\nds_read_b64_tr_b16 a[12:13],v11 offset:33280\nds_read_b64_tr_b16 a[14:15],v12 offset:33280\nM v[132:135],a[52:55],a[80:83],v[132:135]\nbuffer_load_dwordx4 v21,s[32:35],s47 offen offset:0 lds\nM v[136:139],a[56:59],a[80:83],v[136:139]\ns_add_u32 m0,s51,9088\nM v[168:171],a[56:59],a[84:87],v[168:171]\n" \
"s_and_b32 s85,s85,0xffff\nds_read_b64_tr_b16 a[16:17],v9 offset:33792\nds_read_b64_tr_b16 a[18:19],v10 offset:33792\nM a[232:235],a[56:59],a[88:91],a[232:235]\ns_nop 0\ns_lshl_b32 s72,%1,2\nds_read_b64_tr_b16 a[20:21],v11 offset:33792\nM a[200:203],a[56:59],a[92:95],a[200:203]\n" \
"buffer_load_dwordx4 v21,s[32:35],s47 offen offset:128 lds\nM a[204:207],a[60:63],a[92:95],a[204:207]\ns_add_u32 m0,s50,0\nds_read_b64_tr_b16 a[22:23],v12 offset:33792\nM a[236:239],a[60:63],a[88:91],a[236:239]\ns_mov_b32 s47,5767168\nds_read_b64_tr_b16 a[24:25],v9 offset:34304\nM v[172:175],a[60:63],a[84:87],v[172:175]\n" \
"s_add_u32 s72,s72,%2\nds_read_b64_tr_b16 a[26:27],v10 offset:34304\nM v[140:143],a[60:63],a[80:83],v[140:143]\nbuffer_load_dwordx4 v13,s[28:31],s46 offen lds\nM v[144:147],a[64:67],a[80:83],v[144:147]\ns_add_u32 m0,s50,4096\nds_read_b64_tr_b16 a[28:29],v11 offset:34304\nM v[176:179],a[64:67],a[84:87],v[176:179]\n" \
"s_mul_i32 s72,s72,12288\nds_read_b64_tr_b16 a[30:31],v12 offset:34304\nM a[240:243],a[64:67],a[88:91],a[240:243]\ns_add_u32 s72,s72,109060096\nds_read_b64_tr_b16 a[48:49],v9 offset:40960\nds_read_b64_tr_b16 a[50:51],v10 offset:40960\nM a[208:211],a[64:67],a[92:95],a[208:211]\nbuffer_load_dwordx4 v14,s[28:31],s46 offen lds\n" \
"M a[212:215],a[68:71],a[92:95],a[212:215]\ns_add_u32 m0,s50,8192\nM a[244:247],a[68:71],a[88:91],a[244:247]\ns_lshl_b32 s68,s68,5\nds_read_b64_tr_b16 a[52:53],v11 offset:40960\nds_read_b64_tr_b16 a[54:55],v12 offset:40960\nM v[180:183],a[68:71],a[84:87],v[180:183]\ns_add_u32 s68,s68,s6\n" \
"ds_read_b64_tr_b16 a[56:57],v9 offset:41472\nM v[148:151],a[68:71],a[80:83],v[148:151]\nbuffer_load_dwordx4 v15,s[28:31],s46 offen lds\nM v[152:155],a[72:75],a[80:83],v[152:155]\ns_add_u32 m0,s50,12288\nds_read_b64_tr_b16 a[58:59],v10 offset:41472\nM v[184:187],a[72:75],a[84:87],v[184:187]\ns_lshl_b32 s68,s68,12\n" \
"ds_read_b64_tr_b16 a[60:61],v11 offset:41472\nM a[248:251],a[72:75],a[88:91],a[248:251]\ns_add_u32 s68,s68,50331648\nds_read_b64_tr_b16 a[62:63],v12 offset:41472\nM a[216:219],a[72:75],a[92:95],a[216:219]\nbuffer_load_dwordx4 v16,s[28:31],s46 offen lds\nM a[220:223],a[76:79],a[92:95],a[220:223]\ns_mov_b32 s46,1280\n" \
"ds_read_b64_tr_b16 a[64:65],v9 offset:41984\nM a[252:255],a[76:79],a[88:91],a[252:255]\ns_add_u32 m0,s50,16384\nds_read_b64_tr_b16 a[66:67],v10 offset:41984\nM v[188:191],a[76:79],a[84:87],v[188:191]\ns_lshl_b32 s69,s69,5\nds_read_b128 a[84:87],v6 offset:36864\nM v[156:159],a[76:79],a[80:83],v[156:159]\n" \
"s_waitcnt lgkmcnt(4)\nbuffer_store_dwordx4 v[240:243],v60,s[36:39],s21 offen offset:0 sc1\nM v[128:131],a[0:3],a[32:35],v[128:131]\ns_add_u32 s21,s21,1048576\nds_read_b128 a[80:83],v6 offset:32768\nds_read_b64_tr_b16 a[68:69],v11 offset:41984\nM v[160:163],a[0:3],a[36:39],v[160:163]\ns_add_u32 s69,s69,s6\n" \
"ds_read_b64_tr_b16 a[70:71],v12 offset:41984\nM a[224:227],a[0:3],a[40:43],a[224:227]\ns_lshl_b32 s69,s69,3\nds_read_b64_tr_b16 a[72:73],v9 offset:42496\nM a[192:195],a[0:3],a[44:47],a[192:195]\ns_add_u32 s69,s69,94388224\nds_read_b64_tr_b16 a[74:75],v10 offset:42496\nds_read_b64_tr_b16 a[76:77],v11 offset:42496\n" \
"M a[196:199],a[4:7],a[44:47],a[196:199]\ns_and_b32 s89,s89,0xffff\nds_read_b64_tr_b16 a[78:79],v12 offset:42496\nM a[228:231],a[4:7],a[40:43],a[228:231]\ns_mov_b32 s90,4194304\nds_read_b128 a[88:91],v6 offset:40960\nM v[164:167],a[4:7],a[36:39],v[164:167]\ns_mov_b32 s91,0x00020000\n" \
"ds_read_b128 a[92:95],v6 offset:45056\nM v[132:135],a[4:7],a[32:35],v[132:135]\ns_lshl_b32 s71,s6,20\nM v[136:139],a[8:11],a[32:35],v[136:139]\ns_mov_b32 s75,0\nM v[168:171],a[8:11],a[36:39],v[168:171]\ns_add_u32 s71,s71,16777216\nM a[232:235],a[8:11],a[40:43],a[232:235]\n" \
"M a[200:203],a[8:11],a[44:47],a[200:203]\nM a[204:207],a[12:15],a[44:47],a[204:207]\nM a[236:239],a[12:15],a[40:43],a[236:239]\nM v[172:175],a[12:15],a[36:39],v[172:175]\nM v[140:143],a[12:15],a[32:35],v[140:143]\nbuffer_load_dwordx4 v17,s[28:31],s46 offen lds\nM v[144:147],a[16:19],a[32:35],v[144:147]\ns_add_u32 m0,s50,20480\n" \
"M v[176:179],a[16:19],a[36:39],v[176:179]\nM a[240:243],a[16:19],a[40:43],a[240:243]\nM a[208:211],a[16:19],a[44:47],a[208:211]\nbuffer_load_dwordx4 v18,s[28:31],s46 offen lds\nM a[212:215],a[20:23],a[44:47],a[212:215]\ns_add_u32 m0,s50,24576\nM a[244:247],a[20:23],a[40:43],a[244:247]\nM v[180:183],a[20:23],a[36:39],v[180:183]\n" \
"M v[148:151],a[20:23],a[32:35],v[148:151]\nbuffer_load_dwordx4 v19,s[28:31],s46 offen lds\nM v[152:155],a[24:27],a[32:35],v[152:155]\ns_waitcnt vmcnt(12) lgkmcnt(0)\nM v[184:187],a[24:27],a[36:39],v[184:187]\ns_add_u32 m0,s50,28672\ns_barrier\nds_read_b64_tr_b16 a[0:1],v9 offset:49152\n" \
"M a[248:251],a[24:27],a[40:43],a[248:251]\nds_read_b64_tr_b16 a[2:3],v10 offset:49152\nds_read_b64_tr_b16 a[4:5],v11 offset:49152\nM a[216:219],a[24:27],a[44:47],a[216:219]\nbuffer_load_dwordx4 v20,s[28:31],s46 offen lds\nM a[220:223],a[28:31],a[44:47],a[220:223]\ns_add_u32 m0,s51,16384\nds_read_b128 a[44:47],v7 offset:12288\n" \
"M a[252:255],a[28:31],a[40:43],a[252:255]\ns_mov_b32 s46,1408\nds_read_b128 a[40:43],v7 offset:8192\nM v[188:191],a[28:31],a[36:39],v[188:191]\nds_read_b128 a[36:39],v7 offset:4096\nM v[156:159],a[28:31],a[32:35],v[156:159]\nbuffer_load_dwordx4 v21,s[32:35],s47 offen offset:0 lds\nM v[128:131],a[48:51],a[80:83],v[128:131]\n" \
"s_add_u32 m0,s51,17280\nM v[160:163],a[48:51],a[84:87],v[160:163]\nds_read_b128 a[32:35],v7 offset:0\nM a[224:227],a[48:51],a[88:91],a[224:227]\nds_read_b64_tr_b16 a[6:7],v12 offset:49152\nM a[192:195],a[48:51],a[92:95],a[192:195]\nbuffer_load_dwordx4 v21,s[32:35],s47 offen offset:128 lds\nM a[196:199],a[52:55],a[92:95],a[196:199]\n" \
"s_add_u32 m0,s51,24576\nds_read_b64_tr_b16 a[8:9],v9 offset:49664\nM a[228:231],a[52:55],a[88:91],a[228:231]\ns_mov_b32 s47,6029312\nds_read_b64_tr_b16 a[10:11],v10 offset:49664\nM v[164:167],a[52:55],a[84:87],v[164:167]\nds_read_b64_tr_b16 a[12:13],v11 offset:49664\nds_read_b64_tr_b16 a[14:15],v12 offset:49664\n" \
"M v[132:135],a[52:55],a[80:83],v[132:135]\nbuffer_load_dwordx4 v21,s[32:35],s47 offen offset:0 lds\nM v[136:139],a[56:59],a[80:83],v[136:139]\ns_add_u32 m0,s51,25472\nM v[168:171],a[56:59],a[84:87],v[168:171]\nds_read_b64_tr_b16 a[16:17],v9 offset:50176\nds_read_b64_tr_b16 a[18:19],v10 offset:50176\nM a[232:235],a[56:59],a[88:91],a[232:235]\n" \
"ds_read_b64_tr_b16 a[20:21],v11 offset:50176\nM a[200:203],a[56:59],a[92:95],a[200:203]\nbuffer_store_dwordx4 v[92:95],v4,s[36:39],s96 offen offset:1024 sc1\nM a[204:207],a[60:63],a[92:95],a[204:207]\nds_read_b64_tr_b16 a[22:23],v12 offset:50176\nds_read_b64_tr_b16 a[24:25],v9 offset:50688\nM a[236:239],a[60:63],a[88:91],a[236:239]\nds_read_b64_tr_b16 a[26:27],v10 offset:50688\n" \
"M v[172:175],a[60:63],a[84:87],v[172:175]\nds_read_b64_tr_b16 a[28:29],v11 offset:50688\nds_read_b64_tr_b16 a[30:31],v12 offset:50688\nM v[140:143],a[60:63],a[80:83],v[140:143]\nbuffer_load_dwordx4 v21,s[32:35],s47 offen offset:128 lds\nM v[144:147],a[64:67],a[80:83],v[144:147]\ns_add_u32 m0,s50,32768\nM v[176:179],a[64:67],a[84:87],v[176:179]\n" \
"s_mov_b32 s47,6291456\nds_read_b64_tr_b16 a[48:49],v9 offset:57344\nds_read_b64_tr_b16 a[50:51],v10 offset:57344\nM a[240:243],a[64:67],a[88:91],a[240:243]\nds_read_b64_tr_b16 a[52:53],v11 offset:57344\nM a[208:211],a[64:67],a[92:95],a[208:211]\nbuffer_load_dwordx4 v13,s[28:31],s46 offen lds\nM a[212:215],a[68:71],a[92:95],a[212:215]\n" \
"s_add_u32 m0,s50,36864\nds_read_b64_tr_b16 a[54:55],v12 offset:57344\nM a[244:247],a[68:71],a[88:91],a[244:247]\nds_read_b64_tr_b16 a[56:57],v9 offset:57856\nM v[180:183],a[68:71],a[84:87],v[180:183]\nds_read_b64_tr_b16 a[58:59],v10 offset:57856\nM v[148:151],a[68:71],a[80:83],v[148:151]\nbuffer_load_dwordx4 v14,s[28:31],s46 offen lds\n" \
"M v[152:155],a[72:75],a[80:83],v[152:155]\ns_add_u32 m0,s50,40960\nds_read_b64_tr_b16 a[60:61],v11 offset:57856\nM v[184:187],a[72:75],a[84:87],v[184:187]\nds_read_b64_tr_b16 a[62:63],v12 offset:57856\nM a[248:251],a[72:75],a[88:91],a[248:251]\nds_read_b64_tr_b16 a[64:65],v9 offset:58368\nds_read_b64_tr_b16 a[66:67],v10 offset:58368\n" \
"M a[216:219],a[72:75],a[92:95],a[216:219]\nbuffer_load_dwordx4 v15,s[28:31],s46 offen lds\nM a[220:223],a[76:79],a[92:95],a[220:223]\ns_add_u32 m0,s50,45056\nM a[252:255],a[76:79],a[88:91],a[252:255]\nds_read_b64_tr_b16 a[68:69],v11 offset:58368\nds_read_b64_tr_b16 a[70:71],v12 offset:58368\nM v[188:191],a[76:79],a[84:87],v[188:191]\n" \
"s_waitcnt lgkmcnt(5)\nds_read_b128 a[84:87],v8 offset:4096\nM v[156:159],a[76:79],a[80:83],v[156:159]\nbuffer_load_dwordx4 v16,s[28:31],s46 offen lds\nM v[128:131],a[0:3],a[32:35],v[128:131]\ns_mov_b32 s46,1408\nM v[160:163],a[0:3],a[36:39],v[160:163]\ns_add_u32 m0,s50,49152\n" \
"ds_read_b128 a[80:83],v8 offset:0\nM a[224:227],a[0:3],a[40:43],a[224:227]\nds_read_b64_tr_b16 a[72:73],v9 offset:58880\nds_read_b64_tr_b16 a[74:75],v10 offset:58880\nM a[192:195],a[0:3],a[44:47],a[192:195]\nbuffer_load_dwordx4 v17,s[28:31],s46 offen lds\nM a[196:199],a[4:7],a[44:47],a[196:199]\ns_add_u32 m0,s50,53248\n" \
"M a[228:231],a[4:7],a[40:43],a[228:231]\nds_read_b64_tr_b16 a[76:77],v11 offset:58880\nds_read_b64_tr_b16 a[78:79],v12 offset:58880\nM v[164:167],a[4:7],a[36:39],v[164:167]\nds_read_b128 a[88:91],v8 offset:8192\nM v[132:135],a[4:7],a[32:35],v[132:135]\nbuffer_load_dwordx4 v18,s[28:31],s46 offen lds\nM v[136:139],a[8:11],a[32:35],v[136:139]\n" \
"s_add_u32 m0,s50,57344\nM v[168:171],a[8:11],a[36:39],v[168:171]\nds_read_b128 a[92:95],v8 offset:12288\nM a[232:235],a[8:11],a[40:43],a[232:235]\nM a[200:203],a[8:11],a[44:47],a[200:203]\nbuffer_load_dwordx4 v19,s[28:31],s46 offen lds\nM a[204:207],a[12:15],a[44:47],a[204:207]\ns_add_u32 m0,s50,61440\n" \
"M a[236:239],a[12:15],a[40:43],a[236:239]\nM v[172:175],a[12:15],a[36:39],v[172:175]\nM v[140:143],a[12:15],a[32:35],v[140:143]\nbuffer_load_dwordx4 v20,s[28:31],s46 offen lds\nM v[144:147],a[16:19],a[32:35],v[144:147]\ns_add_u32 m0,s51,32768\nM v[176:179],a[16:19],a[36:39],v[176:179]\ns_mov_b32 s46,1536\n" \
"M a[240:243],a[16:19],a[40:43],a[240:243]\nM a[208:211],a[16:19],a[44:47],a[208:211]\nbuffer_load_dwordx4 v21,s[32:35],s47 offen offset:0 lds\nM a[212:215],a[20:23],a[44:47],a[212:215]\ns_add_u32 m0,s51,33664\nM a[244:247],a[20:23],a[40:43],a[244:247]\nM v[180:183],a[20:23],a[36:39],v[180:183]\nM v[148:151],a[20:23],a[32:35],v[148:151]\n" \
"buffer_store_dwordx4 v[248:251],v60,s[36:39],s21 offen offset:0 sc1\nM v[152:155],a[24:27],a[32:35],v[152:155]\ns_waitcnt vmcnt(15) lgkmcnt(0)\nv_lshrrev_b32 v60,8,v4\nv_lshlrev_b32 v60,2,v60\nM v[184:187],a[24:27],a[36:39],v[184:187]\ns_barrier\nds_read_b64_tr_b16 a[0:1],v9 offset:0\n" \
"M a[248:251],a[24:27],a[40:43],a[248:251]\nds_read_b64_tr_b16 a[2:3],v10 offset:0\nds_read_b64_tr_b16 a[4:5],v11 offset:0\nM a[216:219],a[24:27],a[44:47],a[216:219]\nds_read_b64_tr_b16 a[6:7],v12 offset:0\nv_sub_u32 v60,v60,v61\nM a[220:223],a[28:31],a[44:47],a[220:223]\nds_read_b128 a[44:47],v5 offset:12288\n" \
"v_subrev_u32 v60,s62,v60\nM a[252:255],a[28:31],a[40:43],a[252:255]\ns_xor_b32 s62,s22,0x2000\nds_read_b128 a[40:43],v5 offset:8192\nv_mov_b32 v61,0xff800000\nM v[188:191],a[28:31],a[36:39],v[188:191]\ns_add_u32 s62,s62,84992\nds_read_b128 a[36:39],v5 offset:4096\n" \
"v_add_u32 v23,s62,v4\nM v[156:159],a[28:31],a[32:35],v[156:159]\ns_and_b32 s62,%2,1\nds_read_b128 a[32:35],v5 offset:0\nv_cmp_lt_i32 vcc,0,v60\nM v[128:131],a[48:51],a[80:83],v[128:131]\ns_lshl_b32 s62,s62,15\nds_read_b64_tr_b16 a[8:9],v9 offset:512\n" \
"ds_read_b64_tr_b16 a[10:11],v10 offset:512\nM v[160:163],a[48:51],a[84:87],v[160:163]\ns_add_u32 s62,s62,s54\nds_read_b64_tr_b16 a[12:13],v11 offset:512\nv_cndmask_b32 v48,0,v61,vcc\nM a[224:227],a[48:51],a[88:91],a[224:227]\ns_add_u32 s62,s62,32768\nds_read_b64_tr_b16 a[14:15],v12 offset:512\n" \
"v_add_u32 v1,s62,v4\nM a[192:195],a[48:51],a[92:95],a[192:195]\nds_read_b64_tr_b16 a[16:17],v9 offset:1024\nds_read_b64_tr_b16 a[18:19],v10 offset:1024\nM a[196:199],a[52:55],a[92:95],a[196:199]\nds_read_b64_tr_b16 a[20:21],v11 offset:1024\nv_cmp_lt_i32 vcc,-1,v60\nM a[228:231],a[52:55],a[88:91],a[228:231]\n" \
"ds_read_b64_tr_b16 a[22:23],v12 offset:1024\nv_cndmask_b32 v49,0,v61,vcc\nM v[164:167],a[52:55],a[84:87],v[164:167]\nds_read_b64_tr_b16 a[24:25],v9 offset:1536\nds_read_b64_tr_b16 a[26:27],v10 offset:1536\nM v[132:135],a[52:55],a[80:83],v[132:135]\nbuffer_load_dwordx4 v21,s[32:35],s47 offen offset:128 lds\nM v[136:139],a[56:59],a[80:83],v[136:139]\n" \
"s_add_u32 m0,s51,40960\nv_cmp_lt_i32 vcc,-2,v60\nM v[168:171],a[56:59],a[84:87],v[168:171]\ns_mov_b32 s47,6553600\nds_read_b64_tr_b16 a[28:29],v11 offset:1536\nds_read_b64_tr_b16 a[30:31],v12 offset:1536\nM a[232:235],a[56:59],a[88:91],a[232:235]\nds_read_b64_tr_b16 a[48:49],v9 offset:8192\n" \
"v_cndmask_b32 v50,0,v61,vcc\nM a[200:203],a[56:59],a[92:95],a[200:203]\ns_mov_b64 exec,0xff\nglobal_load_dword v0,v2,s[100:101]\ns_mov_b64 exec,-1\nM a[204:207],a[60:63],a[92:95],a[204:207]\nds_read_b64_tr_b16 a[50:51],v10 offset:8192\nds_read_b64_tr_b16 a[52:53],v11 offset:8192\n" \
"M a[236:239],a[60:63],a[88:91],a[236:239]\nbuffer_load_dwordx4 v21,s[32:35],s47 offen offset:0 lds\nM v[172:175],a[60:63],a[84:87],v[172:175]\ns_add_u32 m0,s51,41856\nds_read_b64_tr_b16 a[54:55],v12 offset:8192\nv_cmp_lt_i32 vcc,-3,v60\nM v[140:143],a[60:63],a[80:83],v[140:143]\nds_read_b64_tr_b16 a[56:57],v9 offset:8704\n" \
"v_cndmask_b32 v51,0,v61,vcc\nM v[144:147],a[64:67],a[80:83],v[144:147]\nds_read_b64_tr_b16 a[58:59],v10 offset:8704\nv_cmp_lt_i32 vcc,-16,v60\nM v[176:179],a[64:67],a[84:87],v[176:179]\nbuffer_store_dwordx4 v[104:107],v4,s[36:39],s96 offen offset:2048 sc1\nM a[240:243],a[64:67],a[88:91],a[240:243]\nds_read_b64_tr_b16 a[60:61],v11 offset:8704\n" \
"ds_read_b64_tr_b16 a[62:63],v12 offset:8704\nM a[208:211],a[64:67],a[92:95],a[208:211]\nds_read_b64_tr_b16 a[64:65],v9 offset:9216\nv_cndmask_b32 v52,0,v61,vcc\nM a[212:215],a[68:71],a[92:95],a[212:215]\nds_read_b64_tr_b16 a[66:67],v10 offset:9216\nv_cmp_lt_i32 vcc,-17,v60\nM a[244:247],a[68:71],a[88:91],a[244:247]\n" \
"buffer_load_dwordx4 v21,s[32:35],s47 offen offset:128 lds\nM v[180:183],a[68:71],a[84:87],v[180:183]\ns_add_u32 m0,s50,65536\nv_cndmask_b32 v53,0,v61,vcc\nv_cmp_lt_i32 vcc,-18,v60\nM v[148:151],a[68:71],a[80:83],v[148:151]\ns_mov_b32 s47,6815744\nds_read_b64_tr_b16 a[68:69],v11 offset:9216\n" \
"ds_read_b64_tr_b16 a[70:71],v12 offset:9216\nM v[152:155],a[72:75],a[80:83],v[152:155]\nv_cndmask_b32 v54,0,v61,vcc\nv_cmp_lt_i32 vcc,-19,v60\nM v[184:187],a[72:75],a[84:87],v[184:187]\nbuffer_load_dwordx4 v13,s[28:31],s46 offen lds\nM a[248:251],a[72:75],a[88:91],a[248:251]\ns_add_u32 m0,s50,69632\n" \
"v_cndmask_b32 v55,0,v61,vcc\nM a[216:219],a[72:75],a[92:95],a[216:219]\nds_read_b64_tr_b16 a[72:73],v9 offset:9728\nds_read_b64_tr_b16 a[74:75],v10 offset:9728\nM a[220:223],a[76:79],a[92:95],a[220:223]\nds_read_b128 a[92:95],v6 offset:12288\nM a[252:255],a[76:79],a[88:91],a[252:255]\nbuffer_load_dwordx4 v14,s[28:31],s46 offen lds\n" \
"M v[188:191],a[76:79],a[84:87],v[188:191]\ns_add_u32 m0,s50,73728\nds_read_b128 a[84:87],v6 offset:4096\nM v[156:159],a[76:79],a[80:83],v[156:159]\ns_waitcnt lgkmcnt(4)\nds_read_b128 a[80:83],v6 offset:0\nM v[128:131],a[0:3],a[32:35],v[128:131]\nds_read_b64_tr_b16 a[76:77],v11 offset:9728\n" \
"M v[160:163],a[0:3],a[36:39],v[160:163]\nbuffer_load_dwordx4 v15,s[28:31],s46 offen lds\nM a[224:227],a[0:3],a[40:43],a[224:227]\ns_add_u32 m0,s50,77824\nM a[192:195],a[0:3],a[44:47],a[192:195]\nds_read_b64_tr_b16 a[78:79],v12 offset:9728\nds_read_b128 a[88:91],v6 offset:8192\nM a[196:199],a[4:7],a[44:47],a[196:199]\n" \
"M a[228:231],a[4:7],a[40:43],a[228:231]\nbuffer_load_dwordx4 v16,s[28:31],s46 offen lds\nM v[164:167],a[4:7],a[36:39],v[164:167]\ns_add_u32 m0,s50,81920\nM v[132:135],a[4:7],a[32:35],v[132:135]\ns_mov_b32 s46,1536\nM v[136:139],a[8:11],a[32:35],v[136:139]\nM v[168:171],a[8:11],a[36:39],v[168:171]\n" \
"buffer_load_dwordx4 v17,s[28:31],s46 offen lds\nM a[232:235],a[8:11],a[40:43],a[232:235]\ns_add_u32 m0,s50,86016\nM a[200:203],a[8:11],a[44:47],a[200:203]\nM a[204:207],a[12:15],a[44:47],a[204:207]\nM a[236:239],a[12:15],a[40:43],a[236:239]\nbuffer_load_dwordx4 v18,s[28:31],s46 offen lds\nM v[172:175],a[12:15],a[36:39],v[172:175]\n" \
"s_add_u32 m0,s50,90112\nM v[140:143],a[12:15],a[32:35],v[140:143]\nM v[144:147],a[16:19],a[32:35],v[144:147]\nM v[176:179],a[16:19],a[36:39],v[176:179]\nbuffer_load_dwordx4 v19,s[28:31],s46 offen lds\nM a[240:243],a[16:19],a[40:43],a[240:243]\ns_add_u32 m0,s50,94208\nM a[208:211],a[16:19],a[44:47],a[208:211]\n" \
"M a[212:215],a[20:23],a[44:47],a[212:215]\nM a[244:247],a[20:23],a[40:43],a[244:247]\nbuffer_load_dwordx4 v20,s[28:31],s46 offen lds\nM v[180:183],a[20:23],a[36:39],v[180:183]\ns_add_u32 m0,s51,49152\nM v[148:151],a[20:23],a[32:35],v[148:151]\ns_mov_b32 s46,1664\nM v[152:155],a[24:27],a[32:35],v[152:155]\n" \
"s_waitcnt vmcnt(15) lgkmcnt(0)\nM v[184:187],a[24:27],a[36:39],v[184:187]\ns_barrier\nbuffer_store_dwordx4 v[108:111],v4,s[36:39],s96 offen offset:3072 sc1\nM a[248:251],a[24:27],a[40:43],a[248:251]\ns_add_u32 s96,s57,53248\nds_read_b64_tr_b16 a[0:1],v9 offset:16384\nds_read_b64_tr_b16 a[2:3],v10 offset:16384\n" \
"M a[216:219],a[24:27],a[44:47],a[216:219]\nds_read_b64_tr_b16 a[4:5],v11 offset:16384\nM a[220:223],a[28:31],a[44:47],a[220:223]\nds_read_b128 a[44:47],v5 offset:45056\nM a[252:255],a[28:31],a[40:43],a[252:255]\nbuffer_load_dwordx4 v21,s[32:35],s47 offen offset:0 lds\nM v[188:191],a[28:31],a[36:39],v[188:191]\ns_add_u32 m0,s51,50048\n" \
"ds_read_b128 a[36:39],v5 offset:36864\nM v[156:159],a[28:31],a[32:35],v[156:159]\nds_read_b128 a[32:35],v5 offset:32768\nM v[128:131],a[48:51],a[80:83],v[128:131]\nds_read_b128 a[40:43],v5 offset:40960\nM v[160:163],a[48:51],a[84:87],v[160:163]\nbuffer_load_dwordx4 v21,s[32:35],s47 offen offset:128 lds\nM a[224:227],a[48:51],a[88:91],a[224:227]\n" \
"s_add_u32 m0,s51,57344\nM a[192:195],a[48:51],a[92:95],a[192:195]\ns_mov_b32 s47,7077888\nds_read_b64_tr_b16 a[6:7],v12 offset:16384\nds_read_b64_tr_b16 a[8:9],v9 offset:16896\nM a[196:199],a[52:55],a[92:95],a[196:199]\nds_read_b64_tr_b16 a[10:11],v10 offset:16896\nM a[228:231],a[52:55],a[88:91],a[228:231]\n" \
"buffer_load_dwordx4 v21,s[32:35],s47 offen offset:0 lds\nM v[164:167],a[52:55],a[84:87],v[164:167]\ns_add_u32 m0,s51,58240\nds_read_b64_tr_b16 a[12:13],v11 offset:16896\nM v[132:135],a[52:55],a[80:83],v[132:135]\nds_read_b64_tr_b16 a[14:15],v12 offset:16896\nM v[136:139],a[56:59],a[80:83],v[136:139]\nds_read_b64_tr_b16 a[16:17],v9 offset:17408\n" \
"M v[168:171],a[56:59],a[84:87],v[168:171]\nbuffer_load_dwordx4 v21,s[32:35],s47 offen offset:128 lds\nM a[232:235],a[56:59],a[88:91],a[232:235]\ns_add_u32 m0,s50,0\nds_read_b64_tr_b16 a[18:19],v10 offset:17408\nM a[200:203],a[56:59],a[92:95],a[200:203]\ns_mov_b32 s47,7340032\nds_read_b64_tr_b16 a[20:21],v11 offset:17408\n" \
"M a[204:207],a[60:63],a[92:95],a[204:207]\nds_read_b64_tr_b16 a[22:23],v12 offset:17408\nds_read_b64_tr_b16 a[24:25],v9 offset:17920\nM a[236:239],a[60:63],a[88:91],a[236:239]\nbuffer_load_dwordx4 v13,s[28:31],s46 offen lds\nM v[172:175],a[60:63],a[84:87],v[172:175]\ns_add_u32 m0,s50,4096\nM v[140:143],a[60:63],a[80:83],v[140:143]\n" \
"ds_read_b64_tr_b16 a[26:27],v10 offset:17920\nds_read_b64_tr_b16 a[28:29],v11 offset:17920\nM v[144:147],a[64:67],a[80:83],v[144:147]\nds_read_b64_tr_b16 a[30:31],v12 offset:17920\nM v[176:179],a[64:67],a[84:87],v[176:179]\nbuffer_load_dwordx4 v14,s[28:31],s46 offen lds\nM a[240:243],a[64:67],a[88:91],a[240:243]\ns_add_u32 m0,s50,8192\n" \
"ds_read_b64_tr_b16 a[48:49],v9 offset:24576\nM a[208:211],a[64:67],a[92:95],a[208:211]\nds_read_b64_tr_b16 a[50:51],v10 offset:24576\nM a[212:215],a[68:71],a[92:95],a[212:215]\nds_read_b64_tr_b16 a[52:53],v11 offset:24576\nM a[244:247],a[68:71],a[88:91],a[244:247]\nbuffer_load_dwordx4 v15,s[28:31],s46 offen lds\nM v[180:183],a[68:71],a[84:87],v[180:183]\n" \
"s_add_u32 m0,s50,12288\nds_read_b64_tr_b16 a[54:55],v12 offset:24576\nM v[148:151],a[68:71],a[80:83],v[148:151]\nds_read_b64_tr_b16 a[56:57],v9 offset:25088\nM v[152:155],a[72:75],a[80:83],v[152:155]\nds_read_b64_tr_b16 a[58:59],v10 offset:25088\nds_read_b64_tr_b16 a[60:61],v11 offset:25088\nM v[184:187],a[72:75],a[84:87],v[184:187]\n" \
"buffer_load_dwordx4 v16,s[28:31],s46 offen lds\nM a[248:251],a[72:75],a[88:91],a[248:251]\ns_mov_b32 s46,1664\nM a[216:219],a[72:75],a[92:95],a[216:219]\ns_add_u32 m0,s50,16384\nds_read_b64_tr_b16 a[62:63],v12 offset:25088\nds_read_b64_tr_b16 a[64:65],v9 offset:25600\nM a[220:223],a[76:79],a[92:95],a[220:223]\n" \
"ds_read_b64_tr_b16 a[66:67],v10 offset:25600\nM a[252:255],a[76:79],a[88:91],a[252:255]\nbuffer_load_dwordx4 v17,s[28:31],s46 offen lds\nM v[188:191],a[76:79],a[84:87],v[188:191]\ns_add_u32 m0,s50,20480\nds_read_b128 a[84:87],v6 offset:36864\nM v[156:159],a[76:79],a[80:83],v[156:159]\ns_waitcnt lgkmcnt(4)\n" \
"ds_read_b128 a[80:83],v6 offset:32768\nM v[128:131],a[0:3],a[32:35],v[128:131]\nds_read_b64_tr_b16 a[68:69],v11 offset:25600\nM v[160:163],a[0:3],a[36:39],v[160:163]\nbuffer_load_dwordx4 v18,s[28:31],s46 offen lds\nM a[224:227],a[0:3],a[40:43],a[224:227]\ns_add_u32 m0,s50,24576\nM a[192:195],a[0:3],a[44:47],a[192:195]\n" \
"ds_read_b64_tr_b16 a[70:71],v12 offset:25600\nds_read_b64_tr_b16 a[72:73],v9 offset:26112\nM a[196:199],a[4:7],a[44:47],a[196:199]\nds_read_b64_tr_b16 a[74:75],v10 offset:26112\nM a[228:231],a[4:7],a[40:43],a[228:231]\nbuffer_load_dwordx4 v19,s[28:31],s46 offen lds\nM v[164:167],a[4:7],a[36:39],v[164:167]\ns_add_u32 m0,s50,28672\n" \
"ds_read_b64_tr_b16 a[76:77],v11 offset:26112\nM v[132:135],a[4:7],a[32:35],v[132:135]\nds_read_b64_tr_b16 a[78:79],v12 offset:26112\nM v[136:139],a[8:11],a[32:35],v[136:139]\ns_nop 0\nds_read_b128 a[88:91],v6 offset:40960\nM v[168:171],a[8:11],a[36:39],v[168:171]\nbuffer_load_dwordx4 v20,s[28:31],s46 offen lds\n" \
"M a[232:235],a[8:11],a[40:43],a[232:235]\ns_add_u32 m0,s51,0\nds_read_b128 a[92:95],v6 offset:45056\nM a[200:203],a[8:11],a[44:47],a[200:203]\ns_mov_b32 s46,1792\nM a[204:207],a[12:15],a[44:47],a[204:207]\nM a[236:239],a[12:15],a[40:43],a[236:239]\nbuffer_load_dwordx4 v21,s[32:35],s47 offen offset:0 lds\n" \
"M v[172:175],a[12:15],a[36:39],v[172:175]\ns_add_u32 m0,s51,896\nM v[140:143],a[12:15],a[32:35],v[140:143]\nM v[144:147],a[16:19],a[32:35],v[144:147]\nM v[176:179],a[16:19],a[36:39],v[176:179]\nbuffer_load_dwordx4 v21,s[32:35],s47 offen offset:128 lds\nM a[240:243],a[16:19],a[40:43],a[240:243]\ns_add_u32 m0,s51,8192\n" \
"M a[208:211],a[16:19],a[44:47],a[208:211]\ns_mov_b32 s47,7602176\nM a[212:215],a[20:23],a[44:47],a[212:215]\nM a[244:247],a[20:23],a[40:43],a[244:247]\nbuffer_load_dwordx4 v21,s[32:35],s47 offen offset:0 lds\nM v[180:183],a[20:23],a[36:39],v[180:183]\ns_add_u32 m0,s51,9088\nM v[148:151],a[20:23],a[32:35],v[148:151]\n" \
"M v[152:155],a[24:27],a[32:35],v[152:155]\ns_waitcnt vmcnt(16) lgkmcnt(0)\nM v[184:187],a[24:27],a[36:39],v[184:187]\nbuffer_store_dwordx4 v[120:123],v4,s[36:39],s96 offen offset:0 sc1\ns_barrier\nM a[248:251],a[24:27],a[40:43],a[248:251]\nds_read_b64_tr_b16 a[0:1],v9 offset:32768\nds_read_b64_tr_b16 a[2:3],v10 offset:32768\n" \
"M a[216:219],a[24:27],a[44:47],a[216:219]\nds_read_b64_tr_b16 a[4:5],v11 offset:32768\nM a[220:223],a[28:31],a[44:47],a[220:223]\nds_read_b128 a[44:47],v7 offset:12288\nM a[252:255],a[28:31],a[40:43],a[252:255]\nbuffer_load_dwordx4 v21,s[32:35],s47 offen offset:128 lds\nM v[188:191],a[28:31],a[36:39],v[188:191]\ns_add_u32 m0,s50,32768\n" \
"ds_read_b128 a[36:39],v7 offset:4096\nM v[156:159],a[28:31],a[32:35],v[156:159]\ns_mov_b32 s47,7864320\nds_read_b128 a[32:35],v7 offset:0\nM v[128:131],a[48:51],a[80:83],v[128:131]\nds_read_b128 a[40:43],v7 offset:8192\nM v[160:163],a[48:51],a[84:87],v[160:163]\nbuffer_load_dwordx4 v13,s[28:31],s46 offen lds\n" \
"M a[224:227],a[48:51],a[88:91],a[224:227]\ns_add_u32 m0,s50,36864\nM a[192:195],a[48:51],a[92:95],a[192:195]\nds_read_b64_tr_b16 a[6:7],v12 offset:32768\nds_read_b64_tr_b16 a[8:9],v9 offset:33280\nM a[196:199],a[52:55],a[92:95],a[196:199]\nds_read_b64_tr_b16 a[10:11],v10 offset:33280\nM a[228:231],a[52:55],a[88:91],a[228:231]\n" \
"buffer_load_dwordx4 v14,s[28:31],s46 offen lds\nM v[164:167],a[52:55],a[84:87],v[164:167]\ns_add_u32 m0,s50,40960\nds_read_b64_tr_b16 a[12:13],v11 offset:33280\nM v[132:135],a[52:55],a[80:83],v[132:135]\nds_read_b64_tr_b16 a[14:15],v12 offset:33280\nM v[136:139],a[56:59],a[80:83],v[136:139]\nds_read_b64_tr_b16 a[16:17],v9 offset:33792\n" \
"M v[168:171],a[56:59],a[84:87],v[168:171]\nbuffer_load_dwordx4 v15,s[28:31],s46 offen lds\nM a[232:235],a[56:59],a[88:91],a[232:235]\ns_add_u32 m0,s50,45056\nds_read_b64_tr_b16 a[18:19],v10 offset:33792\nM a[200:203],a[56:59],a[92:95],a[200:203]\nds_read_b64_tr_b16 a[20:21],v11 offset:33792\nM a[204:207],a[60:63],a[92:95],a[204:207]\n" \
"ds_read_b64_tr_b16 a[22:23],v12 offset:33792\nds_read_b64_tr_b16 a[24:25],v9 offset:34304\nM a[236:239],a[60:63],a[88:91],a[236:239]\nbuffer_load_dwordx4 v16,s[28:31],s46 offen lds\nM v[172:175],a[60:63],a[84:87],v[172:175]\ns_add_u32 m0,s50,49152\nM v[140:143],a[60:63],a[80:83],v[140:143]\ns_mov_b32 s46,1792\n" \
"ds_read_b64_tr_b16 a[26:27],v10 offset:34304\nds_read_b64_tr_b16 a[28:29],v11 offset:34304\nM v[144:147],a[64:67],a[80:83],v[144:147]\nds_read_b64_tr_b16 a[30:31],v12 offset:34304\nM v[176:179],a[64:67],a[84:87],v[176:179]\nbuffer_load_dwordx4 v17,s[28:31],s46 offen lds\nM a[240:243],a[64:67],a[88:91],a[240:243]\ns_add_u32 m0,s50,53248\n" \
"ds_read_b64_tr_b16 a[48:49],v9 offset:40960\nM a[208:211],a[64:67],a[92:95],a[208:211]\nds_read_b64_tr_b16 a[50:51],v10 offset:40960\nM a[212:215],a[68:71],a[92:95],a[212:215]\nds_read_b64_tr_b16 a[52:53],v11 offset:40960\nM a[244:247],a[68:71],a[88:91],a[244:247]\nbuffer_load_dwordx4 v18,s[28:31],s46 offen lds\nM v[180:183],a[68:71],a[84:87],v[180:183]\n" \
"s_add_u32 m0,s50,57344\nds_read_b64_tr_b16 a[54:55],v12 offset:40960\nM v[148:151],a[68:71],a[80:83],v[148:151]\nds_read_b64_tr_b16 a[56:57],v9 offset:41472\nM v[152:155],a[72:75],a[80:83],v[152:155]\nds_read_b64_tr_b16 a[58:59],v10 offset:41472\nds_read_b64_tr_b16 a[60:61],v11 offset:41472\nM v[184:187],a[72:75],a[84:87],v[184:187]\n" \
"buffer_load_dwordx4 v19,s[28:31],s46 offen lds\nM a[248:251],a[72:75],a[88:91],a[248:251]\ns_add_u32 m0,s50,61440\nM a[216:219],a[72:75],a[92:95],a[216:219]\nds_read_b64_tr_b16 a[62:63],v12 offset:41472\nds_read_b64_tr_b16 a[64:65],v9 offset:41984\nM a[220:223],a[76:79],a[92:95],a[220:223]\ns_waitcnt lgkmcnt(5)\n" \
"ds_read_b64_tr_b16 a[66:67],v10 offset:41984\nM a[252:255],a[76:79],a[88:91],a[252:255]\nbuffer_load_dwordx4 v20,s[28:31],s46 offen lds\nM v[188:191],a[76:79],a[84:87],v[188:191]\ns_add_u32 m0,s51,16384\nds_read_b128 a[84:87],v8 offset:4096\nM v[156:159],a[76:79],a[80:83],v[156:159]\ns_mov_b32 s46,1920\n" \
"ds_read_b128 a[80:83],v8 offset:0\nM v[128:131],a[0:3],a[32:35],v[128:131]\nds_read_b64_tr_b16 a[68:69],v11 offset:41984\nM v[160:163],a[0:3],a[36:39],v[160:163]\nbuffer_load_dwordx4 v21,s[32:35],s47 offen offset:0 lds\nM a[224:227],a[0:3],a[40:43],a[224:227]\ns_add_u32 m0,s51,17280\nM a[192:195],a[0:3],a[44:47],a[192:195]\n" \
"ds_read_b64_tr_b16 a[70:71],v12 offset:41984\nds_read_b64_tr_b16 a[72:73],v9 offset:42496\nM a[196:199],a[4:7],a[44:47],a[196:199]\nds_read_b64_tr_b16 a[74:75],v10 offset:42496\nM a[228:231],a[4:7],a[40:43],a[228:231]\nbuffer_load_dwordx4 v21,s[32:35],s47 offen offset:128 lds\nM v[164:167],a[4:7],a[36:39],v[164:167]\ns_add_u32 m0,s51,24576\n" \
"ds_read_b64_tr_b16 a[76:77],v11 offset:42496\nM v[132:135],a[4:7],a[32:35],v[132:135]\ns_mov_b32 s47,8126464\nds_read_b64_tr_b16 a[78:79],v12 offset:42496\nM v[136:139],a[8:11],a[32:35],v[136:139]\nds_read_b128 a[88:91],v8 offset:8192\nM v[168:171],a[8:11],a[36:39],v[168:171]\nbuffer_load_dwordx4 v21,s[32:35],s47 offen offset:0 lds\n" \
"M a[232:235],a[8:11],a[40:43],a[232:235]\ns_add_u32 m0,s51,25472\nds_read_b128 a[92:95],v8 offset:12288\nM a[200:203],a[8:11],a[44:47],a[200:203]\nM a[204:207],a[12:15],a[44:47],a[204:207]\nM a[236:239],a[12:15],a[40:43],a[236:239]\nM v[172:175],a[12:15],a[36:39],v[172:175]\nbuffer_load_dwordx4 v21,s[32:35],s47 offen offset:128 lds\n" \
"M v[140:143],a[12:15],a[32:35],v[140:143]\ns_add_u32 m0,s50,65536\nv_add_u32 v21,s63,v4\nM v[144:147],a[16:19],a[32:35],v[144:147]\ns_add_u32 s63,s22,16384\nv_add_u32 v62,s63,v4\nM v[176:179],a[16:19],a[36:39],v[176:179]\ns_cmp_lt_u32 %2,2\n" \
"s_cselect_b64 s[62:63],-1,0\nM a[240:243],a[16:19],a[40:43],a[240:243]\nbuffer_store_dwordx4 v[124:127],v4,s[36:39],s96 offen offset:1024 sc1\nM a[208:211],a[16:19],a[44:47],a[208:211]\nM a[212:215],a[20:23],a[44:47],a[212:215]\nM a[244:247],a[20:23],a[40:43],a[244:247]\nM v[180:183],a[20:23],a[36:39],v[180:183]\nM v[148:151],a[20:23],a[32:35],v[148:151]\n" \
"M v[152:155],a[24:27],a[32:35],v[152:155]\ns_waitcnt vmcnt(18) lgkmcnt(0)\nM v[184:187],a[24:27],a[36:39],v[184:187]\ns_barrier\nds_read_b64_tr_b16 a[0:1],v9 offset:49152\nM a[248:251],a[24:27],a[40:43],a[248:251]\nbuffer_load_dwordx4 v13,s[28:31],s46 offen lds\nM a[216:219],a[24:27],a[44:47],a[216:219]\n" \
"s_add_u32 m0,s50,69632\nds_read_b64_tr_b16 a[2:3],v10 offset:49152\nv_add_u32 v13,s54,v4\nM a[220:223],a[28:31],a[44:47],a[220:223]\nds_read_b128 a[44:47],v5 offset:12288\nM a[252:255],a[28:31],a[40:43],a[252:255]\nds_read_b128 a[40:43],v5 offset:8192\nM v[188:191],a[28:31],a[36:39],v[188:191]\n" \
"buffer_load_dwordx4 v14,s[28:31],s46 offen lds\nM v[156:159],a[28:31],a[32:35],v[156:159]\ns_add_u32 m0,s50,73728\nds_read_b128 a[32:35],v5 offset:0\nv_add_u32 v14,0x10000,v13\nM v[128:131],a[48:51],a[80:83],v[128:131]\nds_read_b128 a[36:39],v5 offset:4096\nM v[160:163],a[48:51],a[84:87],v[160:163]\n" \
"ds_read_b64_tr_b16 a[4:5],v11 offset:49152\nM a[224:227],a[48:51],a[88:91],a[224:227]\nbuffer_load_dwordx4 v15,s[28:31],s46 offen lds\nM a[192:195],a[48:51],a[92:95],a[192:195]\ns_add_u32 m0,s50,77824\nds_read_b64_tr_b16 a[6:7],v12 offset:49152\nM a[196:199],a[52:55],a[92:95],a[196:199]\nds_read_b64_tr_b16 a[8:9],v9 offset:49664\n" \
"M a[228:231],a[52:55],a[88:91],a[228:231]\nds_read_b64_tr_b16 a[10:11],v10 offset:49664\nM v[164:167],a[52:55],a[84:87],v[164:167]\nbuffer_load_dwordx4 v16,s[28:31],s46 offen lds\nM v[132:135],a[52:55],a[80:83],v[132:135]\ns_add_u32 m0,s50,81920\nds_read_b64_tr_b16 a[12:13],v11 offset:49664\nM v[136:139],a[56:59],a[80:83],v[136:139]\n" \
"s_mov_b32 s46,1920\nds_read_b64_tr_b16 a[14:15],v12 offset:49664\nM v[168:171],a[56:59],a[84:87],v[168:171]\nds_read_b64_tr_b16 a[16:17],v9 offset:50176\nds_read_b64_tr_b16 a[18:19],v10 offset:50176\nM a[232:235],a[56:59],a[88:91],a[232:235]\nbuffer_store_dwordx4 v[72:75],v4,s[36:39],s96 offen offset:2048 sc1\nM a[200:203],a[56:59],a[92:95],a[200:203]\n" \
"ds_read_b64_tr_b16 a[20:21],v11 offset:50176\nds_read_b64_tr_b16 a[22:23],v12 offset:50176\nM a[204:207],a[60:63],a[92:95],a[204:207]\nds_read_b64_tr_b16 a[24:25],v9 offset:50688\nM a[236:239],a[60:63],a[88:91],a[236:239]\nds_read_b64_tr_b16 a[26:27],v10 offset:50688\nds_read_b64_tr_b16 a[28:29],v11 offset:50688\nM v[172:175],a[60:63],a[84:87],v[172:175]\n" \
"buffer_load_dwordx4 v17,s[28:31],s46 offen lds\nM v[140:143],a[60:63],a[80:83],v[140:143]\ns_add_u32 m0,s50,86016\nM v[144:147],a[64:67],a[80:83],v[144:147]\nds_read_b64_tr_b16 a[30:31],v12 offset:50688\nds_read_b64_tr_b16 a[48:49],v9 offset:57344\nM v[176:179],a[64:67],a[84:87],v[176:179]\nds_read_b64_tr_b16 a[50:51],v10 offset:57344\n" \
"M a[240:243],a[64:67],a[88:91],a[240:243]\nbuffer_load_dwordx4 v18,s[28:31],s46 offen lds\nM a[208:211],a[64:67],a[92:95],a[208:211]\ns_add_u32 m0,s50,90112\nds_read_b64_tr_b16 a[52:53],v11 offset:57344\nM a[212:215],a[68:71],a[92:95],a[212:215]\nds_read_b64_tr_b16 a[54:55],v12 offset:57344\nM a[244:247],a[68:71],a[88:91],a[244:247]\n" \
"ds_read_b64_tr_b16 a[56:57],v9 offset:57856\nM v[180:183],a[68:71],a[84:87],v[180:183]\nbuffer_load_dwordx4 v19,s[28:31],s46 offen lds\nM v[148:151],a[68:71],a[80:83],v[148:151]\ns_add_u32 m0,s50,94208\nds_read_b64_tr_b16 a[58:59],v10 offset:57856\nM v[152:155],a[72:75],a[80:83],v[152:155]\nds_read_b64_tr_b16 a[60:61],v11 offset:57856\n" \
"M v[184:187],a[72:75],a[84:87],v[184:187]\nds_read_b64_tr_b16 a[62:63],v12 offset:57856\nds_read_b64_tr_b16 a[64:65],v9 offset:58368\nM a[248:251],a[72:75],a[88:91],a[248:251]\nbuffer_load_dwordx4 v20,s[28:31],s46 offen lds\nM a[216:219],a[72:75],a[92:95],a[216:219]\ns_add_u32 m0,s22,49152\nv_add_u32 v20,12288,v21\n" \
"v_cndmask_b32 v20,v20,v62,s[62:63]\nM a[220:223],a[76:79],a[92:95],a[220:223]\ns_lshr_b32 s62,%1,3\nds_read_b64_tr_b16 a[66:67],v10 offset:58368\nds_read_b64_tr_b16 a[68:69],v11 offset:58368\nM a[252:255],a[76:79],a[88:91],a[252:255]\ns_lshl_b32 s62,s62,2\nds_read_b64_tr_b16 a[70:71],v12 offset:58368\n" \
"M v[188:191],a[76:79],a[84:87],v[188:191]\nbuffer_store_dwordx4 v[76:79],v4,s[36:39],s96 offen offset:3072 sc1\nM v[156:159],a[76:79],a[80:83],v[156:159]\ns_waitcnt lgkmcnt(3)\ns_add_u32 s62,s62,%2\nds_read_b128 a[80:83],v6 offset:0\nds_read_b128 a[84:87],v6 offset:4096\nM v[128:131],a[0:3],a[32:35],v[128:131]\n" \
"s_lshr_b32 s63,%1,5\nds_read_b64_tr_b16 a[72:73],v9 offset:58880\nM v[160:163],a[0:3],a[36:39],v[160:163]\ns_lshl_b32 s62,s62,14\nds_read_b64_tr_b16 a[74:75],v10 offset:58880\nM a[224:227],a[0:3],a[40:43],a[224:227]\ns_lshl_b32 s63,s63,8\nds_read_b64_tr_b16 a[76:77],v11 offset:58880\n" \
"M a[192:195],a[0:3],a[44:47],a[192:195]\ns_sub_u32 s62,s62,s63\nds_read_b64_tr_b16 a[78:79],v12 offset:58880\nds_read_b128 a[88:91],v6 offset:8192\nM a[196:199],a[4:7],a[44:47],a[196:199]\ns_mov_b32 s46,0\nds_read_b128 a[92:95],v6 offset:12288\nM a[228:231],a[4:7],a[40:43],a[228:231]\n" \
"M v[164:167],a[4:7],a[36:39],v[164:167]\nM v[132:135],a[4:7],a[32:35],v[132:135]\nM v[136:139],a[8:11],a[32:35],v[136:139]\nM v[168:171],a[8:11],a[36:39],v[168:171]\nM a[232:235],a[8:11],a[40:43],a[232:235]\nM a[200:203],a[8:11],a[44:47],a[200:203]\nM a[204:207],a[12:15],a[44:47],a[204:207]\nM a[236:239],a[12:15],a[40:43],a[236:239]\n" \
"M v[172:175],a[12:15],a[36:39],v[172:175]\nM v[140:143],a[12:15],a[32:35],v[140:143]\nM v[144:147],a[16:19],a[32:35],v[144:147]\nM v[176:179],a[16:19],a[36:39],v[176:179]\nM a[240:243],a[16:19],a[40:43],a[240:243]\nM a[208:211],a[16:19],a[44:47],a[208:211]\nM a[212:215],a[20:23],a[44:47],a[212:215]\nM a[244:247],a[20:23],a[40:43],a[244:247]\n" \
"M v[180:183],a[20:23],a[36:39],v[180:183]\nM v[148:151],a[20:23],a[32:35],v[148:151]\nM v[152:155],a[24:27],a[32:35],v[152:155]\ns_waitcnt vmcnt(15) lgkmcnt(0)\nM v[184:187],a[24:27],a[36:39],v[184:187]\ns_barrier\nds_read_b64_tr_b16 a[0:1],v9 offset:0\nM a[248:251],a[24:27],a[40:43],a[248:251]\n" \
"buffer_load_dwordx4 v22,s[40:43],s61 offen offset:0 sc1 lds\nM a[216:219],a[24:27],a[44:47],a[216:219]\nds_read_b64_tr_b16 a[2:3],v10 offset:0\nM a[220:223],a[28:31],a[44:47],a[220:223]\nds_read_b64_tr_b16 a[4:5],v11 offset:0\nds_read_b64_tr_b16 a[6:7],v12 offset:0\nM a[252:255],a[28:31],a[40:43],a[252:255]\nds_read_b64_tr_b16 a[8:9],v9 offset:512\n" \
"M v[188:191],a[28:31],a[36:39],v[188:191]\nbuffer_load_dwordx4 v22,s[40:43],s61 offen offset:1024 sc1 lds\nM v[156:159],a[28:31],a[32:35],v[156:159]\nds_read_b128 a[32:35],v5 offset:45056\nM v[128:131],a[48:51],a[80:83],v[128:131]\nds_read_b64_tr_b16 a[10:11],v10 offset:512\nM v[160:163],a[48:51],a[84:87],v[160:163]\nds_read_b64_tr_b16 a[12:13],v11 offset:512\n" \
"M a[224:227],a[48:51],a[88:91],a[224:227]\nbuffer_load_dwordx4 v22,s[40:43],s61 offen offset:2048 sc1 lds\nM a[192:195],a[48:51],a[92:95],a[192:195]\nds_read_b64_tr_b16 a[14:15],v12 offset:512\nM a[196:199],a[52:55],a[92:95],a[196:199]\nds_read_b64_tr_b16 a[16:17],v9 offset:1024\nM a[228:231],a[52:55],a[88:91],a[228:231]\nds_read_b64_tr_b16 a[18:19],v10 offset:1024\n" \
"M v[164:167],a[52:55],a[84:87],v[164:167]\nbuffer_load_dwordx4 v22,s[40:43],s61 offen offset:3072 sc1 lds\nM v[132:135],a[52:55],a[80:83],v[132:135]\ns_add_u32 m0,s22,81920\nds_read_b64_tr_b16 a[20:21],v11 offset:1024\nM v[136:139],a[56:59],a[80:83],v[136:139]\ns_add_u32 s61,s44,40960\nds_read_b64_tr_b16 a[22:23],v12 offset:1024\n" \
"M v[168:171],a[56:59],a[84:87],v[168:171]\nds_read_b64_tr_b16 a[24:25],v9 offset:1536\nds_read_b64_tr_b16 a[26:27],v10 offset:1536\nM a[232:235],a[56:59],a[88:91],a[232:235]\nbuffer_load_dwordx4 v22,s[40:43],s61 offen offset:0 sc1 lds\nM a[200:203],a[56:59],a[92:95],a[200:203]\nM a[204:207],a[60:63],a[92:95],a[204:207]\nds_read_b64_tr_b16 a[28:29],v11 offset:1536\n" \
"ds_read_b64_tr_b16 a[30:31],v12 offset:1536\nM a[236:239],a[60:63],a[88:91],a[236:239]\nds_read_b128 a[44:47],v6 offset:45056\nM v[172:175],a[60:63],a[84:87],v[172:175]\nbuffer_load_dwordx4 v22,s[40:43],s61 offen offset:1024 sc1 lds\nM v[140:143],a[60:63],a[80:83],v[140:143]\nM v[144:147],a[64:67],a[80:83],v[144:147]\nds_read_b64_tr_b16 a[48:49],v9 offset:8192\n" \
"ds_read_b64_tr_b16 a[50:51],v10 offset:8192\nM v[176:179],a[64:67],a[84:87],v[176:179]\nds_read_b64_tr_b16 a[52:53],v11 offset:8192\nM a[240:243],a[64:67],a[88:91],a[240:243]\ns_waitcnt vmcnt(7)\nds_read_b64_tr_b16 a[54:55],v12 offset:8192\nM a[208:211],a[64:67],a[92:95],a[208:211]\ns_barrier\n" \
"ds_read_b128 a[36:39],v7 offset:12288\nM a[212:215],a[68:71],a[92:95],a[212:215]\nbuffer_load_dwordx4 v22,s[40:43],s61 offen offset:2048 sc1 lds\nM a[244:247],a[68:71],a[88:91],a[244:247]\nds_read_b64_tr_b16 a[96:97],v9 offset:16384\nM v[180:183],a[68:71],a[84:87],v[180:183]\nds_read_b64_tr_b16 a[98:99],v10 offset:16384\nM v[148:151],a[68:71],a[80:83],v[148:151]\n" \
"ds_read_b64_tr_b16 a[100:101],v11 offset:16384\nds_read_b64_tr_b16 a[102:103],v12 offset:16384\nM v[152:155],a[72:75],a[80:83],v[152:155]\nds_read_b64_tr_b16 a[104:105],v9 offset:16896\nM v[184:187],a[72:75],a[84:87],v[184:187]\nds_read_b64_tr_b16 a[106:107],v10 offset:16896\nM a[248:251],a[72:75],a[88:91],a[248:251]\nds_read_b64_tr_b16 a[108:109],v11 offset:16896\n" \
"ds_read_b64_tr_b16 a[110:111],v12 offset:16896\nM a[216:219],a[72:75],a[92:95],a[216:219]\nds_read_b64_tr_b16 a[112:113],v9 offset:17408\nM a[220:223],a[76:79],a[92:95],a[220:223]\nds_read_b64_tr_b16 a[114:115],v10 offset:17408\nM a[252:255],a[76:79],a[88:91],a[252:255]\nds_read_b64_tr_b16 a[116:117],v11 offset:17408\nds_read_b64_tr_b16 a[118:119],v12 offset:17408\n" \
"M v[188:191],a[76:79],a[84:87],v[188:191]\nds_read_b64_tr_b16 a[120:121],v9 offset:17920\nM v[156:159],a[76:79],a[80:83],v[156:159]\ns_waitcnt lgkmcnt(7)\nds_read_b64_tr_b16 a[122:123],v10 offset:17920\nM a[192:195],a[0:3],a[32:35],a[192:195]\nds_read_b64_tr_b16 a[124:125],v11 offset:17920\nds_read_b64_tr_b16 a[126:127],v12 offset:17920\n" \
"M a[196:199],a[4:7],a[32:35],a[196:199]\nds_read_b128 a[40:43],v8 offset:12288\nM a[200:203],a[8:11],a[32:35],a[200:203]\nds_read_b64_tr_b16 a[128:129],v9 offset:24576\nM a[204:207],a[12:15],a[32:35],a[204:207]\nds_read_b64_tr_b16 a[130:131],v10 offset:24576\nM a[208:211],a[16:19],a[32:35],a[208:211]\nds_read_b64_tr_b16 a[132:133],v11 offset:24576\n" \
"ds_read_b64_tr_b16 a[134:135],v12 offset:24576\nM a[212:215],a[20:23],a[32:35],a[212:215]\nds_read_b64_tr_b16 a[136:137],v9 offset:25088\nM a[216:219],a[24:27],a[32:35],a[216:219]\nds_read_b64_tr_b16 a[138:139],v10 offset:25088\nM a[220:223],a[28:31],a[32:35],a[220:223]\nds_read_b64_tr_b16 a[140:141],v11 offset:25088\nds_read_b64_tr_b16 a[142:143],v12 offset:25088\n" \
"M a[192:195],a[96:99],a[36:39],a[192:195]\nds_read_b64_tr_b16 a[144:145],v9 offset:25600\nM a[196:199],a[100:103],a[36:39],a[196:199]\nds_read_b64_tr_b16 a[146:147],v10 offset:25600\nM a[200:203],a[104:107],a[36:39],a[200:203]\ns_waitcnt lgkmcnt(6)\nds_read_b64_tr_b16 a[148:149],v11 offset:25600\nds_read_b64_tr_b16 a[150:151],v12 offset:25600\n" \
"M a[204:207],a[108:111],a[36:39],a[204:207]\nds_read_b64_tr_b16 a[152:153],v9 offset:26112\nds_read_b64_tr_b16 a[154:155],v10 offset:26112\nM a[208:211],a[112:115],a[36:39],a[208:211]\nds_read_b64_tr_b16 a[156:157],v11 offset:26112\nds_read_b64_tr_b16 a[158:159],v12 offset:26112\nM a[212:215],a[116:119],a[36:39],a[212:215]\nbuffer_load_dwordx4 v22,s[40:43],s61 offen offset:3072 sc1 lds\n" \
"M a[216:219],a[120:123],a[36:39],a[216:219]\ns_add_u32 m0,s22,98304\nM a[220:223],a[124:127],a[36:39],a[220:223]\ns_add_u32 s61,s44,16384\nds_read_b64_tr_b16 a[56:57],v9 offset:8704\nds_read_b64_tr_b16 a[58:59],v10 offset:8704\nM a[192:195],a[128:131],a[40:43],a[192:195]\nds_read_b64_tr_b16 a[60:61],v11 offset:8704\n" \
"ds_read_b64_tr_b16 a[62:63],v12 offset:8704\nM a[196:199],a[132:135],a[40:43],a[196:199]\ns_waitcnt lgkmcnt(6)\nds_read_b64_tr_b16 a[64:65],v9 offset:9216\nds_read_b64_tr_b16 a[66:67],v10 offset:9216\nM a[200:203],a[136:139],a[40:43],a[200:203]\nds_read_b128 a[80:83],v5 offset:40960\nds_read_b128 v[240:243],v5 offset:36864\n" \
"ds_read_b128 v[224:227],v5 offset:32768\nds_read_b128 a[84:87],v7 offset:8192\nds_read_b128 v[244:247],v7 offset:4096\nds_read_b128 v[228:231],v7 offset:0\nds_read_b128 a[88:91],v8 offset:8192\nds_read_b128 v[248:251],v8 offset:4096\nds_read_b128 v[232:235],v8 offset:0\nds_read_b64_tr_b16 a[68:69],v11 offset:9216\n" \
"ds_read_b64_tr_b16 a[70:71],v12 offset:9216\nds_read_b64_tr_b16 a[72:73],v9 offset:9728\nds_read_b64_tr_b16 a[74:75],v10 offset:9728\nds_read_b64_tr_b16 a[76:77],v11 offset:9728\nds_read_b64_tr_b16 a[78:79],v12 offset:9728\nds_read_b128 a[92:95],v6 offset:40960\nds_read_b128 v[252:255],v6 offset:36864\nds_read_b128 v[236:239],v6 offset:32768\n" \
"M a[204:207],a[140:143],a[40:43],a[204:207]\nM a[208:211],a[144:147],a[40:43],a[208:211]\ns_mov_b64 exec,0\ns_cmp_eq_u32 %2,0\ns_cbranch_scc1 LG27_%=\ns_cmp_eq_u32 %2,1\ns_cbranch_scc1 LG28_%=\ns_cmp_eq_u32 %2,2\n" \
"s_cbranch_scc1 LG14_%=\ns_cmp_eq_u32 %2,3\ns_cbranch_scc1 LG15_%=\nLHR1_%=:\ns_mov_b64 exec,-1\nM a[212:215],a[148:151],a[40:43],a[212:215]\nds_write_b128 v1,v[88:91] offset:0\nM a[216:219],a[152:155],a[40:43],a[216:219]\n" \
"s_waitcnt lgkmcnt(15)\nM a[220:223],a[156:159],a[40:43],a[220:223]\nM a[192:195],a[48:51],a[44:47],a[192:195]\ns_waitcnt vmcnt(8) lgkmcnt(1)\nM a[196:199],a[52:55],a[44:47],a[196:199]\ns_barrier\nM a[200:203],a[56:59],a[44:47],a[200:203]\nbuffer_store_dwordx2 v[58:59],v56,s[36:39],s24 offen sc1\n" \
"M a[204:207],a[60:63],a[44:47],a[204:207]\nds_write_b128 v1,v[92:95] offset:1024\nM a[208:211],a[64:67],a[44:47],a[208:211]\nbuffer_load_dwordx4 v22,s[40:43],s61 offen offset:0 sc1 lds\nM a[212:215],a[68:71],a[44:47],a[212:215]\nv_accvgpr_read_b32 v5,a192\nv_accvgpr_read_b32 v10,a193\nv_accvgpr_read_b32 v11,a194\n" \
"M a[216:219],a[72:75],a[44:47],a[216:219]\nv_accvgpr_read_b32 v12,a195\nC v6,v5,v10\nC v7,v11,v12\nM a[220:223],a[76:79],a[44:47],a[220:223]\nds_write_b128 v1,v[104:107] offset:2048\nv_accvgpr_read_b32 v5,a196\nM a[224:227],a[0:3],a[80:83],a[224:227]\n" \
"buffer_load_dwordx4 v22,s[40:43],s61 offen offset:1024 sc1 lds\nv_accvgpr_read_b32 v10,a197\nM a[224:227],a[96:99],a[84:87],a[224:227]\nv_accvgpr_read_b32 v11,a198\nv_accvgpr_read_b32 v12,a199\nC v8,v5,v10\nM a[224:227],a[128:131],a[88:91],a[224:227]\nC v9,v11,v12\n" \
"v_accvgpr_read_b32 v32,a200\nv_accvgpr_read_b32 v33,a201\nM a[224:227],a[48:51],a[92:95],a[224:227]\nds_write_b128 v20,v[6:9] offset:0\nv_accvgpr_read_b32 v34,a202\nM a[228:231],a[52:55],a[92:95],a[228:231]\nbuffer_load_dwordx4 v22,s[40:43],s61 offen offset:2048 sc1 lds\nv_accvgpr_read_b32 v35,a203\n" \
"M a[228:231],a[132:135],a[88:91],a[228:231]\nC v36,v32,v33\nC v37,v34,v35\nv_accvgpr_read_b32 v32,a204\nM a[228:231],a[100:103],a[84:87],a[228:231]\nv_accvgpr_read_b32 v33,a205\nv_accvgpr_read_b32 v34,a206\nv_accvgpr_read_b32 v35,a207\n" \
"M a[228:231],a[4:7],a[80:83],a[228:231]\nLG14_%=:\ns_cbranch_execz LHR1_%=\nC v38,v32,v33\nC v39,v34,v35\nv_accvgpr_read_b32 v5,a208\nM a[232:235],a[8:11],a[80:83],a[232:235]\nbuffer_load_dwordx4 v22,s[40:43],s61 offen offset:3072 sc1 lds\n" \
"v_accvgpr_read_b32 v10,a209\nM a[232:235],a[104:107],a[84:87],a[232:235]\ns_add_u32 m0,s22,114688\nv_accvgpr_read_b32 v11,a210\nv_accvgpr_read_b32 v12,a211\nC v6,v5,v10\nM a[232:235],a[136:139],a[88:91],a[232:235]\ns_add_u32 s61,s44,0\n" \
"ds_write_b128 v1,v[108:111] offset:3072\nC v7,v11,v12\nM a[232:235],a[56:59],a[92:95],a[232:235]\nv_accvgpr_read_b32 v5,a212\nv_accvgpr_read_b32 v10,a213\nv_accvgpr_read_b32 v11,a214\nM a[236:239],a[60:63],a[92:95],a[236:239]\nbuffer_load_dwordx4 v22,s[40:43],s61 offen offset:0 sc1 lds\n" \
"v_accvgpr_read_b32 v12,a215\nM a[236:239],a[140:143],a[88:91],a[236:239]\nC v8,v5,v10\nC v9,v11,v12\nv_accvgpr_read_b32 v32,a216\nM a[236:239],a[108:111],a[84:87],a[236:239]\nv_accvgpr_read_b32 v33,a217\nv_accvgpr_read_b32 v34,a218\n" \
"v_accvgpr_read_b32 v35,a219\nM a[236:239],a[12:15],a[80:83],a[236:239]\nds_write_b128 v20,v[36:39] offset:1024\nC v36,v32,v33\nM a[240:243],a[16:19],a[80:83],a[240:243]\nbuffer_load_dwordx4 v22,s[40:43],s61 offen offset:1024 sc1 lds\nC v37,v34,v35\nM a[240:243],a[112:115],a[84:87],a[240:243]\n" \
"v_accvgpr_read_b32 v32,a220\nv_accvgpr_read_b32 v33,a221\nv_accvgpr_read_b32 v34,a222\nM a[240:243],a[144:147],a[88:91],a[240:243]\nv_accvgpr_read_b32 v35,a223\nC v38,v32,v33\nC v39,v34,v35\nM a[240:243],a[64:67],a[92:95],a[240:243]\n" \
"ds_write_b128 v20,v[6:9] offset:2048\nM a[244:247],a[68:71],a[92:95],a[244:247]\nv_accvgpr_read_b32 v5,a224\nv_accvgpr_read_b32 v10,a225\nM a[244:247],a[148:151],a[88:91],a[244:247]\nv_accvgpr_read_b32 v11,a226\nv_accvgpr_read_b32 v12,a227\nM a[244:247],a[116:119],a[84:87],a[244:247]\n" \
"ds_write_b128 v1,v[120:123] offset:4096\nds_write_b128 v1,v[124:127] offset:5120\nds_write_b128 v1,v[72:75] offset:6144\nds_write_b128 v1,v[76:79] offset:7168\nM a[244:247],a[20:23],a[80:83],a[244:247]\nC v6,v5,v10\nC v7,v11,v12\nM a[248:251],a[24:27],a[80:83],a[248:251]\n" \
"v_accvgpr_read_b32 v32,a232\nv_accvgpr_read_b32 v33,a233\nM a[248:251],a[120:123],a[84:87],a[248:251]\nv_accvgpr_read_b32 v34,a234\nv_accvgpr_read_b32 v35,a235\nM a[248:251],a[152:155],a[88:91],a[248:251]\nbuffer_load_dwordx4 v22,s[40:43],s61 offen offset:2048 sc1 lds\nM a[248:251],a[72:75],a[92:95],a[248:251]\n" \
"v_accvgpr_read_b32 v5,a228\nv_accvgpr_read_b32 v10,a229\nM a[252:255],a[76:79],a[92:95],a[252:255]\ns_waitcnt lgkmcnt(0)\nv_accvgpr_read_b32 v11,a230\nv_accvgpr_read_b32 v12,a231\nM a[252:255],a[156:159],a[88:91],a[252:255]\ns_barrier\n" \
"C v8,v5,v10\nM a[252:255],a[124:127],a[84:87],a[252:255]\nds_write_b128 v20,v[36:39] offset:3072\nM a[252:255],a[28:31],a[80:83],a[252:255]\nC v36,v32,v33\nC v37,v34,v35\nM v[160:163],a[0:3],v[240:243],v[160:163]\nbuffer_load_dwordx4 v22,s[40:43],s61 offen offset:3072 sc1 lds\n" \
"M v[160:163],a[96:99],v[244:247],v[160:163]\ns_add_u32 m0,s22,0\nC v9,v11,v12\nv_accvgpr_read_b32 v32,a236\nM v[164:167],a[100:103],v[244:247],v[164:167]\ns_add_u32 s61,s44,24576\nds_read_b128 a[176:179],v20 offset:0\nv_accvgpr_read_b32 v33,a237\n" \
"M v[164:167],a[4:7],v[240:243],v[164:167]\nds_read_b128 a[180:183],v20 offset:1024\nv_accvgpr_read_b32 v34,a238\nM v[168:171],a[8:11],v[240:243],v[168:171]\nds_read_b128 a[184:187],v20 offset:2048\nv_accvgpr_read_b32 v35,a239\nM v[168:171],a[104:107],v[244:247],v[168:171]\nds_read_b128 a[192:195],v13 offset:32768\n" \
"C v38,v32,v33\nM v[172:175],a[108:111],v[244:247],v[172:175]\nds_read_b128 a[196:199],v13 offset:34816\nC v39,v34,v35\nM v[172:175],a[12:15],v[240:243],v[172:175]\nds_read_b128 a[200:203],v13 offset:36864\nv_accvgpr_read_b32 v5,a240\nM v[176:179],a[16:19],v[240:243],v[176:179]\n" \
"ds_read_b128 a[188:191],v20 offset:3072\nds_read_b128 a[204:207],v13 offset:38912\nv_add_u32 v20,s65,v20\nM v[176:179],a[112:115],v[244:247],v[176:179]\nds_read_b128 a[208:211],v13 offset:33792\nv_accvgpr_read_b32 v10,a241\nM v[180:183],a[116:119],v[244:247],v[180:183]\nds_read_b128 a[212:215],v13 offset:35840\n" \
"v_accvgpr_read_b32 v11,a242\nM v[180:183],a[20:23],v[240:243],v[180:183]\nds_read_b128 a[216:219],v13 offset:37888\nv_accvgpr_read_b32 v12,a243\nM v[184:187],a[24:27],v[240:243],v[184:187]\nds_read_b128 a[220:223],v13 offset:39936\nv_accvgpr_read_b32 v32,a248\nM v[184:187],a[120:123],v[244:247],v[184:187]\n" \
"buffer_load_dwordx4 v22,s[40:43],s61 offen offset:0 sc1 lds\nM v[188:191],a[124:127],v[244:247],v[188:191]\nv_accvgpr_read_b32 v33,a249\nv_accvgpr_read_b32 v34,a250\nM v[188:191],a[28:31],v[240:243],v[188:191]\ns_waitcnt lgkmcnt(3)\nv_accvgpr_read_b32 v35,a251\nM v[88:91],a[192:195],a[176:179],v[48:51]\n" \
"ds_write_b128 v20,v[6:9] offset:0\nC v6,v5,v10\nM v[88:91],a[196:199],a[180:183],v[88:91]\nC v7,v11,v12\nv_accvgpr_read_b32 v5,a244\nM v[88:91],a[200:203],a[184:187],v[88:91]\nbuffer_load_dwordx4 v22,s[40:43],s61 offen offset:1024 sc1 lds\nM v[88:91],a[204:207],a[188:191],v[88:91]\n" \
"v_accvgpr_read_b32 v10,a245\nv_accvgpr_read_b32 v11,a246\nM v[92:95],a[208:211],a[176:179],v[52:55]\ns_waitcnt lgkmcnt(1)\nv_accvgpr_read_b32 v12,a247\nC v8,v5,v10\nM v[92:95],a[212:215],a[180:183],v[92:95]\nds_write_b128 v20,v[36:39] offset:1024\n" \
"C v36,v32,v33\nM v[92:95],a[216:219],a[184:187],v[92:95]\nC v37,v34,v35\nC v9,v11,v12\nM v[92:95],a[220:223],a[188:191],v[92:95]\nbuffer_load_dwordx4 v22,s[40:43],s61 offen offset:2048 sc1 lds\nM v[160:163],a[128:131],v[248:251],v[160:163]\nv_accvgpr_read_b32 v32,a252\n" \
"v_accvgpr_read_b32 v33,a253\nM v[160:163],a[48:51],v[252:255],v[160:163]\nv_accvgpr_read_b32 v34,a254\nv_accvgpr_read_b32 v35,a255\nM v[164:167],a[52:55],v[252:255],v[164:167]\nv_max3_f32 v60,v88,v89,v90\nC v38,v32,v33\nM v[164:167],a[132:135],v[248:251],v[164:167]\n" \
"C v39,v34,v35\nv_max3_f32 v61,v91,v92,v93\nM v[168:171],a[136:139],v[248:251],v[168:171]\nds_write_b128 v20,v[6:9] offset:2048\nv_max3_f32 v60,v60,v94,v95\nM v[168:171],a[56:59],v[252:255],v[168:171]\nv_max3_f32 v60,v60,v61,s76\nv_mov_b32 v61,v60\n" \
"M v[172:175],a[60:63],v[252:255],v[172:175]\ns_nop 0\nv_permlane32_swap_b32 v60,v61\nv_max_f32 v60,v60,v61\nM v[172:175],a[140:143],v[248:251],v[172:175]\nv_mov_b32 v61,v60\nC v160,v160,v161\nM v[176:179],a[144:147],v[248:251],v[176:179]\n" \
"s_waitcnt vmcnt(12)\nds_write_b128 v20,v[36:39] offset:3072\nM v[176:179],a[64:67],v[252:255],v[176:179]\ns_barrier\nC v161,v162,v163\nM v[180:183],a[68:71],v[252:255],v[180:183]\nv_permlane16_swap_b32 v60,v61\nv_min_f32 v44,-v60,-v61\n" \
"M v[180:183],a[148:151],v[248:251],v[180:183]\nds_read_b128 a[160:163],v20 offset:0\nv_add_f32 v88,v88,v44\nM v[184:187],a[152:155],v[248:251],v[184:187]\nds_read_b128 a[164:167],v20 offset:1024\nv_exp_f32 v88,v88\nM v[184:187],a[72:75],v[252:255],v[184:187]\nds_read_b128 a[168:171],v20 offset:2048\n" \
"v_add_f32 v89,v89,v44\nM v[188:191],a[76:79],v[252:255],v[188:191]\nds_read_b128 a[172:175],v20 offset:3072\nv_add_f32 v90,v90,v44\nM v[188:191],a[156:159],v[248:251],v[188:191]\nv_exp_f32 v89,v89\nM v[128:131],a[128:131],v[232:235],v[128:131]\nbuffer_load_dwordx4 v22,s[40:43],s61 offen offset:3072 sc1 lds\n" \
"M v[128:131],a[0:3],v[224:227],v[128:131]\ns_add_u32 m0,s22,32768\nds_read_b128 v[192:195],v13 offset:49152\nC v168,v168,v169\nM v[132:135],a[4:7],v[224:227],v[132:135]\ns_add_u32 s61,s44,8192\nds_read_b128 v[196:199],v13 offset:51200\nC v176,v176,v177\n" \
"M v[132:135],a[132:135],v[232:235],v[132:135]\nds_read_b128 v[200:203],v13 offset:53248\nv_add_u32 v20,s65,v20\nM v[136:139],a[136:139],v[232:235],v[136:139]\nds_read_b128 v[204:207],v13 offset:55296\nC v162,v164,v165\nM v[136:139],a[8:11],v[224:227],v[136:139]\nds_read_b128 v[208:211],v13 offset:50176\n" \
"C v163,v166,v167\nM v[140:143],a[12:15],v[224:227],v[140:143]\nds_read_b128 v[212:215],v13 offset:52224\nC v169,v170,v171\nM v[140:143],a[140:143],v[232:235],v[140:143]\nds_read_b128 v[216:219],v13 offset:54272\nv_add_f32 v91,v91,v44\nM v[144:147],a[144:147],v[232:235],v[144:147]\n" \
"v_exp_f32 v90,v90\nM v[144:147],a[16:19],v[224:227],v[144:147]\nds_read_b128 v[220:223],v13 offset:56320\nC v177,v178,v179\nM v[148:151],a[20:23],v[224:227],v[148:151]\nds_write_b128 v20,v[160:163] offset:0\nM v[148:151],a[148:151],v[232:235],v[148:151]\nC v170,v172,v173\n" \
"C v171,v174,v175\nM v[152:155],a[152:155],v[232:235],v[152:155]\nC v178,v180,v181\nC v179,v182,v183\nM v[152:155],a[24:27],v[224:227],v[152:155]\nv_exp_f32 v91,v91\nM v[156:159],a[28:31],v[224:227],v[156:159]\nv_add_f32 v92,v92,v44\n" \
"v_min_f32 v45,-v60,-v61\nM v[156:159],a[156:159],v[232:235],v[156:159]\ns_waitcnt lgkmcnt(1)\nds_write_b128 v20,v[168:171] offset:1024\nM v[112:115],v[192:195],a[160:163],v[48:51]\nC v184,v184,v185\nv_min_f32 v46,-v60,-v61\nM v[112:115],v[196:199],a[164:167],v[112:115]\n" \
"v_min_f32 v47,-v60,-v61\nC v185,v186,v187\nM v[112:115],v[200:203],a[168:171],v[112:115]\nv_exp_f32 v92,v92\nM v[112:115],v[204:207],a[172:175],v[112:115]\nds_write_b128 v20,v[176:179] offset:2048\nM v[116:119],v[208:211],a[160:163],v[52:55]\nv_min_f32 v29,-v60,-v61\n" \
"v_add_f32 v93,v93,v44\nM v[116:119],v[212:215],a[164:167],v[116:119]\nC v186,v188,v189\nC v187,v190,v191\nM v[116:119],v[216:219],a[168:171],v[116:119]\nds_read_b128 a[192:195],v14 offset:0\nv_max3_f32 v60,v112,v113,v114\nM v[116:119],v[220:223],a[172:175],v[116:119]\n" \
"ds_read_b128 a[196:199],v14 offset:1024\nv_add_f32 v94,v94,v44\nM v[120:123],v[192:195],a[176:179],v[44:47]\nv_exp_f32 v93,v93\nM v[120:123],v[196:199],a[180:183],v[120:123]\nds_write_b128 v20,v[184:187] offset:3072\nM v[120:123],v[200:203],a[184:187],v[120:123]\nv_max3_f32 v61,v115,v116,v117\n" \
"v_max3_f32 v60,v60,v118,v119\nM v[120:123],v[204:207],a[188:191],v[120:123]\nv_max3_f32 v60,v60,v61,s76\nv_mov_b32 v61,v60\nM v[124:127],v[208:211],a[176:179],v[44:47]\nds_read_b128 a[200:203],v14 offset:2048\nv_add_f32 v95,v95,v44\nM v[124:127],v[212:215],a[180:183],v[124:127]\n" \
"v_permlane32_swap_b32 v60,v61\nv_max_f32 v60,v60,v61\nM v[124:127],v[216:219],a[184:187],v[124:127]\nds_read_b128 a[204:207],v14 offset:3072\nv_mov_b32 v61,v60\nv_exp_f32 v94,v94\nM v[124:127],v[220:223],a[188:191],v[124:127]\nv_permlane16_swap_b32 v60,v61\n" \
"v_min_f32 v40,-v60,-v61\nM v[128:131],a[96:99],v[228:231],v[128:131]\nv_add_f32 v112,v112,v40\nv_add_f32 v113,v113,v40\nv_add_f32 v114,v114,v40\nM v[128:131],a[48:51],v[236:239],v[128:131]\nv_exp_f32 v112,v112\nv_add_f32 v115,v115,v40\n" \
"M v[132:135],a[52:55],v[236:239],v[132:135]\ns_waitcnt vmcnt(8) lgkmcnt(15)\nv_exp_f32 v113,v113\nv_add_f32 v116,v116,v40\nM v[132:135],a[100:103],v[228:231],v[132:135]\ns_barrier\nv_add_f32 v117,v117,v40\nv_exp_f32 v114,v114\n" \
"M v[136:139],a[104:107],v[228:231],v[136:139]\nbuffer_load_dwordx2 v[58:59],v57,s[36:39],0 offen sc1\nv_add_f32 v118,v118,v40\nM v[136:139],a[56:59],v[236:239],v[136:139]\nv_exp_f32 v115,v115\nv_exp_f32 v95,v95\nM v[140:143],a[60:63],v[236:239],v[140:143]\nds_read_b128 a[208:211],v14 offset:4096\n" \
"v_exp_f32 v116,v116\nM v[140:143],a[108:111],v[228:231],v[140:143]\nds_read_b128 a[212:215],v14 offset:5120\nv_exp_f32 v117,v117\nM v[144:147],a[112:115],v[228:231],v[144:147]\nds_read_b128 a[216:219],v14 offset:6144\nv_add_f32 v119,v119,v40\nv_exp_f32 v118,v118\n" \
"M v[144:147],a[64:67],v[236:239],v[144:147]\nds_read_b128 a[220:223],v14 offset:7168\nC v88,v88,v89\nv_exp_f32 v119,v119\nM v[148:151],a[68:71],v[236:239],v[148:151]\nds_read_b128 a[144:147],v20 offset:0\nC v89,v90,v91\nv_exp_f32 v120,v120\n" \
"M v[148:151],a[116:119],v[228:231],v[148:151]\nv_exp_f32 v121,v121\nv_xor_b32 v13,0x10000,v13\nM v[152:155],a[120:123],v[228:231],v[152:155]\nds_read_b128 a[148:151],v20 offset:1024\nds_read_b128 a[152:155],v20 offset:2048\nv_exp_f32 v122,v122\nM v[152:155],a[72:75],v[236:239],v[152:155]\n" \
"C v128,v128,v129\nC v90,v92,v93\nC v91,v94,v95\nM v[156:159],a[76:79],v[236:239],v[156:159]\nds_read_b128 a[0:3],v13 offset:32768\nds_read_b128 a[156:159],v20 offset:3072\nC v136,v136,v137\nM v[156:159],a[124:127],v[228:231],v[156:159]\n" \
"s_waitcnt lgkmcnt(5)\nds_read_b128 a[4:7],v13 offset:34816\nC v129,v130,v131\nC v144,v144,v145\nM v[224:227],a[192:195],v[88:91],0\nds_read_b128 a[8:11],v13 offset:36864\nC v137,v138,v139\nv_exp_f32 v123,v123\n" \
"M v[228:231],a[196:199],v[88:91],0\nds_read_b128 a[12:15],v13 offset:38912\nv_add_u32 v20,s65,v20\nM v[232:235],a[200:203],v[88:91],0\nds_read_b128 a[16:19],v13 offset:33792\nC v130,v132,v133\nC v131,v134,v135\nM v[236:239],a[204:207],v[88:91],0\n" \
"ds_read_b128 a[20:23],v13 offset:35840\nC v145,v146,v147\nM v[240:243],a[208:211],v[88:91],0\nds_read_b128 a[24:27],v13 offset:37888\nC v138,v140,v141\nM v[244:247],a[212:215],v[88:91],0\nds_read_b128 a[28:31],v13 offset:39936\nC v152,v152,v153\n" \
"M v[248:251],a[216:219],v[88:91],0\ns_waitcnt lgkmcnt(5)\nv_exp_f32 v124,v124\nM v[252:255],a[220:223],v[88:91],0\nds_write_b128 v20,v[128:131] offset:0\ns_waitcnt vmcnt(0)\nv_cmp_gt_u64 s[98:99],s[14:15],v[58:59]\nv_cmp_le_u64 s[18:19],s[16:17],v[58:59]\n" \
"s_or_b64 s[98:99],s[98:99],s[18:19]\ns_cbranch_scc1 LPK_%=\nLPKR_%=:\nv_and_b32 v58,0xf0,v4\nv_lshl_add_u32 v57,v58,3,s62\ns_mov_b32 s62,0x11111111\ns_mov_b32 s63,s62\nv_cndmask_b32 v62,0,v63,s[62:63]\n" \
"v_accvgpr_write_b32 a224,v62\nv_accvgpr_write_b32 a225,v62\nv_accvgpr_write_b32 a226,v62\nv_accvgpr_write_b32 a227,v62\ns_mov_b32 s62,0x22222222\ns_mov_b32 s63,s62\nv_cndmask_b32 v62,0,v63,s[62:63]\nv_accvgpr_write_b32 a228,v62\n" \
"v_accvgpr_write_b32 a229,v62\nv_accvgpr_write_b32 a230,v62\nv_accvgpr_write_b32 a231,v62\ns_mov_b32 s62,0x44444444\ns_mov_b32 s63,s62\nv_cndmask_b32 v62,0,v63,s[62:63]\nv_accvgpr_write_b32 a232,v62\nv_accvgpr_write_b32 a233,v62\n" \
"v_accvgpr_write_b32 a234,v62\nv_accvgpr_write_b32 a235,v62\ns_mov_b32 s62,0x88888888\ns_mov_b32 s63,s62\nv_cndmask_b32 v62,0,v63,s[62:63]\nv_accvgpr_write_b32 a236,v62\nv_accvgpr_write_b32 a237,v62\nv_accvgpr_write_b32 a238,v62\n" \
"v_accvgpr_write_b32 a239,v62\ns_nop 1\nM v[16:19],a[236:239],v[88:91],v[24:27]\ns_waitcnt vmcnt(5) lgkmcnt(13)\nC v112,v112,v113\nC v139,v142,v143\nM v[72:75],a[0:3],a[144:147],v[48:51]\ns_barrier\n" \
"C v146,v148,v149\nM v[72:75],a[4:7],a[148:151],v[72:75]\nC v147,v150,v151\nv_min_f32 v41,-v60,-v61\nM v[72:75],a[8:11],a[152:155],v[72:75]\ns_waitcnt lgkmcnt(1)\nC v153,v154,v155\nv_min_f32 v42,-v60,-v61\n" \
"M v[72:75],a[12:15],a[156:159],v[72:75]\nv_min_f32 v43,-v60,-v61\nv_min_f32 v28,-v60,-v61\nM v[76:79],a[16:19],a[144:147],v[52:55]\nds_write_b128 v20,v[136:139] offset:1024\nM v[76:79],a[20:23],a[148:151],v[76:79]\nbuffer_load_dwordx4 v22,s[40:43],s45 offen offset:0 sc1 lds\nM v[76:79],a[24:27],a[152:155],v[76:79]\n" \
"v_exp_f32 v125,v125\nM v[76:79],a[28:31],a[156:159],v[76:79]\nds_read_b128 a[96:99],v14 offset:16384\nC v113,v114,v115\nC v154,v156,v157\nM v[80:83],a[0:3],a[160:163],v[40:43]\nC v155,v158,v159\nv_max3_f32 v60,v72,v73,v74\n" \
"M v[80:83],a[4:7],a[164:167],v[80:83]\nds_write_b128 v20,v[144:147] offset:2048\nM v[80:83],a[8:11],a[168:171],v[80:83]\nv_exp_f32 v126,v126\nM v[80:83],a[12:15],a[172:175],v[80:83]\nC v114,v116,v117\nC v120,v120,v121\nM v[88:91],a[0:3],a[176:179],v[44:47]\n" \
"ds_read_b128 a[100:103],v14 offset:17408\nC v115,v118,v119\nv_max3_f32 v61,v75,v76,v77\nM v[88:91],a[4:7],a[180:183],v[88:91]\nv_max3_f32 v60,v60,v78,v79\nv_max3_f32 v60,v60,v61,s76\nv_mov_b32 v61,v60\nM v[88:91],a[8:11],a[184:187],v[88:91]\n" \
"ds_write_b128 v20,v[152:155] offset:3072\nC v121,v122,v123\nM v[24:27],a[232:235],v[112:115],v[16:19]\nv_exp_f32 v127,v127\nM v[88:91],a[12:15],a[188:191],v[88:91]\nC v122,v124,v125\nC v123,v126,v127\nM v[84:87],a[16:19],a[160:163],v[40:43]\n" \
"v_permlane32_swap_b32 v60,v61\nv_max_f32 v60,v60,v61\nM v[24:27],a[236:239],v[120:123],v[24:27]\nds_read_b128 a[104:107],v14 offset:18432\nds_read_b128 a[108:111],v14 offset:19456\nv_mov_b32 v61,v60\nM v[84:87],a[20:23],a[164:167],v[84:87]\nbuffer_load_dwordx4 v22,s[40:43],s45 offen offset:1024 sc1 lds\n" \
"M v[84:87],a[24:27],a[168:171],v[84:87]\nv_permlane16_swap_b32 v60,v61\nv_min_f32 v36,-v60,-v61\nM v[84:87],a[28:31],a[172:175],v[84:87]\nv_max3_f32 v15,v24,v25,v26\nv_max_f32 v15,v15,v27\nv_cmp_nge_f32 vcc,0x53800000,v15\nM v[92:95],a[16:19],a[176:179],v[44:47]\n" \
"v_add_f32 v72,v72,v36\nv_add_f32 v73,v73,v36\nv_add_f32 v74,v74,v36\nM v[92:95],a[20:23],a[180:183],v[92:95]\nbuffer_load_dwordx4 v22,s[40:43],s45 offen offset:2048 sc1 lds\nds_read_b128 a[128:131],v20 offset:0\nds_read_b128 a[132:135],v20 offset:1024\nds_read_b128 a[136:139],v20 offset:2048\n" \
"ds_read_b128 a[140:143],v20 offset:3072\nds_read_b128 a[112:115],v14 offset:20480\ns_cbranch_vccnz Lcs1_%=\nLcr2_%=:\nM v[92:95],a[24:27],a[184:187],v[92:95]\nv_exp_f32 v72,v72\nv_add_f32 v75,v75,v36\nM v[92:95],a[28:31],a[188:191],v[92:95]\n" \
"s_waitcnt lgkmcnt(5)\nds_read_b128 a[116:119],v14 offset:21504\nv_add_f32 v76,v76,v36\nv_exp_f32 v73,v73\nM v[192:195],a[96:99],v[112:115],0\nds_read_b128 a[120:123],v14 offset:22528\nds_read_b128 a[124:127],v14 offset:23552\nv_exp_f32 v74,v74\n" \
"M v[224:227],a[96:99],v[120:123],v[224:227]\nbuffer_load_dwordx4 v22,s[40:43],s45 offen offset:3072 sc1 lds\nv_exp_f32 v75,v75\nM v[228:231],a[100:103],v[120:123],v[228:231]\ns_add_u32 s45,s45,s49\ns_add_u32 m0,s22,49152\nv_exp_f32 v76,v76\nv_add_f32 v77,v77,v36\n" \
"M v[196:199],a[100:103],v[112:115],0\ns_waitcnt lgkmcnt(4)\nv_exp_f32 v77,v77\nv_add_f32 v78,v78,v36\nM v[200:203],a[104:107],v[112:115],0\nds_read_b128 a[48:51],v13 offset:49152\nv_exp_f32 v78,v78\nM v[232:235],a[104:107],v[120:123],v[232:235]\n" \
"buffer_load_dwordx4 v22,s[40:43],s45 offen offset:0 sc1 lds\nv_add_f32 v79,v79,v36\nM v[236:239],a[108:111],v[120:123],v[236:239]\ns_waitcnt vmcnt(6) lgkmcnt(1)\nds_read_b128 a[52:55],v13 offset:51200\nv_min_f32 v37,-v60,-v61\nv_exp_f32 v79,v79\nM v[204:207],a[108:111],v[112:115],0\n" \
"s_barrier\nds_read_b128 a[56:59],v13 offset:53248\nv_exp_f32 v80,v80\nM v[208:211],a[112:115],v[112:115],0\nds_read_b128 a[60:63],v13 offset:55296\nv_min_f32 v38,-v60,-v61\nv_exp_f32 v81,v81\nM v[240:243],a[112:115],v[120:123],v[240:243]\n" \
"buffer_load_dwordx4 v22,s[40:43],s45 offen offset:1024 sc1 lds\nds_read_b128 a[64:67],v13 offset:50176\nM v[244:247],a[116:119],v[120:123],v[244:247]\nds_read_b128 a[68:71],v13 offset:52224\nv_min_f32 v39,-v60,-v61\nv_exp_f32 v82,v82\nM v[212:215],a[116:119],v[112:115],0\nds_read_b128 a[72:75],v13 offset:54272\n" \
"v_min_f32 v31,-v60,-v61\nv_exp_f32 v83,v83\nM v[216:219],a[120:123],v[112:115],0\nds_read_b128 a[76:79],v13 offset:56320\nC v72,v72,v73\nv_exp_f32 v84,v84\nM v[248:251],a[120:123],v[120:123],v[248:251]\nbuffer_load_dwordx4 v22,s[40:43],s45 offen offset:2048 sc1 lds\n" \
"M v[252:255],a[124:127],v[120:123],v[252:255]\nv_exp_f32 v85,v85\nM v[220:223],a[124:127],v[112:115],0\ns_waitcnt lgkmcnt(3)\nv_exp_f32 v86,v86\nM v[96:99],a[48:51],a[128:131],v[48:51]\nv_exp_f32 v87,v87\nM v[96:99],a[52:55],a[132:135],v[96:99]\n" \
"buffer_load_dwordx4 v22,s[40:43],s45 offen offset:3072 sc1 lds\nM v[96:99],a[56:59],a[136:139],v[96:99]\ns_add_u32 m0,s22,65536\ns_add_u32 s45,s45,s49\nv_exp_f32 v88,v88\nM v[96:99],a[60:63],a[140:143],v[96:99]\nLG15_%=:\ns_cbranch_execz LHR1_%=\n" \
"v_exp_f32 v89,v89\nM v[100:103],a[64:67],a[128:131],v[52:55]\ns_waitcnt lgkmcnt(0)\nv_exp_f32 v90,v90\nM v[100:103],a[68:71],a[132:135],v[100:103]\nbuffer_load_dwordx4 v22,s[40:43],s48 offen offset:0 sc1 lds\nM v[100:103],a[72:75],a[136:139],v[100:103]\nv_exp_f32 v91,v91\n" \
"M v[100:103],a[76:79],a[140:143],v[100:103]\nv_exp_f32 v92,v92\nM v[104:107],a[48:51],a[144:147],v[36:39]\nv_max3_f32 v60,v96,v97,v98\nC v73,v74,v75\nM v[104:107],a[52:55],a[148:151],v[104:107]\nbuffer_load_dwordx4 v22,s[40:43],s48 offen offset:1024 sc1 lds\nM v[104:107],a[56:59],a[152:155],v[104:107]\n" \
"v_max3_f32 v61,v99,v100,v101\nv_max3_f32 v60,v60,v102,v103\nM v[104:107],a[60:63],a[156:159],v[104:107]\nv_max3_f32 v60,v60,v61,s76\nv_mov_b32 v61,v60\nM v[112:115],a[48:51],a[160:163],v[40:43]\nv_exp_f32 v93,v93\nM v[112:115],a[52:55],a[164:167],v[112:115]\n" \
"buffer_load_dwordx4 v22,s[40:43],s48 offen offset:2048 sc1 lds\nM v[112:115],a[56:59],a[168:171],v[112:115]\nv_permlane32_swap_b32 v60,v61\nM v[112:115],a[60:63],a[172:175],v[112:115]\nC v80,v80,v81\nC v88,v88,v89\nM v[120:123],a[48:51],a[176:179],v[44:47]\nv_max_f32 v60,v60,v61\n" \
"C v74,v76,v77\nM v[120:123],a[52:55],a[180:183],v[120:123]\nbuffer_load_dwordx4 v22,s[40:43],s48 offen offset:3072 sc1 lds\nv_exp_f32 v94,v94\nM v[120:123],a[56:59],a[184:187],v[120:123]\ns_add_u32 m0,s22,98304\nC v75,v78,v79\nC v81,v82,v83\n" \
"M v[120:123],a[60:63],a[188:191],v[120:123]\ns_add_u32 s48,s48,s49\nv_mov_b32 v61,v60\nC v89,v90,v91\nM v[108:111],a[64:67],a[144:147],v[36:39]\nv_xor_b32 v14,0x10000,v14\nC v82,v84,v85\nM v[16:19],a[228:231],v[72:75],v[24:27]\n" \
"buffer_load_dwordx4 v22,s[40:43],s45 offen offset:0 sc1 lds\nv_exp_f32 v95,v95\nM v[108:111],a[68:71],a[148:151],v[108:111]\nds_read_b128 a[0:3],v14 offset:0\nC v83,v86,v87\nM v[108:111],a[72:75],a[152:155],v[108:111]\nv_permlane16_swap_b32 v60,v61\nM v[16:19],a[232:235],v[80:83],v[16:19]\n" \
"ds_read_b128 a[4:7],v14 offset:1024\nC v90,v92,v93\nM v[108:111],a[76:79],a[156:159],v[108:111]\nbuffer_load_dwordx4 v22,s[40:43],s45 offen offset:1024 sc1 lds\nC v91,v94,v95\nM v[116:119],a[64:67],a[160:163],v[40:43]\nds_read_b128 a[8:11],v14 offset:2048\nv_min_f32 v32,-v60,-v61\n" \
"M v[16:19],a[236:239],v[88:91],v[16:19]\nv_xor_b32 v13,s54,v13\nv_min_f32 v33,-v60,-v61\nM v[116:119],a[68:71],a[164:167],v[116:119]\nds_read_b128 a[12:15],v14 offset:3072\nv_min_f32 v34,-v60,-v61\nM v[116:119],a[72:75],a[168:171],v[116:119]\nbuffer_load_dwordx4 v22,s[40:43],s45 offen offset:2048 sc1 lds\n" \
"v_min_f32 v35,-v60,-v61\nM v[116:119],a[76:79],a[172:175],v[116:119]\nv_max3_f32 v15,v16,v17,v18\nv_max_f32 v15,v15,v19\nv_cmp_nge_f32 vcc,0x53800000,v15\nM v[124:127],a[64:67],a[176:179],v[44:47]\ns_waitcnt lgkmcnt(2)\nv_min_f32 v30,-v60,-v61\n" \
"v_add_f32 v96,v96,v32\nv_add_f32 v97,v97,v32\nM v[124:127],a[68:71],a[180:183],v[124:127]\ns_cbranch_vccnz Lcs3_%=\nLcr4_%=:\nv_add_f32 v98,v98,v32\nv_add_f32 v99,v99,v32\nv_add_f32 v100,v100,v32\n" \
"v_add_f32 v101,v101,v32\nv_add_f32 v102,v102,v32\nv_add_f32 v103,v103,v32\nM v[124:127],a[72:75],a[184:187],v[124:127]\ns_cmp_eq_u32 s52,s55\nbuffer_load_dwordx4 v22,s[40:43],s45 offen offset:3072 sc1 lds\nds_write_b128 v23,v[28:31] offset:0\nds_read_b128 a[16:19],v14 offset:4096\n" \
"ds_read_b128 a[20:23],v14 offset:5120\nds_read_b128 a[24:27],v14 offset:6144\nds_read_b128 a[28:31],v14 offset:7168\nM v[124:127],a[76:79],a[188:191],v[124:127]\ns_cbranch_scc1 LH1_%=\nLH1R0_%=:\ns_add_u32 m0,s22,16384\nv_exp_f32 v96,v96\n" \
"M v[160:163],a[0:3],v[72:75],0\ns_add_u32 s45,s45,s49\nv_exp_f32 v97,v97\nM v[192:195],a[0:3],v[80:83],v[192:195]\nv_exp_f32 v98,v98\nM v[224:227],a[0:3],v[88:91],v[224:227]\nbuffer_load_dwordx4 v22,s[40:43],s61 offen offset:0 sc1 lds\nM v[228:231],a[4:7],v[88:91],v[228:231]\n" \
"v_exp_f32 v99,v99\nM v[196:199],a[4:7],v[80:83],v[196:199]\ns_waitcnt vmcnt(13) lgkmcnt(0)\nv_exp_f32 v100,v100\nM v[164:167],a[4:7],v[72:75],0\ns_barrier\nv_xor_b32 v59,0x2000,v23\nM v[168:171],a[8:11],v[72:75],0\n" \
"buffer_load_dwordx4 v22,s[40:43],s61 offen offset:1024 sc1 lds\nM v[200:203],a[8:11],v[80:83],v[200:203]\nds_read_b128 v[28:31],v59 offset:0\nv_xor_b32 v13,0x10000,v13\nv_exp_f32 v101,v101\nM v[232:235],a[8:11],v[88:91],v[232:235]\nv_exp_f32 v102,v102\nM v[236:239],a[12:15],v[88:91],v[236:239]\n" \
"v_exp_f32 v103,v103\nM v[204:207],a[12:15],v[80:83],v[204:207]\nbuffer_load_dwordx4 v22,s[40:43],s61 offen offset:2048 sc1 lds\nv_exp_f32 v104,v104\nM v[172:175],a[12:15],v[72:75],0\nv_exp_f32 v105,v105\nM v[176:179],a[16:19],v[72:75],0\nv_exp_f32 v106,v106\n" \
"M v[208:211],a[16:19],v[80:83],v[208:211]\ns_waitcnt lgkmcnt(0)\nds_read_b128 a[192:195],v13 offset:32768\nds_read_b128 a[196:199],v13 offset:34816\nv_exp_f32 v107,v107\nM v[240:243],a[16:19],v[88:91],v[240:243]\nbuffer_load_dwordx4 v22,s[40:43],s61 offen offset:3072 sc1 lds\nv_exp_f32 v108,v108\n" \
"M v[244:247],a[20:23],v[88:91],v[244:247]\ns_add_u32 m0,s22,81920\nv_exp_f32 v109,v109\nM v[212:215],a[20:23],v[80:83],v[212:215]\nds_read_b128 a[200:203],v13 offset:36864\nC v96,v96,v97\nv_exp_f32 v110,v110\nM v[180:183],a[20:23],v[72:75],0\n" \
"ds_read_b128 a[204:207],v13 offset:38912\nC v97,v98,v99\nv_exp_f32 v111,v111\nM v[184:187],a[24:27],v[72:75],0\nv_exp_f32 v112,v112\nM v[216:219],a[24:27],v[80:83],v[216:219]\nv_exp_f32 v113,v113\nM v[248:251],a[24:27],v[88:91],v[248:251]\n" \
"v_exp_f32 v114,v114\nM v[252:255],a[28:31],v[88:91],v[252:255]\nbuffer_load_dwordx4 v22,s[40:43],s48 offen offset:0 sc1 lds\nM v[220:223],a[28:31],v[80:83],v[220:223]\nv_cndmask_b32 v32,v32,v28,s[66:67]\nv_cndmask_b32 v33,v33,v28,s[66:67]\nv_cndmask_b32 v34,v34,v28,s[66:67]\nv_cndmask_b32 v35,v35,v28,s[66:67]\n" \
"M v[188:191],a[28:31],v[72:75],0\ns_waitcnt lgkmcnt(0)\nds_read_b128 a[208:211],v13 offset:33792\nds_read_b128 a[212:215],v13 offset:35840\nM v[64:67],a[192:195],a[128:131],v[32:35]\nC v98,v100,v101\nC v99,v102,v103\nv_exp_f32 v115,v115\n" \
"M v[64:67],a[196:199],a[132:135],v[64:67]\nds_read_b128 a[216:219],v13 offset:37888\nC v104,v104,v105\nM v[24:27],a[224:227],v[96:99],v[16:19]\nds_read_b128 a[220:223],v13 offset:39936\nC v105,v106,v107\nM v[64:67],a[200:203],a[136:139],v[64:67]\nv_cndmask_b32 v36,v36,v29,s[66:67]\n" \
"v_cndmask_b32 v37,v37,v29,s[66:67]\nv_cndmask_b32 v38,v38,v29,s[66:67]\nv_cndmask_b32 v39,v39,v29,s[66:67]\nM v[64:67],a[204:207],a[140:143],v[64:67]\nv_exp_f32 v116,v116\nM v[72:75],a[192:195],a[144:147],v[36:39]\nC v106,v108,v109\nC v107,v110,v111\n" \
"M v[72:75],a[196:199],a[148:151],v[72:75]\nbuffer_load_dwordx4 v22,s[40:43],s48 offen offset:1024 sc1 lds\nM v[24:27],a[228:231],v[104:107],v[24:27]\nv_exp_f32 v117,v117\nM v[72:75],a[200:203],a[152:155],v[72:75]\nv_cndmask_b32 v40,v40,v30,s[66:67]\nv_cndmask_b32 v41,v41,v30,s[66:67]\nv_cndmask_b32 v42,v42,v30,s[66:67]\n" \
"v_cndmask_b32 v43,v43,v30,s[66:67]\nM v[72:75],a[204:207],a[156:159],v[72:75]\ns_waitcnt vmcnt(2)\nv_exp_f32 v118,v118\nM v[80:83],a[192:195],a[160:163],v[40:43]\ns_barrier\nv_cndmask_b32 v44,v44,v31,s[66:67]\nM v[80:83],a[196:199],a[164:167],v[80:83]\n" \
"buffer_load_dwordx4 v22,s[40:43],s48 offen offset:2048 sc1 lds\nM v[80:83],a[200:203],a[168:171],v[80:83]\nv_cndmask_b32 v45,v45,v31,s[66:67]\nv_cndmask_b32 v46,v46,v31,s[66:67]\nv_cndmask_b32 v47,v47,v31,s[66:67]\nM v[80:83],a[204:207],a[172:175],v[80:83]\nv_exp_f32 v119,v119\nM v[88:91],a[192:195],a[176:179],v[44:47]\n" \
"C v112,v112,v113\nC v113,v114,v115\nM v[88:91],a[196:199],a[180:183],v[88:91]\nbuffer_load_dwordx4 v22,s[40:43],s48 offen offset:3072 sc1 lds\nM v[88:91],a[200:203],a[184:187],v[88:91]\ns_add_u32 m0,s22,114688\nC v114,v116,v117\nC v115,v118,v119\n" \
"M v[88:91],a[204:207],a[188:191],v[88:91]\ns_add_u32 s48,s48,s49\nv_exp_f32 v120,v120\nM v[24:27],a[232:235],v[112:115],v[24:27]\ns_waitcnt lgkmcnt(0)\nv_exp_f32 v121,v121\nM v[68:71],a[208:211],a[128:131],v[32:35]\nv_exp_f32 v122,v122\n" \
"M v[68:71],a[212:215],a[132:135],v[68:71]\nv_exp_f32 v123,v123\nM v[68:71],a[216:219],a[136:139],v[68:71]\nv_exp_f32 v124,v124\nM v[68:71],a[220:223],a[140:143],v[68:71]\nv_exp_f32 v125,v125\nM v[76:79],a[208:211],a[144:147],v[36:39]\nv_exp_f32 v126,v126\n" \
"M v[76:79],a[212:215],a[148:151],v[76:79]\nv_exp_f32 v127,v127\nM v[76:79],a[216:219],a[152:155],v[76:79]\nds_read_b128 a[192:195],v14 offset:16384\nC v120,v120,v121\nC v121,v122,v123\nC v122,v124,v125\nC v123,v126,v127\n" \
"M v[76:79],a[220:223],a[156:159],v[76:79]\nbuffer_load_dwordx4 v22,s[40:43],s45 offen offset:0 sc1 lds\nM v[24:27],a[236:239],v[120:123],v[24:27]\nds_read_b128 a[196:199],v14 offset:17408\nv_lshrrev_b32 v58,8,v4\nM v[84:87],a[208:211],a[160:163],v[40:43]\nds_read_b128 a[200:203],v14 offset:18432\nv_lshl_add_u32 v57,v58,27,v57\n" \
"M v[84:87],a[212:215],a[164:167],v[84:87]\nds_read_b128 a[204:207],v14 offset:19456\nv_max3_f32 v15,v24,v25,v26\nM v[84:87],a[216:219],a[168:171],v[84:87]\nbuffer_load_dwordx4 v22,s[40:43],s45 offen offset:1024 sc1 lds\nM v[84:87],a[220:223],a[172:175],v[84:87]\nv_max_f32 v15,v15,v27\nv_cmp_nge_f32 vcc,0x53800000,v15\n" \
"M v[92:95],a[208:211],a[176:179],v[44:47]\nds_read_b128 a[208:211],v14 offset:20480\nM v[92:95],a[212:215],a[180:183],v[92:95]\ns_cbranch_vccnz Lcs5_%=\nLcr6_%=:\nds_read_b128 a[212:215],v14 offset:21504\nM v[92:95],a[216:219],a[184:187],v[92:95]\nbuffer_load_dwordx4 v22,s[40:43],s45 offen offset:2048 sc1 lds\n" \
"M v[92:95],a[220:223],a[188:191],v[92:95]\ns_waitcnt lgkmcnt(2)\nds_read_b128 a[216:219],v14 offset:22528\nM v[128:131],a[192:195],v[96:99],0\nds_read_b128 a[220:223],v14 offset:23552\nv_xor_b32 v14,s54,v14\nM v[160:163],a[192:195],v[104:107],v[160:163]\nv_exp_f32 v64,v64\n" \
"M v[192:195],a[192:195],v[112:115],v[192:195]\nbuffer_load_dwordx4 v22,s[40:43],s45 offen offset:3072 sc1 lds\nM v[224:227],a[192:195],v[120:123],v[224:227]\ns_add_u32 m0,s22,0\nv_exp_f32 v65,v65\nM v[228:231],a[196:199],v[120:123],v[228:231]\ns_add_u32 s45,s45,s49\nv_exp_f32 v66,v66\n" \
"M v[196:199],a[196:199],v[112:115],v[196:199]\nv_exp_f32 v67,v67\nM v[164:167],a[196:199],v[104:107],v[164:167]\ns_waitcnt vmcnt(16) lgkmcnt(0)\nv_exp_f32 v68,v68\nM v[132:135],a[196:199],v[96:99],0\ns_barrier\nds_read_b128 a[192:195],v13 offset:49152\n" \
"M v[136:139],a[200:203],v[96:99],0\nbuffer_load_dwordx4 v22,s[40:43],s48 offen offset:0 sc1 lds\nM v[168:171],a[200:203],v[104:107],v[168:171]\nv_exp_f32 v69,v69\nM v[200:203],a[200:203],v[112:115],v[200:203]\nv_exp_f32 v70,v70\nM v[232:235],a[200:203],v[120:123],v[232:235]\nv_exp_f32 v71,v71\n" \
"M v[236:239],a[204:207],v[120:123],v[236:239]\nbuffer_load_dwordx4 v22,s[40:43],s48 offen offset:1024 sc1 lds\nM v[204:207],a[204:207],v[112:115],v[204:207]\nds_read_b128 a[196:199],v13 offset:51200\nC v64,v64,v65\nM v[172:175],a[204:207],v[104:107],v[172:175]\nv_exp_f32 v72,v72\nM v[140:143],a[204:207],v[96:99],0\n" \
"ds_read_b128 a[200:203],v13 offset:53248\nds_read_b128 a[204:207],v13 offset:55296\nM v[144:147],a[208:211],v[96:99],0\nbuffer_load_dwordx4 v22,s[40:43],s48 offen offset:2048 sc1 lds\nM v[176:179],a[208:211],v[104:107],v[176:179]\nv_exp_f32 v73,v73\nM v[208:211],a[208:211],v[112:115],v[208:211]\nv_exp_f32 v74,v74\n" \
"M v[240:243],a[208:211],v[120:123],v[240:243]\nv_exp_f32 v75,v75\nM v[244:247],a[212:215],v[120:123],v[244:247]\nbuffer_load_dwordx4 v22,s[40:43],s48 offen offset:3072 sc1 lds\nM v[212:215],a[212:215],v[112:115],v[212:215]\ns_add_u32 m0,s53,32768\nv_exp_f32 v76,v76\nM v[180:183],a[212:215],v[104:107],v[180:183]\n" \
"s_add_u32 s52,s52,1\nv_exp_f32 v77,v77\nM v[148:151],a[212:215],v[96:99],0\ns_add_u32 s48,s48,s49\nv_exp_f32 v78,v78\nM v[152:155],a[216:219],v[96:99],0\nbuffer_load_dwordx4 v22,s[40:43],s45 offen offset:0 sc1 lds\nM v[184:187],a[216:219],v[104:107],v[184:187]\n" \
"v_exp_f32 v79,v79\nM v[216:219],a[216:219],v[112:115],v[216:219]\nv_exp_f32 v80,v80\nM v[248:251],a[216:219],v[120:123],v[248:251]\nC v65,v66,v67\nC v66,v68,v69\nM v[252:255],a[220:223],v[120:123],v[252:255]\nbuffer_load_dwordx4 v22,s[40:43],s45 offen offset:1024 sc1 lds\n" \
"M v[220:223],a[220:223],v[112:115],v[220:223]\ns_waitcnt lgkmcnt(0)\nv_exp_f32 v81,v81\nM v[188:191],a[220:223],v[104:107],v[188:191]\ns_cmp_eq_u32 s52,s56\nC v67,v70,v71\nC v72,v72,v73\nM v[156:159],a[220:223],v[96:99],0\n" \
"s_cbranch_scc1 LEV_%=\nLEVR0_%=:\nds_read_b128 a[208:211],v13 offset:50176\nds_read_b128 a[212:215],v13 offset:52224\nM v[96:99],a[192:195],a[128:131],v[32:35]\nbuffer_load_dwordx4 v22,s[40:43],s45 offen offset:2048 sc1 lds\nM v[96:99],a[196:199],a[132:135],v[96:99]\nv_exp_f32 v82,v82\n" \
"M v[16:19],a[224:227],v[64:67],v[24:27]\nds_read_b128 a[216:219],v13 offset:54272\nC v73,v74,v75\nM v[96:99],a[200:203],a[136:139],v[96:99]\nds_read_b128 a[220:223],v13 offset:56320\nC v74,v76,v77\nM v[96:99],a[204:207],a[140:143],v[96:99]\nv_exp_f32 v83,v83\n" \
"M v[104:107],a[192:195],a[144:147],v[36:39]\nC v75,v78,v79\nC v80,v80,v81\nM v[104:107],a[196:199],a[148:151],v[104:107]\nv_exp_f32 v84,v84\nM v[16:19],a[228:231],v[72:75],v[16:19]\nbuffer_load_dwordx4 v22,s[40:43],s45 offen offset:3072 sc1 lds\nM v[104:107],a[200:203],a[152:155],v[104:107]\n" \
"s_add_u32 m0,s53,16384\nv_exp_f32 v85,v85\nM v[104:107],a[204:207],a[156:159],v[104:107]\ns_add_u32 s45,s45,s49\nv_exp_f32 v86,v86\nM v[112:115],a[192:195],a[160:163],v[40:43]\nv_exp_f32 v87,v87\nM v[112:115],a[196:199],a[164:167],v[112:115]\n" \
"buffer_load_dwordx4 v22,s[40:43],s48 offen offset:0 sc1 lds\nM v[112:115],a[200:203],a[168:171],v[112:115]\nv_exp_f32 v88,v88\nM v[112:115],a[204:207],a[172:175],v[112:115]\nC v81,v82,v83\nC v82,v84,v85\nM v[120:123],a[192:195],a[176:179],v[44:47]\nv_exp_f32 v89,v89\n" \
"M v[120:123],a[196:199],a[180:183],v[120:123]\nbuffer_load_dwordx4 v22,s[40:43],s48 offen offset:1024 sc1 lds\nM v[120:123],a[200:203],a[184:187],v[120:123]\nC v83,v86,v87\nC v88,v88,v89\nM v[120:123],a[204:207],a[188:191],v[120:123]\nv_exp_f32 v90,v90\nM v[16:19],a[232:235],v[80:83],v[16:19]\n" \
"s_waitcnt lgkmcnt(0)\nv_exp_f32 v91,v91\nM v[100:103],a[208:211],a[128:131],v[32:35]\nbuffer_load_dwordx4 v22,s[40:43],s48 offen offset:2048 sc1 lds\nM v[100:103],a[212:215],a[132:135],v[100:103]\nv_exp_f32 v92,v92\nM v[100:103],a[216:219],a[136:139],v[100:103]\nv_exp_f32 v93,v93\n" \
"M v[100:103],a[220:223],a[140:143],v[100:103]\nv_exp_f32 v94,v94\nM v[108:111],a[208:211],a[144:147],v[36:39]\nbuffer_load_dwordx4 v22,s[40:43],s48 offen offset:3072 sc1 lds\nM v[108:111],a[212:215],a[148:151],v[108:111]\ns_add_u32 m0,s53,49152\nv_exp_f32 v95,v95\nv_xor_b32 v14,0x10000,v14\n" \
"M v[108:111],a[216:219],a[152:155],v[108:111]\ns_xor_b32 s53,s53,0x10000\nds_read_b128 a[192:195],v14 offset:0\nC v89,v90,v91\nC v90,v92,v93\nC v91,v94,v95\nM v[108:111],a[220:223],a[156:159],v[108:111]\ns_add_u32 s48,s48,s49\n" \
"ds_read_b128 a[196:199],v14 offset:1024\nds_read_b128 a[200:203],v14 offset:2048\nM v[16:19],a[236:239],v[88:91],v[16:19]\nds_read_b128 a[204:207],v14 offset:3072\nM v[116:119],a[208:211],a[160:163],v[40:43]\nM v[116:119],a[212:215],a[164:167],v[116:119]\nv_max3_f32 v15,v16,v17,v18\nv_max_f32 v15,v15,v19\n" \
"M v[116:119],a[216:219],a[168:171],v[116:119]\nv_cmp_nge_f32 vcc,0x53800000,v15\nM v[116:119],a[220:223],a[172:175],v[116:119]\nM v[124:127],a[208:211],a[176:179],v[44:47]\ns_waitcnt lgkmcnt(1)\nds_read_b128 a[208:211],v14 offset:4096\nM v[124:127],a[212:215],a[180:183],v[124:127]\ns_cbranch_vccnz Lcs7_%=\n" \
"Lcr8_%=:\nds_read_b128 a[212:215],v14 offset:5120\nM v[124:127],a[216:219],a[184:187],v[124:127]\ns_cmp_eq_u32 s52,s55\nds_read_b128 a[216:219],v14 offset:6144\nM v[124:127],a[220:223],a[188:191],v[124:127]\ns_cbranch_scc1 LH1_%=\nLH1R1_%=:\n" \
"ds_read_b128 a[220:223],v14 offset:7168\nv_xor_b32 v13,0x10000,v13\nM v[128:131],a[192:195],v[64:67],v[128:131]\nv_exp_f32 v96,v96\nM v[160:163],a[192:195],v[72:75],v[160:163]\nv_exp_f32 v97,v97\nM v[192:195],a[192:195],v[80:83],v[192:195]\nv_exp_f32 v98,v98\n" \
"M v[224:227],a[192:195],v[88:91],v[224:227]\nv_exp_f32 v99,v99\nM v[228:231],a[196:199],v[88:91],v[228:231]\nv_exp_f32 v100,v100\nM v[196:199],a[196:199],v[80:83],v[196:199]\nv_exp_f32 v101,v101\nM v[164:167],a[196:199],v[72:75],v[164:167]\ns_waitcnt vmcnt(16) lgkmcnt(0)\n" \
"v_exp_f32 v102,v102\nM v[132:135],a[196:199],v[64:67],v[132:135]\ns_barrier\nds_read_b128 a[192:195],v13 offset:32768\nM v[136:139],a[200:203],v[64:67],v[136:139]\nbuffer_load_dwordx4 v22,s[40:43],s45 offen offset:0 sc1 lds\nM v[168:171],a[200:203],v[72:75],v[168:171]\nv_exp_f32 v103,v103\n" \
"M v[200:203],a[200:203],v[80:83],v[200:203]\nv_exp_f32 v104,v104\nM v[232:235],a[200:203],v[88:91],v[232:235]\nv_exp_f32 v105,v105\nM v[236:239],a[204:207],v[88:91],v[236:239]\nbuffer_load_dwordx4 v22,s[40:43],s45 offen offset:1024 sc1 lds\nM v[204:207],a[204:207],v[80:83],v[204:207]\nds_read_b128 a[196:199],v13 offset:34816\n" \
"C v96,v96,v97\nM v[172:175],a[204:207],v[72:75],v[172:175]\nv_exp_f32 v106,v106\nM v[140:143],a[204:207],v[64:67],v[140:143]\nds_read_b128 a[200:203],v13 offset:36864\nds_read_b128 a[204:207],v13 offset:38912\nM v[144:147],a[208:211],v[64:67],v[144:147]\nbuffer_load_dwordx4 v22,s[40:43],s45 offen offset:2048 sc1 lds\n" \
"M v[176:179],a[208:211],v[72:75],v[176:179]\nv_exp_f32 v107,v107\nM v[208:211],a[208:211],v[80:83],v[208:211]\nv_exp_f32 v108,v108\nM v[240:243],a[208:211],v[88:91],v[240:243]\nv_exp_f32 v109,v109\nM v[244:247],a[212:215],v[88:91],v[244:247]\nbuffer_load_dwordx4 v22,s[40:43],s45 offen offset:3072 sc1 lds\n" \
"M v[212:215],a[212:215],v[80:83],v[212:215]\ns_add_u32 m0,s53,0\nv_exp_f32 v110,v110\nM v[180:183],a[212:215],v[72:75],v[180:183]\ns_add_u32 s45,s45,s49\nv_exp_f32 v111,v111\nM v[148:151],a[212:215],v[64:67],v[148:151]\nv_exp_f32 v112,v112\n" \
"M v[152:155],a[216:219],v[64:67],v[152:155]\nbuffer_load_dwordx4 v22,s[40:43],s48 offen offset:0 sc1 lds\nM v[184:187],a[216:219],v[72:75],v[184:187]\nv_exp_f32 v113,v113\nM v[216:219],a[216:219],v[80:83],v[216:219]\nC v97,v98,v99\nC v98,v100,v101\nM v[248:251],a[216:219],v[88:91],v[248:251]\n" \
"v_exp_f32 v114,v114\nM v[252:255],a[220:223],v[88:91],v[252:255]\nbuffer_load_dwordx4 v22,s[40:43],s48 offen offset:1024 sc1 lds\nM v[220:223],a[220:223],v[80:83],v[220:223]\nC v99,v102,v103\nC v104,v104,v105\nM v[188:191],a[220:223],v[72:75],v[188:191]\nv_exp_f32 v115,v115\n" \
"M v[156:159],a[220:223],v[64:67],v[156:159]\ns_waitcnt lgkmcnt(0)\nv_exp_f32 v116,v116\nM v[64:67],a[192:195],a[128:131],v[32:35]\nbuffer_load_dwordx4 v22,s[40:43],s48 offen offset:2048 sc1 lds\nM v[64:67],a[196:199],a[132:135],v[64:67]\nds_read_b128 a[208:211],v13 offset:33792\nC v105,v106,v107\n" \
"M v[24:27],a[224:227],v[96:99],v[16:19]\nds_read_b128 a[212:215],v13 offset:35840\nC v106,v108,v109\nM v[64:67],a[200:203],a[136:139],v[64:67]\nds_read_b128 a[216:219],v13 offset:37888\nC v107,v110,v111\nM v[64:67],a[204:207],a[140:143],v[64:67]\nbuffer_load_dwordx4 v22,s[40:43],s48 offen offset:3072 sc1 lds\n" \
"M v[72:75],a[192:195],a[144:147],v[36:39]\ns_add_u32 m0,s53,32768\nds_read_b128 a[220:223],v13 offset:39936\nC v112,v112,v113\nM v[72:75],a[196:199],a[148:151],v[72:75]\ns_add_u32 s48,s48,s49\nv_exp_f32 v117,v117\nM v[24:27],a[228:231],v[104:107],v[24:27]\n" \
"v_exp_f32 v118,v118\nM v[72:75],a[200:203],a[152:155],v[72:75]\nv_exp_f32 v119,v119\nM v[72:75],a[204:207],a[156:159],v[72:75]\nv_exp_f32 v120,v120\nM v[80:83],a[192:195],a[160:163],v[40:43]\nv_exp_f32 v121,v121\nM v[80:83],a[196:199],a[164:167],v[80:83]\n" \
"C v113,v114,v115\nC v114,v116,v117\nM v[80:83],a[200:203],a[168:171],v[80:83]\nv_exp_f32 v122,v122\nM v[80:83],a[204:207],a[172:175],v[80:83]\nC v115,v118,v119\nC v120,v120,v121\nM v[88:91],a[192:195],a[176:179],v[44:47]\n" \
"v_exp_f32 v123,v123\nM v[88:91],a[196:199],a[180:183],v[88:91]\nv_exp_f32 v124,v124\nM v[88:91],a[200:203],a[184:187],v[88:91]\nv_exp_f32 v125,v125\nM v[88:91],a[204:207],a[188:191],v[88:91]\nds_read_b128 a[192:195],v14 offset:16384\nC v121,v122,v123\n" \
"M v[24:27],a[232:235],v[112:115],v[24:27]\ns_waitcnt lgkmcnt(1)\nv_exp_f32 v126,v126\nM v[68:71],a[208:211],a[128:131],v[32:35]\nds_read_b128 a[196:199],v14 offset:17408\nC v122,v124,v125\nM v[68:71],a[212:215],a[132:135],v[68:71]\nv_exp_f32 v127,v127\n" \
"M v[68:71],a[216:219],a[136:139],v[68:71]\nds_read_b128 a[200:203],v14 offset:18432\nC v123,v126,v127\nM v[68:71],a[220:223],a[140:143],v[68:71]\nds_read_b128 a[204:207],v14 offset:19456\nM v[76:79],a[208:211],a[144:147],v[36:39]\nM v[76:79],a[212:215],a[148:151],v[76:79]\nM v[76:79],a[216:219],a[152:155],v[76:79]\n" \
"M v[76:79],a[220:223],a[156:159],v[76:79]\nM v[24:27],a[236:239],v[120:123],v[24:27]\nM v[84:87],a[208:211],a[160:163],v[40:43]\ns_mov_b64 exec,0xff\nglobal_load_dword v0,v2,s[100:101]\ns_mov_b64 exec,-1\nM v[84:87],a[212:215],a[164:167],v[84:87]\nv_max3_f32 v15,v24,v25,v26\n" \
"v_max_f32 v15,v15,v27\nM v[84:87],a[216:219],a[168:171],v[84:87]\nv_cmp_nge_f32 vcc,0x53800000,v15\nM v[84:87],a[220:223],a[172:175],v[84:87]\nM v[92:95],a[208:211],a[176:179],v[44:47]\nds_read_b128 a[208:211],v14 offset:20480\nM v[92:95],a[212:215],a[180:183],v[92:95]\ns_cbranch_vccnz Lcs9_%=\n" \
"Lcr10_%=:\nds_read_b128 a[212:215],v14 offset:21504\nM v[92:95],a[216:219],a[184:187],v[92:95]\nLG16_%=:\ns_cbranch_execz LHR2_%=\nds_read_b128 a[216:219],v14 offset:22528\nM v[92:95],a[220:223],a[188:191],v[92:95]\ns_waitcnt lgkmcnt(3)\n" \
"ds_read_b128 a[220:223],v14 offset:23552\nM v[128:131],a[192:195],v[96:99],v[128:131]\nv_exp_f32 v64,v64\nM v[160:163],a[192:195],v[104:107],v[160:163]\nv_exp_f32 v65,v65\nM v[192:195],a[192:195],v[112:115],v[192:195]\nbuffer_load_dword v58,v57,s[84:87],s74 offen\nM v[224:227],a[192:195],v[120:123],v[224:227]\n" \
"s_add_u32 s74,s74,2048\nv_exp_f32 v66,v66\nM v[228:231],a[196:199],v[120:123],v[228:231]\nv_exp_f32 v67,v67\nM v[196:199],a[196:199],v[112:115],v[196:199]\nv_exp_f32 v68,v68\nM v[164:167],a[196:199],v[104:107],v[164:167]\ns_waitcnt vmcnt(18) lgkmcnt(0)\n" \
"v_exp_f32 v69,v69\nM v[132:135],a[196:199],v[96:99],v[132:135]\ns_barrier\nds_read_b128 a[192:195],v13 offset:49152\nM v[136:139],a[200:203],v[96:99],v[136:139]\nbuffer_load_dwordx4 v22,s[40:43],s45 offen offset:0 sc1 lds\nM v[168:171],a[200:203],v[104:107],v[168:171]\nv_exp_f32 v70,v70\n" \
"M v[200:203],a[200:203],v[112:115],v[200:203]\nv_exp_f32 v71,v71\nM v[232:235],a[200:203],v[120:123],v[232:235]\nv_exp_f32 v72,v72\nM v[236:239],a[204:207],v[120:123],v[236:239]\nbuffer_load_dwordx4 v22,s[40:43],s45 offen offset:1024 sc1 lds\nM v[204:207],a[204:207],v[112:115],v[204:207]\nds_read_b128 a[196:199],v13 offset:51200\n" \
"C v64,v64,v65\nM v[172:175],a[204:207],v[104:107],v[172:175]\nv_exp_f32 v73,v73\nM v[140:143],a[204:207],v[96:99],v[140:143]\nds_read_b128 a[200:203],v13 offset:53248\nds_read_b128 a[204:207],v13 offset:55296\nM v[144:147],a[208:211],v[96:99],v[144:147]\nbuffer_load_dwordx4 v22,s[40:43],s45 offen offset:2048 sc1 lds\n" \
"M v[176:179],a[208:211],v[104:107],v[176:179]\nv_exp_f32 v74,v74\nM v[208:211],a[208:211],v[112:115],v[208:211]\nv_exp_f32 v75,v75\nM v[240:243],a[208:211],v[120:123],v[240:243]\nv_exp_f32 v76,v76\nM v[244:247],a[212:215],v[120:123],v[244:247]\nbuffer_load_dwordx4 v22,s[40:43],s45 offen offset:3072 sc1 lds\n" \
"M v[212:215],a[212:215],v[112:115],v[212:215]\ns_add_u32 m0,s53,16384\nv_exp_f32 v77,v77\nM v[180:183],a[212:215],v[104:107],v[180:183]\ns_add_u32 s52,s52,1\nv_exp_f32 v78,v78\nM v[148:151],a[212:215],v[96:99],v[148:151]\ns_add_u32 s45,s45,s49\n" \
"v_exp_f32 v79,v79\nM v[152:155],a[216:219],v[96:99],v[152:155]\nbuffer_load_dwordx4 v22,s[40:43],s48 offen offset:0 sc1 lds\nM v[184:187],a[216:219],v[104:107],v[184:187]\nv_exp_f32 v80,v80\nM v[216:219],a[216:219],v[112:115],v[216:219]\nC v65,v66,v67\nC v66,v68,v69\n" \
"M v[248:251],a[216:219],v[120:123],v[248:251]\nv_exp_f32 v81,v81\nM v[252:255],a[220:223],v[120:123],v[252:255]\nbuffer_load_dwordx4 v22,s[40:43],s48 offen offset:1024 sc1 lds\nM v[220:223],a[220:223],v[112:115],v[220:223]\ns_waitcnt lgkmcnt(0)\nC v67,v70,v71\nC v72,v72,v73\n" \
"M v[188:191],a[220:223],v[104:107],v[188:191]\ns_cmp_eq_u32 s52,s56\nv_exp_f32 v82,v82\nM v[156:159],a[220:223],v[96:99],v[156:159]\ns_cbranch_scc1 LEV_%=\nLEVR1_%=:\nds_read_b128 a[208:211],v13 offset:50176\nds_read_b128 a[212:215],v13 offset:52224\n" \
"M v[96:99],a[192:195],a[128:131],v[32:35]\nbuffer_load_dwordx4 v22,s[40:43],s48 offen offset:2048 sc1 lds\nM v[96:99],a[196:199],a[132:135],v[96:99]\nv_exp_f32 v83,v83\nM v[16:19],a[224:227],v[64:67],v[24:27]\nds_read_b128 a[216:219],v13 offset:54272\nC v73,v74,v75\nM v[96:99],a[200:203],a[136:139],v[96:99]\n" \
"ds_read_b128 a[220:223],v13 offset:56320\nC v74,v76,v77\nM v[96:99],a[204:207],a[140:143],v[96:99]\nbuffer_load_dwordx4 v22,s[40:43],s48 offen offset:3072 sc1 lds\nM v[104:107],a[192:195],a[144:147],v[36:39]\ns_add_u32 m0,s53,49152\nC v75,v78,v79\nC v80,v80,v81\n" \
"M v[104:107],a[196:199],a[148:151],v[104:107]\ns_xor_b32 s53,s53,0x10000\nv_exp_f32 v84,v84\nM v[16:19],a[228:231],v[72:75],v[16:19]\ns_add_u32 s48,s48,s49\nv_exp_f32 v85,v85\nM v[104:107],a[200:203],a[152:155],v[104:107]\nv_exp_f32 v86,v86\n" \
"M v[104:107],a[204:207],a[156:159],v[104:107]\nv_exp_f32 v87,v87\nM v[112:115],a[192:195],a[160:163],v[40:43]\nv_exp_f32 v88,v88\nM v[112:115],a[196:199],a[164:167],v[112:115]\nC v81,v82,v83\nC v82,v84,v85\nM v[112:115],a[200:203],a[168:171],v[112:115]\n" \
"v_exp_f32 v89,v89\nM v[112:115],a[204:207],a[172:175],v[112:115]\nC v83,v86,v87\nC v88,v88,v89\nM v[120:123],a[192:195],a[176:179],v[44:47]\nv_exp_f32 v90,v90\nM v[120:123],a[196:199],a[180:183],v[120:123]\nv_exp_f32 v91,v91\n" \
"M v[120:123],a[200:203],a[184:187],v[120:123]\nv_exp_f32 v92,v92\nM v[120:123],a[204:207],a[188:191],v[120:123]\nv_exp_f32 v93,v93\nM v[16:19],a[232:235],v[80:83],v[16:19]\ns_waitcnt lgkmcnt(0)\nv_exp_f32 v94,v94\nM v[100:103],a[208:211],a[128:131],v[32:35]\n" \
"C v89,v90,v91\nC v90,v92,v93\nM v[100:103],a[212:215],a[132:135],v[100:103]\nv_exp_f32 v95,v95\nM v[100:103],a[216:219],a[136:139],v[100:103]\nv_xor_b32 v14,0x10000,v14\nC v91,v94,v95\nM v[100:103],a[220:223],a[140:143],v[100:103]\n" \
"ds_read_b128 a[192:195],v14 offset:0\nds_read_b128 a[196:199],v14 offset:1024\nM v[108:111],a[208:211],a[144:147],v[36:39]\nds_read_b128 a[200:203],v14 offset:2048\nM v[108:111],a[212:215],a[148:151],v[108:111]\nds_read_b128 a[204:207],v14 offset:3072\nM v[108:111],a[216:219],a[152:155],v[108:111]\nM v[108:111],a[220:223],a[156:159],v[108:111]\n" \
"M v[16:19],a[236:239],v[88:91],v[16:19]\nM v[116:119],a[208:211],a[160:163],v[40:43]\nM v[116:119],a[212:215],a[164:167],v[116:119]\nv_max3_f32 v15,v16,v17,v18\nv_max_f32 v15,v15,v19\nM v[116:119],a[216:219],a[168:171],v[116:119]\nv_cmp_nge_f32 vcc,0x53800000,v15\nM v[116:119],a[220:223],a[172:175],v[116:119]\n" \
"M v[124:127],a[208:211],a[176:179],v[44:47]\ns_waitcnt lgkmcnt(0)\nds_read_b128 a[208:211],v14 offset:4096\nM v[124:127],a[212:215],a[180:183],v[124:127]\ns_cbranch_vccnz Lcs11_%=\nLcr12_%=:\nds_read_b128 a[212:215],v14 offset:5120\nM v[124:127],a[216:219],a[184:187],v[124:127]\n" \
"s_cmp_eq_u32 s52,s55\nds_read_b128 a[216:219],v14 offset:6144\nM v[124:127],a[220:223],a[188:191],v[124:127]\ns_cbranch_scc1 LH1_%=\nLH1R2_%=:\nds_read_b128 a[220:223],v14 offset:7168\nv_xor_b32 v13,0x10000,v13\nM v[128:131],a[192:195],v[64:67],v[128:131]\n" \
"v_exp_f32 v96,v96\nM v[160:163],a[192:195],v[72:75],v[160:163]\nv_exp_f32 v97,v97\nM v[192:195],a[192:195],v[80:83],v[192:195]\nv_exp_f32 v98,v98\nM v[224:227],a[192:195],v[88:91],v[224:227]\nv_exp_f32 v99,v99\nM v[228:231],a[196:199],v[88:91],v[228:231]\n" \
"v_exp_f32 v100,v100\nM v[196:199],a[196:199],v[80:83],v[196:199]\nv_exp_f32 v101,v101\nM v[164:167],a[196:199],v[72:75],v[164:167]\ns_waitcnt vmcnt(18) lgkmcnt(0)\nv_exp_f32 v102,v102\nM v[132:135],a[196:199],v[64:67],v[132:135]\ns_barrier\n" \
"ds_read_b128 a[192:195],v13 offset:32768\nM v[136:139],a[200:203],v[64:67],v[136:139]\nbuffer_load_dwordx4 v22,s[40:43],s45 offen offset:0 sc1 lds\nM v[168:171],a[200:203],v[72:75],v[168:171]\nv_exp_f32 v103,v103\nM v[200:203],a[200:203],v[80:83],v[200:203]\nv_exp_f32 v104,v104\nM v[232:235],a[200:203],v[88:91],v[232:235]\n" \
"v_exp_f32 v105,v105\nM v[236:239],a[204:207],v[88:91],v[236:239]\nbuffer_load_dwordx4 v22,s[40:43],s45 offen offset:1024 sc1 lds\nM v[204:207],a[204:207],v[80:83],v[204:207]\nds_read_b128 a[196:199],v13 offset:34816\nC v96,v96,v97\nM v[172:175],a[204:207],v[72:75],v[172:175]\nv_exp_f32 v106,v106\n" \
"M v[140:143],a[204:207],v[64:67],v[140:143]\nds_read_b128 a[200:203],v13 offset:36864\nds_read_b128 a[204:207],v13 offset:38912\nM v[144:147],a[208:211],v[64:67],v[144:147]\nbuffer_load_dwordx4 v22,s[40:43],s45 offen offset:2048 sc1 lds\nM v[176:179],a[208:211],v[72:75],v[176:179]\nv_exp_f32 v107,v107\nM v[208:211],a[208:211],v[80:83],v[208:211]\n" \
"v_exp_f32 v108,v108\nM v[240:243],a[208:211],v[88:91],v[240:243]\nv_exp_f32 v109,v109\nM v[244:247],a[212:215],v[88:91],v[244:247]\nbuffer_load_dwordx4 v22,s[40:43],s45 offen offset:3072 sc1 lds\nM v[212:215],a[212:215],v[80:83],v[212:215]\ns_add_u32 m0,s53,0\nv_exp_f32 v110,v110\n" \
"M v[180:183],a[212:215],v[72:75],v[180:183]\ns_add_u32 s45,s45,s49\nv_exp_f32 v111,v111\nM v[148:151],a[212:215],v[64:67],v[148:151]\nv_exp_f32 v112,v112\nM v[152:155],a[216:219],v[64:67],v[152:155]\nbuffer_load_dwordx4 v22,s[40:43],s48 offen offset:0 sc1 lds\nM v[184:187],a[216:219],v[72:75],v[184:187]\n" \
"C v97,v98,v99\nC v98,v100,v101\nM v[216:219],a[216:219],v[80:83],v[216:219]\nv_exp_f32 v113,v113\nM v[248:251],a[216:219],v[88:91],v[248:251]\nC v99,v102,v103\nC v104,v104,v105\nM v[252:255],a[220:223],v[88:91],v[252:255]\n" \
"buffer_load_dwordx4 v22,s[40:43],s48 offen offset:1024 sc1 lds\nM v[220:223],a[220:223],v[80:83],v[220:223]\nv_exp_f32 v114,v114\nM v[188:191],a[220:223],v[72:75],v[188:191]\nv_exp_f32 v115,v115\nM v[156:159],a[220:223],v[64:67],v[156:159]\ns_waitcnt lgkmcnt(0)\nds_read_b128 a[208:211],v13 offset:33792\n" \
"ds_read_b128 a[212:215],v13 offset:35840\nM v[64:67],a[192:195],a[128:131],v[32:35]\nbuffer_load_dwordx4 v22,s[40:43],s48 offen offset:2048 sc1 lds\nM v[64:67],a[196:199],a[132:135],v[64:67]\nC v105,v106,v107\nC v106,v108,v109\nM v[24:27],a[224:227],v[96:99],v[16:19]\nds_read_b128 a[216:219],v13 offset:37888\n" \
"C v107,v110,v111\nM v[64:67],a[200:203],a[136:139],v[64:67]\nds_read_b128 a[220:223],v13 offset:39936\nC v112,v112,v113\nM v[64:67],a[204:207],a[140:143],v[64:67]\nbuffer_load_dwordx4 v22,s[40:43],s48 offen offset:3072 sc1 lds\nM v[72:75],a[192:195],a[144:147],v[36:39]\ns_add_u32 m0,s53,32768\n" \
"v_exp_f32 v116,v116\nM v[72:75],a[196:199],a[148:151],v[72:75]\ns_add_u32 s48,s48,s49\nv_exp_f32 v117,v117\nM v[24:27],a[228:231],v[104:107],v[24:27]\nv_exp_f32 v118,v118\nM v[72:75],a[200:203],a[152:155],v[72:75]\nv_exp_f32 v119,v119\n" \
"M v[72:75],a[204:207],a[156:159],v[72:75]\nv_exp_f32 v120,v120\nM v[80:83],a[192:195],a[160:163],v[40:43]\nC v113,v114,v115\nC v114,v116,v117\nM v[80:83],a[196:199],a[164:167],v[80:83]\nv_exp_f32 v121,v121\nM v[80:83],a[200:203],a[168:171],v[80:83]\n" \
"C v115,v118,v119\nC v120,v120,v121\nM v[80:83],a[204:207],a[172:175],v[80:83]\nv_exp_f32 v122,v122\nM v[88:91],a[192:195],a[176:179],v[44:47]\nv_exp_f32 v123,v123\nM v[88:91],a[196:199],a[180:183],v[88:91]\nv_exp_f32 v124,v124\n" \
"M v[88:91],a[200:203],a[184:187],v[88:91]\nv_exp_f32 v125,v125\nM v[88:91],a[204:207],a[188:191],v[88:91]\nds_read_b128 a[192:195],v14 offset:16384\nC v121,v122,v123\nM v[24:27],a[232:235],v[112:115],v[24:27]\ns_waitcnt lgkmcnt(1)\nv_exp_f32 v126,v126\n" \
"M v[68:71],a[208:211],a[128:131],v[32:35]\nds_read_b128 a[196:199],v14 offset:17408\nC v122,v124,v125\nM v[68:71],a[212:215],a[132:135],v[68:71]\nv_exp_f32 v127,v127\nM v[68:71],a[216:219],a[136:139],v[68:71]\nds_read_b128 a[200:203],v14 offset:18432\nC v123,v126,v127\n" \
"M v[68:71],a[220:223],a[140:143],v[68:71]\nds_read_b128 a[204:207],v14 offset:19456\nM v[76:79],a[208:211],a[144:147],v[36:39]\nM v[76:79],a[212:215],a[148:151],v[76:79]\nM v[76:79],a[216:219],a[152:155],v[76:79]\nM v[76:79],a[220:223],a[156:159],v[76:79]\nM v[24:27],a[236:239],v[120:123],v[24:27]\nM v[84:87],a[208:211],a[160:163],v[40:43]\n" \
"s_mov_b64 exec,0xff\nglobal_load_dword v0,v2,s[100:101]\ns_mov_b64 exec,-1\nM v[84:87],a[212:215],a[164:167],v[84:87]\nv_max3_f32 v15,v24,v25,v26\nv_max_f32 v15,v15,v27\nM v[84:87],a[216:219],a[168:171],v[84:87]\nv_cmp_nge_f32 vcc,0x53800000,v15\n" \
"M v[84:87],a[220:223],a[172:175],v[84:87]\nM v[92:95],a[208:211],a[176:179],v[44:47]\nds_read_b128 a[208:211],v14 offset:20480\nM v[92:95],a[212:215],a[180:183],v[92:95]\ns_cbranch_vccnz Lcs13_%=\nLcr14_%=:\nds_read_b128 a[212:215],v14 offset:21504\nM v[92:95],a[216:219],a[184:187],v[92:95]\n" \
"ds_read_b128 a[216:219],v14 offset:22528\nM v[92:95],a[220:223],a[188:191],v[92:95]\ns_waitcnt lgkmcnt(3)\nds_read_b128 a[220:223],v14 offset:23552\nM v[128:131],a[192:195],v[96:99],v[128:131]\nv_exp_f32 v64,v64\nM v[160:163],a[192:195],v[104:107],v[160:163]\nv_exp_f32 v65,v65\n" \
"M v[192:195],a[192:195],v[112:115],v[192:195]\nbuffer_load_dword v58,v57,s[84:87],s74 offen\nM v[224:227],a[192:195],v[120:123],v[224:227]\ns_add_u32 s74,s74,2048\nv_exp_f32 v66,v66\nM v[228:231],a[196:199],v[120:123],v[228:231]\nv_exp_f32 v67,v67\nM v[196:199],a[196:199],v[112:115],v[196:199]\n" \
"v_exp_f32 v68,v68\nM v[164:167],a[196:199],v[104:107],v[164:167]\ns_waitcnt vmcnt(20) lgkmcnt(0)\nv_exp_f32 v69,v69\nM v[132:135],a[196:199],v[96:99],v[132:135]\ns_barrier\nds_read_b128 a[192:195],v13 offset:49152\nM v[136:139],a[200:203],v[96:99],v[136:139]\n" \
"buffer_load_dwordx4 v22,s[40:43],s45 offen offset:0 sc1 lds\nM v[168:171],a[200:203],v[104:107],v[168:171]\nv_exp_f32 v70,v70\nM v[200:203],a[200:203],v[112:115],v[200:203]\nv_exp_f32 v71,v71\nM v[232:235],a[200:203],v[120:123],v[232:235]\nv_exp_f32 v72,v72\nM v[236:239],a[204:207],v[120:123],v[236:239]\n" \
"buffer_load_dwordx4 v22,s[40:43],s45 offen offset:1024 sc1 lds\nM v[204:207],a[204:207],v[112:115],v[204:207]\nds_read_b128 a[196:199],v13 offset:51200\nC v64,v64,v65\nM v[172:175],a[204:207],v[104:107],v[172:175]\nv_exp_f32 v73,v73\nM v[140:143],a[204:207],v[96:99],v[140:143]\nds_read_b128 a[200:203],v13 offset:53248\n" \
"ds_read_b128 a[204:207],v13 offset:55296\nM v[144:147],a[208:211],v[96:99],v[144:147]\nbuffer_load_dwordx4 v22,s[40:43],s45 offen offset:2048 sc1 lds\nM v[176:179],a[208:211],v[104:107],v[176:179]\nv_exp_f32 v74,v74\nM v[208:211],a[208:211],v[112:115],v[208:211]\nv_exp_f32 v75,v75\nM v[240:243],a[208:211],v[120:123],v[240:243]\n" \
"v_exp_f32 v76,v76\nM v[244:247],a[212:215],v[120:123],v[244:247]\nbuffer_load_dwordx4 v22,s[40:43],s45 offen offset:3072 sc1 lds\nM v[212:215],a[212:215],v[112:115],v[212:215]\ns_add_u32 m0,s53,16384\nv_exp_f32 v77,v77\nM v[180:183],a[212:215],v[104:107],v[180:183]\ns_add_u32 s52,s52,1\n" \
"v_exp_f32 v78,v78\nM v[148:151],a[212:215],v[96:99],v[148:151]\ns_add_u32 s45,s45,s49\nv_exp_f32 v79,v79\nM v[152:155],a[216:219],v[96:99],v[152:155]\nbuffer_load_dwordx4 v22,s[40:43],s48 offen offset:0 sc1 lds\nM v[184:187],a[216:219],v[104:107],v[184:187]\nv_exp_f32 v80,v80\n" \
"M v[216:219],a[216:219],v[112:115],v[216:219]\nC v65,v66,v67\nC v66,v68,v69\nM v[248:251],a[216:219],v[120:123],v[248:251]\nv_exp_f32 v81,v81\nM v[252:255],a[220:223],v[120:123],v[252:255]\nbuffer_load_dwordx4 v22,s[40:43],s48 offen offset:1024 sc1 lds\nM v[220:223],a[220:223],v[112:115],v[220:223]\n" \
"s_waitcnt lgkmcnt(0)\nC v67,v70,v71\nC v72,v72,v73\nM v[188:191],a[220:223],v[104:107],v[188:191]\ns_cmp_eq_u32 s52,s56\nv_exp_f32 v82,v82\nM v[156:159],a[220:223],v[96:99],v[156:159]\ns_cbranch_scc0 LEVR1_%=\n" \
"s_branch LEV_%=\nLEVR8_%=:\nds_read_b128 a[208:211],v13 offset:50176\nds_read_b128 a[212:215],v13 offset:52224\nM v[96:99],a[192:195],a[128:131],v[32:35]\nbuffer_load_dwordx4 v22,s[40:43],s48 offen offset:2048 sc1 lds\nM v[96:99],a[196:199],a[132:135],v[96:99]\ns_lshl_b32 s58,%2,2\n" \
"v_exp_f32 v83,v83\nM v[16:19],a[224:227],v[64:67],v[24:27]\ns_lshl_b32 s59,%2,1\nds_read_b128 a[216:219],v13 offset:54272\nC v73,v74,v75\nM v[96:99],a[200:203],a[136:139],v[96:99]\nds_read_b128 a[220:223],v13 offset:56320\nC v74,v76,v77\n" \
"M v[96:99],a[204:207],a[140:143],v[96:99]\nbuffer_load_dwordx4 v22,s[40:43],s48 offen offset:3072 sc1 lds\nM v[104:107],a[192:195],a[144:147],v[36:39]\ns_add_u32 m0,s22,49152\nC v75,v78,v79\nC v80,v80,v81\nM v[104:107],a[196:199],a[148:151],v[104:107]\ns_add_u32 s48,s48,s49\n" \
"v_exp_f32 v84,v84\nM v[16:19],a[228:231],v[72:75],v[16:19]\nv_exp_f32 v85,v85\nM v[104:107],a[200:203],a[152:155],v[104:107]\nv_exp_f32 v86,v86\nM v[104:107],a[204:207],a[156:159],v[104:107]\nv_exp_f32 v87,v87\nM v[112:115],a[192:195],a[160:163],v[40:43]\n" \
"v_exp_f32 v88,v88\nM v[112:115],a[196:199],a[164:167],v[112:115]\nC v81,v82,v83\nC v82,v84,v85\nM v[112:115],a[200:203],a[168:171],v[112:115]\nv_exp_f32 v89,v89\nM v[112:115],a[204:207],a[172:175],v[112:115]\nC v83,v86,v87\n" \
"C v88,v88,v89\nM v[120:123],a[192:195],a[176:179],v[44:47]\nv_exp_f32 v90,v90\nM v[120:123],a[196:199],a[180:183],v[120:123]\nv_exp_f32 v91,v91\nM v[120:123],a[200:203],a[184:187],v[120:123]\nv_exp_f32 v92,v92\nM v[120:123],a[204:207],a[188:191],v[120:123]\n" \
"v_exp_f32 v93,v93\nM v[16:19],a[232:235],v[80:83],v[16:19]\ns_waitcnt lgkmcnt(0)\nv_exp_f32 v94,v94\nM v[100:103],a[208:211],a[128:131],v[32:35]\nC v89,v90,v91\nv_xor_b32 v14,0x10000,v14\nM v[100:103],a[212:215],a[132:135],v[100:103]\n" \
"ds_read_b128 a[192:195],v14 offset:0\nds_read_b128 a[196:199],v14 offset:1024\nM v[100:103],a[216:219],a[136:139],v[100:103]\nv_exp_f32 v95,v95\nM v[100:103],a[220:223],a[140:143],v[100:103]\nC v90,v92,v93\nC v91,v94,v95\nM v[108:111],a[208:211],a[144:147],v[36:39]\n" \
"ds_read_b128 a[200:203],v14 offset:2048\nv_bfe_u32 v52,v4,6,2\nM v[108:111],a[212:215],a[148:151],v[108:111]\nv_add_u32 v52,s58,v52\nv_lshrrev_b32 v5,1,v4\nM v[108:111],a[216:219],a[152:155],v[108:111]\ns_lshl_b32 s58,%2,9\nds_read_b128 a[204:207],v14 offset:3072\n" \
"v_xor_b32 v13,0x10000,v13\nM v[108:111],a[220:223],a[156:159],v[108:111]\ns_add_u32 s58,s58,94375936\nv_add_u32 v5,s58,v5\nv_bfe_u32 v53,v4,8,1\nM v[16:19],a[236:239],v[88:91],v[16:19]\ns_lshl_b32 s58,%2,9\nv_lshl_add_u32 v52,v53,4,v52\n" \
"v_add_u32 v53,s59,v53\nM v[116:119],a[208:211],a[160:163],v[40:43]\nv_lshrrev_b32 v54,4,v4\nv_xor_b32 v53,v53,v54\nM v[116:119],a[212:215],a[164:167],v[116:119]\nbuffer_load_dwordx2 v[10:11],v5,s[36:39],0 offen sc1\nM v[116:119],a[216:219],a[168:171],v[116:119]\nv_max3_f32 v15,v16,v17,v18\n" \
"v_max_f32 v15,v15,v19\nM v[116:119],a[220:223],a[172:175],v[116:119]\nv_cmp_nge_f32 vcc,0x53800000,v15\nv_lshlrev_b32 v52,11,v52\nM v[124:127],a[208:211],a[176:179],v[44:47]\nds_read_b128 a[208:211],v14 offset:4096\nv_and_b32 v53,3,v53\nM v[124:127],a[212:215],a[180:183],v[124:127]\n" \
"s_cbranch_vccnz Lcs39_%=\nLcr40_%=:\nds_read_b128 a[212:215],v14 offset:5120\nv_lshrrev_b32 v54,9,v4\nM v[124:127],a[216:219],a[184:187],v[124:127]\nds_read_b128 a[216:219],v14 offset:6144\nv_lshl_add_u32 v53,v54,2,v53\nM v[124:127],a[220:223],a[188:191],v[124:127]\n" \
"s_waitcnt lgkmcnt(3)\nds_read_b128 a[220:223],v14 offset:7168\nv_lshl_add_u32 v1,v53,4,v52\nM v[128:131],a[192:195],v[64:67],v[128:131]\nv_exp_f32 v96,v96\nM v[160:163],a[192:195],v[72:75],v[160:163]\nv_exp_f32 v97,v97\nM v[192:195],a[192:195],v[80:83],v[192:195]\n" \
"v_exp_f32 v98,v98\nM v[224:227],a[192:195],v[88:91],v[224:227]\nv_exp_f32 v99,v99\nM v[228:231],a[196:199],v[88:91],v[228:231]\nv_exp_f32 v100,v100\nM v[196:199],a[196:199],v[80:83],v[196:199]\nv_exp_f32 v101,v101\nM v[164:167],a[196:199],v[72:75],v[164:167]\n" \
"s_waitcnt vmcnt(19) lgkmcnt(0)\nv_exp_f32 v102,v102\nM v[132:135],a[196:199],v[64:67],v[132:135]\ns_barrier\nds_read_b128 a[192:195],v13 offset:32768\nM v[136:139],a[200:203],v[64:67],v[136:139]\nbuffer_load_dwordx4 v1,s[84:87],s46 offen offset:0 lds\nM v[168:171],a[200:203],v[72:75],v[168:171]\n" \
"s_add_u32 m0,s22,50048\nv_exp_f32 v103,v103\nM v[200:203],a[200:203],v[80:83],v[200:203]\nv_exp_f32 v104,v104\nM v[232:235],a[200:203],v[88:91],v[232:235]\nv_exp_f32 v105,v105\nM v[236:239],a[204:207],v[88:91],v[236:239]\nbuffer_load_dwordx4 v1,s[84:87],s46 offen offset:128 lds\n" \
"M v[204:207],a[204:207],v[80:83],v[204:207]\ns_add_u32 m0,s22,51200\nv_exp_f32 v106,v106\nM v[172:175],a[204:207],v[72:75],v[172:175]\ns_mov_b32 s46,65536\nds_read_b128 a[196:199],v13 offset:34816\nds_read_b128 a[200:203],v13 offset:36864\nM v[140:143],a[204:207],v[64:67],v[140:143]\n" \
"v_exp_f32 v107,v107\nM v[144:147],a[208:211],v[64:67],v[144:147]\nbuffer_load_dwordx4 v1,s[84:87],s46 offen offset:0 lds\nM v[176:179],a[208:211],v[72:75],v[176:179]\ns_add_u32 m0,s22,52096\nds_read_b128 a[204:207],v13 offset:38912\nC v96,v96,v97\nM v[208:211],a[208:211],v[80:83],v[208:211]\n" \
"v_exp_f32 v108,v108\nM v[240:243],a[208:211],v[88:91],v[240:243]\nv_exp_f32 v109,v109\nM v[244:247],a[212:215],v[88:91],v[244:247]\nbuffer_load_dwordx4 v1,s[84:87],s46 offen offset:128 lds\nM v[212:215],a[212:215],v[80:83],v[212:215]\ns_add_u32 m0,s22,65536\nv_exp_f32 v110,v110\n" \
"M v[180:183],a[212:215],v[72:75],v[180:183]\ns_mov_b32 s46,131072\nv_exp_f32 v111,v111\nM v[148:151],a[212:215],v[64:67],v[148:151]\ns_and_b32 s61,%1,1\nv_exp_f32 v112,v112\nM v[152:155],a[216:219],v[64:67],v[152:155]\nbuffer_load_dwordx4 v1,s[84:87],s46 offen offset:0 lds\n" \
"M v[184:187],a[216:219],v[72:75],v[184:187]\ns_add_u32 m0,s22,66432\nv_exp_f32 v113,v113\nM v[216:219],a[216:219],v[80:83],v[216:219]\ns_lshl_b32 s61,s61,2\nv_exp_f32 v114,v114\nM v[248:251],a[216:219],v[88:91],v[248:251]\ns_bfe_u32 s60,%1,0x20001\n" \
"C v97,v98,v99\nC v98,v100,v101\nM v[252:255],a[220:223],v[88:91],v[252:255]\nbuffer_load_dwordx4 v1,s[84:87],s46 offen offset:128 lds\nM v[220:223],a[220:223],v[80:83],v[220:223]\ns_add_u32 m0,s22,67584\nv_exp_f32 v115,v115\nM v[188:191],a[220:223],v[72:75],v[188:191]\n" \
"s_mov_b32 s46,196608\nC v99,v102,v103\nC v104,v104,v105\nM v[156:159],a[220:223],v[64:67],v[156:159]\ns_cmp_eq_u32 s60,0\ns_cselect_b64 s[96:97],-1,0\ns_waitcnt lgkmcnt(0)\nv_exp_f32 v116,v116\n" \
"M v[64:67],a[192:195],a[128:131],v[32:35]\nbuffer_load_dwordx4 v1,s[84:87],s46 offen offset:0 lds\nM v[64:67],a[196:199],a[132:135],v[64:67]\ns_waitcnt vmcnt(7)\nv_cmp_gt_u64 s[98:99],s[14:15],v[10:11]\nv_cmp_le_u64 s[18:19],s[16:17],v[10:11]\nM v[24:27],a[224:227],v[96:99],v[16:19]\ns_or_b64 s[98:99],s[98:99],s[18:19]\n" \
"s_cbranch_scc1 LPZ_%=\nLPZR_%=:\nds_read_b128 a[208:211],v13 offset:33792\nds_read_b128 a[212:215],v13 offset:35840\nM v[64:67],a[200:203],a[136:139],v[64:67]\nLG17_%=:\ns_cbranch_execz LHR2_%=\ns_add_u32 m0,s22,68480\n" \
"ds_read_b128 a[216:219],v13 offset:37888\nC v105,v106,v107\nM v[64:67],a[204:207],a[140:143],v[64:67]\nbuffer_load_dwordx4 v1,s[84:87],s46 offen offset:128 lds\nM v[72:75],a[192:195],a[144:147],v[36:39]\ns_add_u32 m0,s22,98304\nC v106,v108,v109\nC v107,v110,v111\n" \
"v_exp_f32 v117,v117\nM v[72:75],a[196:199],a[148:151],v[72:75]\ns_mov_b32 s46,262144\nds_read_b128 a[220:223],v13 offset:39936\nC v112,v112,v113\nM v[24:27],a[228:231],v[104:107],v[24:27]\ns_add_u32 s14,s14,3000\ns_addc_u32 s15,s15,0\n" \
"v_exp_f32 v118,v118\nM v[72:75],a[200:203],a[152:155],v[72:75]\ns_sub_u32 s73,s72,2048\nv_exp_f32 v119,v119\nM v[72:75],a[204:207],a[156:159],v[72:75]\ns_lshl_b32 s95,%1,2\nv_exp_f32 v120,v120\nM v[80:83],a[192:195],a[160:163],v[40:43]\n" \
"s_add_u32 s95,s95,%2\nv_exp_f32 v121,v121\nM v[80:83],a[196:199],a[164:167],v[80:83]\ns_lshl_b32 s95,s95,5\nC v113,v114,v115\nC v114,v116,v117\nM v[80:83],a[200:203],a[168:171],v[80:83]\ns_add_u32 s95,s95,94437376\n" \
"v_exp_f32 v122,v122\nM v[80:83],a[204:207],a[172:175],v[80:83]\nC v115,v118,v119\nC v120,v120,v121\nM v[88:91],a[192:195],a[176:179],v[44:47]\nv_exp_f32 v123,v123\nM v[88:91],a[196:199],a[180:183],v[88:91]\nv_exp_f32 v124,v124\n" \
"M v[88:91],a[200:203],a[184:187],v[88:91]\nv_exp_f32 v125,v125\nM v[88:91],a[204:207],a[188:191],v[88:91]\nds_read_b128 a[192:195],v14 offset:16384\nC v121,v122,v123\nM v[24:27],a[232:235],v[112:115],v[24:27]\ns_waitcnt lgkmcnt(1)\nv_exp_f32 v126,v126\n" \
"M v[68:71],a[208:211],a[128:131],v[32:35]\nds_read_b128 a[196:199],v14 offset:17408\nC v122,v124,v125\nM v[68:71],a[212:215],a[132:135],v[68:71]\nv_exp_f32 v127,v127\nM v[68:71],a[216:219],a[136:139],v[68:71]\nds_read_b128 a[200:203],v14 offset:18432\nC v123,v126,v127\n" \
"M v[68:71],a[220:223],a[140:143],v[68:71]\nds_read_b128 a[204:207],v14 offset:19456\nv_and_b32 v52,0xc0,v4\nM v[76:79],a[208:211],a[144:147],v[36:39]\nv_bfe_u32 v53,v4,4,1\nv_lshl_add_u32 v52,v53,3,v52\nM v[76:79],a[212:215],a[148:151],v[76:79]\nv_lshrrev_b32 v54,7,v4\n" \
"v_bfe_u32 v53,v4,5,1\nM v[76:79],a[216:219],a[152:155],v[76:79]\nv_and_b32 v54,2,v54\nv_xor_b32 v53,v53,v54\nM v[76:79],a[220:223],a[156:159],v[76:79]\nv_lshl_add_u32 v52,v53,4,v52\nv_lshrrev_b32 v53,8,v4\nM v[24:27],a[236:239],v[120:123],v[24:27]\n" \
"v_lshl_add_u32 v52,v53,12,v52\nv_add_u32 v6,s58,v52\nM v[84:87],a[208:211],a[160:163],v[40:43]\ns_bfe_u32 s58,%1,0x20003\nv_xor_b32 v7,0x110,v6\nv_xor_b32 v8,32,v6\nM v[84:87],a[212:215],a[164:167],v[84:87]\ns_add_u32 s61,s61,s58\n" \
"v_max3_f32 v15,v24,v25,v26\nv_max_f32 v15,v15,v27\nM v[84:87],a[216:219],a[168:171],v[84:87]\ns_and_b32 s58,s61,6\nv_cmp_nge_f32 vcc,0x53800000,v15\nv_xor_b32 v9,0x130,v6\nM v[84:87],a[220:223],a[172:175],v[84:87]\ns_add_u32 s58,s58,s61\n" \
"v_bfe_u32 v52,v4,7,2\nv_mov_b32 v48,-1\nM v[92:95],a[208:211],a[176:179],v[44:47]\ns_lshl_b32 s58,s58,2\nds_read_b128 a[208:211],v14 offset:20480\nv_mov_b32 v49,-1\nM v[92:95],a[212:215],a[180:183],v[92:95]\ns_cbranch_vccnz Lcs41_%=\n" \
"Lcr42_%=:\nds_read_b128 a[212:215],v14 offset:21504\nv_mov_b32 v50,-1\nM v[92:95],a[216:219],a[184:187],v[92:95]\ns_add_u32 s58,s58,s60\nds_read_b128 a[216:219],v14 offset:22528\nv_mov_b32 v51,-1\nM v[92:95],a[220:223],a[188:191],v[92:95]\n" \
"s_waitcnt lgkmcnt(3)\nbuffer_store_dwordx4 v[48:51],v4,s[36:39],s72 offen offset:0 sc1\nM v[128:131],a[192:195],v[96:99],v[128:131]\ns_lshl_b32 s58,s58,15\nds_read_b128 a[220:223],v14 offset:23552\nv_mov_b32 v53,s11\nM v[160:163],a[192:195],v[104:107],v[160:163]\ns_add_u32 s28,s58,50331648\n" \
"v_exp_f32 v64,v64\nM v[192:195],a[192:195],v[112:115],v[192:195]\ns_add_u32 s29,s58,52428800\nv_exp_f32 v65,v65\nM v[224:227],a[192:195],v[120:123],v[224:227]\ns_add_u32 s30,s58,54525952\nv_exp_f32 v66,v66\nM v[228:231],a[196:199],v[120:123],v[228:231]\n" \
"s_add_u32 s31,s58,56623104\nv_exp_f32 v67,v67\nM v[196:199],a[196:199],v[112:115],v[196:199]\ns_lshr_b32 s58,%2,1\nv_exp_f32 v68,v68\nM v[164:167],a[196:199],v[104:107],v[164:167]\ns_waitcnt vmcnt(20) lgkmcnt(0)\nv_exp_f32 v69,v69\n" \
"M v[132:135],a[196:199],v[96:99],v[132:135]\ns_lshl_b32 s58,s58,18\ns_barrier\nds_read_b128 a[192:195],v13 offset:49152\nM v[136:139],a[200:203],v[96:99],v[136:139]\nbuffer_store_dwordx4 v[48:51],v4,s[36:39],s72 offen offset:1024 sc1\nM v[168:171],a[200:203],v[104:107],v[168:171]\ns_add_u32 s47,s28,0\n" \
"v_exp_f32 v70,v70\nM v[200:203],a[200:203],v[112:115],v[200:203]\nv_exp_f32 v71,v71\nM v[232:235],a[200:203],v[120:123],v[232:235]\nv_exp_f32 v72,v72\nM v[236:239],a[204:207],v[120:123],v[236:239]\nv_exp_f32 v73,v73\nM v[204:207],a[204:207],v[112:115],v[204:207]\n" \
"v_exp_f32 v74,v74\nM v[172:175],a[204:207],v[104:107],v[172:175]\nds_read_b128 a[196:199],v13 offset:51200\nds_read_b128 a[200:203],v13 offset:53248\nM v[140:143],a[204:207],v[96:99],v[140:143]\nv_exp_f32 v75,v75\nM v[144:147],a[208:211],v[96:99],v[144:147]\nds_read_b128 a[204:207],v13 offset:55296\n" \
"C v64,v64,v65\nM v[176:179],a[208:211],v[104:107],v[176:179]\nv_exp_f32 v76,v76\nM v[208:211],a[208:211],v[112:115],v[208:211]\nv_exp_f32 v77,v77\nM v[240:243],a[208:211],v[120:123],v[240:243]\nbuffer_load_dwordx4 v22,s[40:43],s45 offen offset:0 sc1 lds\nM v[244:247],a[212:215],v[120:123],v[244:247]\n" \
"v_exp_f32 v78,v78\nM v[212:215],a[212:215],v[112:115],v[212:215]\nv_exp_f32 v79,v79\nM v[180:183],a[212:215],v[104:107],v[180:183]\ns_add_u32 s52,s52,1\nv_exp_f32 v80,v80\nM v[148:151],a[212:215],v[96:99],v[148:151]\nv_exp_f32 v81,v81\n" \
"M v[152:155],a[216:219],v[96:99],v[152:155]\nbuffer_load_dwordx4 v22,s[40:43],s45 offen offset:1024 sc1 lds\nM v[184:187],a[216:219],v[104:107],v[184:187]\nC v65,v66,v67\nC v66,v68,v69\nM v[216:219],a[216:219],v[112:115],v[216:219]\nv_exp_f32 v82,v82\nM v[248:251],a[216:219],v[120:123],v[248:251]\n" \
"C v67,v70,v71\nC v72,v72,v73\nM v[252:255],a[220:223],v[120:123],v[252:255]\nbuffer_load_dwordx4 v22,s[40:43],s45 offen offset:2048 sc1 lds\nM v[220:223],a[220:223],v[112:115],v[220:223]\ns_waitcnt lgkmcnt(0)\nv_exp_f32 v83,v83\nM v[188:191],a[220:223],v[104:107],v[188:191]\n" \
"s_cmp_eq_u32 s52,s56\nv_exp_f32 v84,v84\nM v[156:159],a[220:223],v[96:99],v[156:159]\ns_cbranch_scc1 LEV_%=\nLEVR9_%=:\nC v73,v74,v75\nC v74,v76,v77\nM v[96:99],a[192:195],a[128:131],v[32:35]\n" \
"ds_read_b128 a[208:211],v13 offset:50176\nds_read_b128 a[212:215],v13 offset:52224\nM v[96:99],a[196:199],a[132:135],v[96:99]\nbuffer_load_dwordx4 v22,s[40:43],s45 offen offset:3072 sc1 lds\nM v[16:19],a[224:227],v[64:67],v[24:27]\ns_add_u32 m0,s22,81920\nv_exp_f32 v85,v85\nM v[96:99],a[200:203],a[136:139],v[96:99]\n" \
"s_add_u32 s45,s45,s49\nds_read_b128 a[216:219],v13 offset:54272\nC v75,v78,v79\nM v[96:99],a[204:207],a[140:143],v[96:99]\nds_read_b128 a[220:223],v13 offset:56320\nC v80,v80,v81\nM v[104:107],a[192:195],a[144:147],v[36:39]\nbuffer_load_dwordx4 v22,s[40:43],s48 offen offset:0 sc1 lds\n" \
"M v[104:107],a[196:199],a[148:151],v[104:107]\nv_exp_f32 v86,v86\nM v[16:19],a[228:231],v[72:75],v[16:19]\nv_exp_f32 v87,v87\nM v[104:107],a[200:203],a[152:155],v[104:107]\nv_exp_f32 v88,v88\nM v[104:107],a[204:207],a[156:159],v[104:107]\nC v81,v82,v83\n" \
"C v82,v84,v85\nM v[112:115],a[192:195],a[160:163],v[40:43]\nbuffer_store_dwordx4 v[48:51],v4,s[36:39],s72 offen offset:2048 sc1\nM v[112:115],a[196:199],a[164:167],v[112:115]\nv_exp_f32 v89,v89\nM v[112:115],a[200:203],a[168:171],v[112:115]\nC v83,v86,v87\nC v88,v88,v89\n" \
"M v[112:115],a[204:207],a[172:175],v[112:115]\nv_exp_f32 v90,v90\nM v[120:123],a[192:195],a[176:179],v[44:47]\nv_exp_f32 v91,v91\nM v[120:123],a[196:199],a[180:183],v[120:123]\nv_exp_f32 v92,v92\nM v[120:123],a[200:203],a[184:187],v[120:123]\nv_exp_f32 v93,v93\n" \
"M v[120:123],a[204:207],a[188:191],v[120:123]\nv_exp_f32 v94,v94\nM v[16:19],a[232:235],v[80:83],v[16:19]\ns_waitcnt lgkmcnt(0)\nC v89,v90,v91\nC v90,v92,v93\nM v[100:103],a[208:211],a[128:131],v[32:35]\nv_exp_f32 v95,v95\n" \
"M v[100:103],a[212:215],a[132:135],v[100:103]\nv_xor_b32 v14,0x10000,v14\nC v91,v94,v95\nM v[100:103],a[216:219],a[136:139],v[100:103]\nds_read_b128 a[192:195],v14 offset:0\nds_read_b128 a[196:199],v14 offset:1024\nM v[100:103],a[220:223],a[140:143],v[100:103]\nds_read_b128 a[200:203],v14 offset:2048\n" \
"v_add_u32 v2,s58,v22\nM v[108:111],a[208:211],a[144:147],v[36:39]\nbuffer_store_dwordx4 v[48:51],v4,s[36:39],s72 offen offset:3072 sc1\nM v[108:111],a[212:215],a[148:151],v[108:111]\ns_lshl_b32 s58,s61,2\nds_read_b128 a[204:207],v14 offset:3072\nM v[108:111],a[216:219],a[152:155],v[108:111]\ns_add_u32 s58,s58,s60\n" \
"M v[108:111],a[220:223],a[156:159],v[108:111]\ns_lshl_b32 s58,s58,6\nM v[16:19],a[236:239],v[88:91],v[16:19]\ns_add_u32 s58,s58,94388224\nv_lshl_add_u32 v5,v52,11,s58\nv_bfe_u32 v52,v4,4,3\nM v[116:119],a[208:211],a[160:163],v[40:43]\ns_lshl_b32 s58,%2,13\n" \
"v_lshl_add_u32 v5,v52,3,v5\nv_mov_b32 v52,s10\nM v[116:119],a[212:215],a[164:167],v[116:119]\ns_add_u32 s58,s58,0x20000\nv_max3_f32 v15,v16,v17,v18\nv_max_f32 v15,v15,v19\nM v[116:119],a[216:219],a[168:171],v[116:119]\nv_cmp_nge_f32 vcc,0x53800000,v15\n" \
"v_add_u32 v12,s58,v4\nM v[116:119],a[220:223],a[172:175],v[116:119]\ns_lshl_b32 s58,s7,1\nv_xor_b32 v57,0x4000,v12\nM v[124:127],a[208:211],a[176:179],v[44:47]\ns_sub_u32 s58,15,s58\nds_read_b128 a[208:211],v14 offset:4096\nM v[124:127],a[212:215],a[180:183],v[124:127]\n" \
"s_cbranch_vccnz Lcs43_%=\nLcr44_%=:\nds_read_b128 a[212:215],v14 offset:5120\nM v[124:127],a[216:219],a[184:187],v[124:127]\ns_cmp_eq_u32 s52,s55\nds_read_b128 a[216:219],v14 offset:6144\nM v[124:127],a[220:223],a[188:191],v[124:127]\ns_cbranch_scc1 LH1_%=\n" \
"LH1R10_%=:\ns_waitcnt lgkmcnt(3)\nds_read_b128 a[220:223],v14 offset:7168\nv_xor_b32 v13,0x10000,v13\nM v[128:131],a[192:195],v[64:67],v[128:131]\nbuffer_store_dwordx4 v[48:51],v4,s[36:39],s73 offen offset:0 sc1\nM v[160:163],a[192:195],v[72:75],v[160:163]\ns_lshl_b32 s59,s58,20\n" \
"v_exp_f32 v96,v96\nM v[192:195],a[192:195],v[80:83],v[192:195]\ns_add_u32 s51,s68,s59\nv_exp_f32 v97,v97\nM v[224:227],a[192:195],v[88:91],v[224:227]\ns_lshl_b32 s59,s54,6\nv_exp_f32 v98,v98\nM v[228:231],a[196:199],v[88:91],v[228:231]\n" \
"buffer_load_dwordx4 v22,s[40:43],s48 offen offset:1024 sc1 lds\nM v[196:199],a[196:199],v[80:83],v[196:199]\ns_add_u32 s51,s51,s59\nv_exp_f32 v99,v99\nM v[164:167],a[196:199],v[72:75],v[164:167]\ns_waitcnt vmcnt(15) lgkmcnt(0)\nv_exp_f32 v100,v100\nM v[132:135],a[196:199],v[64:67],v[132:135]\n" \
"s_add_u32 s62,s51,0\ns_barrier\nds_read_b128 a[192:195],v13 offset:32768\nv_exp_f32 v101,v101\nM v[136:139],a[200:203],v[64:67],v[136:139]\ns_lshl_b32 s59,s58,10\nv_exp_f32 v102,v102\nM v[168:171],a[200:203],v[72:75],v[168:171]\n" \
"s_add_u32 s50,s69,s59\nv_exp_f32 v103,v103\nM v[200:203],a[200:203],v[80:83],v[200:203]\nbuffer_load_dwordx4 v22,s[40:43],s48 offen offset:2048 sc1 lds\nv_exp_f32 v104,v104\nM v[232:235],a[200:203],v[88:91],v[232:235]\ns_lshr_b32 s59,s54,4\nv_exp_f32 v105,v105\n" \
"M v[236:239],a[204:207],v[88:91],v[236:239]\ns_add_u32 s50,s50,s59\nv_exp_f32 v106,v106\nM v[204:207],a[204:207],v[80:83],v[204:207]\nv_exp_f32 v107,v107\nM v[172:175],a[204:207],v[72:75],v[172:175]\nbuffer_load_dwordx4 v22,s[40:43],s48 offen offset:3072 sc1 lds\nv_exp_f32 v108,v108\n" \
"M v[140:143],a[204:207],v[64:67],v[140:143]\ns_add_u32 m0,s22,114688\nv_exp_f32 v109,v109\nM v[144:147],a[208:211],v[64:67],v[144:147]\ns_add_u32 s48,s48,s49\nv_exp_f32 v110,v110\nM v[176:179],a[208:211],v[72:75],v[176:179]\nv_exp_f32 v111,v111\n" \
"M v[208:211],a[208:211],v[80:83],v[208:211]\nv_exp_f32 v112,v112\nM v[240:243],a[208:211],v[88:91],v[240:243]\nv_exp_f32 v113,v113\nM v[244:247],a[212:215],v[88:91],v[244:247]\nv_exp_f32 v114,v114\nM v[212:215],a[212:215],v[80:83],v[212:215]\nv_exp_f32 v115,v115\n" \
"M v[180:183],a[212:215],v[72:75],v[180:183]\nds_read_b128 a[196:199],v13 offset:34816\nds_read_b128 a[200:203],v13 offset:36864\nv_exp_f32 v116,v116\nM v[148:151],a[212:215],v[64:67],v[148:151]\nv_exp_f32 v117,v117\nM v[152:155],a[216:219],v[64:67],v[152:155]\nbuffer_load_dwordx4 v1,s[84:87],s46 offen offset:0 lds\n" \
"M v[184:187],a[216:219],v[72:75],v[184:187]\ns_add_u32 m0,s22,115584\nds_read_b128 a[204:207],v13 offset:38912\nC v96,v96,v97\nv_exp_f32 v118,v118\nM v[216:219],a[216:219],v[80:83],v[216:219]\nv_exp_f32 v119,v119\nM v[248:251],a[216:219],v[88:91],v[248:251]\n" \
"C v97,v98,v99\nC v104,v104,v105\nM v[252:255],a[220:223],v[88:91],v[252:255]\nbuffer_load_dwordx4 v1,s[84:87],s46 offen offset:128 lds\nM v[220:223],a[220:223],v[80:83],v[220:223]\ns_add_u32 m0,s22,116736\nds_read_b128 a[208:211],v13 offset:33792\nC v98,v100,v101\n" \
"M v[188:191],a[220:223],v[72:75],v[188:191]\ns_mov_b32 s46,327680\nds_read_b128 a[212:215],v13 offset:35840\nC v99,v102,v103\nM v[156:159],a[220:223],v[64:67],v[156:159]\ns_waitcnt lgkmcnt(2)\nv_exp_f32 v120,v120\nM v[64:67],a[192:195],a[128:131],v[32:35]\n" \
"buffer_load_dwordx4 v1,s[84:87],s46 offen offset:0 lds\nM v[64:67],a[196:199],a[132:135],v[64:67]\ns_add_u32 m0,s22,117632\nds_read_b128 a[216:219],v13 offset:37888\nC v105,v106,v107\nM v[24:27],a[224:227],v[96:99],v[16:19]\nds_read_b128 a[220:223],v13 offset:39936\nC v106,v108,v109\n" \
"M v[64:67],a[200:203],a[136:139],v[64:67]\nv_exp_f32 v121,v121\nM v[64:67],a[204:207],a[140:143],v[64:67]\nbuffer_load_dwordx4 v1,s[84:87],s46 offen offset:128 lds\nM v[72:75],a[192:195],a[144:147],v[36:39]\ns_add_u32 m0,s22,0\nC v107,v110,v111\nC v112,v112,v113\n" \
"M v[72:75],a[196:199],a[148:151],v[72:75]\ns_mov_b32 s46,393216\nv_exp_f32 v122,v122\nM v[24:27],a[228:231],v[104:107],v[24:27]\nv_exp_f32 v123,v123\nM v[72:75],a[200:203],a[152:155],v[72:75]\nbuffer_load_dwordx4 v22,s[40:43],s45 offen offset:0 sc1 lds\nM v[72:75],a[204:207],a[156:159],v[72:75]\n" \
"v_exp_f32 v124,v124\nM v[80:83],a[192:195],a[160:163],v[40:43]\nC v113,v114,v115\nC v114,v116,v117\nM v[80:83],a[196:199],a[164:167],v[80:83]\nC v115,v118,v119\nC v120,v120,v121\nM v[80:83],a[200:203],a[168:171],v[80:83]\n" \
"buffer_load_dwordx4 v22,s[40:43],s45 offen offset:1024 sc1 lds\nM v[80:83],a[204:207],a[172:175],v[80:83]\nv_exp_f32 v125,v125\nM v[88:91],a[192:195],a[176:179],v[44:47]\nds_read_b128 a[192:195],v14 offset:16384\nC v121,v122,v123\nM v[88:91],a[196:199],a[180:183],v[88:91]\nv_exp_f32 v126,v126\n" \
"M v[88:91],a[200:203],a[184:187],v[88:91]\nbuffer_load_dwordx4 v22,s[40:43],s45 offen offset:2048 sc1 lds\nM v[88:91],a[204:207],a[188:191],v[88:91]\nds_read_b128 a[196:199],v14 offset:17408\nC v122,v124,v125\nM v[24:27],a[232:235],v[112:115],v[24:27]\ns_waitcnt lgkmcnt(2)\nds_read_b128 a[200:203],v14 offset:18432\n" \
"v_xor_b32 v13,0x10000,v13\nM v[68:71],a[208:211],a[128:131],v[32:35]\nds_read_b64_tr_b16 a[0:1],v6 offset:49152\nM v[68:71],a[212:215],a[132:135],v[68:71]\nbuffer_load_dwordx4 v22,s[40:43],s45 offen offset:3072 sc1 lds\nM v[68:71],a[216:219],a[136:139],v[68:71]\ns_add_u32 m0,s22,32768\nds_read_b64_tr_b16 a[2:3],v7 offset:49152\n" \
"M v[68:71],a[220:223],a[140:143],v[68:71]\ns_add_u32 s45,s45,s49\nds_read_b64_tr_b16 a[4:5],v8 offset:49152\nM v[76:79],a[208:211],a[144:147],v[36:39]\nv_exp_f32 v127,v127\nM v[76:79],a[212:215],a[148:151],v[76:79]\nbuffer_store_dwordx4 v[48:51],v4,s[36:39],s73 offen offset:1024 sc1\nM v[76:79],a[216:219],a[152:155],v[76:79]\n" \
"ds_read_b64_tr_b16 a[6:7],v9 offset:49152\nC v123,v126,v127\nM v[76:79],a[220:223],a[156:159],v[76:79]\nds_read_b64_tr_b16 a[8:9],v6 offset:51200\nds_read_b64_tr_b16 a[10:11],v7 offset:51200\nM v[24:27],a[236:239],v[120:123],v[24:27]\nds_read_b64_tr_b16 a[12:13],v8 offset:51200\nv_xor_b32 v6,0x10000,v6\n" \
"M v[84:87],a[208:211],a[160:163],v[40:43]\nds_read_b64_tr_b16 a[14:15],v9 offset:51200\nds_read_b128 a[204:207],v14 offset:19456\nM v[84:87],a[212:215],a[164:167],v[84:87]\nv_max3_f32 v15,v24,v25,v26\nv_max_f32 v15,v15,v27\nM v[84:87],a[216:219],a[168:171],v[84:87]\nv_cmp_nge_f32 vcc,0x53800000,v15\n" \
"v_xor_b32 v7,0x10000,v7\nM v[84:87],a[220:223],a[172:175],v[84:87]\nv_xor_b32 v8,0x10000,v8\nv_xor_b32 v9,0x10000,v9\nM v[92:95],a[208:211],a[176:179],v[44:47]\nds_read_b128 a[208:211],v14 offset:20480\nM v[92:95],a[212:215],a[180:183],v[92:95]\ns_cbranch_vccnz Lcs45_%=\n" \
"Lcr46_%=:\nds_read_b128 a[212:215],v14 offset:21504\nM v[92:95],a[216:219],a[184:187],v[92:95]\nds_read_b128 a[216:219],v14 offset:22528\nM v[92:95],a[220:223],a[188:191],v[92:95]\ns_waitcnt lgkmcnt(3)\nds_read_b128 a[220:223],v14 offset:23552\nM v[128:131],a[192:195],v[96:99],v[128:131]\n" \
"v_exp_f32 v64,v64\nM v[160:163],a[192:195],v[104:107],v[160:163]\nv_exp_f32 v65,v65\nM v[192:195],a[192:195],v[112:115],v[192:195]\nv_exp_f32 v66,v66\nM v[224:227],a[192:195],v[120:123],v[224:227]\nv_exp_f32 v67,v67\nM v[228:231],a[196:199],v[120:123],v[228:231]\n" \
"v_exp_f32 v68,v68\nM v[196:199],a[196:199],v[112:115],v[196:199]\nv_exp_f32 v69,v69\nM v[164:167],a[196:199],v[104:107],v[164:167]\ns_waitcnt vmcnt(9) lgkmcnt(0)\nv_exp_f32 v70,v70\nM v[132:135],a[196:199],v[96:99],v[132:135]\ns_barrier\n" \
"buffer_load_dwordx4 v22,s[40:43],s48 offen offset:0 sc1 lds\nM v[136:139],a[200:203],v[96:99],v[136:139]\nv_exp_f32 v71,v71\nM v[168:171],a[200:203],v[104:107],v[168:171]\nv_exp_f32 v72,v72\nM v[200:203],a[200:203],v[112:115],v[200:203]\nv_exp_f32 v73,v73\nM v[232:235],a[200:203],v[120:123],v[232:235]\n" \
"buffer_load_dwordx4 v22,s[40:43],s48 offen offset:1024 sc1 lds\nM v[236:239],a[204:207],v[120:123],v[236:239]\nds_read_b128 a[192:195],v13 offset:32768\nC v64,v64,v65\nM v[204:207],a[204:207],v[112:115],v[204:207]\nv_exp_f32 v74,v74\nM v[172:175],a[204:207],v[104:107],v[172:175]\nv_exp_f32 v75,v75\n" \
"M v[140:143],a[204:207],v[96:99],v[140:143]\nbuffer_load_dwordx4 v22,s[40:43],s48 offen offset:2048 sc1 lds\nv_exp_f32 v76,v76\nM v[144:147],a[208:211],v[96:99],v[144:147]\nv_exp_f32 v77,v77\nM v[176:179],a[208:211],v[104:107],v[176:179]\nds_read_b128 a[196:199],v13 offset:34816\nds_read_b128 a[200:203],v13 offset:36864\n" \
"M v[208:211],a[208:211],v[112:115],v[208:211]\nv_exp_f32 v78,v78\nM v[240:243],a[208:211],v[120:123],v[240:243]\nbuffer_load_dwordx4 v22,s[40:43],s48 offen offset:3072 sc1 lds\nv_exp_f32 v79,v79\nM v[244:247],a[212:215],v[120:123],v[244:247]\ns_add_u32 m0,s22,16384\nv_exp_f32 v80,v80\n" \
"M v[212:215],a[212:215],v[112:115],v[212:215]\ns_add_u32 s48,s48,s49\nds_read_b128 a[204:207],v13 offset:38912\nC v65,v66,v67\nM v[180:183],a[212:215],v[104:107],v[180:183]\ns_add_u32 s52,s52,1\nv_exp_f32 v81,v81\nM v[148:151],a[212:215],v[96:99],v[148:151]\n" \
"buffer_load_dwordx4 v1,s[84:87],s46 offen offset:0 lds\nM v[152:155],a[216:219],v[96:99],v[152:155]\ns_add_u32 m0,s22,17280\nv_exp_f32 v82,v82\nM v[184:187],a[216:219],v[104:107],v[184:187]\nv_exp_f32 v83,v83\nM v[216:219],a[216:219],v[112:115],v[216:219]\nv_exp_f32 v84,v84\n" \
"M v[248:251],a[216:219],v[120:123],v[248:251]\nbuffer_load_dwordx4 v1,s[84:87],s46 offen offset:128 lds\nM v[252:255],a[220:223],v[120:123],v[252:255]\ns_add_u32 m0,s22,18432\nv_exp_f32 v85,v85\nM v[220:223],a[220:223],v[112:115],v[220:223]\ns_mov_b32 s46,458752\nv_exp_f32 v86,v86\n" \
"M v[188:191],a[220:223],v[104:107],v[188:191]\ns_cmp_eq_u32 s52,s56\nC v66,v68,v69\nC v67,v70,v71\nM v[156:159],a[220:223],v[96:99],v[156:159]\ns_waitcnt lgkmcnt(0)\nbuffer_load_dwordx4 v1,s[84:87],s46 offen offset:0 lds\nM v[96:99],a[192:195],a[128:131],v[32:35]\n" \
"s_cbranch_scc1 LEV_%=\nLEVR10_%=:\nds_read_b128 a[208:211],v13 offset:33792\nC v72,v72,v73\nM v[96:99],a[196:199],a[132:135],v[96:99]\ns_add_u32 m0,s22,19328\nds_read_b128 a[212:215],v13 offset:35840\nC v73,v74,v75\n" \
"M v[16:19],a[224:227],v[64:67],v[24:27]\nds_read_b128 a[216:219],v13 offset:37888\nC v74,v76,v77\nM v[96:99],a[200:203],a[136:139],v[96:99]\nv_exp_f32 v87,v87\nM v[96:99],a[204:207],a[140:143],v[96:99]\nbuffer_load_dwordx4 v1,s[84:87],s46 offen offset:128 lds\nM v[104:107],a[192:195],a[144:147],v[36:39]\n" \
"s_add_u32 m0,s22,49152\nds_read_b128 a[220:223],v13 offset:39936\nC v75,v78,v79\nv_exp_f32 v88,v88\nM v[104:107],a[196:199],a[148:151],v[104:107]\ns_mov_b32 s46,524288\nv_exp_f32 v89,v89\nM v[16:19],a[228:231],v[72:75],v[16:19]\n" \
"v_exp_f32 v90,v90\nM v[104:107],a[200:203],a[152:155],v[104:107]\nbuffer_load_dwordx4 v22,s[40:43],s45 offen offset:0 sc1 lds\nM v[104:107],a[204:207],a[156:159],v[104:107]\nv_exp_f32 v91,v91\nM v[112:115],a[192:195],a[160:163],v[40:43]\nv_exp_f32 v92,v92\nM v[112:115],a[196:199],a[164:167],v[112:115]\n" \
"C v80,v80,v81\nC v81,v82,v83\nM v[112:115],a[200:203],a[168:171],v[112:115]\nbuffer_load_dwordx4 v22,s[40:43],s45 offen offset:1024 sc1 lds\nM v[112:115],a[204:207],a[172:175],v[112:115]\nv_exp_f32 v93,v93\nM v[120:123],a[192:195],a[176:179],v[44:47]\nC v82,v84,v85\n" \
"C v83,v86,v87\nM v[120:123],a[196:199],a[180:183],v[120:123]\nv_exp_f32 v94,v94\nM v[120:123],a[200:203],a[184:187],v[120:123]\nbuffer_load_dwordx4 v22,s[40:43],s45 offen offset:2048 sc1 lds\nM v[120:123],a[204:207],a[188:191],v[120:123]\nLG18_%=:\ns_cbranch_execz LHR2_%=\n" \
"C v88,v88,v89\nC v89,v90,v91\nM v[16:19],a[232:235],v[80:83],v[16:19]\ns_waitcnt lgkmcnt(0)\nds_read_b64_tr_b16 a[16:17],v6 offset:0\nds_read_b64_tr_b16 a[18:19],v7 offset:0\nM v[100:103],a[208:211],a[128:131],v[32:35]\nds_read_b64_tr_b16 a[20:21],v8 offset:0\n" \
"C v90,v92,v93\nM v[100:103],a[212:215],a[132:135],v[100:103]\nbuffer_load_dwordx4 v22,s[40:43],s45 offen offset:3072 sc1 lds\nM v[100:103],a[216:219],a[136:139],v[100:103]\ns_add_u32 m0,s22,65536\nv_exp_f32 v95,v95\nM v[100:103],a[220:223],a[140:143],v[100:103]\ns_add_u32 s45,s45,s49\n" \
"ds_read_b64_tr_b16 a[22:23],v9 offset:0\nds_read_b64_tr_b16 a[24:25],v6 offset:2048\nM v[108:111],a[208:211],a[144:147],v[36:39]\nds_read_b64_tr_b16 a[26:27],v7 offset:2048\nv_xor_b32 v14,0x10000,v14\nM v[108:111],a[212:215],a[148:151],v[108:111]\nds_read_b128 a[192:195],v14 offset:16384\nC v91,v94,v95\n" \
"M v[108:111],a[216:219],a[152:155],v[108:111]\nds_read_b128 a[196:199],v14 offset:17408\nv_xor_b32 v13,0x10000,v13\nM v[108:111],a[220:223],a[156:159],v[108:111]\nds_read_b128 a[200:203],v14 offset:18432\nM v[16:19],a[236:239],v[88:91],v[16:19]\nds_read_b64_tr_b16 a[28:29],v8 offset:2048\nds_read_b64_tr_b16 a[30:31],v9 offset:2048\n" \
"M v[116:119],a[208:211],a[160:163],v[40:43]\nds_read_b128 a[204:207],v14 offset:19456\nM v[116:119],a[212:215],a[164:167],v[116:119]\nv_max3_f32 v15,v16,v17,v18\nv_max_f32 v15,v15,v19\nM v[116:119],a[216:219],a[168:171],v[116:119]\nv_cmp_nge_f32 vcc,0x53800000,v15\nM v[116:119],a[220:223],a[172:175],v[116:119]\n" \
"M v[124:127],a[208:211],a[176:179],v[44:47]\nds_read_b128 a[208:211],v14 offset:20480\nM v[124:127],a[212:215],a[180:183],v[124:127]\ns_cbranch_vccnz Lcs47_%=\nLcr48_%=:\nds_read_b128 a[212:215],v14 offset:21504\nM v[124:127],a[216:219],a[184:187],v[124:127]\nds_read_b128 a[216:219],v14 offset:22528\n" \
"M v[124:127],a[220:223],a[188:191],v[124:127]\ns_waitcnt lgkmcnt(3)\nds_read_b128 a[220:223],v14 offset:23552\nv_xor_b32 v14,0x10000,v14\nM v[128:131],a[192:195],v[64:67],v[128:131]\nv_exp_f32 v96,v96\nM v[160:163],a[192:195],v[72:75],v[160:163]\nv_exp_f32 v97,v97\n" \
"M v[192:195],a[192:195],v[80:83],v[192:195]\nv_exp_f32 v98,v98\nM v[224:227],a[192:195],v[88:91],v[224:227]\nv_exp_f32 v99,v99\nM v[228:231],a[196:199],v[88:91],v[228:231]\nv_exp_f32 v100,v100\nM v[196:199],a[196:199],v[80:83],v[196:199]\nv_exp_f32 v101,v101\n" \
"M v[164:167],a[196:199],v[72:75],v[164:167]\ns_waitcnt vmcnt(8) lgkmcnt(0)\nv_exp_f32 v102,v102\nM v[132:135],a[196:199],v[64:67],v[132:135]\ns_barrier\nbuffer_load_dwordx4 v22,s[40:43],s48 offen offset:0 sc1 lds\nM v[136:139],a[200:203],v[64:67],v[136:139]\nv_exp_f32 v103,v103\n" \
"M v[168:171],a[200:203],v[72:75],v[168:171]\nv_exp_f32 v104,v104\nM v[200:203],a[200:203],v[80:83],v[200:203]\nv_exp_f32 v105,v105\nM v[232:235],a[200:203],v[88:91],v[232:235]\nbuffer_load_dwordx4 v22,s[40:43],s48 offen offset:1024 sc1 lds\nM v[236:239],a[204:207],v[88:91],v[236:239]\nds_read_b128 a[192:195],v13 offset:0\n" \
"C v96,v96,v97\nM v[204:207],a[204:207],v[80:83],v[204:207]\nv_exp_f32 v106,v106\nM v[172:175],a[204:207],v[72:75],v[172:175]\nds_read_b128 a[196:199],v13 offset:2048\nds_read_b128 a[200:203],v13 offset:4096\nM v[140:143],a[204:207],v[64:67],v[140:143]\nbuffer_load_dwordx4 v22,s[40:43],s48 offen offset:2048 sc1 lds\n" \
"M v[144:147],a[208:211],v[64:67],v[144:147]\nv_exp_f32 v107,v107\nM v[176:179],a[208:211],v[72:75],v[176:179]\nds_read_b128 a[204:207],v13 offset:6144\nC v97,v98,v99\nM v[208:211],a[208:211],v[80:83],v[208:211]\nv_exp_f32 v108,v108\nM v[240:243],a[208:211],v[88:91],v[240:243]\n" \
"buffer_load_dwordx4 v22,s[40:43],s48 offen offset:3072 sc1 lds\nv_exp_f32 v109,v109\nM v[244:247],a[212:215],v[88:91],v[244:247]\ns_add_u32 m0,s22,98304\nv_exp_f32 v110,v110\nM v[212:215],a[212:215],v[80:83],v[212:215]\ns_add_u32 s48,s48,s49\nv_exp_f32 v111,v111\n" \
"M v[180:183],a[212:215],v[72:75],v[180:183]\nv_exp_f32 v112,v112\nM v[148:151],a[212:215],v[64:67],v[148:151]\nbuffer_load_dwordx4 v1,s[84:87],s46 offen offset:0 lds\nM v[152:155],a[216:219],v[64:67],v[152:155]\ns_add_u32 m0,s22,99200\nv_exp_f32 v113,v113\nM v[184:187],a[216:219],v[72:75],v[184:187]\n" \
"v_exp_f32 v114,v114\nM v[216:219],a[216:219],v[80:83],v[216:219]\nv_exp_f32 v115,v115\nM v[248:251],a[216:219],v[88:91],v[248:251]\nbuffer_load_dwordx4 v1,s[84:87],s46 offen offset:128 lds\nM v[252:255],a[220:223],v[88:91],v[252:255]\ns_add_u32 m0,s22,100352\nv_exp_f32 v116,v116\n" \
"M v[220:223],a[220:223],v[80:83],v[220:223]\ns_mov_b32 s46,589824\nC v98,v100,v101\nC v99,v102,v103\nM v[188:191],a[220:223],v[72:75],v[188:191]\ns_waitcnt lgkmcnt(0)\nv_exp_f32 v117,v117\nM v[156:159],a[220:223],v[64:67],v[156:159]\n" \
"buffer_load_dwordx4 v1,s[84:87],s46 offen offset:0 lds\nM v[64:67],a[192:195],a[128:131],v[32:35]\ns_add_u32 m0,s22,101248\nds_read_b128 a[208:211],v13 offset:1024\nC v104,v104,v105\nM v[64:67],a[196:199],a[132:135],v[64:67]\nds_read_b128 a[212:215],v13 offset:3072\nC v105,v106,v107\n" \
"M v[24:27],a[224:227],v[96:99],v[16:19]\nds_read_b128 a[216:219],v13 offset:5120\nC v106,v108,v109\nM v[64:67],a[200:203],a[136:139],v[64:67]\nbuffer_load_dwordx4 v1,s[84:87],s46 offen offset:128 lds\nM v[64:67],a[204:207],a[140:143],v[64:67]\ns_add_u32 m0,s22,81920\nds_read_b128 a[220:223],v13 offset:7168\n" \
"C v107,v110,v111\nM v[72:75],a[192:195],a[144:147],v[36:39]\ns_mov_b32 s46,655360\nv_exp_f32 v118,v118\nM v[72:75],a[196:199],a[148:151],v[72:75]\nv_exp_f32 v119,v119\nM v[24:27],a[228:231],v[104:107],v[24:27]\nbuffer_load_dwordx4 v22,s[40:43],s45 offen offset:0 sc1 lds\n" \
"M v[72:75],a[200:203],a[152:155],v[72:75]\ns_waitcnt vmcnt(21)\nv_exp_f32 v120,v120\nM v[72:75],a[204:207],a[156:159],v[72:75]\nv_exp_f32 v121,v121\nM v[80:83],a[192:195],a[160:163],v[40:43]\nv_exp_f32 v122,v122\nM v[80:83],a[196:199],a[164:167],v[80:83]\n" \
"buffer_store_dwordx2 v[52:53],v56,s[36:39],s95 offen offset:24 sc1\nM v[80:83],a[200:203],a[168:171],v[80:83]\nC v112,v112,v113\nC v113,v114,v115\nM v[80:83],a[204:207],a[172:175],v[80:83]\nbuffer_load_dwordx4 v22,s[40:43],s45 offen offset:1024 sc1 lds\nM v[88:91],a[192:195],a[176:179],v[44:47]\nds_read_b128 a[192:195],v14 offset:32768\n" \
"C v114,v116,v117\nM v[88:91],a[196:199],a[180:183],v[88:91]\nv_exp_f32 v123,v123\nM v[88:91],a[200:203],a[184:187],v[88:91]\nds_read_b128 a[196:199],v14 offset:33792\nC v115,v118,v119\nM v[88:91],a[204:207],a[188:191],v[88:91]\nbuffer_load_dwordx4 v22,s[40:43],s45 offen offset:2048 sc1 lds\n" \
"M v[24:27],a[232:235],v[112:115],v[24:27]\ns_waitcnt lgkmcnt(2)\nds_read_b128 a[200:203],v14 offset:34816\nC v120,v120,v121\nM v[68:71],a[208:211],a[128:131],v[32:35]\nv_exp_f32 v124,v124\nM v[68:71],a[212:215],a[132:135],v[68:71]\nv_exp_f32 v125,v125\n" \
"M v[68:71],a[216:219],a[136:139],v[68:71]\nv_exp_f32 v126,v126\nM v[68:71],a[220:223],a[140:143],v[68:71]\nbuffer_load_dwordx4 v22,s[40:43],s45 offen offset:3072 sc1 lds\nM v[76:79],a[208:211],a[144:147],v[36:39]\ns_add_u32 m0,s22,114688\nds_read_b64_tr_b16 a[32:33],v6 offset:49152\nC v121,v122,v123\n" \
"M v[76:79],a[212:215],a[148:151],v[76:79]\ns_add_u32 s45,s45,s49\nv_exp_f32 v127,v127\nM v[76:79],a[216:219],a[152:155],v[76:79]\nC v122,v124,v125\nC v123,v126,v127\nM v[76:79],a[220:223],a[156:159],v[76:79]\nds_read_b64_tr_b16 a[34:35],v7 offset:49152\n" \
"ds_read_b64_tr_b16 a[36:37],v8 offset:49152\nM v[24:27],a[236:239],v[120:123],v[24:27]\nds_read_b64_tr_b16 a[38:39],v9 offset:49152\nM v[84:87],a[208:211],a[160:163],v[40:43]\nds_read_b64_tr_b16 a[40:41],v6 offset:51200\nds_read_b64_tr_b16 a[42:43],v7 offset:51200\nM v[84:87],a[212:215],a[164:167],v[84:87]\nds_read_b64_tr_b16 a[44:45],v8 offset:51200\n" \
"v_max3_f32 v15,v24,v25,v26\nM v[84:87],a[216:219],a[168:171],v[84:87]\nds_read_b64_tr_b16 a[46:47],v9 offset:51200\nv_max_f32 v15,v15,v27\nM v[84:87],a[220:223],a[172:175],v[84:87]\nds_read_b128 a[204:207],v14 offset:35840\nv_cmp_nge_f32 vcc,0x53800000,v15\nM v[92:95],a[208:211],a[176:179],v[44:47]\n" \
"ds_read_b128 a[208:211],v14 offset:36864\nv_xor_b32 v6,0x10000,v6\nM v[92:95],a[212:215],a[180:183],v[92:95]\ns_cbranch_vccnz Lcs49_%=\nLcr50_%=:\nds_read_b128 a[212:215],v14 offset:37888\nv_xor_b32 v7,0x10000,v7\nM v[92:95],a[216:219],a[184:187],v[92:95]\n" \
"ds_read_b128 a[216:219],v14 offset:38912\nv_xor_b32 v8,0x10000,v8\nM v[92:95],a[220:223],a[188:191],v[92:95]\ns_waitcnt lgkmcnt(5)\nds_read_b128 a[220:223],v14 offset:39936\nv_xor_b32 v9,0x10000,v9\nM v[128:131],a[192:195],v[96:99],v[128:131]\nv_exp_f32 v64,v64\n" \
"M v[160:163],a[192:195],v[104:107],v[160:163]\nv_exp_f32 v65,v65\nM v[192:195],a[192:195],v[112:115],v[192:195]\nv_exp_f32 v66,v66\nM v[224:227],a[192:195],v[120:123],v[224:227]\nv_exp_f32 v67,v67\nM v[228:231],a[196:199],v[120:123],v[228:231]\nv_exp_f32 v68,v68\n" \
"M v[196:199],a[196:199],v[112:115],v[196:199]\nv_exp_f32 v69,v69\nM v[164:167],a[196:199],v[104:107],v[164:167]\ns_waitcnt vmcnt(9) lgkmcnt(0)\nv_exp_f32 v70,v70\nM v[132:135],a[196:199],v[96:99],v[132:135]\ns_barrier\nbuffer_load_dwordx4 v22,s[40:43],s48 offen offset:0 sc1 lds\n" \
"M v[136:139],a[200:203],v[96:99],v[136:139]\nv_exp_f32 v71,v71\nM v[168:171],a[200:203],v[104:107],v[168:171]\nv_exp_f32 v72,v72\nM v[200:203],a[200:203],v[112:115],v[200:203]\nv_exp_f32 v73,v73\nM v[232:235],a[200:203],v[120:123],v[232:235]\nbuffer_load_dwordx4 v22,s[40:43],s48 offen offset:1024 sc1 lds\n" \
"M v[236:239],a[204:207],v[120:123],v[236:239]\nds_read_b128 a[192:195],v13 offset:49152\nC v64,v64,v65\nM v[204:207],a[204:207],v[112:115],v[204:207]\nv_exp_f32 v74,v74\nM v[172:175],a[204:207],v[104:107],v[172:175]\nds_read_b128 a[196:199],v13 offset:51200\nds_read_b128 a[200:203],v13 offset:53248\n" \
"M v[140:143],a[204:207],v[96:99],v[140:143]\nbuffer_load_dwordx4 v22,s[40:43],s48 offen offset:2048 sc1 lds\nv_exp_f32 v75,v75\nM v[144:147],a[208:211],v[96:99],v[144:147]\nv_exp_f32 v76,v76\nM v[176:179],a[208:211],v[104:107],v[176:179]\nds_read_b128 a[204:207],v13 offset:55296\nC v65,v66,v67\n" \
"M v[208:211],a[208:211],v[112:115],v[208:211]\nv_exp_f32 v77,v77\nM v[240:243],a[208:211],v[120:123],v[240:243]\nbuffer_load_dwordx4 v22,s[40:43],s48 offen offset:3072 sc1 lds\nv_exp_f32 v78,v78\nM v[244:247],a[212:215],v[120:123],v[244:247]\ns_add_u32 m0,s22,0\nv_exp_f32 v79,v79\n" \
"M v[212:215],a[212:215],v[112:115],v[212:215]\ns_add_u32 s48,s48,s49\nv_exp_f32 v80,v80\nM v[180:183],a[212:215],v[104:107],v[180:183]\ns_add_u32 s52,s52,1\nv_exp_f32 v81,v81\nM v[148:151],a[212:215],v[96:99],v[148:151]\nbuffer_load_dwordx4 v1,s[84:87],s46 offen offset:0 lds\n" \
"M v[152:155],a[216:219],v[96:99],v[152:155]\ns_add_u32 m0,s22,896\nv_exp_f32 v82,v82\nM v[184:187],a[216:219],v[104:107],v[184:187]\nv_exp_f32 v83,v83\nM v[216:219],a[216:219],v[112:115],v[216:219]\nv_exp_f32 v84,v84\nM v[248:251],a[216:219],v[120:123],v[248:251]\n" \
"buffer_load_dwordx4 v1,s[84:87],s46 offen offset:128 lds\nM v[252:255],a[220:223],v[120:123],v[252:255]\ns_add_u32 m0,s22,2048\nv_exp_f32 v85,v85\nM v[220:223],a[220:223],v[112:115],v[220:223]\ns_mov_b32 s46,720896\nv_exp_f32 v86,v86\nM v[188:191],a[220:223],v[104:107],v[188:191]\n" \
"s_cmp_eq_u32 s52,s56\nC v66,v68,v69\nC v67,v70,v71\nM v[156:159],a[220:223],v[96:99],v[156:159]\ns_waitcnt lgkmcnt(0)\nbuffer_load_dwordx4 v1,s[84:87],s46 offen offset:0 lds\nM v[96:99],a[192:195],a[128:131],v[32:35]\ns_cbranch_scc1 LEV_%=\n" \
"LEVR11_%=:\nds_read_b128 a[208:211],v13 offset:50176\nC v72,v72,v73\nM v[96:99],a[196:199],a[132:135],v[96:99]\ns_add_u32 m0,s22,2944\nds_read_b128 a[212:215],v13 offset:52224\nC v73,v74,v75\nM v[16:19],a[224:227],v[64:67],v[24:27]\n" \
"ds_read_b128 a[216:219],v13 offset:54272\nC v74,v76,v77\nM v[96:99],a[200:203],a[136:139],v[96:99]\nv_exp_f32 v87,v87\nM v[96:99],a[204:207],a[140:143],v[96:99]\nbuffer_load_dwordx4 v1,s[84:87],s46 offen offset:128 lds\nM v[104:107],a[192:195],a[144:147],v[36:39]\ns_add_u32 m0,s22,32768\n" \
"ds_read_b128 a[220:223],v13 offset:56320\nC v75,v78,v79\nv_exp_f32 v88,v88\nM v[104:107],a[196:199],a[148:151],v[104:107]\ns_mov_b32 s46,786432\nv_exp_f32 v89,v89\nM v[16:19],a[228:231],v[72:75],v[16:19]\nv_exp_f32 v90,v90\n" \
"M v[104:107],a[200:203],a[152:155],v[104:107]\nbuffer_load_dwordx4 v22,s[40:43],s45 offen offset:0 sc1 lds\nM v[104:107],a[204:207],a[156:159],v[104:107]\nv_exp_f32 v91,v91\nM v[112:115],a[192:195],a[160:163],v[40:43]\nv_exp_f32 v92,v92\nM v[112:115],a[196:199],a[164:167],v[112:115]\nC v80,v80,v81\n" \
"C v81,v82,v83\nM v[112:115],a[200:203],a[168:171],v[112:115]\nbuffer_load_dwordx4 v22,s[40:43],s45 offen offset:1024 sc1 lds\nM v[112:115],a[204:207],a[172:175],v[112:115]\nv_exp_f32 v93,v93\nM v[120:123],a[192:195],a[176:179],v[44:47]\nC v82,v84,v85\nC v83,v86,v87\n" \
"M v[120:123],a[196:199],a[180:183],v[120:123]\nv_exp_f32 v94,v94\nM v[120:123],a[200:203],a[184:187],v[120:123]\nbuffer_load_dwordx4 v22,s[40:43],s45 offen offset:2048 sc1 lds\nM v[120:123],a[204:207],a[188:191],v[120:123]\nC v88,v88,v89\nC v89,v90,v91\nM v[16:19],a[232:235],v[80:83],v[16:19]\n" \
"s_waitcnt lgkmcnt(0)\nds_read_b64_tr_b16 a[48:49],v6 offset:16384\nds_read_b64_tr_b16 a[50:51],v7 offset:16384\nM v[100:103],a[208:211],a[128:131],v[32:35]\nds_read_b64_tr_b16 a[52:53],v8 offset:16384\nC v90,v92,v93\nM v[100:103],a[212:215],a[132:135],v[100:103]\nbuffer_load_dwordx4 v22,s[40:43],s45 offen offset:3072 sc1 lds\n" \
"M v[100:103],a[216:219],a[136:139],v[100:103]\ns_add_u32 m0,s22,16384\nv_exp_f32 v95,v95\nM v[100:103],a[220:223],a[140:143],v[100:103]\ns_add_u32 s45,s45,s49\nds_read_b64_tr_b16 a[54:55],v9 offset:16384\nds_read_b64_tr_b16 a[56:57],v6 offset:18432\nM v[108:111],a[208:211],a[144:147],v[36:39]\n" \
"ds_read_b64_tr_b16 a[58:59],v7 offset:18432\nv_xor_b32 v14,0x10000,v14\nM v[108:111],a[212:215],a[148:151],v[108:111]\nds_read_b128 a[192:195],v14 offset:0\nC v91,v94,v95\nM v[108:111],a[216:219],a[152:155],v[108:111]\nds_read_b128 a[196:199],v14 offset:1024\nv_xor_b32 v6,0x10000,v6\n" \
"M v[108:111],a[220:223],a[156:159],v[108:111]\nds_read_b128 a[200:203],v14 offset:2048\nv_xor_b32 v7,0x10000,v7\nM v[16:19],a[236:239],v[88:91],v[16:19]\nds_read_b64_tr_b16 a[60:61],v8 offset:18432\nds_read_b64_tr_b16 a[62:63],v9 offset:18432\nM v[116:119],a[208:211],a[160:163],v[40:43]\nds_read_b128 a[204:207],v14 offset:3072\n" \
"v_xor_b32 v8,0x10000,v8\nM v[116:119],a[212:215],a[164:167],v[116:119]\nv_max3_f32 v15,v16,v17,v18\nv_max_f32 v15,v15,v19\nM v[116:119],a[216:219],a[168:171],v[116:119]\nv_cmp_nge_f32 vcc,0x53800000,v15\nv_xor_b32 v9,0x10000,v9\nM v[116:119],a[220:223],a[172:175],v[116:119]\n" \
"M v[124:127],a[208:211],a[176:179],v[44:47]\ns_waitcnt lgkmcnt(3)\nds_read_b128 a[208:211],v14 offset:4096\nM v[124:127],a[212:215],a[180:183],v[124:127]\ns_cbranch_vccnz Lcs51_%=\nLcr52_%=:\nds_read_b128 a[212:215],v14 offset:5120\nM v[124:127],a[216:219],a[184:187],v[124:127]\n" \
"s_cmp_eq_u32 s52,s55\nds_read_b128 a[216:219],v14 offset:6144\nM v[124:127],a[220:223],a[188:191],v[124:127]\ns_cbranch_scc1 LH1_%=\nLH1R12_%=:\nds_read_b128 a[220:223],v14 offset:7168\nv_xor_b32 v13,0x10000,v13\nM v[128:131],a[192:195],v[64:67],v[128:131]\n" \
"v_exp_f32 v96,v96\nM v[160:163],a[192:195],v[72:75],v[160:163]\nv_exp_f32 v97,v97\nM v[192:195],a[192:195],v[80:83],v[192:195]\nv_exp_f32 v98,v98\nM v[224:227],a[192:195],v[88:91],v[224:227]\nv_exp_f32 v99,v99\nM v[228:231],a[196:199],v[88:91],v[228:231]\n" \
"v_exp_f32 v100,v100\nM v[196:199],a[196:199],v[80:83],v[196:199]\nv_exp_f32 v101,v101\nM v[164:167],a[196:199],v[72:75],v[164:167]\ns_waitcnt vmcnt(8) lgkmcnt(0)\nv_exp_f32 v102,v102\nM v[132:135],a[196:199],v[64:67],v[132:135]\ns_barrier\n" \
"buffer_load_dwordx4 v22,s[40:43],s48 offen offset:0 sc1 lds\nM v[136:139],a[200:203],v[64:67],v[136:139]\nv_exp_f32 v103,v103\nM v[168:171],a[200:203],v[72:75],v[168:171]\nv_exp_f32 v104,v104\nM v[200:203],a[200:203],v[80:83],v[200:203]\nv_exp_f32 v105,v105\nM v[232:235],a[200:203],v[88:91],v[232:235]\n" \
"buffer_load_dwordx4 v22,s[40:43],s48 offen offset:1024 sc1 lds\nM v[236:239],a[204:207],v[88:91],v[236:239]\nds_read_b128 a[192:195],v13 offset:16384\nC v96,v96,v97\nM v[204:207],a[204:207],v[80:83],v[204:207]\nv_exp_f32 v106,v106\nM v[172:175],a[204:207],v[72:75],v[172:175]\nv_exp_f32 v107,v107\n" \
"M v[140:143],a[204:207],v[64:67],v[140:143]\nbuffer_load_dwordx4 v22,s[40:43],s48 offen offset:2048 sc1 lds\nv_exp_f32 v108,v108\nM v[144:147],a[208:211],v[64:67],v[144:147]\nv_exp_f32 v109,v109\nM v[176:179],a[208:211],v[72:75],v[176:179]\nds_read_b128 a[196:199],v13 offset:18432\nds_read_b128 a[200:203],v13 offset:20480\n" \
"M v[208:211],a[208:211],v[80:83],v[208:211]\nv_exp_f32 v110,v110\nM v[240:243],a[208:211],v[88:91],v[240:243]\nbuffer_load_dwordx4 v22,s[40:43],s48 offen offset:3072 sc1 lds\nv_exp_f32 v111,v111\nM v[244:247],a[212:215],v[88:91],v[244:247]\ns_add_u32 m0,s22,49152\nv_exp_f32 v112,v112\n" \
"M v[212:215],a[212:215],v[80:83],v[212:215]\ns_add_u32 s48,s48,s49\nds_read_b128 a[204:207],v13 offset:22528\nC v97,v98,v99\nM v[180:183],a[212:215],v[72:75],v[180:183]\nv_exp_f32 v113,v113\nM v[148:151],a[212:215],v[64:67],v[148:151]\nbuffer_load_dwordx4 v1,s[84:87],s46 offen offset:0 lds\n" \
"M v[152:155],a[216:219],v[64:67],v[152:155]\ns_add_u32 m0,s22,50048\nv_exp_f32 v114,v114\nM v[184:187],a[216:219],v[72:75],v[184:187]\nv_exp_f32 v115,v115\nM v[216:219],a[216:219],v[80:83],v[216:219]\nv_exp_f32 v116,v116\nM v[248:251],a[216:219],v[88:91],v[248:251]\n" \
"buffer_load_dwordx4 v1,s[84:87],s46 offen offset:128 lds\nM v[252:255],a[220:223],v[88:91],v[252:255]\ns_add_u32 m0,s22,51200\nv_exp_f32 v117,v117\nM v[220:223],a[220:223],v[80:83],v[220:223]\ns_mov_b32 s46,851968\nv_exp_f32 v118,v118\nM v[188:191],a[220:223],v[72:75],v[188:191]\n" \
"s_waitcnt lgkmcnt(0)\nC v98,v100,v101\nC v99,v102,v103\nM v[156:159],a[220:223],v[64:67],v[156:159]\nbuffer_load_dwordx4 v1,s[84:87],s46 offen offset:0 lds\nM v[64:67],a[192:195],a[128:131],v[32:35]\ns_add_u32 m0,s22,52096\nds_read_b128 a[208:211],v13 offset:17408\n" \
"C v104,v104,v105\nM v[64:67],a[196:199],a[132:135],v[64:67]\nds_read_b128 a[212:215],v13 offset:19456\nC v105,v106,v107\nM v[24:27],a[224:227],v[96:99],v[16:19]\nds_read_b128 a[216:219],v13 offset:21504\nC v106,v108,v109\nM v[64:67],a[200:203],a[136:139],v[64:67]\n" \
"v_exp_f32 v119,v119\nM v[64:67],a[204:207],a[140:143],v[64:67]\nbuffer_load_dwordx4 v1,s[84:87],s46 offen offset:128 lds\nM v[72:75],a[192:195],a[144:147],v[36:39]\ns_add_u32 m0,s22,65536\nds_read_b128 a[220:223],v13 offset:23552\nC v107,v110,v111\nv_exp_f32 v120,v120\n" \
"M v[72:75],a[196:199],a[148:151],v[72:75]\ns_mov_b32 s46,917504\nv_exp_f32 v121,v121\nM v[24:27],a[228:231],v[104:107],v[24:27]\nv_exp_f32 v122,v122\nM v[72:75],a[200:203],a[152:155],v[72:75]\nbuffer_load_dwordx4 v22,s[40:43],s45 offen offset:0 sc1 lds\nM v[72:75],a[204:207],a[156:159],v[72:75]\n" \
"v_exp_f32 v123,v123\nM v[80:83],a[192:195],a[160:163],v[40:43]\nv_exp_f32 v124,v124\nM v[80:83],a[196:199],a[164:167],v[80:83]\nC v112,v112,v113\nC v113,v114,v115\nM v[80:83],a[200:203],a[168:171],v[80:83]\nbuffer_load_dwordx4 v22,s[40:43],s45 offen offset:1024 sc1 lds\n" \
"M v[80:83],a[204:207],a[172:175],v[80:83]\nv_exp_f32 v125,v125\nM v[88:91],a[192:195],a[176:179],v[44:47]\nds_read_b128 a[192:195],v14 offset:49152\nC v114,v116,v117\nM v[88:91],a[196:199],a[180:183],v[88:91]\nC v115,v118,v119\nC v120,v120,v121\n" \
"M v[88:91],a[200:203],a[184:187],v[88:91]\nbuffer_load_dwordx4 v22,s[40:43],s45 offen offset:2048 sc1 lds\nM v[88:91],a[204:207],a[188:191],v[88:91]\nds_read_b128 a[196:199],v14 offset:50176\nC v121,v122,v123\nM v[24:27],a[232:235],v[112:115],v[24:27]\ns_waitcnt lgkmcnt(2)\nds_read_b128 a[200:203],v14 offset:51200\n" \
"C v122,v124,v125\nM v[68:71],a[208:211],a[128:131],v[32:35]\nv_exp_f32 v126,v126\nM v[68:71],a[212:215],a[132:135],v[68:71]\nbuffer_load_dwordx4 v22,s[40:43],s45 offen offset:3072 sc1 lds\nM v[68:71],a[216:219],a[136:139],v[68:71]\ns_add_u32 m0,s22,98304\nv_exp_f32 v127,v127\n" \
"M v[68:71],a[220:223],a[140:143],v[68:71]\ns_add_u32 s45,s45,s49\nds_read_b64_tr_b16 a[64:65],v6 offset:32768\nds_read_b64_tr_b16 a[66:67],v7 offset:32768\nM v[76:79],a[208:211],a[144:147],v[36:39]\nds_read_b64_tr_b16 a[68:69],v8 offset:32768\nC v123,v126,v127\nM v[76:79],a[212:215],a[148:151],v[76:79]\n" \
"ds_read_b64_tr_b16 a[70:71],v9 offset:32768\nds_read_b64_tr_b16 a[72:73],v6 offset:34816\nM v[76:79],a[216:219],a[152:155],v[76:79]\nds_read_b64_tr_b16 a[74:75],v7 offset:34816\nv_xor_b32 v13,0x10000,v13\nM v[76:79],a[220:223],a[156:159],v[76:79]\nds_read_b64_tr_b16 a[76:77],v8 offset:34816\nv_xor_b32 v6,0x10000,v6\n" \
"M v[24:27],a[236:239],v[120:123],v[24:27]\nds_read_b64_tr_b16 a[78:79],v9 offset:34816\nds_read_b128 a[204:207],v14 offset:52224\nM v[84:87],a[208:211],a[160:163],v[40:43]\nv_xor_b32 v7,0x10000,v7\nv_xor_b32 v8,0x10000,v8\nM v[84:87],a[212:215],a[164:167],v[84:87]\nv_max3_f32 v15,v24,v25,v26\n" \
"v_max_f32 v15,v15,v27\nM v[84:87],a[216:219],a[168:171],v[84:87]\nv_cmp_nge_f32 vcc,0x53800000,v15\nv_xor_b32 v9,0x10000,v9\nM v[84:87],a[220:223],a[172:175],v[84:87]\nM v[92:95],a[208:211],a[176:179],v[44:47]\nds_read_b128 a[208:211],v14 offset:53248\nM v[92:95],a[212:215],a[180:183],v[92:95]\n" \
"LG19_%=:\ns_cbranch_execz LHR2_%=\ns_cbranch_vccnz Lcs53_%=\nLcr54_%=:\nds_read_b128 a[212:215],v14 offset:54272\nM v[92:95],a[216:219],a[184:187],v[92:95]\nds_read_b128 a[216:219],v14 offset:55296\nM v[92:95],a[220:223],a[188:191],v[92:95]\n" \
"s_waitcnt lgkmcnt(3)\nds_read_b128 a[220:223],v14 offset:56320\nM v[128:131],a[192:195],v[96:99],v[128:131]\nv_exp_f32 v64,v64\nM v[160:163],a[192:195],v[104:107],v[160:163]\nv_exp_f32 v65,v65\nM v[192:195],a[192:195],v[112:115],v[192:195]\nv_exp_f32 v66,v66\n" \
"M v[224:227],a[192:195],v[120:123],v[224:227]\nv_exp_f32 v67,v67\nM v[228:231],a[196:199],v[120:123],v[228:231]\nv_exp_f32 v68,v68\nM v[196:199],a[196:199],v[112:115],v[196:199]\nv_exp_f32 v69,v69\nM v[164:167],a[196:199],v[104:107],v[164:167]\ns_waitcnt vmcnt(8) lgkmcnt(0)\n" \
"v_exp_f32 v70,v70\nM v[132:135],a[196:199],v[96:99],v[132:135]\ns_barrier\nbuffer_load_dwordx4 v22,s[40:43],s48 offen offset:0 sc1 lds\nM v[136:139],a[200:203],v[96:99],v[136:139]\nv_exp_f32 v71,v71\nM v[168:171],a[200:203],v[104:107],v[168:171]\nv_exp_f32 v72,v72\n" \
"M v[200:203],a[200:203],v[112:115],v[200:203]\nv_exp_f32 v73,v73\nM v[232:235],a[200:203],v[120:123],v[232:235]\nbuffer_load_dwordx4 v22,s[40:43],s48 offen offset:1024 sc1 lds\nv_exp_f32 v74,v74\nM v[236:239],a[204:207],v[120:123],v[236:239]\nds_read_b128 a[192:195],v13 offset:32768\nC v64,v64,v65\n" \
"M v[204:207],a[204:207],v[112:115],v[204:207]\nv_exp_f32 v75,v75\nM v[172:175],a[204:207],v[104:107],v[172:175]\nv_exp_f32 v76,v76\nM v[140:143],a[204:207],v[96:99],v[140:143]\nbuffer_load_dwordx4 v22,s[40:43],s48 offen offset:2048 sc1 lds\nv_exp_f32 v77,v77\nM v[144:147],a[208:211],v[96:99],v[144:147]\n" \
"v_exp_f32 v78,v78\nM v[176:179],a[208:211],v[104:107],v[176:179]\nds_read_b128 a[196:199],v13 offset:34816\nds_read_b128 a[200:203],v13 offset:36864\nM v[208:211],a[208:211],v[112:115],v[208:211]\nv_exp_f32 v79,v79\nM v[240:243],a[208:211],v[120:123],v[240:243]\nbuffer_load_dwordx4 v22,s[40:43],s48 offen offset:3072 sc1 lds\n" \
"v_exp_f32 v80,v80\nM v[244:247],a[212:215],v[120:123],v[244:247]\ns_add_u32 m0,s22,81920\nv_exp_f32 v81,v81\nM v[212:215],a[212:215],v[112:115],v[212:215]\ns_add_u32 s48,s48,s49\nds_read_b128 a[204:207],v13 offset:38912\nC v65,v66,v67\n" \
"M v[180:183],a[212:215],v[104:107],v[180:183]\ns_add_u32 s52,s52,1\nv_exp_f32 v82,v82\nM v[148:151],a[212:215],v[96:99],v[148:151]\nbuffer_load_dwordx4 v1,s[84:87],s46 offen offset:0 lds\nM v[152:155],a[216:219],v[96:99],v[152:155]\ns_add_u32 m0,s22,82816\nv_exp_f32 v83,v83\n" \
"M v[184:187],a[216:219],v[104:107],v[184:187]\nv_exp_f32 v84,v84\nM v[216:219],a[216:219],v[112:115],v[216:219]\nv_exp_f32 v85,v85\nM v[248:251],a[216:219],v[120:123],v[248:251]\nbuffer_load_dwordx4 v1,s[84:87],s46 offen offset:128 lds\nM v[252:255],a[220:223],v[120:123],v[252:255]\ns_add_u32 m0,s22,83968\n" \
"v_exp_f32 v86,v86\nM v[220:223],a[220:223],v[112:115],v[220:223]\ns_mov_b32 s46,983040\nv_exp_f32 v87,v87\nM v[188:191],a[220:223],v[104:107],v[188:191]\ns_cmp_eq_u32 s52,s56\nds_read_b128 a[208:211],v13 offset:33792\nC v66,v68,v69\n" \
"M v[156:159],a[220:223],v[96:99],v[156:159]\ns_waitcnt lgkmcnt(1)\nbuffer_load_dwordx4 v1,s[84:87],s46 offen offset:0 lds\nM v[96:99],a[192:195],a[128:131],v[32:35]\ns_cbranch_scc1 LEV_%=\nLEVR12_%=:\nC v67,v70,v71\nC v72,v72,v73\n" \
"M v[96:99],a[196:199],a[132:135],v[96:99]\ns_add_u32 m0,s22,84864\nds_read_b128 a[212:215],v13 offset:35840\nds_read_b128 a[216:219],v13 offset:37888\nM v[16:19],a[224:227],v[64:67],v[24:27]\nv_exp_f32 v88,v88\nM v[96:99],a[200:203],a[136:139],v[96:99]\nbuffer_load_dwordx4 v1,s[84:87],s46 offen offset:128 lds\n" \
"M v[96:99],a[204:207],a[140:143],v[96:99]\ns_add_u32 m0,s22,114688\nds_read_b128 a[220:223],v13 offset:39936\nC v73,v74,v75\nM v[104:107],a[192:195],a[144:147],v[36:39]\ns_mov_b32 s46,1048576\nC v74,v76,v77\nC v75,v78,v79\n" \
"v_exp_f32 v89,v89\nM v[104:107],a[196:199],a[148:151],v[104:107]\nv_exp_f32 v90,v90\nM v[16:19],a[228:231],v[72:75],v[16:19]\nbuffer_load_dwordx4 v22,s[40:43],s45 offen offset:0 sc1 lds\nM v[104:107],a[200:203],a[152:155],v[104:107]\nv_exp_f32 v91,v91\nM v[104:107],a[204:207],a[156:159],v[104:107]\n" \
"v_exp_f32 v92,v92\nM v[112:115],a[192:195],a[160:163],v[40:43]\nC v80,v80,v81\nC v81,v82,v83\nM v[112:115],a[196:199],a[164:167],v[112:115]\nbuffer_load_dwordx4 v22,s[40:43],s45 offen offset:1024 sc1 lds\nM v[112:115],a[200:203],a[168:171],v[112:115]\nv_exp_f32 v93,v93\n" \
"M v[112:115],a[204:207],a[172:175],v[112:115]\nC v82,v84,v85\nC v83,v86,v87\nM v[120:123],a[192:195],a[176:179],v[44:47]\nv_exp_f32 v94,v94\nM v[120:123],a[196:199],a[180:183],v[120:123]\nbuffer_load_dwordx4 v22,s[40:43],s45 offen offset:2048 sc1 lds\nM v[120:123],a[200:203],a[184:187],v[120:123]\n" \
"C v88,v88,v89\nC v89,v90,v91\nM v[120:123],a[204:207],a[188:191],v[120:123]\nds_read_b64_tr_b16 a[80:81],v6 offset:0\nv_xor_b32 v14,0x10000,v14\nM v[16:19],a[232:235],v[80:83],v[16:19]\ns_waitcnt lgkmcnt(1)\nds_read_b128 a[192:195],v14 offset:16384\n" \
"ds_read_b128 a[196:199],v14 offset:17408\nM v[100:103],a[208:211],a[128:131],v[32:35]\nbuffer_load_dwordx4 v22,s[40:43],s45 offen offset:3072 sc1 lds\nM v[100:103],a[212:215],a[132:135],v[100:103]\ns_add_u32 m0,s22,0\nv_exp_f32 v95,v95\nM v[100:103],a[216:219],a[136:139],v[100:103]\ns_add_u32 s45,s45,s49\n" \
"ds_read_b128 a[200:203],v14 offset:18432\nC v90,v92,v93\nM v[100:103],a[220:223],a[140:143],v[100:103]\nds_read_b64_tr_b16 a[82:83],v7 offset:0\nC v91,v94,v95\nM v[108:111],a[208:211],a[144:147],v[36:39]\nds_read_b64_tr_b16 a[84:85],v8 offset:0\nds_read_b64_tr_b16 a[86:87],v9 offset:0\n" \
"M v[108:111],a[212:215],a[148:151],v[108:111]\nds_read_b64_tr_b16 a[88:89],v6 offset:2048\nv_xor_b32 v13,0x10000,v13\nM v[108:111],a[216:219],a[152:155],v[108:111]\nds_read_b64_tr_b16 a[90:91],v7 offset:2048\nM v[108:111],a[220:223],a[156:159],v[108:111]\nds_read_b64_tr_b16 a[92:93],v8 offset:2048\nds_read_b64_tr_b16 a[94:95],v9 offset:2048\n" \
"M v[16:19],a[236:239],v[88:91],v[16:19]\nds_read_b128 a[204:207],v14 offset:19456\nM v[116:119],a[208:211],a[160:163],v[40:43]\nM v[116:119],a[212:215],a[164:167],v[116:119]\nv_max3_f32 v15,v16,v17,v18\nv_max_f32 v15,v15,v19\nM v[116:119],a[216:219],a[168:171],v[116:119]\nv_cmp_nge_f32 vcc,0x53800000,v15\n" \
"M v[116:119],a[220:223],a[172:175],v[116:119]\nM v[124:127],a[208:211],a[176:179],v[44:47]\nds_read_b128 a[208:211],v14 offset:20480\nM v[124:127],a[212:215],a[180:183],v[124:127]\ns_cbranch_vccnz Lcs55_%=\nLcr56_%=:\nds_read_b128 a[212:215],v14 offset:21504\nM v[124:127],a[216:219],a[184:187],v[124:127]\n" \
"ds_read_b128 a[216:219],v14 offset:22528\nM v[124:127],a[220:223],a[188:191],v[124:127]\ns_waitcnt lgkmcnt(3)\nds_read_b128 a[220:223],v14 offset:23552\nv_xor_b32 v14,0x10000,v14\nM v[128:131],a[192:195],v[64:67],v[128:131]\nv_exp_f32 v96,v96\nM v[160:163],a[192:195],v[72:75],v[160:163]\n" \
"v_exp_f32 v97,v97\nM v[192:195],a[192:195],v[80:83],v[192:195]\nv_exp_f32 v98,v98\nM v[224:227],a[192:195],v[88:91],v[224:227]\nv_exp_f32 v99,v99\nM v[228:231],a[196:199],v[88:91],v[228:231]\nv_exp_f32 v100,v100\nM v[196:199],a[196:199],v[80:83],v[196:199]\n" \
"v_exp_f32 v101,v101\nM v[164:167],a[196:199],v[72:75],v[164:167]\ns_waitcnt vmcnt(8) lgkmcnt(0)\nv_exp_f32 v102,v102\nM v[132:135],a[196:199],v[64:67],v[132:135]\ns_barrier\nbuffer_load_dwordx4 v22,s[40:43],s48 offen offset:0 sc1 lds\nM v[136:139],a[200:203],v[64:67],v[136:139]\n" \
"v_exp_f32 v103,v103\nM v[168:171],a[200:203],v[72:75],v[168:171]\nv_exp_f32 v104,v104\nM v[200:203],a[200:203],v[80:83],v[200:203]\nv_exp_f32 v105,v105\nM v[232:235],a[200:203],v[88:91],v[232:235]\nbuffer_load_dwordx4 v22,s[40:43],s48 offen offset:1024 sc1 lds\nM v[236:239],a[204:207],v[88:91],v[236:239]\n" \
"ds_read_b128 a[192:195],v13 offset:0\nC v96,v96,v97\nM v[204:207],a[204:207],v[80:83],v[204:207]\nv_exp_f32 v106,v106\nM v[172:175],a[204:207],v[72:75],v[172:175]\nv_exp_f32 v107,v107\nM v[140:143],a[204:207],v[64:67],v[140:143]\nbuffer_load_dwordx4 v22,s[40:43],s48 offen offset:2048 sc1 lds\n" \
"v_exp_f32 v108,v108\nM v[144:147],a[208:211],v[64:67],v[144:147]\nv_exp_f32 v109,v109\nM v[176:179],a[208:211],v[72:75],v[176:179]\nds_read_b128 a[196:199],v13 offset:2048\nds_read_b128 a[200:203],v13 offset:4096\nM v[208:211],a[208:211],v[80:83],v[208:211]\nv_exp_f32 v110,v110\n" \
"M v[240:243],a[208:211],v[88:91],v[240:243]\nbuffer_load_dwordx4 v22,s[40:43],s48 offen offset:3072 sc1 lds\nv_exp_f32 v111,v111\nM v[244:247],a[212:215],v[88:91],v[244:247]\ns_add_u32 m0,s22,32768\nv_exp_f32 v112,v112\nM v[212:215],a[212:215],v[80:83],v[212:215]\ns_add_u32 s48,s48,s49\n" \
"ds_read_b128 a[204:207],v13 offset:6144\nC v97,v98,v99\nM v[180:183],a[212:215],v[72:75],v[180:183]\nv_exp_f32 v113,v113\nM v[148:151],a[212:215],v[64:67],v[148:151]\nbuffer_load_dwordx4 v22,s[40:43],s45 offen offset:0 sc1 lds\nM v[152:155],a[216:219],v[64:67],v[152:155]\nv_exp_f32 v114,v114\n" \
"M v[184:187],a[216:219],v[72:75],v[184:187]\nv_exp_f32 v115,v115\nM v[216:219],a[216:219],v[80:83],v[216:219]\nv_exp_f32 v116,v116\nM v[248:251],a[216:219],v[88:91],v[248:251]\nbuffer_load_dwordx4 v22,s[40:43],s45 offen offset:1024 sc1 lds\nM v[252:255],a[220:223],v[88:91],v[252:255]\nv_exp_f32 v117,v117\n" \
"M v[220:223],a[220:223],v[80:83],v[220:223]\nC v98,v100,v101\nC v99,v102,v103\nM v[188:191],a[220:223],v[72:75],v[188:191]\ns_waitcnt lgkmcnt(0)\nv_exp_f32 v118,v118\nM v[156:159],a[220:223],v[64:67],v[156:159]\nbuffer_load_dwordx4 v22,s[40:43],s45 offen offset:2048 sc1 lds\n" \
"M v[64:67],a[192:195],a[128:131],v[32:35]\nds_read_b128 a[208:211],v13 offset:1024\nC v104,v104,v105\nM v[64:67],a[196:199],a[132:135],v[64:67]\nds_read_b128 a[212:215],v13 offset:3072\nC v105,v106,v107\nM v[24:27],a[224:227],v[96:99],v[16:19]\nds_read_b128 a[216:219],v13 offset:5120\n" \
"C v106,v108,v109\nM v[64:67],a[200:203],a[136:139],v[64:67]\nbuffer_load_dwordx2 v[10:11],v5,s[36:39],0 offen sc1\nM v[64:67],a[204:207],a[140:143],v[64:67]\nds_read_b128 a[220:223],v13 offset:7168\nC v107,v110,v111\nM v[72:75],a[192:195],a[144:147],v[36:39]\nbuffer_load_dwordx4 v22,s[40:43],s45 offen offset:3072 sc1 lds\n" \
"M v[72:75],a[196:199],a[148:151],v[72:75]\ns_add_u32 m0,s22,16384\nv_exp_f32 v119,v119\nM v[24:27],a[228:231],v[104:107],v[24:27]\ns_add_u32 s45,s45,s49\nv_exp_f32 v120,v120\nM v[72:75],a[200:203],a[152:155],v[72:75]\nv_exp_f32 v121,v121\n" \
"M v[72:75],a[204:207],a[156:159],v[72:75]\nbuffer_load_dwordx4 v22,s[40:43],s48 offen offset:0 sc1 lds\nM v[80:83],a[192:195],a[160:163],v[40:43]\nv_exp_f32 v122,v122\nM v[80:83],a[196:199],a[164:167],v[80:83]\nv_exp_f32 v123,v123\nM v[80:83],a[200:203],a[168:171],v[80:83]\nC v112,v112,v113\n" \
"C v113,v114,v115\nM v[80:83],a[204:207],a[172:175],v[80:83]\nbuffer_load_dwordx4 v22,s[40:43],s48 offen offset:1024 sc1 lds\nM v[88:91],a[192:195],a[176:179],v[44:47]\nds_read_b128 a[192:195],v14 offset:32768\nC v114,v116,v117\nM v[88:91],a[196:199],a[180:183],v[88:91]\nds_read_b128 a[196:199],v14 offset:33792\n" \
"C v115,v118,v119\nM v[88:91],a[200:203],a[184:187],v[88:91]\nv_exp_f32 v124,v124\nM v[88:91],a[204:207],a[188:191],v[88:91]\nbuffer_load_dwordx4 v22,s[40:43],s48 offen offset:2048 sc1 lds\nM v[24:27],a[232:235],v[112:115],v[24:27]\ns_waitcnt lgkmcnt(2)\nds_read_b64_tr_b16 a[96:97],v6 offset:49152\n" \
"C v120,v120,v121\nM v[68:71],a[208:211],a[128:131],v[32:35]\nv_exp_f32 v125,v125\nM v[68:71],a[212:215],a[132:135],v[68:71]\nds_read_b128 a[200:203],v14 offset:34816\nds_read_b64_tr_b16 a[98:99],v7 offset:49152\nM v[68:71],a[216:219],a[136:139],v[68:71]\nbuffer_load_dwordx4 v22,s[40:43],s48 offen offset:3072 sc1 lds\n" \
"M v[68:71],a[220:223],a[140:143],v[68:71]\ns_add_u32 m0,s22,49152\nv_exp_f32 v126,v126\nM v[76:79],a[208:211],a[144:147],v[36:39]\ns_add_u32 s48,s48,s49\nds_read_b64_tr_b16 a[100:101],v8 offset:49152\nds_read_b64_tr_b16 a[102:103],v9 offset:49152\nM v[76:79],a[212:215],a[148:151],v[76:79]\n" \
"v_exp_f32 v127,v127\nM v[76:79],a[216:219],a[152:155],v[76:79]\nC v121,v122,v123\nC v122,v124,v125\nC v123,v126,v127\nM v[76:79],a[220:223],a[156:159],v[76:79]\nds_read_b64_tr_b16 a[104:105],v6 offset:51200\nds_read_b64_tr_b16 a[106:107],v7 offset:51200\n" \
"M v[24:27],a[236:239],v[120:123],v[24:27]\nds_read_b64_tr_b16 a[108:109],v8 offset:51200\nv_xor_b32 v6,0x10000,v6\nM v[84:87],a[208:211],a[160:163],v[40:43]\nds_read_b64_tr_b16 a[110:111],v9 offset:51200\nds_read_b128 a[204:207],v14 offset:35840\nM v[84:87],a[212:215],a[164:167],v[84:87]\nv_max3_f32 v15,v24,v25,v26\n" \
"v_max_f32 v15,v15,v27\nM v[84:87],a[216:219],a[168:171],v[84:87]\nv_cmp_nge_f32 vcc,0x53800000,v15\nv_xor_b32 v7,0x10000,v7\nM v[84:87],a[220:223],a[172:175],v[84:87]\nv_xor_b32 v8,0x10000,v8\nv_xor_b32 v9,0x10000,v9\nM v[92:95],a[208:211],a[176:179],v[44:47]\n" \
"ds_read_b128 a[208:211],v14 offset:36864\nM v[92:95],a[212:215],a[180:183],v[92:95]\ns_cbranch_vccnz Lcs57_%=\nLcr58_%=:\nds_read_b128 a[212:215],v14 offset:37888\nM v[92:95],a[216:219],a[184:187],v[92:95]\nds_read_b128 a[216:219],v14 offset:38912\nM v[92:95],a[220:223],a[188:191],v[92:95]\n" \
"s_waitcnt lgkmcnt(3)\nds_read_b128 a[220:223],v14 offset:39936\nM v[128:131],a[192:195],v[96:99],v[128:131]\nv_exp_f32 v64,v64\nM v[160:163],a[192:195],v[104:107],v[160:163]\nv_exp_f32 v65,v65\nM v[192:195],a[192:195],v[112:115],v[192:195]\nv_exp_f32 v66,v66\n" \
"M v[224:227],a[192:195],v[120:123],v[224:227]\nv_exp_f32 v67,v67\nM v[228:231],a[196:199],v[120:123],v[228:231]\nv_exp_f32 v68,v68\nM v[196:199],a[196:199],v[112:115],v[196:199]\nv_exp_f32 v69,v69\nM v[164:167],a[196:199],v[104:107],v[164:167]\ns_waitcnt vmcnt(9) lgkmcnt(0)\n" \
"v_exp_f32 v70,v70\nM v[132:135],a[196:199],v[96:99],v[132:135]\ns_barrier\nds_read_b128 a[192:195],v13 offset:49152\nM v[136:139],a[200:203],v[96:99],v[136:139]\nv_exp_f32 v71,v71\nM v[168:171],a[200:203],v[104:107],v[168:171]\nv_exp_f32 v72,v72\n" \
"M v[200:203],a[200:203],v[112:115],v[200:203]\nv_exp_f32 v73,v73\nM v[232:235],a[200:203],v[120:123],v[232:235]\nbuffer_load_dwordx4 v22,s[40:43],s45 offen offset:0 sc1 lds\nM v[236:239],a[204:207],v[120:123],v[236:239]\nv_exp_f32 v74,v74\nM v[204:207],a[204:207],v[112:115],v[204:207]\nv_exp_f32 v75,v75\n" \
"M v[172:175],a[204:207],v[104:107],v[172:175]\nv_exp_f32 v76,v76\nM v[140:143],a[204:207],v[96:99],v[140:143]\nbuffer_load_dwordx4 v22,s[40:43],s45 offen offset:1024 sc1 lds\nM v[144:147],a[208:211],v[96:99],v[144:147]\nv_exp_f32 v77,v77\nM v[176:179],a[208:211],v[104:107],v[176:179]\ns_waitcnt vmcnt(7)\n" \
"v_cmp_gt_u64 s[98:99],s[14:15],v[10:11]\nv_cmp_le_u64 s[18:19],s[16:17],v[10:11]\nv_exp_f32 v78,v78\nM v[208:211],a[208:211],v[112:115],v[208:211]\ns_or_b64 s[98:99],s[98:99],s[18:19]\ns_and_b32 s98,s98,0xffff\ns_cbranch_scc1 LPE_%=\nLPER_%=:\n" \
"v_exp_f32 v79,v79\nM v[240:243],a[208:211],v[120:123],v[240:243]\nbuffer_load_dwordx4 v22,s[40:43],s45 offen offset:2048 sc1 lds\nM v[244:247],a[212:215],v[120:123],v[244:247]\nds_read_b128 a[196:199],v13 offset:51200\nC v64,v64,v65\nM v[212:215],a[212:215],v[112:115],v[212:215]\nv_exp_f32 v80,v80\n" \
"M v[180:183],a[212:215],v[104:107],v[180:183]\ns_add_u32 s52,s52,1\nv_exp_f32 v81,v81\nM v[148:151],a[212:215],v[96:99],v[148:151]\nbuffer_load_dwordx4 v22,s[40:43],s45 offen offset:3072 sc1 lds\nM v[152:155],a[216:219],v[96:99],v[152:155]\ns_add_u32 m0,s22,65536\nds_read_b128 a[200:203],v13 offset:53248\n" \
"C v65,v66,v67\nv_exp_f32 v82,v82\nM v[184:187],a[216:219],v[104:107],v[184:187]\nds_read_b128 a[204:207],v13 offset:55296\nC v66,v68,v69\nM v[216:219],a[216:219],v[112:115],v[216:219]\nv_exp_f32 v83,v83\nM v[248:251],a[216:219],v[120:123],v[248:251]\n" \
"buffer_load_dwordx4 v22,s[40:43],s48 offen offset:0 sc1 lds\nM v[252:255],a[220:223],v[120:123],v[252:255]\nv_exp_f32 v84,v84\nM v[220:223],a[220:223],v[112:115],v[220:223]\ns_waitcnt lgkmcnt(2)\nv_exp_f32 v85,v85\nM v[188:191],a[220:223],v[104:107],v[188:191]\ns_cmp_eq_u32 s52,s56\n" \
"C v72,v72,v73\nC v67,v70,v71\nM v[156:159],a[220:223],v[96:99],v[156:159]\nbuffer_load_dwordx4 v22,s[40:43],s48 offen offset:1024 sc1 lds\nM v[96:99],a[192:195],a[128:131],v[32:35]\ns_cbranch_scc1 LEV_%=\nLEVR13_%=:\nds_read_b128 a[208:211],v13 offset:50176\n" \
"C v73,v74,v75\nM v[96:99],a[196:199],a[132:135],v[96:99]\nds_read_b128 a[212:215],v13 offset:52224\nC v74,v76,v77\nM v[16:19],a[224:227],v[64:67],v[24:27]\ns_waitcnt lgkmcnt(2)\nds_read_b128 a[216:219],v13 offset:54272\nC v75,v78,v79\n" \
"M v[96:99],a[200:203],a[136:139],v[96:99]\nbuffer_load_dwordx4 v22,s[40:43],s48 offen offset:2048 sc1 lds\nv_exp_f32 v86,v86\nM v[96:99],a[204:207],a[140:143],v[96:99]\nds_read_b128 a[220:223],v13 offset:56320\nC v80,v80,v81\nM v[104:107],a[192:195],a[144:147],v[36:39]\nv_exp_f32 v87,v87\n" \
"M v[104:107],a[196:199],a[148:151],v[104:107]\nv_exp_f32 v88,v88\nM v[16:19],a[228:231],v[72:75],v[16:19]\nbuffer_load_dwordx4 v22,s[40:43],s48 offen offset:3072 sc1 lds\nM v[104:107],a[200:203],a[152:155],v[104:107]\ns_add_u32 m0,s22,98304\nv_exp_f32 v89,v89\nM v[104:107],a[204:207],a[156:159],v[104:107]\n" \
"s_add_u32 s48,s48,s49\nv_exp_f32 v90,v90\nM v[112:115],a[192:195],a[160:163],v[40:43]\nC v81,v82,v83\nC v82,v84,v85\nM v[112:115],a[196:199],a[164:167],v[112:115]\nbuffer_load_dwordx4 v1,s[84:87],s46 offen offset:0 lds\nM v[112:115],a[200:203],a[168:171],v[112:115]\n" \
"s_add_u32 m0,s22,99200\nv_exp_f32 v91,v91\nM v[112:115],a[204:207],a[172:175],v[112:115]\nC v83,v86,v87\nC v88,v88,v89\nM v[120:123],a[192:195],a[176:179],v[44:47]\nv_exp_f32 v92,v92\nM v[120:123],a[196:199],a[180:183],v[120:123]\n" \
"buffer_load_dwordx4 v1,s[84:87],s46 offen offset:128 lds\nM v[120:123],a[200:203],a[184:187],v[120:123]\ns_add_u32 m0,s22,100352\nv_exp_f32 v93,v93\nM v[120:123],a[204:207],a[188:191],v[120:123]\ns_mov_b32 s46,1114112\nv_exp_f32 v94,v94\nM v[16:19],a[232:235],v[80:83],v[16:19]\n" \
"s_waitcnt lgkmcnt(0)\nC v89,v90,v91\nC v90,v92,v93\nM v[100:103],a[208:211],a[128:131],v[32:35]\nbuffer_load_dwordx4 v1,s[84:87],s46 offen offset:0 lds\nM v[100:103],a[212:215],a[132:135],v[100:103]\ns_add_u32 m0,s22,101248\nds_read_b64_tr_b16 a[112:113],v6 offset:16384\n" \
"v_xor_b32 v14,0x10000,v14\nM v[100:103],a[216:219],a[136:139],v[100:103]\nds_read_b128 a[192:195],v14 offset:0\nM v[100:103],a[220:223],a[140:143],v[100:103]\nds_read_b128 a[196:199],v14 offset:1024\nM v[108:111],a[208:211],a[144:147],v[36:39]\nbuffer_load_dwordx4 v1,s[84:87],s46 offen offset:128 lds\nM v[108:111],a[212:215],a[148:151],v[108:111]\n" \
"s_add_u32 m0,s22,81920\nv_exp_f32 v95,v95\nM v[108:111],a[216:219],a[152:155],v[108:111]\ns_mov_b32 s46,1179648\nds_read_b128 a[200:203],v14 offset:2048\nC v91,v94,v95\nM v[108:111],a[220:223],a[156:159],v[108:111]\nds_read_b64_tr_b16 a[114:115],v7 offset:16384\n" \
"ds_read_b64_tr_b16 a[116:117],v8 offset:16384\nM v[16:19],a[236:239],v[88:91],v[16:19]\nds_read_b64_tr_b16 a[118:119],v9 offset:16384\nM v[116:119],a[208:211],a[160:163],v[40:43]\nds_read_b64_tr_b16 a[120:121],v6 offset:18432\nds_read_b64_tr_b16 a[122:123],v7 offset:18432\nM v[116:119],a[212:215],a[164:167],v[116:119]\nds_read_b64_tr_b16 a[124:125],v8 offset:18432\n" \
"v_max3_f32 v15,v16,v17,v18\nM v[116:119],a[216:219],a[168:171],v[116:119]\nds_read_b64_tr_b16 a[126:127],v9 offset:18432\nv_max_f32 v15,v15,v19\nM v[116:119],a[220:223],a[172:175],v[116:119]\nds_read_b128 a[204:207],v14 offset:3072\nv_cmp_nge_f32 vcc,0x53800000,v15\nM v[124:127],a[208:211],a[176:179],v[44:47]\n" \
"s_waitcnt lgkmcnt(6)\nds_read_b128 a[208:211],v14 offset:4096\nM v[124:127],a[212:215],a[180:183],v[124:127]\ns_cbranch_vccnz Lcs59_%=\nLcr60_%=:\nds_read_b128 a[212:215],v14 offset:5120\nM v[124:127],a[216:219],a[184:187],v[124:127]\ns_cmp_eq_u32 s52,s55\n" \
"ds_read_b128 a[216:219],v14 offset:6144\nM v[124:127],a[220:223],a[188:191],v[124:127]\ns_cbranch_scc1 LH1_%=\nLH1R14_%=:\nds_read_b128 a[220:223],v14 offset:7168\nv_xor_b32 v13,0x10000,v13\nM v[128:131],a[192:195],v[64:67],v[128:131]\nv_exp_f32 v96,v96\n" \
"M v[160:163],a[192:195],v[72:75],v[160:163]\nv_exp_f32 v97,v97\nM v[192:195],a[192:195],v[80:83],v[192:195]\nv_exp_f32 v98,v98\nM v[224:227],a[192:195],v[88:91],v[224:227]\nv_exp_f32 v99,v99\nM v[228:231],a[196:199],v[88:91],v[228:231]\nv_exp_f32 v100,v100\n" \
"M v[196:199],a[196:199],v[80:83],v[196:199]\nv_exp_f32 v101,v101\nM v[164:167],a[196:199],v[72:75],v[164:167]\ns_waitcnt vmcnt(12) lgkmcnt(0)\nv_exp_f32 v102,v102\nM v[132:135],a[196:199],v[64:67],v[132:135]\ns_barrier\nds_read_b128 a[192:195],v13 offset:32768\n" \
"M v[136:139],a[200:203],v[64:67],v[136:139]\nbuffer_load_dwordx4 v1,s[84:87],s46 offen offset:0 lds\nM v[168:171],a[200:203],v[72:75],v[168:171]\ns_add_u32 m0,s22,82816\nv_exp_f32 v103,v103\nM v[200:203],a[200:203],v[80:83],v[200:203]\nv_exp_f32 v104,v104\nM v[232:235],a[200:203],v[88:91],v[232:235]\n" \
"v_exp_f32 v105,v105\nM v[236:239],a[204:207],v[88:91],v[236:239]\nbuffer_load_dwordx4 v1,s[84:87],s46 offen offset:128 lds\nM v[204:207],a[204:207],v[80:83],v[204:207]\ns_mov_b32 s46,1245184\nds_read_b128 a[196:199],v13 offset:34816\nC v96,v96,v97\nM v[172:175],a[204:207],v[72:75],v[172:175]\n" \
"s_add_u32 m0,s22,83968\nv_exp_f32 v106,v106\nM v[140:143],a[204:207],v[64:67],v[140:143]\nds_read_b128 a[200:203],v13 offset:36864\nds_read_b128 a[204:207],v13 offset:38912\nM v[144:147],a[208:211],v[64:67],v[144:147]\nbuffer_load_dwordx4 v1,s[84:87],s46 offen offset:0 lds\nM v[176:179],a[208:211],v[72:75],v[176:179]\n" \
"s_add_u32 m0,s22,84864\nv_exp_f32 v107,v107\nM v[208:211],a[208:211],v[80:83],v[208:211]\nv_exp_f32 v108,v108\nM v[240:243],a[208:211],v[88:91],v[240:243]\nLG20_%=:\ns_cbranch_execz LG22_%=\nv_exp_f32 v109,v109\n" \
"M v[244:247],a[212:215],v[88:91],v[244:247]\nbuffer_load_dwordx4 v1,s[84:87],s46 offen offset:128 lds\nM v[212:215],a[212:215],v[80:83],v[212:215]\ns_add_u32 m0,s22,114688\nv_exp_f32 v110,v110\nM v[180:183],a[212:215],v[72:75],v[180:183]\ns_mov_b32 s46,1310720\nv_exp_f32 v111,v111\n" \
"M v[148:151],a[212:215],v[64:67],v[148:151]\nv_exp_f32 v112,v112\nM v[152:155],a[216:219],v[64:67],v[152:155]\nbuffer_load_dwordx4 v22,s[40:43],s48 offen offset:0 sc1 lds\nM v[184:187],a[216:219],v[72:75],v[184:187]\nC v97,v98,v99\nC v98,v100,v101\nM v[216:219],a[216:219],v[80:83],v[216:219]\n" \
"v_exp_f32 v113,v113\nM v[248:251],a[216:219],v[88:91],v[248:251]\nC v99,v102,v103\nC v104,v104,v105\nM v[252:255],a[220:223],v[88:91],v[252:255]\nbuffer_load_dwordx4 v22,s[40:43],s48 offen offset:1024 sc1 lds\nM v[220:223],a[220:223],v[80:83],v[220:223]\nv_exp_f32 v114,v114\n" \
"M v[188:191],a[220:223],v[72:75],v[188:191]\nC v105,v106,v107\nC v106,v108,v109\nM v[156:159],a[220:223],v[64:67],v[156:159]\ns_waitcnt lgkmcnt(0)\nv_exp_f32 v115,v115\nM v[64:67],a[192:195],a[128:131],v[32:35]\nbuffer_load_dwordx4 v22,s[40:43],s48 offen offset:2048 sc1 lds\n" \
"M v[64:67],a[196:199],a[132:135],v[64:67]\nds_read_b128 a[208:211],v13 offset:33792\nC v107,v110,v111\nM v[24:27],a[224:227],v[96:99],v[16:19]\nds_read_b128 a[212:215],v13 offset:35840\nC v112,v112,v113\nM v[64:67],a[200:203],a[136:139],v[64:67]\nds_read_b128 a[216:219],v13 offset:37888\n" \
"C v113,v114,v115\nM v[64:67],a[204:207],a[140:143],v[64:67]\nbuffer_load_dwordx4 v22,s[40:43],s48 offen offset:3072 sc1 lds\nM v[72:75],a[192:195],a[144:147],v[36:39]\ns_add_u32 m0,s22,0\nds_read_b128 a[220:223],v13 offset:39936\nM v[72:75],a[196:199],a[148:151],v[72:75]\nv_exp_f32 v116,v116\n" \
"M v[24:27],a[228:231],v[104:107],v[24:27]\nv_exp_f32 v117,v117\nM v[72:75],a[200:203],a[152:155],v[72:75]\nbuffer_load_dwordx4 v2,s[36:39],s47 offen offset:0 sc1 lds\nM v[72:75],a[204:207],a[156:159],v[72:75]\nv_exp_f32 v118,v118\nM v[80:83],a[192:195],a[160:163],v[40:43]\nv_exp_f32 v119,v119\n" \
"M v[80:83],a[196:199],a[164:167],v[80:83]\nv_exp_f32 v120,v120\nM v[80:83],a[200:203],a[168:171],v[80:83]\nbuffer_load_dwordx4 v2,s[36:39],s47 offen offset:1024 sc1 lds\nM v[80:83],a[204:207],a[172:175],v[80:83]\nv_exp_f32 v121,v121\nM v[88:91],a[192:195],a[176:179],v[44:47]\nC v114,v116,v117\n" \
"C v115,v118,v119\nM v[88:91],a[196:199],a[180:183],v[88:91]\nv_exp_f32 v122,v122\nM v[88:91],a[200:203],a[184:187],v[88:91]\nbuffer_load_dwordx4 v2,s[36:39],s47 offen offset:2048 sc1 lds\nM v[88:91],a[204:207],a[188:191],v[88:91]\nds_read_b128 a[192:195],v14 offset:16384\nC v120,v120,v121\n" \
"M v[24:27],a[232:235],v[112:115],v[24:27]\ns_waitcnt lgkmcnt(1)\nds_read_b128 a[196:199],v14 offset:17408\nM v[68:71],a[208:211],a[128:131],v[32:35]\nv_exp_f32 v123,v123\nM v[68:71],a[212:215],a[132:135],v[68:71]\nbuffer_load_dwordx4 v2,s[36:39],s47 offen offset:3072 sc1 lds\nM v[68:71],a[216:219],a[136:139],v[68:71]\n" \
"s_add_u32 m0,s22,32768\nv_exp_f32 v124,v124\nM v[68:71],a[220:223],a[140:143],v[68:71]\ns_add_u32 s47,s28,8192\nv_exp_f32 v125,v125\nM v[76:79],a[208:211],a[144:147],v[36:39]\nv_exp_f32 v126,v126\nM v[76:79],a[212:215],a[148:151],v[76:79]\n" \
"v_exp_f32 v127,v127\nM v[76:79],a[216:219],a[152:155],v[76:79]\nC v121,v122,v123\nC v122,v124,v125\nC v123,v126,v127\nM v[76:79],a[220:223],a[156:159],v[76:79]\nds_read_b128 a[200:203],v14 offset:18432\nds_read_b128 a[204:207],v14 offset:19456\n" \
"M v[24:27],a[236:239],v[120:123],v[24:27]\nM v[84:87],a[208:211],a[160:163],v[40:43]\nM v[84:87],a[212:215],a[164:167],v[84:87]\nv_max3_f32 v15,v24,v25,v26\nv_max_f32 v15,v15,v27\nM v[84:87],a[216:219],a[168:171],v[84:87]\nv_cmp_nge_f32 vcc,0x53800000,v15\nM v[84:87],a[220:223],a[172:175],v[84:87]\n" \
"M v[92:95],a[208:211],a[176:179],v[44:47]\nds_read_b128 a[208:211],v14 offset:20480\nM v[92:95],a[212:215],a[180:183],v[92:95]\ns_cbranch_vccnz Lcs61_%=\nLcr62_%=:\nds_read_b128 a[212:215],v14 offset:21504\nM v[92:95],a[216:219],a[184:187],v[92:95]\nds_read_b128 a[216:219],v14 offset:22528\n" \
"M v[92:95],a[220:223],a[188:191],v[92:95]\ns_waitcnt lgkmcnt(3)\nds_read_b128 a[220:223],v14 offset:23552\nM v[128:131],a[192:195],v[96:99],v[128:131]\nv_exp_f32 v64,v64\nM v[160:163],a[192:195],v[104:107],v[160:163]\nv_exp_f32 v65,v65\nM v[192:195],a[192:195],v[112:115],v[192:195]\n" \
"v_exp_f32 v66,v66\nM v[224:227],a[192:195],v[120:123],v[224:227]\nv_exp_f32 v67,v67\nM v[228:231],a[196:199],v[120:123],v[228:231]\nv_exp_f32 v68,v68\nM v[196:199],a[196:199],v[112:115],v[196:199]\nv_exp_f32 v69,v69\nM v[164:167],a[196:199],v[104:107],v[164:167]\n" \
"s_waitcnt vmcnt(16) lgkmcnt(0)\nv_exp_f32 v70,v70\nM v[132:135],a[196:199],v[96:99],v[132:135]\ns_barrier\nds_read_b128 a[192:195],v13 offset:49152\nM v[136:139],a[200:203],v[96:99],v[136:139]\nbuffer_load_dwordx4 v1,s[84:87],s46 offen offset:0 lds\nM v[168:171],a[200:203],v[104:107],v[168:171]\n" \
"s_add_u32 m0,s22,33664\nv_exp_f32 v71,v71\nM v[200:203],a[200:203],v[112:115],v[200:203]\nv_exp_f32 v72,v72\nM v[232:235],a[200:203],v[120:123],v[232:235]\nv_exp_f32 v73,v73\nM v[236:239],a[204:207],v[120:123],v[236:239]\nbuffer_load_dwordx4 v1,s[84:87],s46 offen offset:128 lds\n" \
"M v[204:207],a[204:207],v[112:115],v[204:207]\ns_add_u32 m0,s22,34816\nds_read_b128 a[196:199],v13 offset:51200\nC v64,v64,v65\nM v[172:175],a[204:207],v[104:107],v[172:175]\ns_mov_b32 s46,1376256\nv_exp_f32 v74,v74\nM v[140:143],a[204:207],v[96:99],v[140:143]\n" \
"ds_read_b128 a[200:203],v13 offset:53248\nds_read_b128 a[204:207],v13 offset:55296\nM v[144:147],a[208:211],v[96:99],v[144:147]\nbuffer_load_dwordx4 v1,s[84:87],s46 offen offset:0 lds\nM v[176:179],a[208:211],v[104:107],v[176:179]\ns_add_u32 m0,s22,35712\nv_exp_f32 v75,v75\nM v[208:211],a[208:211],v[112:115],v[208:211]\n" \
"v_exp_f32 v76,v76\nM v[240:243],a[208:211],v[120:123],v[240:243]\nv_exp_f32 v77,v77\nM v[244:247],a[212:215],v[120:123],v[244:247]\nbuffer_load_dwordx4 v1,s[84:87],s46 offen offset:128 lds\nM v[212:215],a[212:215],v[112:115],v[212:215]\ns_add_u32 m0,s22,16384\nv_exp_f32 v78,v78\n" \
"M v[180:183],a[212:215],v[104:107],v[180:183]\ns_add_u32 s52,s52,1\nv_exp_f32 v79,v79\nM v[148:151],a[212:215],v[96:99],v[148:151]\ns_mov_b32 s46,1441792\nv_exp_f32 v80,v80\nM v[152:155],a[216:219],v[96:99],v[152:155]\nbuffer_load_dwordx4 v1,s[84:87],s46 offen offset:0 lds\n" \
"M v[184:187],a[216:219],v[104:107],v[184:187]\ns_add_u32 m0,s22,17280\nv_exp_f32 v81,v81\nM v[216:219],a[216:219],v[112:115],v[216:219]\nv_exp_f32 v82,v82\nM v[248:251],a[216:219],v[120:123],v[248:251]\ns_waitcnt lgkmcnt(0)\nC v65,v66,v67\n" \
"C v66,v68,v69\nM v[252:255],a[220:223],v[120:123],v[252:255]\nbuffer_load_dwordx4 v1,s[84:87],s46 offen offset:128 lds\nM v[220:223],a[220:223],v[112:115],v[220:223]\ns_add_u32 m0,s22,18432\nv_exp_f32 v83,v83\nM v[188:191],a[220:223],v[104:107],v[188:191]\ns_cmp_eq_u32 s52,s56\n" \
"C v67,v70,v71\nC v72,v72,v73\nM v[156:159],a[220:223],v[96:99],v[156:159]\ns_cbranch_scc1 LEV_%=\nLEVR14_%=:\nv_exp_f32 v84,v84\nM v[96:99],a[192:195],a[128:131],v[32:35]\nbuffer_load_dwordx2 v[10:11],v5,s[36:39],0 offen sc1\n" \
"M v[96:99],a[196:199],a[132:135],v[96:99]\ns_mov_b32 s46,1507328\nds_read_b128 a[208:211],v13 offset:50176\nds_read_b128 a[212:215],v13 offset:52224\nM v[16:19],a[224:227],v[64:67],v[24:27]\nbuffer_load_dwordx4 v1,s[84:87],s46 offen offset:0 lds\nM v[96:99],a[200:203],a[136:139],v[96:99]\ns_add_u32 m0,s22,19328\n" \
"C v73,v74,v75\nC v74,v76,v77\nM v[96:99],a[204:207],a[140:143],v[96:99]\nds_read_b128 a[216:219],v13 offset:54272\nC v75,v78,v79\nM v[104:107],a[192:195],a[144:147],v[36:39]\nds_read_b128 a[220:223],v13 offset:56320\nC v80,v80,v81\n" \
"M v[104:107],a[196:199],a[148:151],v[104:107]\nv_exp_f32 v85,v85\nM v[16:19],a[228:231],v[72:75],v[16:19]\nbuffer_load_dwordx4 v1,s[84:87],s46 offen offset:128 lds\nM v[104:107],a[200:203],a[152:155],v[104:107]\ns_add_u32 m0,s22,49152\nv_exp_f32 v86,v86\nM v[104:107],a[204:207],a[156:159],v[104:107]\n" \
"s_mov_b32 s46,1966080\nv_exp_f32 v87,v87\nM v[112:115],a[192:195],a[160:163],v[40:43]\nv_exp_f32 v88,v88\nM v[112:115],a[196:199],a[164:167],v[112:115]\nC v81,v82,v83\nC v82,v84,v85\nM v[112:115],a[200:203],a[168:171],v[112:115]\n" \
"v_exp_f32 v89,v89\nM v[112:115],a[204:207],a[172:175],v[112:115]\nC v83,v86,v87\nC v88,v88,v89\nM v[120:123],a[192:195],a[176:179],v[44:47]\nv_exp_f32 v90,v90\nM v[120:123],a[196:199],a[180:183],v[120:123]\nv_exp_f32 v91,v91\n" \
"M v[120:123],a[200:203],a[184:187],v[120:123]\nv_exp_f32 v92,v92\nM v[120:123],a[204:207],a[188:191],v[120:123]\nv_exp_f32 v93,v93\nM v[16:19],a[232:235],v[80:83],v[16:19]\ns_waitcnt lgkmcnt(0)\nv_exp_f32 v94,v94\nM v[100:103],a[208:211],a[128:131],v[32:35]\n" \
"C v89,v90,v91\nC v90,v92,v93\nM v[100:103],a[212:215],a[132:135],v[100:103]\nv_exp_f32 v95,v95\nM v[100:103],a[216:219],a[136:139],v[100:103]\nv_xor_b32 v14,0x10000,v14\nC v91,v94,v95\nM v[100:103],a[220:223],a[140:143],v[100:103]\n" \
"ds_read_b128 a[192:195],v14 offset:0\nds_read_b128 a[196:199],v14 offset:1024\nM v[108:111],a[208:211],a[144:147],v[36:39]\nds_read_b128 a[200:203],v14 offset:2048\nv_add_u32 v20,0x10000,v4\nM v[108:111],a[212:215],a[148:151],v[108:111]\nds_read_b128 a[204:207],v14 offset:3072\nv_add_u32 v23,0x20000,v4\n" \
"M v[108:111],a[216:219],a[152:155],v[108:111]\nM v[108:111],a[220:223],a[156:159],v[108:111]\nM v[16:19],a[236:239],v[88:91],v[16:19]\nM v[116:119],a[208:211],a[160:163],v[40:43]\nM v[116:119],a[212:215],a[164:167],v[116:119]\nv_max3_f32 v15,v16,v17,v18\nv_max_f32 v15,v15,v19\nM v[116:119],a[216:219],a[168:171],v[116:119]\n" \
"v_cmp_nge_f32 vcc,0x53800000,v15\nM v[116:119],a[220:223],a[172:175],v[116:119]\nM v[124:127],a[208:211],a[176:179],v[44:47]\nds_read_b128 a[208:211],v14 offset:4096\nM v[124:127],a[212:215],a[180:183],v[124:127]\ns_cbranch_vccnz Lcs63_%=\nLcr64_%=:\nds_read_b128 a[212:215],v14 offset:5120\n" \
"v_max3_f32 v60,v96,v97,v98\nM v[124:127],a[216:219],a[184:187],v[124:127]\ns_waitcnt lgkmcnt(2)\nds_read_b128 a[216:219],v14 offset:6144\nv_max3_f32 v61,v99,v104,v105\nM v[124:127],a[220:223],a[188:191],v[124:127]\nds_read_b128 a[220:223],v14 offset:7168\nv_max3_f32 v60,v60,v106,v107\n" \
"v_max3_f32 v61,v61,v112,v113\nv_max3_f32 v60,v60,v114,v115\nv_max3_f32 v61,v61,v120,v121\nv_max3_f32 v60,v60,v122,v123\nv_max3_f32 v61,v61,v100,v101\nv_max3_f32 v60,v60,v102,v103\nv_max3_f32 v61,v61,v108,v109\nv_max3_f32 v60,v60,v110,v111\n" \
"v_max3_f32 v61,v61,v116,v117\nv_max3_f32 v60,v60,v118,v119\nv_max3_f32 v61,v61,v124,v125\nv_max3_f32 v61,v61,v126,v127\nv_max_f32 v60,v60,v61\nv_cmp_nge_f32 vcc,0x41f00000,v60\ns_cbranch_vccnz LRS_%=\nLH1R15_%=:\n" \
"M v[128:131],a[192:195],v[64:67],v[128:131]\nv_exp_f32 v96,v96\nM v[160:163],a[192:195],v[72:75],v[160:163]\nv_exp_f32 v97,v97\nM v[192:195],a[192:195],v[80:83],v[192:195]\nv_exp_f32 v98,v98\nM v[224:227],a[192:195],v[88:91],v[224:227]\nv_exp_f32 v99,v99\n" \
"M v[228:231],a[196:199],v[88:91],v[228:231]\nv_exp_f32 v100,v100\nM v[196:199],a[196:199],v[80:83],v[196:199]\nv_exp_f32 v101,v101\nM v[164:167],a[196:199],v[72:75],v[164:167]\ns_waitcnt vmcnt(9) lgkmcnt(0)\nv_exp_f32 v102,v102\nM v[132:135],a[196:199],v[64:67],v[132:135]\n" \
"s_barrier\nbuffer_load_dwordx4 v1,s[84:87],s46 offen offset:0 lds\nM v[136:139],a[200:203],v[64:67],v[136:139]\ns_add_u32 m0,s22,50048\nds_read_b128 v[76:79],v4 offset:0\nC v96,v96,v97\nM v[168:171],a[200:203],v[72:75],v[168:171]\nv_exp_f32 v103,v103\n" \
"M v[200:203],a[200:203],v[80:83],v[200:203]\nv_exp_f32 v104,v104\nM v[232:235],a[200:203],v[88:91],v[232:235]\nbuffer_load_dwordx4 v1,s[84:87],s46 offen offset:128 lds\nM v[236:239],a[204:207],v[88:91],v[236:239]\ns_mov_b32 s46,2031616\ns_add_u32 m0,s22,51200\nds_read_b128 v[84:87],v4 offset:1024\n" \
"C v97,v98,v99\nM v[204:207],a[204:207],v[80:83],v[204:207]\nds_read_b128 v[92:95],v4 offset:2048\nC v98,v100,v101\nM v[172:175],a[204:207],v[72:75],v[172:175]\ns_waitcnt vmcnt(4)\nv_cmp_gt_u64 s[98:99],s[14:15],v[10:11]\nv_cmp_le_u64 s[18:19],s[16:17],v[10:11]\n" \
"M v[140:143],a[204:207],v[64:67],v[140:143]\nbuffer_load_dwordx4 v1,s[84:87],s46 offen offset:0 lds\nv_exp_f32 v105,v105\nM v[144:147],a[208:211],v[64:67],v[144:147]\ns_add_u32 m0,s22,52096\nv_exp_f32 v106,v106\nM v[176:179],a[208:211],v[72:75],v[176:179]\ns_or_b64 s[98:99],s[98:99],s[18:19]\n" \
"s_and_b32 s98,s98,0xff0000\ns_cbranch_scc1 LPF_%=\nLPFR_%=:\nv_exp_f32 v107,v107\nM v[208:211],a[208:211],v[80:83],v[208:211]\nv_exp_f32 v108,v108\nM v[240:243],a[208:211],v[88:91],v[240:243]\nbuffer_load_dwordx4 v1,s[84:87],s46 offen offset:128 lds\n" \
"v_exp_f32 v109,v109\nM v[244:247],a[212:215],v[88:91],v[244:247]\ns_add_u32 m0,s22,65536\nv_exp_f32 v110,v110\nM v[212:215],a[212:215],v[80:83],v[212:215]\ns_mov_b32 s46,1572864\nv_exp_f32 v111,v111\nM v[180:183],a[212:215],v[72:75],v[180:183]\n" \
"v_exp_f32 v112,v112\nM v[148:151],a[212:215],v[64:67],v[148:151]\nbuffer_load_dwordx4 v2,s[36:39],s47 offen offset:0 sc1 lds\nv_exp_f32 v113,v113\nM v[152:155],a[216:219],v[64:67],v[152:155]\nds_read_b128 a[192:195],v14 offset:49152\nv_exp_f32 v114,v114\nM v[184:187],a[216:219],v[72:75],v[184:187]\n" \
"ds_read_b128 a[196:199],v14 offset:50176\nv_exp_f32 v115,v115\nM v[216:219],a[216:219],v[80:83],v[216:219]\nds_read_b128 a[200:203],v14 offset:51200\nC v104,v104,v105\nv_exp_f32 v116,v116\nM v[248:251],a[216:219],v[88:91],v[248:251]\nbuffer_load_dwordx4 v2,s[36:39],s47 offen offset:1024 sc1 lds\n" \
"v_exp_f32 v117,v117\nM v[252:255],a[220:223],v[88:91],v[252:255]\nds_read_b128 a[204:207],v14 offset:52224\nC v99,v102,v103\nv_exp_f32 v118,v118\nM v[220:223],a[220:223],v[80:83],v[220:223]\nds_read_b128 a[208:211],v14 offset:53248\nC v105,v106,v107\n" \
"v_exp_f32 v119,v119\nM v[188:191],a[220:223],v[72:75],v[188:191]\ns_waitcnt lgkmcnt(4)\nds_read_b128 v[72:75],v4 offset:3072\nC v112,v112,v113\nv_exp_f32 v120,v120\nM v[156:159],a[220:223],v[64:67],v[156:159]\nbuffer_load_dwordx4 v2,s[36:39],s47 offen offset:2048 sc1 lds\n" \
"v_exp_f32 v121,v121\nM v[48:51],a[0:3],v[76:79],0\nv_exp_f32 v122,v122\nM v[48:51],a[8:11],v[84:87],v[48:51]\nds_read_b128 v[80:83],v4 offset:8192\nC v106,v108,v109\nv_exp_f32 v123,v123\nM v[48:51],a[16:19],v[92:95],v[48:51]\n" \
"s_waitcnt lgkmcnt(1)\nds_read_b128 v[88:91],v4 offset:9216\nC v107,v110,v111\nM v[48:51],a[24:27],v[72:75],v[48:51]\nbuffer_load_dwordx4 v2,s[36:39],s47 offen offset:3072 sc1 lds\nM v[52:55],a[28:31],v[72:75],0\ns_add_u32 m0,s22,98304\nds_read_b128 a[212:215],v14 offset:54272\n" \
"C v113,v114,v115\nM v[52:55],a[20:23],v[92:95],v[52:55]\ns_add_u32 s47,s29,0\nds_read_b128 v[72:75],v4 offset:10240\nds_read_b128 v[92:95],v4 offset:11264\nM v[52:55],a[12:15],v[84:87],v[52:55]\nv_exp_f32 v124,v124\nM v[52:55],a[4:7],v[76:79],v[52:55]\n" \
"s_waitcnt lgkmcnt(4)\nds_read_b128 a[216:219],v14 offset:55296\nC v120,v120,v121\nM v[64:67],a[0:3],v[80:83],0\ns_waitcnt lgkmcnt(4)\nds_read_b64_tr_b16 a[128:129],v6 offset:32768\nC v121,v122,v123\nM v[64:67],a[8:11],v[88:91],v[64:67]\n" \
"s_waitcnt lgkmcnt(3)\nds_read_b64_tr_b16 a[130:131],v7 offset:32768\nds_read_b64_tr_b16 a[132:133],v8 offset:32768\nds_read_b64_tr_b16 a[134:135],v9 offset:32768\nds_read_b128 a[220:223],v14 offset:56320\nds_read_b64_tr_b16 a[136:137],v6 offset:34816\nds_read_b64_tr_b16 a[138:139],v7 offset:34816\nds_read_b64_tr_b16 a[140:141],v8 offset:34816\n" \
"ds_read_b64_tr_b16 a[142:143],v9 offset:34816\nds_read_b64_tr_b16 a[144:145],v6 offset:16384\nds_read_b64_tr_b16 a[146:147],v7 offset:16384\nds_read_b64_tr_b16 a[148:149],v8 offset:16384\nds_read_b64_tr_b16 a[150:151],v9 offset:16384\nds_read_b64_tr_b16 a[152:153],v6 offset:18432\nds_read_b64_tr_b16 a[154:155],v7 offset:18432\nds_read_b64_tr_b16 a[156:157],v8 offset:18432\n" \
"ds_read_b64_tr_b16 a[158:159],v9 offset:18432\nM v[64:67],a[16:19],v[72:75],v[64:67]\ns_waitcnt lgkmcnt(15)\nds_read_b128 v[84:87],v4 offset:4096\nv_xor_b32 v6,0x10000,v6\nM v[64:67],a[24:27],v[92:95],v[64:67]\nds_read_b128 v[76:79],v4 offset:5120\nv_xor_b32 v7,0x10000,v7\n" \
"M v[68:71],a[28:31],v[92:95],0\nds_read_b128 v[92:95],v4 offset:6144\nv_xor_b32 v8,0x10000,v8\nM v[68:71],a[20:23],v[72:75],v[68:71]\nds_read_b128 v[72:75],v4 offset:7168\nv_xor_b32 v9,0x10000,v9\nv_exp_f32 v125,v125\nM v[68:71],a[12:15],v[88:91],v[68:71]\n" \
"s_waitcnt lgkmcnt(3)\nv_exp_f32 v126,v126\nM v[68:71],a[4:7],v[80:83],v[68:71]\ns_waitcnt vmcnt(8) lgkmcnt(4)\nds_read_b128 v[88:91],v4 offset:12288\nC v114,v116,v117\nM v[24:27],a[224:227],v[96:99],v[16:19]\ns_barrier\n" \
"buffer_load_dwordx4 v2,s[36:39],s47 offen offset:0 sc1 lds\nM v[48:51],a[32:35],v[84:87],v[48:51]\ns_waitcnt lgkmcnt(3)\nds_read_b128 v[80:83],v4 offset:13312\nC v115,v118,v119\nM v[48:51],a[40:43],v[76:79],v[48:51]\ns_waitcnt lgkmcnt(3)\nds_read_b64_tr_b16 a[160:161],v6 offset:32768\n" \
"C v122,v124,v125\nM v[48:51],a[48:51],v[92:95],v[48:51]\ns_waitcnt lgkmcnt(3)\nds_read_b64_tr_b16 a[162:163],v7 offset:32768\nM v[48:51],a[56:59],v[72:75],v[48:51]\nbuffer_load_dwordx4 v2,s[36:39],s47 offen offset:1024 sc1 lds\nM v[52:55],a[60:63],v[72:75],v[52:55]\nds_read_b128 v[72:75],v4 offset:14336\n" \
"M v[52:55],a[52:55],v[92:95],v[52:55]\nds_read_b128 v[92:95],v4 offset:15360\nM v[52:55],a[44:47],v[76:79],v[52:55]\ns_waitcnt lgkmcnt(4)\nv_exp_f32 v127,v127\nM v[52:55],a[36:39],v[84:87],v[52:55]\nbuffer_load_dwordx4 v2,s[36:39],s47 offen offset:2048 sc1 lds\nM v[24:27],a[228:231],v[104:107],v[24:27]\n" \
"s_add_u32 s52,s52,1\nds_read_b64_tr_b16 a[164:165],v8 offset:32768\nC v123,v126,v127\nM v[64:67],a[32:35],v[88:91],v[64:67]\ns_waitcnt lgkmcnt(1)\nds_read_b64_tr_b16 a[166:167],v9 offset:32768\nds_read_b64_tr_b16 a[168:169],v6 offset:34816\nM v[64:67],a[40:43],v[80:83],v[64:67]\n" \
"s_waitcnt lgkmcnt(4)\nds_read_b64_tr_b16 a[170:171],v7 offset:34816\nM v[64:67],a[48:51],v[72:75],v[64:67]\nbuffer_load_dwordx4 v2,s[36:39],s47 offen offset:3072 sc1 lds\nM v[64:67],a[56:59],v[92:95],v[64:67]\ns_add_u32 s47,s29,8192\nds_read_b64_tr_b16 a[172:173],v8 offset:34816\nM v[68:71],a[60:63],v[92:95],v[68:71]\n" \
"s_add_u32 m0,s22,81920\nds_read_b64_tr_b16 a[174:175],v9 offset:34816\nM v[68:71],a[52:55],v[72:75],v[68:71]\nds_read_b64_tr_b16 a[176:177],v6 offset:16384\nM v[68:71],a[44:47],v[80:83],v[68:71]\ns_cmp_eq_u32 s52,s56\nds_read_b64_tr_b16 a[178:179],v7 offset:16384\nds_read_b64_tr_b16 a[180:181],v8 offset:16384\n" \
"M v[68:71],a[36:39],v[88:91],v[68:71]\nbuffer_load_dwordx4 v2,s[36:39],s47 offen offset:0 sc1 lds\nM v[24:27],a[232:235],v[112:115],v[24:27]\ns_cbranch_scc1 LEV_%=\nLEVR15_%=:\nds_read_b64_tr_b16 a[182:183],v9 offset:16384\nds_read_b64_tr_b16 a[184:185],v6 offset:18432\nds_read_b64_tr_b16 a[186:187],v7 offset:18432\n" \
"ds_read_b64_tr_b16 a[188:189],v8 offset:18432\nds_read_b64_tr_b16 a[190:191],v9 offset:18432\nds_write2_b32 v57,v40,v44 offset1:1\nM v[24:27],a[236:239],v[120:123],v[24:27]\nM v[192:195],a[192:195],v[112:115],v[192:195]\nM v[196:199],a[196:199],v[112:115],v[196:199]\nds_write_b64 v57,v[26:27] offset:8\nM v[200:203],a[200:203],v[112:115],v[200:203]\n" \
"buffer_load_dwordx4 v2,s[36:39],s47 offen offset:1024 sc1 lds\nM v[204:207],a[204:207],v[112:115],v[204:207]\nC v192,v192,v193\nC v193,v194,v195\nM v[208:211],a[208:211],v[112:115],v[208:211]\ns_waitcnt vmcnt(6) lgkmcnt(0)\nC v194,v196,v197\nC v195,v198,v199\n" \
"M v[212:215],a[212:215],v[112:115],v[212:215]\ns_barrier\nds_read_b128 v[28:31],v12 offset:0\nM v[216:219],a[216:219],v[112:115],v[216:219]\nds_write_b128 v12,v[192:195] offset:0\nM v[220:223],a[220:223],v[112:115],v[220:223]\nbuffer_load_dwordx4 v2,s[36:39],s47 offen offset:2048 sc1 lds\nM v[224:227],a[192:195],v[120:123],v[224:227]\n" \
"C v200,v200,v201\nC v201,v202,v203\nM v[228:231],a[196:199],v[120:123],v[228:231]\nds_read_b64_tr_b16 a[240:241],v6 offset:49152\nC v202,v204,v205\nM v[232:235],a[200:203],v[120:123],v[232:235]\nds_read_b64_tr_b16 a[242:243],v7 offset:49152\nC v203,v206,v207\n" \
"M v[236:239],a[204:207],v[120:123],v[236:239]\nbuffer_load_dwordx4 v2,s[36:39],s47 offen offset:3072 sc1 lds\nM v[240:243],a[208:211],v[120:123],v[240:243]\ns_add_u32 m0,s22,114688\nC v208,v208,v209\nC v209,v210,v211\nM v[244:247],a[212:215],v[120:123],v[244:247]\ns_add_u32 s47,s30,0\n" \
"ds_write_b128 v12,v[200:203] offset:1024\nM v[248:251],a[216:219],v[120:123],v[248:251]\nC v210,v212,v213\nC v211,v214,v215\nM v[252:255],a[220:223],v[120:123],v[252:255]\nbuffer_load_dwordx4 v2,s[36:39],s47 offen offset:0 sc1 lds\nM v[128:131],a[192:195],v[96:99],v[128:131]\nC v216,v216,v217\n" \
"C v217,v218,v219\nM v[132:135],a[196:199],v[96:99],v[132:135]\nC v218,v220,v221\nC v219,v222,v223\nM v[136:139],a[200:203],v[96:99],v[136:139]\nLG21_%=:\ns_cbranch_execz LG23_%=\nds_write_b128 v12,v[208:211] offset:2048\n" \
"M v[140:143],a[204:207],v[96:99],v[140:143]\nbuffer_load_dwordx4 v2,s[36:39],s47 offen offset:1024 sc1 lds\nM v[144:147],a[208:211],v[96:99],v[144:147]\nC v224,v224,v225\nC v232,v232,v233\nM v[148:151],a[212:215],v[96:99],v[148:151]\nC v225,v226,v227\nC v233,v234,v235\n" \
"M v[152:155],a[216:219],v[96:99],v[152:155]\nds_write_b128 v12,v[216:219] offset:3072\nM v[156:159],a[220:223],v[96:99],v[156:159]\nbuffer_load_dwordx4 v2,s[36:39],s47 offen offset:2048 sc1 lds\nM v[160:163],a[192:195],v[104:107],v[160:163]\nC v226,v228,v229\nC v227,v230,v231\nM v[164:167],a[196:199],v[104:107],v[164:167]\n" \
"ds_read_b128 v[76:79],v20 offset:0\nC v234,v236,v237\nM v[168:171],a[200:203],v[104:107],v[168:171]\nds_read_b128 v[84:87],v20 offset:1024\nC v235,v238,v239\nM v[172:175],a[204:207],v[104:107],v[172:175]\nds_read_b128 v[92:95],v20 offset:2048\nC v240,v240,v241\n" \
"M v[176:179],a[208:211],v[104:107],v[176:179]\ns_waitcnt lgkmcnt(3)\nds_read_b128 v[72:75],v20 offset:3072\nv_min_f32 v15,v32,v28\nM v[180:183],a[212:215],v[104:107],v[180:183]\nbuffer_load_dwordx4 v2,s[36:39],s47 offen offset:3072 sc1 lds\nM v[184:187],a[216:219],v[104:107],v[184:187]\ns_add_u32 m0,s22,0\n" \
"C v248,v248,v249\nC v241,v242,v243\nM v[188:191],a[220:223],v[104:107],v[188:191]\ns_waitcnt lgkmcnt(3)\ns_add_u32 s47,s30,8192\nC v249,v250,v251\nv_sub_f32 v16,v15,v32\nM v[48:51],a[64:67],v[76:79],v[48:51]\n" \
"s_waitcnt lgkmcnt(2)\nds_write_b128 v12,v[224:227] offset:4096\nM v[48:51],a[72:75],v[84:87],v[48:51]\ns_waitcnt lgkmcnt(2)\nds_read_b128 v[80:83],v20 offset:8192\nC v242,v244,v245\nM v[48:51],a[80:83],v[92:95],v[48:51]\ns_waitcnt lgkmcnt(2)\n" \
"ds_read_b128 v[88:91],v20 offset:9216\nC v243,v246,v247\nM v[48:51],a[88:91],v[72:75],v[48:51]\nds_read_b128 v[60:63],v20 offset:10240\nC v250,v252,v253\nM v[52:55],a[92:95],v[72:75],v[52:55]\nds_read_b128 v[72:75],v20 offset:11264\nC v251,v254,v255\n" \
"M v[52:55],a[84:87],v[92:95],v[52:55]\nbuffer_load_dwordx4 v2,s[36:39],s47 offen offset:0 sc1 lds\nM v[52:55],a[76:79],v[84:87],v[52:55]\nv_exp_f32 v16,v16\nM v[52:55],a[68:71],v[76:79],v[52:55]\ns_waitcnt lgkmcnt(3)\nv_sub_f32 v18,v15,v28\nv_mul_f32 v15,v24,v16\n" \
"M v[64:67],a[64:67],v[80:83],v[64:67]\ns_waitcnt lgkmcnt(2)\nds_write_b128 v12,v[232:235] offset:5120\nM v[64:67],a[72:75],v[88:91],v[64:67]\ns_waitcnt lgkmcnt(2)\nds_read_b128 v[92:95],v20 offset:4096\nv_mov_b32 v28,s10\nM v[64:67],a[80:83],v[60:63],v[64:67]\n" \
"s_waitcnt lgkmcnt(2)\nds_read_b128 v[84:87],v20 offset:5120\nv_exp_f32 v18,v18\nM v[64:67],a[88:91],v[72:75],v[64:67]\nds_write_b128 v12,v[240:243] offset:6144\nds_write_b128 v12,v[248:251] offset:7168\nds_read_b64_tr_b16 a[244:245],v8 offset:49152\nds_read_b64_tr_b16 a[246:247],v9 offset:49152\n" \
"ds_read_b64_tr_b16 a[248:249],v6 offset:51200\nds_read_b64_tr_b16 a[250:251],v7 offset:51200\nds_read_b64_tr_b16 a[252:253],v8 offset:51200\nds_read_b64_tr_b16 a[254:255],v9 offset:51200\nds_read_b128 v[76:79],v20 offset:6144\nM v[68:71],a[92:95],v[72:75],v[68:71]\nds_read_b128 v[72:75],v20 offset:7168\nv_fmac_f32 v15,v30,v18\n" \
"v_rcp_f32 v15,v15\nM v[68:71],a[84:87],v[60:63],v[68:71]\nbuffer_load_dwordx4 v2,s[36:39],s47 offen offset:1024 sc1 lds\nM v[68:71],a[76:79],v[88:91],v[68:71]\nv_mul_f32 v16,v16,v15\nv_mul_f32 v18,v18,v15\nM v[68:71],a[68:71],v[80:83],v[68:71]\ns_waitcnt lgkmcnt(11)\n" \
"ds_read_b128 v[60:63],v20 offset:12288\nv_mul_f32 v128,v16,v128\nM v[48:51],a[96:99],v[92:95],v[48:51]\ns_waitcnt lgkmcnt(11)\nds_read_b128 v[88:91],v20 offset:13312\nv_mul_f32 v129,v16,v129\nM v[48:51],a[104:107],v[84:87],v[48:51]\ns_waitcnt vmcnt(10) lgkmcnt(4)\n" \
"s_waitcnt lgkmcnt(3)\nds_read_b128 v[80:83],v20 offset:14336\nv_mul_f32 v130,v16,v130\nM v[48:51],a[112:115],v[76:79],v[48:51]\ns_barrier\ns_waitcnt lgkmcnt(3)\nv_mul_f32 v131,v16,v131\nv_mul_f32 v132,v16,v132\n" \
"M v[48:51],a[120:123],v[72:75],v[48:51]\nbuffer_load_dwordx4 v2,s[36:39],s47 offen offset:2048 sc1 lds\nv_mul_f32 v133,v16,v133\nM v[52:55],a[124:127],v[72:75],v[52:55]\nds_read_b128 v[72:75],v20 offset:15360\nv_mul_f32 v134,v16,v134\nv_mul_f32 v135,v16,v135\nM v[52:55],a[116:119],v[76:79],v[52:55]\n" \
"s_waitcnt lgkmcnt(1)\nds_read_b128 v[96:99],v57 offset:0\nv_mul_f32 v136,v16,v136\nv_mul_f32 v137,v16,v137\nM v[52:55],a[108:111],v[84:87],v[52:55]\ns_waitcnt lgkmcnt(3)\nds_read_b128 v[100:103],v57 offset:1024\nv_mul_f32 v138,v16,v138\n" \
"v_mul_f32 v139,v16,v139\nM v[52:55],a[100:103],v[92:95],v[52:55]\nbuffer_load_dwordx4 v2,s[36:39],s47 offen offset:3072 sc1 lds\nv_mul_f32 v140,v16,v140\nM v[64:67],a[96:99],v[60:63],v[64:67]\ns_add_u32 m0,s22,32768\nds_read_b128 v[104:107],v57 offset:2048\nv_mul_f32 v141,v16,v141\n" \
"v_mul_f32 v142,v16,v142\nM v[64:67],a[104:107],v[88:91],v[64:67]\ns_add_u32 s47,s28,16384\nds_read_b128 v[76:79],v20 offset:32768\nv_mul_f32 v143,v16,v143\nM v[64:67],a[112:115],v[80:83],v[64:67]\ns_waitcnt lgkmcnt(4)\nds_read_b128 v[84:87],v20 offset:33792\n" \
"v_min_f32 v15,v36,v29\nM v[64:67],a[120:123],v[72:75],v[64:67]\nds_read_b128 v[92:95],v20 offset:34816\nv_mul_f32 v144,v16,v144\nM v[68:71],a[124:127],v[72:75],v[68:71]\ns_waitcnt lgkmcnt(4)\nds_read_b128 v[72:75],v20 offset:35840\nv_mul_f32 v145,v16,v145\n" \
"v_lshlrev_b32 v228,16,v96\nM v[68:71],a[116:119],v[80:83],v[68:71]\nbuffer_load_dwordx4 v2,s[36:39],s47 offen offset:0 sc1 lds\nv_and_b32 v96,0xffff0000,v96\nM v[68:71],a[108:111],v[88:91],v[68:71]\nv_fmac_f32 v128,v228,v18\nv_fmac_f32 v129,v96,v18\nv_lshlrev_b32 v229,16,v97\n" \
"M v[68:71],a[100:103],v[60:63],v[68:71]\ns_waitcnt lgkmcnt(3)\nds_read_b128 v[108:111],v57 offset:3072\nv_and_b32 v97,0xffff0000,v97\nv_fmac_f32 v130,v229,v18\nM v[32:35],a[0:3],v[76:79],0\ns_waitcnt lgkmcnt(3)\nv_fmac_f32 v131,v97,v18\n" \
"v_lshlrev_b32 v230,16,v98\nv_and_b32 v98,0xffff0000,v98\nM v[32:35],a[8:11],v[84:87],v[32:35]\ns_waitcnt lgkmcnt(2)\nds_read_b128 v[80:83],v20 offset:40960\nv_fmac_f32 v132,v230,v18\nv_fmac_f32 v133,v98,v18\nM v[32:35],a[16:19],v[92:95],v[32:35]\n" \
"s_waitcnt lgkmcnt(2)\nds_read_b128 v[88:91],v20 offset:41984\nv_lshlrev_b32 v231,16,v99\nv_and_b32 v99,0xffff0000,v99\nM v[32:35],a[24:27],v[72:75],v[32:35]\nds_read_b128 v[60:63],v20 offset:43008\nv_sub_f32 v17,v15,v36\nv_fmac_f32 v134,v231,v18\n" \
"M v[36:39],a[28:31],v[72:75],0\nds_read_b128 v[72:75],v20 offset:44032\nv_fmac_f32 v135,v99,v18\nC v128,v128,v129\nM v[36:39],a[20:23],v[92:95],v[36:39]\nbuffer_load_dwordx4 v2,s[36:39],s47 offen offset:1024 sc1 lds\nC v129,v130,v131\nM v[36:39],a[12:15],v[84:87],v[36:39]\n" \
"C v130,v132,v133\nC v131,v134,v135\nv_lshlrev_b32 v236,16,v100\nM v[36:39],a[4:7],v[76:79],v[36:39]\ns_waitcnt lgkmcnt(3)\nv_and_b32 v100,0xffff0000,v100\nv_fmac_f32 v136,v236,v18\nv_fmac_f32 v137,v100,v18\n" \
"M v[40:43],a[0:3],v[80:83],0\ns_waitcnt lgkmcnt(2)\nv_lshlrev_b32 v237,16,v101\nv_and_b32 v101,0xffff0000,v101\nv_fmac_f32 v138,v237,v18\nM v[40:43],a[8:11],v[88:91],v[40:43]\ns_waitcnt lgkmcnt(1)\nds_read_b128 v[92:95],v20 offset:36864\n" \
"v_fmac_f32 v139,v101,v18\nv_lshlrev_b32 v238,16,v102\nM v[40:43],a[16:19],v[60:63],v[40:43]\ns_waitcnt lgkmcnt(1)\nds_read_b128 v[84:87],v20 offset:37888\nv_and_b32 v102,0xffff0000,v102\nv_fmac_f32 v140,v238,v18\nM v[40:43],a[24:27],v[72:75],v[40:43]\n" \
"buffer_store_dwordx4 v[128:131],v4,s[36:39],s62 offen offset:0 sc1\nds_read_b128 v[112:115],v57 offset:4096\nds_read_b128 v[116:119],v57 offset:5120\nds_read_b128 v[120:123],v57 offset:6144\nds_read_b128 v[124:127],v57 offset:7168\nds_read_b128 v[76:79],v20 offset:38912\nM v[44:47],a[28:31],v[72:75],0\nds_read_b128 v[72:75],v20 offset:39936\n" \
"v_fmac_f32 v141,v102,v18\nv_lshlrev_b32 v239,16,v103\nM v[44:47],a[20:23],v[60:63],v[44:47]\nv_and_b32 v103,0xffff0000,v103\nv_fmac_f32 v142,v239,v18\nv_fmac_f32 v143,v103,v18\nM v[44:47],a[12:15],v[88:91],v[44:47]\ns_waitcnt lgkmcnt(7)\n" \
"C v136,v136,v137\nC v137,v138,v139\nv_lshlrev_b32 v244,16,v104\nM v[44:47],a[4:7],v[80:83],v[44:47]\nbuffer_load_dwordx4 v2,s[36:39],s47 offen offset:2048 sc1 lds\nv_and_b32 v104,0xffff0000,v104\nM v[32:35],a[32:35],v[92:95],v[32:35]\ns_waitcnt lgkmcnt(6)\n" \
"v_lshlrev_b32 v245,16,v105\nC v138,v140,v141\nC v139,v142,v143\nM v[32:35],a[40:43],v[84:87],v[32:35]\ns_waitcnt vmcnt(12) lgkmcnt(2)\ns_waitcnt lgkmcnt(1)\nds_read_b128 v[60:63],v20 offset:45056\nv_lshlrev_b32 v246,16,v106\n" \
"v_mul_f32 v146,v16,v146\nM v[32:35],a[48:51],v[76:79],v[32:35]\ns_barrier\ns_waitcnt lgkmcnt(1)\nds_read_b128 v[88:91],v20 offset:46080\nv_mul_f32 v147,v16,v147\nM v[32:35],a[56:59],v[72:75],v[32:35]\nds_read_b128 v[80:83],v20 offset:47104\n" \
"v_fmac_f32 v144,v244,v18\nv_and_b32 v105,0xffff0000,v105\nM v[36:39],a[60:63],v[72:75],v[36:39]\nds_read_b128 v[72:75],v20 offset:48128\nv_fmac_f32 v145,v104,v18\nv_lshlrev_b32 v247,16,v107\nM v[36:39],a[52:55],v[76:79],v[36:39]\nbuffer_load_dwordx4 v2,s[36:39],s47 offen offset:3072 sc1 lds\n" \
"v_mul_f32 v148,v16,v148\nM v[36:39],a[44:47],v[84:87],v[36:39]\ns_add_u32 m0,s22,16384\nv_mul_f32 v149,v16,v149\nv_mul_f32 v150,v16,v150\nv_and_b32 v106,0xffff0000,v106\nM v[36:39],a[36:39],v[92:95],v[36:39]\ns_waitcnt lgkmcnt(3)\n" \
"s_add_u32 s47,s29,16384\nv_fmac_f32 v146,v245,v18\nv_fmac_f32 v147,v105,v18\nC v144,v144,v145\nM v[40:43],a[32:35],v[60:63],v[40:43]\ns_waitcnt lgkmcnt(2)\nv_mul_f32 v151,v16,v151\nv_and_b32 v107,0xffff0000,v107\n" \
"v_fmac_f32 v148,v246,v18\nM v[40:43],a[40:43],v[88:91],v[40:43]\ns_waitcnt lgkmcnt(1)\nds_read_b128 v[76:79],v20 offset:16384\nv_fmac_f32 v149,v106,v18\nv_fmac_f32 v150,v247,v18\nM v[40:43],a[48:51],v[80:83],v[40:43]\ns_waitcnt lgkmcnt(1)\n" \
"ds_read_b128 v[84:87],v20 offset:17408\nC v145,v146,v147\nv_fmac_f32 v151,v107,v18\nM v[40:43],a[56:59],v[72:75],v[40:43]\nbuffer_store_dwordx4 v[136:139],v4,s[36:39],s62 offen offset:1024 sc1\nds_read_b128 v[92:95],v20 offset:18432\nM v[44:47],a[60:63],v[72:75],v[44:47]\nds_read_b128 v[72:75],v20 offset:19456\n" \
"v_sub_f32 v19,v15,v29\nC v146,v148,v149\nM v[44:47],a[52:55],v[80:83],v[44:47]\nv_exp_f32 v17,v17\nv_lshlrev_b32 v252,16,v108\nM v[44:47],a[44:47],v[88:91],v[44:47]\ns_waitcnt lgkmcnt(3)\nC v147,v150,v151\n" \
"v_lshlrev_b32 v253,16,v109\nv_mul_f32 v152,v16,v152\nM v[44:47],a[36:39],v[60:63],v[44:47]\nbuffer_load_dwordx4 v2,s[36:39],s47 offen offset:0 sc1 lds\nv_mul_f32 v153,v16,v153\nM v[32:35],a[64:67],v[76:79],v[32:35]\ns_waitcnt lgkmcnt(2)\nv_exp_f32 v19,v19\n" \
"v_and_b32 v108,0xffff0000,v108\nM v[32:35],a[72:75],v[84:87],v[32:35]\ns_waitcnt lgkmcnt(1)\nds_read_b128 v[80:83],v20 offset:24576\nv_mul_f32 v15,v25,v17\nv_mul_f32 v154,v16,v154\nM v[32:35],a[80:83],v[92:95],v[32:35]\ns_waitcnt lgkmcnt(1)\n" \
"ds_read_b128 v[88:91],v20 offset:25600\nv_lshlrev_b32 v254,16,v110\nv_mul_f32 v155,v16,v155\nM v[32:35],a[88:91],v[72:75],v[32:35]\nds_read_b128 v[60:63],v20 offset:26624\nv_and_b32 v109,0xffff0000,v109\nv_fmac_f32 v152,v252,v18\nM v[36:39],a[92:95],v[72:75],v[36:39]\n" \
"ds_read_b128 v[72:75],v20 offset:27648\nv_fmac_f32 v153,v108,v18\nv_lshlrev_b32 v255,16,v111\nM v[36:39],a[84:87],v[92:95],v[36:39]\nbuffer_load_dwordx4 v2,s[36:39],s47 offen offset:1024 sc1 lds\nv_fmac_f32 v15,v31,v19\nM v[36:39],a[76:79],v[84:87],v[36:39]\nv_mul_f32 v156,v16,v156\n" \
"v_mul_f32 v157,v16,v157\nv_and_b32 v110,0xffff0000,v110\nM v[36:39],a[68:71],v[76:79],v[36:39]\ns_waitcnt lgkmcnt(3)\nv_fmac_f32 v154,v253,v18\nv_fmac_f32 v155,v109,v18\nv_mul_f32 v158,v16,v158\nM v[40:43],a[64:67],v[80:83],v[40:43]\n" \
"s_waitcnt lgkmcnt(2)\nC v152,v152,v153\nv_mul_f32 v159,v16,v159\nv_and_b32 v111,0xffff0000,v111\nM v[40:43],a[72:75],v[88:91],v[40:43]\ns_waitcnt lgkmcnt(1)\nds_read_b128 v[92:95],v20 offset:20480\nv_fmac_f32 v156,v254,v18\n" \
"v_rcp_f32 v15,v15\nM v[40:43],a[80:83],v[60:63],v[40:43]\ns_waitcnt lgkmcnt(1)\nds_read_b128 v[84:87],v20 offset:21504\nv_fmac_f32 v157,v110,v18\nC v153,v154,v155\nM v[40:43],a[88:91],v[72:75],v[40:43]\nbuffer_store_dwordx4 v[144:147],v4,s[36:39],s62 offen offset:2048 sc1\n" \
"ds_read_b128 v[76:79],v20 offset:22528\nM v[44:47],a[92:95],v[72:75],v[44:47]\nds_read_b128 v[72:75],v20 offset:23552\nv_fmac_f32 v158,v255,v18\nv_fmac_f32 v159,v111,v18\nM v[44:47],a[84:87],v[60:63],v[44:47]\nv_mul_f32 v17,v17,v15\nC v154,v156,v157\n" \
"C v155,v158,v159\nM v[44:47],a[76:79],v[88:91],v[44:47]\ns_waitcnt lgkmcnt(3)\nv_lshlrev_b32 v228,16,v112\nv_mul_f32 v19,v19,v15\nv_lshlrev_b32 v229,16,v113\nM v[44:47],a[68:71],v[80:83],v[44:47]\nbuffer_load_dwordx2 v[10:11],v5,s[36:39],0 offen sc1\n" \
"v_mul_f32 v160,v17,v160\nM v[32:35],a[96:99],v[92:95],v[32:35]\ns_waitcnt lgkmcnt(2)\nv_mul_f32 v161,v17,v161\nv_and_b32 v112,0xffff0000,v112\nv_mul_f32 v162,v17,v162\nM v[32:35],a[104:107],v[84:87],v[32:35]\ns_waitcnt vmcnt(14) lgkmcnt(12)\n" \
"s_waitcnt lgkmcnt(1)\nds_read_b128 v[60:63],v20 offset:28672\nv_lshlrev_b32 v230,16,v114\nv_mul_f32 v163,v17,v163\nM v[32:35],a[112:115],v[76:79],v[32:35]\ns_barrier\ns_waitcnt lgkmcnt(1)\nds_read_b128 v[88:91],v20 offset:29696\n" \
"v_and_b32 v113,0xffff0000,v113\nM v[32:35],a[120:123],v[72:75],v[32:35]\nds_read_b128 v[80:83],v20 offset:30720\nv_fmac_f32 v160,v228,v19\nv_fmac_f32 v161,v112,v19\nM v[36:39],a[124:127],v[72:75],v[36:39]\nds_read_b128 v[72:75],v20 offset:31744\nv_mul_f32 v164,v17,v164\n" \
"v_mul_f32 v165,v17,v165\nM v[36:39],a[116:119],v[76:79],v[36:39]\nbuffer_load_dwordx4 v2,s[36:39],s47 offen offset:2048 sc1 lds\nv_and_b32 v114,0xffff0000,v114\nM v[36:39],a[108:111],v[84:87],v[36:39]\nv_lshlrev_b32 v231,16,v115\nv_mul_f32 v166,v17,v166\nv_fmac_f32 v162,v229,v19\n" \
"M v[36:39],a[100:103],v[92:95],v[36:39]\ns_waitcnt lgkmcnt(3)\nv_fmac_f32 v163,v113,v19\nC v160,v160,v161\nv_mul_f32 v167,v17,v167\nM v[40:43],a[96:99],v[60:63],v[40:43]\ns_waitcnt lgkmcnt(2)\nv_fmac_f32 v164,v230,v19\n" \
"v_fmac_f32 v165,v114,v19\nv_and_b32 v115,0xffff0000,v115\nM v[40:43],a[104:107],v[88:91],v[40:43]\ns_waitcnt lgkmcnt(1)\nds_read_b128 v[76:79],v20 offset:49152\nv_fmac_f32 v166,v231,v19\nC v161,v162,v163\nM v[40:43],a[112:115],v[80:83],v[40:43]\n" \
"s_waitcnt lgkmcnt(1)\nds_read_b128 v[84:87],v20 offset:50176\nv_fmac_f32 v167,v115,v19\nC v162,v164,v165\nM v[40:43],a[120:123],v[72:75],v[40:43]\nbuffer_store_dwordx4 v[152:155],v4,s[36:39],s62 offen offset:3072 sc1\nds_read_b128 v[92:95],v20 offset:51200\nM v[44:47],a[124:127],v[72:75],v[44:47]\n" \
"s_add_u32 s62,s51,262144\nds_read_b128 v[72:75],v20 offset:52224\nC v163,v166,v167\nv_lshlrev_b32 v236,16,v116\nM v[44:47],a[116:119],v[80:83],v[44:47]\ns_lshl_b32 s59,s61,20\nv_mul_f32 v168,v17,v168\nv_mul_f32 v169,v17,v169\n" \
"v_and_b32 v116,0xffff0000,v116\nM v[44:47],a[108:111],v[88:91],v[44:47]\ns_lshl_b32 s58,s60,15\nv_lshlrev_b32 v237,16,v117\nv_lshlrev_b32 v238,16,v118\nM v[44:47],a[100:103],v[60:63],v[44:47]\ns_waitcnt lgkmcnt(3)\nbuffer_load_dwordx4 v2,s[36:39],s47 offen offset:3072 sc1 lds\n" \
"v_mul_f32 v170,v17,v170\nM v[192:195],a[0:3],v[76:79],0\ns_waitcnt lgkmcnt(2)\ns_add_u32 m0,s22,49152\nv_lshlrev_b32 v239,16,v119\nv_mul_f32 v171,v17,v171\nv_fmac_f32 v168,v236,v19\nM v[192:195],a[8:11],v[84:87],v[192:195]\n" \
"s_waitcnt lgkmcnt(1)\ns_add_u32 s47,s30,16384\nds_read_b128 v[80:83],v20 offset:57344\nv_fmac_f32 v169,v116,v19\nv_and_b32 v117,0xffff0000,v117\nM v[192:195],a[16:19],v[92:95],v[192:195]\ns_waitcnt lgkmcnt(1)\ns_add_u32 s94,s59,s58\n" \
"ds_read_b128 v[88:91],v20 offset:58368\nds_read_b128 v[60:63],v20 offset:59392\nv_mul_f32 v172,v17,v172\nM v[192:195],a[24:27],v[72:75],v[192:195]\nbuffer_load_dwordx4 v2,s[36:39],s47 offen offset:0 sc1 lds\nv_mul_f32 v173,v17,v173\nM v[196:199],a[28:31],v[72:75],0\ns_add_u32 s58,s60,1\n" \
"ds_read_b128 v[72:75],v20 offset:60416\nv_and_b32 v118,0xffff0000,v118\nv_mul_f32 v174,v17,v174\nM v[196:199],a[20:23],v[92:95],v[196:199]\ns_and_b32 s58,s58,3\nv_mul_f32 v175,v17,v175\nv_fmac_f32 v170,v237,v19\nv_and_b32 v119,0xffff0000,v119\n" \
"M v[196:199],a[12:15],v[84:87],v[196:199]\ns_and_b32 s59,s58,2\nv_fmac_f32 v171,v117,v19\nC v168,v168,v169\nM v[196:199],a[4:7],v[76:79],v[196:199]\ns_waitcnt lgkmcnt(3)\ns_add_u32 s59,s59,s58\nv_fmac_f32 v172,v238,v19\n" \
"v_fmac_f32 v173,v118,v19\nM v[200:203],a[0:3],v[80:83],0\ns_waitcnt lgkmcnt(1)\ns_add_u32 s94,s94,58720256\nv_fmac_f32 v174,v239,v19\nv_fmac_f32 v175,v119,v19\nM v[200:203],a[8:11],v[88:91],v[200:203]\ns_lshl_b32 s59,s59,17\n" \
"ds_read_b128 v[92:95],v20 offset:53248\nds_read_b128 v[84:87],v20 offset:54272\nM v[200:203],a[16:19],v[60:63],v[200:203]\ns_waitcnt lgkmcnt(2)\nbuffer_load_dwordx4 v2,s[36:39],s47 offen offset:1024 sc1 lds\nC v169,v170,v171\nM v[200:203],a[24:27],v[72:75],v[200:203]\ns_add_u32 s32,s94,s59\n" \
"ds_read_b128 v[76:79],v20 offset:55296\nC v170,v172,v173\nC v171,v174,v175\nM v[204:207],a[28:31],v[72:75],0\ns_lshl_b32 s58,s58,1\nds_read_b128 v[72:75],v20 offset:56320\nv_lshlrev_b32 v244,16,v120\nM v[204:207],a[20:23],v[60:63],v[204:207]\n" \
"s_and_b32 s59,%1,0xf9\nv_lshlrev_b32 v245,16,v121\nv_mul_f32 v176,v17,v176\nM v[204:207],a[12:15],v[88:91],v[204:207]\nbuffer_store_dwordx4 v[160:163],v4,s[36:39],s62 offen offset:0 sc1\nM v[204:207],a[4:7],v[80:83],v[204:207]\ns_waitcnt lgkmcnt(2)\ns_or_b32 s58,s58,s59\n" \
"ds_read_b128 v[60:63],v20 offset:61440\nv_mul_f32 v177,v17,v177\nM v[192:195],a[32:35],v[92:95],v[192:195]\ns_lshl_b32 s58,s58,2\nds_read_b128 v[88:91],v20 offset:62464\nv_and_b32 v120,0xffff0000,v120\nM v[192:195],a[40:43],v[84:87],v[192:195]\ns_waitcnt vmcnt(16) lgkmcnt(14)\n" \
"s_waitcnt lgkmcnt(3)\nds_read_b128 v[80:83],v20 offset:63488\nv_lshlrev_b32 v246,16,v122\nM v[192:195],a[48:51],v[76:79],v[192:195]\ns_add_u32 s58,s58,%2\ns_barrier\ns_waitcnt lgkmcnt(3)\nv_mul_f32 v178,v17,v178\n" \
"v_lshlrev_b32 v247,16,v123\nM v[192:195],a[56:59],v[72:75],v[192:195]\ns_mul_i32 s58,s58,12288\nv_mul_f32 v179,v17,v179\nv_and_b32 v121,0xffff0000,v121\nM v[196:199],a[60:63],v[72:75],v[196:199]\ns_add_u32 s44,s58,109058048\nds_read_b128 v[72:75],v20 offset:64512\n" \
"v_fmac_f32 v176,v244,v19\nv_fmac_f32 v177,v120,v19\nM v[196:199],a[52:55],v[76:79],v[196:199]\ns_add_u32 s58,s60,2\nv_mul_f32 v180,v17,v180\nv_mul_f32 v181,v17,v181\nM v[196:199],a[44:47],v[84:87],v[196:199]\ns_and_b32 s58,s58,3\n" \
"v_mul_f32 v182,v17,v182\nv_and_b32 v122,0xffff0000,v122\nM v[196:199],a[36:39],v[92:95],v[196:199]\ns_waitcnt lgkmcnt(1)\ns_and_b32 s59,s58,2\nv_mul_f32 v183,v17,v183\nv_and_b32 v123,0xffff0000,v123\nM v[200:203],a[32:35],v[60:63],v[200:203]\n" \
"s_add_u32 s59,s59,s58\nds_read_b128 v[76:79],v4 offset:0\nv_fmac_f32 v178,v245,v19\nv_fmac_f32 v179,v121,v19\nM v[200:203],a[40:43],v[88:91],v[200:203]\ns_lshl_b32 s59,s59,17\nds_read_b128 v[84:87],v4 offset:1024\nC v176,v176,v177\n" \
"M v[200:203],a[48:51],v[80:83],v[200:203]\ns_waitcnt lgkmcnt(2)\ns_add_u32 s33,s94,s59\nv_fmac_f32 v180,v246,v19\nv_fmac_f32 v181,v122,v19\nM v[200:203],a[56:59],v[72:75],v[200:203]\nbuffer_store_dwordx4 v[168:171],v4,s[36:39],s62 offen offset:1024 sc1\nds_read_b128 v[92:95],v4 offset:2048\n" \
"M v[204:207],a[60:63],v[72:75],v[204:207]\ns_lshl_b32 s58,s58,1\nds_read_b128 v[72:75],v4 offset:3072\nv_fmac_f32 v182,v247,v19\nv_fmac_f32 v183,v123,v19\nM v[204:207],a[52:55],v[80:83],v[204:207]\ns_and_b32 s59,%1,0xf9\nds_read_b128 v[80:83],v4 offset:8192\n" \
"C v177,v178,v179\nM v[204:207],a[44:47],v[88:91],v[204:207]\ns_or_b32 s58,s58,s59\nC v178,v180,v181\nC v179,v182,v183\nM v[204:207],a[36:39],v[60:63],v[204:207]\ns_waitcnt lgkmcnt(3)\nbuffer_load_dwordx4 v2,s[36:39],s47 offen offset:2048 sc1 lds\n" \
"v_lshlrev_b32 v252,16,v124\nM v[192:195],a[64:67],v[76:79],v[192:195]\ns_lshl_b32 s58,s58,2\nds_read_b128 v[88:91],v4 offset:9216\nv_mul_f32 v184,v17,v184\nM v[192:195],a[72:75],v[84:87],v[192:195]\ns_waitcnt lgkmcnt(3)\ns_add_u32 s58,s58,%2\n" \
"ds_read_b128 v[60:63],v4 offset:10240\nv_mul_f32 v185,v17,v185\nM v[192:195],a[80:83],v[92:95],v[192:195]\ns_waitcnt lgkmcnt(3)\ns_mul_i32 s58,s58,12288\nv_lshlrev_b32 v253,16,v125\nv_and_b32 v124,0xffff0000,v124\nM v[192:195],a[88:91],v[72:75],v[192:195]\n" \
"s_add_u32 s45,s58,109060096\nv_lshlrev_b32 v254,16,v126\nv_mul_f32 v186,v17,v186\nM v[196:199],a[92:95],v[72:75],v[196:199]\ns_add_u32 s58,s60,3\nds_read_b128 v[72:75],v4 offset:11264\nv_mul_f32 v187,v17,v187\nM v[196:199],a[84:87],v[92:95],v[196:199]\n" \
"buffer_load_dwordx4 v2,s[36:39],s47 offen offset:3072 sc1 lds\nM v[196:199],a[76:79],v[84:87],v[196:199]\ns_add_u32 m0,s22,131072\nds_read_b128 v[92:95],v4 offset:4096\nv_fmac_f32 v184,v252,v19\nM v[196:199],a[68:71],v[76:79],v[196:199]\ns_waitcnt lgkmcnt(2)\ns_add_u32 s47,s31,0\n" \
"ds_read_b128 v[84:87],v4 offset:5120\nv_and_b32 v125,0xffff0000,v125\nM v[200:203],a[64:67],v[80:83],v[200:203]\ns_and_b32 s58,s58,3\nds_read_b128 v[76:79],v4 offset:6144\nv_fmac_f32 v185,v124,v19\nM v[200:203],a[72:75],v[88:91],v[200:203]\ns_and_b32 s59,s58,2\n" \
"v_mul_f32 v188,v17,v188\nv_mul_f32 v189,v17,v189\nM v[200:203],a[80:83],v[60:63],v[200:203]\ns_waitcnt lgkmcnt(3)\ns_add_u32 s59,s59,s58\nv_and_b32 v126,0xffff0000,v126\nv_fmac_f32 v186,v253,v19\nM v[200:203],a[88:91],v[72:75],v[200:203]\n" \
"buffer_store_dwordx4 v[176:179],v4,s[36:39],s62 offen offset:2048 sc1\nv_fmac_f32 v187,v125,v19\nM v[204:207],a[92:95],v[72:75],v[204:207]\ns_lshl_b32 s59,s59,17\nds_read_b128 v[72:75],v4 offset:7168\nv_lshlrev_b32 v255,16,v127\nC v184,v184,v185\nM v[204:207],a[84:87],v[60:63],v[204:207]\n" \
"s_add_u32 s34,s94,s59\nds_read_b128 v[60:63],v4 offset:12288\nv_mul_f32 v190,v17,v190\nM v[204:207],a[76:79],v[88:91],v[204:207]\nLG22_%=:\ns_cbranch_execz LG24_%=\ns_lshl_b32 s58,s58,1\nds_read_b128 v[88:91],v4 offset:13312\n" \
"v_mul_f32 v191,v17,v191\nM v[204:207],a[68:71],v[80:83],v[204:207]\ns_waitcnt lgkmcnt(3)\ns_and_b32 s59,%1,0xf9\nv_fmac_f32 v188,v254,v19\nv_fmac_f32 v189,v126,v19\nM v[192:195],a[96:99],v[92:95],v[192:195]\ns_or_b32 s58,s58,s59\n" \
"v_and_b32 v127,0xffff0000,v127\nC v185,v186,v187\nM v[192:195],a[104:107],v[84:87],v[192:195]\ns_waitcnt vmcnt(15) lgkmcnt(14)\nds_read_b128 v[80:83],v4 offset:14336\nv_fmac_f32 v190,v255,v19\nM v[192:195],a[112:115],v[76:79],v[192:195]\ns_waitcnt vmcnt(10)\n" \
"s_barrier\ns_waitcnt lgkmcnt(3)\nv_cmp_gt_u64 s[98:99],s[14:15],v[10:11]\nv_cmp_le_u64 s[18:19],s[16:17],v[10:11]\nM v[192:195],a[120:123],v[72:75],v[192:195]\ns_or_b64 s[98:99],s[98:99],s[18:19]\ns_and_b32 s98,s98,0xff000000\ns_cbranch_scc1 LPM_%=\n" \
"LPMR_%=:\nbuffer_load_dwordx4 v2,s[36:39],s47 offen offset:0 sc1 lds\nv_fmac_f32 v191,v127,v19\nM v[196:199],a[124:127],v[72:75],v[196:199]\ns_lshl_b32 s58,s58,2\nds_read_b128 v[72:75],v4 offset:15360\nC v186,v188,v189\nC v187,v190,v191\n" \
"M v[196:199],a[116:119],v[76:79],v[196:199]\ns_add_u32 s58,s58,%2\nds_read_b128 v[76:79],v4 offset:32768\nv_mov_b32 v29,s11\nM v[196:199],a[108:111],v[84:87],v[196:199]\ns_mul_i32 s58,s58,12288\nds_read_b128 v[84:87],v4 offset:33792\nv_xor_b32 v6,0x20000,v6\n" \
"M v[196:199],a[100:103],v[92:95],v[196:199]\ns_waitcnt lgkmcnt(3)\nbuffer_load_dwordx4 v2,s[36:39],s47 offen offset:1024 sc1 lds\nM v[200:203],a[96:99],v[60:63],v[200:203]\ns_add_u32 s48,s58,109062144\nds_read_b128 v[92:95],v4 offset:34816\nv_xor_b32 v7,0x20000,v7\nM v[200:203],a[104:107],v[88:91],v[200:203]\n" \
"s_add_u32 s58,s60,4\nv_xor_b32 v8,0x20000,v8\nv_xor_b32 v9,0x20000,v9\nM v[200:203],a[112:115],v[80:83],v[200:203]\ns_waitcnt lgkmcnt(3)\ns_and_b32 s58,s58,3\nv_and_b32 v30,0xf0,v4\nv_lshrrev_b32 v15,2,v30\n" \
"M v[200:203],a[120:123],v[72:75],v[200:203]\nbuffer_store_dwordx4 v[184:187],v4,s[36:39],s62 offen offset:3072 sc1\nM v[204:207],a[124:127],v[72:75],v[204:207]\ns_and_b32 s59,s58,2\nds_read_b128 v[72:75],v4 offset:35840\nv_lshlrev_b32 v13,7,v30\nM v[204:207],a[116:119],v[80:83],v[204:207]\ns_add_u32 s59,s59,s58\n" \
"ds_read_b128 v[80:83],v4 offset:40960\nv_lshl_add_u32 v114,v30,2,s22\nM v[204:207],a[108:111],v[88:91],v[204:207]\ns_and_b32 s58,%1,0xf9\nds_read_b128 v[88:91],v4 offset:41984\nv_bfe_u32 v30,v4,8,1\nM v[204:207],a[100:103],v[60:63],v[204:207]\ns_waitcnt lgkmcnt(3)\n" \
"buffer_load_dwordx4 v2,s[36:39],s47 offen offset:2048 sc1 lds\nM v[48:51],a[128:131],v[76:79],v[48:51]\ns_lshl_b32 s59,s59,17\nds_read_b128 v[60:63],v4 offset:43008\nv_lshrrev_b32 v31,9,v4\nM v[48:51],a[136:139],v[84:87],v[48:51]\ns_lshl_b32 s58,s58,2\nv_lshl_add_u32 v13,v30,5,v13\n" \
"v_lshl_add_u32 v114,v30,3,v114\nM v[48:51],a[144:147],v[92:95],v[48:51]\ns_waitcnt lgkmcnt(3)\ns_add_u32 s35,s94,s59\nv_lshl_add_u32 v13,v31,4,v13\nv_lshl_add_u32 v114,v31,4,v114\nM v[48:51],a[152:155],v[72:75],v[48:51]\nbuffer_load_dwordx4 v2,s[36:39],s47 offen offset:3072 sc1 lds\n" \
"M v[52:55],a[156:159],v[72:75],v[52:55]\ns_add_u32 m0,s22,147456\nds_read_b128 v[72:75],v4 offset:44032\nv_and_b32 v30,0x3c0,v4\nM v[52:55],a[148:151],v[92:95],v[52:55]\ns_add_u32 s47,s31,8192\nds_read_b128 v[92:95],v4 offset:36864\nv_and_b32 v31,0x30,v4\n" \
"M v[52:55],a[140:143],v[84:87],v[52:55]\ns_add_u32 s58,s58,%2\nds_read_b128 v[84:87],v4 offset:37888\nv_lshl_add_u32 v112,v30,5,v31\nM v[52:55],a[132:135],v[76:79],v[52:55]\ns_waitcnt lgkmcnt(3)\nbuffer_load_dwordx4 v1,s[84:87],s46 offen offset:0 lds\nM v[64:67],a[128:131],v[80:83],v[64:67]\n" \
"s_add_u32 m0,s22,148352\nds_read_b128 v[76:79],v4 offset:38912\nv_lshl_add_u32 v113,v30,1,v31\nM v[64:67],a[136:139],v[88:91],v[64:67]\ns_sub_u32 s59,3,s60\nv_lshrrev_b32 v31,1,v31\nv_add3_u32 v115,v30,v31,s22\nM v[64:67],a[144:147],v[60:63],v[64:67]\n" \
"s_waitcnt lgkmcnt(3)\ns_lshl_b32 s95,s58,5\nv_bfe_u32 v30,v4,7,2\nv_xor_b32 v114,0x20000,v114\nM v[64:67],a[152:155],v[72:75],v[64:67]\nbuffer_load_dwordx4 v1,s[84:87],s46 offen offset:128 lds\nM v[68:71],a[156:159],v[72:75],v[68:71]\ns_mov_b32 s46,1638400\n" \
"s_add_u32 m0,s22,149504\nds_read_b128 v[72:75],v4 offset:39936\nv_xor_b32 v115,0x20000,v115\nM v[68:71],a[148:151],v[60:63],v[68:71]\ns_and_b32 s59,s59,3\nds_read_b128 v[60:63],v4 offset:45056\nv_lshrrev_b32 v190,4,v4\nM v[68:71],a[140:143],v[88:91],v[68:71]\n" \
"s_mul_i32 s58,s58,12288\nds_read_b128 v[88:91],v4 offset:46080\nM v[68:71],a[132:135],v[80:83],v[68:71]\ns_waitcnt lgkmcnt(3)\nbuffer_load_dwordx4 v1,s[84:87],s46 offen offset:0 lds\nM v[48:51],a[160:163],v[92:95],v[48:51]\ns_add_u32 m0,s22,150400\nds_read_b128 v[80:83],v4 offset:47104\n" \
"M v[48:51],a[168:171],v[84:87],v[48:51]\ns_waitcnt vmcnt(15) lgkmcnt(15)\nM v[48:51],a[176:179],v[76:79],v[48:51]\ns_lshl_b32 s59,s59,11\ns_barrier\ns_waitcnt lgkmcnt(3)\nds_read_b128 v[176:179],v4 offset:27648\nM v[48:51],a[184:187],v[72:75],v[48:51]\n" \
"buffer_load_dwordx4 v1,s[84:87],s46 offen offset:128 lds\nM v[52:55],a[188:191],v[72:75],v[52:55]\ns_add_u32 m0,s22,65536\ns_mov_b32 s46,1703936\nds_read_b128 v[72:75],v4 offset:48128\nM v[52:55],a[180:183],v[76:79],v[52:55]\ns_add_u32 s59,s59,s58\nds_read_b128 v[76:79],v4 offset:16384\n" \
"M v[52:55],a[172:175],v[84:87],v[52:55]\ns_add_u32 s59,s59,109051904\nds_read_b128 v[84:87],v4 offset:17408\nM v[52:55],a[164:167],v[92:95],v[52:55]\ns_waitcnt lgkmcnt(4)\nbuffer_load_dwordx4 v1,s[84:87],s46 offen offset:0 lds\nM v[64:67],a[160:163],v[60:63],v[64:67]\ns_add_u32 m0,s22,66432\n" \
"ds_read_b128 v[92:95],v4 offset:18432\nM v[64:67],a[168:171],v[88:91],v[64:67]\ns_cmp_eq_u32 s60,0\ns_cselect_b32 s40,0x40000000,s59\nds_read_b128 v[180:183],v4 offset:20480\nM v[64:67],a[176:179],v[80:83],v[64:67]\ns_waitcnt lgkmcnt(4)\ns_sub_u32 s59,4,s60\n" \
"M v[64:67],a[184:187],v[72:75],v[64:67]\nbuffer_load_dwordx4 v1,s[84:87],s46 offen offset:128 lds\nM v[68:71],a[188:191],v[72:75],v[68:71]\ns_add_u32 m0,s22,67584\ns_mov_b32 s46,1769472\nds_read_b128 v[72:75],v4 offset:19456\nM v[68:71],a[180:183],v[80:83],v[68:71]\ns_and_b32 s59,s59,3\n" \
"ds_read_b128 v[80:83],v4 offset:24576\nM v[68:71],a[172:175],v[88:91],v[68:71]\ns_lshl_b32 s59,s59,11\nds_read_b128 v[88:91],v4 offset:25600\nM v[68:71],a[164:167],v[60:63],v[68:71]\ns_waitcnt lgkmcnt(3)\nbuffer_load_dwordx4 v1,s[84:87],s46 offen offset:0 lds\nM v[32:35],a[128:131],v[76:79],v[32:35]\n" \
"s_add_u32 m0,s22,68480\nds_read_b128 v[60:63],v4 offset:26624\nM v[32:35],a[136:139],v[84:87],v[32:35]\ns_add_u32 s59,s59,s58\nM v[32:35],a[144:147],v[92:95],v[32:35]\ns_waitcnt lgkmcnt(3)\ns_add_u32 s59,s59,109150208\nM v[32:35],a[152:155],v[72:75],v[32:35]\n" \
"buffer_load_dwordx4 v1,s[84:87],s46 offen offset:128 lds\nM v[36:39],a[156:159],v[72:75],v[36:39]\ns_add_u32 m0,s22,98304\nds_read_b128 v[72:75],v4 offset:21504\nM v[36:39],a[148:151],v[92:95],v[36:39]\ns_mov_b32 s46,1835008\nds_read_b128 v[92:95],v4 offset:22528\nM v[36:39],a[140:143],v[84:87],v[36:39]\n" \
"s_cmp_eq_u32 s60,1\ns_cselect_b32 s41,0x40000000,s59\nds_read_b128 v[84:87],v4 offset:23552\nM v[36:39],a[132:135],v[76:79],v[36:39]\ns_waitcnt lgkmcnt(3)\nbuffer_load_dwordx4 v2,s[36:39],s47 offen offset:0 sc1 lds\nM v[40:43],a[128:131],v[80:83],v[40:43]\ns_sub_u32 s59,5,s60\n" \
"ds_read_b128 v[76:79],v4 offset:28672\nM v[40:43],a[136:139],v[88:91],v[40:43]\ns_and_b32 s59,s59,3\nM v[40:43],a[144:147],v[60:63],v[40:43]\ns_lshl_b32 s59,s59,11\nM v[40:43],a[152:155],v[176:179],v[40:43]\nbuffer_load_dwordx4 v2,s[36:39],s47 offen offset:1024 sc1 lds\nM v[44:47],a[156:159],v[176:179],v[44:47]\n" \
"s_add_u32 s59,s59,s58\nds_read_b128 v[176:179],v4 offset:29696\nM v[44:47],a[148:151],v[60:63],v[44:47]\ns_waitcnt vmcnt(16) lgkmcnt(13)\nds_read_b128 v[60:63],v4 offset:30720\nM v[44:47],a[140:143],v[88:91],v[44:47]\ns_add_u32 s59,s59,109248512\ns_barrier\n" \
"ds_read_b128 v[88:91],v4 offset:31744\nM v[44:47],a[132:135],v[80:83],v[44:47]\nbuffer_load_dwordx4 v2,s[36:39],s47 offen offset:2048 sc1 lds\nM v[32:35],a[160:163],v[180:183],v[32:35]\ns_cmp_eq_u32 s60,2\ns_cselect_b32 s42,0x40000000,s59\ns_waitcnt lgkmcnt(3)\nds_read_b128 v[80:83],v4 offset:49152\n" \
"M v[32:35],a[168:171],v[72:75],v[32:35]\ns_sub_u32 s59,6,s60\nM v[32:35],a[176:179],v[92:95],v[32:35]\ns_and_b32 s59,s59,3\nM v[32:35],a[184:187],v[84:87],v[32:35]\nbuffer_load_dwordx4 v2,s[36:39],s47 offen offset:3072 sc1 lds\nM v[36:39],a[188:191],v[84:87],v[36:39]\ns_add_u32 m0,s22,81920\n" \
"s_add_u32 s47,s31,16384\nds_read_b128 v[84:87],v4 offset:50176\nM v[36:39],a[180:183],v[92:95],v[36:39]\ns_lshl_b32 s59,s59,11\nds_read_b128 v[92:95],v4 offset:51200\nM v[36:39],a[172:175],v[72:75],v[36:39]\ns_add_u32 s59,s59,s58\nds_read_b128 v[72:75],v4 offset:52224\n" \
"M v[36:39],a[164:167],v[180:183],v[36:39]\nbuffer_load_dwordx4 v2,s[36:39],s47 offen offset:0 sc1 lds\nM v[40:43],a[160:163],v[76:79],v[40:43]\ns_waitcnt lgkmcnt(3)\ns_add_u32 s59,s59,109346816\nds_read_b128 v[180:183],v4 offset:57344\nM v[40:43],a[168:171],v[176:179],v[40:43]\ns_and_b32 s58,s61,6\n" \
"M v[40:43],a[176:179],v[60:63],v[40:43]\ns_cmp_eq_u32 s60,3\ns_cselect_b32 s43,0x40000000,s59\nM v[40:43],a[184:187],v[88:91],v[40:43]\nbuffer_load_dwordx4 v2,s[36:39],s47 offen offset:1024 sc1 lds\nM v[44:47],a[188:191],v[88:91],v[44:47]\ns_add_u32 s58,s58,s61\nds_read_b128 v[88:91],v4 offset:58368\n" \
"M v[44:47],a[180:183],v[60:63],v[44:47]\ns_lshl_b32 s59,s60,4\nds_read_b128 v[60:63],v4 offset:59392\nM v[44:47],a[172:175],v[176:179],v[44:47]\ns_add_u32 s92,s58,s59\nds_read_b128 v[176:179],v4 offset:60416\nM v[44:47],a[164:167],v[76:79],v[44:47]\nbuffer_load_dwordx4 v2,s[36:39],s47 offen offset:2048 sc1 lds\n" \
"M v[192:195],a[128:131],v[80:83],v[192:195]\ns_waitcnt lgkmcnt(3)\ns_and_b32 s58,s60,2\nds_read_b128 v[76:79],v4 offset:53248\nM v[192:195],a[136:139],v[84:87],v[192:195]\ns_add_u32 s58,s58,s60\nM v[192:195],a[144:147],v[92:95],v[192:195]\ns_lshl_b32 s59,s61,3\n" \
"M v[192:195],a[152:155],v[72:75],v[192:195]\nbuffer_load_dwordx4 v2,s[36:39],s47 offen offset:3072 sc1 lds\nM v[196:199],a[156:159],v[72:75],v[196:199]\ns_add_u32 m0,s22,114688\nds_read_b128 v[72:75],v4 offset:54272\nM v[196:199],a[148:151],v[92:95],v[196:199]\ns_add_u32 s47,s28,24576\nds_read_b128 v[92:95],v4 offset:55296\n" \
"M v[196:199],a[140:143],v[84:87],v[196:199]\ns_lshr_b32 s63,%1,5\nds_read_b128 v[84:87],v4 offset:56320\nM v[196:199],a[132:135],v[80:83],v[196:199]\nbuffer_load_dwordx4 v1,s[84:87],s46 offen offset:0 lds\nM v[200:203],a[128:131],v[180:183],v[200:203]\ns_waitcnt lgkmcnt(3)\ns_add_u32 m0,s22,115584\n" \
"ds_read_b128 v[80:83],v4 offset:61440\nM v[200:203],a[136:139],v[88:91],v[200:203]\ns_add_u32 s58,s58,s59\nM v[200:203],a[144:147],v[60:63],v[200:203]\ns_lshl_b32 s63,s63,2\nM v[200:203],a[152:155],v[176:179],v[200:203]\nbuffer_load_dwordx4 v1,s[84:87],s46 offen offset:128 lds\nM v[204:207],a[156:159],v[176:179],v[204:207]\n" \
"s_add_u32 m0,s22,116736\nds_read_b128 v[176:179],v4 offset:62464\nM v[204:207],a[148:151],v[60:63],v[204:207]\ns_waitcnt vmcnt(18) lgkmcnt(14)\nds_read_b128 v[60:63],v4 offset:63488\nM v[204:207],a[140:143],v[88:91],v[204:207]\ns_mov_b32 s46,1900544\ns_barrier\n" \
"ds_read_b128 v[88:91],v4 offset:64512\nM v[204:207],a[132:135],v[180:183],v[204:207]\nbuffer_load_dwordx4 v1,s[84:87],s46 offen offset:0 lds\nM v[192:195],a[160:163],v[76:79],v[192:195]\ns_waitcnt lgkmcnt(3)\ns_add_u32 m0,s22,117632\nds_read_b128 v[180:183],v23 offset:0\nM v[192:195],a[168:171],v[72:75],v[192:195]\n" \
"s_add_u32 s93,s58,64\nM v[192:195],a[176:179],v[92:95],v[192:195]\ns_add_u32 s63,s63,%2\nM v[192:195],a[184:187],v[84:87],v[192:195]\ns_waitcnt vmcnt(21)\nM v[196:199],a[188:191],v[84:87],v[196:199]\nbuffer_store_dwordx2 v[28:29],v56,s[36:39],s50 offen offset:0 sc1\nM v[196:199],a[180:183],v[92:95],v[196:199]\n" \
"s_lshl_b32 s58,s92,15\nds_read_b128 v[84:87],v23 offset:1024\nds_read_b128 v[92:95],v23 offset:2048\nM v[196:199],a[172:175],v[72:75],v[196:199]\nbuffer_load_dwordx4 v1,s[84:87],s46 offen offset:128 lds\nM v[196:199],a[164:167],v[76:79],v[196:199]\ns_add_u32 m0,s22,0\nM v[200:203],a[160:163],v[80:83],v[200:203]\n" \
"s_waitcnt lgkmcnt(2)\ns_lshl_b32 s59,s63,6\nds_read_b128 v[72:75],v23 offset:3072\nM v[200:203],a[168:171],v[176:179],v[200:203]\ns_add_u32 s80,s58,s59\nds_read_b128 v[76:79],v23 offset:8192\nM v[200:203],a[176:179],v[60:63],v[200:203]\nbuffer_load_dwordx4 v2,s[36:39],s47 offen offset:0 sc1 lds\n" \
"M v[200:203],a[184:187],v[88:91],v[200:203]\ns_and_b32 s58,s58,0x3e0000\nM v[204:207],a[188:191],v[88:91],v[204:207]\ns_lshl_b32 s59,s63,12\nds_read_b128 v[88:91],v23 offset:9216\nM v[204:207],a[180:183],v[60:63],v[204:207]\ns_add_u32 s58,s58,s59\nds_read_b128 v[60:63],v23 offset:10240\n" \
"M v[204:207],a[172:175],v[176:179],v[204:207]\nbuffer_load_dwordx4 v2,s[36:39],s47 offen offset:1024 sc1 lds\nM v[204:207],a[164:167],v[80:83],v[204:207]\ns_and_b32 s59,s92,1\nds_read_b128 v[176:179],v23 offset:11264\nM v[208:211],a[0:3],v[180:183],0\ns_waitcnt lgkmcnt(3)\ns_lshl_b32 s59,s59,6\n" \
"ds_read_b128 v[80:83],v23 offset:4096\nM v[208:211],a[8:11],v[84:87],v[208:211]\ns_add_u32 s58,s58,s59\nM v[208:211],a[16:19],v[92:95],v[208:211]\nbuffer_load_dwordx4 v2,s[36:39],s47 offen offset:2048 sc1 lds\nM v[208:211],a[24:27],v[72:75],v[208:211]\ns_add_u32 s82,s58,67108864\nM v[212:215],a[28:31],v[72:75],0\n" \
"s_lshl_b32 s58,s93,15\nds_read_b128 v[72:75],v23 offset:5120\nM v[212:215],a[20:23],v[92:95],v[212:215]\ns_lshl_b32 s59,s63,6\nds_read_b128 v[92:95],v23 offset:6144\nM v[212:215],a[12:15],v[84:87],v[212:215]\nbuffer_load_dwordx4 v2,s[36:39],s47 offen offset:3072 sc1 lds\nM v[212:215],a[4:7],v[180:183],v[212:215]\n" \
"s_add_u32 m0,s22,32768\ns_add_u32 s47,s29,24576\nds_read_b128 v[84:87],v23 offset:7168\nM v[216:219],a[0:3],v[76:79],0\ns_waitcnt lgkmcnt(3)\ns_add_u32 s81,s58,s59\nds_read_b128 v[180:183],v23 offset:12288\nM v[216:219],a[8:11],v[88:91],v[216:219]\n" \
"s_and_b32 s58,s58,0x3e0000\nM v[216:219],a[16:19],v[60:63],v[216:219]\nbuffer_load_dwordx4 v2,s[36:39],s47 offen offset:0 sc1 lds\nM v[216:219],a[24:27],v[176:179],v[216:219]\ns_lshl_b32 s59,s63,12\nM v[220:223],a[28:31],v[176:179],0\ns_add_u32 s58,s58,s59\nds_read_b128 v[176:179],v23 offset:13312\n" \
"M v[220:223],a[20:23],v[60:63],v[220:223]\ns_waitcnt vmcnt(10) lgkmcnt(14)\nds_read_b128 v[60:63],v23 offset:14336\nM v[220:223],a[12:15],v[88:91],v[220:223]\nbuffer_load_dwordx4 v2,s[36:39],s47 offen offset:1024 sc1 lds\ns_barrier\nM v[220:223],a[4:7],v[76:79],v[220:223]\ns_and_b32 s59,s93,1\n" \
"ds_read_b128 v[88:91],v23 offset:15360\nM v[208:211],a[32:35],v[80:83],v[208:211]\ns_waitcnt lgkmcnt(3)\ns_lshl_b32 s59,s59,6\nds_read_b128 v[76:79],v20 offset:32768\nM v[208:211],a[40:43],v[72:75],v[208:211]\ns_add_u32 s58,s58,s59\nds_read_b64_tr_b16 a[192:193],v6 offset:16384\n" \
"M v[208:211],a[48:51],v[92:95],v[208:211]\nbuffer_load_dwordx4 v2,s[36:39],s47 offen offset:2048 sc1 lds\nM v[208:211],a[56:59],v[84:87],v[208:211]\ns_add_u32 s83,s58,67108864\nds_read_b64_tr_b16 a[194:195],v7 offset:16384\nM v[212:215],a[60:63],v[84:87],v[212:215]\ns_lshl_b32 s58,s61,10\nds_read_b128 v[84:87],v20 offset:33792\n" \
"M v[212:215],a[52:55],v[92:95],v[212:215]\ns_lshl_b32 s59,s60,6\nds_read_b128 v[92:95],v20 offset:34816\nM v[212:215],a[44:47],v[72:75],v[212:215]\nbuffer_load_dwordx4 v2,s[36:39],s47 offen offset:3072 sc1 lds\nM v[212:215],a[36:39],v[80:83],v[212:215]\ns_add_u32 m0,s22,16384\nds_read_b128 v[72:75],v20 offset:35840\n" \
"M v[216:219],a[32:35],v[180:183],v[216:219]\ns_waitcnt lgkmcnt(4)\ns_add_u32 s47,s30,24576\nds_read_b128 v[80:83],v20 offset:40960\nM v[216:219],a[40:43],v[176:179],v[216:219]\ns_add_u32 s58,s58,s59\nds_read_b64_tr_b16 a[200:201],v6 offset:18432\nv_xor_b32 v6,0x30000,v6\n" \
"M v[216:219],a[48:51],v[60:63],v[216:219]\nbuffer_load_dwordx4 v2,s[36:39],s47 offen offset:0 sc1 lds\nM v[216:219],a[56:59],v[88:91],v[216:219]\ns_add_u32 s95,s95,94437376\nM v[220:223],a[60:63],v[88:91],v[220:223]\ns_add_u32 s58,s58,94396416\nds_read_b128 v[88:91],v20 offset:41984\nv_lshl_add_u32 v58,v30,8,s58\n" \
"M v[220:223],a[52:55],v[60:63],v[220:223]\ns_mov_b64 s[64:65],s[12:13]\nds_read_b128 v[60:63],v20 offset:43008\nv_bfe_u32 v30,v4,4,3\nM v[220:223],a[44:47],v[176:179],v[220:223]\nbuffer_load_dwordx4 v2,s[36:39],s47 offen offset:1024 sc1 lds\nM v[220:223],a[36:39],v[180:183],v[220:223]\ns_add_u32 s58,s95,24\n" \
"ds_read_b128 v[176:179],v20 offset:44032\nv_lshl_add_u32 v58,v30,3,v58\nM v[208:211],a[64:67],v[76:79],v[208:211]\ns_waitcnt lgkmcnt(3)\ns_and_b32 s65,s65,0xffff\nds_read_b128 v[180:183],v20 offset:36864\nv_and_b32 v30,0x30,v4\nM v[208:211],a[72:75],v[84:87],v[208:211]\n" \
"s_mov_b32 s66,4194304\nds_read_b64_tr_b16 a[196:197],v8 offset:16384\ns_mov_b32 exec_lo,0\nv_lshl_add_u32 v58,v30,4,s58\ns_mov_b32 exec_lo,-1\ns_nop 1\nM v[208:211],a[80:83],v[92:95],v[208:211]\ns_mov_b32 s67,0x00020000\n" \
"ds_read_b64_tr_b16 a[202:203],v7 offset:18432\nv_xor_b32 v7,0x30000,v7\nM v[208:211],a[88:91],v[72:75],v[208:211]\nbuffer_load_dwordx4 v2,s[36:39],s47 offen offset:2048 sc1 lds\nM v[212:215],a[92:95],v[72:75],v[212:215]\ns_mov_b32 s58,0x300003\nds_read_b128 v[72:75],v20 offset:37888\nv_and_b32 v30,16,v4\n" \
"M v[212:215],a[84:87],v[92:95],v[212:215]\ns_mov_b32 s59,0x30000300\nds_read_b128 v[92:95],v20 offset:38912\nv_mov_b32 v31,0x3f80\nM v[212:215],a[76:79],v[84:87],v[212:215]\ns_lshl_b32 s92,s92,6\nds_read_b128 v[84:87],v20 offset:39936\nv_lshlrev_b32 v30,v30,v31\n" \
"M v[212:215],a[68:71],v[76:79],v[212:215]\nbuffer_load_dwordx4 v2,s[36:39],s47 offen offset:3072 sc1 lds\nM v[216:219],a[64:67],v[80:83],v[216:219]\ns_waitcnt lgkmcnt(3)\ns_add_u32 m0,s22,49152\nv_cndmask_b32 v186,0,v30,s[58:59]\nv_mov_b32 v188,0\nM v[216:219],a[72:75],v[88:91],v[216:219]\n" \
"s_add_u32 s47,s31,24576\nds_read_b128 v[76:79],v20 offset:45056\nv_mov_b32 v189,0\nM v[216:219],a[80:83],v[60:63],v[216:219]\ns_mov_b32 s58,0xc0000c\nds_read_b64_tr_b16 a[198:199],v9 offset:16384\nds_read_b64_tr_b16 a[204:205],v8 offset:18432\nM v[216:219],a[88:91],v[176:179],v[216:219]\n" \
"s_mov_b32 s59,0xc0000c00\nds_read_b64_tr_b16 a[206:207],v9 offset:18432\nv_xor_b32 v8,0x30000,v8\nM v[220:223],a[92:95],v[176:179],v[220:223]\nbuffer_load_dwordx4 v2,s[36:39],s47 offen offset:0 sc1 lds\nM v[220:223],a[84:87],v[60:63],v[220:223]\ns_add_u32 s92,s92,71303168\nds_read_b128 v[176:179],v20 offset:46080\n" \
"v_xor_b32 v9,0x30000,v9\nM v[220:223],a[76:79],v[88:91],v[220:223]\ns_lshl_b32 s93,s93,6\nds_read_b128 v[60:63],v20 offset:47104\nv_cndmask_b32 v187,0,v30,s[58:59]\nM v[220:223],a[68:71],v[80:83],v[220:223]\ns_add_u32 s58,s60,1\nds_read_b128 v[88:91],v20 offset:48128\n" \
"v_add_u32 v191,s58,v190\nM v[208:211],a[96:99],v[180:183],v[208:211]\ns_waitcnt lgkmcnt(4)\nbuffer_load_dwordx4 v2,s[36:39],s47 offen offset:1024 sc1 lds\nM v[208:211],a[104:107],v[72:75],v[208:211]\ns_add_u32 s93,s93,71303168\nv_and_b32 v191,3,v191\nv_lshlrev_b32 v191,8,v191\n" \
"M v[208:211],a[112:115],v[92:95],v[208:211]\ns_lshl_b32 s58,s60,8\nds_read_b128 v[80:83],v20 offset:16384\nv_lshl_add_u32 v191,v190,3,v191\nM v[208:211],a[120:123],v[84:87],v[208:211]\ns_add_u32 s58,s58,s95\nds_read_b64_tr_b16 a[208:209],v6 offset:0\nv_add_u32 v190,1,v190\n" \
"M v[212:215],a[124:127],v[84:87],v[212:215]\ns_sub_u32 s49,s72,8192\nds_read_b128 v[84:87],v20 offset:17408\nv_lshrrev_b32 v190,2,v190\nM v[212:215],a[116:119],v[92:95],v[212:215]\nbuffer_load_dwordx4 v2,s[36:39],s47 offen offset:2048 sc1 lds\nM v[212:215],a[108:111],v[72:75],v[212:215]\ns_add_u32 s63,s49,0\n" \
"ds_read_b128 v[92:95],v20 offset:18432\nv_lshl_add_u32 v191,v190,27,v191\nM v[212:215],a[100:103],v[180:183],v[212:215]\ns_add_u32 s59,s80,65536\nds_read_b128 v[72:75],v20 offset:19456\nv_bfe_u32 v190,v4,4,2\nM v[216:219],a[96:99],v[76:79],v[216:219]\ns_waitcnt lgkmcnt(4)\n" \
"ds_read_b128 v[180:183],v20 offset:24576\nv_lshl_add_u32 v190,v190,3,s58\nM v[216:219],a[104:107],v[176:179],v[216:219]\nbuffer_load_dwordx4 v2,s[36:39],s47 offen offset:3072 sc1 lds\nM v[216:219],a[112:115],v[60:63],v[216:219]\ns_add_u32 m0,s22,131072\nv_mov_b32 v184,0\nv_mov_b32 v185,0\n" \
"M v[216:219],a[120:123],v[88:91],v[216:219]\ns_add_u32 s47,s80,0\nds_read_b64_tr_b16 a[210:211],v7 offset:0\nds_read_b64_tr_b16 a[212:213],v8 offset:0\nM v[220:223],a[124:127],v[88:91],v[220:223]\ns_add_u32 s58,s80,0\nds_read_b128 v[88:91],v20 offset:25600\nM v[220:223],a[116:119],v[60:63],v[220:223]\n" \
"ds_read_b128 v[60:63],v20 offset:26624\nM v[220:223],a[108:111],v[176:179],v[220:223]\nds_read_b128 v[176:179],v20 offset:27648\nM v[220:223],a[100:103],v[76:79],v[220:223]\nds_read_b64_tr_b16 a[214:215],v9 offset:0\nM v[208:211],a[128:131],v[80:83],v[208:211]\ns_waitcnt lgkmcnt(6)\nds_read_b64_tr_b16 a[216:217],v6 offset:2048\n" \
"M v[208:211],a[136:139],v[84:87],v[208:211]\nds_read_b64_tr_b16 a[218:219],v7 offset:2048\nds_read_b64_tr_b16 a[220:221],v8 offset:2048\nM v[208:211],a[144:147],v[92:95],v[208:211]\nds_read_b64_tr_b16 a[222:223],v9 offset:2048\nM v[208:211],a[152:155],v[72:75],v[208:211]\nds_read_b128 v[76:79],v20 offset:20480\nM v[212:215],a[156:159],v[72:75],v[212:215]\n" \
"ds_read_b128 v[72:75],v20 offset:21504\nM v[212:215],a[148:151],v[92:95],v[212:215]\nds_read_b128 v[92:95],v20 offset:22528\nM v[212:215],a[140:143],v[84:87],v[212:215]\nds_read_b128 v[84:87],v20 offset:23552\nM v[212:215],a[132:135],v[80:83],v[212:215]\nds_read_b128 v[80:83],v20 offset:28672\nM v[216:219],a[128:131],v[180:183],v[216:219]\n" \
"LG23_%=:\ns_cbranch_execz LG25_%=\ns_waitcnt lgkmcnt(5)\nM v[216:219],a[136:139],v[88:91],v[216:219]\nM v[216:219],a[144:147],v[60:63],v[216:219]\nM v[216:219],a[152:155],v[176:179],v[216:219]\nM v[220:223],a[156:159],v[176:179],v[220:223]\ns_waitcnt lgkmcnt(1)\n" \
"ds_read_b128 v[176:179],v20 offset:29696\nM v[220:223],a[148:151],v[60:63],v[220:223]\ns_waitcnt vmcnt(8) lgkmcnt(6)\nds_read_b128 v[60:63],v20 offset:30720\nM v[220:223],a[140:143],v[88:91],v[220:223]\ns_barrier\nds_read_b128 v[88:91],v20 offset:31744\nM v[220:223],a[132:135],v[180:183],v[220:223]\n" \
"buffer_load_dwordx4 v112,s[64:67],s47 offen offset:0 lds\nM v[208:211],a[160:163],v[76:79],v[208:211]\ns_add_u32 m0,s22,132096\nds_read_b128 v[180:183],v4 offset:0\nM v[208:211],a[168:171],v[72:75],v[208:211]\ns_add_u32 s47,s80,65536\nds_read_b64_tr_b16 a[224:225],v6 offset:49152\nM v[208:211],a[176:179],v[92:95],v[208:211]\n" \
"ds_read_b64_tr_b16 a[226:227],v7 offset:49152\nds_read_b64_tr_b16 a[232:233],v6 offset:51200\nM v[208:211],a[184:187],v[84:87],v[208:211]\nbuffer_load_dwordx4 v112,s[64:67],s47 offen offset:0 lds\nM v[212:215],a[188:191],v[84:87],v[212:215]\ns_add_u32 m0,s22,133120\nM v[212:215],a[180:183],v[92:95],v[212:215]\ns_add_u32 s47,s81,0\n" \
"ds_read_b128 v[84:87],v4 offset:1024\nM v[212:215],a[172:175],v[72:75],v[212:215]\ns_waitcnt lgkmcnt(4)\nds_read_b128 v[92:95],v4 offset:2048\nM v[212:215],a[164:167],v[76:79],v[212:215]\nbuffer_load_dwordx4 v112,s[64:67],s47 offen offset:0 lds\nM v[216:219],a[160:163],v[80:83],v[216:219]\ns_add_u32 m0,s22,134144\n" \
"ds_read_b128 v[72:75],v4 offset:3072\nM v[216:219],a[168:171],v[176:179],v[216:219]\ns_add_u32 s47,s81,65536\nds_read_b128 v[76:79],v4 offset:8192\nM v[216:219],a[176:179],v[60:63],v[216:219]\nds_read_b64_tr_b16 a[234:235],v7 offset:51200\nM v[216:219],a[184:187],v[88:91],v[216:219]\nbuffer_load_dwordx4 v112,s[64:67],s47 offen offset:0 lds\n" \
"M v[220:223],a[188:191],v[88:91],v[220:223]\ns_add_u32 m0,s22,147456\nds_read_b128 v[88:91],v4 offset:9216\nM v[220:223],a[180:183],v[60:63],v[220:223]\ns_add_u32 s47,s32,0\nds_read_b128 v[60:63],v4 offset:10240\nM v[220:223],a[172:175],v[176:179],v[220:223]\ns_waitcnt lgkmcnt(4)\n" \
"ds_read_b128 v[176:179],v4 offset:11264\nM v[220:223],a[164:167],v[80:83],v[220:223]\nbuffer_load_dwordx4 v113,s[36:39],s82 offen offset:0 sc1 lds\nM v[48:51],a[192:195],v[180:183],v[48:51]\ns_add_u32 m0,s22,146432\nM v[48:51],a[200:203],v[84:87],v[48:51]\nds_read_b128 v[80:83],v4 offset:4096\nM v[48:51],a[208:211],v[92:95],v[48:51]\n" \
"ds_read_b64_tr_b16 a[236:237],v8 offset:51200\nM v[48:51],a[216:219],v[72:75],v[48:51]\nbuffer_load_dwordx4 v113,s[36:39],s82 offen offset:2048 sc1 lds\nM v[52:55],a[220:223],v[72:75],v[52:55]\ns_add_u32 m0,s22,149504\nds_read_b128 v[72:75],v4 offset:5120\nM v[52:55],a[212:215],v[92:95],v[52:55]\nds_read_b64_tr_b16 a[238:239],v9 offset:51200\n" \
"M v[52:55],a[204:207],v[84:87],v[52:55]\ns_waitcnt lgkmcnt(4)\nds_read_b64_tr_b16 a[228:229],v8 offset:49152\nM v[52:55],a[196:199],v[180:183],v[52:55]\nbuffer_load_dwordx2 v[10:11],v58,s[36:39],0 offen sc1\nM v[64:67],a[192:195],v[76:79],v[64:67]\nds_read_b128 v[92:95],v4 offset:6144\nds_read_b64_tr_b16 a[230:231],v9 offset:49152\n" \
"M v[64:67],a[200:203],v[88:91],v[64:67]\nbuffer_load_dwordx4 v113,s[36:39],s83 offen offset:0 sc1 lds\nM v[64:67],a[208:211],v[60:63],v[64:67]\ns_add_u32 m0,s22,148480\nM v[64:67],a[216:219],v[176:179],v[64:67]\nds_read_b128 v[84:87],v4 offset:7168\nM v[68:71],a[220:223],v[176:179],v[68:71]\nds_read_b128 v[180:183],v4 offset:12288\n" \
"M v[68:71],a[212:215],v[60:63],v[68:71]\nbuffer_load_dwordx4 v113,s[36:39],s83 offen offset:2048 sc1 lds\nM v[68:71],a[204:207],v[88:91],v[68:71]\ns_add_u32 m0,s22,65536\nds_read_b128 v[176:179],v4 offset:13312\nM v[68:71],a[196:199],v[76:79],v[68:71]\ns_waitcnt lgkmcnt(3)\nds_read_b128 v[60:63],v4 offset:14336\n" \
"M v[48:51],a[224:227],v[80:83],v[48:51]\nds_read_b128 v[88:91],v4 offset:15360\nM v[48:51],a[232:235],v[72:75],v[48:51]\nbuffer_load_dword v16,v15,s[36:39],s92 offen offset:0 sc1\nM v[48:51],a[240:243],v[92:95],v[48:51]\ns_waitcnt lgkmcnt(3)\nds_read_b128 v[76:79],v4 offset:32768\nM v[48:51],a[248:251],v[84:87],v[48:51]\n" \
"buffer_load_dword v17,v15,s[36:39],s92 offen offset:128 sc1\nM v[52:55],a[252:255],v[84:87],v[52:55]\nds_read_b128 v[84:87],v4 offset:33792\nM v[52:55],a[244:247],v[92:95],v[52:55]\nbuffer_load_dword v18,v15,s[36:39],s93 offen offset:0 sc1\nM v[52:55],a[236:239],v[72:75],v[52:55]\nds_read_b128 v[92:95],v4 offset:34816\nds_read_b128 v[72:75],v4 offset:35840\n" \
"M v[52:55],a[228:231],v[80:83],v[52:55]\nbuffer_load_dword v19,v15,s[36:39],s93 offen offset:128 sc1\nM v[64:67],a[224:227],v[180:183],v[64:67]\ns_waitcnt lgkmcnt(3)\nds_read_b128 v[80:83],v4 offset:40960\nC v48,v48,v49\nM v[64:67],a[232:235],v[176:179],v[64:67]\nC v49,v50,v51\n" \
"C v50,v52,v53\nM v[64:67],a[240:243],v[60:63],v[64:67]\nC v51,v54,v55\nv_cndmask_b32 v120,v120,v48,s[96:97]\nM v[64:67],a[248:251],v[88:91],v[64:67]\nv_cndmask_b32 v121,v121,v49,s[96:97]\nv_cndmask_b32 v122,v122,v50,s[96:97]\nM v[68:71],a[252:255],v[88:91],v[68:71]\n" \
"ds_read_b128 v[88:91],v4 offset:41984\nv_cndmask_b32 v123,v123,v51,s[96:97]\nM v[68:71],a[244:247],v[60:63],v[68:71]\nds_read_b128 v[60:63],v4 offset:43008\nC v64,v64,v65\nM v[68:71],a[236:239],v[176:179],v[68:71]\nds_read_b128 v[176:179],v4 offset:44032\nC v65,v66,v67\n" \
"M v[68:71],a[228:231],v[180:183],v[68:71]\nds_read_b128 v[180:183],v4 offset:36864\nv_cndmask_b32 v124,v124,v64,s[96:97]\nM v[32:35],a[192:195],v[76:79],v[32:35]\ns_waitcnt lgkmcnt(4)\nv_cndmask_b32 v125,v125,v65,s[96:97]\nM v[32:35],a[200:203],v[84:87],v[32:35]\nC v66,v68,v69\n" \
"C v67,v70,v71\nM v[32:35],a[208:211],v[92:95],v[32:35]\nv_cndmask_b32 v126,v126,v66,s[96:97]\nv_cndmask_b32 v127,v127,v67,s[96:97]\nM v[32:35],a[216:219],v[72:75],v[32:35]\ns_cmp_eq_u32 s60,1\ns_cselect_b64 s[96:97],-1,0\nM v[36:39],a[220:223],v[72:75],v[36:39]\n" \
"ds_read_b128 v[72:75],v4 offset:37888\nM v[36:39],a[212:215],v[92:95],v[36:39]\nds_read_b128 v[92:95],v4 offset:38912\nM v[36:39],a[204:207],v[84:87],v[36:39]\nds_read_b128 v[84:87],v4 offset:39936\nM v[36:39],a[196:199],v[76:79],v[36:39]\nds_read_b128 v[76:79],v4 offset:45056\nM v[40:43],a[192:195],v[80:83],v[40:43]\n" \
"s_waitcnt lgkmcnt(4)\nM v[40:43],a[200:203],v[88:91],v[40:43]\nM v[40:43],a[208:211],v[60:63],v[40:43]\nM v[40:43],a[216:219],v[176:179],v[40:43]\nM v[44:47],a[220:223],v[176:179],v[44:47]\nds_read_b128 v[176:179],v4 offset:46080\nM v[44:47],a[212:215],v[60:63],v[44:47]\ns_waitcnt vmcnt(13) lgkmcnt(14)\n" \
"ds_read_b128 v[60:63],v4 offset:47104\nM v[44:47],a[204:207],v[88:91],v[44:47]\ns_barrier\nds_read_b128 v[88:91],v4 offset:48128\nM v[44:47],a[196:199],v[80:83],v[44:47]\nds_read_b128 v[80:83],v4 offset:16384\nM v[32:35],a[224:227],v[180:183],v[32:35]\ns_waitcnt lgkmcnt(4)\n" \
"M v[32:35],a[232:235],v[72:75],v[32:35]\nM v[32:35],a[240:243],v[92:95],v[32:35]\nM v[32:35],a[248:251],v[84:87],v[32:35]\nM v[36:39],a[252:255],v[84:87],v[36:39]\ns_waitcnt vmcnt(6)\nv_cmp_gt_u64 s[98:99],s[14:15],v[10:11]\nv_cmp_le_u64 s[18:19],s[16:17],v[10:11]\nM v[36:39],a[244:247],v[92:95],v[36:39]\n" \
"s_or_b64 s[98:99],s[98:99],s[18:19]\ns_cbranch_scc1 LPL_%=\nLPLR_%=:\nbuffer_load_dwordx4 v2,s[36:39],s47 offen offset:0 sc1 lds\nM v[36:39],a[236:239],v[72:75],v[36:39]\nds_read_b128 v[84:87],v4 offset:17408\nC v32,v32,v33\nM v[36:39],a[228:231],v[180:183],v[36:39]\n" \
"ds_read_b128 v[92:95],v4 offset:18432\nC v33,v34,v35\nM v[40:43],a[224:227],v[76:79],v[40:43]\ns_waitcnt lgkmcnt(2)\nds_read_b128 v[72:75],v4 offset:19456\nv_cndmask_b32 v120,v120,v32,s[96:97]\nM v[40:43],a[232:235],v[176:179],v[40:43]\nbuffer_load_dwordx4 v2,s[36:39],s47 offen offset:1024 sc1 lds\n" \
"M v[40:43],a[240:243],v[60:63],v[40:43]\nds_read_b128 v[180:183],v4 offset:24576\nC v34,v36,v37\nM v[40:43],a[248:251],v[88:91],v[40:43]\nC v35,v38,v39\nv_cndmask_b32 v121,v121,v33,s[96:97]\nM v[44:47],a[252:255],v[88:91],v[44:47]\nds_read_b128 v[88:91],v4 offset:25600\n" \
"v_cndmask_b32 v122,v122,v34,s[96:97]\nM v[44:47],a[244:247],v[60:63],v[44:47]\nbuffer_load_dwordx4 v2,s[36:39],s47 offen offset:2048 sc1 lds\nM v[44:47],a[236:239],v[176:179],v[44:47]\nds_read_b128 v[60:63],v4 offset:26624\nC v40,v40,v41\nM v[44:47],a[228:231],v[76:79],v[44:47]\nds_read_b128 v[176:179],v4 offset:27648\n" \
"C v41,v42,v43\nM v[192:195],a[192:195],v[80:83],v[192:195]\ns_waitcnt lgkmcnt(3)\nds_read_b128 v[76:79],v4 offset:20480\nv_cndmask_b32 v123,v123,v35,s[96:97]\nM v[192:195],a[200:203],v[84:87],v[192:195]\nbuffer_load_dwordx4 v2,s[36:39],s47 offen offset:3072 sc1 lds\nM v[192:195],a[208:211],v[92:95],v[192:195]\n" \
"s_add_u32 m0,s22,98304\ns_add_u32 s47,s32,8192\nC v42,v44,v45\nC v43,v46,v47\nM v[192:195],a[216:219],v[72:75],v[192:195]\nv_cndmask_b32 v124,v124,v40,s[96:97]\nv_cndmask_b32 v125,v125,v41,s[96:97]\nM v[196:199],a[220:223],v[72:75],v[196:199]\n" \
"ds_read_b128 v[72:75],v4 offset:21504\nv_cndmask_b32 v126,v126,v42,s[96:97]\nM v[196:199],a[212:215],v[92:95],v[196:199]\nbuffer_load_dwordx4 v2,s[36:39],s47 offen offset:0 sc1 lds\nM v[196:199],a[204:207],v[84:87],v[196:199]\nds_read_b128 v[92:95],v4 offset:22528\nv_cndmask_b32 v127,v127,v43,s[96:97]\nM v[196:199],a[196:199],v[80:83],v[196:199]\n" \
"s_cmp_eq_u32 s60,2\ns_cselect_b64 s[96:97],-1,0\nds_read_b128 v[84:87],v4 offset:23552\nM v[200:203],a[192:195],v[180:183],v[200:203]\ns_waitcnt lgkmcnt(3)\nds_read_b128 v[80:83],v4 offset:28672\nM v[200:203],a[200:203],v[88:91],v[200:203]\nbuffer_load_dwordx4 v2,s[36:39],s47 offen offset:1024 sc1 lds\n" \
"M v[200:203],a[208:211],v[60:63],v[200:203]\nM v[200:203],a[216:219],v[176:179],v[200:203]\nM v[204:207],a[220:223],v[176:179],v[204:207]\nds_read_b128 v[176:179],v4 offset:29696\nM v[204:207],a[212:215],v[60:63],v[204:207]\nbuffer_load_dwordx4 v2,s[36:39],s47 offen offset:2048 sc1 lds\nM v[204:207],a[204:207],v[88:91],v[204:207]\nds_read_b128 v[60:63],v4 offset:30720\n" \
"M v[204:207],a[196:199],v[180:183],v[204:207]\nds_read_b128 v[88:91],v4 offset:31744\nM v[192:195],a[224:227],v[76:79],v[192:195]\ns_waitcnt lgkmcnt(3)\nds_read_b128 v[180:183],v4 offset:49152\nM v[192:195],a[232:235],v[72:75],v[192:195]\nbuffer_load_dwordx4 v2,s[36:39],s47 offen offset:3072 sc1 lds\nM v[192:195],a[240:243],v[92:95],v[192:195]\n" \
"s_add_u32 m0,s22,81920\nM v[192:195],a[248:251],v[84:87],v[192:195]\ns_add_u32 s47,s32,16384\nM v[196:199],a[252:255],v[84:87],v[196:199]\ns_waitcnt vmcnt(8)\nds_read_b128 v[84:87],v4 offset:50176\nv_sub_f32 v24,1.0,v16\nM v[196:199],a[244:247],v[92:95],v[196:199]\n" \
"buffer_store_dwordx4 v[48:51],v4,s[36:39],s40 offen offset:0 sc1\nM v[196:199],a[236:239],v[72:75],v[196:199]\nds_read_b128 v[92:95],v4 offset:51200\nds_read_b128 v[72:75],v4 offset:52224\nM v[196:199],a[228:231],v[76:79],v[196:199]\nds_read_b128 v[76:79],v4 offset:57344\nC v192,v192,v193\nM v[200:203],a[224:227],v[80:83],v[200:203]\n" \
"s_waitcnt lgkmcnt(4)\nC v193,v194,v195\nv_cndmask_b32 v120,v120,v192,s[96:97]\nM v[200:203],a[232:235],v[176:179],v[200:203]\nbuffer_load_dwordx4 v2,s[36:39],s47 offen offset:0 sc1 lds\nM v[200:203],a[240:243],v[60:63],v[200:203]\nC v194,v196,v197\nC v195,v198,v199\n" \
"M v[200:203],a[248:251],v[88:91],v[200:203]\nv_cndmask_b32 v121,v121,v193,s[96:97]\nv_cndmask_b32 v122,v122,v194,s[96:97]\nM v[204:207],a[252:255],v[88:91],v[204:207]\nds_read_b128 v[88:91],v4 offset:58368\nv_cndmask_b32 v123,v123,v195,s[96:97]\nM v[204:207],a[244:247],v[60:63],v[204:207]\nbuffer_load_dwordx4 v2,s[36:39],s47 offen offset:1024 sc1 lds\n" \
"M v[204:207],a[236:239],v[176:179],v[204:207]\nds_read_b128 v[60:63],v4 offset:59392\nC v200,v200,v201\nM v[204:207],a[228:231],v[80:83],v[204:207]\nds_read_b128 v[176:179],v4 offset:60416\nC v201,v202,v203\nM v[208:211],a[192:195],v[180:183],v[208:211]\ns_waitcnt lgkmcnt(3)\n" \
"ds_read_b128 v[80:83],v4 offset:53248\nv_cndmask_b32 v124,v124,v200,s[96:97]\nM v[208:211],a[200:203],v[84:87],v[208:211]\nbuffer_load_dwordx4 v2,s[36:39],s47 offen offset:2048 sc1 lds\nM v[208:211],a[208:211],v[92:95],v[208:211]\nC v202,v204,v205\nC v203,v206,v207\nM v[208:211],a[216:219],v[72:75],v[208:211]\n" \
"v_cndmask_b32 v125,v125,v201,s[96:97]\nv_cndmask_b32 v126,v126,v202,s[96:97]\nM v[212:215],a[220:223],v[72:75],v[212:215]\nds_read_b128 v[72:75],v4 offset:54272\nv_cndmask_b32 v127,v127,v203,s[96:97]\nM v[212:215],a[212:215],v[92:95],v[212:215]\nbuffer_store_dwordx4 v[64:67],v4,s[36:39],s40 offen offset:1024 sc1\nM v[212:215],a[204:207],v[84:87],v[212:215]\n" \
"s_cmp_eq_u32 s60,3\ns_cselect_b64 s[96:97],-1,0\nds_read_b128 v[92:95],v4 offset:55296\nds_read_b128 v[84:87],v4 offset:56320\nM v[212:215],a[196:199],v[180:183],v[212:215]\nds_read_b128 v[180:183],v4 offset:61440\nC v24,v24,v24\nM v[216:219],a[192:195],v[76:79],v[216:219]\n" \
"s_waitcnt lgkmcnt(4)\nv_and_b32 v24,0xffff0000,v24\nv_sub_f32 v25,1.0,v17\nM v[216:219],a[200:203],v[88:91],v[216:219]\nbuffer_load_dwordx4 v2,s[36:39],s47 offen offset:3072 sc1 lds\nM v[216:219],a[208:211],v[60:63],v[216:219]\ns_add_u32 m0,s22,114688\ns_add_u32 s47,s32,24576\n" \
"C v25,v25,v25\nv_and_b32 v25,0xffff0000,v25\nM v[216:219],a[216:219],v[176:179],v[216:219]\nv_sub_f32 v26,1.0,v18\nv_sub_f32 v27,1.0,v19\nM v[220:223],a[220:223],v[176:179],v[220:223]\nds_read_b128 v[176:179],v4 offset:62464\nC v26,v26,v26\n" \
"M v[220:223],a[212:215],v[60:63],v[220:223]\ns_waitcnt vmcnt(6) lgkmcnt(14)\nbuffer_load_dwordx4 v2,s[36:39],s47 offen offset:0 sc1 lds\nM v[220:223],a[204:207],v[88:91],v[220:223]\ns_barrier\nds_read_b128 v[60:63],v4 offset:63488\nM v[220:223],a[196:199],v[76:79],v[220:223]\nds_read_b128 v[88:91],v4 offset:64512\n" \
"C v27,v27,v27\nM v[208:211],a[224:227],v[80:83],v[208:211]\ns_waitcnt lgkmcnt(3)\nds_read_b128 v[76:79],v20 offset:0\nv_and_b32 v26,0xffff0000,v26\nM v[208:211],a[232:235],v[72:75],v[208:211]\nbuffer_load_dwordx4 v2,s[36:39],s47 offen offset:1024 sc1 lds\nM v[208:211],a[240:243],v[92:95],v[208:211]\n" \
"ds_read_b64 v[160:161],v114 offset:0\nv_and_b32 v27,0xffff0000,v27\nM v[208:211],a[248:251],v[84:87],v[208:211]\nds_read_b64_tr_b16 v[96:97],v115 offset:16384\nM v[212:215],a[252:255],v[84:87],v[212:215]\nds_read_b128 v[84:87],v20 offset:1024\nM v[212:215],a[244:247],v[92:95],v[212:215]\nbuffer_load_dwordx4 v2,s[36:39],s47 offen offset:2048 sc1 lds\n" \
"M v[212:215],a[236:239],v[72:75],v[212:215]\nds_read_b128 v[92:95],v20 offset:2048\nC v208,v208,v209\nM v[212:215],a[228:231],v[80:83],v[212:215]\nds_read_b128 v[72:75],v20 offset:3072\nC v209,v210,v211\nM v[216:219],a[224:227],v[180:183],v[216:219]\ns_waitcnt lgkmcnt(4)\n" \
"ds_read_b128 v[80:83],v20 offset:8192\nv_cndmask_b32 v120,v120,v208,s[96:97]\nM v[216:219],a[232:235],v[176:179],v[216:219]\nbuffer_store_dwordx4 v[32:35],v4,s[36:39],s41 offen offset:0 sc1\nM v[216:219],a[240:243],v[60:63],v[216:219]\nds_read_b64 v[162:163],v114 offset:32\nds_read_b64_tr_b16 v[98:99],v115 offset:17408\nM v[216:219],a[248:251],v[88:91],v[216:219]\n" \
"ds_read_b64 v[164:165],v114 offset:1024\nC v210,v212,v213\nM v[220:223],a[252:255],v[88:91],v[220:223]\nds_read_b128 v[88:91],v20 offset:9216\nC v211,v214,v215\nM v[220:223],a[244:247],v[60:63],v[220:223]\nbuffer_load_dwordx4 v2,s[36:39],s47 offen offset:3072 sc1 lds\nM v[220:223],a[236:239],v[176:179],v[220:223]\n" \
"s_add_u32 m0,s22,0\nds_read_b128 v[60:63],v20 offset:10240\nC v216,v216,v217\nM v[220:223],a[228:231],v[180:183],v[220:223]\ns_add_u32 s47,s33,0\nds_read_b128 v[176:179],v20 offset:11264\nC v217,v218,v219\nM v[224:227],a[0:3],v[76:79],0\n" \
"s_waitcnt lgkmcnt(4)\nds_read_b128 v[180:183],v20 offset:4096\nv_lshlrev_b32 v128,16,v160\nM v[224:227],a[8:11],v[84:87],v[224:227]\nbuffer_load_dwordx4 v2,s[36:39],s47 offen offset:0 sc1 lds\nM v[224:227],a[16:19],v[92:95],v[224:227]\nC v218,v220,v221\nC v219,v222,v223\n" \
"M v[224:227],a[24:27],v[72:75],v[224:227]\nds_read_b64_tr_b16 v[100:101],v115 offset:16416\nds_read_b64 v[166:167],v114 offset:1056\nM v[228:231],a[28:31],v[72:75],0\nds_read_b128 v[72:75],v20 offset:5120\nv_and_b32 v129,0xffff0000,v160\nM v[228:231],a[20:23],v[92:95],v[228:231]\nbuffer_store_dwordx4 v[40:43],v4,s[36:39],s41 offen offset:1024 sc1\n" \
"M v[228:231],a[12:15],v[84:87],v[228:231]\nds_read_b128 v[92:95],v20 offset:6144\nds_read_b128 v[84:87],v20 offset:7168\nM v[228:231],a[4:7],v[76:79],v[228:231]\nds_read_b128 v[76:79],v20 offset:12288\nv_lshlrev_b32 v130,16,v161\nM v[232:235],a[0:3],v[80:83],0\ns_waitcnt lgkmcnt(6)\n" \
"ds_read_b64_tr_b16 v[102:103],v115 offset:17440\nv_and_b32 v131,0xffff0000,v161\nM v[232:235],a[8:11],v[88:91],v[232:235]\nbuffer_load_dwordx4 v2,s[36:39],s47 offen offset:1024 sc1 lds\nM v[232:235],a[16:19],v[60:63],v[232:235]\nv_lshlrev_b32 v132,16,v162\nv_and_b32 v133,0xffff0000,v162\nM v[232:235],a[24:27],v[176:179],v[232:235]\n" \
"ds_read_b64 v[168:169],v114 offset:2048\nds_read_b64_tr_b16 v[104:105],v115 offset:18432\nM v[236:239],a[28:31],v[176:179],0\nds_read_b128 v[176:179],v20 offset:13312\nv_lshlrev_b32 v134,16,v163\nM v[236:239],a[20:23],v[60:63],v[236:239]\nbuffer_load_dwordx4 v2,s[36:39],s47 offen offset:2048 sc1 lds\nM v[236:239],a[12:15],v[88:91],v[236:239]\n" \
"v_and_b32 v135,0xffff0000,v163\nv_lshlrev_b32 v136,16,v164\nM v[236:239],a[4:7],v[80:83],v[236:239]\nds_read_b128 v[60:63],v20 offset:14336\nv_and_b32 v137,0xffff0000,v164\nM v[224:227],a[32:35],v[180:183],v[224:227]\ns_waitcnt lgkmcnt(4)\nds_read_b128 v[88:91],v20 offset:15360\n" \
"v_lshlrev_b32 v138,16,v165\nM v[224:227],a[40:43],v[72:75],v[224:227]\nbuffer_load_dwordx4 v2,s[36:39],s47 offen offset:3072 sc1 lds\nM v[224:227],a[48:51],v[92:95],v[224:227]\ns_add_u32 m0,s22,32768\nds_read_b128 v[80:83],v20 offset:32768\nv_and_b32 v139,0xffff0000,v165\nM v[224:227],a[56:59],v[84:87],v[224:227]\n" \
"s_add_u32 s47,s33,8192\nds_read_b64 v[170:171],v114 offset:2080\nv_lshlrev_b32 v140,16,v166\nM v[228:231],a[60:63],v[84:87],v[228:231]\nds_read_b128 v[84:87],v20 offset:33792\nv_and_b32 v141,0xffff0000,v166\nM v[228:231],a[52:55],v[92:95],v[228:231]\nbuffer_load_dwordx4 v2,s[36:39],s47 offen offset:0 sc1 lds\n" \
"M v[228:231],a[44:47],v[72:75],v[228:231]\nds_read_b128 v[92:95],v20 offset:34816\nv_lshlrev_b32 v142,16,v167\nM v[228:231],a[36:39],v[180:183],v[228:231]\nds_read_b128 v[72:75],v20 offset:35840\nv_lshlrev_b32 v160,16,v96\nM v[232:235],a[32:35],v[76:79],v[232:235]\ns_waitcnt lgkmcnt(4)\n" \
"ds_read_b128 v[180:183],v20 offset:40960\nv_and_b32 v143,0xffff0000,v167\nM v[232:235],a[40:43],v[176:179],v[232:235]\nbuffer_store_dwordx4 v[192:195],v4,s[36:39],s42 offen offset:0 sc1\nM v[232:235],a[48:51],v[60:63],v[232:235]\nds_read_b64_tr_b16 v[106:107],v115 offset:19456\nds_read_b64 v[172:173],v114 offset:3072\nM v[232:235],a[56:59],v[88:91],v[232:235]\n" \
"ds_read_b64_tr_b16 v[108:109],v115 offset:18464\nv_lshlrev_b32 v161,16,v97\nM v[236:239],a[60:63],v[88:91],v[236:239]\nds_read_b128 v[88:91],v20 offset:41984\nv_lshlrev_b32 v162,16,v98\nM v[236:239],a[52:55],v[60:63],v[236:239]\nbuffer_load_dwordx4 v2,s[36:39],s47 offen offset:1024 sc1 lds\nM v[236:239],a[44:47],v[176:179],v[236:239]\n" \
"ds_read_b128 v[60:63],v20 offset:43008\nv_lshlrev_b32 v163,16,v99\nM v[236:239],a[36:39],v[76:79],v[236:239]\nds_read_b128 v[176:179],v20 offset:44032\nv_lshlrev_b32 v164,16,v100\nM v[224:227],a[64:67],v[80:83],v[224:227]\ns_waitcnt lgkmcnt(4)\nds_read_b64 v[174:175],v114 offset:3104\n" \
"v_lshlrev_b32 v165,16,v101\nM v[224:227],a[72:75],v[84:87],v[224:227]\nds_read_b64_tr_b16 v[110:111],v115 offset:19488\nv_lshlrev_b32 v166,16,v102\nM v[224:227],a[80:83],v[92:95],v[224:227]\nbuffer_load_dwordx4 v2,s[36:39],s47 offen offset:2048 sc1 lds\nM v[224:227],a[88:91],v[72:75],v[224:227]\nds_read_b128 v[76:79],v20 offset:36864\n" \
"v_lshlrev_b32 v167,16,v103\nM v[228:231],a[92:95],v[72:75],v[228:231]\nds_read_b128 v[72:75],v20 offset:37888\nv_lshlrev_b32 v144,16,v168\nM v[228:231],a[84:87],v[92:95],v[228:231]\ns_waitcnt lgkmcnt(4)\nds_read_b128 v[92:95],v20 offset:38912\nv_and_b32 v96,0xffff0000,v96\n" \
"M v[228:231],a[76:79],v[84:87],v[228:231]\nbuffer_load_dwordx4 v2,s[36:39],s47 offen offset:3072 sc1 lds\nM v[228:231],a[68:71],v[80:83],v[228:231]\ns_add_u32 m0,s22,16384\nv_and_b32 v145,0xffff0000,v168\nv_and_b32 v97,0xffff0000,v97\nM v[232:235],a[64:67],v[180:183],v[232:235]\ns_add_u32 s47,s33,16384\n" \
"ds_read_b128 v[84:87],v20 offset:39936\nv_and_b32 v98,0xffff0000,v98\nM v[232:235],a[72:75],v[88:91],v[232:235]\nds_read_b128 v[80:83],v20 offset:45056\nv_and_b32 v99,0xffff0000,v99\nM v[232:235],a[80:83],v[60:63],v[232:235]\nbuffer_store_dwordx4 v[200:203],v4,s[36:39],s42 offen offset:1024 sc1\nM v[232:235],a[88:91],v[176:179],v[232:235]\n" \
"v_lshlrev_b32 v146,16,v169\nv_and_b32 v147,0xffff0000,v169\nM v[236:239],a[92:95],v[176:179],v[236:239]\nds_read_b128 v[176:179],v20 offset:46080\nv_lshlrev_b32 v148,16,v170\nM v[236:239],a[84:87],v[60:63],v[236:239]\ns_waitcnt vmcnt(11) lgkmcnt(6)\nds_read_b128 v[60:63],v20 offset:47104\n" \
"v_and_b32 v149,0xffff0000,v170\nM v[236:239],a[76:79],v[88:91],v[236:239]\ns_barrier\nds_read_b128 v[88:91],v20 offset:48128\nM v[236:239],a[68:71],v[180:183],v[236:239]\ns_waitcnt lgkmcnt(3)\nds_read_b128 v[180:183],v20 offset:16384\nv_lshlrev_b32 v150,16,v171\n" \
"M v[224:227],a[96:99],v[76:79],v[224:227]\nv_and_b32 v100,0xffff0000,v100\nv_and_b32 v151,0xffff0000,v171\nM v[224:227],a[104:107],v[72:75],v[224:227]\nv_and_b32 v101,0xffff0000,v101\nv_and_b32 v102,0xffff0000,v102\nM v[224:227],a[112:115],v[92:95],v[224:227]\nLG24_%=:\n" \
"s_cbranch_execz LG26_%=\nv_and_b32 v103,0xffff0000,v103\nv_lshlrev_b32 v168,16,v104\nM v[224:227],a[120:123],v[84:87],v[224:227]\nv_mul_f32 v129,v129,v96\nv_mul_f32 v131,v131,v97\nM v[228:231],a[124:127],v[84:87],v[228:231]\nds_read_b128 v[84:87],v20 offset:17408\n" \
"v_mul_f32 v133,v133,v98\nM v[228:231],a[116:119],v[92:95],v[228:231]\nds_read_b128 v[92:95],v20 offset:18432\nv_mul_f32 v135,v135,v99\nM v[228:231],a[108:111],v[72:75],v[228:231]\nds_read_b128 v[72:75],v20 offset:19456\nv_lshlrev_b32 v169,16,v105\nM v[228:231],a[100:103],v[76:79],v[228:231]\n" \
"buffer_load_dwordx4 v2,s[36:39],s47 offen offset:0 sc1 lds\nM v[232:235],a[96:99],v[80:83],v[232:235]\ns_waitcnt lgkmcnt(3)\nds_read_b128 v[76:79],v20 offset:24576\nv_lshlrev_b32 v170,16,v106\nM v[232:235],a[104:107],v[176:179],v[232:235]\nv_lshlrev_b32 v171,16,v107\nv_lshlrev_b32 v152,16,v172\n" \
"M v[232:235],a[112:115],v[60:63],v[232:235]\nv_and_b32 v153,0xffff0000,v172\nv_lshlrev_b32 v154,16,v173\nM v[232:235],a[120:123],v[88:91],v[232:235]\nv_and_b32 v155,0xffff0000,v173\nv_lshlrev_b32 v156,16,v174\nM v[236:239],a[124:127],v[88:91],v[236:239]\nbuffer_store_dwordx4 v[208:211],v4,s[36:39],s43 offen offset:0 sc1\n" \
"M v[236:239],a[116:119],v[60:63],v[236:239]\nds_read_b128 v[88:91],v20 offset:25600\nds_read_b128 v[60:63],v20 offset:26624\nM v[236:239],a[108:111],v[176:179],v[236:239]\nds_read_b128 v[176:179],v20 offset:27648\nv_mul_f32 v137,v137,v100\nM v[236:239],a[100:103],v[80:83],v[236:239]\nds_read_b128 v[80:83],v20 offset:20480\n" \
"v_and_b32 v157,0xffff0000,v174\nM v[224:227],a[128:131],v[180:183],v[224:227]\ns_waitcnt lgkmcnt(4)\nv_mul_f32 v139,v139,v101\nv_mul_f32 v141,v141,v102\nM v[224:227],a[136:139],v[84:87],v[224:227]\nv_mul_f32 v143,v143,v103\nv_and_b32 v104,0xffff0000,v104\n" \
"M v[224:227],a[144:147],v[92:95],v[224:227]\nv_and_b32 v105,0xffff0000,v105\nv_and_b32 v106,0xffff0000,v106\nM v[224:227],a[152:155],v[72:75],v[224:227]\nv_and_b32 v107,0xffff0000,v107\nv_lshlrev_b32 v172,16,v108\nM v[228:231],a[156:159],v[72:75],v[228:231]\nds_read_b128 v[72:75],v20 offset:21504\n" \
"v_lshlrev_b32 v158,16,v175\nM v[228:231],a[148:151],v[92:95],v[228:231]\nds_read_b128 v[92:95],v20 offset:22528\nv_and_b32 v159,0xffff0000,v175\nM v[228:231],a[140:143],v[84:87],v[228:231]\nds_read_b128 v[84:87],v20 offset:23552\nv_lshlrev_b32 v173,16,v109\nM v[228:231],a[132:135],v[180:183],v[228:231]\n" \
"ds_read_b128 v[180:183],v20 offset:28672\nv_lshlrev_b32 v174,16,v110\nM v[232:235],a[128:131],v[76:79],v[232:235]\ns_waitcnt lgkmcnt(4)\nv_mul_f32 v145,v145,v104\nv_mul_f32 v147,v147,v105\nM v[232:235],a[136:139],v[88:91],v[232:235]\nbuffer_load_dwordx4 v2,s[36:39],s47 offen offset:1024 sc1 lds\n" \
"M v[232:235],a[144:147],v[60:63],v[232:235]\nv_mul_f32 v149,v149,v106\nv_mul_f32 v151,v151,v107\nM v[232:235],a[152:155],v[176:179],v[232:235]\nv_and_b32 v108,0xffff0000,v108\nv_lshlrev_b32 v175,16,v111\nM v[236:239],a[156:159],v[176:179],v[236:239]\nds_read_b128 v[176:179],v20 offset:29696\n" \
"v_and_b32 v109,0xffff0000,v109\nM v[236:239],a[148:151],v[60:63],v[236:239]\nbuffer_load_dwordx4 v2,s[36:39],s47 offen offset:2048 sc1 lds\nM v[236:239],a[140:143],v[88:91],v[236:239]\nds_read_b128 v[60:63],v20 offset:30720\nv_and_b32 v110,0xffff0000,v110\nM v[236:239],a[132:135],v[76:79],v[236:239]\nds_read_b128 v[88:91],v20 offset:31744\n" \
"v_and_b32 v111,0xffff0000,v111\nM v[224:227],a[160:163],v[80:83],v[224:227]\ns_waitcnt lgkmcnt(3)\nds_read_b128 v[76:79],v20 offset:49152\nv_mul_f32 v153,v153,v108\nM v[224:227],a[168:171],v[72:75],v[224:227]\nbuffer_store_dwordx4 v[216:219],v4,s[36:39],s43 offen offset:1024 sc1\nM v[224:227],a[176:179],v[92:95],v[224:227]\n" \
"v_mul_f32 v155,v155,v109\nv_mul_f32 v157,v157,v110\nM v[224:227],a[184:187],v[84:87],v[224:227]\nv_mul_f32 v159,v159,v111\nv_cndmask_b32 v121,v121,v209,s[96:97]\nM v[228:231],a[188:191],v[84:87],v[228:231]\nds_read_b128 v[84:87],v20 offset:50176\nv_cndmask_b32 v122,v122,v210,s[96:97]\n" \
"M v[228:231],a[180:183],v[92:95],v[228:231]\nds_read_b128 v[92:95],v20 offset:51200\nv_cndmask_b32 v123,v123,v211,s[96:97]\nM v[228:231],a[172:175],v[72:75],v[228:231]\nds_read_b128 v[72:75],v20 offset:52224\nv_cndmask_b32 v124,v124,v216,s[96:97]\nM v[228:231],a[164:167],v[80:83],v[228:231]\nds_read_b128 v[80:83],v20 offset:57344\n" \
"v_cndmask_b32 v125,v125,v217,s[96:97]\nM v[232:235],a[160:163],v[180:183],v[232:235]\ns_waitcnt lgkmcnt(4)\nv_cndmask_b32 v126,v126,v218,s[96:97]\nv_cndmask_b32 v127,v127,v219,s[96:97]\nM v[232:235],a[168:171],v[176:179],v[232:235]\nv_mul_f32 v128,v128,v160\nv_mul_f32 v130,v130,v161\n" \
"M v[232:235],a[176:179],v[60:63],v[232:235]\nC v129,v128,v129\nv_mul_f32 v132,v132,v162\nM v[232:235],a[184:187],v[88:91],v[232:235]\nC v131,v130,v131\nv_mul_f32 v134,v134,v163\nM v[236:239],a[188:191],v[88:91],v[236:239]\nbuffer_load_dwordx4 v2,s[36:39],s47 offen offset:3072 sc1 lds\n" \
"M v[236:239],a[180:183],v[60:63],v[236:239]\ns_add_u32 m0,s22,49152\nds_read_b128 v[88:91],v20 offset:58368\nv_lshlrev_b32 v128,16,v129\nM v[236:239],a[172:175],v[176:179],v[236:239]\ns_add_u32 s47,s33,24576\nds_read_b128 v[60:63],v20 offset:59392\nC v133,v132,v133\n" \
"M v[236:239],a[164:167],v[180:183],v[236:239]\nds_read_b128 v[176:179],v20 offset:60416\nC v135,v134,v135\nM v[224:227],a[192:195],v[76:79],v[224:227]\ns_waitcnt lgkmcnt(3)\nds_read_b128 v[180:183],v20 offset:53248\nv_lshlrev_b32 v130,16,v131\nM v[224:227],a[200:203],v[84:87],v[224:227]\n" \
"buffer_load_dwordx4 v2,s[36:39],s47 offen offset:0 sc1 lds\nM v[224:227],a[208:211],v[92:95],v[224:227]\nv_lshlrev_b32 v132,16,v133\nv_and_b32 v129,0xffff0000,v129\nM v[224:227],a[216:219],v[72:75],v[224:227]\nv_lshlrev_b32 v134,16,v135\nv_and_b32 v133,0xffff0000,v133\nM v[228:231],a[220:223],v[72:75],v[228:231]\n" \
"ds_read_b128 v[72:75],v20 offset:54272\nv_and_b32 v131,0xffff0000,v131\nM v[228:231],a[212:215],v[92:95],v[228:231]\nbuffer_load_dwordx4 v2,s[36:39],s47 offen offset:1024 sc1 lds\nM v[228:231],a[204:207],v[84:87],v[228:231]\nds_read_b128 v[92:95],v20 offset:55296\nv_and_b32 v135,0xffff0000,v135\nM v[228:231],a[196:199],v[76:79],v[228:231]\n" \
"ds_read_b128 v[84:87],v20 offset:56320\nv_mul_f32 v128,v16,v128\nM v[232:235],a[192:195],v[80:83],v[232:235]\ns_waitcnt lgkmcnt(3)\nds_read_b128 v[76:79],v20 offset:61440\nv_mul_f32 v129,v16,v129\nM v[232:235],a[200:203],v[88:91],v[232:235]\nv_mul_f32 v132,v16,v132\n" \
"v_mul_f32 v133,v16,v133\nM v[232:235],a[208:211],v[60:63],v[232:235]\nbuffer_load_dwordx4 v2,s[36:39],s47 offen offset:2048 sc1 lds\nM v[232:235],a[216:219],v[176:179],v[232:235]\nv_mul_f32 v130,v16,v130\nv_mul_f32 v131,v16,v131\nM v[236:239],a[220:223],v[176:179],v[236:239]\ns_waitcnt lgkmcnt(2)\n" \
"ds_read_b128 v[176:179],v20 offset:62464\nv_mul_f32 v134,v16,v134\nM v[236:239],a[212:215],v[60:63],v[236:239]\ns_waitcnt vmcnt(10) lgkmcnt(14)\nds_read_b128 v[60:63],v20 offset:63488\nv_mul_f32 v135,v16,v135\nM v[236:239],a[204:207],v[88:91],v[236:239]\ns_barrier\n" \
"ds_read_b128 v[88:91],v20 offset:64512\nM v[236:239],a[196:199],v[80:83],v[236:239]\nbuffer_load_dwordx4 v2,s[36:39],s47 offen offset:3072 sc1 lds\nM v[224:227],a[224:227],v[180:183],v[224:227]\ns_add_u32 m0,s22,131072\nds_read_b128 v[80:83],v4 offset:0\nv_mul_f32 v136,v136,v164\nM v[224:227],a[232:235],v[72:75],v[224:227]\n" \
"s_add_u32 s47,s34,0\nv_mul_f32 v138,v138,v165\nv_mul_f32 v140,v140,v166\nM v[224:227],a[240:243],v[92:95],v[224:227]\ns_waitcnt lgkmcnt(3)\nC v129,v128,v129\nv_mul_f32 v142,v142,v167\nM v[224:227],a[248:251],v[84:87],v[224:227]\n" \
"buffer_load_dwordx4 v2,s[36:39],s47 offen offset:0 sc1 lds\nM v[228:231],a[252:255],v[84:87],v[228:231]\nds_read_b128 v[84:87],v4 offset:1024\nC v133,v132,v133\nM v[228:231],a[244:247],v[92:95],v[228:231]\nds_read_b128 v[92:95],v4 offset:2048\nC v224,v224,v225\nM v[228:231],a[236:239],v[72:75],v[228:231]\n" \
"ds_read_b128 v[72:75],v4 offset:3072\nC v225,v226,v227\nM v[228:231],a[228:231],v[180:183],v[228:231]\nbuffer_load_dwordx4 v2,s[36:39],s47 offen offset:1024 sc1 lds\nM v[232:235],a[224:227],v[76:79],v[232:235]\nds_read_b128 v[180:183],v4 offset:8192\nC v131,v130,v131\nM v[232:235],a[232:235],v[176:179],v[232:235]\n" \
"s_waitcnt lgkmcnt(4)\nC v226,v228,v229\nC v227,v230,v231\nM v[232:235],a[240:243],v[60:63],v[232:235]\nC v135,v134,v135\nC v137,v136,v137\nM v[232:235],a[248:251],v[88:91],v[232:235]\nC v139,v138,v139\n" \
"C v141,v140,v141\nM v[236:239],a[252:255],v[88:91],v[236:239]\nbuffer_load_dwordx4 v2,s[36:39],s47 offen offset:2048 sc1 lds\nM v[236:239],a[244:247],v[60:63],v[236:239]\nds_read_b128 v[88:91],v4 offset:9216\nC v232,v232,v233\nM v[236:239],a[236:239],v[176:179],v[236:239]\nds_read_b128 v[60:63],v4 offset:10240\n" \
"C v233,v234,v235\nM v[236:239],a[228:231],v[76:79],v[236:239]\ns_waitcnt lgkmcnt(2)\nds_read_b128 v[176:179],v4 offset:11264\nv_lshlrev_b32 v128,16,v129\nM v[240:243],a[0:3],v[80:83],0\nbuffer_load_dwordx4 v2,s[36:39],s47 offen offset:3072 sc1 lds\nM v[240:243],a[8:11],v[84:87],v[240:243]\n" \
"s_add_u32 m0,s22,147456\nds_read_b128 v[76:79],v4 offset:4096\nC v234,v236,v237\nM v[240:243],a[16:19],v[92:95],v[240:243]\ns_add_u32 s47,s34,8192\nC v235,v238,v239\nC v143,v142,v143\nM v[240:243],a[24:27],v[72:75],v[240:243]\n" \
"v_lshlrev_b32 v132,16,v133\nv_lshlrev_b32 v130,16,v131\nM v[244:247],a[28:31],v[72:75],0\nds_read_b128 v[72:75],v4 offset:5120\nv_lshlrev_b32 v134,16,v135\nM v[244:247],a[20:23],v[92:95],v[244:247]\nbuffer_load_dwordx4 v2,s[36:39],s47 offen offset:0 sc1 lds\nM v[244:247],a[12:15],v[84:87],v[244:247]\n" \
"ds_read_b128 v[92:95],v4 offset:6144\nv_lshlrev_b32 v136,16,v137\nM v[244:247],a[4:7],v[80:83],v[244:247]\nds_read_b128 v[84:87],v4 offset:7168\nv_lshlrev_b32 v140,16,v141\nM v[248:251],a[0:3],v[180:183],0\ns_waitcnt lgkmcnt(3)\nds_read_b128 v[80:83],v4 offset:12288\n" \
"v_lshlrev_b32 v138,16,v139\nM v[248:251],a[8:11],v[88:91],v[248:251]\nbuffer_store_dwordx4 v[224:227],v4,s[36:39],s44 offen offset:0 sc1\nM v[248:251],a[16:19],v[60:63],v[248:251]\nv_lshlrev_b32 v142,16,v143\nv_and_b32 v129,0xffff0000,v129\nM v[248:251],a[24:27],v[176:179],v[248:251]\nv_and_b32 v133,0xffff0000,v133\n" \
"v_and_b32 v131,0xffff0000,v131\nM v[252:255],a[28:31],v[176:179],0\nds_read_b128 v[176:179],v4 offset:13312\nv_and_b32 v135,0xffff0000,v135\nM v[252:255],a[20:23],v[60:63],v[252:255]\nds_read_b128 v[60:63],v4 offset:14336\nv_and_b32 v137,0xffff0000,v137\nM v[252:255],a[12:15],v[88:91],v[252:255]\n" \
"ds_read_b128 v[88:91],v4 offset:15360\nv_and_b32 v141,0xffff0000,v141\nM v[252:255],a[4:7],v[180:183],v[252:255]\nds_read_b128 v[180:183],v4 offset:32768\nv_and_b32 v139,0xffff0000,v139\nM v[240:243],a[32:35],v[76:79],v[240:243]\ns_waitcnt lgkmcnt(4)\nv_and_b32 v143,0xffff0000,v143\n" \
"v_mul_f32 v136,v17,v136\nM v[240:243],a[40:43],v[72:75],v[240:243]\nv_mul_f32 v137,v17,v137\nv_mul_f32 v140,v17,v140\nM v[240:243],a[48:51],v[92:95],v[240:243]\nv_mul_f32 v141,v17,v141\nv_mul_f32 v138,v17,v138\nM v[240:243],a[56:59],v[84:87],v[240:243]\n" \
"v_mul_f32 v139,v17,v139\nv_mul_f32 v142,v17,v142\nM v[244:247],a[60:63],v[84:87],v[244:247]\nbuffer_store_dwordx4 v[232:235],v4,s[36:39],s44 offen offset:1024 sc1\nM v[244:247],a[52:55],v[92:95],v[244:247]\nds_read_b128 v[84:87],v4 offset:33792\nds_read_b128 v[92:95],v4 offset:34816\nM v[244:247],a[44:47],v[72:75],v[244:247]\n" \
"ds_read_b128 v[72:75],v4 offset:35840\nv_mul_f32 v143,v17,v143\nM v[244:247],a[36:39],v[76:79],v[244:247]\nds_read_b128 v[76:79],v4 offset:40960\nv_mul_f32 v144,v144,v168\nM v[248:251],a[32:35],v[80:83],v[248:251]\ns_waitcnt lgkmcnt(4)\nv_mul_f32 v148,v148,v170\n" \
"v_mul_f32 v146,v146,v169\nM v[248:251],a[40:43],v[176:179],v[248:251]\nv_mul_f32 v150,v150,v171\nC v137,v136,v137\nM v[248:251],a[48:51],v[60:63],v[248:251]\nC v141,v140,v141\nC v139,v138,v139\nM v[248:251],a[56:59],v[88:91],v[248:251]\n" \
"C v143,v142,v143\nC v145,v144,v145\nM v[252:255],a[60:63],v[88:91],v[252:255]\nds_read_b128 v[88:91],v4 offset:41984\nC v149,v148,v149\nM v[252:255],a[52:55],v[60:63],v[252:255]\nds_read_b128 v[60:63],v4 offset:43008\nC v147,v146,v147\n" \
"M v[252:255],a[44:47],v[176:179],v[252:255]\nds_read_b128 v[176:179],v4 offset:44032\nC v151,v150,v151\nM v[252:255],a[36:39],v[80:83],v[252:255]\nds_read_b128 v[80:83],v4 offset:36864\nv_lshlrev_b32 v136,16,v137\nM v[240:243],a[64:67],v[180:183],v[240:243]\ns_waitcnt lgkmcnt(4)\n" \
"v_lshlrev_b32 v140,16,v141\nv_lshlrev_b32 v138,16,v139\nM v[240:243],a[72:75],v[84:87],v[240:243]\nbuffer_load_dwordx4 v2,s[36:39],s47 offen offset:1024 sc1 lds\nM v[240:243],a[80:83],v[92:95],v[240:243]\nv_lshlrev_b32 v142,16,v143\nv_lshlrev_b32 v144,16,v145\nM v[240:243],a[88:91],v[72:75],v[240:243]\n" \
"v_lshlrev_b32 v148,16,v149\nv_lshlrev_b32 v146,16,v147\nM v[244:247],a[92:95],v[72:75],v[244:247]\nds_read_b128 v[72:75],v4 offset:37888\nv_lshlrev_b32 v150,16,v151\nM v[244:247],a[84:87],v[92:95],v[244:247]\nds_read_b128 v[92:95],v4 offset:38912\nv_and_b32 v137,0xffff0000,v137\n" \
"M v[244:247],a[76:79],v[84:87],v[244:247]\nbuffer_load_dwordx4 v2,s[36:39],s47 offen offset:2048 sc1 lds\nM v[244:247],a[68:71],v[180:183],v[244:247]\nds_read_b128 v[84:87],v4 offset:39936\nv_and_b32 v141,0xffff0000,v141\nM v[248:251],a[64:67],v[76:79],v[248:251]\ns_waitcnt lgkmcnt(3)\nds_read_b128 v[180:183],v4 offset:45056\n" \
"v_and_b32 v139,0xffff0000,v139\nM v[248:251],a[72:75],v[88:91],v[248:251]\nv_and_b32 v143,0xffff0000,v143\nv_and_b32 v145,0xffff0000,v145\nM v[248:251],a[80:83],v[60:63],v[248:251]\nbuffer_load_dwordx4 v2,s[36:39],s47 offen offset:3072 sc1 lds\nM v[248:251],a[88:91],v[176:179],v[248:251]\ns_add_u32 m0,s22,65536\n" \
"v_and_b32 v149,0xffff0000,v149\nv_and_b32 v147,0xffff0000,v147\nM v[252:255],a[92:95],v[176:179],v[252:255]\ns_add_u32 s47,s34,16384\nds_read_b128 v[176:179],v4 offset:46080\nv_and_b32 v151,0xffff0000,v151\nM v[252:255],a[84:87],v[60:63],v[252:255]\ns_waitcnt vmcnt(10) lgkmcnt(14)\n" \
"ds_read_b128 v[60:63],v4 offset:47104\nv_mul_f32 v144,v18,v144\nM v[252:255],a[76:79],v[88:91],v[252:255]\ns_barrier\nds_read_b128 v[88:91],v4 offset:48128\nM v[252:255],a[68:71],v[76:79],v[252:255]\nbuffer_load_dwordx4 v2,s[36:39],s47 offen offset:0 sc1 lds\nM v[240:243],a[96:99],v[80:83],v[240:243]\n" \
"s_waitcnt lgkmcnt(3)\nds_read_b128 v[76:79],v4 offset:16384\nv_mul_f32 v145,v18,v145\nM v[240:243],a[104:107],v[72:75],v[240:243]\nv_mul_f32 v148,v18,v148\nv_mul_f32 v149,v18,v149\nM v[240:243],a[112:115],v[92:95],v[240:243]\nv_mul_f32 v146,v18,v146\n" \
"v_mul_f32 v147,v18,v147\nM v[240:243],a[120:123],v[84:87],v[240:243]\nbuffer_load_dwordx4 v2,s[36:39],s47 offen offset:1024 sc1 lds\nM v[244:247],a[124:127],v[84:87],v[244:247]\nds_read_b128 v[84:87],v4 offset:17408\nv_mul_f32 v150,v18,v150\nM v[244:247],a[116:119],v[92:95],v[244:247]\ns_waitcnt vmcnt(17)\n" \
"ds_read_b128 v[92:95],v4 offset:18432\nv_mul_f32 v151,v18,v151\nM v[244:247],a[108:111],v[72:75],v[244:247]\nds_read_b128 v[72:75],v4 offset:19456\nv_mul_f32 v152,v152,v172\nM v[244:247],a[100:103],v[80:83],v[244:247]\nbuffer_store_dwordx2 v[28:29],v191,s[36:39],s95 offen offset:0 sc1\nM v[248:251],a[96:99],v[180:183],v[248:251]\n" \
"s_waitcnt lgkmcnt(3)\nds_read_b128 v[80:83],v4 offset:24576\nv_mul_f32 v156,v156,v174\nM v[248:251],a[104:107],v[176:179],v[248:251]\nbuffer_load_dwordx4 v2,s[36:39],s47 offen offset:2048 sc1 lds\nM v[248:251],a[112:115],v[60:63],v[248:251]\nv_mul_f32 v154,v154,v173\nv_mul_f32 v158,v158,v175\n" \
"M v[248:251],a[120:123],v[88:91],v[248:251]\nC v145,v144,v145\nC v149,v148,v149\nM v[252:255],a[124:127],v[88:91],v[252:255]\nds_read_b128 v[88:91],v4 offset:25600\nC v147,v146,v147\nM v[252:255],a[116:119],v[60:63],v[252:255]\ns_waitcnt lgkmcnt(2)\n" \
"ds_read_b128 v[60:63],v4 offset:26624\nC v151,v150,v151\nM v[252:255],a[108:111],v[176:179],v[252:255]\nbuffer_load_dwordx4 v2,s[36:39],s47 offen offset:3072 sc1 lds\nM v[252:255],a[100:103],v[180:183],v[252:255]\ns_add_u32 m0,s22,98304\nds_read_b128 v[176:179],v4 offset:27648\nC v153,v152,v153\n" \
"M v[240:243],a[128:131],v[76:79],v[240:243]\ns_add_u32 s47,s34,24576\nds_read_b128 v[180:183],v4 offset:20480\nC v157,v156,v157\nM v[240:243],a[136:139],v[84:87],v[240:243]\nC v155,v154,v155\nC v159,v158,v159\nM v[240:243],a[144:147],v[92:95],v[240:243]\n" \
"buffer_load_dwordx4 v2,s[36:39],s47 offen offset:0 sc1 lds\nM v[240:243],a[152:155],v[72:75],v[240:243]\nv_lshlrev_b32 v144,16,v145\nv_lshlrev_b32 v148,16,v149\nM v[244:247],a[156:159],v[72:75],v[244:247]\nds_read_b128 v[72:75],v4 offset:21504\nv_lshlrev_b32 v146,16,v147\nM v[244:247],a[148:151],v[92:95],v[244:247]\n" \
"ds_read_b128 v[92:95],v4 offset:22528\nv_lshlrev_b32 v150,16,v151\nM v[244:247],a[140:143],v[84:87],v[244:247]\ns_waitcnt lgkmcnt(2)\nds_read_b128 v[84:87],v4 offset:23552\nv_lshlrev_b32 v152,16,v153\nM v[244:247],a[132:135],v[76:79],v[244:247]\nbuffer_load_dwordx4 v2,s[36:39],s47 offen offset:1024 sc1 lds\n" \
"M v[248:251],a[128:131],v[80:83],v[248:251]\nds_read_b128 v[76:79],v4 offset:28672\nv_lshlrev_b32 v156,16,v157\nM v[248:251],a[136:139],v[88:91],v[248:251]\nv_lshlrev_b32 v154,16,v155\nv_lshlrev_b32 v158,16,v159\nM v[248:251],a[144:147],v[60:63],v[248:251]\nv_and_b32 v145,0xffff0000,v145\n" \
"v_and_b32 v149,0xffff0000,v149\nM v[248:251],a[152:155],v[176:179],v[248:251]\nbuffer_load_dwordx4 v2,s[36:39],s47 offen offset:2048 sc1 lds\nM v[252:255],a[156:159],v[176:179],v[252:255]\nds_read_b128 v[176:179],v4 offset:29696\nv_and_b32 v147,0xffff0000,v147\nM v[252:255],a[148:151],v[60:63],v[252:255]\nds_read_b128 v[60:63],v4 offset:30720\n" \
"v_and_b32 v151,0xffff0000,v151\nM v[252:255],a[140:143],v[88:91],v[252:255]\nds_read_b128 v[88:91],v4 offset:31744\nv_and_b32 v153,0xffff0000,v153\nM v[252:255],a[132:135],v[80:83],v[252:255]\ns_waitcnt lgkmcnt(3)\nds_read_b128 v[80:83],v4 offset:49152\nv_and_b32 v157,0xffff0000,v157\n" \
"M v[240:243],a[160:163],v[180:183],v[240:243]\nbuffer_load_dwordx4 v2,s[36:39],s47 offen offset:3072 sc1 lds\nM v[240:243],a[168:171],v[72:75],v[240:243]\ns_add_u32 m0,s22,81920\nv_and_b32 v155,0xffff0000,v155\nv_and_b32 v159,0xffff0000,v159\nM v[240:243],a[176:179],v[92:95],v[240:243]\ns_add_u32 s47,s35,0\n" \
"v_mul_f32 v152,v19,v152\nv_mul_f32 v153,v19,v153\nM v[240:243],a[184:187],v[84:87],v[240:243]\nv_mul_f32 v156,v19,v156\nv_mul_f32 v157,v19,v157\nM v[244:247],a[188:191],v[84:87],v[244:247]\nbuffer_load_dwordx4 v2,s[36:39],s47 offen offset:0 sc1 lds\nM v[244:247],a[180:183],v[92:95],v[244:247]\n" \
"ds_read_b128 v[84:87],v4 offset:50176\nv_mul_f32 v154,v19,v154\nM v[244:247],a[172:175],v[72:75],v[244:247]\nds_read_b128 v[92:95],v4 offset:51200\nv_mul_f32 v155,v19,v155\nM v[244:247],a[164:167],v[180:183],v[244:247]\nds_read_b128 v[72:75],v4 offset:52224\nv_mul_f32 v158,v19,v158\n" \
"M v[248:251],a[160:163],v[76:79],v[248:251]\ns_waitcnt lgkmcnt(3)\nds_read_b128 v[180:183],v4 offset:57344\nv_mul_f32 v159,v19,v159\nM v[248:251],a[168:171],v[176:179],v[248:251]\nbuffer_load_dwordx4 v2,s[36:39],s47 offen offset:1024 sc1 lds\nM v[248:251],a[176:179],v[60:63],v[248:251]\nC v153,v152,v153\n" \
"C v157,v156,v157\nM v[248:251],a[184:187],v[88:91],v[248:251]\nC v155,v154,v155\nC v159,v158,v159\nM v[252:255],a[188:191],v[88:91],v[252:255]\nds_read_b128 v[88:91],v4 offset:58368\nv_lshlrev_b32 v152,16,v153\nM v[252:255],a[180:183],v[60:63],v[252:255]\n" \
"buffer_load_dwordx4 v2,s[36:39],s47 offen offset:2048 sc1 lds\nM v[252:255],a[172:175],v[176:179],v[252:255]\nds_read_b128 v[60:63],v4 offset:59392\nv_lshlrev_b32 v156,16,v157\nM v[252:255],a[164:167],v[76:79],v[252:255]\nds_read_b128 v[176:179],v4 offset:60416\nv_lshlrev_b32 v154,16,v155\nM v[240:243],a[192:195],v[80:83],v[240:243]\n" \
"s_waitcnt lgkmcnt(3)\nds_read_b128 v[76:79],v4 offset:53248\nv_lshlrev_b32 v158,16,v159\nM v[240:243],a[200:203],v[84:87],v[240:243]\nv_and_b32 v153,0xffff0000,v153\nv_and_b32 v157,0xffff0000,v157\nM v[240:243],a[208:211],v[92:95],v[240:243]\nbuffer_load_dwordx4 v2,s[36:39],s47 offen offset:3072 sc1 lds\n" \
"M v[240:243],a[216:219],v[72:75],v[240:243]\ns_add_u32 m0,s22,114688\nv_and_b32 v155,0xffff0000,v155\nv_and_b32 v159,0xffff0000,v159\nM v[244:247],a[220:223],v[72:75],v[244:247]\ns_add_u32 s47,s35,8192\nds_read_b128 v[72:75],v4 offset:54272\nM v[244:247],a[212:215],v[92:95],v[244:247]\n" \
"ds_read_b128 v[92:95],v4 offset:55296\nM v[244:247],a[204:207],v[84:87],v[244:247]\nbuffer_load_dwordx4 v2,s[36:39],s47 offen offset:0 sc1 lds\nM v[244:247],a[196:199],v[80:83],v[244:247]\nds_read_b128 v[84:87],v4 offset:56320\nM v[248:251],a[192:195],v[180:183],v[248:251]\ns_waitcnt lgkmcnt(3)\nds_read_b128 v[80:83],v4 offset:61440\n" \
"M v[248:251],a[200:203],v[88:91],v[248:251]\nM v[248:251],a[208:211],v[60:63],v[248:251]\nM v[248:251],a[216:219],v[176:179],v[248:251]\nbuffer_load_dwordx4 v2,s[36:39],s47 offen offset:1024 sc1 lds\nM v[252:255],a[220:223],v[176:179],v[252:255]\nds_read_b128 v[176:179],v4 offset:62464\nM v[252:255],a[212:215],v[60:63],v[252:255]\ns_waitcnt vmcnt(15) lgkmcnt(14)\n" \
"ds_read_b128 v[60:63],v4 offset:63488\nM v[252:255],a[204:207],v[88:91],v[252:255]\ns_barrier\nds_read_b128 v[88:91],v4 offset:64512\nM v[252:255],a[196:199],v[180:183],v[252:255]\nbuffer_load_dwordx4 v2,s[36:39],s47 offen offset:2048 sc1 lds\nM v[240:243],a[224:227],v[76:79],v[240:243]\ns_waitcnt lgkmcnt(3)\n" \
"ds_read_b128 v[180:183],v23 offset:0\nM v[240:243],a[232:235],v[72:75],v[240:243]\nM v[240:243],a[240:243],v[92:95],v[240:243]\nM v[240:243],a[248:251],v[84:87],v[240:243]\nbuffer_load_dwordx2 v[10:11],v190,s[36:39],0 offen sc1\nM v[244:247],a[252:255],v[84:87],v[244:247]\nds_read_b128 v[84:87],v23 offset:1024\nM v[244:247],a[244:247],v[92:95],v[244:247]\n" \
"buffer_load_dwordx4 v2,s[36:39],s47 offen offset:3072 sc1 lds\nM v[244:247],a[236:239],v[72:75],v[244:247]\ns_add_u32 m0,s22,0\nds_read_b128 v[92:95],v23 offset:2048\nC v240,v240,v241\nM v[244:247],a[228:231],v[76:79],v[244:247]\ns_add_u32 s47,s35,16384\nds_read_b128 v[72:75],v23 offset:3072\n" \
"C v241,v242,v243\nM v[248:251],a[224:227],v[80:83],v[248:251]\nLG25_%=:\ns_cbranch_execz LHR3_%=\ns_waitcnt lgkmcnt(3)\nds_read_b128 v[76:79],v23 offset:8192\nM v[248:251],a[232:235],v[176:179],v[248:251]\nC v242,v244,v245\n" \
"C v243,v246,v247\nM v[248:251],a[240:243],v[60:63],v[248:251]\nbuffer_store_dwordx4 v[240:243],v4,s[36:39],s45 offen offset:0 sc1\nM v[248:251],a[248:251],v[88:91],v[248:251]\nM v[252:255],a[252:255],v[88:91],v[252:255]\nds_read_b128 v[88:91],v23 offset:9216\nM v[252:255],a[244:247],v[60:63],v[252:255]\nds_read_b128 v[60:63],v23 offset:10240\n" \
"C v248,v248,v249\nM v[252:255],a[236:239],v[176:179],v[252:255]\nds_read_b128 v[176:179],v23 offset:11264\nC v249,v250,v251\nM v[252:255],a[228:231],v[80:83],v[252:255]\nds_read_b128 v[80:83],v23 offset:4096\nM v[192:195],a[0:3],v[180:183],0\ns_waitcnt lgkmcnt(4)\n" \
"M v[192:195],a[8:11],v[84:87],v[192:195]\nC v250,v252,v253\nC v251,v254,v255\nM v[192:195],a[16:19],v[92:95],v[192:195]\nM v[192:195],a[24:27],v[72:75],v[192:195]\nM v[196:199],a[28:31],v[72:75],0\nds_read_b128 v[72:75],v23 offset:5120\nM v[196:199],a[20:23],v[92:95],v[196:199]\n" \
"ds_read_b128 v[92:95],v23 offset:6144\nM v[196:199],a[12:15],v[84:87],v[196:199]\nds_read_b128 v[84:87],v23 offset:7168\nM v[196:199],a[4:7],v[180:183],v[196:199]\nbuffer_store_dwordx4 v[248:251],v4,s[36:39],s45 offen offset:1024 sc1\nM v[200:203],a[0:3],v[76:79],0\ns_waitcnt lgkmcnt(3)\nM v[200:203],a[8:11],v[88:91],v[200:203]\n" \
"M v[200:203],a[16:19],v[60:63],v[200:203]\nM v[200:203],a[24:27],v[176:179],v[200:203]\nM v[204:207],a[28:31],v[176:179],0\nM v[204:207],a[20:23],v[60:63],v[204:207]\nds_read_b128 v[60:63],v23 offset:12288\nM v[204:207],a[12:15],v[88:91],v[204:207]\nds_read_b128 v[88:91],v23 offset:13312\nM v[204:207],a[4:7],v[76:79],v[204:207]\n" \
"ds_read_b128 v[76:79],v23 offset:14336\nM v[192:195],a[32:35],v[80:83],v[192:195]\ns_waitcnt lgkmcnt(3)\nM v[192:195],a[40:43],v[72:75],v[192:195]\nM v[192:195],a[48:51],v[92:95],v[192:195]\nM v[192:195],a[56:59],v[84:87],v[192:195]\nM v[196:199],a[60:63],v[84:87],v[196:199]\nds_read_b128 v[84:87],v23 offset:15360\n" \
"M v[196:199],a[52:55],v[92:95],v[196:199]\nbuffer_load_dwordx4 v2,s[36:39],s47 offen offset:0 sc1 lds\nM v[196:199],a[44:47],v[72:75],v[196:199]\nds_read_b128 v[92:95],v23 offset:16384\nM v[196:199],a[36:39],v[80:83],v[196:199]\ns_waitcnt lgkmcnt(2)\nds_read_b128 v[72:75],v23 offset:17408\nM v[200:203],a[32:35],v[60:63],v[200:203]\n" \
"ds_read_b128 v[80:83],v23 offset:18432\nM v[200:203],a[40:43],v[88:91],v[200:203]\nbuffer_load_dwordx4 v2,s[36:39],s47 offen offset:1024 sc1 lds\nM v[200:203],a[48:51],v[76:79],v[200:203]\ns_waitcnt lgkmcnt(3)\nM v[200:203],a[56:59],v[84:87],v[200:203]\nM v[204:207],a[60:63],v[84:87],v[204:207]\nds_read_b128 v[84:87],v23 offset:19456\n" \
"M v[204:207],a[52:55],v[76:79],v[204:207]\nbuffer_load_dwordx4 v2,s[36:39],s47 offen offset:2048 sc1 lds\nM v[204:207],a[44:47],v[88:91],v[204:207]\nds_read_b128 v[76:79],v23 offset:24576\nM v[204:207],a[36:39],v[60:63],v[204:207]\ns_waitcnt lgkmcnt(2)\nds_read_b128 v[88:91],v23 offset:25600\nM v[192:195],a[64:67],v[92:95],v[192:195]\n" \
"s_waitcnt lgkmcnt(2)\nds_read_b128 v[60:63],v23 offset:26624\nM v[192:195],a[72:75],v[72:75],v[192:195]\nbuffer_load_dwordx4 v2,s[36:39],s47 offen offset:3072 sc1 lds\nM v[192:195],a[80:83],v[80:83],v[192:195]\ns_add_u32 m0,s22,32768\nM v[192:195],a[88:91],v[84:87],v[192:195]\ns_add_u32 s47,s35,24576\n" \
"M v[196:199],a[92:95],v[84:87],v[196:199]\ns_waitcnt lgkmcnt(2)\nds_read_b128 v[84:87],v23 offset:27648\nM v[196:199],a[84:87],v[80:83],v[196:199]\ns_waitcnt vmcnt(7)\nv_cmp_gt_u64 s[98:99],s[14:15],v[10:11]\nv_cmp_le_u64 s[18:19],s[16:17],v[10:11]\nM v[196:199],a[76:79],v[72:75],v[196:199]\n" \
"s_or_b64 s[98:99],s[98:99],s[18:19]\ns_and_b32 s98,s98,0x7\ns_cbranch_scc1 LPP_%=\nLPPR_%=:\nds_read_b128 v[80:83],v23 offset:20480\nds_read_b128 v[72:75],v23 offset:21504\nM v[196:199],a[68:71],v[92:95],v[196:199]\nbuffer_load_dwordx4 v[96:99],v4,s[36:39],s63 offen offset:0 sc1\n" \
"M v[200:203],a[64:67],v[76:79],v[200:203]\ns_waitcnt lgkmcnt(3)\nds_read_b128 v[92:95],v23 offset:22528\nM v[200:203],a[72:75],v[88:91],v[200:203]\nM v[200:203],a[80:83],v[60:63],v[200:203]\ns_waitcnt lgkmcnt(3)\nM v[200:203],a[88:91],v[84:87],v[200:203]\nbuffer_load_dwordx4 v[100:103],v4,s[36:39],s63 offen offset:1024 sc1\n" \
"M v[204:207],a[92:95],v[84:87],v[204:207]\nds_read_b128 v[84:87],v23 offset:23552\nM v[204:207],a[84:87],v[60:63],v[204:207]\nds_read_b128 v[60:63],v23 offset:28672\nM v[204:207],a[76:79],v[88:91],v[204:207]\ns_waitcnt lgkmcnt(2)\nds_read_b128 v[88:91],v23 offset:29696\nM v[204:207],a[68:71],v[76:79],v[204:207]\n" \
"buffer_load_dwordx4 v[104:107],v4,s[36:39],s63 offen offset:2048 sc1\nM v[192:195],a[96:99],v[80:83],v[192:195]\ns_waitcnt lgkmcnt(2)\nds_read_b128 v[76:79],v23 offset:30720\nM v[192:195],a[104:107],v[72:75],v[192:195]\ns_waitcnt vmcnt(18) lgkmcnt(15)\nM v[192:195],a[112:115],v[92:95],v[192:195]\ns_barrier\n" \
"M v[192:195],a[120:123],v[84:87],v[192:195]\nbuffer_load_dwordx4 v[108:111],v4,s[36:39],s63 offen offset:3072 sc1\nM v[196:199],a[124:127],v[84:87],v[196:199]\ns_add_u32 s63,s49,4096\nds_read_b128 v[84:87],v23 offset:31744\nM v[196:199],a[116:119],v[92:95],v[196:199]\nds_read_b128 v[92:95],v20 offset:0\nM v[196:199],a[108:111],v[72:75],v[196:199]\n" \
"ds_read_b128 v[72:75],v20 offset:1024\nM v[196:199],a[100:103],v[80:83],v[196:199]\ns_waitcnt lgkmcnt(3)\nds_read_b128 v[80:83],v20 offset:2048\nM v[200:203],a[96:99],v[60:63],v[200:203]\nbuffer_load_dwordx4 v[112:115],v4,s[36:39],s63 offen offset:0 sc1\nM v[200:203],a[104:107],v[88:91],v[200:203]\nM v[200:203],a[112:115],v[76:79],v[200:203]\n" \
"s_waitcnt lgkmcnt(3)\nM v[200:203],a[120:123],v[84:87],v[200:203]\nM v[204:207],a[124:127],v[84:87],v[204:207]\nds_read_b128 v[84:87],v20 offset:3072\nM v[204:207],a[116:119],v[76:79],v[204:207]\nbuffer_load_dwordx4 v[116:119],v4,s[36:39],s63 offen offset:1024 sc1\nM v[204:207],a[108:111],v[88:91],v[204:207]\nds_read_b128 v[76:79],v20 offset:8192\n" \
"ds_read_b128 v[88:91],v20 offset:9216\nM v[204:207],a[100:103],v[60:63],v[204:207]\ns_waitcnt lgkmcnt(3)\nds_read_b128 v[60:63],v20 offset:10240\nM v[192:195],a[128:131],v[92:95],v[192:195]\nM v[192:195],a[136:139],v[72:75],v[192:195]\nbuffer_load_dwordx4 v2,s[36:39],s47 offen offset:0 sc1 lds\nM v[192:195],a[144:147],v[80:83],v[192:195]\n" \
"s_waitcnt lgkmcnt(3)\nM v[192:195],a[152:155],v[84:87],v[192:195]\nM v[196:199],a[156:159],v[84:87],v[196:199]\nds_read_b128 v[84:87],v20 offset:11264\nM v[196:199],a[148:151],v[80:83],v[196:199]\nbuffer_load_dwordx4 v2,s[36:39],s47 offen offset:1024 sc1 lds\nM v[196:199],a[140:143],v[72:75],v[196:199]\nds_read_b128 v[80:83],v20 offset:4096\n" \
"M v[196:199],a[132:135],v[92:95],v[196:199]\ns_waitcnt lgkmcnt(2)\nds_read_b128 v[72:75],v20 offset:5120\nM v[200:203],a[128:131],v[76:79],v[200:203]\nds_read_b128 v[92:95],v20 offset:6144\nM v[200:203],a[136:139],v[88:91],v[200:203]\ns_waitcnt lgkmcnt(3)\nM v[200:203],a[144:147],v[60:63],v[200:203]\n" \
"buffer_load_dwordx4 v2,s[36:39],s47 offen offset:2048 sc1 lds\nM v[200:203],a[152:155],v[84:87],v[200:203]\nM v[204:207],a[156:159],v[84:87],v[204:207]\nds_read_b128 v[84:87],v20 offset:7168\nM v[204:207],a[148:151],v[60:63],v[204:207]\nds_read_b128 v[60:63],v20 offset:12288\nM v[204:207],a[140:143],v[88:91],v[204:207]\nbuffer_load_dwordx4 v2,s[36:39],s47 offen offset:3072 sc1 lds\n" \
"M v[204:207],a[132:135],v[76:79],v[204:207]\ns_waitcnt lgkmcnt(2)\nds_read_b128 v[88:91],v20 offset:13312\nM v[192:195],a[160:163],v[80:83],v[192:195]\nds_read_b128 v[76:79],v20 offset:14336\nM v[192:195],a[168:171],v[72:75],v[192:195]\nM v[192:195],a[176:179],v[92:95],v[192:195]\ns_waitcnt lgkmcnt(3)\n" \
"M v[192:195],a[184:187],v[84:87],v[192:195]\nM v[196:199],a[188:191],v[84:87],v[196:199]\nds_read_b128 v[84:87],v20 offset:15360\nM v[196:199],a[180:183],v[92:95],v[196:199]\nds_read_b128 v[92:95],v20 offset:32768\nM v[196:199],a[172:175],v[72:75],v[196:199]\nds_read_b128 v[72:75],v20 offset:33792\nM v[196:199],a[164:167],v[80:83],v[196:199]\n" \
"s_waitcnt lgkmcnt(3)\nds_read_b128 v[80:83],v20 offset:34816\nM v[200:203],a[160:163],v[60:63],v[200:203]\nM v[200:203],a[168:171],v[88:91],v[200:203]\nM v[200:203],a[176:179],v[76:79],v[200:203]\ns_waitcnt lgkmcnt(3)\nM v[200:203],a[184:187],v[84:87],v[200:203]\nM v[204:207],a[188:191],v[84:87],v[204:207]\n" \
"ds_read_b128 v[84:87],v20 offset:35840\nM v[204:207],a[180:183],v[76:79],v[204:207]\nds_read_b128 v[76:79],v20 offset:40960\nM v[204:207],a[172:175],v[88:91],v[204:207]\nds_read_b128 v[88:91],v20 offset:41984\nM v[204:207],a[164:167],v[60:63],v[204:207]\ns_waitcnt lgkmcnt(3)\nds_read_b128 v[60:63],v20 offset:43008\n" \
"M v[192:195],a[192:195],v[92:95],v[192:195]\nM v[192:195],a[200:203],v[72:75],v[192:195]\nM v[192:195],a[208:211],v[80:83],v[192:195]\ns_waitcnt lgkmcnt(3)\nM v[192:195],a[216:219],v[84:87],v[192:195]\nM v[196:199],a[220:223],v[84:87],v[196:199]\nds_read_b128 v[84:87],v20 offset:44032\nM v[196:199],a[212:215],v[80:83],v[196:199]\n" \
"ds_read_b128 v[80:83],v20 offset:36864\nM v[196:199],a[204:207],v[72:75],v[196:199]\nds_read_b128 v[72:75],v20 offset:37888\nM v[196:199],a[196:199],v[92:95],v[196:199]\ns_waitcnt lgkmcnt(3)\nds_read_b128 v[92:95],v20 offset:38912\nM v[200:203],a[192:195],v[76:79],v[200:203]\nM v[200:203],a[200:203],v[88:91],v[200:203]\n" \
"M v[200:203],a[208:211],v[60:63],v[200:203]\ns_waitcnt lgkmcnt(3)\nM v[200:203],a[216:219],v[84:87],v[200:203]\ns_add_u32 s63,s49,4096\nM v[204:207],a[220:223],v[84:87],v[204:207]\nds_read_b128 v[84:87],v20 offset:39936\nM v[204:207],a[212:215],v[60:63],v[204:207]\nbuffer_load_dwordx4 v[176:179],v4,s[36:39],s63 offen offset:2048 sc1\n" \
"M v[204:207],a[204:207],v[88:91],v[204:207]\ns_waitcnt lgkmcnt(1)\nds_read_b128 v[60:63],v20 offset:45056\nds_read_b128 v[88:91],v20 offset:46080\nM v[204:207],a[196:199],v[76:79],v[204:207]\ns_waitcnt vmcnt(5)\nds_read_b128 v[76:79],v20 offset:47104\nM v[192:195],a[224:227],v[80:83],v[192:195]\n" \
"s_waitcnt lgkmcnt(3)\nM v[192:195],a[232:235],v[72:75],v[192:195]\ns_waitcnt vmcnt(17) lgkmcnt(15)\nM v[192:195],a[240:243],v[92:95],v[192:195]\ns_barrier\nM v[192:195],a[248:251],v[84:87],v[192:195]\nbuffer_load_dwordx4 v[180:183],v4,s[36:39],s63 offen offset:3072 sc1\nM v[196:199],a[252:255],v[84:87],v[196:199]\n" \
"s_add_u32 s63,s49,8192\nds_read_b128 v[84:87],v20 offset:48128\nM v[196:199],a[244:247],v[92:95],v[196:199]\nC v192,v192,v193\nC v193,v194,v195\nM v[196:199],a[236:239],v[72:75],v[196:199]\ns_waitcnt lgkmcnt(1)\nds_read_b128 v[92:95],v20 offset:16384\n" \
"ds_read_b128 v[72:75],v20 offset:17408\nM v[196:199],a[228:231],v[80:83],v[196:199]\nbuffer_load_dwordx4 v[32:35],v4,s[36:39],s63 offen offset:0 sc1\nM v[200:203],a[224:227],v[60:63],v[200:203]\nds_read_b128 v[80:83],v20 offset:18432\nM v[200:203],a[232:235],v[88:91],v[200:203]\nC v194,v196,v197\nC v195,v198,v199\n" \
"M v[200:203],a[240:243],v[76:79],v[200:203]\ns_waitcnt lgkmcnt(3)\nM v[200:203],a[248:251],v[84:87],v[200:203]\nbuffer_load_dwordx4 v[36:39],v4,s[36:39],s63 offen offset:1024 sc1\nM v[204:207],a[252:255],v[84:87],v[204:207]\nds_read_b128 v[84:87],v20 offset:19456\nM v[204:207],a[244:247],v[76:79],v[204:207]\nds_read_b128 v[76:79],v20 offset:24576\n" \
"C v200,v200,v201\nM v[204:207],a[236:239],v[88:91],v[204:207]\nds_read_b128 v[88:91],v20 offset:25600\nC v201,v202,v203\nM v[204:207],a[228:231],v[60:63],v[204:207]\nbuffer_store_dwordx4 v[192:195],v4,s[36:39],s48 offen offset:0 sc1\nM v[48:51],v[186:189],v[120:123],0\nds_read_b128 v[60:63],v20 offset:26624\n" \
"M v[52:55],v[184:187],v[120:123],0\nC v202,v204,v205\nC v203,v206,v207\nM v[64:67],v[186:189],v[124:127],0\nM v[68:71],v[184:187],v[124:127],0\nM v[48:51],v[186:189],v[96:99],v[48:51]\nM v[52:55],v[184:187],v[96:99],v[52:55]\nM v[64:67],v[186:189],v[100:103],v[64:67]\n" \
"M v[68:71],v[184:187],v[100:103],v[68:71]\nM v[48:51],v[186:189],v[104:107],v[48:51]\nbuffer_store_dwordx4 v[200:203],v4,s[36:39],s48 offen offset:1024 sc1\nM v[52:55],v[184:187],v[104:107],v[52:55]\nM v[64:67],v[186:189],v[108:111],v[64:67]\nM v[68:71],v[184:187],v[108:111],v[68:71]\nM v[48:51],v[186:189],v[112:115],v[48:51]\nM v[52:55],v[184:187],v[112:115],v[52:55]\n" \
"M v[64:67],v[186:189],v[116:119],v[64:67]\nC v160,v48,v49\nC v161,v50,v51\nM v[68:71],v[184:187],v[116:119],v[68:71]\ns_waitcnt lgkmcnt(0)\nC v162,v52,v53\nC v163,v54,v55\nM v[208:211],a[0:3],v[92:95],0\n" \
"v_lshlrev_b32 v48,16,v160\nv_lshlrev_b32 v52,16,v162\nM v[208:211],a[8:11],v[72:75],v[208:211]\nv_and_b32 v49,0xffff0000,v160\nv_and_b32 v53,0xffff0000,v162\nM v[208:211],a[16:19],v[80:83],v[208:211]\nv_lshlrev_b32 v50,16,v161\nv_lshlrev_b32 v54,16,v163\n" \
"M v[208:211],a[24:27],v[84:87],v[208:211]\nv_and_b32 v51,0xffff0000,v161\nv_and_b32 v55,0xffff0000,v163\nM v[212:215],a[28:31],v[84:87],0\nds_read_b128 v[84:87],v20 offset:27648\nv_mul_f32 v48,v24,v48\nM v[212:215],a[20:23],v[80:83],v[212:215]\nds_read_b128 v[80:83],v20 offset:20480\n" \
"v_mul_f32 v52,v24,v52\nM v[212:215],a[12:15],v[72:75],v[212:215]\nds_read_b128 v[72:75],v20 offset:21504\nv_mul_f32 v49,v24,v49\nM v[212:215],a[4:7],v[92:95],v[212:215]\nds_read_b128 v[92:95],v20 offset:22528\nv_mul_f32 v53,v24,v53\nM v[216:219],a[0:3],v[76:79],0\n" \
"v_mul_f32 v50,v24,v50\nv_mul_f32 v51,v24,v51\nM v[216:219],a[8:11],v[88:91],v[216:219]\nv_mul_f32 v54,v24,v54\nv_mul_f32 v55,v24,v55\nM v[216:219],a[16:19],v[60:63],v[216:219]\ns_waitcnt lgkmcnt(3)\nC v160,v48,v49\n" \
"C v162,v52,v53\nM v[216:219],a[24:27],v[84:87],v[216:219]\nC v161,v50,v51\nC v163,v54,v55\nM v[220:223],a[28:31],v[84:87],0\nds_read_b128 v[84:87],v20 offset:23552\nv_lshlrev_b32 v48,16,v160\nM v[220:223],a[20:23],v[60:63],v[220:223]\n" \
"ds_read_b128 v[60:63],v20 offset:28672\nv_lshlrev_b32 v52,16,v162\nM v[220:223],a[12:15],v[88:91],v[220:223]\nds_read_b128 v[88:91],v20 offset:29696\nv_and_b32 v49,0xffff0000,v160\nM v[220:223],a[4:7],v[76:79],v[220:223]\ns_waitcnt lgkmcnt(3)\nds_read_b128 v[76:79],v20 offset:30720\n" \
"v_lshlrev_b32 v50,16,v161\nM v[208:211],a[32:35],v[80:83],v[208:211]\nv_and_b32 v53,0xffff0000,v162\nv_lshlrev_b32 v54,16,v163\nM v[208:211],a[40:43],v[72:75],v[208:211]\nv_and_b32 v51,0xffff0000,v161\nv_and_b32 v55,0xffff0000,v163\nM v[208:211],a[48:51],v[92:95],v[208:211]\n" \
"s_waitcnt lgkmcnt(3)\nC v164,v64,v65\nC v166,v68,v69\nM v[208:211],a[56:59],v[84:87],v[208:211]\nC v165,v66,v67\nC v167,v70,v71\nM v[212:215],a[60:63],v[84:87],v[212:215]\ns_waitcnt vmcnt(2)\n" \
"ds_read_b128 v[84:87],v20 offset:31744\nv_max_u32 v10,v176,v177\nM v[212:215],a[52:55],v[92:95],v[212:215]\nds_read_b128 v[92:95],v20 offset:49152\nv_max3_u32 v10,v10,v178,v179\nM v[212:215],a[44:47],v[72:75],v[212:215]\nds_read_b128 v[72:75],v20 offset:50176\nv_max3_u32 v10,v10,v180,v181\n" \
"M v[212:215],a[36:39],v[80:83],v[212:215]\ns_waitcnt lgkmcnt(3)\nds_read_b128 v[80:83],v20 offset:51200\nv_max3_u32 v10,v10,v182,v183\nM v[216:219],a[32:35],v[60:63],v[216:219]\nv_max3_u32 v10,v10,v32,v33\nv_max3_u32 v10,v10,v34,v35\nM v[216:219],a[40:43],v[88:91],v[216:219]\n" \
"v_max3_u32 v10,v10,v36,v37\nv_max3_u32 v10,v10,v38,v39\nM v[216:219],a[48:51],v[76:79],v[216:219]\ns_waitcnt lgkmcnt(3)\nv_cmp_eq_u32 vcc,-1,v10\nv_add_f32 v48,v48,v128\nM v[216:219],a[56:59],v[84:87],v[216:219]\nv_add_f32 v52,v52,v132\n" \
"v_add_f32 v49,v49,v129\nM v[220:223],a[60:63],v[84:87],v[220:223]\ns_cbranch_vccnz LIN_%=\nLINR_%=:\nds_read_b128 v[84:87],v20 offset:52224\nv_add_f32 v50,v50,v130\nM v[220:223],a[52:55],v[76:79],v[220:223]\nds_read_b128 v[76:79],v20 offset:57344\n" \
"v_add_f32 v53,v53,v133\nM v[220:223],a[44:47],v[88:91],v[220:223]\nds_read_b128 v[88:91],v20 offset:58368\nv_add_f32 v54,v54,v134\nM v[220:223],a[36:39],v[60:63],v[220:223]\ns_waitcnt lgkmcnt(3)\nds_read_b128 v[60:63],v20 offset:59392\nv_add_f32 v51,v51,v131\n" \
"M v[208:211],a[64:67],v[92:95],v[208:211]\nv_add_f32 v55,v55,v135\nv_lshlrev_b32 v64,16,v164\nM v[208:211],a[72:75],v[72:75],v[208:211]\nv_lshlrev_b32 v68,16,v166\nv_and_b32 v65,0xffff0000,v164\nM v[208:211],a[80:83],v[80:83],v[208:211]\ns_waitcnt lgkmcnt(3)\n" \
"v_and_b32 v69,0xffff0000,v166\nv_lshlrev_b32 v66,16,v165\nM v[208:211],a[88:91],v[84:87],v[208:211]\nv_lshlrev_b32 v70,16,v167\nv_and_b32 v67,0xffff0000,v165\nM v[212:215],a[92:95],v[84:87],v[212:215]\nds_read_b128 v[84:87],v20 offset:60416\nv_and_b32 v71,0xffff0000,v167\n" \
"M v[212:215],a[84:87],v[80:83],v[212:215]\nds_read_b128 v[80:83],v20 offset:53248\nC v160,v48,v49\nM v[212:215],a[76:79],v[72:75],v[212:215]\nds_read_b128 v[72:75],v20 offset:54272\nC v162,v52,v53\nM v[212:215],a[68:71],v[92:95],v[212:215]\ns_waitcnt lgkmcnt(3)\n" \
"ds_read_b128 v[92:95],v20 offset:55296\nC v161,v50,v51\nM v[216:219],a[64:67],v[76:79],v[216:219]\nC v163,v54,v55\nv_mul_f32 v64,v25,v64\nM v[216:219],a[72:75],v[88:91],v[216:219]\nv_mul_f32 v68,v25,v68\nv_mul_f32 v65,v25,v65\n" \
"M v[216:219],a[80:83],v[60:63],v[216:219]\ns_waitcnt lgkmcnt(3)\nv_mul_f32 v69,v25,v69\nv_mul_f32 v66,v25,v66\nM v[216:219],a[88:91],v[84:87],v[216:219]\nv_mul_f32 v70,v25,v70\nv_mul_f32 v67,v25,v67\nM v[220:223],a[92:95],v[84:87],v[220:223]\n" \
"ds_read_b128 v[84:87],v20 offset:56320\nv_mul_f32 v71,v25,v71\nM v[220:223],a[84:87],v[60:63],v[220:223]\nds_read_b128 v[60:63],v20 offset:61440\nC v164,v64,v65\nM v[220:223],a[76:79],v[88:91],v[220:223]\nds_read_b128 v[88:91],v20 offset:62464\nC v166,v68,v69\n" \
"M v[220:223],a[68:71],v[76:79],v[220:223]\ns_waitcnt lgkmcnt(3)\nds_read_b128 v[76:79],v20 offset:63488\nC v165,v66,v67\nM v[208:211],a[96:99],v[80:83],v[208:211]\ns_waitcnt lgkmcnt(3)\nv_permlane16_swap_b32 v160,v162\nM v[208:211],a[104:107],v[72:75],v[208:211]\n" \
"s_waitcnt vmcnt(6) lgkmcnt(15)\nv_permlane16_swap_b32 v161,v163\nM v[208:211],a[112:115],v[92:95],v[208:211]\ns_barrier\nC v167,v70,v71\nM v[208:211],a[120:123],v[84:87],v[208:211]\nv_lshlrev_b32 v64,16,v164\nv_lshlrev_b32 v68,16,v166\n" \
"M v[212:215],a[124:127],v[84:87],v[212:215]\nds_read_b128 v[84:87],v20 offset:64512\nv_and_b32 v65,0xffff0000,v164\nM v[212:215],a[116:119],v[92:95],v[212:215]\nds_read_b128 v[92:95],v4 offset:0\nv_and_b32 v69,0xffff0000,v166\nM v[212:215],a[108:111],v[72:75],v[212:215]\nds_read_b128 v[72:75],v4 offset:1024\n" \
"v_lshlrev_b32 v66,16,v165\nM v[212:215],a[100:103],v[80:83],v[212:215]\ns_waitcnt lgkmcnt(3)\nds_read_b128 v[80:83],v4 offset:2048\nv_lshlrev_b32 v70,16,v167\nM v[216:219],a[96:99],v[60:63],v[216:219]\nv_and_b32 v67,0xffff0000,v165\nv_and_b32 v71,0xffff0000,v167\n" \
"M v[216:219],a[104:107],v[88:91],v[216:219]\nv_add_f32 v64,v64,v136\nv_add_f32 v68,v68,v140\nM v[216:219],a[112:115],v[76:79],v[216:219]\ns_waitcnt lgkmcnt(3)\nv_add_f32 v65,v65,v137\nv_add_f32 v69,v69,v141\nM v[216:219],a[120:123],v[84:87],v[216:219]\n" \
"v_add_f32 v66,v66,v138\nv_add_f32 v70,v70,v142\nM v[220:223],a[124:127],v[84:87],v[220:223]\nds_read_b128 v[84:87],v4 offset:3072\nv_add_f32 v67,v67,v139\nM v[220:223],a[116:119],v[76:79],v[220:223]\nds_read_b128 v[76:79],v4 offset:8192\nv_add_f32 v71,v71,v143\n" \
"M v[220:223],a[108:111],v[88:91],v[220:223]\nds_read_b128 v[88:91],v4 offset:9216\nC v164,v64,v65\nM v[220:223],a[100:103],v[60:63],v[220:223]\ns_waitcnt lgkmcnt(3)\nds_read_b128 v[60:63],v4 offset:10240\nC v166,v68,v69\nM v[208:211],a[128:131],v[92:95],v[208:211]\n" \
"C v165,v66,v67\nC v167,v70,v71\nM v[208:211],a[136:139],v[72:75],v[208:211]\nv_permlane16_swap_b32 v164,v166\nM v[208:211],a[144:147],v[80:83],v[208:211]\ns_waitcnt lgkmcnt(3)\nv_permlane16_swap_b32 v165,v167\nM v[208:211],a[152:155],v[84:87],v[208:211]\n" \
"M v[212:215],a[156:159],v[84:87],v[212:215]\nds_read_b128 v[84:87],v4 offset:11264\nM v[212:215],a[148:151],v[80:83],v[212:215]\nds_read_b128 v[80:83],v4 offset:4096\nM v[212:215],a[140:143],v[72:75],v[212:215]\nds_read_b128 v[72:75],v4 offset:5120\nM v[212:215],a[132:135],v[92:95],v[212:215]\ns_waitcnt lgkmcnt(3)\n" \
"ds_read_b128 v[92:95],v4 offset:6144\nM v[216:219],a[128:131],v[76:79],v[216:219]\nM v[216:219],a[136:139],v[88:91],v[216:219]\nM v[216:219],a[144:147],v[60:63],v[216:219]\ns_waitcnt lgkmcnt(3)\nM v[216:219],a[152:155],v[84:87],v[216:219]\nM v[220:223],a[156:159],v[84:87],v[220:223]\nds_read_b128 v[84:87],v4 offset:7168\n" \
"M v[220:223],a[148:151],v[60:63],v[220:223]\nds_read_b128 v[60:63],v4 offset:12288\nM v[220:223],a[140:143],v[88:91],v[220:223]\nds_read_b128 v[88:91],v4 offset:13312\nM v[220:223],a[132:135],v[76:79],v[220:223]\ns_waitcnt lgkmcnt(3)\nds_read_b128 v[76:79],v4 offset:14336\nM v[208:211],a[160:163],v[80:83],v[208:211]\n" \
"M v[208:211],a[168:171],v[72:75],v[208:211]\nM v[208:211],a[176:179],v[92:95],v[208:211]\ns_waitcnt lgkmcnt(3)\nM v[208:211],a[184:187],v[84:87],v[208:211]\nM v[212:215],a[188:191],v[84:87],v[212:215]\nds_read_b128 v[84:87],v4 offset:15360\nM v[212:215],a[180:183],v[92:95],v[212:215]\ns_add_u32 s63,s49,8192\n" \
"ds_read_b128 v[92:95],v4 offset:32768\nM v[212:215],a[172:175],v[72:75],v[212:215]\nbuffer_load_dwordx4 v[40:43],v4,s[36:39],s63 offen offset:2048 sc1\nM v[212:215],a[164:167],v[80:83],v[212:215]\ns_waitcnt lgkmcnt(2)\nds_read_b128 v[72:75],v4 offset:33792\nds_read_b128 v[80:83],v4 offset:34816\nM v[216:219],a[160:163],v[60:63],v[216:219]\n" \
"M v[216:219],a[168:171],v[88:91],v[216:219]\ns_waitcnt lgkmcnt(3)\nM v[216:219],a[176:179],v[76:79],v[216:219]\nbuffer_load_dwordx4 v[44:47],v4,s[36:39],s63 offen offset:3072 sc1\nM v[216:219],a[184:187],v[84:87],v[216:219]\nM v[220:223],a[188:191],v[84:87],v[220:223]\nds_read_b128 v[84:87],v4 offset:35840\nM v[220:223],a[180:183],v[76:79],v[220:223]\n" \
"LG26_%=:\ns_cbranch_execz LHR3_%=\ns_waitcnt lgkmcnt(1)\nds_read_b128 v[76:79],v4 offset:40960\nM v[220:223],a[172:175],v[88:91],v[220:223]\nbuffer_store_dwordx4 v[160:163],v13,s[88:91],s58 offen offset:0\nM v[220:223],a[164:167],v[60:63],v[220:223]\ns_add_u32 s58,s81,0\n" \
"ds_read_b128 v[88:91],v4 offset:41984\nds_read_b128 v[60:63],v4 offset:43008\nM v[208:211],a[192:195],v[92:95],v[208:211]\nM v[208:211],a[200:203],v[72:75],v[208:211]\nM v[208:211],a[208:211],v[80:83],v[208:211]\ns_waitcnt lgkmcnt(3)\nM v[208:211],a[216:219],v[84:87],v[208:211]\nM v[212:215],a[220:223],v[84:87],v[212:215]\n" \
"ds_read_b128 v[84:87],v4 offset:44032\nM v[212:215],a[212:215],v[80:83],v[212:215]\ns_waitcnt lgkmcnt(1)\nds_read_b128 v[80:83],v4 offset:36864\nM v[212:215],a[204:207],v[72:75],v[212:215]\nbuffer_store_dwordx4 v[164:167],v13,s[88:91],s59 offen offset:0\nM v[212:215],a[196:199],v[92:95],v[212:215]\ns_add_u32 s59,s81,65536\n" \
"ds_read_b128 v[72:75],v4 offset:37888\nds_read_b128 v[92:95],v4 offset:38912\nM v[216:219],a[192:195],v[76:79],v[216:219]\nM v[216:219],a[200:203],v[88:91],v[216:219]\nM v[216:219],a[208:211],v[60:63],v[216:219]\ns_waitcnt lgkmcnt(3)\nM v[216:219],a[216:219],v[84:87],v[216:219]\nM v[220:223],a[220:223],v[84:87],v[220:223]\n" \
"ds_read_b128 v[84:87],v4 offset:39936\nM v[220:223],a[212:215],v[60:63],v[220:223]\nds_read_b128 v[60:63],v4 offset:45056\nM v[220:223],a[204:207],v[88:91],v[220:223]\nds_read_b128 v[88:91],v4 offset:46080\nM v[220:223],a[196:199],v[76:79],v[220:223]\ns_waitcnt lgkmcnt(3)\nds_read_b128 v[76:79],v4 offset:47104\n" \
"M v[208:211],a[224:227],v[80:83],v[208:211]\nM v[208:211],a[232:235],v[72:75],v[208:211]\nM v[208:211],a[240:243],v[92:95],v[208:211]\ns_waitcnt lgkmcnt(3)\nM v[208:211],a[248:251],v[84:87],v[208:211]\nM v[212:215],a[252:255],v[84:87],v[212:215]\nds_read_b128 v[84:87],v4 offset:48128\nM v[212:215],a[244:247],v[92:95],v[212:215]\n" \
"M v[212:215],a[236:239],v[72:75],v[212:215]\nM v[212:215],a[228:231],v[80:83],v[212:215]\ns_waitcnt lgkmcnt(1)\nM v[216:219],a[224:227],v[60:63],v[216:219]\nM v[216:219],a[232:235],v[88:91],v[216:219]\nM v[216:219],a[240:243],v[76:79],v[216:219]\ns_waitcnt lgkmcnt(0)\nM v[216:219],a[248:251],v[84:87],v[216:219]\n" \
"M v[220:223],a[252:255],v[84:87],v[220:223]\nM v[220:223],a[244:247],v[76:79],v[220:223]\nM v[220:223],a[236:239],v[88:91],v[220:223]\nM v[220:223],a[228:231],v[60:63],v[220:223]\nM v[208:211],v[186:189],v[176:179],v[208:211]\nM v[212:215],v[184:187],v[176:179],v[212:215]\nM v[216:219],v[186:189],v[180:183],v[216:219]\ns_waitcnt vmcnt(2)\n" \
"v_max_u32 v10,v40,v41\nv_max3_u32 v10,v10,v42,v43\nM v[220:223],v[184:187],v[180:183],v[220:223]\nv_max3_u32 v10,v10,v44,v45\nv_max3_u32 v10,v10,v46,v47\nM v[208:211],v[186:189],v[32:35],v[208:211]\nv_cmp_eq_u32 vcc,-1,v10\nM v[212:215],v[184:187],v[32:35],v[212:215]\n" \
"M v[216:219],v[186:189],v[36:39],v[216:219]\ns_cbranch_vccnz LIO_%=\nLIOR_%=:\nM v[220:223],v[184:187],v[36:39],v[220:223]\nM v[208:211],v[186:189],v[40:43],v[208:211]\nM v[212:215],v[184:187],v[40:43],v[212:215]\nM v[216:219],v[186:189],v[44:47],v[216:219]\nC v160,v208,v209\n" \
"C v161,v210,v211\nM v[220:223],v[184:187],v[44:47],v[220:223]\nC v162,v212,v213\nC v163,v214,v215\nv_lshlrev_b32 v208,16,v160\nv_lshlrev_b32 v210,16,v161\nv_lshlrev_b32 v212,16,v162\nv_lshlrev_b32 v214,16,v163\n" \
"v_and_b32 v209,0xffff0000,v160\nv_and_b32 v211,0xffff0000,v161\nv_and_b32 v213,0xffff0000,v162\nv_and_b32 v215,0xffff0000,v163\nv_pk_mul_f32 v[208:209],v[208:209],v[26:27] op_sel_hi:[1,0]\nv_pk_mul_f32 v[210:211],v[210:211],v[26:27] op_sel_hi:[1,0]\nv_pk_mul_f32 v[212:213],v[212:213],v[26:27] op_sel_hi:[1,0]\nv_pk_mul_f32 v[214:215],v[214:215],v[26:27] op_sel_hi:[1,0]\n" \
"C v160,v208,v209\nC v161,v210,v211\nC v162,v212,v213\nC v163,v214,v215\nv_lshlrev_b32 v208,16,v160\nv_lshlrev_b32 v210,16,v161\nv_lshlrev_b32 v212,16,v162\nv_lshlrev_b32 v214,16,v163\n" \
"v_and_b32 v209,0xffff0000,v160\nv_and_b32 v211,0xffff0000,v161\nv_and_b32 v213,0xffff0000,v162\nv_and_b32 v215,0xffff0000,v163\nv_pk_add_f32 v[208:209],v[208:209],v[144:145]\nv_pk_add_f32 v[210:211],v[210:211],v[146:147]\nv_pk_add_f32 v[212:213],v[212:213],v[148:149]\nv_pk_add_f32 v[214:215],v[214:215],v[150:151]\n" \
"C v160,v208,v209\nC v161,v210,v211\nC v162,v212,v213\nC v163,v214,v215\ns_nop 0\nv_permlane16_swap_b32 v160,v162\nv_permlane16_swap_b32 v161,v163\nbuffer_store_dwordx4 v[160:163],v13,s[88:91],s58 offen offset:0\n" \
"C v164,v216,v217\nC v165,v218,v219\nC v166,v220,v221\nC v167,v222,v223\nv_lshlrev_b32 v216,16,v164\nv_lshlrev_b32 v218,16,v165\nv_lshlrev_b32 v220,16,v166\nv_lshlrev_b32 v222,16,v167\n" \
"v_and_b32 v217,0xffff0000,v164\nv_and_b32 v219,0xffff0000,v165\nv_and_b32 v221,0xffff0000,v166\nv_and_b32 v223,0xffff0000,v167\nv_pk_mul_f32 v[216:217],v[216:217],v[26:27] op_sel:[0,1] op_sel_hi:[1,1]\nv_pk_mul_f32 v[218:219],v[218:219],v[26:27] op_sel:[0,1] op_sel_hi:[1,1]\nv_pk_mul_f32 v[220:221],v[220:221],v[26:27] op_sel:[0,1] op_sel_hi:[1,1]\nv_pk_mul_f32 v[222:223],v[222:223],v[26:27] op_sel:[0,1] op_sel_hi:[1,1]\n" \
"C v164,v216,v217\nC v165,v218,v219\nC v166,v220,v221\nC v167,v222,v223\nv_lshlrev_b32 v216,16,v164\nv_lshlrev_b32 v218,16,v165\nv_lshlrev_b32 v220,16,v166\nv_lshlrev_b32 v222,16,v167\n" \
"v_and_b32 v217,0xffff0000,v164\nv_and_b32 v219,0xffff0000,v165\nv_and_b32 v221,0xffff0000,v166\nv_and_b32 v223,0xffff0000,v167\nv_pk_add_f32 v[216:217],v[216:217],v[152:153]\nv_pk_add_f32 v[218:219],v[218:219],v[154:155]\nv_pk_add_f32 v[220:221],v[220:221],v[156:157]\nv_pk_add_f32 v[222:223],v[222:223],v[158:159]\n" \
"C v164,v216,v217\nC v165,v218,v219\nC v166,v220,v221\nC v167,v222,v223\ns_nop 0\nv_permlane16_swap_b32 v164,v166\nv_permlane16_swap_b32 v165,v167\nbuffer_store_dwordx4 v[164:167],v13,s[88:91],s59 offen offset:0\n" \
"s_branch LEND_%=\n.p2align 6\nLG27_%=:\ns_cbranch_execz LHR1_%=\nLH1_%=:\nds_read_b128 a[128:131],v21 offset:8192\nds_read_b128 a[132:135],v21 offset:9216\nds_read_b128 a[136:139],v21 offset:10240\n" \
"ds_read_b128 a[140:143],v21 offset:11264\nds_read_b128 a[144:147],v21 offset:12288\nds_read_b128 a[148:151],v21 offset:13312\nds_read_b128 a[152:155],v21 offset:14336\nds_read_b128 a[156:159],v21 offset:15360\nds_read_b128 a[160:163],v21 offset:0\nds_read_b128 a[164:167],v21 offset:1024\nds_read_b128 a[168:171],v21 offset:2048\n" \
"ds_read_b128 a[172:175],v21 offset:3072\nds_read_b128 a[176:179],v21 offset:4096\nds_read_b128 a[180:183],v21 offset:5120\nds_read_b128 a[184:187],v21 offset:6144\nds_read_b128 a[188:191],v21 offset:7168\nv_max3_f32 v60,v96,v97,v98\nv_max3_f32 v61,v99,v104,v105\nv_max3_f32 v60,v60,v106,v107\n" \
"v_max3_f32 v61,v61,v112,v113\nv_max3_f32 v60,v60,v114,v115\nv_max3_f32 v61,v61,v120,v121\nv_max3_f32 v60,v60,v122,v123\nv_max3_f32 v61,v61,v100,v101\nv_max3_f32 v60,v60,v102,v103\nv_max3_f32 v61,v61,v108,v109\nv_max3_f32 v60,v60,v110,v111\n" \
"v_max3_f32 v61,v61,v116,v117\nv_max3_f32 v60,v60,v118,v119\nv_max3_f32 v61,v61,v124,v125\nv_max3_f32 v61,v61,v126,v127\nv_max_f32 v60,v60,v61\ns_mov_b32 s75,1\nv_cmp_nge_f32 vcc,0x41f00000,v60\nv_xor_b32 v13,0x2000,v13\n" \
"s_cbranch_vccz LH1X_%=\nLRS_%=:\ns_nop 7\nv_max3_f32 v60,v96,v97,v98\nv_max3_f32 v61,v99,v100,v101\nv_max3_f32 v60,v60,v102,v103\nv_max3_f32 v60,v60,v61,0\nv_mov_b32 v61,v60\n" \
"s_nop 1\nv_permlane32_swap_b32 v60,v61\nv_max_f32 v60,v60,v61\nv_mov_b32 v61,v60\ns_nop 1\nv_permlane16_swap_b32 v60,v61\nv_max_f32 v60,v60,v61\nv_exp_f32 v62,-v60\n" \
"v_sub_f32 v96,v96,v60\nv_sub_f32 v97,v97,v60\nv_sub_f32 v98,v98,v60\nv_sub_f32 v99,v99,v60\nv_sub_f32 v100,v100,v60\nv_sub_f32 v101,v101,v60\nv_sub_f32 v102,v102,v60\nv_sub_f32 v103,v103,v60\n" \
"v_sub_f32 v32,v32,v60\nv_sub_f32 v33,v33,v60\nv_sub_f32 v34,v34,v60\nv_sub_f32 v35,v35,v60\nv_mul_f32 v16,v16,v62\nv_mul_f32 v128,v128,v62\nv_mul_f32 v129,v129,v62\nv_mul_f32 v130,v130,v62\n" \
"v_mul_f32 v131,v131,v62\nv_mul_f32 v132,v132,v62\nv_mul_f32 v133,v133,v62\nv_mul_f32 v134,v134,v62\nv_mul_f32 v135,v135,v62\nv_mul_f32 v136,v136,v62\nv_mul_f32 v137,v137,v62\nv_mul_f32 v138,v138,v62\n" \
"v_mul_f32 v139,v139,v62\nv_mul_f32 v140,v140,v62\nv_mul_f32 v141,v141,v62\nv_mul_f32 v142,v142,v62\nv_mul_f32 v143,v143,v62\nv_mul_f32 v144,v144,v62\nv_mul_f32 v145,v145,v62\nv_mul_f32 v146,v146,v62\n" \
"v_mul_f32 v147,v147,v62\nv_mul_f32 v148,v148,v62\nv_mul_f32 v149,v149,v62\nv_mul_f32 v150,v150,v62\nv_mul_f32 v151,v151,v62\nv_mul_f32 v152,v152,v62\nv_mul_f32 v153,v153,v62\nv_mul_f32 v154,v154,v62\n" \
"v_mul_f32 v155,v155,v62\nv_mul_f32 v156,v156,v62\nv_mul_f32 v157,v157,v62\nv_mul_f32 v158,v158,v62\nv_mul_f32 v159,v159,v62\nv_lshlrev_b32 v61,16,v64\nv_and_b32 v63,0xffff0000,v64\nv_mul_f32 v61,v61,v62\n" \
"v_mul_f32 v63,v63,v62\nC v64,v61,v63\nv_lshlrev_b32 v61,16,v65\nv_and_b32 v63,0xffff0000,v65\nv_mul_f32 v61,v61,v62\nv_mul_f32 v63,v63,v62\nC v65,v61,v63\nv_lshlrev_b32 v61,16,v66\n" \
"v_and_b32 v63,0xffff0000,v66\nv_mul_f32 v61,v61,v62\nv_mul_f32 v63,v63,v62\nC v66,v61,v63\nv_lshlrev_b32 v61,16,v67\nv_and_b32 v63,0xffff0000,v67\nv_mul_f32 v61,v61,v62\nv_mul_f32 v63,v63,v62\n" \
"C v67,v61,v63\nv_max3_f32 v60,v104,v105,v106\nv_max3_f32 v61,v107,v108,v109\nv_max3_f32 v60,v60,v110,v111\nv_max3_f32 v60,v60,v61,0\nv_mov_b32 v61,v60\ns_nop 1\nv_permlane32_swap_b32 v60,v61\n" \
"v_max_f32 v60,v60,v61\nv_mov_b32 v61,v60\ns_nop 1\nv_permlane16_swap_b32 v60,v61\nv_max_f32 v60,v60,v61\nv_exp_f32 v62,-v60\nv_sub_f32 v104,v104,v60\nv_sub_f32 v105,v105,v60\n" \
"v_sub_f32 v106,v106,v60\nv_sub_f32 v107,v107,v60\nv_sub_f32 v108,v108,v60\nv_sub_f32 v109,v109,v60\nv_sub_f32 v110,v110,v60\nv_sub_f32 v111,v111,v60\nv_sub_f32 v36,v36,v60\nv_sub_f32 v37,v37,v60\n" \
"v_sub_f32 v38,v38,v60\nv_sub_f32 v39,v39,v60\nv_mul_f32 v17,v17,v62\nv_mul_f32 v160,v160,v62\nv_mul_f32 v161,v161,v62\nv_mul_f32 v162,v162,v62\nv_mul_f32 v163,v163,v62\nv_mul_f32 v164,v164,v62\n" \
"v_mul_f32 v165,v165,v62\nv_mul_f32 v166,v166,v62\nv_mul_f32 v167,v167,v62\nv_mul_f32 v168,v168,v62\nv_mul_f32 v169,v169,v62\nv_mul_f32 v170,v170,v62\nv_mul_f32 v171,v171,v62\nv_mul_f32 v172,v172,v62\n" \
"v_mul_f32 v173,v173,v62\nv_mul_f32 v174,v174,v62\nv_mul_f32 v175,v175,v62\nv_mul_f32 v176,v176,v62\nv_mul_f32 v177,v177,v62\nv_mul_f32 v178,v178,v62\nv_mul_f32 v179,v179,v62\nv_mul_f32 v180,v180,v62\n" \
"v_mul_f32 v181,v181,v62\nv_mul_f32 v182,v182,v62\nv_mul_f32 v183,v183,v62\nv_mul_f32 v184,v184,v62\nv_mul_f32 v185,v185,v62\nv_mul_f32 v186,v186,v62\nv_mul_f32 v187,v187,v62\nv_mul_f32 v188,v188,v62\n" \
"v_mul_f32 v189,v189,v62\nv_mul_f32 v190,v190,v62\nv_mul_f32 v191,v191,v62\nv_lshlrev_b32 v61,16,v72\nv_and_b32 v63,0xffff0000,v72\nv_mul_f32 v61,v61,v62\nv_mul_f32 v63,v63,v62\nC v72,v61,v63\n" \
"v_lshlrev_b32 v61,16,v73\nv_and_b32 v63,0xffff0000,v73\nv_mul_f32 v61,v61,v62\nv_mul_f32 v63,v63,v62\nC v73,v61,v63\nv_lshlrev_b32 v61,16,v74\nv_and_b32 v63,0xffff0000,v74\nv_mul_f32 v61,v61,v62\n" \
"v_mul_f32 v63,v63,v62\nC v74,v61,v63\nv_lshlrev_b32 v61,16,v75\nv_and_b32 v63,0xffff0000,v75\nv_mul_f32 v61,v61,v62\nv_mul_f32 v63,v63,v62\nC v75,v61,v63\nv_max3_f32 v60,v112,v113,v114\n" \
"v_max3_f32 v61,v115,v116,v117\nv_max3_f32 v60,v60,v118,v119\nv_max3_f32 v60,v60,v61,0\nv_mov_b32 v61,v60\ns_nop 1\nv_permlane32_swap_b32 v60,v61\nv_max_f32 v60,v60,v61\nv_mov_b32 v61,v60\n" \
"s_nop 1\nv_permlane16_swap_b32 v60,v61\nv_max_f32 v60,v60,v61\nv_exp_f32 v62,-v60\nv_sub_f32 v112,v112,v60\nv_sub_f32 v113,v113,v60\nv_sub_f32 v114,v114,v60\nv_sub_f32 v115,v115,v60\n" \
"v_sub_f32 v116,v116,v60\nv_sub_f32 v117,v117,v60\nv_sub_f32 v118,v118,v60\nv_sub_f32 v119,v119,v60\nv_sub_f32 v40,v40,v60\nv_sub_f32 v41,v41,v60\nv_sub_f32 v42,v42,v60\nv_sub_f32 v43,v43,v60\n" \
"v_mul_f32 v18,v18,v62\nv_mul_f32 v192,v192,v62\nv_mul_f32 v193,v193,v62\nv_mul_f32 v194,v194,v62\nv_mul_f32 v195,v195,v62\nv_mul_f32 v196,v196,v62\nv_mul_f32 v197,v197,v62\nv_mul_f32 v198,v198,v62\n" \
"v_mul_f32 v199,v199,v62\nv_mul_f32 v200,v200,v62\nv_mul_f32 v201,v201,v62\nv_mul_f32 v202,v202,v62\nv_mul_f32 v203,v203,v62\nv_mul_f32 v204,v204,v62\nv_mul_f32 v205,v205,v62\nv_mul_f32 v206,v206,v62\n" \
"v_mul_f32 v207,v207,v62\nv_mul_f32 v208,v208,v62\nv_mul_f32 v209,v209,v62\nv_mul_f32 v210,v210,v62\nv_mul_f32 v211,v211,v62\nv_mul_f32 v212,v212,v62\nv_mul_f32 v213,v213,v62\nv_mul_f32 v214,v214,v62\n" \
"v_mul_f32 v215,v215,v62\nv_mul_f32 v216,v216,v62\nv_mul_f32 v217,v217,v62\nv_mul_f32 v218,v218,v62\nv_mul_f32 v219,v219,v62\nv_mul_f32 v220,v220,v62\nv_mul_f32 v221,v221,v62\nv_mul_f32 v222,v222,v62\n" \
"v_mul_f32 v223,v223,v62\nv_lshlrev_b32 v61,16,v80\nv_and_b32 v63,0xffff0000,v80\nv_mul_f32 v61,v61,v62\nv_mul_f32 v63,v63,v62\nC v80,v61,v63\nv_lshlrev_b32 v61,16,v81\nv_and_b32 v63,0xffff0000,v81\n" \
"v_mul_f32 v61,v61,v62\nv_mul_f32 v63,v63,v62\nC v81,v61,v63\nv_lshlrev_b32 v61,16,v82\nv_and_b32 v63,0xffff0000,v82\nv_mul_f32 v61,v61,v62\nv_mul_f32 v63,v63,v62\nC v82,v61,v63\n" \
"v_lshlrev_b32 v61,16,v83\nv_and_b32 v63,0xffff0000,v83\nv_mul_f32 v61,v61,v62\nv_mul_f32 v63,v63,v62\nC v83,v61,v63\nv_max3_f32 v60,v120,v121,v122\nv_max3_f32 v61,v123,v124,v125\nv_max3_f32 v60,v60,v126,v127\n" \
"v_max3_f32 v60,v60,v61,0\nv_mov_b32 v61,v60\ns_nop 1\nv_permlane32_swap_b32 v60,v61\nv_max_f32 v60,v60,v61\nv_mov_b32 v61,v60\ns_nop 1\nv_permlane16_swap_b32 v60,v61\n" \
"v_max_f32 v60,v60,v61\nv_exp_f32 v62,-v60\nv_sub_f32 v120,v120,v60\nv_sub_f32 v121,v121,v60\nv_sub_f32 v122,v122,v60\nv_sub_f32 v123,v123,v60\nv_sub_f32 v124,v124,v60\nv_sub_f32 v125,v125,v60\n" \
"v_sub_f32 v126,v126,v60\nv_sub_f32 v127,v127,v60\nv_sub_f32 v44,v44,v60\nv_sub_f32 v45,v45,v60\nv_sub_f32 v46,v46,v60\nv_sub_f32 v47,v47,v60\nv_mul_f32 v19,v19,v62\nv_mul_f32 v224,v224,v62\n" \
"v_mul_f32 v225,v225,v62\nv_mul_f32 v226,v226,v62\nv_mul_f32 v227,v227,v62\nv_mul_f32 v228,v228,v62\nv_mul_f32 v229,v229,v62\nv_mul_f32 v230,v230,v62\nv_mul_f32 v231,v231,v62\nv_mul_f32 v232,v232,v62\n" \
"v_mul_f32 v233,v233,v62\nv_mul_f32 v234,v234,v62\nv_mul_f32 v235,v235,v62\nv_mul_f32 v236,v236,v62\nv_mul_f32 v237,v237,v62\nv_mul_f32 v238,v238,v62\nv_mul_f32 v239,v239,v62\nv_mul_f32 v240,v240,v62\n" \
"v_mul_f32 v241,v241,v62\nv_mul_f32 v242,v242,v62\nv_mul_f32 v243,v243,v62\nv_mul_f32 v244,v244,v62\nv_mul_f32 v245,v245,v62\nv_mul_f32 v246,v246,v62\nv_mul_f32 v247,v247,v62\nv_mul_f32 v248,v248,v62\n" \
"v_mul_f32 v249,v249,v62\nv_mul_f32 v250,v250,v62\nv_mul_f32 v251,v251,v62\nv_mul_f32 v252,v252,v62\nv_mul_f32 v253,v253,v62\nv_mul_f32 v254,v254,v62\nv_mul_f32 v255,v255,v62\nv_lshlrev_b32 v61,16,v88\n" \
"v_and_b32 v63,0xffff0000,v88\nv_mul_f32 v61,v61,v62\nv_mul_f32 v63,v63,v62\nC v88,v61,v63\nv_lshlrev_b32 v61,16,v89\nv_and_b32 v63,0xffff0000,v89\nv_mul_f32 v61,v61,v62\nv_mul_f32 v63,v63,v62\n" \
"C v89,v61,v63\nv_lshlrev_b32 v61,16,v90\nv_and_b32 v63,0xffff0000,v90\nv_mul_f32 v61,v61,v62\nv_mul_f32 v63,v63,v62\nC v90,v61,v63\nv_lshlrev_b32 v61,16,v91\nv_and_b32 v63,0xffff0000,v91\n" \
"v_mul_f32 v61,v61,v62\nv_mul_f32 v63,v63,v62\nC v91,v61,v63\ns_cmp_eq_u32 s52,14\ns_cbranch_scc1 LH1R15_%=\nLH1X_%=:\nv_pk_mov_b32 v[32:33],v[28:29],v[28:29] op_sel:[0,0]\nv_pk_mov_b32 v[34:35],v[28:29],v[28:29] op_sel:[0,0]\n" \
"v_pk_mov_b32 v[36:37],v[28:29],v[28:29] op_sel:[1,1]\nv_pk_mov_b32 v[38:39],v[28:29],v[28:29] op_sel:[1,1]\nv_pk_mov_b32 v[40:41],v[30:31],v[30:31] op_sel:[0,0]\nv_pk_mov_b32 v[42:43],v[30:31],v[30:31] op_sel:[0,0]\nv_pk_mov_b32 v[44:45],v[30:31],v[30:31] op_sel:[1,1]\nv_pk_mov_b32 v[46:47],v[30:31],v[30:31] op_sel:[1,1]\ns_cmp_eq_u32 s52,-1\ns_cbranch_scc1 LH1R0_%=\n" \
"s_cmp_eq_u32 s52,0\ns_cbranch_scc1 LH1R1_%=\ns_cmp_eq_u32 s52,9\ns_cbranch_scc1 LH1R10_%=\ns_cmp_eq_u32 s52,11\ns_cbranch_scc1 LH1R12_%=\ns_cmp_eq_u32 s52,13\ns_cbranch_scc1 LH1R14_%=\n" \
"s_branch LH1R2_%=\n.p2align 6\nLG28_%=:\ns_cbranch_execz LHR1_%=\nLEV_%=:\ns_cmp_eq_u32 s52,s4\ns_cbranch_scc0 LEV3_%=\ns_cmp_lt_u32 %2,2\n" \
"s_cbranch_scc1 LEV2_%=\ns_mov_b64 exec,0\ns_cmp_eq_u32 %2,2\ns_cbranch_scc1 LG20_%=\ns_cmp_eq_u32 %2,3\ns_cbranch_scc1 LG21_%=\nLHR3_%=:\ns_mov_b64 exec,-1\n" \
"s_mov_b32 s4,50\ns_mov_b32 s56,50\ns_branch LEV4_%=\nLEV2_%=:\nv_rcp_f32 v60,v24\nv_rcp_f32 v61,v25\nv_rcp_f32 v62,v26\nv_rcp_f32 v63,v27\n" \
"v_xor_b32 v14,0x2000,v14\ns_mov_b32 s75,0\ns_mov_b32 s77,s68\nv_mov_b32 v24,0\nv_mov_b32 v25,0\nv_mov_b32 v26,0\nv_mov_b32 v27,0\nv_pk_mul_f32 v[128:129],v[128:129],v[60:61] op_sel_hi:[1,0]\n" \
"v_pk_mul_f32 v[130:131],v[130:131],v[60:61] op_sel_hi:[1,0]\nv_pk_mul_f32 v[132:133],v[132:133],v[60:61] op_sel_hi:[1,0]\nv_pk_mul_f32 v[134:135],v[134:135],v[60:61] op_sel_hi:[1,0]\nv_pk_mul_f32 v[136:137],v[136:137],v[60:61] op_sel_hi:[1,0]\nv_pk_mul_f32 v[138:139],v[138:139],v[60:61] op_sel_hi:[1,0]\nv_pk_mul_f32 v[140:141],v[140:141],v[60:61] op_sel_hi:[1,0]\nv_pk_mul_f32 v[142:143],v[142:143],v[60:61] op_sel_hi:[1,0]\nv_pk_mul_f32 v[144:145],v[144:145],v[60:61] op_sel_hi:[1,0]\n" \
"v_pk_mul_f32 v[146:147],v[146:147],v[60:61] op_sel_hi:[1,0]\nv_pk_mul_f32 v[148:149],v[148:149],v[60:61] op_sel_hi:[1,0]\nv_pk_mul_f32 v[150:151],v[150:151],v[60:61] op_sel_hi:[1,0]\nv_pk_mul_f32 v[152:153],v[152:153],v[60:61] op_sel_hi:[1,0]\nv_pk_mul_f32 v[154:155],v[154:155],v[60:61] op_sel_hi:[1,0]\nv_pk_mul_f32 v[156:157],v[156:157],v[60:61] op_sel_hi:[1,0]\nv_pk_mul_f32 v[158:159],v[158:159],v[60:61] op_sel_hi:[1,0]\nv_pk_mul_f32 v[160:161],v[160:161],v[60:61] op_sel:[0,1] op_sel_hi:[1,1]\n" \
"v_pk_mul_f32 v[162:163],v[162:163],v[60:61] op_sel:[0,1] op_sel_hi:[1,1]\nv_pk_mul_f32 v[164:165],v[164:165],v[60:61] op_sel:[0,1] op_sel_hi:[1,1]\nv_pk_mul_f32 v[166:167],v[166:167],v[60:61] op_sel:[0,1] op_sel_hi:[1,1]\nv_pk_mul_f32 v[168:169],v[168:169],v[60:61] op_sel:[0,1] op_sel_hi:[1,1]\nv_pk_mul_f32 v[170:171],v[170:171],v[60:61] op_sel:[0,1] op_sel_hi:[1,1]\nv_pk_mul_f32 v[172:173],v[172:173],v[60:61] op_sel:[0,1] op_sel_hi:[1,1]\nv_pk_mul_f32 v[174:175],v[174:175],v[60:61] op_sel:[0,1] op_sel_hi:[1,1]\nv_pk_mul_f32 v[176:177],v[176:177],v[60:61] op_sel:[0,1] op_sel_hi:[1,1]\n" \
"v_pk_mul_f32 v[178:179],v[178:179],v[60:61] op_sel:[0,1] op_sel_hi:[1,1]\nv_pk_mul_f32 v[180:181],v[180:181],v[60:61] op_sel:[0,1] op_sel_hi:[1,1]\nv_pk_mul_f32 v[182:183],v[182:183],v[60:61] op_sel:[0,1] op_sel_hi:[1,1]\nv_pk_mul_f32 v[184:185],v[184:185],v[60:61] op_sel:[0,1] op_sel_hi:[1,1]\nv_pk_mul_f32 v[186:187],v[186:187],v[60:61] op_sel:[0,1] op_sel_hi:[1,1]\nv_pk_mul_f32 v[188:189],v[188:189],v[60:61] op_sel:[0,1] op_sel_hi:[1,1]\nv_pk_mul_f32 v[190:191],v[190:191],v[60:61] op_sel:[0,1] op_sel_hi:[1,1]\nv_pk_mul_f32 v[192:193],v[192:193],v[62:63] op_sel_hi:[1,0]\n" \
"v_pk_mul_f32 v[194:195],v[194:195],v[62:63] op_sel_hi:[1,0]\nv_pk_mul_f32 v[196:197],v[196:197],v[62:63] op_sel_hi:[1,0]\nv_pk_mul_f32 v[198:199],v[198:199],v[62:63] op_sel_hi:[1,0]\nv_pk_mul_f32 v[200:201],v[200:201],v[62:63] op_sel_hi:[1,0]\nv_pk_mul_f32 v[202:203],v[202:203],v[62:63] op_sel_hi:[1,0]\nv_pk_mul_f32 v[204:205],v[204:205],v[62:63] op_sel_hi:[1,0]\nv_pk_mul_f32 v[206:207],v[206:207],v[62:63] op_sel_hi:[1,0]\nv_pk_mul_f32 v[208:209],v[208:209],v[62:63] op_sel_hi:[1,0]\n" \
"v_pk_mul_f32 v[210:211],v[210:211],v[62:63] op_sel_hi:[1,0]\nv_pk_mul_f32 v[212:213],v[212:213],v[62:63] op_sel_hi:[1,0]\nv_pk_mul_f32 v[214:215],v[214:215],v[62:63] op_sel_hi:[1,0]\nv_pk_mul_f32 v[216:217],v[216:217],v[62:63] op_sel_hi:[1,0]\nv_pk_mul_f32 v[218:219],v[218:219],v[62:63] op_sel_hi:[1,0]\nv_pk_mul_f32 v[220:221],v[220:221],v[62:63] op_sel_hi:[1,0]\nv_pk_mul_f32 v[222:223],v[222:223],v[62:63] op_sel_hi:[1,0]\nv_pk_mul_f32 v[224:225],v[224:225],v[62:63] op_sel:[0,1] op_sel_hi:[1,1]\n" \
"v_pk_mul_f32 v[226:227],v[226:227],v[62:63] op_sel:[0,1] op_sel_hi:[1,1]\nv_pk_mul_f32 v[228:229],v[228:229],v[62:63] op_sel:[0,1] op_sel_hi:[1,1]\nv_pk_mul_f32 v[230:231],v[230:231],v[62:63] op_sel:[0,1] op_sel_hi:[1,1]\nv_pk_mul_f32 v[232:233],v[232:233],v[62:63] op_sel:[0,1] op_sel_hi:[1,1]\nv_pk_mul_f32 v[234:235],v[234:235],v[62:63] op_sel:[0,1] op_sel_hi:[1,1]\nv_pk_mul_f32 v[236:237],v[236:237],v[62:63] op_sel:[0,1] op_sel_hi:[1,1]\nv_pk_mul_f32 v[238:239],v[238:239],v[62:63] op_sel:[0,1] op_sel_hi:[1,1]\nv_pk_mul_f32 v[240:241],v[240:241],v[62:63] op_sel:[0,1] op_sel_hi:[1,1]\n" \
"v_pk_mul_f32 v[242:243],v[242:243],v[62:63] op_sel:[0,1] op_sel_hi:[1,1]\nv_pk_mul_f32 v[244:245],v[244:245],v[62:63] op_sel:[0,1] op_sel_hi:[1,1]\nv_pk_mul_f32 v[246:247],v[246:247],v[62:63] op_sel:[0,1] op_sel_hi:[1,1]\nv_pk_mul_f32 v[248:249],v[248:249],v[62:63] op_sel:[0,1] op_sel_hi:[1,1]\nv_pk_mul_f32 v[250:251],v[250:251],v[62:63] op_sel:[0,1] op_sel_hi:[1,1]\nv_pk_mul_f32 v[252:253],v[252:253],v[62:63] op_sel:[0,1] op_sel_hi:[1,1]\nv_pk_mul_f32 v[254:255],v[254:255],v[62:63] op_sel:[0,1] op_sel_hi:[1,1]\nC v128,v128,v129\n" \
"C v129,v130,v131\nC v130,v132,v133\nC v131,v134,v135\nbuffer_store_dwordx4 v[128:131],v4,s[36:39],s77 offen offset:0 sc1\nC v136,v136,v137\nC v137,v138,v139\nC v138,v140,v141\nC v139,v142,v143\n" \
"buffer_store_dwordx4 v[136:139],v4,s[36:39],s77 offen offset:1024 sc1\nC v144,v144,v145\nC v145,v146,v147\nC v146,v148,v149\nC v147,v150,v151\nv_mfma_f32_32x32x16_bf16 v[128:143],v[24:27],v[24:27],0\nbuffer_store_dwordx4 v[144:147],v4,s[36:39],s77 offen offset:2048 sc1\nC v152,v152,v153\n" \
"C v153,v154,v155\nC v154,v156,v157\nC v155,v158,v159\nbuffer_store_dwordx4 v[152:155],v4,s[36:39],s77 offen offset:3072 sc1\ns_add_u32 s77,s77,262144\nC v160,v160,v161\nC v161,v162,v163\nC v162,v164,v165\n" \
"C v163,v166,v167\nv_mfma_f32_32x32x16_bf16 v[144:159],v[24:27],v[24:27],0\nbuffer_store_dwordx4 v[160:163],v4,s[36:39],s77 offen offset:0 sc1\nC v168,v168,v169\nC v169,v170,v171\nC v170,v172,v173\nC v171,v174,v175\nbuffer_store_dwordx4 v[168:171],v4,s[36:39],s77 offen offset:1024 sc1\n" \
"C v176,v176,v177\nC v177,v178,v179\nC v178,v180,v181\nC v179,v182,v183\nv_mfma_f32_32x32x16_bf16 v[160:175],v[24:27],v[24:27],0\nbuffer_store_dwordx4 v[176:179],v4,s[36:39],s77 offen offset:2048 sc1\nC v184,v184,v185\nC v185,v186,v187\n" \
"C v186,v188,v189\nC v187,v190,v191\nbuffer_store_dwordx4 v[184:187],v4,s[36:39],s77 offen offset:3072 sc1\ns_add_u32 s77,s77,262144\nC v192,v192,v193\nC v193,v194,v195\nC v194,v196,v197\nC v195,v198,v199\n" \
"v_mfma_f32_32x32x16_bf16 v[176:191],v[24:27],v[24:27],0\nbuffer_store_dwordx4 v[192:195],v4,s[36:39],s77 offen offset:0 sc1\nC v200,v200,v201\nC v201,v202,v203\nC v202,v204,v205\nC v203,v206,v207\nbuffer_store_dwordx4 v[200:203],v4,s[36:39],s77 offen offset:1024 sc1\nC v208,v208,v209\n" \
"C v209,v210,v211\nC v210,v212,v213\nC v211,v214,v215\nv_mfma_f32_32x32x16_bf16 v[192:207],v[24:27],v[24:27],0\nbuffer_store_dwordx4 v[208:211],v4,s[36:39],s77 offen offset:2048 sc1\nC v216,v216,v217\nC v217,v218,v219\nC v218,v220,v221\n" \
"C v219,v222,v223\nbuffer_store_dwordx4 v[216:219],v4,s[36:39],s77 offen offset:3072 sc1\ns_add_u32 s77,s77,262144\nC v224,v224,v225\nC v225,v226,v227\nC v226,v228,v229\nC v227,v230,v231\nv_mfma_f32_32x32x16_bf16 v[208:223],v[24:27],v[24:27],0\n" \
"buffer_store_dwordx4 v[224:227],v4,s[36:39],s77 offen offset:0 sc1\nC v232,v232,v233\nC v233,v234,v235\nC v234,v236,v237\nC v235,v238,v239\nbuffer_store_dwordx4 v[232:235],v4,s[36:39],s77 offen offset:1024 sc1\nC v240,v240,v241\nC v241,v242,v243\n" \
"C v242,v244,v245\nC v243,v246,v247\nv_mfma_f32_32x32x16_bf16 v[224:239],v[24:27],v[24:27],0\nbuffer_store_dwordx4 v[240:243],v4,s[36:39],s77 offen offset:2048 sc1\nC v248,v248,v249\nC v249,v250,v251\nC v250,v252,v253\nC v251,v254,v255\n" \
"buffer_store_dwordx4 v[248:251],v4,s[36:39],s77 offen offset:3072 sc1\ns_add_u32 s77,s77,262144\ns_nop 0\nv_mfma_f32_32x32x16_bf16 v[240:255],v[24:27],v[24:27],0\ns_add_u32 s56,s4,1\ns_branch LEV4_%=\nLEV3_%=:\ns_mov_b32 s56,s4\n" \
"s_cmp_lt_u32 s52,s4\ns_cbranch_scc1 LEV4_%=\ns_mov_b32 s56,50\ns_add_u32 s77,s4,1\ns_cmp_eq_u32 s52,s77\ns_cbranch_scc0 LEV4_%=\nv_lshrrev_b32 v62,5,v4\nv_mov_b32 v60,s10\n" \
"v_lshlrev_b32 v62,27,v62\nv_mov_b32 v61,s11\nv_lshl_add_u32 v62,v4,5,v62\ns_waitcnt vmcnt(16)\nbuffer_store_dwordx2 v[60:61],v62,s[36:39],s69 offen offset:0 sc1\nLEV4_%=:\ns_cmp_lt_u32 s52,8\ns_cbranch_scc0 LEV5_%=\n" \
"s_min_u32 s56,s56,8\nLEV5_%=:\ns_cmp_eq_u32 s52,8\ns_cbranch_scc0 LEV6_%=\ns_mov_b64 exec,0\ns_cmp_eq_u32 %2,0\ns_cbranch_scc1 LG16_%=\ns_cmp_eq_u32 %2,1\n" \
"s_cbranch_scc1 LG17_%=\ns_cmp_eq_u32 %2,2\ns_cbranch_scc1 LG18_%=\ns_cmp_eq_u32 %2,3\ns_cbranch_scc1 LG19_%=\nLHR2_%=:\ns_mov_b64 exec,-1\nLEV6_%=:\n" \
"s_cmp_eq_u32 s52,0\ns_cbranch_scc1 LEVR0_%=\ns_cmp_eq_u32 s52,8\ns_cbranch_scc1 LEVR8_%=\ns_cmp_eq_u32 s52,9\ns_cbranch_scc1 LEVR9_%=\ns_cmp_eq_u32 s52,10\ns_cbranch_scc1 LEVR10_%=\n" \
"s_cmp_eq_u32 s52,11\ns_cbranch_scc1 LEVR11_%=\ns_cmp_eq_u32 s52,12\ns_cbranch_scc1 LEVR12_%=\ns_cmp_eq_u32 s52,13\ns_cbranch_scc1 LEVR13_%=\ns_cmp_eq_u32 s52,14\ns_cbranch_scc1 LEVR14_%=\n" \
"s_cmp_eq_u32 s52,15\ns_cbranch_scc1 LEVR15_%=\ns_branch LEVR1_%=\nLcs1_%=:\ns_add_u32 s9,s5,2\ns_mov_b32 s8,3\ns_mov_b32 s64,0\ns_branch LSLOW1_%=\n" \
"Lcs3_%=:\ns_add_u32 s9,s5,1\ns_mov_b32 s8,2\ns_mov_b32 s64,0\ns_branch LSLOW0_%=\nLcs5_%=:\ns_add_u32 s9,s5,0\ns_mov_b32 s8,1\n" \
"s_mov_b32 s64,1\ns_branch LSLOW1_%=\nLcs7_%=:\ns_lshl_b32 s9,s52,1\ns_add_u32 s9,s9,0\ns_sub_u32 s8,59,s9\ns_cmp_ge_u32 s9,s70\ns_cselect_b32 s9,s8,s9\n" \
"s_mov_b32 s8,0\ns_mov_b32 s64,1\ns_branch LSLOW0_%=\nLcs9_%=:\ns_lshl_b32 s9,s52,1\ns_add_u32 s9,s9,1\ns_sub_u32 s8,59,s9\ns_cmp_ge_u32 s9,s70\n" \
"s_cselect_b32 s9,s8,s9\ns_mov_b32 s8,0\ns_mov_b32 s64,2\ns_branch LSLOW1_%=\nLcs11_%=:\ns_lshl_b32 s9,s52,1\ns_add_u32 s9,s9,0\ns_sub_u32 s8,59,s9\n" \
"s_cmp_ge_u32 s9,s70\ns_cselect_b32 s9,s8,s9\ns_mov_b32 s8,0\ns_mov_b32 s64,2\ns_branch LSLOW0_%=\nLcs13_%=:\ns_lshl_b32 s9,s52,1\ns_add_u32 s9,s9,1\n" \
"s_sub_u32 s8,59,s9\ns_cmp_ge_u32 s9,s70\ns_cselect_b32 s9,s8,s9\ns_mov_b32 s8,0\ns_mov_b32 s64,3\ns_branch LSLOW1_%=\nLcs39_%=:\ns_lshl_b32 s9,s52,1\n" \
"s_add_u32 s9,s9,0\ns_sub_u32 s8,59,s9\ns_cmp_ge_u32 s9,s70\ns_cselect_b32 s9,s8,s9\ns_mov_b32 s8,0\ns_mov_b32 s64,3\ns_branch LSLOW0_%=\nLcs41_%=:\n" \
"s_lshl_b32 s9,s52,1\ns_add_u32 s9,s9,1\ns_sub_u32 s8,59,s9\ns_cmp_ge_u32 s9,s70\ns_cselect_b32 s9,s8,s9\ns_mov_b32 s8,0\ns_mov_b32 s64,4\ns_branch LSLOW1_%=\n" \
"Lcs43_%=:\ns_lshl_b32 s9,s52,1\ns_add_u32 s9,s9,0\ns_sub_u32 s8,59,s9\ns_cmp_ge_u32 s9,s70\ns_cselect_b32 s9,s8,s9\ns_mov_b32 s8,0\ns_mov_b32 s64,4\n" \
"s_branch LSLOW0_%=\nLcs45_%=:\ns_lshl_b32 s9,s52,1\ns_add_u32 s9,s9,1\ns_sub_u32 s8,59,s9\ns_cmp_ge_u32 s9,s70\ns_cselect_b32 s9,s8,s9\ns_mov_b32 s8,0\n" \
"s_mov_b32 s64,5\ns_branch LSLOW1_%=\nLcs47_%=:\ns_lshl_b32 s9,s52,1\ns_add_u32 s9,s9,0\ns_sub_u32 s8,59,s9\ns_cmp_ge_u32 s9,s70\ns_cselect_b32 s9,s8,s9\n" \
"s_mov_b32 s8,0\ns_mov_b32 s64,5\ns_branch LSLOW0_%=\nLcs49_%=:\ns_lshl_b32 s9,s52,1\ns_add_u32 s9,s9,1\ns_sub_u32 s8,59,s9\ns_cmp_ge_u32 s9,s70\n" \
"s_cselect_b32 s9,s8,s9\ns_mov_b32 s8,0\ns_mov_b32 s64,6\ns_branch LSLOW1_%=\nLcs51_%=:\ns_lshl_b32 s9,s52,1\ns_add_u32 s9,s9,0\ns_sub_u32 s8,59,s9\n" \
"s_cmp_ge_u32 s9,s70\ns_cselect_b32 s9,s8,s9\ns_mov_b32 s8,0\ns_mov_b32 s64,6\ns_branch LSLOW0_%=\nLcs53_%=:\ns_lshl_b32 s9,s52,1\ns_add_u32 s9,s9,1\n" \
"s_sub_u32 s8,59,s9\ns_cmp_ge_u32 s9,s70\ns_cselect_b32 s9,s8,s9\ns_mov_b32 s8,0\ns_mov_b32 s64,7\ns_branch LSLOW1_%=\nLcs55_%=:\ns_lshl_b32 s9,s52,1\n" \
"s_add_u32 s9,s9,0\ns_sub_u32 s8,59,s9\ns_cmp_ge_u32 s9,s70\ns_cselect_b32 s9,s8,s9\ns_mov_b32 s8,0\ns_mov_b32 s64,7\ns_branch LSLOW0_%=\nLcs57_%=:\n" \
"s_lshl_b32 s9,s52,1\ns_add_u32 s9,s9,1\ns_sub_u32 s8,59,s9\ns_cmp_ge_u32 s9,s70\ns_cselect_b32 s9,s8,s9\ns_mov_b32 s8,0\ns_mov_b32 s64,8\ns_branch LSLOW1_%=\n" \
"Lcs59_%=:\ns_lshl_b32 s9,s52,1\ns_add_u32 s9,s9,0\ns_sub_u32 s8,59,s9\ns_cmp_ge_u32 s9,s70\ns_cselect_b32 s9,s8,s9\ns_mov_b32 s8,0\ns_mov_b32 s64,8\n" \
"s_branch LSLOW0_%=\nLcs61_%=:\ns_lshl_b32 s9,s52,1\ns_add_u32 s9,s9,1\ns_sub_u32 s8,59,s9\ns_cmp_ge_u32 s9,s70\ns_cselect_b32 s9,s8,s9\ns_mov_b32 s8,0\n" \
"s_mov_b32 s64,9\ns_branch LSLOW1_%=\nLcs63_%=:\ns_lshl_b32 s9,s52,1\ns_add_u32 s9,s9,0\ns_sub_u32 s8,59,s9\ns_cmp_ge_u32 s9,s70\ns_cselect_b32 s9,s8,s9\n" \
"s_mov_b32 s8,0\ns_mov_b32 s64,9\ns_branch LSLOW0_%=\nLSLOW0_%=:\ns_lshl_b32 s9,s9,14\ns_add_u32 s9,s9,s71\ns_nop 7\ns_waitcnt lgkmcnt(0)\n" \
"buffer_load_dwordx4 a[240:243],v4,s[36:39],s9 offen offset:0 sc1\nbuffer_load_dwordx4 a[244:247],v4,s[36:39],s9 offen offset:2048 sc1\ns_add_u32 s9,s9,4096\nbuffer_load_dwordx4 a[248:251],v4,s[36:39],s9 offen offset:0 sc1\nbuffer_load_dwordx4 a[252:255],v4,s[36:39],s9 offen offset:2048 sc1\ns_sub_u32 s9,s9,3072\ns_waitcnt vmcnt(0)\ns_cmp_gt_u32 s8,0\n" \
"s_cbranch_scc1 Lk65_%=\nM v[64:67],a[240:243],a[128:131],v[32:35]\nM v[64:67],a[244:247],a[132:135],v[64:67]\nM v[64:67],a[248:251],a[136:139],v[64:67]\nM v[64:67],a[252:255],a[140:143],v[64:67]\nLk65_%=:\ns_cmp_gt_u32 s8,1\ns_cbranch_scc1 Lk66_%=\n" \
"M v[72:75],a[240:243],a[144:147],v[36:39]\nM v[72:75],a[244:247],a[148:151],v[72:75]\nM v[72:75],a[248:251],a[152:155],v[72:75]\nM v[72:75],a[252:255],a[156:159],v[72:75]\nLk66_%=:\ns_cmp_gt_u32 s8,2\ns_cbranch_scc1 Lk67_%=\nM v[80:83],a[240:243],a[160:163],v[40:43]\n" \
"M v[80:83],a[244:247],a[164:167],v[80:83]\nM v[80:83],a[248:251],a[168:171],v[80:83]\nM v[80:83],a[252:255],a[172:175],v[80:83]\nLk67_%=:\ns_cmp_gt_u32 s8,3\ns_cbranch_scc1 Lk68_%=\nM v[88:91],a[240:243],a[176:179],v[44:47]\nM v[88:91],a[244:247],a[180:183],v[88:91]\n" \
"M v[88:91],a[248:251],a[184:187],v[88:91]\nM v[88:91],a[252:255],a[188:191],v[88:91]\nLk68_%=:\ns_nop 7\nbuffer_load_dwordx4 a[240:243],v4,s[36:39],s9 offen offset:0 sc1\nbuffer_load_dwordx4 a[244:247],v4,s[36:39],s9 offen offset:2048 sc1\ns_add_u32 s9,s9,4096\nbuffer_load_dwordx4 a[248:251],v4,s[36:39],s9 offen offset:0 sc1\n" \
"buffer_load_dwordx4 a[252:255],v4,s[36:39],s9 offen offset:2048 sc1\ns_sub_u32 s9,s9,3072\ns_waitcnt vmcnt(0)\ns_cmp_gt_u32 s8,0\ns_cbranch_scc1 Lk69_%=\nM v[68:71],a[240:243],a[128:131],v[32:35]\nM v[68:71],a[244:247],a[132:135],v[68:71]\nM v[68:71],a[248:251],a[136:139],v[68:71]\n" \
"M v[68:71],a[252:255],a[140:143],v[68:71]\nLk69_%=:\ns_cmp_gt_u32 s8,1\ns_cbranch_scc1 Lk70_%=\nM v[76:79],a[240:243],a[144:147],v[36:39]\nM v[76:79],a[244:247],a[148:151],v[76:79]\nM v[76:79],a[248:251],a[152:155],v[76:79]\nM v[76:79],a[252:255],a[156:159],v[76:79]\n" \
"Lk70_%=:\ns_cmp_gt_u32 s8,2\ns_cbranch_scc1 Lk71_%=\nM v[84:87],a[240:243],a[160:163],v[40:43]\nM v[84:87],a[244:247],a[164:167],v[84:87]\nM v[84:87],a[248:251],a[168:171],v[84:87]\nM v[84:87],a[252:255],a[172:175],v[84:87]\nLk71_%=:\n" \
"s_cmp_gt_u32 s8,3\ns_cbranch_scc1 Lk72_%=\nM v[92:95],a[240:243],a[176:179],v[44:47]\nM v[92:95],a[244:247],a[180:183],v[92:95]\nM v[92:95],a[248:251],a[184:187],v[92:95]\nM v[92:95],a[252:255],a[188:191],v[92:95]\nLk72_%=:\ns_nop 7\n" \
"s_cmp_gt_u32 s8,0\ns_cbranch_scc0 Lk74_%=\nv_mov_b32 v16,v24\ns_cmp_eq_u32 s8,1\ns_cbranch_scc0 Lk73_%=\nM v[16:19],a[224:227],v[64:67],v[16:19]\ns_nop 7\ns_branch Lk73_%=\n" \
"Lk74_%=:\nv_max3_f32 v62,v64,v65,v66\nv_max3_f32 v63,v67,v68,v69\nv_max3_f32 v62,v62,v70,v71\nv_max3_f32 v62,v62,v63,0\nv_mov_b32 v63,v62\ns_nop 1\nv_permlane32_swap_b32 v62,v63\n" \
"v_max_f32 v62,v62,v63\nv_mov_b32 v63,v62\ns_nop 1\nv_permlane16_swap_b32 v62,v63\nv_max_f32 v62,v62,v63\nv_exp_f32 v63,-v62\nv_sub_f32 v64,v64,v62\nv_sub_f32 v65,v65,v62\n" \
"v_sub_f32 v66,v66,v62\nv_sub_f32 v67,v67,v62\nv_sub_f32 v68,v68,v62\nv_sub_f32 v69,v69,v62\nv_sub_f32 v70,v70,v62\nv_sub_f32 v71,v71,v62\nv_exp_f32 v64,v64\nv_exp_f32 v65,v65\n" \
"v_exp_f32 v66,v66\nv_exp_f32 v67,v67\nv_exp_f32 v68,v68\nv_exp_f32 v69,v69\nv_exp_f32 v70,v70\nv_exp_f32 v71,v71\nv_mul_f32 v16,v24,v63\nC v64,v64,v65\n" \
"C v65,v66,v67\nC v66,v68,v69\nC v67,v70,v71\nv_sub_f32 v32,v32,v62\nv_sub_f32 v33,v33,v62\nv_sub_f32 v34,v34,v62\nv_sub_f32 v35,v35,v62\nM v[16:19],a[224:227],v[64:67],v[16:19]\n" \
"v_mul_f32 v128,v128,v63\nv_mul_f32 v129,v129,v63\nv_mul_f32 v130,v130,v63\nv_mul_f32 v131,v131,v63\nv_mul_f32 v132,v132,v63\nv_mul_f32 v133,v133,v63\nv_mul_f32 v134,v134,v63\nv_mul_f32 v135,v135,v63\n" \
"v_mul_f32 v136,v136,v63\nv_mul_f32 v137,v137,v63\nv_mul_f32 v138,v138,v63\nv_mul_f32 v139,v139,v63\nv_mul_f32 v140,v140,v63\nv_mul_f32 v141,v141,v63\nv_mul_f32 v142,v142,v63\nv_mul_f32 v143,v143,v63\n" \
"v_mul_f32 v144,v144,v63\nv_mul_f32 v145,v145,v63\nv_mul_f32 v146,v146,v63\nv_mul_f32 v147,v147,v63\nv_mul_f32 v148,v148,v63\nv_mul_f32 v149,v149,v63\nv_mul_f32 v150,v150,v63\nv_mul_f32 v151,v151,v63\n" \
"v_mul_f32 v152,v152,v63\nv_mul_f32 v153,v153,v63\nv_mul_f32 v154,v154,v63\nv_mul_f32 v155,v155,v63\nv_mul_f32 v156,v156,v63\nv_mul_f32 v157,v157,v63\nv_mul_f32 v158,v158,v63\nv_mul_f32 v159,v159,v63\n" \
"s_bitcmp1_b32 s64,6\ns_cbranch_scc1 Lk73_%=\nv_sub_f32 v96,v96,v62\nv_sub_f32 v97,v97,v62\nv_sub_f32 v98,v98,v62\nv_sub_f32 v99,v99,v62\nv_sub_f32 v100,v100,v62\nv_sub_f32 v101,v101,v62\n" \
"v_sub_f32 v102,v102,v62\nv_sub_f32 v103,v103,v62\nLk73_%=:\ns_cmp_gt_u32 s8,1\ns_cbranch_scc0 Lk76_%=\nv_mov_b32 v17,v25\ns_cmp_eq_u32 s8,2\ns_cbranch_scc0 Lk75_%=\n" \
"M v[16:19],a[228:231],v[72:75],v[16:19]\ns_nop 7\ns_branch Lk75_%=\nLk76_%=:\nv_max3_f32 v62,v72,v73,v74\nv_max3_f32 v63,v75,v76,v77\nv_max3_f32 v62,v62,v78,v79\nv_max3_f32 v62,v62,v63,0\n" \
"v_mov_b32 v63,v62\ns_nop 1\nv_permlane32_swap_b32 v62,v63\nv_max_f32 v62,v62,v63\nv_mov_b32 v63,v62\ns_nop 1\nv_permlane16_swap_b32 v62,v63\nv_max_f32 v62,v62,v63\n" \
"v_exp_f32 v63,-v62\nv_sub_f32 v72,v72,v62\nv_sub_f32 v73,v73,v62\nv_sub_f32 v74,v74,v62\nv_sub_f32 v75,v75,v62\nv_sub_f32 v76,v76,v62\nv_sub_f32 v77,v77,v62\nv_sub_f32 v78,v78,v62\n" \
"v_sub_f32 v79,v79,v62\nv_exp_f32 v72,v72\nv_exp_f32 v73,v73\nv_exp_f32 v74,v74\nv_exp_f32 v75,v75\nv_exp_f32 v76,v76\nv_exp_f32 v77,v77\nv_exp_f32 v78,v78\n" \
"v_exp_f32 v79,v79\nv_mul_f32 v17,v25,v63\nC v72,v72,v73\nC v73,v74,v75\nC v74,v76,v77\nC v75,v78,v79\nv_sub_f32 v36,v36,v62\nv_sub_f32 v37,v37,v62\n" \
"v_sub_f32 v38,v38,v62\nv_sub_f32 v39,v39,v62\nM v[16:19],a[228:231],v[72:75],v[16:19]\nv_mul_f32 v160,v160,v63\nv_mul_f32 v161,v161,v63\nv_mul_f32 v162,v162,v63\nv_mul_f32 v163,v163,v63\nv_mul_f32 v164,v164,v63\n" \
"v_mul_f32 v165,v165,v63\nv_mul_f32 v166,v166,v63\nv_mul_f32 v167,v167,v63\nv_mul_f32 v168,v168,v63\nv_mul_f32 v169,v169,v63\nv_mul_f32 v170,v170,v63\nv_mul_f32 v171,v171,v63\nv_mul_f32 v172,v172,v63\n" \
"v_mul_f32 v173,v173,v63\nv_mul_f32 v174,v174,v63\nv_mul_f32 v175,v175,v63\nv_mul_f32 v176,v176,v63\nv_mul_f32 v177,v177,v63\nv_mul_f32 v178,v178,v63\nv_mul_f32 v179,v179,v63\nv_mul_f32 v180,v180,v63\n" \
"v_mul_f32 v181,v181,v63\nv_mul_f32 v182,v182,v63\nv_mul_f32 v183,v183,v63\nv_mul_f32 v184,v184,v63\nv_mul_f32 v185,v185,v63\nv_mul_f32 v186,v186,v63\nv_mul_f32 v187,v187,v63\nv_mul_f32 v188,v188,v63\n" \
"v_mul_f32 v189,v189,v63\nv_mul_f32 v190,v190,v63\nv_mul_f32 v191,v191,v63\ns_bitcmp1_b32 s64,6\ns_cbranch_scc1 Lk75_%=\nv_sub_f32 v104,v104,v62\nv_sub_f32 v105,v105,v62\nv_sub_f32 v106,v106,v62\n" \
"v_sub_f32 v107,v107,v62\nv_sub_f32 v108,v108,v62\nv_sub_f32 v109,v109,v62\nv_sub_f32 v110,v110,v62\nv_sub_f32 v111,v111,v62\nLk75_%=:\ns_cmp_gt_u32 s8,2\ns_cbranch_scc0 Lk78_%=\n" \
"v_mov_b32 v18,v26\ns_cmp_eq_u32 s8,3\ns_cbranch_scc0 Lk77_%=\nM v[16:19],a[232:235],v[80:83],v[16:19]\ns_nop 7\ns_branch Lk77_%=\nLk78_%=:\nv_max3_f32 v62,v80,v81,v82\n" \
"v_max3_f32 v63,v83,v84,v85\nv_max3_f32 v62,v62,v86,v87\nv_max3_f32 v62,v62,v63,0\nv_mov_b32 v63,v62\ns_nop 1\nv_permlane32_swap_b32 v62,v63\nv_max_f32 v62,v62,v63\nv_mov_b32 v63,v62\n" \
"s_nop 1\nv_permlane16_swap_b32 v62,v63\nv_max_f32 v62,v62,v63\nv_exp_f32 v63,-v62\nv_sub_f32 v80,v80,v62\nv_sub_f32 v81,v81,v62\nv_sub_f32 v82,v82,v62\nv_sub_f32 v83,v83,v62\n" \
"v_sub_f32 v84,v84,v62\nv_sub_f32 v85,v85,v62\nv_sub_f32 v86,v86,v62\nv_sub_f32 v87,v87,v62\nv_exp_f32 v80,v80\nv_exp_f32 v81,v81\nv_exp_f32 v82,v82\nv_exp_f32 v83,v83\n" \
"v_exp_f32 v84,v84\nv_exp_f32 v85,v85\nv_exp_f32 v86,v86\nv_exp_f32 v87,v87\nv_mul_f32 v18,v26,v63\nC v80,v80,v81\nC v81,v82,v83\nC v82,v84,v85\n" \
"C v83,v86,v87\nv_sub_f32 v40,v40,v62\nv_sub_f32 v41,v41,v62\nv_sub_f32 v42,v42,v62\nv_sub_f32 v43,v43,v62\nM v[16:19],a[232:235],v[80:83],v[16:19]\nv_mul_f32 v192,v192,v63\nv_mul_f32 v193,v193,v63\n" \
"v_mul_f32 v194,v194,v63\nv_mul_f32 v195,v195,v63\nv_mul_f32 v196,v196,v63\nv_mul_f32 v197,v197,v63\nv_mul_f32 v198,v198,v63\nv_mul_f32 v199,v199,v63\nv_mul_f32 v200,v200,v63\nv_mul_f32 v201,v201,v63\n" \
"v_mul_f32 v202,v202,v63\nv_mul_f32 v203,v203,v63\nv_mul_f32 v204,v204,v63\nv_mul_f32 v205,v205,v63\nv_mul_f32 v206,v206,v63\nv_mul_f32 v207,v207,v63\nv_mul_f32 v208,v208,v63\nv_mul_f32 v209,v209,v63\n" \
"v_mul_f32 v210,v210,v63\nv_mul_f32 v211,v211,v63\nv_mul_f32 v212,v212,v63\nv_mul_f32 v213,v213,v63\nv_mul_f32 v214,v214,v63\nv_mul_f32 v215,v215,v63\nv_mul_f32 v216,v216,v63\nv_mul_f32 v217,v217,v63\n" \
"v_mul_f32 v218,v218,v63\nv_mul_f32 v219,v219,v63\nv_mul_f32 v220,v220,v63\nv_mul_f32 v221,v221,v63\nv_mul_f32 v222,v222,v63\nv_mul_f32 v223,v223,v63\ns_bitcmp1_b32 s64,6\ns_cbranch_scc1 Lk77_%=\n" \
"v_sub_f32 v112,v112,v62\nv_sub_f32 v113,v113,v62\nv_sub_f32 v114,v114,v62\nv_sub_f32 v115,v115,v62\nv_sub_f32 v116,v116,v62\nv_sub_f32 v117,v117,v62\nv_sub_f32 v118,v118,v62\nv_sub_f32 v119,v119,v62\n" \
"Lk77_%=:\ns_cmp_gt_u32 s8,3\ns_cbranch_scc0 Lk80_%=\nv_mov_b32 v19,v27\ns_cmp_eq_u32 s8,4\ns_cbranch_scc0 Lk79_%=\nM v[16:19],a[236:239],v[88:91],v[16:19]\ns_nop 7\n" \
"s_branch Lk79_%=\nLk80_%=:\nv_max3_f32 v62,v88,v89,v90\nv_max3_f32 v63,v91,v92,v93\nv_max3_f32 v62,v62,v94,v95\nv_max3_f32 v62,v62,v63,0\nv_mov_b32 v63,v62\ns_nop 1\n" \
"v_permlane32_swap_b32 v62,v63\nv_max_f32 v62,v62,v63\nv_mov_b32 v63,v62\ns_nop 1\nv_permlane16_swap_b32 v62,v63\nv_max_f32 v62,v62,v63\nv_exp_f32 v63,-v62\nv_sub_f32 v88,v88,v62\n" \
"v_sub_f32 v89,v89,v62\nv_sub_f32 v90,v90,v62\nv_sub_f32 v91,v91,v62\nv_sub_f32 v92,v92,v62\nv_sub_f32 v93,v93,v62\nv_sub_f32 v94,v94,v62\nv_sub_f32 v95,v95,v62\nv_exp_f32 v88,v88\n" \
"v_exp_f32 v89,v89\nv_exp_f32 v90,v90\nv_exp_f32 v91,v91\nv_exp_f32 v92,v92\nv_exp_f32 v93,v93\nv_exp_f32 v94,v94\nv_exp_f32 v95,v95\nv_mul_f32 v19,v27,v63\n" \
"C v88,v88,v89\nC v89,v90,v91\nC v90,v92,v93\nC v91,v94,v95\nv_sub_f32 v44,v44,v62\nv_sub_f32 v45,v45,v62\nv_sub_f32 v46,v46,v62\nv_sub_f32 v47,v47,v62\n" \
"M v[16:19],a[236:239],v[88:91],v[16:19]\nv_mul_f32 v224,v224,v63\nv_mul_f32 v225,v225,v63\nv_mul_f32 v226,v226,v63\nv_mul_f32 v227,v227,v63\nv_mul_f32 v228,v228,v63\nv_mul_f32 v229,v229,v63\nv_mul_f32 v230,v230,v63\n" \
"v_mul_f32 v231,v231,v63\nv_mul_f32 v232,v232,v63\nv_mul_f32 v233,v233,v63\nv_mul_f32 v234,v234,v63\nv_mul_f32 v235,v235,v63\nv_mul_f32 v236,v236,v63\nv_mul_f32 v237,v237,v63\nv_mul_f32 v238,v238,v63\n" \
"v_mul_f32 v239,v239,v63\nv_mul_f32 v240,v240,v63\nv_mul_f32 v241,v241,v63\nv_mul_f32 v242,v242,v63\nv_mul_f32 v243,v243,v63\nv_mul_f32 v244,v244,v63\nv_mul_f32 v245,v245,v63\nv_mul_f32 v246,v246,v63\n" \
"v_mul_f32 v247,v247,v63\nv_mul_f32 v248,v248,v63\nv_mul_f32 v249,v249,v63\nv_mul_f32 v250,v250,v63\nv_mul_f32 v251,v251,v63\nv_mul_f32 v252,v252,v63\nv_mul_f32 v253,v253,v63\nv_mul_f32 v254,v254,v63\n" \
"v_mul_f32 v255,v255,v63\ns_bitcmp1_b32 s64,6\ns_cbranch_scc1 Lk79_%=\nv_sub_f32 v120,v120,v62\nv_sub_f32 v121,v121,v62\nv_sub_f32 v122,v122,v62\nv_sub_f32 v123,v123,v62\nv_sub_f32 v124,v124,v62\n" \
"v_sub_f32 v125,v125,v62\nv_sub_f32 v126,v126,v62\nv_sub_f32 v127,v127,v62\nLk79_%=:\ns_nop 7\nLSR0_%=:\ns_cmp_eq_u32 s64,0\ns_cbranch_scc1 Lcr4_%=\n" \
"s_cmp_eq_u32 s64,1\ns_cbranch_scc1 Lcr8_%=\ns_cmp_eq_u32 s64,2\ns_cbranch_scc1 Lcr12_%=\ns_cmp_eq_u32 s64,3\ns_cbranch_scc1 Lcr40_%=\ns_cmp_eq_u32 s64,4\ns_cbranch_scc1 Lcr44_%=\n" \
"s_cmp_eq_u32 s64,5\ns_cbranch_scc1 Lcr48_%=\ns_cmp_eq_u32 s64,6\ns_cbranch_scc1 Lcr52_%=\ns_cmp_eq_u32 s64,7\ns_cbranch_scc1 Lcr56_%=\ns_cmp_eq_u32 s64,8\ns_cbranch_scc1 Lcr60_%=\n" \
"s_branch Lcr64_%=\nLSLOW1_%=:\ns_cmp_eq_u32 s75,1\ns_cbranch_scc1 LSR1_%=\ns_lshl_b32 s9,s9,14\ns_add_u32 s9,s9,s71\ns_nop 7\ns_waitcnt lgkmcnt(0)\n" \
"buffer_load_dwordx4 a[240:243],v4,s[36:39],s9 offen offset:0 sc1\nbuffer_load_dwordx4 a[244:247],v4,s[36:39],s9 offen offset:2048 sc1\ns_add_u32 s9,s9,4096\nbuffer_load_dwordx4 a[248:251],v4,s[36:39],s9 offen offset:0 sc1\nbuffer_load_dwordx4 a[252:255],v4,s[36:39],s9 offen offset:2048 sc1\ns_sub_u32 s9,s9,3072\ns_waitcnt vmcnt(0)\ns_cmp_gt_u32 s8,0\n" \
"s_cbranch_scc1 Lk81_%=\nM v[96:99],a[240:243],a[128:131],v[32:35]\nM v[96:99],a[244:247],a[132:135],v[96:99]\nM v[96:99],a[248:251],a[136:139],v[96:99]\nM v[96:99],a[252:255],a[140:143],v[96:99]\nLk81_%=:\ns_cmp_gt_u32 s8,1\ns_cbranch_scc1 Lk82_%=\n" \
"M v[104:107],a[240:243],a[144:147],v[36:39]\nM v[104:107],a[244:247],a[148:151],v[104:107]\nM v[104:107],a[248:251],a[152:155],v[104:107]\nM v[104:107],a[252:255],a[156:159],v[104:107]\nLk82_%=:\ns_cmp_gt_u32 s8,2\ns_cbranch_scc1 Lk83_%=\nM v[112:115],a[240:243],a[160:163],v[40:43]\n" \
"M v[112:115],a[244:247],a[164:167],v[112:115]\nM v[112:115],a[248:251],a[168:171],v[112:115]\nM v[112:115],a[252:255],a[172:175],v[112:115]\nLk83_%=:\ns_cmp_gt_u32 s8,3\ns_cbranch_scc1 Lk84_%=\nM v[120:123],a[240:243],a[176:179],v[44:47]\nM v[120:123],a[244:247],a[180:183],v[120:123]\n" \
"M v[120:123],a[248:251],a[184:187],v[120:123]\nM v[120:123],a[252:255],a[188:191],v[120:123]\nLk84_%=:\ns_nop 7\nbuffer_load_dwordx4 a[240:243],v4,s[36:39],s9 offen offset:0 sc1\nbuffer_load_dwordx4 a[244:247],v4,s[36:39],s9 offen offset:2048 sc1\ns_add_u32 s9,s9,4096\nbuffer_load_dwordx4 a[248:251],v4,s[36:39],s9 offen offset:0 sc1\n" \
"buffer_load_dwordx4 a[252:255],v4,s[36:39],s9 offen offset:2048 sc1\ns_sub_u32 s9,s9,3072\ns_waitcnt vmcnt(0)\ns_cmp_gt_u32 s8,0\ns_cbranch_scc1 Lk85_%=\nM v[100:103],a[240:243],a[128:131],v[32:35]\nM v[100:103],a[244:247],a[132:135],v[100:103]\nM v[100:103],a[248:251],a[136:139],v[100:103]\n" \
"M v[100:103],a[252:255],a[140:143],v[100:103]\nLk85_%=:\ns_cmp_gt_u32 s8,1\ns_cbranch_scc1 Lk86_%=\nM v[108:111],a[240:243],a[144:147],v[36:39]\nM v[108:111],a[244:247],a[148:151],v[108:111]\nM v[108:111],a[248:251],a[152:155],v[108:111]\nM v[108:111],a[252:255],a[156:159],v[108:111]\n" \
"Lk86_%=:\ns_cmp_gt_u32 s8,2\ns_cbranch_scc1 Lk87_%=\nM v[116:119],a[240:243],a[160:163],v[40:43]\nM v[116:119],a[244:247],a[164:167],v[116:119]\nM v[116:119],a[248:251],a[168:171],v[116:119]\nM v[116:119],a[252:255],a[172:175],v[116:119]\nLk87_%=:\n" \
"s_cmp_gt_u32 s8,3\ns_cbranch_scc1 Lk88_%=\nM v[124:127],a[240:243],a[176:179],v[44:47]\nM v[124:127],a[244:247],a[180:183],v[124:127]\nM v[124:127],a[248:251],a[184:187],v[124:127]\nM v[124:127],a[252:255],a[188:191],v[124:127]\nLk88_%=:\ns_nop 7\n" \
"s_cmp_gt_u32 s8,0\ns_cbranch_scc0 Lk90_%=\nv_mov_b32 v24,v16\ns_cmp_eq_u32 s8,1\ns_cbranch_scc0 Lk89_%=\nM v[24:27],a[224:227],v[96:99],v[24:27]\ns_nop 7\ns_branch Lk89_%=\n" \
"Lk90_%=:\nv_max3_f32 v62,v96,v97,v98\nv_max3_f32 v63,v99,v100,v101\nv_max3_f32 v62,v62,v102,v103\nv_max3_f32 v62,v62,v63,0\nv_mov_b32 v63,v62\ns_nop 1\nv_permlane32_swap_b32 v62,v63\n" \
"v_max_f32 v62,v62,v63\nv_mov_b32 v63,v62\ns_nop 1\nv_permlane16_swap_b32 v62,v63\nv_max_f32 v62,v62,v63\nv_exp_f32 v63,-v62\nv_sub_f32 v96,v96,v62\nv_sub_f32 v97,v97,v62\n" \
"v_sub_f32 v98,v98,v62\nv_sub_f32 v99,v99,v62\nv_sub_f32 v100,v100,v62\nv_sub_f32 v101,v101,v62\nv_sub_f32 v102,v102,v62\nv_sub_f32 v103,v103,v62\nv_exp_f32 v96,v96\nv_exp_f32 v97,v97\n" \
"v_exp_f32 v98,v98\nv_exp_f32 v99,v99\nv_exp_f32 v100,v100\nv_exp_f32 v101,v101\nv_exp_f32 v102,v102\nv_exp_f32 v103,v103\nv_mul_f32 v24,v16,v63\nC v96,v96,v97\n" \
"C v97,v98,v99\nC v98,v100,v101\nC v99,v102,v103\nv_sub_f32 v32,v32,v62\nv_sub_f32 v33,v33,v62\nv_sub_f32 v34,v34,v62\nv_sub_f32 v35,v35,v62\nM v[24:27],a[224:227],v[96:99],v[24:27]\n" \
"v_mul_f32 v128,v128,v63\nv_mul_f32 v129,v129,v63\nv_mul_f32 v130,v130,v63\nv_mul_f32 v131,v131,v63\nv_mul_f32 v132,v132,v63\nv_mul_f32 v133,v133,v63\nv_mul_f32 v134,v134,v63\nv_mul_f32 v135,v135,v63\n" \
"v_mul_f32 v136,v136,v63\nv_mul_f32 v137,v137,v63\nv_mul_f32 v138,v138,v63\nv_mul_f32 v139,v139,v63\nv_mul_f32 v140,v140,v63\nv_mul_f32 v141,v141,v63\nv_mul_f32 v142,v142,v63\nv_mul_f32 v143,v143,v63\n" \
"v_mul_f32 v144,v144,v63\nv_mul_f32 v145,v145,v63\nv_mul_f32 v146,v146,v63\nv_mul_f32 v147,v147,v63\nv_mul_f32 v148,v148,v63\nv_mul_f32 v149,v149,v63\nv_mul_f32 v150,v150,v63\nv_mul_f32 v151,v151,v63\n" \
"v_mul_f32 v152,v152,v63\nv_mul_f32 v153,v153,v63\nv_mul_f32 v154,v154,v63\nv_mul_f32 v155,v155,v63\nv_mul_f32 v156,v156,v63\nv_mul_f32 v157,v157,v63\nv_mul_f32 v158,v158,v63\nv_mul_f32 v159,v159,v63\n" \
"s_bitcmp1_b32 s64,6\ns_cbranch_scc1 Lk89_%=\nv_sub_f32 v64,v64,v62\nv_sub_f32 v65,v65,v62\nv_sub_f32 v66,v66,v62\nv_sub_f32 v67,v67,v62\nv_sub_f32 v68,v68,v62\nv_sub_f32 v69,v69,v62\n" \
"v_sub_f32 v70,v70,v62\nv_sub_f32 v71,v71,v62\nLk89_%=:\ns_cmp_gt_u32 s8,1\ns_cbranch_scc0 Lk92_%=\nv_mov_b32 v25,v17\ns_cmp_eq_u32 s8,2\ns_cbranch_scc0 Lk91_%=\n" \
"M v[24:27],a[228:231],v[104:107],v[24:27]\ns_nop 7\ns_branch Lk91_%=\nLk92_%=:\nv_max3_f32 v62,v104,v105,v106\nv_max3_f32 v63,v107,v108,v109\nv_max3_f32 v62,v62,v110,v111\nv_max3_f32 v62,v62,v63,0\n" \
"v_mov_b32 v63,v62\ns_nop 1\nv_permlane32_swap_b32 v62,v63\nv_max_f32 v62,v62,v63\nv_mov_b32 v63,v62\ns_nop 1\nv_permlane16_swap_b32 v62,v63\nv_max_f32 v62,v62,v63\n" \
"v_exp_f32 v63,-v62\nv_sub_f32 v104,v104,v62\nv_sub_f32 v105,v105,v62\nv_sub_f32 v106,v106,v62\nv_sub_f32 v107,v107,v62\nv_sub_f32 v108,v108,v62\nv_sub_f32 v109,v109,v62\nv_sub_f32 v110,v110,v62\n" \
"v_sub_f32 v111,v111,v62\nv_exp_f32 v104,v104\nv_exp_f32 v105,v105\nv_exp_f32 v106,v106\nv_exp_f32 v107,v107\nv_exp_f32 v108,v108\nv_exp_f32 v109,v109\nv_exp_f32 v110,v110\n" \
"v_exp_f32 v111,v111\nv_mul_f32 v25,v17,v63\nC v104,v104,v105\nC v105,v106,v107\nC v106,v108,v109\nC v107,v110,v111\nv_sub_f32 v36,v36,v62\nv_sub_f32 v37,v37,v62\n" \
"v_sub_f32 v38,v38,v62\nv_sub_f32 v39,v39,v62\nM v[24:27],a[228:231],v[104:107],v[24:27]\nv_mul_f32 v160,v160,v63\nv_mul_f32 v161,v161,v63\nv_mul_f32 v162,v162,v63\nv_mul_f32 v163,v163,v63\nv_mul_f32 v164,v164,v63\n" \
"v_mul_f32 v165,v165,v63\nv_mul_f32 v166,v166,v63\nv_mul_f32 v167,v167,v63\nv_mul_f32 v168,v168,v63\nv_mul_f32 v169,v169,v63\nv_mul_f32 v170,v170,v63\nv_mul_f32 v171,v171,v63\nv_mul_f32 v172,v172,v63\n" \
"v_mul_f32 v173,v173,v63\nv_mul_f32 v174,v174,v63\nv_mul_f32 v175,v175,v63\nv_mul_f32 v176,v176,v63\nv_mul_f32 v177,v177,v63\nv_mul_f32 v178,v178,v63\nv_mul_f32 v179,v179,v63\nv_mul_f32 v180,v180,v63\n" \
"v_mul_f32 v181,v181,v63\nv_mul_f32 v182,v182,v63\nv_mul_f32 v183,v183,v63\nv_mul_f32 v184,v184,v63\nv_mul_f32 v185,v185,v63\nv_mul_f32 v186,v186,v63\nv_mul_f32 v187,v187,v63\nv_mul_f32 v188,v188,v63\n" \
"v_mul_f32 v189,v189,v63\nv_mul_f32 v190,v190,v63\nv_mul_f32 v191,v191,v63\ns_bitcmp1_b32 s64,6\ns_cbranch_scc1 Lk91_%=\nv_sub_f32 v72,v72,v62\nv_sub_f32 v73,v73,v62\nv_sub_f32 v74,v74,v62\n" \
"v_sub_f32 v75,v75,v62\nv_sub_f32 v76,v76,v62\nv_sub_f32 v77,v77,v62\nv_sub_f32 v78,v78,v62\nv_sub_f32 v79,v79,v62\nLk91_%=:\ns_cmp_gt_u32 s8,2\ns_cbranch_scc0 Lk94_%=\n" \
"v_mov_b32 v26,v18\ns_cmp_eq_u32 s8,3\ns_cbranch_scc0 Lk93_%=\nM v[24:27],a[232:235],v[112:115],v[24:27]\ns_nop 7\ns_branch Lk93_%=\nLk94_%=:\nv_max3_f32 v62,v112,v113,v114\n" \
"v_max3_f32 v63,v115,v116,v117\nv_max3_f32 v62,v62,v118,v119\nv_max3_f32 v62,v62,v63,0\nv_mov_b32 v63,v62\ns_nop 1\nv_permlane32_swap_b32 v62,v63\nv_max_f32 v62,v62,v63\nv_mov_b32 v63,v62\n" \
"s_nop 1\nv_permlane16_swap_b32 v62,v63\nv_max_f32 v62,v62,v63\nv_exp_f32 v63,-v62\nv_sub_f32 v112,v112,v62\nv_sub_f32 v113,v113,v62\nv_sub_f32 v114,v114,v62\nv_sub_f32 v115,v115,v62\n" \
"v_sub_f32 v116,v116,v62\nv_sub_f32 v117,v117,v62\nv_sub_f32 v118,v118,v62\nv_sub_f32 v119,v119,v62\nv_exp_f32 v112,v112\nv_exp_f32 v113,v113\nv_exp_f32 v114,v114\nv_exp_f32 v115,v115\n" \
"v_exp_f32 v116,v116\nv_exp_f32 v117,v117\nv_exp_f32 v118,v118\nv_exp_f32 v119,v119\nv_mul_f32 v26,v18,v63\nC v112,v112,v113\nC v113,v114,v115\nC v114,v116,v117\n" \
"C v115,v118,v119\nv_sub_f32 v40,v40,v62\nv_sub_f32 v41,v41,v62\nv_sub_f32 v42,v42,v62\nv_sub_f32 v43,v43,v62\nM v[24:27],a[232:235],v[112:115],v[24:27]\nv_mul_f32 v192,v192,v63\nv_mul_f32 v193,v193,v63\n" \
"v_mul_f32 v194,v194,v63\nv_mul_f32 v195,v195,v63\nv_mul_f32 v196,v196,v63\nv_mul_f32 v197,v197,v63\nv_mul_f32 v198,v198,v63\nv_mul_f32 v199,v199,v63\nv_mul_f32 v200,v200,v63\nv_mul_f32 v201,v201,v63\n" \
"v_mul_f32 v202,v202,v63\nv_mul_f32 v203,v203,v63\nv_mul_f32 v204,v204,v63\nv_mul_f32 v205,v205,v63\nv_mul_f32 v206,v206,v63\nv_mul_f32 v207,v207,v63\nv_mul_f32 v208,v208,v63\nv_mul_f32 v209,v209,v63\n" \
"v_mul_f32 v210,v210,v63\nv_mul_f32 v211,v211,v63\nv_mul_f32 v212,v212,v63\nv_mul_f32 v213,v213,v63\nv_mul_f32 v214,v214,v63\nv_mul_f32 v215,v215,v63\nv_mul_f32 v216,v216,v63\nv_mul_f32 v217,v217,v63\n" \
"v_mul_f32 v218,v218,v63\nv_mul_f32 v219,v219,v63\nv_mul_f32 v220,v220,v63\nv_mul_f32 v221,v221,v63\nv_mul_f32 v222,v222,v63\nv_mul_f32 v223,v223,v63\ns_bitcmp1_b32 s64,6\ns_cbranch_scc1 Lk93_%=\n" \
"v_sub_f32 v80,v80,v62\nv_sub_f32 v81,v81,v62\nv_sub_f32 v82,v82,v62\nv_sub_f32 v83,v83,v62\nv_sub_f32 v84,v84,v62\nv_sub_f32 v85,v85,v62\nv_sub_f32 v86,v86,v62\nv_sub_f32 v87,v87,v62\n" \
"Lk93_%=:\ns_cmp_gt_u32 s8,3\ns_cbranch_scc0 Lk96_%=\nv_mov_b32 v27,v19\ns_cmp_eq_u32 s8,4\ns_cbranch_scc0 Lk95_%=\nM v[24:27],a[236:239],v[120:123],v[24:27]\ns_nop 7\n" \
"s_branch Lk95_%=\nLk96_%=:\nv_max3_f32 v62,v120,v121,v122\nv_max3_f32 v63,v123,v124,v125\nv_max3_f32 v62,v62,v126,v127\nv_max3_f32 v62,v62,v63,0\nv_mov_b32 v63,v62\ns_nop 1\n" \
"v_permlane32_swap_b32 v62,v63\nv_max_f32 v62,v62,v63\nv_mov_b32 v63,v62\ns_nop 1\nv_permlane16_swap_b32 v62,v63\nv_max_f32 v62,v62,v63\nv_exp_f32 v63,-v62\nv_sub_f32 v120,v120,v62\n" \
"v_sub_f32 v121,v121,v62\nv_sub_f32 v122,v122,v62\nv_sub_f32 v123,v123,v62\nv_sub_f32 v124,v124,v62\nv_sub_f32 v125,v125,v62\nv_sub_f32 v126,v126,v62\nv_sub_f32 v127,v127,v62\nv_exp_f32 v120,v120\n" \
"v_exp_f32 v121,v121\nv_exp_f32 v122,v122\nv_exp_f32 v123,v123\nv_exp_f32 v124,v124\nv_exp_f32 v125,v125\nv_exp_f32 v126,v126\nv_exp_f32 v127,v127\nv_mul_f32 v27,v19,v63\n" \
"C v120,v120,v121\nC v121,v122,v123\nC v122,v124,v125\nC v123,v126,v127\nv_sub_f32 v44,v44,v62\nv_sub_f32 v45,v45,v62\nv_sub_f32 v46,v46,v62\nv_sub_f32 v47,v47,v62\n" \
"M v[24:27],a[236:239],v[120:123],v[24:27]\nv_mul_f32 v224,v224,v63\nv_mul_f32 v225,v225,v63\nv_mul_f32 v226,v226,v63\nv_mul_f32 v227,v227,v63\nv_mul_f32 v228,v228,v63\nv_mul_f32 v229,v229,v63\nv_mul_f32 v230,v230,v63\n" \
"v_mul_f32 v231,v231,v63\nv_mul_f32 v232,v232,v63\nv_mul_f32 v233,v233,v63\nv_mul_f32 v234,v234,v63\nv_mul_f32 v235,v235,v63\nv_mul_f32 v236,v236,v63\nv_mul_f32 v237,v237,v63\nv_mul_f32 v238,v238,v63\n" \
"v_mul_f32 v239,v239,v63\nv_mul_f32 v240,v240,v63\nv_mul_f32 v241,v241,v63\nv_mul_f32 v242,v242,v63\nv_mul_f32 v243,v243,v63\nv_mul_f32 v244,v244,v63\nv_mul_f32 v245,v245,v63\nv_mul_f32 v246,v246,v63\n" \
"v_mul_f32 v247,v247,v63\nv_mul_f32 v248,v248,v63\nv_mul_f32 v249,v249,v63\nv_mul_f32 v250,v250,v63\nv_mul_f32 v251,v251,v63\nv_mul_f32 v252,v252,v63\nv_mul_f32 v253,v253,v63\nv_mul_f32 v254,v254,v63\n" \
"v_mul_f32 v255,v255,v63\ns_bitcmp1_b32 s64,6\ns_cbranch_scc1 Lk95_%=\nv_sub_f32 v88,v88,v62\nv_sub_f32 v89,v89,v62\nv_sub_f32 v90,v90,v62\nv_sub_f32 v91,v91,v62\nv_sub_f32 v92,v92,v62\n" \
"v_sub_f32 v93,v93,v62\nv_sub_f32 v94,v94,v62\nv_sub_f32 v95,v95,v62\nLk95_%=:\ns_nop 7\nLSR1_%=:\ns_cmp_eq_u32 s64,0\ns_cbranch_scc1 Lcr2_%=\n" \
"s_cmp_eq_u32 s64,1\ns_cbranch_scc1 Lcr6_%=\ns_cmp_eq_u32 s64,2\ns_cbranch_scc1 Lcr10_%=\ns_cmp_eq_u32 s64,3\ns_cbranch_scc1 Lcr14_%=\ns_cmp_eq_u32 s64,4\ns_cbranch_scc1 Lcr42_%=\n" \
"s_cmp_eq_u32 s64,5\ns_cbranch_scc1 Lcr46_%=\ns_cmp_eq_u32 s64,6\ns_cbranch_scc1 Lcr50_%=\ns_cmp_eq_u32 s64,7\ns_cbranch_scc1 Lcr54_%=\ns_cmp_eq_u32 s64,8\ns_cbranch_scc1 Lcr58_%=\n" \
"s_branch Lcr62_%=\nLPU_%=:\ns_mov_b32 s25,0\nLPUL_%=:\ns_sleep 2\nbuffer_load_dwordx2 v[58:59],v57,s[36:39],0 offen sc1\ns_add_u32 s25,s25,1\ns_waitcnt vmcnt(0)\n" \
"v_cmp_gt_u64 s[98:99],s[14:15],v[58:59]\nv_cmp_le_u64 s[18:19],s[16:17],v[58:59]\ns_cmp_gt_u32 s25,262144\ns_cbranch_scc1 LPUR_%=\ns_or_b64 s[98:99],s[98:99],s[18:19]\ns_cbranch_scc1 LPUL_%=\ns_branch LPUR_%=\nLPK_%=:\n" \
"s_mov_b32 s25,0\nLPKL_%=:\ns_sleep 2\nbuffer_load_dwordx2 v[58:59],v57,s[36:39],0 offen sc1\ns_add_u32 s25,s25,1\ns_waitcnt vmcnt(0)\nv_cmp_gt_u64 s[98:99],s[14:15],v[58:59]\nv_cmp_le_u64 s[18:19],s[16:17],v[58:59]\n" \
"s_cmp_gt_u32 s25,262144\ns_cbranch_scc1 LPKR_%=\ns_or_b64 s[98:99],s[98:99],s[18:19]\ns_cbranch_scc1 LPKL_%=\ns_branch LPKR_%=\nLPZ_%=:\ns_mov_b32 s25,0\nLPZL_%=:\n" \
"s_sleep 2\nbuffer_load_dwordx2 v[10:11],v5,s[36:39],0 offen sc1\ns_add_u32 s25,s25,1\ns_waitcnt vmcnt(0)\nv_cmp_gt_u64 s[98:99],s[14:15],v[10:11]\nv_cmp_le_u64 s[18:19],s[16:17],v[10:11]\ns_cmp_gt_u32 s25,262144\ns_cbranch_scc1 LPZR_%=\n" \
"s_or_b64 s[98:99],s[98:99],s[18:19]\ns_cbranch_scc1 LPZL_%=\ns_branch LPZR_%=\nLPE_%=:\ns_mov_b32 s25,0\nLPEL_%=:\ns_sleep 2\nbuffer_load_dwordx2 v[10:11],v5,s[36:39],0 offen sc1\n" \
"s_add_u32 s25,s25,1\ns_waitcnt vmcnt(0)\nv_cmp_gt_u64 s[98:99],s[14:15],v[10:11]\nv_cmp_le_u64 s[18:19],s[16:17],v[10:11]\ns_cmp_gt_u32 s25,262144\ns_cbranch_scc1 LPER_%=\ns_or_b64 s[98:99],s[98:99],s[18:19]\ns_and_b32 s98,s98,0xffff\n" \
"s_cbranch_scc1 LPEL_%=\ns_branch LPER_%=\nLPF_%=:\ns_mov_b32 s25,0\nLPFL_%=:\ns_sleep 2\nbuffer_load_dwordx2 v[10:11],v5,s[36:39],0 offen sc1\ns_add_u32 s25,s25,1\n" \
"s_waitcnt vmcnt(0)\nv_cmp_gt_u64 s[98:99],s[14:15],v[10:11]\nv_cmp_le_u64 s[18:19],s[16:17],v[10:11]\ns_cmp_gt_u32 s25,262144\ns_cbranch_scc1 LPFR_%=\ns_or_b64 s[98:99],s[98:99],s[18:19]\ns_and_b32 s98,s98,0xff0000\ns_cbranch_scc1 LPFL_%=\n" \
"s_branch LPFR_%=\nLPM_%=:\ns_mov_b32 s25,0\nLPML_%=:\ns_sleep 2\nbuffer_load_dwordx2 v[10:11],v5,s[36:39],0 offen sc1\ns_add_u32 s25,s25,1\ns_waitcnt vmcnt(0)\n" \
"v_cmp_gt_u64 s[98:99],s[14:15],v[10:11]\nv_cmp_le_u64 s[18:19],s[16:17],v[10:11]\ns_cmp_gt_u32 s25,262144\ns_cbranch_scc1 LPMR_%=\ns_or_b64 s[98:99],s[98:99],s[18:19]\ns_and_b32 s98,s98,0xff000000\ns_cbranch_scc1 LPML_%=\ns_branch LPMR_%=\n" \
"LPL_%=:\ns_mov_b32 s25,0\nLPLL_%=:\ns_sleep 2\nbuffer_load_dwordx2 v[10:11],v58,s[36:39],0 offen sc1\ns_add_u32 s25,s25,1\ns_waitcnt vmcnt(0)\nv_cmp_gt_u64 s[98:99],s[14:15],v[10:11]\n" \
"v_cmp_le_u64 s[18:19],s[16:17],v[10:11]\ns_cmp_gt_u32 s25,262144\ns_cbranch_scc1 LPLR_%=\ns_or_b64 s[98:99],s[98:99],s[18:19]\ns_cbranch_scc1 LPLL_%=\ns_branch LPLR_%=\nLPP_%=:\ns_mov_b32 s25,0\n" \
"LPPL_%=:\ns_sleep 2\nbuffer_load_dwordx2 v[10:11],v190,s[36:39],0 offen sc1\ns_add_u32 s25,s25,1\ns_waitcnt vmcnt(0)\nv_cmp_gt_u64 s[98:99],s[14:15],v[10:11]\nv_cmp_le_u64 s[18:19],s[16:17],v[10:11]\ns_cmp_gt_u32 s25,262144\n" \
"s_cbranch_scc1 LPPR_%=\ns_or_b64 s[98:99],s[98:99],s[18:19]\ns_and_b32 s98,s98,0x7\ns_cbranch_scc1 LPPL_%=\ns_branch LPPR_%=\nLIN_%=:\ns_mov_b32 s25,0\nLINL_%=:\n" \
"s_sleep 1\ns_add_u32 s57,s49,4096\nbuffer_load_dwordx4 v[176:179],v4,s[36:39],s57 offen offset:2048 sc1\nbuffer_load_dwordx4 v[180:183],v4,s[36:39],s57 offen offset:3072 sc1\ns_add_u32 s57,s49,8192\nbuffer_load_dwordx4 v[32:35],v4,s[36:39],s57 offen offset:0 sc1\nbuffer_load_dwordx4 v[36:39],v4,s[36:39],s57 offen offset:1024 sc1\ns_add_u32 s25,s25,1\n" \
"s_waitcnt vmcnt(0)\nv_max_u32 v10,v176,v177\nv_max3_u32 v10,v10,v178,v179\nv_max3_u32 v10,v10,v180,v181\nv_max3_u32 v10,v10,v182,v183\nv_max3_u32 v10,v10,v32,v33\nv_max3_u32 v10,v10,v34,v35\nv_max3_u32 v10,v10,v36,v37\n" \
"v_max3_u32 v10,v10,v38,v39\nv_cmp_eq_u32 vcc,-1,v10\ns_cmp_gt_u32 s25,262144\ns_cbranch_scc1 LINR_%=\ns_cbranch_vccnz LINL_%=\ns_branch LINR_%=\nLIO_%=:\ns_mov_b32 s25,0\n" \
"LIOL_%=:\ns_sleep 1\ns_add_u32 s57,s49,8192\nbuffer_load_dwordx4 v[40:43],v4,s[36:39],s57 offen offset:2048 sc1\nbuffer_load_dwordx4 v[44:47],v4,s[36:39],s57 offen offset:3072 sc1\ns_add_u32 s25,s25,1\ns_waitcnt vmcnt(0)\nv_max_u32 v10,v40,v41\n" \
"v_max3_u32 v10,v10,v42,v43\nv_max3_u32 v10,v10,v44,v45\nv_max3_u32 v10,v10,v46,v47\nv_cmp_eq_u32 vcc,-1,v10\ns_cmp_gt_u32 s25,262144\ns_cbranch_scc1 LIOR_%=\ns_cbranch_vccnz LIOL_%=\ns_branch LIOR_%=\n" \
"LEND_%=:\ns_waitcnt vmcnt(0) lgkmcnt(0)\n.purgem M\n.purgem C\n" \
""
constexpr int Q_LDS = 163840;
__global__ __launch_bounds__(256, 1) void fused_kernel(const void* x, const void* k, const void* v, const void* shk, const void* ssmk, const void* ssmD, const void* ckv,
                                                       const void* wq, const void* wuk, const void* wuv, const void* wo, const void* gw, void* out, void* ws) {
    const int w = __builtin_amdgcn_readfirstlane(threadIdx.x >> 6);
    const unsigned long long ka = (unsigned long long)__builtin_amdgcn_kernarg_segment_ptr();
    asm volatile(QKV_ASM_TEXT :: "s"(ka), "s"(blockIdx.x), "s"(w) : QKV_ASM_CLOBBERS);
}

extern "C" void kernel_launch(void* const* d_in, const int* in_sizes, int n_in, void* d_out, int out_size, void* d_ws, size_t ws_size,
                              hipStream_t stream) {
    (void)hipFuncSetAttribute((const void*)fused_kernel, hipFuncAttributeMaxDynamicSharedMemorySize, Q_LDS);
    fused_kernel<<<256, 256, Q_LDS, stream>>>(d_in[0], d_in[1], d_in[2], d_in[3], d_in[4], d_in[5], d_in[7], d_in[8], d_in[9], d_in[10], d_in[11], d_in[12], d_out, d_ws);
}
```

```cpp
#include <hip/hip_runtime.h>
#include <stdint.h>
#define QKV_ASM_CLOBBERS "v0", "v1", "v2", "v3", "v4", "v5", "v6", "v7", "v8", "v9", "v10", "v11", "v12", "v13", "v14", "v15", "v16", "v17", "v18", "v19", "v20", "v21", "v22", "v23", "v24", "v25", "v26", "v27", "v28", "v29", "v30", "v31", "v32", "v33", "v34", "v35", "v36", "v37", "v38", "v39", "v40", "v41", "v42", "v43", "v44", "v45", "v46", "v47", "v48", "v49", "v50", "v51", "v52", "v53", "v54", "v55", "v56", "v57", "v58", "v59", "v60", "v61", "v62", "v63", "v64", "v65", "v66", "v67", "v68", "v69", "v70", "v71", "v72", "v73", "v74", "v75", "v76", "v77", "v78", "v79", "v80", "v81", "v82", "v83", "v84", "v85", "v86", "v87", "v88", "v89", "v90", "v91", "v92", "v93", "v94", "v95", "v96", "v97", "v98", "v99", "v100", "v101", "v102", "v103", "v104", "v105", "v106", "v107", "v108", "v109", "v110", "v111", "v112", "v113", "v114", "v115", "v116", "v117", "v118", "v119", "v120", "v121", "v122", "v123", "v124", "v125", "v126", "v127", "v128", "v129", "v130", "v131", "v132", "v133", "v134", "v135", "v136", "v137", "v138", "v139", "v140", "v141", "v142", "v143", "v144", "v145", "v146", "v147", "v148", "v149", "v150", "v151", "v152", "v153", "v154", "v155", "v156", "v157", "v158", "v159", "v160", "v161", "v162", "v163", "v164", "v165", "v166", "v167", "v168", "v169", "v170", "v171", "v172", "v173", "v174", "v175", "v176", "v177", "v178", "v179", "v180", "v181", "v182", "v183", "v184", "v185", "v186", "v187", "v188", "v189", "v190", "v191", "v192", "v193", "v194", "v195", "v196", "v197", "v198", "v199", "v200", "v201", "v202", "v203", "v204", "v205", "v206", "v207", "v208", "v209", "v210", "v211", "v212", "v213", "v214", "v215", "v216", "v217", "v218", "v219", "v220", "v221", "v222", "v223", "v224", "v225", "v226", "v227", "v228", "v229", "v230", "v231", "v232", "v233", "v234", "v235", "v236", "v237", "v238", "v239", "v240", "v241", "v242", "v243", "v244", "v245", "v246", "v247", "v248", "v249", "v250", "v251", "v252", "v253", "v254", "v255", "a0", "a1", "a2", "a3", "a4", "a5", "a6", "a7", "a8", "a9", "a10", "a11", "a12", "a13", "a14", "a15", "a16", "a17", "a18", "a19", "a20", "a21", "a22", "a23", "a24", "a25", "a26", "a27", "a28", "a29", "a30", "a31", "a32", "a33", "a34", "a35", "a36", "a37", "a38", "a39", "a40", "a41", "a42", "a43", "a44", "a45", "a46", "a47", "a48", "a49", "a50", "a51", "a52", "a53", "a54", "a55", "a56", "a57", "a58", "a59", "a60", "a61", "a62", "a63", "a64", "a65", "a66", "a67", "a68", "a69", "a70", "a71", "a72", "a73", "a74", "a75", "a76", "a77", "a78", "a79", "a80", "a81", "a82", "a83", "a84", "a85", "a86", "a87", "a88", "a89", "a90", "a91", "a92", "a93", "a94", "a95", "a96", "a97", "a98", "a99", "a100", "a101", "a102", "a103", "a104", "a105", "a106", "a107", "a108", "a109", "a110", "a111", "a112", "a113", "a114", "a115", "a116", "a117", "a118", "a119", "a120", "a121", "a122", "a123", "a124", "a125", "a126", "a127", "a128", "a129", "a130", "a131", "a132", "a133", "a134", "a135", "a136", "a137", "a138", "a139", "a140", "a141", "a142", "a143", "a144", "a145", "a146", "a147", "a148", "a149", "a150", "a151", "a152", "a153", "a154", "a155", "a156", "a157", "a158", "a159", "a160", "a161", "a162", "a163", "a164", "a165", "a166", "a167", "a168", "a169", "a170", "a171", "a172", "a173", "a174", "a175", "a176", "a177", "a178", "a179", "a180", "a181", "a182", "a183", "a184", "a185", "a186", "a187", "a188", "a189", "a190", "a191", "a192", "a193", "a194", "a195", "a196", "a197", "a198", "a199", "a200", "a201", "a202", "a203", "a204", "a205", "a206", "a207", "a208", "a209", "a210", "a211", "a212", "a213", "a214", "a215", "a216", "a217", "a218", "a219", "a220", "a221", "a222", "a223", "a224", "a225", "a226", "a227", "a228", "a229", "a230", "a231", "a232", "a233", "a234", "a235", "a236", "a237", "a238", "a239", "a240", "a241", "a242", "a243", "a244", "a245", "a246", "a247", "a248", "a249", "a250", "a251", "a252", "a253", "a254", "a255", "s4", "s5", "s6", "s7", "s8", "s9", "s10", "s11", "s12", "s13", "s14", "s15", "s16", "s17", "s18", "s19", "s20", "s21", "s22", "s23", "s24", "s25", "s26", "s27", "s28", "s29", "s30", "s31", "s32", "s33", "s34", "s35", "s36", "s37", "s38", "s39", "s40", "s41", "s42", "s43", "s44", "s45", "s46", "s47", "s48", "s49", "s50", "s51", "s52", "s53", "s54", "s55", "s56", "s57", "s58", "s59", "s60", "s61", "s62", "s63", "s64", "s65", "s66", "s67", "s68", "s69", "s70", "s71", "s72", "s73", "s74", "s75", "s76", "s77", "s78", "s79", "s80", "s81", "s82", "s83", "s84", "s85", "s86", "s87", "s88", "s89", "s90", "s91", "s92", "s93", "s94", "s95", "s96", "s97", "s98", "s99", "s100", "s101", "vcc", "scc", "memory"
#define QKV_ASM_TEXT \
".macro M d,a,b,c\nv_mfma_f32_16x16x32_bf16 \\d,\\a,\\b,\\c\n.endm\n.macro C d,a,b\nv_cvt_pk_bf16_f32 \\d,\\a,\\b\n.endm\ns_memrealtime s[18:19]\ns_load_dwordx16 s[64:79],%0,0x0\n" \
"s_load_dwordx8 s[80:87],%0,0x40\ns_load_dwordx4 s[88:91],%0,0x60\nv_mbcnt_lo_u32_b32 v23,-1,0\nv_mbcnt_hi_u32_b32 v23,-1,v23\ns_bfe_u32 s7,%1,0x30003\ns_and_b32 s6,%1,7\ns_lshl_b32 s6,s6,2\ns_lshr_b32 s60,%1,6\n" \
"s_add_u32 s6,s6,s60\nv_lshlrev_b32 v4,4,v23\ns_getpc_b64 s[40:41]\nLCTBEG_%=:\ns_add_u32 s40,s40,LBEG_%=-LCTBEG_%=\ns_addc_u32 s41,s41,0\ns_mov_b64 s[100:101],s[40:41]\ns_and_b32 s41,s41,0xffff\n" \
"s_mov_b32 s42,LEND_%=-LBEG_%=\ns_mov_b32 s43,0x00020000\ns_lshr_b32 s9,%1,3\ns_lshl_b32 s9,s9,2\ns_add_u32 s9,s9,%2\ns_and_b32 s9,s9,31\ns_lshl_b32 s9,s9,13\nv_lshlrev_b32 v64,3,v4\n" \
"buffer_load_dword v64,v64,s[40:43],s9 offen\ns_lshl_b32 s60,%2,2\nv_lshrrev_b32 v24,4,v23\nv_add_u32 v25,s60,v24\nv_xor_b32 v25,v25,v23\nv_and_b32 v25,7,v25\ns_lshl_b32 s61,%2,3\nv_lshrrev_b32 v26,3,v23\n" \
"v_add_u32 v26,s61,v26\nv_lshlrev_b32 v26,10,v26\nv_lshl_add_u32 v13,v25,4,v26\nv_add_u32 v63,v13,v26\ns_lshl_b32 s58,s7,17\ns_sub_u32 s59,1966080,s58\nv_add_u32 v17,s59,v13\nv_add_u32 v13,s58,v13\n" \
"v_add_u32 v14,0x8000,v13\nv_add_u32 v18,0x8000,v17\nv_add_u32 v15,0x8000,v14\nv_add_u32 v19,0x8000,v18\nv_add_u32 v16,0x8000,v15\nv_add_u32 v20,0x8000,v19\nv_bfe_u32 v25,v23,2,3\nv_add_u32 v25,s61,v25\n" \
"v_lshlrev_b32 v25,13,v25\ns_lshl_b32 s61,%2,1\nv_and_b32 v26,1,v24\nv_add_u32 v26,s61,v26\nv_xor_b32 v26,v26,v23\nv_and_b32 v26,3,v26\nv_lshrrev_b32 v27,5,v23\nv_lshl_add_u32 v26,v27,2,v26\n" \
"v_lshl_add_u32 v21,v26,4,v25\ns_lshl_b32 s50,%2,10\ns_add_u32 s50,s50,65536\ns_lshl_b32 s22,%2,12\ns_lshl_b32 s51,%2,11\ns_mov_b32 s31,0x00020000\ns_mov_b32 s35,0x00020000\ns_mov_b32 s95,0x00020000\n" \
"s_mov_b32 s30,2097152\ns_lshl_b32 s98,s6,8\ns_sub_u32 s34,4194304,s98\ns_mov_b32 s94,s34\ns_sub_u32 s49,8388608,s98\ns_cmp_lt_u32 %2,2\ns_cselect_b32 s57,s58,s59\ns_lshr_b32 s57,s57,1\n" \
"s_lshl_b32 s60,s6,20\ns_add_u32 s57,s57,s60\ns_and_b32 s54,%2,1\ns_lshl_b32 s60,s54,13\ns_add_u32 s57,s57,s60\ns_add_u32 s57,s57,16777216\ns_bitcmp1_b32 %2,0\ns_cselect_b32 s20,1.0,0x3e0293ee\n" \
"s_lshl_b32 s54,s54,11\ns_lshl_b32 s24,s6,3\ns_add_u32 s24,s24,s7\ns_lshl_b32 s24,s24,3\ns_lshl_b32 s60,%2,27\ns_add_u32 s24,s24,s60\ns_add_u32 s24,s24,94375936\ns_and_b32 s8,%1,7\n" \
"s_lshl_b32 s23,s8,8\ns_add_u32 s23,s23,s60\ns_and_b32 s60,%1,0xf8\ns_add_u32 s23,s23,s60\ns_add_u32 s23,s23,94380032\ns_lshl_b32 s8,s8,5\ns_lshr_b32 s58,%1,3\ns_add_u32 s8,s8,s58\n" \
"v_lshrrev_b32 v23,4,v4\nv_and_b32 v24,15,v23\nv_lshrrev_b32 v25,4,v23\ns_lshr_b32 s58,%2,1\ns_lshl_b32 s58,s58,14\ns_add_u32 s58,s58,65536\nv_lshrrev_b32 v26,1,v24\nv_xor_b32 v26,v26,v25\n" \
"v_lshlrev_b32 v26,4,v26\nv_lshl_add_u32 v26,v24,7,v26\ns_waitcnt lgkmcnt(0)\ns_mov_b64 s[28:29],s[76:77]\ns_and_b32 s29,s29,0xffff\ns_add_u32 s32,s80,s98\ns_addc_u32 s33,s81,0\ns_and_b32 s33,s33,0xffff\n" \
"s_add_u32 s92,s82,s98\ns_addc_u32 s93,s83,0\ns_and_b32 s93,s93,0xffff\ns_mov_b32 s47,0\ns_add_u32 m0,s22,0\nv_add_u32 v5,s58,v26\nbuffer_load_dwordx4 v21,s[32:35],s47 offen offset:0 lds\nv_xor_b32 v6,64,v5\n" \
"v_add_u32 v7,0x10000,v5\nv_add_u32 v8,0x10000,v6\nv_lshrrev_b32 v26,2,v24\ns_mov_b32 s46,0\ns_add_u32 m0,s50,65536\nv_lshlrev_b32 v26,6,v26\nbuffer_load_dwordx4 v13,s[28:31],s46 offen lds\nv_and_b32 v27,1,v23\n" \
"v_lshl_add_u32 v26,v27,3,v26\nv_bfe_u32 v27,v23,1,1\nv_lshlrev_b32 v28,1,v25\ns_add_u32 m0,s22,896\nv_xor_b32 v27,v27,v28\nbuffer_load_dwordx4 v21,s[32:35],s47 offen offset:128 lds\nv_and_b32 v27,3,v27\nv_lshl_add_u32 v26,v27,4,v26\n" \
"v_lshl_add_u32 v9,v25,12,v26\nv_add_u32 v9,s54,v9\ns_add_u32 m0,s50,69632\nv_lshl_add_u32 v62,v25,11,s54\nbuffer_load_dwordx4 v14,s[28:31],s46 offen lds\nv_xor_b32 v10,0x110,v9\nv_xor_b32 v11,32,v9\nv_xor_b32 v12,0x130,v9\n" \
"s_and_b32 s62,%1,7\ns_add_u32 m0,s22,2048\ns_lshl_b32 s62,s62,2\nbuffer_load_dwordx4 v21,s[92:95],s47 offen offset:0 lds\ns_add_u32 s62,s62,%2\ns_lshl_b32 s62,s62,5\ns_and_b32 s63,%1,0xf8\nv_bfe_u32 v24,v4,4,3\n" \
"s_add_u32 m0,s50,73728\nv_lshrrev_b32 v23,7,v4\nbuffer_load_dwordx4 v15,s[28:31],s46 offen lds\nv_lshl_add_u32 v24,v24,2,s62\nv_add_u32 v25,s63,v23\nv_lshlrev_b32 v26,14,v25\nv_lshl_add_u32 v59,v24,1,v26\ns_add_u32 m0,s22,2944\n" \
"v_add_u32 v54,0xffffe800,v59\nbuffer_load_dwordx4 v21,s[92:95],s47 offen offset:128 lds\nv_add_u32 v55,0xfffff800,v59\nv_lshlrev_b32 v60,12,v24\nv_lshl_add_u32 v60,v25,4,v60\nv_lshlrev_b32 v27,3,v24\ns_add_u32 m0,s50,77824\ns_lshl_b32 s62,%1,2\n" \
"buffer_load_dwordx4 v16,s[28:31],s46 offen lds\ns_add_u32 s62,s62,%2\ns_lshl_b32 s63,s62,12\nv_add_u32 v26,s63,v4\ns_lshl_b32 s62,s62,3\ns_mov_b32 s46,0\ns_add_u32 m0,s50,81920\ns_add_u32 s62,s62,71303168\n" \
"buffer_load_dwordx4 v17,s[28:31],s46 offen lds\nv_and_b32 v61,4,v23\nv_add_u32 v61,s62,v61\nv_lshlrev_b32 v2,3,v4\nv_and_b32 v1,0x380,v2\ns_add_u32 m0,s50,86016\nv_lshrrev_b32 v23,7,v4\nbuffer_load_dwordx4 v18,s[28:31],s46 offen lds\n" \
"v_lshl_add_u32 v1,v23,13,v1\ns_lshr_b32 s58,%1,3\ns_lshl_b32 s58,s58,2\ns_add_u32 s58,s58,%2\ns_add_u32 m0,s50,90112\ns_and_b32 s60,%1,7\nbuffer_load_dwordx4 v19,s[28:31],s46 offen lds\ns_lshl_b32 s60,s60,10\n" \
"s_lshl_b32 s59,s58,14\ns_add_u32 s59,s59,s60\ns_mov_b64 s[4:5],s[90:91]\ns_add_u32 m0,s50,94208\ns_lshl_b32 s99,s58,11\nbuffer_load_dwordx4 v20,s[28:31],s46 offen lds\ns_lshl_b32 s96,s60,8\ns_add_u32 s99,s99,s96\n" \
"s_add_u32 s96,s76,s99\ns_addc_u32 s97,s77,0\ns_add_u32 s76,s78,s59\ns_addc_u32 s77,s79,0\ns_mov_b32 s47,262144\ns_add_u32 m0,s22,16384\ns_and_b32 s59,s59,1048575\nbuffer_load_dwordx4 v21,s[32:35],s47 offen offset:0 lds\n" \
"s_cmp_lt_u32 s58,64\ns_cselect_b64 s[84:85],s[80:81],s[82:83]\ns_add_u32 s84,s84,s59\ns_addc_u32 s85,s85,0\ns_lshl_b32 s59,s8,2\ns_add_u32 s59,s59,%2\ns_add_u32 m0,s22,17280\ns_lshl_b32 s59,s59,12\n" \
"buffer_load_dwordx4 v21,s[32:35],s47 offen offset:128 lds\ns_add_u32 s88,s72,s59\ns_addc_u32 s89,s73,0\ns_add_u32 m0,s22,18432\ns_nop 0\nbuffer_load_dwordx4 v21,s[92:95],s47 offen offset:0 lds\ns_add_u32 m0,s22,19328\ns_nop 0\n" \
"buffer_load_dwordx4 v21,s[92:95],s47 offen offset:128 lds\ns_mov_b64 exec,0xff\nglobal_load_dword v0,v1,s[84:85]\ns_mov_b64 exec,-1\ns_mov_b64 exec,0xff00\nglobal_load_dword v0,v1,s[84:85]\ns_mov_b64 exec,-1\ns_mov_b64 exec,0xff\n" \
"global_load_dword v0,v2,s[96:97]\ns_mov_b64 exec,-1\ns_mov_b64 exec,0xff00\nglobal_load_dword v0,v2,s[96:97]\ns_mov_b64 exec,-1\ns_mov_b64 exec,0\ns_cmp_eq_u32 %2,0\ns_cbranch_scc1 LG0_%=\n" \
"s_cmp_eq_u32 %2,1\ns_cbranch_scc1 LG1_%=\ns_cmp_eq_u32 %2,2\ns_cbranch_scc1 LG2_%=\ns_cmp_eq_u32 %2,3\ns_cbranch_scc1 LG3_%=\nLHR0_%=:\ns_mov_b64 exec,-1\n" \
"s_waitcnt vmcnt(8)\ns_barrier\ns_branch LBEG_%=\n.p2align 12\nLBEG_%=:\ns_bitcmp1_b32 %2,0\ns_cbranch_scc1 LAV_%=\nds_read_b64_tr_b16 a[32:33],v9 offset:0\n" \
"ds_read_b64_tr_b16 a[34:35],v10 offset:0\nds_read_b64_tr_b16 a[36:37],v11 offset:0\nds_read_b64_tr_b16 a[38:39],v12 offset:0\nds_read_b128 a[24:27],v7 offset:12288\nds_read_b128 a[28:31],v7 offset:14336\ns_waitcnt lgkmcnt(0)\nM v[128:131],a[32:35],a[24:27],0\ns_add_u32 m0,s22,32768\n" \
"s_mov_b32 s47,524288\ns_mov_b32 s46,128\nds_read_b64_tr_b16 a[40:41],v9 offset:512\nds_read_b64_tr_b16 a[42:43],v10 offset:512\nds_read_b64_tr_b16 a[44:45],v11 offset:512\nds_read_b64_tr_b16 a[46:47],v12 offset:512\nds_read_b64_tr_b16 a[48:49],v9 offset:1024\nds_read_b64_tr_b16 a[50:51],v10 offset:1024\n" \
"ds_read_b64_tr_b16 a[52:53],v11 offset:1024\nds_read_b64_tr_b16 a[54:55],v12 offset:1024\nM v[132:135],a[36:39],a[24:27],0\nbuffer_load_dwordx4 v21,s[32:35],s47 offen offset:0 lds\nds_read_b64_tr_b16 a[56:57],v9 offset:1536\nds_read_b64_tr_b16 a[58:59],v10 offset:1536\nds_read_b64_tr_b16 a[60:61],v11 offset:1536\nds_read_b64_tr_b16 a[62:63],v12 offset:1536\n" \
"M v[140:143],a[36:39],a[28:31],0\ns_add_u32 m0,s22,33664\nM v[136:139],a[32:35],a[28:31],0\ns_waitcnt lgkmcnt(8)\ns_mov_b64 s[40:41],s[66:67]\nds_read_b128 a[16:19],v7 offset:8192\nM v[152:155],a[40:43],a[28:31],0\ns_and_b32 s41,s41,0xffff\n" \
"ds_read_b128 a[20:23],v7 offset:10240\nM v[156:159],a[44:47],a[28:31],0\nbuffer_load_dwordx4 v21,s[32:35],s47 offen offset:128 lds\nM v[148:151],a[44:47],a[24:27],0\ns_add_u32 m0,s22,34816\nds_read_b128 a[0:3],v7 offset:0\nM v[144:147],a[40:43],a[24:27],0\ns_waitcnt lgkmcnt(3)\n" \
"s_mov_b32 s42,4194304\nds_read_b128 a[4:7],v7 offset:2048\nM v[160:163],a[48:51],a[24:27],0\ns_mov_b32 s43,0x00020000\nds_read_b128 a[8:11],v7 offset:4096\nM v[164:167],a[52:55],a[24:27],0\nbuffer_load_dwordx4 v21,s[92:95],s47 offen offset:0 lds\nM v[172:175],a[52:55],a[28:31],0\n" \
"s_add_u32 m0,s22,35712\nM v[168:171],a[48:51],a[28:31],0\ns_mov_b64 s[80:81],s[68:69]\nds_read_b128 a[12:15],v7 offset:6144\nM v[184:187],a[56:59],a[28:31],0\ns_mov_b32 s52,0\nds_read_b128 a[88:91],v8 offset:12288\nM v[188:191],a[60:63],a[28:31],0\n" \
"buffer_load_dwordx4 v21,s[92:95],s47 offen offset:128 lds\nM v[180:183],a[60:63],a[24:27],0\ns_add_u32 m0,s50,0\nds_read_b128 a[92:95],v8 offset:14336\nM v[176:179],a[56:59],a[24:27],0\ns_waitcnt lgkmcnt(3)\ns_mov_b32 s47,786432\nds_read_b128 a[80:83],v8 offset:8192\n" \
"M v[64:67],a[32:35],a[16:19],0\ns_waitcnt vmcnt(8) lgkmcnt(9)\nds_read_b128 a[84:87],v8 offset:10240\nM v[68:71],a[36:39],a[16:19],0\nbuffer_load_dwordx4 v13,s[28:31],s46 offen lds\ns_barrier\nM v[76:79],a[36:39],a[20:23],0\ns_add_u32 m0,s50,4096\n" \
"M v[72:75],a[32:35],a[20:23],0\ns_and_b32 s81,s81,0xffff\nds_read_b64_tr_b16 a[96:97],v9 offset:16384\nds_read_b64_tr_b16 a[98:99],v10 offset:16384\nM v[88:91],a[40:43],a[20:23],0\ns_mov_b32 s82,4194304\nds_read_b64_tr_b16 a[100:101],v11 offset:16384\nM v[92:95],a[44:47],a[20:23],0\n" \
"buffer_load_dwordx4 v14,s[28:31],s46 offen lds\nM v[84:87],a[44:47],a[16:19],0\ns_add_u32 m0,s50,8192\nds_read_b64_tr_b16 a[102:103],v12 offset:16384\nM v[80:83],a[40:43],a[16:19],0\ns_mov_b32 s83,0x00020000\nds_read_b64_tr_b16 a[104:105],v9 offset:16896\nM v[96:99],a[48:51],a[16:19],0\n" \
"s_lshl_b32 s60,s7,18\nds_read_b64_tr_b16 a[106:107],v10 offset:16896\nM v[100:103],a[52:55],a[16:19],0\nbuffer_load_dwordx4 v15,s[28:31],s46 offen lds\nM v[108:111],a[52:55],a[20:23],0\ns_add_u32 m0,s50,12288\nds_read_b64_tr_b16 a[108:109],v11 offset:16896\nM v[104:107],a[48:51],a[20:23],0\n" \
"s_sub_u32 s61,3932160,s60\nds_read_b64_tr_b16 a[110:111],v12 offset:16896\nM v[120:123],a[56:59],a[20:23],0\ns_add_u32 s44,s78,s98\ns_addc_u32 s45,s79,0\nds_read_b64_tr_b16 a[112:113],v9 offset:17408\nds_read_b64_tr_b16 a[114:115],v10 offset:17408\nM v[124:127],a[60:63],a[20:23],0\n" \
"buffer_load_dwordx4 v16,s[28:31],s46 offen lds\nM v[116:119],a[60:63],a[16:19],0\ns_add_u32 m0,s50,16384\ns_mov_b32 s46,128\nM v[112:115],a[56:59],a[16:19],0\ns_and_b32 s45,s45,0xffff\nds_read_b64_tr_b16 a[116:117],v11 offset:17408\nds_read_b64_tr_b16 a[118:119],v12 offset:17408\n" \
"M v[192:195],a[32:35],a[0:3],0\ns_mov_b64 s[36:37],s[90:91]\nds_read_b64_tr_b16 a[120:121],v9 offset:17920\nM v[196:199],a[36:39],a[0:3],0\nbuffer_load_dwordx4 v17,s[28:31],s46 offen lds\nM v[204:207],a[36:39],a[4:7],0\ns_add_u32 m0,s50,20480\nds_read_b64_tr_b16 a[122:123],v10 offset:17920\n" \
"M v[200:203],a[32:35],a[4:7],0\ns_and_b32 s37,s37,0xffff\nds_read_b64_tr_b16 a[124:125],v11 offset:17920\nM v[216:219],a[40:43],a[4:7],0\ns_mov_b32 s38,134217728\nds_read_b64_tr_b16 a[126:127],v12 offset:17920\nM v[220:223],a[44:47],a[4:7],0\nbuffer_load_dwordx4 v18,s[28:31],s46 offen lds\n" \
"M v[212:215],a[44:47],a[0:3],0\ns_add_u32 m0,s50,24576\nds_read_b128 a[64:67],v8 offset:0\nM v[208:211],a[40:43],a[0:3],0\ns_mov_b32 s39,0x00020000\nds_read_b128 a[68:71],v8 offset:2048\nM v[224:227],a[48:51],a[0:3],0\ns_add_u32 s96,s57,32768\n" \
"ds_read_b128 a[72:75],v8 offset:4096\nM v[228:231],a[52:55],a[0:3],0\nbuffer_load_dwordx4 v19,s[28:31],s46 offen lds\nM v[236:239],a[52:55],a[4:7],0\ns_add_u32 m0,s50,28672\nM v[232:235],a[48:51],a[4:7],0\ns_lshr_b32 s62,%1,3\nds_read_b128 a[76:79],v8 offset:6144\n" \
"M v[248:251],a[56:59],a[4:7],0\ns_lshl_b32 s62,s62,2\nM v[252:255],a[60:63],a[4:7],0\nbuffer_load_dwordx4 v20,s[28:31],s46 offen lds\nM v[244:247],a[60:63],a[0:3],0\ns_add_u32 m0,s22,49152\nM v[240:243],a[56:59],a[0:3],0\ns_waitcnt lgkmcnt(1)\n" \
"s_mov_b32 s46,256\nM a[128:131],a[32:35],a[8:11],0\ns_add_u32 s62,s62,%2\nM a[132:135],a[36:39],a[8:11],0\nbuffer_load_dwordx4 v21,s[32:35],s47 offen offset:0 lds\nM a[140:143],a[36:39],a[12:15],0\ns_add_u32 m0,s22,50048\nM a[136:139],a[32:35],a[12:15],0\n" \
"s_lshl_b32 s62,s62,10\nM a[152:155],a[40:43],a[12:15],0\ns_min_u32 s62,s62,LEND_%=-LBEG_%=-1024\nM a[156:159],a[44:47],a[12:15],0\nbuffer_load_dwordx4 v21,s[32:35],s47 offen offset:128 lds\nM a[148:151],a[44:47],a[8:11],0\ns_add_u32 m0,s22,51200\nM a[144:147],a[40:43],a[8:11],0\n" \
"s_add_u32 s100,s100,s62\ns_addc_u32 s101,s101,0\nM a[160:163],a[48:51],a[8:11],0\ns_lshl_b32 s62,s8,2\nM a[164:167],a[52:55],a[8:11],0\nbuffer_load_dwordx4 v21,s[92:95],s47 offen offset:0 lds\nM a[172:175],a[52:55],a[12:15],0\ns_add_u32 m0,s22,52096\n" \
"M a[168:171],a[48:51],a[12:15],0\ns_add_u32 s62,s62,%2\nM a[184:187],a[56:59],a[12:15],0\ns_mov_b64 s[10:11],s[72:73]\nM a[188:191],a[60:63],a[12:15],0\nbuffer_load_dwordx4 v21,s[92:95],s47 offen offset:128 lds\nM a[180:183],a[60:63],a[8:11],0\ns_add_u32 m0,s50,32768\n" \
"M a[176:179],a[56:59],a[8:11],0\ns_mov_b32 s47,1048576\nM v[128:131],a[96:99],a[88:91],v[128:131]\ns_lshl_b32 s63,s62,12\nM v[132:135],a[100:103],a[88:91],v[132:135]\nbuffer_load_dwordx4 v13,s[28:31],s46 offen lds\nM v[140:143],a[100:103],a[92:95],v[140:143]\ns_add_u32 m0,s50,36864\n" \
"M v[136:139],a[96:99],a[92:95],v[136:139]\ns_add_u32 s68,s10,s63\ns_addc_u32 s69,s11,0\nM v[152:155],a[104:107],a[92:95],v[152:155]\ns_and_b32 s69,s69,0xffff\nM v[156:159],a[108:111],a[92:95],v[156:159]\nbuffer_load_dwordx4 v14,s[28:31],s46 offen lds\nM v[148:151],a[108:111],a[88:91],v[148:151]\n" \
"s_add_u32 m0,s50,40960\nM v[144:147],a[104:107],a[88:91],v[144:147]\ns_lshl_b32 s62,s62,1\nM v[160:163],a[112:115],a[88:91],v[160:163]\ns_mov_b64 s[12:13],s[74:75]\nM v[164:167],a[116:119],a[88:91],v[164:167]\nbuffer_load_dwordx4 v15,s[28:31],s46 offen lds\nM v[172:175],a[116:119],a[92:95],v[172:175]\n" \
"s_add_u32 m0,s50,45056\nM v[168:171],a[112:115],a[92:95],v[168:171]\ns_sub_u32 s14,s18,2000\ns_subb_u32 s15,s19,0\nM v[184:187],a[120:123],a[92:95],v[184:187]\ns_add_u32 s16,s14,16777216\ns_addc_u32 s17,s15,0\nM v[188:191],a[124:127],a[92:95],v[188:191]\n" \
"buffer_load_dwordx4 v16,s[28:31],s46 offen lds\nM v[180:183],a[124:127],a[88:91],v[180:183]\ns_add_u32 m0,s50,49152\nM v[176:179],a[120:123],a[88:91],v[176:179]\ns_mov_b32 s46,256\nM v[64:67],a[96:99],a[80:83],v[64:67]\ns_lshr_b32 s21,s63,5\nM v[68:71],a[100:103],a[80:83],v[68:71]\n" \
"buffer_load_dwordx4 v17,s[28:31],s46 offen lds\nM v[76:79],a[100:103],a[84:87],v[76:79]\ns_add_u32 m0,s50,53248\nM v[72:75],a[96:99],a[84:87],v[72:75]\ns_mov_b64 s[4:5],s[90:91]\nM v[88:91],a[104:107],a[84:87],v[88:91]\ns_waitcnt vmcnt(9) lgkmcnt(0)\nM v[92:95],a[108:111],a[84:87],v[92:95]\n" \
"s_add_u32 s63,s63,79691776\ns_barrier\nds_read_b64_tr_b16 a[32:33],v9 offset:32768\nM v[84:87],a[108:111],a[80:83],v[84:87]\nbuffer_load_dwordx4 v18,s[28:31],s46 offen lds\nM v[80:83],a[104:107],a[80:83],v[80:83]\ns_add_u32 m0,s50,57344\nds_read_b64_tr_b16 a[34:35],v10 offset:32768\n" \
"M v[96:99],a[112:115],a[80:83],v[96:99]\ns_add_u32 s72,s4,s63\ns_addc_u32 s73,s5,0\nds_read_b64_tr_b16 a[36:37],v11 offset:32768\nds_read_b64_tr_b16 a[38:39],v12 offset:32768\nM v[100:103],a[116:119],a[80:83],v[100:103]\ns_mov_b32 s63,65536\nds_read_b128 a[24:27],v5 offset:12288\n" \
"M v[108:111],a[116:119],a[84:87],v[108:111]\nbuffer_load_dwordx4 v19,s[28:31],s46 offen lds\nM v[104:107],a[112:115],a[84:87],v[104:107]\ns_add_u32 m0,s50,61440\nM v[120:123],a[120:123],a[84:87],v[120:123]\ns_cmp_lt_u32 %2,2\ns_cselect_b32 s63,32768,s63\nds_read_b128 a[28:31],v5 offset:14336\n" \
"M v[124:127],a[124:127],a[84:87],v[124:127]\ns_lshl_b32 s56,%2,14\nds_read_b64_tr_b16 a[40:41],v9 offset:33280\nds_read_b64_tr_b16 a[42:43],v10 offset:33280\nM v[116:119],a[124:127],a[80:83],v[116:119]\nbuffer_load_dwordx4 v20,s[28:31],s46 offen lds\nM v[112:115],a[120:123],a[80:83],v[112:115]\ns_add_u32 m0,s22,0\n" \
"M v[192:195],a[96:99],a[64:67],v[192:195]\ns_mov_b32 s46,384\nds_read_b64_tr_b16 a[44:45],v11 offset:33280\nds_read_b64_tr_b16 a[46:47],v12 offset:33280\nM v[196:199],a[100:103],a[64:67],v[196:199]\ns_add_u32 s56,s56,s63\nds_read_b64_tr_b16 a[48:49],v9 offset:33792\nM v[204:207],a[100:103],a[68:71],v[204:207]\n" \
"buffer_load_dwordx4 v21,s[32:35],s47 offen offset:0 lds\nM v[200:203],a[96:99],a[68:71],v[200:203]\ns_add_u32 m0,s22,896\nds_read_b64_tr_b16 a[50:51],v10 offset:33792\nM v[216:219],a[104:107],a[68:71],v[216:219]\ns_and_b32 s73,s73,0xffff\nds_read_b64_tr_b16 a[52:53],v11 offset:33792\nM v[220:223],a[108:111],a[68:71],v[220:223]\n" \
"s_mov_b32 s74,4096\nds_read_b64_tr_b16 a[54:55],v12 offset:33792\nM v[212:215],a[108:111],a[64:67],v[212:215]\nbuffer_load_dwordx4 v21,s[32:35],s47 offen offset:128 lds\nM v[208:211],a[104:107],a[64:67],v[208:211]\ns_add_u32 m0,s22,2048\nds_read_b64_tr_b16 a[56:57],v9 offset:34304\nM v[224:227],a[112:115],a[64:67],v[224:227]\n" \
"s_mov_b32 s75,0x00020000\nds_read_b64_tr_b16 a[58:59],v10 offset:34304\nM v[228:231],a[116:119],a[64:67],v[228:231]\ns_add_u32 s84,s84,1048576\ns_addc_u32 s85,s85,0\nds_read_b64_tr_b16 a[60:61],v11 offset:34304\nds_read_b64_tr_b16 a[62:63],v12 offset:34304\nM v[236:239],a[116:119],a[68:71],v[236:239]\n" \
"buffer_load_dwordx4 v21,s[92:95],s47 offen offset:0 lds\nM v[232:235],a[112:115],a[68:71],v[232:235]\ns_add_u32 m0,s22,2944\nM v[248:251],a[120:123],a[68:71],v[248:251]\ns_add_u32 s21,s21,67108864\nds_read_b128 a[16:19],v5 offset:8192\nM v[252:255],a[124:127],a[68:71],v[252:255]\ns_mov_b32 s53,s22\n" \
"ds_read_b128 a[20:23],v5 offset:10240\nM v[244:247],a[124:127],a[64:67],v[244:247]\ns_mov_b64 exec,0xff\nglobal_load_dword v0,v1,s[84:85]\ns_mov_b64 exec,-1\nM v[240:243],a[120:123],a[64:67],v[240:243]\nds_read_b128 a[0:3],v5 offset:0\nds_read_b128 a[4:7],v5 offset:2048\n" \
"M a[128:131],a[96:99],a[72:75],a[128:131]\ns_mov_b64 exec,0xff00\nglobal_load_dword v0,v1,s[84:85]\ns_mov_b64 exec,-1\nM a[132:135],a[100:103],a[72:75],a[132:135]\ns_add_u32 s84,s84,1048576\ns_addc_u32 s85,s85,0\nds_read_b128 a[8:11],v5 offset:4096\n" \
"ds_read_b128 a[12:15],v5 offset:6144\nM a[140:143],a[100:103],a[76:79],a[140:143]\nglobal_load_dwordx4 v[46:49],v4,s[86:87] offset:0\nM a[136:139],a[96:99],a[76:79],a[136:139]\nds_read_b128 a[88:91],v6 offset:12288\nds_read_b128 a[92:95],v6 offset:14336\nM a[152:155],a[104:107],a[76:79],a[152:155]\nds_read_b128 a[64:67],v6 offset:0\n" \
"M a[156:159],a[108:111],a[76:79],a[156:159]\nds_read_b128 a[68:71],v6 offset:2048\nM a[148:151],a[108:111],a[72:75],a[148:151]\nglobal_load_dwordx4 v[50:53],v4,s[86:87] offset:1024\nM a[144:147],a[104:107],a[72:75],a[144:147]\nds_read_b128 a[80:83],v6 offset:8192\nds_read_b128 a[84:87],v6 offset:10240\nM a[160:163],a[112:115],a[72:75],a[160:163]\n" \
"M a[164:167],a[116:119],a[72:75],a[164:167]\nM a[172:175],a[116:119],a[76:79],a[172:175]\nglobal_load_dwordx4 v[30:33],v26,s[64:65] offset:0\nM a[168:171],a[112:115],a[76:79],a[168:171]\nM a[184:187],a[120:123],a[76:79],a[184:187]\nM a[188:191],a[124:127],a[76:79],a[188:191]\nM a[180:183],a[124:127],a[72:75],a[180:183]\nglobal_load_dwordx4 v[34:37],v26,s[64:65] offset:1024\n" \
"M a[176:179],a[120:123],a[72:75],a[176:179]\ns_waitcnt lgkmcnt(0)\nds_read_b128 a[72:75],v6 offset:4096\nds_read_b128 a[76:79],v6 offset:6144\nM v[128:131],a[32:35],a[24:27],v[128:131]\nM v[132:135],a[36:39],a[24:27],v[132:135]\nM v[140:143],a[36:39],a[28:31],v[140:143]\nglobal_load_dwordx4 v[38:41],v26,s[64:65] offset:2048\n" \
"M v[136:139],a[32:35],a[28:31],v[136:139]\nM v[152:155],a[40:43],a[28:31],v[152:155]\nM v[156:159],a[44:47],a[28:31],v[156:159]\nM v[148:151],a[44:47],a[24:27],v[148:151]\nglobal_load_dwordx4 v[42:45],v26,s[64:65] offset:3072\nM v[144:147],a[40:43],a[24:27],v[144:147]\nM v[160:163],a[48:51],a[24:27],v[160:163]\nM v[164:167],a[52:55],a[24:27],v[164:167]\n" \
"M v[172:175],a[52:55],a[28:31],v[172:175]\nbuffer_load_dwordx4 v21,s[92:95],s47 offen offset:128 lds\nM v[168:171],a[48:51],a[28:31],v[168:171]\ns_add_u32 m0,s22,16384\nM v[184:187],a[56:59],a[28:31],v[184:187]\ns_waitcnt vmcnt(20) lgkmcnt(14)\nM v[188:191],a[60:63],a[28:31],v[188:191]\ns_mov_b32 s47,1310720\n" \
"s_barrier\nds_read_b64_tr_b16 a[96:97],v9 offset:49152\nM v[180:183],a[60:63],a[24:27],v[180:183]\nbuffer_load_dwordx4 v21,s[32:35],s47 offen offset:0 lds\nM v[176:179],a[56:59],a[24:27],v[176:179]\ns_add_u32 m0,s22,17280\nds_read_b64_tr_b16 a[98:99],v10 offset:49152\nM v[64:67],a[32:35],a[16:19],v[64:67]\n" \
"ds_read_b64_tr_b16 a[100:101],v11 offset:49152\nds_read_b64_tr_b16 a[102:103],v12 offset:49152\nM v[68:71],a[36:39],a[16:19],v[68:71]\nds_read_b64_tr_b16 a[104:105],v9 offset:49664\nM v[76:79],a[36:39],a[20:23],v[76:79]\nbuffer_load_dwordx4 v21,s[32:35],s47 offen offset:128 lds\nM v[72:75],a[32:35],a[20:23],v[72:75]\ns_add_u32 m0,s22,18432\n" \
"ds_read_b64_tr_b16 a[106:107],v10 offset:49664\nM v[88:91],a[40:43],a[20:23],v[88:91]\nds_read_b64_tr_b16 a[108:109],v11 offset:49664\nM v[92:95],a[44:47],a[20:23],v[92:95]\nds_read_b64_tr_b16 a[110:111],v12 offset:49664\nM v[84:87],a[44:47],a[16:19],v[84:87]\nbuffer_load_dwordx4 v21,s[92:95],s47 offen offset:0 lds\nM v[80:83],a[40:43],a[16:19],v[80:83]\n" \
"s_add_u32 m0,s22,19328\nds_read_b64_tr_b16 a[112:113],v9 offset:50176\nM v[96:99],a[48:51],a[16:19],v[96:99]\nds_read_b64_tr_b16 a[114:115],v10 offset:50176\nM v[100:103],a[52:55],a[16:19],v[100:103]\nds_read_b64_tr_b16 a[116:117],v11 offset:50176\nds_read_b64_tr_b16 a[118:119],v12 offset:50176\nM v[108:111],a[52:55],a[20:23],v[108:111]\n" \
"buffer_load_dwordx4 v21,s[92:95],s47 offen offset:128 lds\nM v[104:107],a[48:51],a[20:23],v[104:107]\ns_add_u32 m0,s50,65536\nM v[120:123],a[56:59],a[20:23],v[120:123]\ns_mov_b32 s47,1572864\nds_read_b64_tr_b16 a[120:121],v9 offset:50688\nds_read_b64_tr_b16 a[122:123],v10 offset:50688\nM v[124:127],a[60:63],a[20:23],v[124:127]\n" \
"ds_read_b64_tr_b16 a[124:125],v11 offset:50688\nM v[116:119],a[60:63],a[16:19],v[116:119]\nds_read_b64_tr_b16 a[126:127],v12 offset:50688\nM v[112:115],a[56:59],a[16:19],v[112:115]\nM v[192:195],a[32:35],a[0:3],v[192:195]\nM v[196:199],a[36:39],a[0:3],v[196:199]\nM v[204:207],a[36:39],a[4:7],v[204:207]\nM v[200:203],a[32:35],a[4:7],v[200:203]\n" \
"M v[216:219],a[40:43],a[4:7],v[216:219]\nbuffer_load_dwordx4 v13,s[28:31],s46 offen lds\nM v[220:223],a[44:47],a[4:7],v[220:223]\ns_add_u32 m0,s50,69632\nM v[212:215],a[44:47],a[0:3],v[212:215]\nM v[208:211],a[40:43],a[0:3],v[208:211]\nM v[224:227],a[48:51],a[0:3],v[224:227]\nbuffer_load_dwordx4 v14,s[28:31],s46 offen lds\n" \
"M v[228:231],a[52:55],a[0:3],v[228:231]\ns_add_u32 m0,s50,73728\nM v[236:239],a[52:55],a[4:7],v[236:239]\nM v[232:235],a[48:51],a[4:7],v[232:235]\nM v[248:251],a[56:59],a[4:7],v[248:251]\nbuffer_load_dwordx4 v15,s[28:31],s46 offen lds\nM v[252:255],a[60:63],a[4:7],v[252:255]\ns_add_u32 m0,s50,77824\n" \
"M v[244:247],a[60:63],a[0:3],v[244:247]\nM v[240:243],a[56:59],a[0:3],v[240:243]\nM a[128:131],a[32:35],a[8:11],a[128:131]\nbuffer_load_dwordx4 v16,s[28:31],s46 offen lds\nM a[132:135],a[36:39],a[8:11],a[132:135]\ns_add_u32 m0,s50,81920\nM a[140:143],a[36:39],a[12:15],a[140:143]\ns_mov_b32 s46,384\n" \
"M a[136:139],a[32:35],a[12:15],a[136:139]\nM a[152:155],a[40:43],a[12:15],a[152:155]\nbuffer_load_dwordx4 v17,s[28:31],s46 offen lds\nM a[156:159],a[44:47],a[12:15],a[156:159]\ns_add_u32 m0,s50,86016\nM a[148:151],a[44:47],a[8:11],a[148:151]\nM a[144:147],a[40:43],a[8:11],a[144:147]\nM a[160:163],a[48:51],a[8:11],a[160:163]\n" \
"buffer_load_dwordx4 v18,s[28:31],s46 offen lds\nM a[164:167],a[52:55],a[8:11],a[164:167]\ns_add_u32 m0,s50,90112\nM a[172:175],a[52:55],a[12:15],a[172:175]\nM a[168:171],a[48:51],a[12:15],a[168:171]\nM a[184:187],a[56:59],a[12:15],a[184:187]\nbuffer_load_dwordx4 v19,s[28:31],s46 offen lds\nM a[188:191],a[60:63],a[12:15],a[188:191]\n" \
"s_add_u32 m0,s50,94208\nM a[180:183],a[60:63],a[8:11],a[180:183]\nM a[176:179],a[56:59],a[8:11],a[176:179]\ns_waitcnt lgkmcnt(0)\nM v[128:131],a[96:99],a[88:91],v[128:131]\nbuffer_load_dwordx4 v20,s[28:31],s46 offen lds\nM v[132:135],a[100:103],a[88:91],v[132:135]\ns_add_u32 m0,s22,32768\n" \
"M v[140:143],a[100:103],a[92:95],v[140:143]\ns_waitcnt vmcnt(12) lgkmcnt(0)\nM v[136:139],a[96:99],a[92:95],v[136:139]\ns_mov_b32 s46,512\ns_barrier\nds_read_b64_tr_b16 a[32:33],v9 offset:0\nM v[152:155],a[104:107],a[92:95],v[152:155]\nbuffer_load_dwordx4 v21,s[32:35],s47 offen offset:0 lds\n" \
"M v[156:159],a[108:111],a[92:95],v[156:159]\ns_add_u32 m0,s22,33664\nds_read_b64_tr_b16 a[34:35],v10 offset:0\nM v[148:151],a[108:111],a[88:91],v[148:151]\nds_read_b64_tr_b16 a[36:37],v11 offset:0\nds_read_b64_tr_b16 a[38:39],v12 offset:0\nM v[144:147],a[104:107],a[88:91],v[144:147]\nds_read_b64_tr_b16 a[40:41],v9 offset:512\n" \
"M v[160:163],a[112:115],a[88:91],v[160:163]\nbuffer_load_dwordx4 v21,s[32:35],s47 offen offset:128 lds\nM v[164:167],a[116:119],a[88:91],v[164:167]\ns_add_u32 m0,s22,34816\nds_read_b64_tr_b16 a[42:43],v10 offset:512\nM v[172:175],a[116:119],a[92:95],v[172:175]\nds_read_b64_tr_b16 a[44:45],v11 offset:512\nM v[168:171],a[112:115],a[92:95],v[168:171]\n" \
"ds_read_b64_tr_b16 a[46:47],v12 offset:512\nM v[184:187],a[120:123],a[92:95],v[184:187]\nbuffer_load_dwordx4 v21,s[92:95],s47 offen offset:0 lds\nM v[188:191],a[124:127],a[92:95],v[188:191]\ns_add_u32 m0,s22,35712\nds_read_b64_tr_b16 a[48:49],v9 offset:1024\nM v[180:183],a[124:127],a[88:91],v[180:183]\nds_read_b64_tr_b16 a[50:51],v10 offset:1024\n" \
"M v[176:179],a[120:123],a[88:91],v[176:179]\nds_read_b64_tr_b16 a[52:53],v11 offset:1024\nds_read_b64_tr_b16 a[54:55],v12 offset:1024\nM v[64:67],a[96:99],a[80:83],v[64:67]\nbuffer_load_dwordx4 v21,s[92:95],s47 offen offset:128 lds\nM v[68:71],a[100:103],a[80:83],v[68:71]\ns_add_u32 m0,s22,49152\nM v[76:79],a[100:103],a[84:87],v[76:79]\n" \
"s_mov_b32 s47,1835008\nds_read_b64_tr_b16 a[56:57],v9 offset:1536\nds_read_b64_tr_b16 a[58:59],v10 offset:1536\nM v[72:75],a[96:99],a[84:87],v[72:75]\nds_read_b64_tr_b16 a[60:61],v11 offset:1536\nM v[88:91],a[104:107],a[84:87],v[88:91]\nbuffer_load_dwordx4 v21,s[32:35],s47 offen offset:0 lds\nM v[92:95],a[108:111],a[84:87],v[92:95]\n" \
"s_add_u32 m0,s22,50048\nds_read_b64_tr_b16 a[62:63],v12 offset:1536\nM v[84:87],a[108:111],a[80:83],v[84:87]\nds_read_b128 a[24:27],v5 offset:45056\nM v[80:83],a[104:107],a[80:83],v[80:83]\nds_read_b128 a[28:31],v5 offset:47104\nM v[96:99],a[112:115],a[80:83],v[96:99]\nbuffer_load_dwordx4 v21,s[32:35],s47 offen offset:128 lds\n" \
"M v[100:103],a[116:119],a[80:83],v[100:103]\ns_add_u32 m0,s22,51200\nM v[108:111],a[116:119],a[84:87],v[108:111]\nds_read_b128 a[16:19],v5 offset:40960\nM v[104:107],a[112:115],a[84:87],v[104:107]\nds_read_b128 a[20:23],v5 offset:43008\nM v[120:123],a[120:123],a[84:87],v[120:123]\nbuffer_load_dwordx4 v21,s[92:95],s47 offen offset:0 lds\n" \
"M v[124:127],a[124:127],a[84:87],v[124:127]\ns_add_u32 m0,s22,52096\nds_read_b128 a[0:3],v5 offset:32768\nM v[116:119],a[124:127],a[80:83],v[116:119]\nds_read_b128 a[4:7],v5 offset:34816\nM v[112:115],a[120:123],a[80:83],v[112:115]\nds_read_b128 a[8:11],v5 offset:36864\nM v[192:195],a[96:99],a[64:67],v[192:195]\n" \
"LG0_%=:\ns_cbranch_execz LG4_%=\nbuffer_load_dwordx4 v21,s[92:95],s47 offen offset:128 lds\nM v[196:199],a[100:103],a[64:67],v[196:199]\ns_add_u32 m0,s50,0\nM v[204:207],a[100:103],a[68:71],v[204:207]\ns_mov_b32 s47,2097152\nds_read_b128 a[12:15],v5 offset:38912\n" \
"M v[200:203],a[96:99],a[68:71],v[200:203]\nds_read_b128 a[80:83],v6 offset:40960\nM v[216:219],a[104:107],a[68:71],v[216:219]\nbuffer_load_dwordx4 v13,s[28:31],s46 offen lds\nM v[220:223],a[108:111],a[68:71],v[220:223]\ns_add_u32 m0,s50,4096\nds_read_b128 a[84:87],v6 offset:43008\nM v[212:215],a[108:111],a[64:67],v[212:215]\n" \
"ds_read_b128 a[88:91],v6 offset:45056\nM v[208:211],a[104:107],a[64:67],v[208:211]\nds_read_b128 a[92:95],v6 offset:47104\nM v[224:227],a[112:115],a[64:67],v[224:227]\nbuffer_load_dwordx4 v14,s[28:31],s46 offen lds\nM v[228:231],a[116:119],a[64:67],v[228:231]\ns_add_u32 m0,s50,8192\nM v[236:239],a[116:119],a[68:71],v[236:239]\n" \
"M v[232:235],a[112:115],a[68:71],v[232:235]\nM v[248:251],a[120:123],a[68:71],v[248:251]\nM v[252:255],a[124:127],a[68:71],v[252:255]\nbuffer_load_dwordx4 v15,s[28:31],s46 offen lds\nM v[244:247],a[124:127],a[64:67],v[244:247]\ns_add_u32 m0,s50,12288\nM v[240:243],a[120:123],a[64:67],v[240:243]\nds_read_b128 a[64:67],v6 offset:32768\n" \
"ds_read_b128 a[68:71],v6 offset:34816\nM a[128:131],a[96:99],a[72:75],a[128:131]\nM a[132:135],a[100:103],a[72:75],a[132:135]\nbuffer_load_dwordx4 v16,s[28:31],s46 offen lds\nM a[140:143],a[100:103],a[76:79],a[140:143]\ns_add_u32 m0,s50,16384\nM a[136:139],a[96:99],a[76:79],a[136:139]\ns_mov_b32 s46,512\n" \
"M a[152:155],a[104:107],a[76:79],a[152:155]\nM a[156:159],a[108:111],a[76:79],a[156:159]\nM a[148:151],a[108:111],a[72:75],a[148:151]\ns_waitcnt vmcnt(20) lgkmcnt(14)\nM a[144:147],a[104:107],a[72:75],a[144:147]\ns_barrier\nds_read_b64_tr_b16 a[96:97],v9 offset:16384\nM a[160:163],a[112:115],a[72:75],a[160:163]\n" \
"buffer_load_dwordx4 v17,s[28:31],s46 offen lds\nM a[164:167],a[116:119],a[72:75],a[164:167]\ns_add_u32 m0,s50,20480\nds_read_b64_tr_b16 a[98:99],v10 offset:16384\nM a[172:175],a[116:119],a[76:79],a[172:175]\nds_read_b64_tr_b16 a[100:101],v11 offset:16384\nds_read_b64_tr_b16 a[102:103],v12 offset:16384\nM a[168:171],a[112:115],a[76:79],a[168:171]\n" \
"ds_read_b64_tr_b16 a[104:105],v9 offset:16896\nM a[184:187],a[120:123],a[76:79],a[184:187]\nbuffer_load_dwordx4 v18,s[28:31],s46 offen lds\nM a[188:191],a[124:127],a[76:79],a[188:191]\ns_add_u32 m0,s50,24576\nds_read_b64_tr_b16 a[106:107],v10 offset:16896\nM a[180:183],a[124:127],a[72:75],a[180:183]\nds_read_b64_tr_b16 a[108:109],v11 offset:16896\n" \
"M a[176:179],a[120:123],a[72:75],a[176:179]\ns_waitcnt lgkmcnt(5)\nds_read_b128 a[72:75],v6 offset:36864\nM v[128:131],a[32:35],a[24:27],v[128:131]\nbuffer_load_dwordx4 v19,s[28:31],s46 offen lds\nM v[132:135],a[36:39],a[24:27],v[132:135]\ns_add_u32 m0,s50,28672\nds_read_b128 a[76:79],v6 offset:38912\n" \
"M v[140:143],a[36:39],a[28:31],v[140:143]\nds_read_b64_tr_b16 a[110:111],v12 offset:16896\nM v[136:139],a[32:35],a[28:31],v[136:139]\nds_read_b64_tr_b16 a[112:113],v9 offset:17408\nM v[152:155],a[40:43],a[28:31],v[152:155]\nbuffer_load_dwordx4 v20,s[28:31],s46 offen lds\nM v[156:159],a[44:47],a[28:31],v[156:159]\ns_add_u32 m0,s22,0\n" \
"ds_read_b64_tr_b16 a[114:115],v10 offset:17408\nM v[148:151],a[44:47],a[24:27],v[148:151]\ns_mov_b32 s46,640\nds_read_b64_tr_b16 a[116:117],v11 offset:17408\nM v[144:147],a[40:43],a[24:27],v[144:147]\nds_read_b64_tr_b16 a[118:119],v12 offset:17408\nM v[160:163],a[48:51],a[24:27],v[160:163]\nbuffer_load_dwordx4 v21,s[32:35],s47 offen offset:0 lds\n" \
"M v[164:167],a[52:55],a[24:27],v[164:167]\ns_add_u32 m0,s22,896\nds_read_b64_tr_b16 a[120:121],v9 offset:17920\nM v[172:175],a[52:55],a[28:31],v[172:175]\nds_read_b64_tr_b16 a[122:123],v10 offset:17920\nM v[168:171],a[48:51],a[28:31],v[168:171]\nds_read_b64_tr_b16 a[124:125],v11 offset:17920\nds_read_b64_tr_b16 a[126:127],v12 offset:17920\n" \
"M v[184:187],a[56:59],a[28:31],v[184:187]\nbuffer_load_dwordx4 v21,s[32:35],s47 offen offset:128 lds\nM v[188:191],a[60:63],a[28:31],v[188:191]\ns_add_u32 m0,s22,2048\nM v[180:183],a[60:63],a[24:27],v[180:183]\nM v[176:179],a[56:59],a[24:27],v[176:179]\nM v[64:67],a[32:35],a[16:19],v[64:67]\nM v[68:71],a[36:39],a[16:19],v[68:71]\n" \
"buffer_load_dwordx4 v21,s[92:95],s47 offen offset:0 lds\nM v[76:79],a[36:39],a[20:23],v[76:79]\ns_add_u32 m0,s22,2944\nM v[72:75],a[32:35],a[20:23],v[72:75]\nM v[88:91],a[40:43],a[20:23],v[88:91]\nM v[92:95],a[44:47],a[20:23],v[92:95]\nbuffer_load_dwordx4 v21,s[92:95],s47 offen offset:128 lds\nM v[84:87],a[44:47],a[16:19],v[84:87]\n" \
"s_add_u32 m0,s22,16384\nM v[80:83],a[40:43],a[16:19],v[80:83]\ns_mov_b32 s47,2359296\nM v[96:99],a[48:51],a[16:19],v[96:99]\nM v[100:103],a[52:55],a[16:19],v[100:103]\ns_mov_b64 exec,0xff\nglobal_load_dword v0,v1,s[84:85]\ns_mov_b64 exec,-1\n" \
"M v[108:111],a[52:55],a[20:23],v[108:111]\nM v[104:107],a[48:51],a[20:23],v[104:107]\ns_mov_b64 exec,0xff00\nglobal_load_dword v0,v1,s[84:85]\ns_mov_b64 exec,-1\nM v[120:123],a[56:59],a[20:23],v[120:123]\ns_add_u32 s84,s84,1048576\ns_addc_u32 s85,s85,0\n" \
"M v[124:127],a[60:63],a[20:23],v[124:127]\nglobal_load_dwordx4 a[230:233],v27,s[70:71] offset:0\nM v[116:119],a[60:63],a[16:19],v[116:119]\nM v[112:115],a[56:59],a[16:19],v[112:115]\nM v[192:195],a[32:35],a[0:3],v[192:195]\nM v[196:199],a[36:39],a[0:3],v[196:199]\nglobal_load_dwordx4 a[234:237],v27,s[70:71] offset:16\nM v[204:207],a[36:39],a[4:7],v[204:207]\n" \
"s_mov_b32 s70,4096\nM v[200:203],a[32:35],a[4:7],v[200:203]\ns_mov_b32 s71,0x00020000\nM v[216:219],a[40:43],a[4:7],v[216:219]\nM v[220:223],a[44:47],a[4:7],v[220:223]\nbuffer_load_dwordx2 a[192:193],v54,s[40:43],0 offen offset:0 nt\nM v[212:215],a[44:47],a[0:3],v[212:215]\nM v[208:211],a[40:43],a[0:3],v[208:211]\n" \
"M v[224:227],a[48:51],a[0:3],v[224:227]\nM v[228:231],a[52:55],a[0:3],v[228:231]\nbuffer_load_dwordx2 a[194:195],v54,s[40:43],0 offen offset:2048 nt\nM v[236:239],a[52:55],a[4:7],v[236:239]\nM v[232:235],a[48:51],a[4:7],v[232:235]\nM v[248:251],a[56:59],a[4:7],v[248:251]\nM v[252:255],a[60:63],a[4:7],v[252:255]\nbuffer_load_dwordx2 a[196:197],v55,s[40:43],0 offen offset:0 nt\n" \
"M v[244:247],a[60:63],a[0:3],v[244:247]\nM v[240:243],a[56:59],a[0:3],v[240:243]\nM a[128:131],a[32:35],a[8:11],a[128:131]\ns_waitcnt vmcnt(40)\nv_lshlrev_b32 v25,16,v46\nv_lshlrev_b32 v26,16,v30\nM a[132:135],a[36:39],a[8:11],a[132:135]\nbuffer_load_dwordx2 a[198:199],v59,s[40:43],s52 offen offset:0 nt\n" \
"M a[140:143],a[36:39],a[12:15],a[140:143]\nv_and_b32 v46,0xffff0000,v46\nv_mul_f32 v23,v26,v25\nM a[136:139],a[32:35],a[12:15],a[136:139]\nv_and_b32 v30,0xffff0000,v30\nv_lshlrev_b32 v26,16,v38\nM a[152:155],a[40:43],a[12:15],a[152:155]\nv_fmac_f32 v23,v30,v46\n" \
"v_mul_f32 v24,v26,v25\nM a[156:159],a[44:47],a[12:15],a[156:159]\nbuffer_load_dwordx2 a[214:215],v59,s[80:83],s52 offen offset:0 nt\nM a[148:151],a[44:47],a[8:11],a[148:151]\ns_waitcnt vmcnt(25) lgkmcnt(0)\nv_lshlrev_b32 v25,16,v47\nv_lshlrev_b32 v26,16,v31\nM a[144:147],a[40:43],a[8:11],a[144:147]\n" \
"s_barrier\nds_read_b64_tr_b16 a[32:33],v9 offset:32768\nM a[160:163],a[48:51],a[8:11],a[160:163]\nds_read_b64_tr_b16 a[34:35],v10 offset:32768\nds_read_b64_tr_b16 a[36:37],v11 offset:32768\nM a[164:167],a[52:55],a[8:11],a[164:167]\nbuffer_load_dwordx2 a[200:201],v59,s[40:43],s52 offen offset:2048 nt\nM a[172:175],a[52:55],a[12:15],a[172:175]\n" \
"ds_read_b64_tr_b16 a[38:39],v12 offset:32768\nds_read_b64_tr_b16 a[40:41],v9 offset:33280\nM a[168:171],a[48:51],a[12:15],a[168:171]\nds_read_b64_tr_b16 a[42:43],v10 offset:33280\nv_and_b32 v38,0xffff0000,v38\nM a[184:187],a[56:59],a[12:15],a[184:187]\nds_read_b64_tr_b16 a[44:45],v11 offset:33280\nds_read_b64_tr_b16 a[46:47],v12 offset:33280\n" \
"M a[188:191],a[60:63],a[12:15],a[188:191]\nbuffer_load_dwordx2 a[216:217],v59,s[80:83],s52 offen offset:2048 nt\nM a[180:183],a[60:63],a[8:11],a[180:183]\ns_mov_b32 s52,4096\nds_read_b64_tr_b16 a[48:49],v9 offset:33792\nds_read_b64_tr_b16 a[50:51],v10 offset:33792\nM a[176:179],a[56:59],a[8:11],a[176:179]\nds_read_b64_tr_b16 a[52:53],v11 offset:33792\n" \
"v_fmac_f32 v23,v26,v25\nM v[128:131],a[96:99],a[88:91],v[128:131]\nds_read_b64_tr_b16 a[54:55],v12 offset:33792\nds_read_b64_tr_b16 a[56:57],v9 offset:34304\nM v[132:135],a[100:103],a[88:91],v[132:135]\nbuffer_load_dwordx2 a[202:203],v59,s[40:43],s52 offen offset:0 nt\nM v[140:143],a[100:103],a[92:95],v[140:143]\nds_read_b64_tr_b16 a[58:59],v10 offset:34304\n" \
"ds_read_b64_tr_b16 a[60:61],v11 offset:34304\nM v[136:139],a[96:99],a[92:95],v[136:139]\nds_read_b64_tr_b16 a[62:63],v12 offset:34304\nv_fmac_f32 v24,v38,v46\nM v[152:155],a[104:107],a[92:95],v[152:155]\nds_read_b128 a[24:27],v7 offset:12288\nv_and_b32 v47,0xffff0000,v47\nM v[156:159],a[108:111],a[92:95],v[156:159]\n" \
"buffer_load_dwordx2 a[218:219],v59,s[80:83],s52 offen offset:0 nt\nM v[148:151],a[108:111],a[88:91],v[148:151]\nds_read_b128 a[28:31],v7 offset:14336\nds_read_b128 a[16:19],v7 offset:8192\nM v[144:147],a[104:107],a[88:91],v[144:147]\nds_read_b128 a[20:23],v7 offset:10240\nv_and_b32 v31,0xffff0000,v31\nM v[160:163],a[112:115],a[88:91],v[160:163]\n" \
"ds_read_b128 a[0:3],v7 offset:0\nv_lshlrev_b32 v26,16,v39\nM v[164:167],a[116:119],a[88:91],v[164:167]\nbuffer_load_dwordx2 a[204:205],v59,s[40:43],s52 offen offset:2048 nt\nM v[172:175],a[116:119],a[92:95],v[172:175]\nds_read_b128 a[4:7],v7 offset:2048\nds_read_b128 a[8:11],v7 offset:4096\nM v[168:171],a[112:115],a[92:95],v[168:171]\n" \
"ds_read_b128 a[12:15],v7 offset:6144\nv_fmac_f32 v23,v31,v47\nM v[184:187],a[120:123],a[92:95],v[184:187]\nv_fmac_f32 v24,v26,v25\nv_lshlrev_b32 v25,16,v48\nM v[188:191],a[124:127],a[92:95],v[188:191]\nbuffer_load_dwordx2 a[220:221],v59,s[80:83],s52 offen offset:2048 nt\nM v[180:183],a[124:127],a[88:91],v[180:183]\n" \
"s_mov_b32 s52,8192\nv_lshlrev_b32 v26,16,v32\nv_and_b32 v39,0xffff0000,v39\nM v[176:179],a[120:123],a[88:91],v[176:179]\nds_read_b128 a[88:91],v8 offset:12288\nds_read_b128 a[92:95],v8 offset:14336\nM v[64:67],a[96:99],a[80:83],v[64:67]\nv_fmac_f32 v23,v26,v25\n" \
"v_fmac_f32 v24,v39,v47\nM v[68:71],a[100:103],a[80:83],v[68:71]\nv_and_b32 v48,0xffff0000,v48\nv_and_b32 v32,0xffff0000,v32\nM v[76:79],a[100:103],a[84:87],v[76:79]\nv_lshlrev_b32 v26,16,v40\nv_fmac_f32 v23,v32,v48\nM v[72:75],a[96:99],a[84:87],v[72:75]\n" \
"v_fmac_f32 v24,v26,v25\nv_lshlrev_b32 v25,16,v49\nM v[88:91],a[104:107],a[84:87],v[88:91]\nv_lshlrev_b32 v26,16,v33\nv_and_b32 v40,0xffff0000,v40\nM v[92:95],a[108:111],a[84:87],v[92:95]\nv_fmac_f32 v23,v26,v25\nv_fmac_f32 v24,v40,v48\n" \
"M v[84:87],a[108:111],a[80:83],v[84:87]\nv_and_b32 v49,0xffff0000,v49\nv_and_b32 v33,0xffff0000,v33\nM v[80:83],a[104:107],a[80:83],v[80:83]\nv_lshlrev_b32 v26,16,v41\nv_fmac_f32 v23,v33,v49\nM v[96:99],a[112:115],a[80:83],v[96:99]\nv_fmac_f32 v24,v26,v25\n" \
"v_lshlrev_b32 v25,16,v50\nM v[100:103],a[116:119],a[80:83],v[100:103]\nv_lshlrev_b32 v26,16,v34\nv_and_b32 v41,0xffff0000,v41\nM v[108:111],a[116:119],a[84:87],v[108:111]\nv_fmac_f32 v23,v26,v25\nv_fmac_f32 v24,v41,v49\nM v[104:107],a[112:115],a[84:87],v[104:107]\n" \
"v_and_b32 v50,0xffff0000,v50\nv_and_b32 v34,0xffff0000,v34\nM v[120:123],a[120:123],a[84:87],v[120:123]\nv_lshlrev_b32 v26,16,v42\nv_fmac_f32 v23,v34,v50\nM v[124:127],a[124:127],a[84:87],v[124:127]\nv_fmac_f32 v24,v26,v25\nv_lshlrev_b32 v25,16,v51\n" \
"M v[116:119],a[124:127],a[80:83],v[116:119]\nv_lshlrev_b32 v26,16,v35\nv_and_b32 v42,0xffff0000,v42\nM v[112:115],a[120:123],a[80:83],v[112:115]\nds_read_b128 a[80:83],v8 offset:8192\nds_read_b128 a[84:87],v8 offset:10240\nM v[192:195],a[96:99],a[64:67],v[192:195]\nv_fmac_f32 v23,v26,v25\n" \
"v_fmac_f32 v24,v42,v50\nM v[196:199],a[100:103],a[64:67],v[196:199]\nv_and_b32 v51,0xffff0000,v51\nv_and_b32 v35,0xffff0000,v35\nM v[204:207],a[100:103],a[68:71],v[204:207]\nv_lshlrev_b32 v26,16,v43\nv_fmac_f32 v23,v35,v51\nM v[200:203],a[96:99],a[68:71],v[200:203]\n" \
"v_fmac_f32 v24,v26,v25\nv_lshlrev_b32 v25,16,v52\nM v[216:219],a[104:107],a[68:71],v[216:219]\nv_lshlrev_b32 v26,16,v36\nv_and_b32 v43,0xffff0000,v43\nM v[220:223],a[108:111],a[68:71],v[220:223]\nv_fmac_f32 v23,v26,v25\nv_fmac_f32 v24,v43,v51\n" \
"M v[212:215],a[108:111],a[64:67],v[212:215]\nv_and_b32 v52,0xffff0000,v52\nv_and_b32 v36,0xffff0000,v36\nM v[208:211],a[104:107],a[64:67],v[208:211]\nv_lshlrev_b32 v26,16,v44\nv_fmac_f32 v23,v36,v52\nM v[224:227],a[112:115],a[64:67],v[224:227]\nv_fmac_f32 v24,v26,v25\n" \
"v_lshlrev_b32 v25,16,v53\nM v[228:231],a[116:119],a[64:67],v[228:231]\nv_lshlrev_b32 v26,16,v37\nv_and_b32 v44,0xffff0000,v44\nM v[236:239],a[116:119],a[68:71],v[236:239]\nv_fmac_f32 v23,v26,v25\nv_fmac_f32 v24,v44,v52\nM v[232:235],a[112:115],a[68:71],v[232:235]\n" \
"v_lshlrev_b32 v26,16,v45\nv_and_b32 v53,0xffff0000,v53\nM v[248:251],a[120:123],a[68:71],v[248:251]\nv_and_b32 v45,0xffff0000,v45\nv_fmac_f32 v24,v26,v25\nM v[252:255],a[124:127],a[68:71],v[252:255]\nv_fmac_f32 v24,v45,v53\nv_and_b32 v37,0xffff0000,v37\n" \
"M v[244:247],a[124:127],a[64:67],v[244:247]\nv_fmac_f32 v23,v37,v53\nM v[240:243],a[120:123],a[64:67],v[240:243]\nds_read_b128 a[64:67],v8 offset:0\nds_read_b128 a[68:71],v8 offset:2048\nM a[128:131],a[96:99],a[72:75],a[128:131]\nv_permlane32_swap_b32 v23,v24\nM a[132:135],a[100:103],a[72:75],a[132:135]\n" \
"v_add_f32 v23,v23,v24\nv_mov_b32 v24,v23\nM a[140:143],a[100:103],a[76:79],a[140:143]\ns_nop 0\nv_permlane16_swap_b32 v23,v24\nM a[136:139],a[96:99],a[76:79],a[136:139]\nv_add_f32 v23,v23,v24\ns_nop 1\n" \
"v_add_f32 v23,v23,v23 row_ror:8 row_mask:0xf bank_mask:0xf\nM a[152:155],a[104:107],a[76:79],a[152:155]\ns_nop 0\nv_add_f32 v23,v23,v23 row_ror:4 row_mask:0xf bank_mask:0xf\ns_nop 1\nv_add_f32 v23,v23,v23 row_ror:2 row_mask:0xf bank_mask:0xf\nM a[156:159],a[108:111],a[76:79],a[156:159]\ns_nop 0\n" \
"v_add_f32 v23,v23,v23 row_ror:1 row_mask:0xf bank_mask:0xf\nC v23,v23,v23\nM a[148:151],a[108:111],a[72:75],a[148:151]\ns_waitcnt vmcnt(27) lgkmcnt(14)\nv_and_b32 v23,0xffff0000,v23\nv_mul_f32 v23,0xbfb8aa3b,v23\nM a[144:147],a[104:107],a[72:75],a[144:147]\ns_barrier\n" \
"ds_read_b64_tr_b16 a[96:97],v9 offset:49152\nM a[160:163],a[112:115],a[72:75],a[160:163]\nds_read_b64_tr_b16 a[98:99],v10 offset:49152\nds_read_b64_tr_b16 a[100:101],v11 offset:49152\nM a[164:167],a[116:119],a[72:75],a[164:167]\nds_read_b64_tr_b16 a[102:103],v12 offset:49152\nv_add_u32 v24,992,v4\nM a[172:175],a[116:119],a[76:79],a[172:175]\n" \
"ds_read_b64_tr_b16 a[104:105],v9 offset:49664\nds_read_b64_tr_b16 a[106:107],v10 offset:49664\nM a[168:171],a[112:115],a[76:79],a[168:171]\nds_read_b64_tr_b16 a[108:109],v11 offset:49664\nM a[184:187],a[120:123],a[76:79],a[184:187]\nds_read_b64_tr_b16 a[110:111],v12 offset:49664\nM a[188:191],a[124:127],a[76:79],a[188:191]\nds_read_b64_tr_b16 a[112:113],v9 offset:50176\n" \
"ds_read_b64_tr_b16 a[114:115],v10 offset:50176\nM a[180:183],a[124:127],a[72:75],a[180:183]\nds_read_b64_tr_b16 a[116:117],v11 offset:50176\nM a[176:179],a[120:123],a[72:75],a[176:179]\ns_waitcnt lgkmcnt(7)\nds_read_b128 a[72:75],v8 offset:4096\nM v[128:131],a[32:35],a[24:27],v[128:131]\nds_read_b128 a[76:79],v8 offset:6144\n" \
"M v[132:135],a[36:39],a[24:27],v[132:135]\nbuffer_load_dwordx4 v21,s[32:35],s47 offen offset:0 lds\nM v[140:143],a[36:39],a[28:31],v[140:143]\ns_add_u32 m0,s22,17280\nds_read_b64_tr_b16 a[118:119],v12 offset:50176\nM v[136:139],a[32:35],a[28:31],v[136:139]\nds_read_b64_tr_b16 a[120:121],v9 offset:50688\nM v[152:155],a[40:43],a[28:31],v[152:155]\n" \
"ds_read_b64_tr_b16 a[122:123],v10 offset:50688\nM v[156:159],a[44:47],a[28:31],v[156:159]\nbuffer_load_dwordx4 v21,s[32:35],s47 offen offset:128 lds\nM v[148:151],a[44:47],a[24:27],v[148:151]\ns_add_u32 m0,s22,18432\nds_read_b64_tr_b16 a[124:125],v11 offset:50688\nM v[144:147],a[40:43],a[24:27],v[144:147]\nds_read_b64_tr_b16 a[126:127],v12 offset:50688\n" \
"M v[160:163],a[48:51],a[24:27],v[160:163]\nv_exp_f32 v23,v23\nM v[164:167],a[52:55],a[24:27],v[164:167]\nbuffer_load_dwordx4 v21,s[92:95],s47 offen offset:0 lds\nM v[172:175],a[52:55],a[28:31],v[172:175]\ns_add_u32 m0,s22,19328\nv_add_f32 v23,1.0,v23\nM v[168:171],a[48:51],a[28:31],v[168:171]\n" \
"v_rcp_f32 v23,v23\nM v[184:187],a[56:59],a[28:31],v[184:187]\nC v23,v23,v23\nv_and_b32 v23,0xffff0000,v23\nM v[188:191],a[60:63],a[28:31],v[188:191]\nbuffer_load_dwordx4 v21,s[92:95],s47 offen offset:128 lds\nM v[180:183],a[60:63],a[24:27],v[180:183]\ns_add_u32 m0,s50,32768\n" \
"M v[176:179],a[56:59],a[24:27],v[176:179]\ns_mov_b32 s47,2621440\nM v[64:67],a[32:35],a[16:19],v[64:67]\nM v[68:71],a[36:39],a[16:19],v[68:71]\nbuffer_load_dwordx4 v13,s[28:31],s46 offen lds\nM v[76:79],a[36:39],a[20:23],v[76:79]\ns_add_u32 m0,s50,36864\nM v[72:75],a[32:35],a[20:23],v[72:75]\n" \
"M v[88:91],a[40:43],a[20:23],v[88:91]\nM v[92:95],a[44:47],a[20:23],v[92:95]\nbuffer_load_dwordx4 v14,s[28:31],s46 offen lds\nM v[84:87],a[44:47],a[16:19],v[84:87]\ns_add_u32 m0,s50,40960\nM v[80:83],a[40:43],a[16:19],v[80:83]\nM v[96:99],a[48:51],a[16:19],v[96:99]\nM v[100:103],a[52:55],a[16:19],v[100:103]\n" \
"buffer_load_dwordx4 v15,s[28:31],s46 offen lds\nM v[108:111],a[52:55],a[20:23],v[108:111]\ns_add_u32 m0,s50,45056\nM v[104:107],a[48:51],a[20:23],v[104:107]\nM v[120:123],a[56:59],a[20:23],v[120:123]\nM v[124:127],a[60:63],a[20:23],v[124:127]\nbuffer_load_dwordx4 v16,s[28:31],s46 offen lds\nM v[116:119],a[60:63],a[16:19],v[116:119]\n" \
"s_add_u32 m0,s50,49152\nM v[112:115],a[56:59],a[16:19],v[112:115]\ns_mov_b32 s46,640\nM v[192:195],a[32:35],a[0:3],v[192:195]\nM v[196:199],a[36:39],a[0:3],v[196:199]\nbuffer_load_dwordx4 v17,s[28:31],s46 offen lds\nM v[204:207],a[36:39],a[4:7],v[204:207]\ns_add_u32 m0,s50,53248\n" \
"M v[200:203],a[32:35],a[4:7],v[200:203]\nM v[216:219],a[40:43],a[4:7],v[216:219]\nM v[220:223],a[44:47],a[4:7],v[220:223]\nbuffer_load_dwordx4 v18,s[28:31],s46 offen lds\nM v[212:215],a[44:47],a[0:3],v[212:215]\ns_add_u32 m0,s50,57344\nM v[208:211],a[40:43],a[0:3],v[208:211]\nM v[224:227],a[48:51],a[0:3],v[224:227]\n" \
"M v[228:231],a[52:55],a[0:3],v[228:231]\nbuffer_load_dwordx4 v19,s[28:31],s46 offen lds\nM v[236:239],a[52:55],a[4:7],v[236:239]\ns_add_u32 m0,s50,61440\nM v[232:235],a[48:51],a[4:7],v[232:235]\nM v[248:251],a[56:59],a[4:7],v[248:251]\nM v[252:255],a[60:63],a[4:7],v[252:255]\nbuffer_load_dwordx4 v20,s[28:31],s46 offen lds\n" \
"M v[244:247],a[60:63],a[0:3],v[244:247]\ns_add_u32 m0,s22,32768\nM v[240:243],a[56:59],a[0:3],v[240:243]\ns_mov_b32 s46,768\nM a[128:131],a[32:35],a[8:11],a[128:131]\nM a[132:135],a[36:39],a[8:11],a[132:135]\nbuffer_load_dwordx4 v21,s[32:35],s47 offen offset:0 lds\nM a[140:143],a[36:39],a[12:15],a[140:143]\n" \
"s_add_u32 m0,s22,33664\nM a[136:139],a[32:35],a[12:15],a[136:139]\nM a[152:155],a[40:43],a[12:15],a[152:155]\nM a[156:159],a[44:47],a[12:15],a[156:159]\nbuffer_load_dwordx4 v21,s[32:35],s47 offen offset:128 lds\nM a[148:151],a[44:47],a[8:11],a[148:151]\ns_waitcnt vmcnt(29) lgkmcnt(0)\nM a[144:147],a[40:43],a[8:11],a[144:147]\n" \
"s_add_u32 m0,s22,34816\ns_barrier\nds_read_b64_tr_b16 a[32:33],v9 offset:0\nM a[160:163],a[48:51],a[8:11],a[160:163]\nds_read_b64_tr_b16 a[34:35],v10 offset:0\nds_read_b64_tr_b16 a[36:37],v11 offset:0\nM a[164:167],a[52:55],a[8:11],a[164:167]\nbuffer_load_dwordx4 v21,s[92:95],s47 offen offset:0 lds\n" \
"M a[172:175],a[52:55],a[12:15],a[172:175]\ns_add_u32 m0,s22,35712\nds_read_b64_tr_b16 a[38:39],v12 offset:0\nM a[168:171],a[48:51],a[12:15],a[168:171]\nds_read_b64_tr_b16 a[40:41],v9 offset:512\nM a[184:187],a[56:59],a[12:15],a[184:187]\nds_read_b64_tr_b16 a[42:43],v10 offset:512\nM a[188:191],a[60:63],a[12:15],a[188:191]\n" \
"buffer_load_dwordx4 v21,s[92:95],s47 offen offset:128 lds\nM a[180:183],a[60:63],a[8:11],a[180:183]\ns_add_u32 m0,s22,49152\nds_read_b64_tr_b16 a[44:45],v11 offset:512\nM a[176:179],a[56:59],a[8:11],a[176:179]\ns_mov_b32 s47,2883584\nds_read_b64_tr_b16 a[46:47],v12 offset:512\nM v[128:131],a[96:99],a[88:91],v[128:131]\n" \
"ds_read_b64_tr_b16 a[48:49],v9 offset:1024\nds_read_b64_tr_b16 a[50:51],v10 offset:1024\nM v[132:135],a[100:103],a[88:91],v[132:135]\nbuffer_load_dwordx4 v21,s[32:35],s47 offen offset:0 lds\nM v[140:143],a[100:103],a[92:95],v[140:143]\ns_add_u32 m0,s22,50048\nM v[136:139],a[96:99],a[92:95],v[136:139]\nds_read_b64_tr_b16 a[52:53],v11 offset:1024\n" \
"ds_read_b64_tr_b16 a[54:55],v12 offset:1024\nM v[152:155],a[104:107],a[92:95],v[152:155]\nds_read_b64_tr_b16 a[56:57],v9 offset:1536\nM v[156:159],a[108:111],a[92:95],v[156:159]\nbuffer_load_dwordx4 v21,s[32:35],s47 offen offset:128 lds\nM v[148:151],a[108:111],a[88:91],v[148:151]\ns_add_u32 m0,s22,51200\nds_read_b64_tr_b16 a[58:59],v10 offset:1536\n" \
"M v[144:147],a[104:107],a[88:91],v[144:147]\nds_read_b64_tr_b16 a[60:61],v11 offset:1536\nM v[160:163],a[112:115],a[88:91],v[160:163]\nds_read_b64_tr_b16 a[62:63],v12 offset:1536\nM v[164:167],a[116:119],a[88:91],v[164:167]\nbuffer_load_dwordx4 v21,s[92:95],s47 offen offset:0 lds\nM v[172:175],a[116:119],a[92:95],v[172:175]\ns_add_u32 m0,s22,52096\n" \
"ds_read_b128 a[24:27],v5 offset:12288\nM v[168:171],a[112:115],a[92:95],v[168:171]\nds_read_b128 a[28:31],v5 offset:14336\nM v[184:187],a[120:123],a[92:95],v[184:187]\nds_read_b128 a[16:19],v5 offset:8192\nM v[188:191],a[124:127],a[92:95],v[188:191]\nbuffer_store_dword v23,v61,s[36:39],0 offen sc1\nM v[180:183],a[124:127],a[88:91],v[180:183]\n" \
"ds_read_b128 a[20:23],v5 offset:10240\nds_read_b128 a[0:3],v5 offset:0\nM v[176:179],a[120:123],a[88:91],v[176:179]\nbuffer_load_dwordx4 v21,s[92:95],s47 offen offset:128 lds\nM v[64:67],a[96:99],a[80:83],v[64:67]\ns_add_u32 m0,s50,65536\nv_add_u32 v23,-32,v4\nM v[68:71],a[100:103],a[80:83],v[68:71]\n" \
"s_mov_b32 s47,3145728\nds_read_b128 a[4:7],v5 offset:2048\nM v[76:79],a[100:103],a[84:87],v[76:79]\nds_read_b128 a[8:11],v5 offset:4096\nM v[72:75],a[96:99],a[84:87],v[72:75]\nLG1_%=:\ns_cbranch_execz LG5_%=\nds_read_b128 a[12:15],v5 offset:6144\n" \
"M v[88:91],a[104:107],a[84:87],v[88:91]\nbuffer_load_dwordx4 v13,s[28:31],s46 offen lds\nM v[92:95],a[108:111],a[84:87],v[92:95]\ns_add_u32 m0,s50,69632\nds_read_b128 a[88:91],v6 offset:12288\nM v[84:87],a[108:111],a[80:83],v[84:87]\nds_read_b128 a[92:95],v6 offset:14336\nM v[80:83],a[104:107],a[80:83],v[80:83]\n" \
"M v[96:99],a[112:115],a[80:83],v[96:99]\nbuffer_load_dwordx4 v14,s[28:31],s46 offen lds\nM v[100:103],a[116:119],a[80:83],v[100:103]\ns_add_u32 m0,s50,73728\nM v[108:111],a[116:119],a[84:87],v[108:111]\nM v[104:107],a[112:115],a[84:87],v[104:107]\nM v[120:123],a[120:123],a[84:87],v[120:123]\nM v[124:127],a[124:127],a[84:87],v[124:127]\n" \
"buffer_load_dwordx4 v15,s[28:31],s46 offen lds\nM v[116:119],a[124:127],a[80:83],v[116:119]\ns_add_u32 m0,s50,77824\nM v[112:115],a[120:123],a[80:83],v[112:115]\nds_read_b128 a[80:83],v6 offset:8192\nds_read_b128 a[84:87],v6 offset:10240\nM v[192:195],a[96:99],a[64:67],v[192:195]\nM v[196:199],a[100:103],a[64:67],v[196:199]\n" \
"buffer_load_dwordx4 v16,s[28:31],s46 offen lds\nM v[204:207],a[100:103],a[68:71],v[204:207]\ns_add_u32 m0,s50,81920\nM v[200:203],a[96:99],a[68:71],v[200:203]\ns_mov_b32 s46,768\nM v[216:219],a[104:107],a[68:71],v[216:219]\nM v[220:223],a[108:111],a[68:71],v[220:223]\nM v[212:215],a[108:111],a[64:67],v[212:215]\n" \
"buffer_load_dwordx2 a[206:207],v59,s[40:43],s52 offen offset:0 nt\nM v[208:211],a[104:107],a[64:67],v[208:211]\nM v[224:227],a[112:115],a[64:67],v[224:227]\nM v[228:231],a[116:119],a[64:67],v[228:231]\nM v[236:239],a[116:119],a[68:71],v[236:239]\nbuffer_load_dwordx2 a[222:223],v59,s[80:83],s52 offen offset:0 nt\nM v[232:235],a[112:115],a[68:71],v[232:235]\nM v[248:251],a[120:123],a[68:71],v[248:251]\n" \
"M v[252:255],a[124:127],a[68:71],v[252:255]\nM v[244:247],a[124:127],a[64:67],v[244:247]\nbuffer_load_dwordx2 a[208:209],v59,s[40:43],s52 offen offset:2048 nt\nM v[240:243],a[120:123],a[64:67],v[240:243]\nds_read_b128 a[64:67],v6 offset:0\nds_read_b128 a[68:71],v6 offset:2048\nM a[128:131],a[96:99],a[72:75],a[128:131]\nM a[132:135],a[100:103],a[72:75],a[132:135]\n" \
"M a[140:143],a[100:103],a[76:79],a[140:143]\nbuffer_load_dwordx2 a[224:225],v59,s[80:83],s52 offen offset:2048 nt\nM a[136:139],a[96:99],a[76:79],a[136:139]\ns_mov_b32 s52,12288\nM a[152:155],a[104:107],a[76:79],a[152:155]\nM a[156:159],a[108:111],a[76:79],a[156:159]\nM a[148:151],a[108:111],a[72:75],a[148:151]\ns_waitcnt vmcnt(25) lgkmcnt(14)\n" \
"M a[144:147],a[104:107],a[72:75],a[144:147]\ns_waitcnt vmcnt(29)\ns_barrier\nds_read_b64_tr_b16 a[96:97],v9 offset:16384\nM a[160:163],a[112:115],a[72:75],a[160:163]\nbuffer_load_dwordx2 a[210:211],v59,s[40:43],s52 offen offset:0 nt\nM a[164:167],a[116:119],a[72:75],a[164:167]\nds_read_b64_tr_b16 a[98:99],v10 offset:16384\n" \
"ds_read_b64_tr_b16 a[100:101],v11 offset:16384\nM a[172:175],a[116:119],a[76:79],a[172:175]\nds_read_b64_tr_b16 a[102:103],v12 offset:16384\nv_accvgpr_read_b32 v45,a231\nM a[168:171],a[112:115],a[76:79],a[168:171]\nds_read_b64_tr_b16 a[104:105],v9 offset:16896\nds_read_b64_tr_b16 a[106:107],v10 offset:16896\nM a[184:187],a[120:123],a[76:79],a[184:187]\n" \
"buffer_load_dwordx2 a[226:227],v59,s[80:83],s52 offen offset:0 nt\nM a[188:191],a[124:127],a[76:79],a[188:191]\nds_read_b64_tr_b16 a[108:109],v11 offset:16896\nds_read_b64_tr_b16 a[110:111],v12 offset:16896\nM a[180:183],a[124:127],a[72:75],a[180:183]\nds_read_b64_tr_b16 a[112:113],v9 offset:17408\nv_accvgpr_read_b32 v49,a233\nM a[176:179],a[120:123],a[72:75],a[176:179]\n" \
"s_waitcnt lgkmcnt(6)\nds_read_b128 a[72:75],v6 offset:4096\nv_lshlrev_b32 v44,16,v45\nM v[128:131],a[32:35],a[24:27],v[128:131]\nbuffer_load_dwordx2 a[212:213],v59,s[40:43],s52 offen offset:2048 nt\nM v[132:135],a[36:39],a[24:27],v[132:135]\nds_read_b128 a[76:79],v6 offset:6144\nds_read_b64_tr_b16 a[114:115],v10 offset:17408\n" \
"M v[140:143],a[36:39],a[28:31],v[140:143]\nds_read_b64_tr_b16 a[116:117],v11 offset:17408\nv_accvgpr_read_b32 v36,a192\nM v[136:139],a[32:35],a[28:31],v[136:139]\nds_read_b64_tr_b16 a[118:119],v12 offset:17408\nv_accvgpr_read_b32 v43,a230\nM v[152:155],a[40:43],a[28:31],v[152:155]\nbuffer_load_dwordx2 a[228:229],v59,s[80:83],s52 offen offset:2048 nt\n" \
"M v[156:159],a[44:47],a[28:31],v[156:159]\nds_read_b64_tr_b16 a[120:121],v9 offset:17920\nds_read_b64_tr_b16 a[122:123],v10 offset:17920\nM v[148:151],a[44:47],a[24:27],v[148:151]\nds_read_b64_tr_b16 a[124:125],v11 offset:17920\nv_accvgpr_read_b32 v37,a194\nM v[144:147],a[40:43],a[24:27],v[144:147]\nds_read_b64_tr_b16 a[126:127],v12 offset:17920\n" \
"v_lshlrev_b32 v48,16,v49\nM v[160:163],a[48:51],a[24:27],v[160:163]\ns_mov_b64 exec,0xff\nglobal_load_dword v0,v1,s[84:85]\ns_mov_b64 exec,-1\nM v[164:167],a[52:55],a[24:27],v[164:167]\nv_lshlrev_b32 v30,16,v36\nv_and_b32 v45,0xffff0000,v45\n" \
"M v[172:175],a[52:55],a[28:31],v[172:175]\ns_mov_b64 exec,0xff00\nglobal_load_dword v0,v1,s[84:85]\ns_mov_b64 exec,-1\nM v[168:171],a[48:51],a[28:31],v[168:171]\nv_accvgpr_read_b32 v47,a232\nv_lshlrev_b32 v31,16,v37\nM v[184:187],a[56:59],a[28:31],v[184:187]\n" \
"s_mov_b64 exec,0xff\nglobal_load_dword v0,v1,s[76:77]\ns_mov_b64 exec,-1\nM v[188:191],a[60:63],a[28:31],v[188:191]\nv_accvgpr_read_b32 v38,a196\nv_lshlrev_b32 v42,16,v43\nM v[180:183],a[60:63],a[24:27],v[180:183]\ns_mov_b64 exec,0xff00\n" \
"global_load_dword v0,v1,s[76:77]\ns_mov_b64 exec,-1\nM v[176:179],a[56:59],a[24:27],v[176:179]\ns_add_u32 s76,s76,2097152\ns_addc_u32 s77,s77,0\nv_and_b32 v49,0xffff0000,v49\nv_and_b32 v36,0xffff0000,v36\nM v[64:67],a[32:35],a[16:19],v[64:67]\n" \
"v_accvgpr_read_b32 v39,a198\nv_mul_f32 v25,v30,v45\nM v[68:71],a[36:39],a[16:19],v[68:71]\nv_lshlrev_b32 v46,16,v47\nv_and_b32 v37,0xffff0000,v37\nM v[76:79],a[36:39],a[20:23],v[76:79]\nv_lshlrev_b32 v32,16,v38\nv_and_b32 v43,0xffff0000,v43\n" \
"M v[72:75],a[32:35],a[20:23],v[72:75]\nv_mul_f32 v26,v36,v49\nv_lshlrev_b32 v33,16,v39\nM v[88:91],a[40:43],a[20:23],v[88:91]\nv_fmac_f32 v25,v31,v44\nv_and_b32 v47,0xffff0000,v47\nM v[92:95],a[44:47],a[20:23],v[92:95]\nv_and_b32 v38,0xffff0000,v38\n" \
"v_fmac_f32 v26,v37,v48\nM v[84:87],a[44:47],a[16:19],v[84:87]\nv_and_b32 v39,0xffff0000,v39\nv_fmac_f32 v25,v32,v43\nM v[80:83],a[40:43],a[16:19],v[80:83]\nv_fmac_f32 v26,v38,v47\nv_fmac_f32 v25,v33,v42\nM v[96:99],a[48:51],a[16:19],v[96:99]\n" \
"v_fmac_f32 v26,v39,v46\nv_accvgpr_read_b32 v28,a214\nM v[100:103],a[52:55],a[16:19],v[100:103]\nC v26,v25,v26\nv_lshlrev_b32 v27,16,v28\nM v[108:111],a[52:55],a[20:23],v[108:111]\nv_lshlrev_b32 v25,16,v26\nv_and_b32 v28,0xffff0000,v28\n" \
"M v[104:107],a[48:51],a[20:23],v[104:107]\nv_and_b32 v26,0xffff0000,v26\nv_mul_f32 v29,v25,v27\nM v[120:123],a[56:59],a[20:23],v[120:123]\nv_mul_f32 v58,v26,v28\nv_accvgpr_read_b32 v40,a200\nM v[124:127],a[60:63],a[20:23],v[124:127]\nv_mul_f32 v25,v31,v45\n" \
"v_mul_f32 v26,v37,v49\nM v[116:119],a[60:63],a[16:19],v[116:119]\nv_lshlrev_b32 v34,16,v40\nv_fmac_f32 v25,v32,v44\nM v[112:115],a[56:59],a[16:19],v[112:115]\nv_fmac_f32 v26,v38,v48\nv_fmac_f32 v25,v33,v43\nM v[192:195],a[32:35],a[0:3],v[192:195]\n" \
"v_and_b32 v40,0xffff0000,v40\nv_fmac_f32 v26,v39,v47\nM v[196:199],a[36:39],a[0:3],v[196:199]\nv_fmac_f32 v25,v34,v42\nv_fmac_f32 v26,v40,v46\nM v[204:207],a[36:39],a[4:7],v[204:207]\nv_accvgpr_read_b32 v28,a216\nC v26,v25,v26\n" \
"M v[200:203],a[32:35],a[4:7],v[200:203]\nv_lshlrev_b32 v27,16,v28\nv_lshlrev_b32 v25,16,v26\nM v[216:219],a[40:43],a[4:7],v[216:219]\nv_and_b32 v28,0xffff0000,v28\nv_and_b32 v26,0xffff0000,v26\nM v[220:223],a[44:47],a[4:7],v[220:223]\nv_mul_f32 v25,v25,v27\n" \
"v_mul_f32 v26,v26,v28\nM v[212:215],a[44:47],a[0:3],v[212:215]\nC v53,v25,v29\nC v57,v26,v58\nM v[208:211],a[40:43],a[0:3],v[208:211]\nv_accvgpr_read_b32 v41,a202\nv_lshlrev_b32 v35,16,v41\nM v[224:227],a[48:51],a[0:3],v[224:227]\n" \
"v_mul_f32 v25,v32,v45\nv_mul_f32 v26,v38,v49\nM v[228:231],a[52:55],a[0:3],v[228:231]\nv_fmac_f32 v25,v33,v44\nv_and_b32 v41,0xffff0000,v41\nM v[236:239],a[52:55],a[4:7],v[236:239]\nv_fmac_f32 v26,v39,v48\nv_fmac_f32 v25,v34,v43\n" \
"M v[232:235],a[48:51],a[4:7],v[232:235]\nv_fmac_f32 v26,v40,v47\nv_fmac_f32 v25,v35,v42\nM v[248:251],a[56:59],a[4:7],v[248:251]\nv_fmac_f32 v26,v41,v46\nv_accvgpr_read_b32 v28,a218\nM v[252:255],a[60:63],a[4:7],v[252:255]\nC v26,v25,v26\n" \
"v_lshlrev_b32 v27,16,v28\nM v[244:247],a[60:63],a[0:3],v[244:247]\nv_lshlrev_b32 v25,16,v26\nv_and_b32 v28,0xffff0000,v28\nM v[240:243],a[56:59],a[0:3],v[240:243]\nv_and_b32 v26,0xffff0000,v26\nv_mul_f32 v29,v25,v27\nM a[128:131],a[32:35],a[8:11],a[128:131]\n" \
"v_mul_f32 v58,v26,v28\nv_accvgpr_read_b32 v36,a204\nM a[132:135],a[36:39],a[8:11],a[132:135]\nv_mul_f32 v25,v33,v45\nv_lshlrev_b32 v30,16,v36\nM a[140:143],a[36:39],a[12:15],a[140:143]\nv_mul_f32 v26,v39,v49\nv_fmac_f32 v25,v34,v44\n" \
"M a[136:139],a[32:35],a[12:15],a[136:139]\nv_fmac_f32 v26,v40,v48\nv_and_b32 v36,0xffff0000,v36\nM a[152:155],a[40:43],a[12:15],a[152:155]\nbuffer_load_dwordx4 v17,s[28:31],s46 offen lds\nM a[156:159],a[44:47],a[12:15],a[156:159]\ns_add_u32 m0,s50,86016\nv_fmac_f32 v25,v35,v43\n" \
"v_fmac_f32 v26,v41,v47\nM a[148:151],a[44:47],a[8:11],a[148:151]\ns_waitcnt vmcnt(22) lgkmcnt(0)\nv_fmac_f32 v25,v30,v42\nv_fmac_f32 v26,v36,v46\nM a[144:147],a[40:43],a[8:11],a[144:147]\ns_barrier\nds_read_b64_tr_b16 a[32:33],v9 offset:32768\n" \
"M a[160:163],a[48:51],a[8:11],a[160:163]\nbuffer_load_dwordx4 v18,s[28:31],s46 offen lds\nM a[164:167],a[52:55],a[8:11],a[164:167]\ns_add_u32 m0,s50,90112\nds_read_b64_tr_b16 a[34:35],v10 offset:32768\nv_accvgpr_read_b32 v28,a220\nM a[172:175],a[52:55],a[12:15],a[172:175]\nds_read_b64_tr_b16 a[36:37],v11 offset:32768\n" \
"ds_read_b64_tr_b16 a[38:39],v12 offset:32768\nM a[168:171],a[48:51],a[12:15],a[168:171]\nds_read_b64_tr_b16 a[40:41],v9 offset:33280\nC v26,v25,v26\nM a[184:187],a[56:59],a[12:15],a[184:187]\nbuffer_load_dwordx4 v19,s[28:31],s46 offen lds\nM a[188:191],a[60:63],a[12:15],a[188:191]\ns_add_u32 m0,s50,94208\n" \
"ds_read_b64_tr_b16 a[42:43],v10 offset:33280\nv_lshlrev_b32 v27,16,v28\nM a[180:183],a[60:63],a[8:11],a[180:183]\nds_read_b64_tr_b16 a[44:45],v11 offset:33280\nv_lshlrev_b32 v25,16,v26\nM a[176:179],a[56:59],a[8:11],a[176:179]\nds_read_b64_tr_b16 a[46:47],v12 offset:33280\nv_and_b32 v28,0xffff0000,v28\n" \
"M v[128:131],a[96:99],a[88:91],v[128:131]\nbuffer_load_dwordx4 v20,s[28:31],s46 offen lds\nM v[132:135],a[100:103],a[88:91],v[132:135]\ns_add_u32 m0,s22,0\nds_read_b64_tr_b16 a[48:49],v9 offset:33792\nv_and_b32 v26,0xffff0000,v26\nM v[140:143],a[100:103],a[92:95],v[140:143]\ns_mov_b32 s46,896\n" \
"ds_read_b64_tr_b16 a[50:51],v10 offset:33792\nv_mul_f32 v25,v25,v27\nM v[136:139],a[96:99],a[92:95],v[136:139]\nds_read_b64_tr_b16 a[52:53],v11 offset:33792\nds_read_b64_tr_b16 a[54:55],v12 offset:33792\nM v[152:155],a[104:107],a[92:95],v[152:155]\nbuffer_load_dwordx4 v21,s[32:35],s47 offen offset:0 lds\nM v[156:159],a[108:111],a[92:95],v[156:159]\n" \
"s_add_u32 m0,s22,896\nv_mul_f32 v26,v26,v28\nC v52,v25,v29\nM v[148:151],a[108:111],a[88:91],v[148:151]\nds_read_b64_tr_b16 a[56:57],v9 offset:34304\nds_read_b64_tr_b16 a[58:59],v10 offset:34304\nM v[144:147],a[104:107],a[88:91],v[144:147]\nds_read_b64_tr_b16 a[60:61],v11 offset:34304\n" \
"C v56,v26,v58\nM v[160:163],a[112:115],a[88:91],v[160:163]\nbuffer_load_dwordx4 v21,s[32:35],s47 offen offset:128 lds\nM v[164:167],a[116:119],a[88:91],v[164:167]\ns_add_u32 m0,s22,2048\nds_read_b64_tr_b16 a[62:63],v12 offset:34304\nv_mov_b32 v59,s19\nM v[172:175],a[116:119],a[92:95],v[172:175]\n" \
"ds_read_b128 a[24:27],v5 offset:45056\nM v[168:171],a[112:115],a[92:95],v[168:171]\nds_read_b128 a[28:31],v5 offset:47104\nM v[184:187],a[120:123],a[92:95],v[184:187]\nbuffer_load_dwordx4 v21,s[92:95],s47 offen offset:0 lds\nM v[188:191],a[124:127],a[92:95],v[188:191]\ns_add_u32 m0,s22,2944\nM v[180:183],a[124:127],a[88:91],v[180:183]\n" \
"ds_read_b128 a[16:19],v5 offset:40960\nM v[176:179],a[120:123],a[88:91],v[176:179]\nds_read_b128 a[20:23],v5 offset:43008\nM v[64:67],a[96:99],a[80:83],v[64:67]\nbuffer_load_dwordx4 v21,s[92:95],s47 offen offset:128 lds\nM v[68:71],a[100:103],a[80:83],v[68:71]\ns_add_u32 m0,s22,16384\nds_read_b128 a[0:3],v5 offset:32768\n" \
"M v[76:79],a[100:103],a[84:87],v[76:79]\ns_mov_b32 s47,3407872\nds_read_b128 a[4:7],v5 offset:34816\nM v[72:75],a[96:99],a[84:87],v[72:75]\nds_read_b128 a[8:11],v5 offset:36864\nM v[88:91],a[104:107],a[84:87],v[88:91]\nbuffer_load_dwordx4 v21,s[32:35],s47 offen offset:0 lds\nM v[92:95],a[108:111],a[84:87],v[92:95]\n" \
"s_add_u32 m0,s22,17280\nM v[84:87],a[108:111],a[80:83],v[84:87]\nds_read_b128 a[12:15],v5 offset:38912\nM v[80:83],a[104:107],a[80:83],v[80:83]\nds_read_b128 a[88:91],v6 offset:45056\nM v[96:99],a[112:115],a[80:83],v[96:99]\nbuffer_load_dwordx4 v21,s[32:35],s47 offen offset:128 lds\nM v[100:103],a[116:119],a[80:83],v[100:103]\n" \
"s_add_u32 m0,s22,18432\nds_read_b128 a[92:95],v6 offset:47104\nM v[108:111],a[116:119],a[84:87],v[108:111]\nM v[104:107],a[112:115],a[84:87],v[104:107]\nM v[120:123],a[120:123],a[84:87],v[120:123]\nbuffer_load_dwordx4 v21,s[92:95],s47 offen offset:0 lds\nM v[124:127],a[124:127],a[84:87],v[124:127]\ns_add_u32 m0,s22,19328\n" \
"M v[116:119],a[124:127],a[80:83],v[116:119]\nM v[112:115],a[120:123],a[80:83],v[112:115]\nds_read_b128 a[80:83],v6 offset:40960\nds_read_b128 a[84:87],v6 offset:43008\nM v[192:195],a[96:99],a[64:67],v[192:195]\nbuffer_load_dwordx4 v21,s[92:95],s47 offen offset:128 lds\nM v[196:199],a[100:103],a[64:67],v[196:199]\ns_add_u32 m0,s50,0\n" \
"M v[204:207],a[100:103],a[68:71],v[204:207]\ns_mov_b32 s47,3670016\nM v[200:203],a[96:99],a[68:71],v[200:203]\nM v[216:219],a[104:107],a[68:71],v[216:219]\nbuffer_load_dwordx4 v13,s[28:31],s46 offen lds\nM v[220:223],a[108:111],a[68:71],v[220:223]\ns_add_u32 m0,s50,4096\nv_add_u32 v13,s60,v63\n" \
"M v[212:215],a[108:111],a[64:67],v[212:215]\nM v[208:211],a[104:107],a[64:67],v[208:211]\nM v[224:227],a[112:115],a[64:67],v[224:227]\nbuffer_load_dwordx4 v14,s[28:31],s46 offen lds\nM v[228:231],a[116:119],a[64:67],v[228:231]\ns_add_u32 m0,s50,8192\nv_add_u32 v14,0x10000,v13\nM v[236:239],a[116:119],a[68:71],v[236:239]\n" \
"M v[232:235],a[112:115],a[68:71],v[232:235]\nM v[248:251],a[120:123],a[68:71],v[248:251]\nbuffer_load_dwordx4 v15,s[28:31],s46 offen lds\nM v[252:255],a[124:127],a[68:71],v[252:255]\ns_add_u32 m0,s50,12288\nv_add_u32 v15,0x10000,v14\nM v[244:247],a[124:127],a[64:67],v[244:247]\nM v[240:243],a[120:123],a[64:67],v[240:243]\n" \
"ds_read_b128 a[64:67],v6 offset:32768\nds_read_b128 a[68:71],v6 offset:34816\nM a[128:131],a[96:99],a[72:75],a[128:131]\nbuffer_load_dwordx4 v16,s[28:31],s46 offen lds\nM a[132:135],a[100:103],a[72:75],a[132:135]\ns_add_u32 m0,s50,16384\nv_add_u32 v16,0x10000,v15\nM a[140:143],a[100:103],a[76:79],a[140:143]\n" \
"s_mov_b32 s46,896\nM a[136:139],a[96:99],a[76:79],a[136:139]\nM a[152:155],a[104:107],a[76:79],a[152:155]\nbuffer_load_dwordx4 v17,s[28:31],s46 offen lds\nM a[156:159],a[108:111],a[76:79],a[156:159]\ns_add_u32 m0,s50,20480\nv_add_u32 v17,s61,v63\nv_mov_b32 v63,0x3f803f80\n" \
"M a[148:151],a[108:111],a[72:75],a[148:151]\ns_waitcnt vmcnt(33) lgkmcnt(14)\nM a[144:147],a[104:107],a[72:75],a[144:147]\ns_barrier\nds_read_b64_tr_b16 a[96:97],v9 offset:49152\nM a[160:163],a[112:115],a[72:75],a[160:163]\nds_read_b64_tr_b16 a[98:99],v10 offset:49152\nds_read_b64_tr_b16 a[100:101],v11 offset:49152\n" \
"M a[164:167],a[116:119],a[72:75],a[164:167]\nbuffer_load_dwordx4 v18,s[28:31],s46 offen lds\nM a[172:175],a[116:119],a[76:79],a[172:175]\ns_add_u32 m0,s50,24576\nds_read_b64_tr_b16 a[102:103],v12 offset:49152\nv_add_u32 v18,0x10000,v17\nM a[168:171],a[112:115],a[76:79],a[168:171]\nds_read_b64_tr_b16 a[104:105],v9 offset:49664\n" \
"M a[184:187],a[120:123],a[76:79],a[184:187]\nds_read_b64_tr_b16 a[106:107],v10 offset:49664\nM a[188:191],a[124:127],a[76:79],a[188:191]\nbuffer_load_dwordx4 v19,s[28:31],s46 offen lds\nM a[180:183],a[124:127],a[72:75],a[180:183]\ns_add_u32 m0,s50,28672\nds_read_b64_tr_b16 a[108:109],v11 offset:49664\nv_add_u32 v19,0x10000,v18\n" \
"M a[176:179],a[120:123],a[72:75],a[176:179]\ns_waitcnt lgkmcnt(4)\nds_read_b128 a[72:75],v6 offset:36864\nM v[128:131],a[32:35],a[24:27],v[128:131]\nds_read_b128 a[76:79],v6 offset:38912\nM v[132:135],a[36:39],a[24:27],v[132:135]\nds_read_b64_tr_b16 a[110:111],v12 offset:49664\nM v[140:143],a[36:39],a[28:31],v[140:143]\n" \
"buffer_load_dwordx4 v20,s[28:31],s46 offen lds\nM v[136:139],a[32:35],a[28:31],v[136:139]\ns_add_u32 m0,s22,32768\nds_read_b64_tr_b16 a[112:113],v9 offset:50176\nv_add_u32 v20,0x10000,v19\nM v[152:155],a[40:43],a[28:31],v[152:155]\ns_mov_b64 s[28:29],s[64:65]\nds_read_b64_tr_b16 a[114:115],v10 offset:50176\n" \
"M v[156:159],a[44:47],a[28:31],v[156:159]\ns_and_b32 s29,s29,0xffff\nds_read_b64_tr_b16 a[116:117],v11 offset:50176\nds_read_b64_tr_b16 a[118:119],v12 offset:50176\nM v[148:151],a[44:47],a[24:27],v[148:151]\nbuffer_load_dwordx4 v21,s[32:35],s47 offen offset:0 lds\nM v[144:147],a[40:43],a[24:27],v[144:147]\ns_add_u32 m0,s22,33664\n" \
"M v[160:163],a[48:51],a[24:27],v[160:163]\ns_mov_b32 s30,4194304\nds_read_b64_tr_b16 a[120:121],v9 offset:50688\nds_read_b64_tr_b16 a[122:123],v10 offset:50688\nM v[164:167],a[52:55],a[24:27],v[164:167]\ns_mov_b32 s46,0\nds_read_b64_tr_b16 a[124:125],v11 offset:50688\nM v[172:175],a[52:55],a[28:31],v[172:175]\n" \
"ds_read_b64_tr_b16 a[126:127],v12 offset:50688\nM v[168:171],a[48:51],a[28:31],v[168:171]\nbuffer_load_dwordx4 v21,s[32:35],s47 offen offset:128 lds\nM v[184:187],a[56:59],a[28:31],v[184:187]\ns_add_u32 m0,s22,34816\nM v[188:191],a[60:63],a[28:31],v[188:191]\nM v[180:183],a[60:63],a[24:27],v[180:183]\nM v[176:179],a[56:59],a[24:27],v[176:179]\n" \
"buffer_load_dwordx4 v21,s[92:95],s47 offen offset:0 lds\nM v[64:67],a[32:35],a[16:19],v[64:67]\ns_add_u32 m0,s22,35712\nM v[68:71],a[36:39],a[16:19],v[68:71]\nM v[76:79],a[36:39],a[20:23],v[76:79]\nM v[72:75],a[32:35],a[20:23],v[72:75]\nM v[88:91],a[40:43],a[20:23],v[88:91]\nbuffer_load_dwordx4 v21,s[92:95],s47 offen offset:128 lds\n" \
"M v[92:95],a[44:47],a[20:23],v[92:95]\ns_add_u32 m0,s22,49152\nM v[84:87],a[44:47],a[16:19],v[84:87]\ns_mov_b32 s47,3932160\nM v[80:83],a[40:43],a[16:19],v[80:83]\nM v[96:99],a[48:51],a[16:19],v[96:99]\nM v[100:103],a[52:55],a[16:19],v[100:103]\nM v[108:111],a[52:55],a[20:23],v[108:111]\n" \
"M v[104:107],a[48:51],a[20:23],v[104:107]\nM v[120:123],a[56:59],a[20:23],v[120:123]\nM v[124:127],a[60:63],a[20:23],v[124:127]\nM v[116:119],a[60:63],a[16:19],v[116:119]\nM v[112:115],a[56:59],a[16:19],v[112:115]\nM v[192:195],a[32:35],a[0:3],v[192:195]\ns_mov_b64 exec,0xff\nglobal_load_dword v0,v2,s[88:89]\n" \
"s_mov_b64 exec,-1\nM v[196:199],a[36:39],a[0:3],v[196:199]\nM v[204:207],a[36:39],a[4:7],v[204:207]\ns_mov_b64 exec,0xff00\nglobal_load_dword v0,v2,s[88:89]\ns_mov_b64 exec,-1\nM v[200:203],a[32:35],a[4:7],v[200:203]\ns_add_u32 s88,s88,2048\n" \
"s_addc_u32 s89,s89,0\nM v[216:219],a[40:43],a[4:7],v[216:219]\ns_mov_b64 exec,0xff\nglobal_load_dword v0,v2,s[88:89]\ns_mov_b64 exec,-1\nM v[220:223],a[44:47],a[4:7],v[220:223]\nM v[212:215],a[44:47],a[0:3],v[212:215]\ns_mov_b64 exec,0xff00\n" \
"global_load_dword v0,v2,s[88:89]\ns_mov_b64 exec,-1\nM v[208:211],a[40:43],a[0:3],v[208:211]\nM v[224:227],a[48:51],a[0:3],v[224:227]\ns_mov_b64 exec,0xff\nglobal_load_dword v0,v1,s[76:77]\ns_mov_b64 exec,-1\nM v[228:231],a[52:55],a[0:3],v[228:231]\n" \
"M v[236:239],a[52:55],a[4:7],v[236:239]\ns_mov_b64 exec,0xff00\nglobal_load_dword v0,v1,s[76:77]\ns_mov_b64 exec,-1\nM v[232:235],a[48:51],a[4:7],v[232:235]\ns_add_u32 s76,s76,2097152\ns_addc_u32 s77,s77,0\nM v[248:251],a[56:59],a[4:7],v[248:251]\n" \
"M v[252:255],a[60:63],a[4:7],v[252:255]\nM v[244:247],a[60:63],a[0:3],v[244:247]\ns_waitcnt vmcnt(34)\nv_accvgpr_read_b32 v37,a206\nv_mul_f32 v25,v34,v45\nM v[240:243],a[56:59],a[0:3],v[240:243]\ns_mov_b32 s98,79691776\nv_mul_f32 v26,v40,v49\n" \
"v_lshlrev_b32 v31,16,v37\nM a[128:131],a[32:35],a[8:11],a[128:131]\ns_mov_b32 s99,79695872\nv_fmac_f32 v25,v35,v44\nv_fmac_f32 v26,v41,v48\nM a[132:135],a[36:39],a[8:11],a[132:135]\nv_and_b32 v37,0xffff0000,v37\nv_fmac_f32 v25,v30,v43\n" \
"M a[140:143],a[36:39],a[12:15],a[140:143]\nv_fmac_f32 v26,v36,v47\nv_fmac_f32 v25,v31,v42\nM a[136:139],a[32:35],a[12:15],a[136:139]\nv_fmac_f32 v26,v37,v46\nv_accvgpr_read_b32 v28,a222\nM a[152:155],a[40:43],a[12:15],a[152:155]\nC v26,v25,v26\n" \
"v_lshlrev_b32 v27,16,v28\nM a[156:159],a[44:47],a[12:15],a[156:159]\nv_lshlrev_b32 v25,16,v26\nv_and_b32 v28,0xffff0000,v28\nM a[148:151],a[44:47],a[8:11],a[148:151]\ns_waitcnt vmcnt(22) lgkmcnt(0)\nv_and_b32 v26,0xffff0000,v26\nv_mul_f32 v29,v25,v27\n" \
"M a[144:147],a[40:43],a[8:11],a[144:147]\ns_barrier\nv_mul_f32 v58,v26,v28\nM a[160:163],a[48:51],a[8:11],a[160:163]\nv_accvgpr_read_b32 v38,a208\nv_mul_f32 v25,v35,v45\nM a[164:167],a[52:55],a[8:11],a[164:167]\nv_lshlrev_b32 v32,16,v38\n" \
"v_mul_f32 v26,v41,v49\nM a[172:175],a[52:55],a[12:15],a[172:175]\nv_fmac_f32 v25,v30,v44\nv_and_b32 v38,0xffff0000,v38\nM a[168:171],a[48:51],a[12:15],a[168:171]\nLG2_%=:\ns_cbranch_execz LG6_%=\nv_fmac_f32 v26,v36,v48\n" \
"v_fmac_f32 v25,v31,v43\nM a[184:187],a[56:59],a[12:15],a[184:187]\nds_read_b64_tr_b16 a[32:33],v9 offset:0\nds_read_b64_tr_b16 a[34:35],v10 offset:0\nM a[188:191],a[60:63],a[12:15],a[188:191]\nds_read_b64_tr_b16 a[36:37],v11 offset:0\nv_fmac_f32 v26,v37,v47\nM a[180:183],a[60:63],a[8:11],a[180:183]\n" \
"ds_read_b64_tr_b16 a[38:39],v12 offset:0\nds_read_b64_tr_b16 a[40:41],v9 offset:512\nM a[176:179],a[56:59],a[8:11],a[176:179]\nds_read_b64_tr_b16 a[42:43],v10 offset:512\nv_fmac_f32 v25,v32,v42\nM v[128:131],a[96:99],a[88:91],v[128:131]\nv_fmac_f32 v26,v38,v46\nv_accvgpr_read_b32 v28,a224\n" \
"M v[132:135],a[100:103],a[88:91],v[132:135]\nds_read_b64_tr_b16 a[44:45],v11 offset:512\nC v26,v25,v26\nM v[140:143],a[100:103],a[92:95],v[140:143]\nds_read_b64_tr_b16 a[46:47],v12 offset:512\nv_lshlrev_b32 v27,16,v28\nM v[136:139],a[96:99],a[92:95],v[136:139]\nds_read_b64_tr_b16 a[48:49],v9 offset:1024\n" \
"ds_read_b64_tr_b16 a[50:51],v10 offset:1024\nM v[152:155],a[104:107],a[92:95],v[152:155]\nds_read_b64_tr_b16 a[52:53],v11 offset:1024\nv_lshlrev_b32 v25,16,v26\nM v[156:159],a[108:111],a[92:95],v[156:159]\nds_read_b64_tr_b16 a[54:55],v12 offset:1024\nds_read_b64_tr_b16 a[56:57],v9 offset:1536\nM v[148:151],a[108:111],a[88:91],v[148:151]\n" \
"ds_read_b64_tr_b16 a[58:59],v10 offset:1536\nv_and_b32 v28,0xffff0000,v28\nM v[144:147],a[104:107],a[88:91],v[144:147]\nds_read_b64_tr_b16 a[60:61],v11 offset:1536\nv_and_b32 v26,0xffff0000,v26\nM v[160:163],a[112:115],a[88:91],v[160:163]\nds_read_b64_tr_b16 a[62:63],v12 offset:1536\nv_mul_f32 v25,v25,v27\n" \
"M v[164:167],a[116:119],a[88:91],v[164:167]\nds_read_b128 a[24:27],v7 offset:12288\nv_mul_f32 v26,v26,v28\nM v[172:175],a[116:119],a[92:95],v[172:175]\nds_read_b128 a[28:31],v7 offset:14336\nC v51,v25,v29\nM v[168:171],a[112:115],a[92:95],v[168:171]\nC v55,v26,v58\n" \
"v_accvgpr_read_b32 v39,a210\nM v[184:187],a[120:123],a[92:95],v[184:187]\nv_mul_f32 v25,v30,v45\nv_mul_f32 v26,v36,v49\nM v[188:191],a[124:127],a[92:95],v[188:191]\nv_lshlrev_b32 v33,16,v39\nv_fmac_f32 v25,v31,v44\nM v[180:183],a[124:127],a[88:91],v[180:183]\n" \
"v_fmac_f32 v26,v37,v48\nv_and_b32 v39,0xffff0000,v39\nM v[176:179],a[120:123],a[88:91],v[176:179]\nv_fmac_f32 v25,v32,v43\nv_fmac_f32 v26,v38,v47\nM v[64:67],a[96:99],a[80:83],v[64:67]\nv_fmac_f32 v25,v33,v42\nv_fmac_f32 v26,v39,v46\n" \
"M v[68:71],a[100:103],a[80:83],v[68:71]\nv_accvgpr_read_b32 v28,a226\nC v26,v25,v26\nM v[76:79],a[100:103],a[84:87],v[76:79]\nv_lshlrev_b32 v27,16,v28\nv_lshlrev_b32 v25,16,v26\nM v[72:75],a[96:99],a[84:87],v[72:75]\nv_and_b32 v28,0xffff0000,v28\n" \
"v_and_b32 v26,0xffff0000,v26\nM v[88:91],a[104:107],a[84:87],v[88:91]\nv_mul_f32 v29,v25,v27\nv_mul_f32 v58,v26,v28\nM v[92:95],a[108:111],a[84:87],v[92:95]\nv_accvgpr_read_b32 v40,a212\nv_lshlrev_b32 v34,16,v40\nM v[84:87],a[108:111],a[80:83],v[84:87]\n" \
"v_mul_f32 v25,v31,v45\nv_mul_f32 v26,v37,v49\nM v[80:83],a[104:107],a[80:83],v[80:83]\nds_read_b128 a[16:19],v7 offset:8192\nv_fmac_f32 v25,v32,v44\nM v[96:99],a[112:115],a[80:83],v[96:99]\nds_read_b128 a[20:23],v7 offset:10240\nv_and_b32 v40,0xffff0000,v40\n" \
"M v[100:103],a[116:119],a[80:83],v[100:103]\nv_fmac_f32 v26,v38,v48\nv_fmac_f32 v25,v33,v43\nM v[108:111],a[116:119],a[84:87],v[108:111]\nv_fmac_f32 v26,v39,v47\nv_fmac_f32 v25,v34,v42\nM v[104:107],a[112:115],a[84:87],v[104:107]\nv_fmac_f32 v26,v40,v46\n" \
"v_accvgpr_read_b32 v28,a228\nM v[120:123],a[120:123],a[84:87],v[120:123]\nC v26,v25,v26\nv_lshlrev_b32 v27,16,v28\nM v[124:127],a[124:127],a[84:87],v[124:127]\nv_lshlrev_b32 v25,16,v26\nv_mul_f32 v25,v25,v27\nM v[116:119],a[124:127],a[80:83],v[116:119]\n" \
"C v50,v25,v29\nv_accvgpr_read_b32 v45,a235\nM v[112:115],a[120:123],a[80:83],v[112:115]\nv_and_b32 v28,0xffff0000,v28\nv_and_b32 v26,0xffff0000,v26\nM v[192:195],a[96:99],a[64:67],v[192:195]\nv_lshlrev_b32 v44,16,v45\nv_accvgpr_read_b32 v49,a237\n" \
"M v[196:199],a[100:103],a[64:67],v[196:199]\nds_read_b128 a[0:3],v7 offset:0\nv_accvgpr_read_b32 v36,a193\nM v[204:207],a[100:103],a[68:71],v[204:207]\nds_read_b128 a[4:7],v7 offset:2048\nv_accvgpr_read_b32 v37,a195\nM v[200:203],a[96:99],a[68:71],v[200:203]\nv_mul_f32 v26,v26,v28\n" \
"v_accvgpr_read_b32 v43,a234\nM v[216:219],a[104:107],a[68:71],v[216:219]\nv_accvgpr_read_b32 v47,a236\nv_lshlrev_b32 v48,16,v49\nM v[220:223],a[108:111],a[68:71],v[220:223]\nv_and_b32 v45,0xffff0000,v45\nv_lshlrev_b32 v30,16,v36\nM v[212:215],a[108:111],a[64:67],v[212:215]\n" \
"buffer_store_dwordx4 v[50:53],v60,s[36:39],s98 offen offset:0 sc1\nM v[208:211],a[104:107],a[64:67],v[208:211]\ns_mov_b32 s98,79699968\nv_lshlrev_b32 v31,16,v37\nv_accvgpr_read_b32 v38,a197\nM v[224:227],a[112:115],a[64:67],v[224:227]\nC v54,v26,v58\nv_accvgpr_read_b32 v39,a199\n" \
"M v[228:231],a[116:119],a[64:67],v[228:231]\nv_lshlrev_b32 v42,16,v43\nv_and_b32 v49,0xffff0000,v49\nM v[236:239],a[116:119],a[68:71],v[236:239]\nv_lshlrev_b32 v46,16,v47\nv_and_b32 v36,0xffff0000,v36\nM v[232:235],a[112:115],a[68:71],v[232:235]\nv_lshlrev_b32 v32,16,v38\n" \
"v_mul_f32 v25,v30,v45\nM v[248:251],a[120:123],a[68:71],v[248:251]\nds_read_b128 a[8:11],v7 offset:4096\nds_read_b128 a[12:15],v7 offset:6144\nM v[252:255],a[124:127],a[68:71],v[252:255]\nv_and_b32 v37,0xffff0000,v37\nv_lshlrev_b32 v33,16,v39\nM v[244:247],a[124:127],a[64:67],v[244:247]\n" \
"v_and_b32 v43,0xffff0000,v43\nv_mul_f32 v26,v36,v49\nM v[240:243],a[120:123],a[64:67],v[240:243]\nds_read_b128 a[64:67],v8 offset:0\nds_read_b128 a[68:71],v8 offset:2048\nM a[128:131],a[96:99],a[72:75],a[128:131]\nv_and_b32 v47,0xffff0000,v47\nv_and_b32 v38,0xffff0000,v38\n" \
"M a[132:135],a[100:103],a[72:75],a[132:135]\nds_read_b128 a[80:83],v8 offset:8192\nv_fmac_f32 v25,v31,v44\nM a[140:143],a[100:103],a[76:79],a[140:143]\nv_and_b32 v39,0xffff0000,v39\nv_fmac_f32 v26,v37,v48\nM a[136:139],a[96:99],a[76:79],a[136:139]\nv_fmac_f32 v25,v32,v43\n" \
"v_fmac_f32 v26,v38,v47\nM a[152:155],a[104:107],a[76:79],a[152:155]\nv_fmac_f32 v25,v33,v42\nv_fmac_f32 v26,v39,v46\nM a[156:159],a[108:111],a[76:79],a[156:159]\nds_read_b128 a[84:87],v8 offset:10240\nv_accvgpr_read_b32 v28,a215\nM a[148:151],a[108:111],a[72:75],a[148:151]\n" \
"s_waitcnt vmcnt(19) lgkmcnt(12)\nds_read_b128 a[88:91],v8 offset:12288\nds_read_b128 a[92:95],v8 offset:14336\nM a[144:147],a[104:107],a[72:75],a[144:147]\ns_barrier\nds_read_b64_tr_b16 a[96:97],v9 offset:16384\nM a[160:163],a[112:115],a[72:75],a[160:163]\nds_read_b64_tr_b16 a[98:99],v10 offset:16384\n" \
"C v26,v25,v26\nM a[164:167],a[116:119],a[72:75],a[164:167]\nds_read_b64_tr_b16 a[100:101],v11 offset:16384\nv_lshlrev_b32 v27,16,v28\nM a[172:175],a[116:119],a[76:79],a[172:175]\nds_read_b64_tr_b16 a[102:103],v12 offset:16384\nds_read_b64_tr_b16 a[104:105],v9 offset:16896\nM a[168:171],a[112:115],a[76:79],a[168:171]\n" \
"ds_read_b64_tr_b16 a[106:107],v10 offset:16896\nv_lshlrev_b32 v25,16,v26\nM a[184:187],a[120:123],a[76:79],a[184:187]\nds_read_b64_tr_b16 a[108:109],v11 offset:16896\nv_and_b32 v28,0xffff0000,v28\nM a[188:191],a[124:127],a[76:79],a[188:191]\ns_waitcnt lgkmcnt(6)\nds_read_b64_tr_b16 a[110:111],v12 offset:16896\n" \
"ds_read_b64_tr_b16 a[112:113],v9 offset:17408\nM a[180:183],a[124:127],a[72:75],a[180:183]\nbuffer_store_dwordx4 v[54:57],v60,s[36:39],s99 offen offset:0 sc1\nM a[176:179],a[120:123],a[72:75],a[176:179]\ns_mov_b32 s99,79704064\nds_read_b128 a[72:75],v8 offset:4096\nds_read_b128 a[76:79],v8 offset:6144\nM v[128:131],a[32:35],a[24:27],v[128:131]\n" \
"ds_read_b64_tr_b16 a[114:115],v10 offset:17408\nv_and_b32 v26,0xffff0000,v26\nM v[132:135],a[36:39],a[24:27],v[132:135]\nds_read_b64_tr_b16 a[116:117],v11 offset:17408\nv_mul_f32 v29,v25,v27\nM v[140:143],a[36:39],a[28:31],v[140:143]\nds_read_b64_tr_b16 a[118:119],v12 offset:17408\nv_mul_f32 v58,v26,v28\n" \
"M v[136:139],a[32:35],a[28:31],v[136:139]\nds_read_b64_tr_b16 a[120:121],v9 offset:17920\nds_read_b64_tr_b16 a[122:123],v10 offset:17920\nM v[152:155],a[40:43],a[28:31],v[152:155]\nds_read_b64_tr_b16 a[124:125],v11 offset:17920\nv_accvgpr_read_b32 v40,a201\nM v[156:159],a[44:47],a[28:31],v[156:159]\nds_read_b64_tr_b16 a[126:127],v12 offset:17920\n" \
"v_lshlrev_b32 v34,16,v40\nM v[148:151],a[44:47],a[24:27],v[148:151]\nv_mul_f32 v25,v31,v45\nv_mul_f32 v26,v37,v49\nM v[144:147],a[40:43],a[24:27],v[144:147]\nv_fmac_f32 v25,v32,v44\nv_and_b32 v40,0xffff0000,v40\nM v[160:163],a[48:51],a[24:27],v[160:163]\n" \
"v_fmac_f32 v26,v38,v48\nv_fmac_f32 v25,v33,v43\nM v[164:167],a[52:55],a[24:27],v[164:167]\nv_fmac_f32 v26,v39,v47\nv_fmac_f32 v25,v34,v42\nM v[172:175],a[52:55],a[28:31],v[172:175]\nv_fmac_f32 v26,v40,v46\nv_accvgpr_read_b32 v28,a217\n" \
"M v[168:171],a[48:51],a[28:31],v[168:171]\nC v26,v25,v26\nv_lshlrev_b32 v27,16,v28\nM v[184:187],a[56:59],a[28:31],v[184:187]\nv_lshlrev_b32 v25,16,v26\nv_and_b32 v28,0xffff0000,v28\nM v[188:191],a[60:63],a[28:31],v[188:191]\nv_and_b32 v26,0xffff0000,v26\n" \
"v_mul_f32 v25,v25,v27\nM v[180:183],a[60:63],a[24:27],v[180:183]\nv_mul_f32 v26,v26,v28\nC v53,v25,v29\nM v[176:179],a[56:59],a[24:27],v[176:179]\nC v57,v26,v58\nv_accvgpr_read_b32 v41,a203\nM v[64:67],a[32:35],a[16:19],v[64:67]\n" \
"v_mul_f32 v25,v32,v45\nv_lshlrev_b32 v35,16,v41\nM v[68:71],a[36:39],a[16:19],v[68:71]\nv_mul_f32 v26,v38,v49\nv_fmac_f32 v25,v33,v44\nM v[76:79],a[36:39],a[20:23],v[76:79]\nv_fmac_f32 v26,v39,v48\nv_and_b32 v41,0xffff0000,v41\n" \
"M v[72:75],a[32:35],a[20:23],v[72:75]\nv_fmac_f32 v25,v34,v43\nv_fmac_f32 v26,v40,v47\nM v[88:91],a[40:43],a[20:23],v[88:91]\nv_fmac_f32 v25,v35,v42\nv_fmac_f32 v26,v41,v46\nM v[92:95],a[44:47],a[20:23],v[92:95]\nv_accvgpr_read_b32 v28,a219\n" \
"C v26,v25,v26\nM v[84:87],a[44:47],a[16:19],v[84:87]\ns_mov_b64 exec,0xff\nglobal_load_dword v0,v2,s[100:101]\ns_mov_b64 exec,-1\nM v[80:83],a[40:43],a[16:19],v[80:83]\nv_lshlrev_b32 v27,16,v28\nv_lshlrev_b32 v25,16,v26\n" \
"M v[96:99],a[48:51],a[16:19],v[96:99]\nbuffer_load_dwordx4 v21,s[32:35],s47 offen offset:0 lds\nM v[100:103],a[52:55],a[16:19],v[100:103]\ns_add_u32 m0,s22,50048\nv_and_b32 v28,0xffff0000,v28\nv_and_b32 v26,0xffff0000,v26\nM v[108:111],a[52:55],a[20:23],v[108:111]\nv_mul_f32 v29,v25,v27\n" \
"v_mul_f32 v58,v26,v28\nM v[104:107],a[48:51],a[20:23],v[104:107]\nv_accvgpr_read_b32 v36,a205\nv_lshlrev_b32 v30,16,v36\nM v[120:123],a[56:59],a[20:23],v[120:123]\nbuffer_load_dwordx4 v21,s[32:35],s47 offen offset:128 lds\nM v[124:127],a[60:63],a[20:23],v[124:127]\ns_add_u32 m0,s22,51200\n" \
"v_mul_f32 v25,v33,v45\nv_mul_f32 v26,v39,v49\nM v[116:119],a[60:63],a[16:19],v[116:119]\ns_mov_b64 s[32:33],s[44:45]\nv_fmac_f32 v25,v34,v44\nv_and_b32 v36,0xffff0000,v36\nM v[112:115],a[56:59],a[16:19],v[112:115]\ns_mov_b32 s34,s49\n" \
"v_fmac_f32 v26,v40,v48\nv_fmac_f32 v25,v35,v43\nM v[192:195],a[32:35],a[0:3],v[192:195]\nbuffer_load_dwordx4 v21,s[92:95],s47 offen offset:0 lds\nM v[196:199],a[36:39],a[0:3],v[196:199]\ns_add_u32 m0,s22,52096\nv_fmac_f32 v26,v41,v47\nv_fmac_f32 v25,v30,v42\n" \
"M v[204:207],a[36:39],a[4:7],v[204:207]\nv_fmac_f32 v26,v36,v46\nv_accvgpr_read_b32 v28,a221\nM v[200:203],a[32:35],a[4:7],v[200:203]\nC v26,v25,v26\nv_lshlrev_b32 v27,16,v28\nM v[216:219],a[40:43],a[4:7],v[216:219]\nbuffer_load_dwordx4 v21,s[92:95],s47 offen offset:128 lds\n" \
"M v[220:223],a[44:47],a[4:7],v[220:223]\ns_add_u32 m0,s50,32768\nv_lshlrev_b32 v25,16,v26\nv_and_b32 v28,0xffff0000,v28\nM v[212:215],a[44:47],a[0:3],v[212:215]\ns_mov_b32 s47,0\nv_and_b32 v26,0xffff0000,v26\nv_mul_f32 v25,v25,v27\n" \
"M v[208:211],a[40:43],a[0:3],v[208:211]\nv_mul_f32 v26,v26,v28\nC v52,v25,v29\nM v[224:227],a[48:51],a[0:3],v[224:227]\nbuffer_load_dwordx4 v13,s[28:31],s46 offen lds\nM v[228:231],a[52:55],a[0:3],v[228:231]\ns_add_u32 m0,s50,36864\nC v56,v26,v58\n" \
"v_accvgpr_read_b32 v37,a207\nM v[236:239],a[52:55],a[4:7],v[236:239]\nv_mul_f32 v25,v34,v45\nv_lshlrev_b32 v31,16,v37\nM v[232:235],a[48:51],a[4:7],v[232:235]\nv_mul_f32 v26,v40,v49\nv_fmac_f32 v25,v35,v44\nM v[248:251],a[56:59],a[4:7],v[248:251]\n" \
"buffer_load_dwordx4 v14,s[28:31],s46 offen lds\nM v[252:255],a[60:63],a[4:7],v[252:255]\ns_add_u32 m0,s50,40960\nv_fmac_f32 v26,v41,v48\nv_and_b32 v37,0xffff0000,v37\nM v[244:247],a[60:63],a[0:3],v[244:247]\nv_fmac_f32 v25,v30,v43\nv_fmac_f32 v26,v36,v47\n" \
"M v[240:243],a[56:59],a[0:3],v[240:243]\nv_fmac_f32 v25,v31,v42\nv_fmac_f32 v26,v37,v46\nM a[128:131],a[32:35],a[8:11],a[128:131]\nbuffer_load_dwordx4 v15,s[28:31],s46 offen lds\nM a[132:135],a[36:39],a[8:11],a[132:135]\ns_add_u32 m0,s50,45056\nv_accvgpr_read_b32 v28,a223\n" \
"C v26,v25,v26\nM a[140:143],a[36:39],a[12:15],a[140:143]\nv_lshlrev_b32 v27,16,v28\nv_lshlrev_b32 v25,16,v26\nM a[136:139],a[32:35],a[12:15],a[136:139]\nv_and_b32 v28,0xffff0000,v28\nv_and_b32 v26,0xffff0000,v26\nM a[152:155],a[40:43],a[12:15],a[152:155]\n" \
"buffer_load_dwordx4 v16,s[28:31],s46 offen lds\nM a[156:159],a[44:47],a[12:15],a[156:159]\ns_add_u32 m0,s50,49152\nv_mul_f32 v29,v25,v27\nv_mul_f32 v58,v26,v28\nM a[148:151],a[44:47],a[8:11],a[148:151]\ns_waitcnt vmcnt(17) lgkmcnt(0)\nv_accvgpr_read_b32 v38,a209\n" \
"v_mul_f32 v25,v35,v45\nM a[144:147],a[40:43],a[8:11],a[144:147]\ns_mov_b32 s46,0\ns_barrier\nds_read_b64_tr_b16 a[32:33],v9 offset:32768\nM a[160:163],a[48:51],a[8:11],a[160:163]\nbuffer_load_dwordx4 v17,s[28:31],s46 offen lds\nM a[164:167],a[52:55],a[8:11],a[164:167]\n" \
"s_add_u32 m0,s50,53248\nds_read_b64_tr_b16 a[34:35],v10 offset:32768\nv_mul_f32 v26,v41,v49\nM a[172:175],a[52:55],a[12:15],a[172:175]\nds_read_b64_tr_b16 a[36:37],v11 offset:32768\nds_read_b64_tr_b16 a[38:39],v12 offset:32768\nM a[168:171],a[48:51],a[12:15],a[168:171]\nds_read_b64_tr_b16 a[40:41],v9 offset:33280\n" \
"v_lshlrev_b32 v32,16,v38\nM a[184:187],a[56:59],a[12:15],a[184:187]\nbuffer_load_dwordx4 v18,s[28:31],s46 offen lds\nM a[188:191],a[60:63],a[12:15],a[188:191]\ns_add_u32 m0,s50,57344\nds_read_b64_tr_b16 a[42:43],v10 offset:33280\nv_fmac_f32 v25,v30,v44\nM a[180:183],a[60:63],a[8:11],a[180:183]\n" \
"ds_read_b64_tr_b16 a[44:45],v11 offset:33280\nv_fmac_f32 v26,v36,v48\nM a[176:179],a[56:59],a[8:11],a[176:179]\nds_read_b64_tr_b16 a[46:47],v12 offset:33280\nv_and_b32 v38,0xffff0000,v38\nM v[128:131],a[96:99],a[88:91],v[128:131]\nbuffer_load_dwordx4 v19,s[28:31],s46 offen lds\nM v[132:135],a[100:103],a[88:91],v[132:135]\n" \
"s_add_u32 m0,s50,61440\nds_read_b64_tr_b16 a[48:49],v9 offset:33792\nv_fmac_f32 v25,v31,v43\nM v[140:143],a[100:103],a[92:95],v[140:143]\nds_read_b64_tr_b16 a[50:51],v10 offset:33792\nv_fmac_f32 v26,v37,v47\nM v[136:139],a[96:99],a[92:95],v[136:139]\nds_read_b64_tr_b16 a[52:53],v11 offset:33792\n" \
"ds_read_b64_tr_b16 a[54:55],v12 offset:33792\nM v[152:155],a[104:107],a[92:95],v[152:155]\nbuffer_load_dwordx4 v20,s[28:31],s46 offen lds\nM v[156:159],a[108:111],a[92:95],v[156:159]\ns_add_u32 m0,s51,0\nv_fmac_f32 v25,v32,v42\nv_fmac_f32 v26,v38,v46\nM v[148:151],a[108:111],a[88:91],v[148:151]\n" \
"s_mov_b32 s46,128\nds_read_b64_tr_b16 a[56:57],v9 offset:34304\nds_read_b64_tr_b16 a[58:59],v10 offset:34304\nM v[144:147],a[104:107],a[88:91],v[144:147]\nds_read_b64_tr_b16 a[60:61],v11 offset:34304\nv_accvgpr_read_b32 v28,a225\nM v[160:163],a[112:115],a[88:91],v[160:163]\nbuffer_load_dwordx4 v21,s[32:35],s47 offen offset:0 lds\n" \
"M v[164:167],a[116:119],a[88:91],v[164:167]\ns_add_u32 m0,s51,896\nds_read_b64_tr_b16 a[62:63],v12 offset:34304\nC v26,v25,v26\nM v[172:175],a[116:119],a[92:95],v[172:175]\nds_read_b128 a[24:27],v5 offset:12288\nv_lshlrev_b32 v27,16,v28\nM v[168:171],a[112:115],a[92:95],v[168:171]\n" \
"ds_read_b128 a[28:31],v5 offset:14336\nv_lshlrev_b32 v25,16,v26\nM v[184:187],a[120:123],a[92:95],v[184:187]\nbuffer_load_dwordx4 v21,s[32:35],s47 offen offset:128 lds\nM v[188:191],a[124:127],a[92:95],v[188:191]\ns_add_u32 m0,s51,8192\nv_and_b32 v28,0xffff0000,v28\nv_and_b32 v26,0xffff0000,v26\n" \
"M v[180:183],a[124:127],a[88:91],v[180:183]\ns_mov_b32 s47,262144\nv_mul_f32 v25,v25,v27\nv_mul_f32 v26,v26,v28\nM v[176:179],a[120:123],a[88:91],v[176:179]\nC v51,v25,v29\nC v55,v26,v58\nM v[64:67],a[96:99],a[80:83],v[64:67]\n" \
"buffer_load_dwordx4 v21,s[32:35],s47 offen offset:0 lds\nM v[68:71],a[100:103],a[80:83],v[68:71]\ns_add_u32 m0,s51,9088\nv_accvgpr_read_b32 v39,a211\nv_mul_f32 v25,v30,v45\nM v[76:79],a[100:103],a[84:87],v[76:79]\nv_lshlrev_b32 v33,16,v39\nv_mul_f32 v26,v36,v49\n" \
"M v[72:75],a[96:99],a[84:87],v[72:75]\nv_fmac_f32 v25,v31,v44\nv_fmac_f32 v26,v37,v48\nM v[88:91],a[104:107],a[84:87],v[88:91]\nbuffer_load_dwordx4 v21,s[32:35],s47 offen offset:128 lds\nM v[92:95],a[108:111],a[84:87],v[92:95]\ns_add_u32 m0,s51,16384\nv_and_b32 v39,0xffff0000,v39\n" \
"v_fmac_f32 v25,v32,v43\nM v[84:87],a[108:111],a[80:83],v[84:87]\ns_mov_b32 s47,524288\nv_fmac_f32 v26,v38,v47\nv_fmac_f32 v25,v33,v42\nM v[80:83],a[104:107],a[80:83],v[80:83]\nv_fmac_f32 v26,v39,v46\nv_accvgpr_read_b32 v28,a227\n" \
"M v[96:99],a[112:115],a[80:83],v[96:99]\nbuffer_load_dwordx4 v21,s[32:35],s47 offen offset:0 lds\nM v[100:103],a[116:119],a[80:83],v[100:103]\ns_add_u32 m0,s51,17280\nC v26,v25,v26\nv_lshlrev_b32 v27,16,v28\nM v[108:111],a[116:119],a[84:87],v[108:111]\nds_read_b128 a[16:19],v5 offset:8192\n" \
"v_lshlrev_b32 v25,16,v26\nM v[104:107],a[112:115],a[84:87],v[104:107]\nds_read_b128 a[20:23],v5 offset:10240\nv_and_b32 v28,0xffff0000,v28\nM v[120:123],a[120:123],a[84:87],v[120:123]\nbuffer_load_dwordx4 v21,s[32:35],s47 offen offset:128 lds\nM v[124:127],a[124:127],a[84:87],v[124:127]\ns_add_u32 m0,s51,24576\n" \
"v_and_b32 v26,0xffff0000,v26\nv_mul_f32 v29,v25,v27\nM v[116:119],a[124:127],a[80:83],v[116:119]\ns_mov_b32 s47,786432\nv_mul_f32 v58,v26,v28\nv_accvgpr_read_b32 v40,a213\nM v[112:115],a[120:123],a[80:83],v[112:115]\nv_lshlrev_b32 v34,16,v40\n" \
"v_mul_f32 v25,v31,v45\nM v[192:195],a[96:99],a[64:67],v[192:195]\ns_mov_b64 exec,0xff\nglobal_load_dword v0,v1,s[76:77]\ns_mov_b64 exec,-1\nM v[196:199],a[100:103],a[64:67],v[196:199]\nv_mul_f32 v26,v37,v49\nv_fmac_f32 v25,v32,v44\n" \
"M v[204:207],a[100:103],a[68:71],v[204:207]\ns_mov_b64 exec,0xff00\nglobal_load_dword v0,v1,s[76:77]\ns_mov_b64 exec,-1\nM v[200:203],a[96:99],a[68:71],v[200:203]\ns_add_u32 s76,s76,2097152\ns_addc_u32 s77,s77,0\nv_and_b32 v40,0xffff0000,v40\n" \
"v_fmac_f32 v26,v38,v48\nM v[216:219],a[104:107],a[68:71],v[216:219]\ns_mov_b64 exec,0xff\nglobal_load_dword v0,v1,s[76:77]\ns_mov_b64 exec,-1\nM v[220:223],a[108:111],a[68:71],v[220:223]\nv_fmac_f32 v25,v33,v43\nv_fmac_f32 v26,v39,v47\n" \
"M v[212:215],a[108:111],a[64:67],v[212:215]\ns_mov_b64 exec,0xff00\nglobal_load_dword v0,v1,s[76:77]\ns_mov_b64 exec,-1\nM v[208:211],a[104:107],a[64:67],v[208:211]\ns_mov_b32 s76,0xf149f2ca\nv_fmac_f32 v25,v34,v42\nv_fmac_f32 v26,v40,v46\n" \
"M v[224:227],a[112:115],a[64:67],v[224:227]\nv_accvgpr_read_b32 v28,a229\nC v26,v25,v26\nM v[228:231],a[116:119],a[64:67],v[228:231]\nv_lshlrev_b32 v27,16,v28\nv_lshlrev_b32 v25,16,v26\nM v[236:239],a[116:119],a[68:71],v[236:239]\nv_mul_f32 v25,v25,v27\n" \
"C v50,v25,v29\nM v[232:235],a[112:115],a[68:71],v[232:235]\nds_read_b128 a[0:3],v5 offset:0\nds_read_b128 a[4:7],v5 offset:2048\nM v[248:251],a[120:123],a[68:71],v[248:251]\nds_read_b128 a[8:11],v5 offset:4096\nv_and_b32 v28,0xffff0000,v28\nM v[252:255],a[124:127],a[68:71],v[252:255]\n" \
"ds_read_b128 a[12:15],v5 offset:6144\nv_and_b32 v26,0xffff0000,v26\nM v[244:247],a[124:127],a[64:67],v[244:247]\nv_mul_f32 v26,v26,v28\nC v54,v26,v58\nM v[240:243],a[120:123],a[64:67],v[240:243]\nds_read_b128 a[64:67],v6 offset:0\nds_read_b128 a[68:71],v6 offset:2048\n" \
"M a[128:131],a[96:99],a[72:75],a[128:131]\nds_read_b128 a[80:83],v6 offset:8192\nv_add_u32 v5,s54,v5\nM a[132:135],a[100:103],a[72:75],a[132:135]\nds_read_b128 a[84:87],v6 offset:10240\nv_add_u32 v7,s54,v7\nM a[140:143],a[100:103],a[76:79],a[140:143]\nds_read_b128 a[88:91],v6 offset:12288\n" \
"v_add_u32 v8,s54,v8\nM a[136:139],a[96:99],a[76:79],a[136:139]\nds_read_b128 a[92:95],v6 offset:14336\nv_mov_b32 v58,s18\nM a[152:155],a[104:107],a[76:79],a[152:155]\nv_add_u32 v25,-16,v4\nv_mov_b32 v29,s62\nM a[156:159],a[108:111],a[76:79],a[156:159]\n" \
"s_and_b32 s62,%1,7\nv_mov_b32 v28,0\nv_add_u32 v26,-512,v4\nM a[148:151],a[108:111],a[72:75],a[148:151]\ns_waitcnt vmcnt(18) lgkmcnt(14)\nv_add_u32 v27,512,v4\nM a[144:147],a[104:107],a[72:75],a[144:147]\ns_lshl_b32 s62,s62,8\n" \
"s_barrier\nds_read_b64_tr_b16 a[96:97],v9 offset:49152\nM a[160:163],a[112:115],a[72:75],a[160:163]\ns_add_u32 s62,s62,94380032\nds_read_b64_tr_b16 a[98:99],v10 offset:49152\nds_read_b64_tr_b16 a[100:101],v11 offset:49152\nM a[164:167],a[116:119],a[72:75],a[164:167]\nds_read_b64_tr_b16 a[102:103],v12 offset:49152\n" \
"M a[172:175],a[116:119],a[76:79],a[172:175]\nds_read_b64_tr_b16 a[104:105],v9 offset:49664\nds_read_b64_tr_b16 a[106:107],v10 offset:49664\nM a[168:171],a[112:115],a[76:79],a[168:171]\nLG3_%=:\ns_cbranch_execz LG7_%=\nds_read_b64_tr_b16 a[108:109],v11 offset:49664\nM a[184:187],a[120:123],a[76:79],a[184:187]\n" \
"ds_read_b64_tr_b16 a[110:111],v12 offset:49664\nM a[188:191],a[124:127],a[76:79],a[188:191]\nds_read_b64_tr_b16 a[112:113],v9 offset:50176\nds_read_b64_tr_b16 a[114:115],v10 offset:50176\nM a[180:183],a[124:127],a[72:75],a[180:183]\nds_read_b64_tr_b16 a[116:117],v11 offset:50176\nM a[176:179],a[120:123],a[72:75],a[176:179]\ns_waitcnt lgkmcnt(7)\n" \
"ds_read_b128 a[72:75],v6 offset:4096\nM v[128:131],a[32:35],a[24:27],v[128:131]\nds_read_b128 a[76:79],v6 offset:6144\nv_add_u32 v6,s54,v6\nM v[132:135],a[36:39],a[24:27],v[132:135]\nds_read_b64_tr_b16 a[118:119],v12 offset:50176\nM v[140:143],a[36:39],a[28:31],v[140:143]\nds_read_b64_tr_b16 a[120:121],v9 offset:50688\n" \
"ds_read_b64_tr_b16 a[122:123],v10 offset:50688\nM v[136:139],a[32:35],a[28:31],v[136:139]\nds_read_b64_tr_b16 a[124:125],v11 offset:50688\nv_sub_u32 v9,v9,v62\nM v[152:155],a[40:43],a[28:31],v[152:155]\nds_read_b64_tr_b16 a[126:127],v12 offset:50688\nv_sub_u32 v10,v10,v62\nM v[156:159],a[44:47],a[28:31],v[156:159]\n" \
"v_sub_u32 v11,v11,v62\nv_sub_u32 v12,v12,v62\nM v[148:151],a[44:47],a[24:27],v[148:151]\nM v[144:147],a[40:43],a[24:27],v[144:147]\nM v[160:163],a[48:51],a[24:27],v[160:163]\nM v[164:167],a[52:55],a[24:27],v[164:167]\nM v[172:175],a[52:55],a[28:31],v[172:175]\nM v[168:171],a[48:51],a[28:31],v[168:171]\n" \
"M v[184:187],a[56:59],a[28:31],v[184:187]\nM v[188:191],a[60:63],a[28:31],v[188:191]\nM v[180:183],a[60:63],a[24:27],v[180:183]\nbuffer_load_dwordx4 v21,s[32:35],s47 offen offset:0 lds\nM v[176:179],a[56:59],a[24:27],v[176:179]\ns_add_u32 m0,s51,25472\nM v[64:67],a[32:35],a[16:19],v[64:67]\nM v[68:71],a[36:39],a[16:19],v[68:71]\n" \
"M v[76:79],a[36:39],a[20:23],v[76:79]\nbuffer_store_dwordx4 v[50:53],v60,s[36:39],s98 offen offset:0 sc1\nM v[72:75],a[32:35],a[20:23],v[72:75]\nM v[88:91],a[40:43],a[20:23],v[88:91]\nM v[92:95],a[44:47],a[20:23],v[92:95]\nM v[84:87],a[44:47],a[16:19],v[84:87]\nM v[80:83],a[40:43],a[16:19],v[80:83]\nM v[96:99],a[48:51],a[16:19],v[96:99]\n" \
"M v[100:103],a[52:55],a[16:19],v[100:103]\nM v[108:111],a[52:55],a[20:23],v[108:111]\nM v[104:107],a[48:51],a[20:23],v[104:107]\nM v[120:123],a[56:59],a[20:23],v[120:123]\nM v[124:127],a[60:63],a[20:23],v[124:127]\nM v[116:119],a[60:63],a[16:19],v[116:119]\nM v[112:115],a[56:59],a[16:19],v[112:115]\nM v[192:195],a[32:35],a[0:3],v[192:195]\n" \
"M v[196:199],a[36:39],a[0:3],v[196:199]\nM v[204:207],a[36:39],a[4:7],v[204:207]\nM v[200:203],a[32:35],a[4:7],v[200:203]\nM v[216:219],a[40:43],a[4:7],v[216:219]\nM v[220:223],a[44:47],a[4:7],v[220:223]\nM v[212:215],a[44:47],a[0:3],v[212:215]\nM v[208:211],a[40:43],a[0:3],v[208:211]\nM v[224:227],a[48:51],a[0:3],v[224:227]\n" \
"M v[228:231],a[52:55],a[0:3],v[228:231]\nM v[236:239],a[52:55],a[4:7],v[236:239]\nbuffer_store_dwordx4 v[54:57],v60,s[36:39],s99 offen offset:0 sc1\nM v[232:235],a[48:51],a[4:7],v[232:235]\ns_nop 0\nv_lshlrev_b32 v56,23,v4\nv_bfe_u32 v57,v4,4,5\nM v[248:251],a[56:59],a[4:7],v[248:251]\n" \
"v_lshl_add_u32 v57,v57,3,s62\nv_add_u32 v55,s56,v4\nM v[252:255],a[60:63],a[4:7],v[252:255]\nM v[244:247],a[60:63],a[0:3],v[244:247]\nM v[240:243],a[56:59],a[0:3],v[240:243]\nM a[128:131],a[32:35],a[8:11],a[128:131]\nM a[132:135],a[36:39],a[8:11],a[132:135]\nM a[140:143],a[36:39],a[12:15],a[140:143]\n" \
"M a[136:139],a[32:35],a[12:15],a[136:139]\nM a[152:155],a[40:43],a[12:15],a[152:155]\nM a[156:159],a[44:47],a[12:15],a[156:159]\nM a[148:151],a[44:47],a[8:11],a[148:151]\ns_waitcnt vmcnt(9) lgkmcnt(0)\nM a[144:147],a[40:43],a[8:11],a[144:147]\ns_barrier\nds_read_b64_tr_b16 a[0:1],v9 offset:0\n" \
"M a[160:163],a[48:51],a[8:11],a[160:163]\nds_read_b64_tr_b16 a[2:3],v10 offset:0\nds_read_b64_tr_b16 a[4:5],v11 offset:0\nM a[164:167],a[52:55],a[8:11],a[164:167]\nds_read_b128 a[44:47],v5 offset:45056\nM a[172:175],a[52:55],a[12:15],a[172:175]\nds_read_b64_tr_b16 a[6:7],v12 offset:0\nM a[168:171],a[48:51],a[12:15],a[168:171]\n" \
"ds_read_b64_tr_b16 a[16:17],v9 offset:1024\nds_read_b64_tr_b16 a[18:19],v10 offset:1024\nM a[184:187],a[56:59],a[12:15],a[184:187]\nds_read_b64_tr_b16 a[20:21],v11 offset:1024\nM a[188:191],a[60:63],a[12:15],a[188:191]\nds_read_b64_tr_b16 a[22:23],v12 offset:1024\nM a[180:183],a[60:63],a[8:11],a[180:183]\nds_read_b64_tr_b16 a[24:25],v9 offset:1536\n" \
"ds_read_b64_tr_b16 a[26:27],v10 offset:1536\nM a[176:179],a[56:59],a[8:11],a[176:179]\nds_read_b64_tr_b16 a[8:9],v9 offset:512\nM v[128:131],a[96:99],a[88:91],v[128:131]\nds_read_b64_tr_b16 a[10:11],v10 offset:512\nM v[132:135],a[100:103],a[88:91],v[132:135]\nds_read_b64_tr_b16 a[12:13],v11 offset:512\nds_read_b64_tr_b16 a[14:15],v12 offset:512\n" \
"M v[140:143],a[100:103],a[92:95],v[140:143]\nbuffer_load_dwordx4 v21,s[32:35],s47 offen offset:128 lds\nM v[136:139],a[96:99],a[92:95],v[136:139]\ns_add_u32 m0,s50,65536\nv_mul_f32 v128,s20,v128\nv_mul_f32 v129,s20,v129\nM v[152:155],a[104:107],a[92:95],v[152:155]\ns_mov_b32 s47,1048576\n" \
"v_mul_f32 v130,s20,v130\nv_mul_f32 v131,s20,v131\nM v[156:159],a[108:111],a[92:95],v[156:159]\nv_mul_f32 v132,s20,v132\nv_mul_f32 v133,s20,v133\nM v[148:151],a[108:111],a[88:91],v[148:151]\nbuffer_load_dwordx4 v13,s[28:31],s46 offen lds\nM v[144:147],a[104:107],a[88:91],v[144:147]\n" \
"s_add_u32 m0,s50,69632\nv_mul_f32 v134,s20,v134\nv_mul_f32 v135,s20,v135\nM v[160:163],a[112:115],a[88:91],v[160:163]\nv_mul_f32 v136,s20,v136\nv_mul_f32 v137,s20,v137\nM v[164:167],a[116:119],a[88:91],v[164:167]\nv_mul_f32 v138,s20,v138\n" \
"v_mul_f32 v139,s20,v139\nM v[172:175],a[116:119],a[92:95],v[172:175]\nbuffer_load_dwordx4 v14,s[28:31],s46 offen lds\nM v[168:171],a[112:115],a[92:95],v[168:171]\ns_add_u32 m0,s50,73728\nds_read_b64_tr_b16 a[28:29],v11 offset:1536\nv_mul_f32 v140,s20,v140\nM v[184:187],a[120:123],a[92:95],v[184:187]\n" \
"ds_read_b64_tr_b16 a[30:31],v12 offset:1536\nds_read_b128 a[40:43],v5 offset:40960\nM v[188:191],a[124:127],a[92:95],v[188:191]\nv_mul_f32 v141,s20,v141\nv_mul_f32 v142,s20,v142\nM v[180:183],a[124:127],a[88:91],v[180:183]\nbuffer_load_dwordx4 v15,s[28:31],s46 offen lds\nM v[176:179],a[120:123],a[88:91],v[176:179]\n" \
"s_add_u32 m0,s50,77824\nv_mul_f32 v143,s20,v143\nv_mul_f32 v144,s20,v144\nM v[64:67],a[96:99],a[80:83],v[64:67]\nv_mul_f32 v145,s20,v145\nv_mul_f32 v146,s20,v146\nM v[68:71],a[100:103],a[80:83],v[68:71]\nds_read_b128 a[32:35],v5 offset:32768\n" \
"v_mul_f32 v147,s20,v147\nM v[76:79],a[100:103],a[84:87],v[76:79]\nbuffer_load_dwordx4 v16,s[28:31],s46 offen lds\nM v[72:75],a[96:99],a[84:87],v[72:75]\ns_add_u32 m0,s50,81920\ns_mov_b32 s46,128\nv_mul_f32 v64,s20,v64\nv_mul_f32 v65,s20,v65\n" \
"M v[88:91],a[104:107],a[84:87],v[88:91]\nv_mul_f32 v66,s20,v66\nv_mul_f32 v67,s20,v67\nM v[92:95],a[108:111],a[84:87],v[92:95]\nC v64,v64,v65\nC v65,v66,v67\nM v[84:87],a[108:111],a[80:83],v[84:87]\nbuffer_load_dwordx4 v17,s[28:31],s46 offen lds\n" \
"M v[80:83],a[104:107],a[80:83],v[80:83]\ns_add_u32 m0,s50,86016\nv_mul_f32 v68,s20,v68\nv_mul_f32 v69,s20,v69\nM v[96:99],a[112:115],a[80:83],v[96:99]\nv_mul_f32 v148,s20,v148\nv_mul_f32 v149,s20,v149\nM v[100:103],a[116:119],a[80:83],v[100:103]\n" \
"v_mul_f32 v150,s20,v150\nv_mul_f32 v151,s20,v151\nM v[108:111],a[116:119],a[84:87],v[108:111]\nbuffer_load_dwordx4 v18,s[28:31],s46 offen lds\nM v[104:107],a[112:115],a[84:87],v[104:107]\ns_add_u32 m0,s50,90112\nv_mul_f32 v70,s20,v70\nv_mul_f32 v152,s20,v152\n" \
"M v[120:123],a[120:123],a[84:87],v[120:123]\nv_mul_f32 v71,s20,v71\nv_mul_f32 v153,s20,v153\nM v[124:127],a[124:127],a[84:87],v[124:127]\nv_mul_f32 v154,s20,v154\nv_mul_f32 v155,s20,v155\nM v[116:119],a[124:127],a[80:83],v[116:119]\nbuffer_load_dwordx4 v19,s[28:31],s46 offen lds\n" \
"M v[112:115],a[120:123],a[80:83],v[112:115]\ns_add_u32 m0,s50,94208\nv_mul_f32 v156,s20,v156\nv_mul_f32 v157,s20,v157\nM v[192:195],a[96:99],a[64:67],v[192:195]\nv_mul_f32 v158,s20,v158\nv_mul_f32 v159,s20,v159\nM v[196:199],a[100:103],a[64:67],v[196:199]\n" \
"v_mul_f32 v160,s20,v160\nv_mul_f32 v161,s20,v161\nM v[204:207],a[100:103],a[68:71],v[204:207]\nbuffer_load_dwordx4 v20,s[28:31],s46 offen lds\nM v[200:203],a[96:99],a[68:71],v[200:203]\ns_add_u32 m0,s51,32768\nds_read_b128 a[36:39],v5 offset:36864\nv_mul_f32 v162,s20,v162\n" \
"M v[216:219],a[104:107],a[68:71],v[216:219]\ns_mov_b32 s46,256\nv_mul_f32 v163,s20,v163\nC v66,v68,v69\nM v[220:223],a[108:111],a[68:71],v[220:223]\nv_mul_f32 v72,s20,v72\nv_mul_f32 v164,s20,v164\nM v[212:215],a[108:111],a[64:67],v[212:215]\n" \
"buffer_load_dwordx4 v21,s[32:35],s47 offen offset:0 lds\nM v[208:211],a[104:107],a[64:67],v[208:211]\ns_add_u32 m0,s51,33664\nv_mul_f32 v73,s20,v73\nC v30,v128,v129\nM v[224:227],a[112:115],a[64:67],v[224:227]\nv_mul_f32 v165,s20,v165\nC v31,v130,v131\n" \
"M v[228:231],a[116:119],a[64:67],v[228:231]\nv_mul_f32 v166,s20,v166\nC v32,v132,v133\nM v[236:239],a[116:119],a[68:71],v[236:239]\nbuffer_load_dwordx4 v21,s[32:35],s47 offen offset:128 lds\nM v[232:235],a[112:115],a[68:71],v[232:235]\ns_add_u32 m0,s51,40960\nv_mul_f32 v167,s20,v167\n" \
"C v33,v134,v135\nM v[248:251],a[120:123],a[68:71],v[248:251]\ns_mov_b32 s47,1310720\nv_mul_f32 v168,s20,v168\nC v34,v136,v137\nM v[252:255],a[124:127],a[68:71],v[252:255]\nv_mul_f32 v169,s20,v169\nC v35,v138,v139\n" \
"M v[244:247],a[124:127],a[64:67],v[244:247]\nbuffer_load_dwordx4 v21,s[32:35],s47 offen offset:0 lds\nM v[240:243],a[120:123],a[64:67],v[240:243]\ns_add_u32 m0,s51,41856\nds_read_b64_tr_b16 a[48:49],v9 offset:8192\nv_mul_f32 v170,s20,v170\nM a[128:131],a[96:99],a[72:75],a[128:131]\nds_read_b64_tr_b16 a[50:51],v10 offset:8192\n" \
"ds_read_b128 a[80:83],v6 offset:32768\nM a[132:135],a[100:103],a[72:75],a[132:135]\nds_read_b128 a[84:87],v6 offset:36864\nv_mul_f32 v171,s20,v171\nM a[140:143],a[100:103],a[76:79],a[140:143]\nbuffer_load_dwordx4 v21,s[32:35],s47 offen offset:128 lds\nM a[136:139],a[96:99],a[76:79],a[136:139]\ns_add_u32 m0,s50,0\n" \
"v_mul_f32 v74,s20,v74\nC v36,v140,v141\nM a[152:155],a[104:107],a[76:79],a[152:155]\ns_mov_b32 s47,1572864\nds_read_b64_tr_b16 a[52:53],v11 offset:8192\nv_mul_f32 v75,s20,v75\nM a[156:159],a[108:111],a[76:79],a[156:159]\nds_read_b64_tr_b16 a[54:55],v12 offset:8192\n" \
"ds_read_b64_tr_b16 a[56:57],v9 offset:8704\nM a[148:151],a[108:111],a[72:75],a[148:151]\nbuffer_load_dwordx4 v13,s[28:31],s46 offen lds\nM a[144:147],a[104:107],a[72:75],a[144:147]\ns_add_u32 m0,s50,4096\nC v37,v142,v143\nC v38,v144,v145\nM a[160:163],a[112:115],a[72:75],a[160:163]\n" \
"ds_read_b64_tr_b16 a[58:59],v10 offset:8704\nds_read_b64_tr_b16 a[60:61],v11 offset:8704\nM a[164:167],a[116:119],a[72:75],a[164:167]\nds_read_b64_tr_b16 a[62:63],v12 offset:8704\nC v39,v146,v147\nM a[172:175],a[116:119],a[76:79],a[172:175]\nbuffer_load_dwordx4 v14,s[28:31],s46 offen lds\nM a[168:171],a[112:115],a[76:79],a[168:171]\n" \
"s_add_u32 m0,s50,8192\nds_read_b64_tr_b16 a[64:65],v9 offset:9216\nv_mul_f32 v172,s20,v172\nM a[184:187],a[120:123],a[76:79],a[184:187]\nds_read_b64_tr_b16 a[66:67],v10 offset:9216\nv_mul_f32 v173,s20,v173\nM a[188:191],a[124:127],a[76:79],a[188:191]\ns_waitcnt lgkmcnt(5)\n" \
"ds_read_b64_tr_b16 a[68:69],v11 offset:9216\nv_mul_f32 v174,s20,v174\nM a[180:183],a[124:127],a[72:75],a[180:183]\nbuffer_load_dwordx4 v15,s[28:31],s46 offen lds\nM a[176:179],a[120:123],a[72:75],a[176:179]\ns_branch LC_%=\nLAV_%=:\nds_read_b64_tr_b16 a[32:33],v9 offset:0\n" \
"ds_read_b64_tr_b16 a[34:35],v10 offset:0\nds_read_b64_tr_b16 a[36:37],v11 offset:0\nds_read_b64_tr_b16 a[38:39],v12 offset:0\nds_read_b128 a[24:27],v7 offset:12288\nds_read_b128 a[28:31],v7 offset:14336\ns_waitcnt lgkmcnt(0)\nM v[128:131],a[24:27],a[32:35],0\ns_add_u32 m0,s22,32768\n" \
"s_mov_b32 s47,524288\ns_mov_b32 s46,128\nds_read_b64_tr_b16 a[40:41],v9 offset:512\nds_read_b64_tr_b16 a[42:43],v10 offset:512\nds_read_b64_tr_b16 a[44:45],v11 offset:512\nds_read_b64_tr_b16 a[46:47],v12 offset:512\nds_read_b64_tr_b16 a[48:49],v9 offset:1024\nds_read_b64_tr_b16 a[50:51],v10 offset:1024\n" \
"ds_read_b64_tr_b16 a[52:53],v11 offset:1024\nds_read_b64_tr_b16 a[54:55],v12 offset:1024\nM v[132:135],a[28:31],a[32:35],0\nbuffer_load_dwordx4 v21,s[32:35],s47 offen offset:0 lds\nds_read_b64_tr_b16 a[56:57],v9 offset:1536\nds_read_b64_tr_b16 a[58:59],v10 offset:1536\nds_read_b64_tr_b16 a[60:61],v11 offset:1536\nds_read_b64_tr_b16 a[62:63],v12 offset:1536\n" \
"M v[140:143],a[28:31],a[36:39],0\ns_add_u32 m0,s22,33664\nM v[136:139],a[24:27],a[36:39],0\ns_waitcnt lgkmcnt(8)\ns_mov_b64 s[40:41],s[66:67]\nds_read_b128 a[16:19],v7 offset:8192\nM v[152:155],a[24:27],a[44:47],0\ns_and_b32 s41,s41,0xffff\n" \
"ds_read_b128 a[20:23],v7 offset:10240\nM v[156:159],a[28:31],a[44:47],0\nbuffer_load_dwordx4 v21,s[32:35],s47 offen offset:128 lds\nM v[148:151],a[28:31],a[40:43],0\ns_add_u32 m0,s22,34816\nds_read_b128 a[0:3],v7 offset:0\nM v[144:147],a[24:27],a[40:43],0\ns_waitcnt lgkmcnt(3)\n" \
"s_mov_b32 s42,4194304\nds_read_b128 a[4:7],v7 offset:2048\nM v[160:163],a[24:27],a[48:51],0\ns_mov_b32 s43,0x00020000\nds_read_b128 a[8:11],v7 offset:4096\nM v[164:167],a[28:31],a[48:51],0\nbuffer_load_dwordx4 v21,s[92:95],s47 offen offset:0 lds\nM v[172:175],a[28:31],a[52:55],0\n" \
"s_add_u32 m0,s22,35712\nM v[168:171],a[24:27],a[52:55],0\ns_mov_b64 s[80:81],s[68:69]\nds_read_b128 a[12:15],v7 offset:6144\nM v[184:187],a[24:27],a[60:63],0\ns_mov_b32 s52,0\nds_read_b128 a[88:91],v8 offset:12288\nM v[188:191],a[28:31],a[60:63],0\n" \
"buffer_load_dwordx4 v21,s[92:95],s47 offen offset:128 lds\nM v[180:183],a[28:31],a[56:59],0\ns_add_u32 m0,s50,0\nds_read_b128 a[92:95],v8 offset:14336\nM v[176:179],a[24:27],a[56:59],0\ns_waitcnt lgkmcnt(3)\ns_mov_b32 s47,786432\nds_read_b128 a[80:83],v8 offset:8192\n" \
"M v[64:67],a[16:19],a[32:35],0\ns_waitcnt vmcnt(8) lgkmcnt(9)\nds_read_b128 a[84:87],v8 offset:10240\nM v[68:71],a[20:23],a[32:35],0\nbuffer_load_dwordx4 v13,s[28:31],s46 offen lds\ns_barrier\nM v[76:79],a[20:23],a[36:39],0\ns_add_u32 m0,s50,4096\n" \
"M v[72:75],a[16:19],a[36:39],0\ns_and_b32 s81,s81,0xffff\nds_read_b64_tr_b16 a[96:97],v9 offset:16384\nds_read_b64_tr_b16 a[98:99],v10 offset:16384\nM v[88:91],a[16:19],a[44:47],0\ns_mov_b32 s82,4194304\nds_read_b64_tr_b16 a[100:101],v11 offset:16384\nM v[92:95],a[20:23],a[44:47],0\n" \
"buffer_load_dwordx4 v14,s[28:31],s46 offen lds\nM v[84:87],a[20:23],a[40:43],0\ns_add_u32 m0,s50,8192\nds_read_b64_tr_b16 a[102:103],v12 offset:16384\nM v[80:83],a[16:19],a[40:43],0\ns_mov_b32 s83,0x00020000\nds_read_b64_tr_b16 a[104:105],v9 offset:16896\nM v[96:99],a[16:19],a[48:51],0\n" \
"s_lshl_b32 s60,s7,18\nds_read_b64_tr_b16 a[106:107],v10 offset:16896\nM v[100:103],a[20:23],a[48:51],0\nbuffer_load_dwordx4 v15,s[28:31],s46 offen lds\nM v[108:111],a[20:23],a[52:55],0\ns_add_u32 m0,s50,12288\nds_read_b64_tr_b16 a[108:109],v11 offset:16896\nM v[104:107],a[16:19],a[52:55],0\n" \
"s_sub_u32 s61,3932160,s60\nds_read_b64_tr_b16 a[110:111],v12 offset:16896\nM v[120:123],a[16:19],a[60:63],0\ns_add_u32 s44,s78,s98\ns_addc_u32 s45,s79,0\nds_read_b64_tr_b16 a[112:113],v9 offset:17408\nds_read_b64_tr_b16 a[114:115],v10 offset:17408\nM v[124:127],a[20:23],a[60:63],0\n" \
"buffer_load_dwordx4 v16,s[28:31],s46 offen lds\nM v[116:119],a[20:23],a[56:59],0\ns_add_u32 m0,s50,16384\ns_mov_b32 s46,128\nM v[112:115],a[16:19],a[56:59],0\ns_and_b32 s45,s45,0xffff\nds_read_b64_tr_b16 a[116:117],v11 offset:17408\nds_read_b64_tr_b16 a[118:119],v12 offset:17408\n" \
"M v[192:195],a[0:3],a[32:35],0\ns_mov_b64 s[36:37],s[90:91]\nds_read_b64_tr_b16 a[120:121],v9 offset:17920\nM v[196:199],a[4:7],a[32:35],0\nbuffer_load_dwordx4 v17,s[28:31],s46 offen lds\nM v[204:207],a[4:7],a[36:39],0\ns_add_u32 m0,s50,20480\nds_read_b64_tr_b16 a[122:123],v10 offset:17920\n" \
"M v[200:203],a[0:3],a[36:39],0\ns_and_b32 s37,s37,0xffff\nds_read_b64_tr_b16 a[124:125],v11 offset:17920\nM v[216:219],a[0:3],a[44:47],0\ns_mov_b32 s38,134217728\nds_read_b64_tr_b16 a[126:127],v12 offset:17920\nM v[220:223],a[4:7],a[44:47],0\nbuffer_load_dwordx4 v18,s[28:31],s46 offen lds\n" \
"M v[212:215],a[4:7],a[40:43],0\ns_add_u32 m0,s50,24576\nds_read_b128 a[64:67],v8 offset:0\nM v[208:211],a[0:3],a[40:43],0\ns_mov_b32 s39,0x00020000\nds_read_b128 a[68:71],v8 offset:2048\nM v[224:227],a[0:3],a[48:51],0\ns_add_u32 s96,s57,32768\n" \
"ds_read_b128 a[72:75],v8 offset:4096\nM v[228:231],a[4:7],a[48:51],0\nbuffer_load_dwordx4 v19,s[28:31],s46 offen lds\nM v[236:239],a[4:7],a[52:55],0\ns_add_u32 m0,s50,28672\nM v[232:235],a[0:3],a[52:55],0\ns_lshr_b32 s62,%1,3\nds_read_b128 a[76:79],v8 offset:6144\n" \
"M v[248:251],a[0:3],a[60:63],0\ns_lshl_b32 s62,s62,2\nM v[252:255],a[4:7],a[60:63],0\nbuffer_load_dwordx4 v20,s[28:31],s46 offen lds\nM v[244:247],a[4:7],a[56:59],0\ns_add_u32 m0,s22,49152\nM v[240:243],a[0:3],a[56:59],0\ns_waitcnt lgkmcnt(1)\n" \
"s_mov_b32 s46,256\nM a[128:131],a[8:11],a[32:35],0\ns_add_u32 s62,s62,%2\nM a[132:135],a[12:15],a[32:35],0\nbuffer_load_dwordx4 v21,s[32:35],s47 offen offset:0 lds\nM a[140:143],a[12:15],a[36:39],0\ns_add_u32 m0,s22,50048\nM a[136:139],a[8:11],a[36:39],0\n" \
"s_lshl_b32 s62,s62,10\nM a[152:155],a[8:11],a[44:47],0\ns_min_u32 s62,s62,LEND_%=-LBEG_%=-1024\nM a[156:159],a[12:15],a[44:47],0\nbuffer_load_dwordx4 v21,s[32:35],s47 offen offset:128 lds\nM a[148:151],a[12:15],a[40:43],0\ns_add_u32 m0,s22,51200\nM a[144:147],a[8:11],a[40:43],0\n" \
"s_add_u32 s100,s100,s62\ns_addc_u32 s101,s101,0\nM a[160:163],a[8:11],a[48:51],0\ns_lshl_b32 s62,s8,2\nM a[164:167],a[12:15],a[48:51],0\nbuffer_load_dwordx4 v21,s[92:95],s47 offen offset:0 lds\nM a[172:175],a[12:15],a[52:55],0\ns_add_u32 m0,s22,52096\n" \
"M a[168:171],a[8:11],a[52:55],0\ns_add_u32 s62,s62,%2\nM a[184:187],a[8:11],a[60:63],0\ns_mov_b64 s[10:11],s[72:73]\nM a[188:191],a[12:15],a[60:63],0\nbuffer_load_dwordx4 v21,s[92:95],s47 offen offset:128 lds\nM a[180:183],a[12:15],a[56:59],0\ns_add_u32 m0,s50,32768\n" \
"M a[176:179],a[8:11],a[56:59],0\ns_mov_b32 s47,1048576\nM v[128:131],a[88:91],a[96:99],v[128:131]\ns_lshl_b32 s63,s62,12\nM v[132:135],a[92:95],a[96:99],v[132:135]\nbuffer_load_dwordx4 v13,s[28:31],s46 offen lds\nM v[140:143],a[92:95],a[100:103],v[140:143]\ns_add_u32 m0,s50,36864\n" \
"M v[136:139],a[88:91],a[100:103],v[136:139]\ns_add_u32 s68,s10,s63\ns_addc_u32 s69,s11,0\nM v[152:155],a[88:91],a[108:111],v[152:155]\ns_and_b32 s69,s69,0xffff\nM v[156:159],a[92:95],a[108:111],v[156:159]\nbuffer_load_dwordx4 v14,s[28:31],s46 offen lds\nM v[148:151],a[92:95],a[104:107],v[148:151]\n" \
"s_add_u32 m0,s50,40960\nM v[144:147],a[88:91],a[104:107],v[144:147]\ns_lshl_b32 s62,s62,1\nM v[160:163],a[88:91],a[112:115],v[160:163]\ns_mov_b64 s[12:13],s[74:75]\nM v[164:167],a[92:95],a[112:115],v[164:167]\nbuffer_load_dwordx4 v15,s[28:31],s46 offen lds\nM v[172:175],a[92:95],a[116:119],v[172:175]\n" \
"s_add_u32 m0,s50,45056\nM v[168:171],a[88:91],a[116:119],v[168:171]\ns_sub_u32 s14,s18,2000\ns_subb_u32 s15,s19,0\nM v[184:187],a[88:91],a[124:127],v[184:187]\ns_add_u32 s16,s14,16777216\ns_addc_u32 s17,s15,0\nM v[188:191],a[92:95],a[124:127],v[188:191]\n" \
"buffer_load_dwordx4 v16,s[28:31],s46 offen lds\nM v[180:183],a[92:95],a[120:123],v[180:183]\ns_add_u32 m0,s50,49152\nM v[176:179],a[88:91],a[120:123],v[176:179]\ns_mov_b32 s46,256\nM v[64:67],a[80:83],a[96:99],v[64:67]\ns_lshr_b32 s21,s63,5\nM v[68:71],a[84:87],a[96:99],v[68:71]\n" \
"buffer_load_dwordx4 v17,s[28:31],s46 offen lds\nM v[76:79],a[84:87],a[100:103],v[76:79]\ns_add_u32 m0,s50,53248\nM v[72:75],a[80:83],a[100:103],v[72:75]\ns_mov_b64 s[4:5],s[90:91]\nM v[88:91],a[80:83],a[108:111],v[88:91]\ns_waitcnt vmcnt(9) lgkmcnt(0)\nM v[92:95],a[84:87],a[108:111],v[92:95]\n" \
"s_add_u32 s63,s63,79691776\ns_barrier\nds_read_b64_tr_b16 a[32:33],v9 offset:32768\nM v[84:87],a[84:87],a[104:107],v[84:87]\nbuffer_load_dwordx4 v18,s[28:31],s46 offen lds\nM v[80:83],a[80:83],a[104:107],v[80:83]\ns_add_u32 m0,s50,57344\nds_read_b64_tr_b16 a[34:35],v10 offset:32768\n" \
"M v[96:99],a[80:83],a[112:115],v[96:99]\ns_add_u32 s72,s4,s63\ns_addc_u32 s73,s5,0\nds_read_b64_tr_b16 a[36:37],v11 offset:32768\nds_read_b64_tr_b16 a[38:39],v12 offset:32768\nM v[100:103],a[84:87],a[112:115],v[100:103]\ns_mov_b32 s63,65536\nds_read_b128 a[24:27],v5 offset:12288\n" \
"M v[108:111],a[84:87],a[116:119],v[108:111]\nbuffer_load_dwordx4 v19,s[28:31],s46 offen lds\nM v[104:107],a[80:83],a[116:119],v[104:107]\ns_add_u32 m0,s50,61440\nM v[120:123],a[80:83],a[124:127],v[120:123]\ns_cmp_lt_u32 %2,2\ns_cselect_b32 s63,32768,s63\nds_read_b128 a[28:31],v5 offset:14336\n" \
"M v[124:127],a[84:87],a[124:127],v[124:127]\ns_lshl_b32 s56,%2,14\nds_read_b64_tr_b16 a[40:41],v9 offset:33280\nds_read_b64_tr_b16 a[42:43],v10 offset:33280\nM v[116:119],a[84:87],a[120:123],v[116:119]\nbuffer_load_dwordx4 v20,s[28:31],s46 offen lds\nM v[112:115],a[80:83],a[120:123],v[112:115]\nLG4_%=:\n" \
"s_cbranch_execz LHR0_%=\ns_add_u32 m0,s22,0\nM v[192:195],a[64:67],a[96:99],v[192:195]\ns_mov_b32 s46,384\nds_read_b64_tr_b16 a[44:45],v11 offset:33280\nds_read_b64_tr_b16 a[46:47],v12 offset:33280\nM v[196:199],a[68:71],a[96:99],v[196:199]\ns_add_u32 s56,s56,s63\n" \
"ds_read_b64_tr_b16 a[48:49],v9 offset:33792\nM v[204:207],a[68:71],a[100:103],v[204:207]\nbuffer_load_dwordx4 v21,s[32:35],s47 offen offset:0 lds\nM v[200:203],a[64:67],a[100:103],v[200:203]\ns_add_u32 m0,s22,896\nds_read_b64_tr_b16 a[50:51],v10 offset:33792\nM v[216:219],a[64:67],a[108:111],v[216:219]\ns_and_b32 s73,s73,0xffff\n" \
"ds_read_b64_tr_b16 a[52:53],v11 offset:33792\nM v[220:223],a[68:71],a[108:111],v[220:223]\ns_mov_b32 s74,4096\nds_read_b64_tr_b16 a[54:55],v12 offset:33792\nM v[212:215],a[68:71],a[104:107],v[212:215]\nbuffer_load_dwordx4 v21,s[32:35],s47 offen offset:128 lds\nM v[208:211],a[64:67],a[104:107],v[208:211]\ns_add_u32 m0,s22,2048\n" \
"ds_read_b64_tr_b16 a[56:57],v9 offset:34304\nM v[224:227],a[64:67],a[112:115],v[224:227]\ns_mov_b32 s75,0x00020000\nds_read_b64_tr_b16 a[58:59],v10 offset:34304\nM v[228:231],a[68:71],a[112:115],v[228:231]\ns_add_u32 s84,s84,1048576\ns_addc_u32 s85,s85,0\nds_read_b64_tr_b16 a[60:61],v11 offset:34304\n" \
"ds_read_b64_tr_b16 a[62:63],v12 offset:34304\nM v[236:239],a[68:71],a[116:119],v[236:239]\nbuffer_load_dwordx4 v21,s[92:95],s47 offen offset:0 lds\nM v[232:235],a[64:67],a[116:119],v[232:235]\ns_add_u32 m0,s22,2944\nM v[248:251],a[64:67],a[124:127],v[248:251]\ns_add_u32 s21,s21,67108864\nds_read_b128 a[16:19],v5 offset:8192\n" \
"M v[252:255],a[68:71],a[124:127],v[252:255]\ns_mov_b32 s53,s22\nds_read_b128 a[20:23],v5 offset:10240\nM v[244:247],a[68:71],a[120:123],v[244:247]\ns_mov_b64 exec,0xff\nglobal_load_dword v0,v1,s[84:85]\ns_mov_b64 exec,-1\nM v[240:243],a[64:67],a[120:123],v[240:243]\n" \
"ds_read_b128 a[0:3],v5 offset:0\nds_read_b128 a[4:7],v5 offset:2048\nM a[128:131],a[72:75],a[96:99],a[128:131]\ns_mov_b64 exec,0xff00\nglobal_load_dword v0,v1,s[84:85]\ns_mov_b64 exec,-1\nM a[132:135],a[76:79],a[96:99],a[132:135]\ns_add_u32 s84,s84,1048576\n" \
"s_addc_u32 s85,s85,0\nds_read_b128 a[8:11],v5 offset:4096\nds_read_b128 a[12:15],v5 offset:6144\nM a[140:143],a[76:79],a[100:103],a[140:143]\nglobal_load_dwordx4 v[46:49],v4,s[86:87] offset:0\nM a[136:139],a[72:75],a[100:103],a[136:139]\nds_read_b128 a[88:91],v6 offset:12288\nds_read_b128 a[92:95],v6 offset:14336\n" \
"M a[152:155],a[72:75],a[108:111],a[152:155]\nds_read_b128 a[64:67],v6 offset:0\nM a[156:159],a[76:79],a[108:111],a[156:159]\nds_read_b128 a[68:71],v6 offset:2048\nM a[148:151],a[76:79],a[104:107],a[148:151]\nglobal_load_dwordx4 v[50:53],v4,s[86:87] offset:1024\nM a[144:147],a[72:75],a[104:107],a[144:147]\nds_read_b128 a[80:83],v6 offset:8192\n" \
"ds_read_b128 a[84:87],v6 offset:10240\nM a[160:163],a[72:75],a[112:115],a[160:163]\nM a[164:167],a[76:79],a[112:115],a[164:167]\nM a[172:175],a[76:79],a[116:119],a[172:175]\nglobal_load_dwordx4 v[30:33],v26,s[64:65] offset:0\nM a[168:171],a[72:75],a[116:119],a[168:171]\nM a[184:187],a[72:75],a[124:127],a[184:187]\nM a[188:191],a[76:79],a[124:127],a[188:191]\n" \
"M a[180:183],a[76:79],a[120:123],a[180:183]\nglobal_load_dwordx4 v[34:37],v26,s[64:65] offset:1024\nM a[176:179],a[72:75],a[120:123],a[176:179]\ns_waitcnt lgkmcnt(0)\nds_read_b128 a[72:75],v6 offset:4096\nds_read_b128 a[76:79],v6 offset:6144\nM v[128:131],a[24:27],a[32:35],v[128:131]\nM v[132:135],a[28:31],a[32:35],v[132:135]\n" \
"M v[140:143],a[28:31],a[36:39],v[140:143]\nglobal_load_dwordx4 v[38:41],v26,s[64:65] offset:2048\nM v[136:139],a[24:27],a[36:39],v[136:139]\nM v[152:155],a[24:27],a[44:47],v[152:155]\nM v[156:159],a[28:31],a[44:47],v[156:159]\nM v[148:151],a[28:31],a[40:43],v[148:151]\nglobal_load_dwordx4 v[42:45],v26,s[64:65] offset:3072\nM v[144:147],a[24:27],a[40:43],v[144:147]\n" \
"M v[160:163],a[24:27],a[48:51],v[160:163]\nM v[164:167],a[28:31],a[48:51],v[164:167]\nM v[172:175],a[28:31],a[52:55],v[172:175]\nbuffer_load_dwordx4 v21,s[92:95],s47 offen offset:128 lds\nM v[168:171],a[24:27],a[52:55],v[168:171]\ns_add_u32 m0,s22,16384\nM v[184:187],a[24:27],a[60:63],v[184:187]\ns_waitcnt vmcnt(20) lgkmcnt(14)\n" \
"M v[188:191],a[28:31],a[60:63],v[188:191]\ns_mov_b32 s47,1310720\ns_barrier\nds_read_b64_tr_b16 a[96:97],v9 offset:49152\nM v[180:183],a[28:31],a[56:59],v[180:183]\nbuffer_load_dwordx4 v21,s[32:35],s47 offen offset:0 lds\nM v[176:179],a[24:27],a[56:59],v[176:179]\ns_add_u32 m0,s22,17280\n" \
"ds_read_b64_tr_b16 a[98:99],v10 offset:49152\nM v[64:67],a[16:19],a[32:35],v[64:67]\nds_read_b64_tr_b16 a[100:101],v11 offset:49152\nds_read_b64_tr_b16 a[102:103],v12 offset:49152\nM v[68:71],a[20:23],a[32:35],v[68:71]\nds_read_b64_tr_b16 a[104:105],v9 offset:49664\nM v[76:79],a[20:23],a[36:39],v[76:79]\nbuffer_load_dwordx4 v21,s[32:35],s47 offen offset:128 lds\n" \
"M v[72:75],a[16:19],a[36:39],v[72:75]\ns_add_u32 m0,s22,18432\nds_read_b64_tr_b16 a[106:107],v10 offset:49664\nM v[88:91],a[16:19],a[44:47],v[88:91]\nds_read_b64_tr_b16 a[108:109],v11 offset:49664\nM v[92:95],a[20:23],a[44:47],v[92:95]\nds_read_b64_tr_b16 a[110:111],v12 offset:49664\nM v[84:87],a[20:23],a[40:43],v[84:87]\n" \
"buffer_load_dwordx4 v21,s[92:95],s47 offen offset:0 lds\nM v[80:83],a[16:19],a[40:43],v[80:83]\ns_add_u32 m0,s22,19328\nds_read_b64_tr_b16 a[112:113],v9 offset:50176\nM v[96:99],a[16:19],a[48:51],v[96:99]\nds_read_b64_tr_b16 a[114:115],v10 offset:50176\nM v[100:103],a[20:23],a[48:51],v[100:103]\nds_read_b64_tr_b16 a[116:117],v11 offset:50176\n" \
"ds_read_b64_tr_b16 a[118:119],v12 offset:50176\nM v[108:111],a[20:23],a[52:55],v[108:111]\nbuffer_load_dwordx4 v21,s[92:95],s47 offen offset:128 lds\nM v[104:107],a[16:19],a[52:55],v[104:107]\ns_add_u32 m0,s50,65536\nM v[120:123],a[16:19],a[60:63],v[120:123]\ns_mov_b32 s47,1572864\nds_read_b64_tr_b16 a[120:121],v9 offset:50688\n" \
"ds_read_b64_tr_b16 a[122:123],v10 offset:50688\nM v[124:127],a[20:23],a[60:63],v[124:127]\nds_read_b64_tr_b16 a[124:125],v11 offset:50688\nM v[116:119],a[20:23],a[56:59],v[116:119]\nds_read_b64_tr_b16 a[126:127],v12 offset:50688\nM v[112:115],a[16:19],a[56:59],v[112:115]\nM v[192:195],a[0:3],a[32:35],v[192:195]\nM v[196:199],a[4:7],a[32:35],v[196:199]\n" \
"M v[204:207],a[4:7],a[36:39],v[204:207]\nM v[200:203],a[0:3],a[36:39],v[200:203]\nM v[216:219],a[0:3],a[44:47],v[216:219]\nbuffer_load_dwordx4 v13,s[28:31],s46 offen lds\nM v[220:223],a[4:7],a[44:47],v[220:223]\ns_add_u32 m0,s50,69632\nM v[212:215],a[4:7],a[40:43],v[212:215]\nM v[208:211],a[0:3],a[40:43],v[208:211]\n" \
"M v[224:227],a[0:3],a[48:51],v[224:227]\nbuffer_load_dwordx4 v14,s[28:31],s46 offen lds\nM v[228:231],a[4:7],a[48:51],v[228:231]\ns_add_u32 m0,s50,73728\nM v[236:239],a[4:7],a[52:55],v[236:239]\nM v[232:235],a[0:3],a[52:55],v[232:235]\nM v[248:251],a[0:3],a[60:63],v[248:251]\nbuffer_load_dwordx4 v15,s[28:31],s46 offen lds\n" \
"M v[252:255],a[4:7],a[60:63],v[252:255]\ns_add_u32 m0,s50,77824\nM v[244:247],a[4:7],a[56:59],v[244:247]\nM v[240:243],a[0:3],a[56:59],v[240:243]\nM a[128:131],a[8:11],a[32:35],a[128:131]\nbuffer_load_dwordx4 v16,s[28:31],s46 offen lds\nM a[132:135],a[12:15],a[32:35],a[132:135]\ns_add_u32 m0,s50,81920\n" \
"M a[140:143],a[12:15],a[36:39],a[140:143]\ns_mov_b32 s46,384\nM a[136:139],a[8:11],a[36:39],a[136:139]\nM a[152:155],a[8:11],a[44:47],a[152:155]\nbuffer_load_dwordx4 v17,s[28:31],s46 offen lds\nM a[156:159],a[12:15],a[44:47],a[156:159]\ns_add_u32 m0,s50,86016\nM a[148:151],a[12:15],a[40:43],a[148:151]\n" \
"M a[144:147],a[8:11],a[40:43],a[144:147]\nM a[160:163],a[8:11],a[48:51],a[160:163]\nbuffer_load_dwordx4 v18,s[28:31],s46 offen lds\nM a[164:167],a[12:15],a[48:51],a[164:167]\ns_add_u32 m0,s50,90112\nM a[172:175],a[12:15],a[52:55],a[172:175]\nM a[168:171],a[8:11],a[52:55],a[168:171]\nM a[184:187],a[8:11],a[60:63],a[184:187]\n" \
"buffer_load_dwordx4 v19,s[28:31],s46 offen lds\nM a[188:191],a[12:15],a[60:63],a[188:191]\ns_add_u32 m0,s50,94208\nM a[180:183],a[12:15],a[56:59],a[180:183]\nM a[176:179],a[8:11],a[56:59],a[176:179]\ns_waitcnt lgkmcnt(0)\nM v[128:131],a[88:91],a[96:99],v[128:131]\nbuffer_load_dwordx4 v20,s[28:31],s46 offen lds\n" \
"M v[132:135],a[92:95],a[96:99],v[132:135]\ns_add_u32 m0,s22,32768\nM v[140:143],a[92:95],a[100:103],v[140:143]\ns_waitcnt vmcnt(12) lgkmcnt(0)\nM v[136:139],a[88:91],a[100:103],v[136:139]\ns_mov_b32 s46,512\ns_barrier\nds_read_b64_tr_b16 a[32:33],v9 offset:0\n" \
"M v[152:155],a[88:91],a[108:111],v[152:155]\nbuffer_load_dwordx4 v21,s[32:35],s47 offen offset:0 lds\nM v[156:159],a[92:95],a[108:111],v[156:159]\ns_add_u32 m0,s22,33664\nds_read_b64_tr_b16 a[34:35],v10 offset:0\nM v[148:151],a[92:95],a[104:107],v[148:151]\nds_read_b64_tr_b16 a[36:37],v11 offset:0\nds_read_b64_tr_b16 a[38:39],v12 offset:0\n" \
"M v[144:147],a[88:91],a[104:107],v[144:147]\nds_read_b64_tr_b16 a[40:41],v9 offset:512\nM v[160:163],a[88:91],a[112:115],v[160:163]\nbuffer_load_dwordx4 v21,s[32:35],s47 offen offset:128 lds\nM v[164:167],a[92:95],a[112:115],v[164:167]\ns_add_u32 m0,s22,34816\nds_read_b64_tr_b16 a[42:43],v10 offset:512\nM v[172:175],a[92:95],a[116:119],v[172:175]\n" \
"ds_read_b64_tr_b16 a[44:45],v11 offset:512\nM v[168:171],a[88:91],a[116:119],v[168:171]\nds_read_b64_tr_b16 a[46:47],v12 offset:512\nM v[184:187],a[88:91],a[124:127],v[184:187]\nbuffer_load_dwordx4 v21,s[92:95],s47 offen offset:0 lds\nM v[188:191],a[92:95],a[124:127],v[188:191]\ns_add_u32 m0,s22,35712\nds_read_b64_tr_b16 a[48:49],v9 offset:1024\n" \
"M v[180:183],a[92:95],a[120:123],v[180:183]\nds_read_b64_tr_b16 a[50:51],v10 offset:1024\nM v[176:179],a[88:91],a[120:123],v[176:179]\nds_read_b64_tr_b16 a[52:53],v11 offset:1024\nds_read_b64_tr_b16 a[54:55],v12 offset:1024\nM v[64:67],a[80:83],a[96:99],v[64:67]\nbuffer_load_dwordx4 v21,s[92:95],s47 offen offset:128 lds\nM v[68:71],a[84:87],a[96:99],v[68:71]\n" \
"s_add_u32 m0,s22,49152\nM v[76:79],a[84:87],a[100:103],v[76:79]\ns_mov_b32 s47,1835008\nds_read_b64_tr_b16 a[56:57],v9 offset:1536\nds_read_b64_tr_b16 a[58:59],v10 offset:1536\nM v[72:75],a[80:83],a[100:103],v[72:75]\nds_read_b64_tr_b16 a[60:61],v11 offset:1536\nM v[88:91],a[80:83],a[108:111],v[88:91]\n" \
"buffer_load_dwordx4 v21,s[32:35],s47 offen offset:0 lds\nM v[92:95],a[84:87],a[108:111],v[92:95]\ns_add_u32 m0,s22,50048\nds_read_b64_tr_b16 a[62:63],v12 offset:1536\nM v[84:87],a[84:87],a[104:107],v[84:87]\nds_read_b128 a[24:27],v5 offset:45056\nM v[80:83],a[80:83],a[104:107],v[80:83]\nds_read_b128 a[28:31],v5 offset:47104\n" \
"M v[96:99],a[80:83],a[112:115],v[96:99]\nbuffer_load_dwordx4 v21,s[32:35],s47 offen offset:128 lds\nM v[100:103],a[84:87],a[112:115],v[100:103]\ns_add_u32 m0,s22,51200\nM v[108:111],a[84:87],a[116:119],v[108:111]\nds_read_b128 a[16:19],v5 offset:40960\nM v[104:107],a[80:83],a[116:119],v[104:107]\nds_read_b128 a[20:23],v5 offset:43008\n" \
"M v[120:123],a[80:83],a[124:127],v[120:123]\nbuffer_load_dwordx4 v21,s[92:95],s47 offen offset:0 lds\nM v[124:127],a[84:87],a[124:127],v[124:127]\ns_add_u32 m0,s22,52096\nds_read_b128 a[0:3],v5 offset:32768\nM v[116:119],a[84:87],a[120:123],v[116:119]\nds_read_b128 a[4:7],v5 offset:34816\nM v[112:115],a[80:83],a[120:123],v[112:115]\n" \
"ds_read_b128 a[8:11],v5 offset:36864\nM v[192:195],a[64:67],a[96:99],v[192:195]\nbuffer_load_dwordx4 v21,s[92:95],s47 offen offset:128 lds\nM v[196:199],a[68:71],a[96:99],v[196:199]\ns_add_u32 m0,s50,0\nM v[204:207],a[68:71],a[100:103],v[204:207]\ns_mov_b32 s47,2097152\nds_read_b128 a[12:15],v5 offset:38912\n" \
"M v[200:203],a[64:67],a[100:103],v[200:203]\nds_read_b128 a[80:83],v6 offset:40960\nM v[216:219],a[64:67],a[108:111],v[216:219]\nbuffer_load_dwordx4 v13,s[28:31],s46 offen lds\nM v[220:223],a[68:71],a[108:111],v[220:223]\ns_add_u32 m0,s50,4096\nds_read_b128 a[84:87],v6 offset:43008\nM v[212:215],a[68:71],a[104:107],v[212:215]\n" \
"ds_read_b128 a[88:91],v6 offset:45056\nM v[208:211],a[64:67],a[104:107],v[208:211]\nds_read_b128 a[92:95],v6 offset:47104\nM v[224:227],a[64:67],a[112:115],v[224:227]\nbuffer_load_dwordx4 v14,s[28:31],s46 offen lds\nM v[228:231],a[68:71],a[112:115],v[228:231]\ns_add_u32 m0,s50,8192\nM v[236:239],a[68:71],a[116:119],v[236:239]\n" \
"M v[232:235],a[64:67],a[116:119],v[232:235]\nM v[248:251],a[64:67],a[124:127],v[248:251]\nM v[252:255],a[68:71],a[124:127],v[252:255]\nbuffer_load_dwordx4 v15,s[28:31],s46 offen lds\nM v[244:247],a[68:71],a[120:123],v[244:247]\ns_add_u32 m0,s50,12288\nM v[240:243],a[64:67],a[120:123],v[240:243]\nds_read_b128 a[64:67],v6 offset:32768\n" \
"ds_read_b128 a[68:71],v6 offset:34816\nM a[128:131],a[72:75],a[96:99],a[128:131]\nM a[132:135],a[76:79],a[96:99],a[132:135]\nbuffer_load_dwordx4 v16,s[28:31],s46 offen lds\nM a[140:143],a[76:79],a[100:103],a[140:143]\ns_add_u32 m0,s50,16384\nM a[136:139],a[72:75],a[100:103],a[136:139]\ns_mov_b32 s46,512\n" \
"M a[152:155],a[72:75],a[108:111],a[152:155]\nM a[156:159],a[76:79],a[108:111],a[156:159]\nM a[148:151],a[76:79],a[104:107],a[148:151]\ns_waitcnt vmcnt(20) lgkmcnt(14)\nM a[144:147],a[72:75],a[104:107],a[144:147]\ns_barrier\nds_read_b64_tr_b16 a[96:97],v9 offset:16384\nM a[160:163],a[72:75],a[112:115],a[160:163]\n" \
"buffer_load_dwordx4 v17,s[28:31],s46 offen lds\nM a[164:167],a[76:79],a[112:115],a[164:167]\ns_add_u32 m0,s50,20480\nds_read_b64_tr_b16 a[98:99],v10 offset:16384\nM a[172:175],a[76:79],a[116:119],a[172:175]\nds_read_b64_tr_b16 a[100:101],v11 offset:16384\nds_read_b64_tr_b16 a[102:103],v12 offset:16384\nM a[168:171],a[72:75],a[116:119],a[168:171]\n" \
"ds_read_b64_tr_b16 a[104:105],v9 offset:16896\nM a[184:187],a[72:75],a[124:127],a[184:187]\nbuffer_load_dwordx4 v18,s[28:31],s46 offen lds\nM a[188:191],a[76:79],a[124:127],a[188:191]\ns_add_u32 m0,s50,24576\nds_read_b64_tr_b16 a[106:107],v10 offset:16896\nM a[180:183],a[76:79],a[120:123],a[180:183]\nds_read_b64_tr_b16 a[108:109],v11 offset:16896\n" \
"M a[176:179],a[72:75],a[120:123],a[176:179]\ns_waitcnt lgkmcnt(5)\nds_read_b128 a[72:75],v6 offset:36864\nM v[128:131],a[24:27],a[32:35],v[128:131]\nbuffer_load_dwordx4 v19,s[28:31],s46 offen lds\nM v[132:135],a[28:31],a[32:35],v[132:135]\ns_add_u32 m0,s50,28672\nds_read_b128 a[76:79],v6 offset:38912\n" \
"M v[140:143],a[28:31],a[36:39],v[140:143]\nds_read_b64_tr_b16 a[110:111],v12 offset:16896\nM v[136:139],a[24:27],a[36:39],v[136:139]\nds_read_b64_tr_b16 a[112:113],v9 offset:17408\nM v[152:155],a[24:27],a[44:47],v[152:155]\nbuffer_load_dwordx4 v20,s[28:31],s46 offen lds\nM v[156:159],a[28:31],a[44:47],v[156:159]\ns_add_u32 m0,s22,0\n" \
"ds_read_b64_tr_b16 a[114:115],v10 offset:17408\nM v[148:151],a[28:31],a[40:43],v[148:151]\ns_mov_b32 s46,640\nds_read_b64_tr_b16 a[116:117],v11 offset:17408\nM v[144:147],a[24:27],a[40:43],v[144:147]\nds_read_b64_tr_b16 a[118:119],v12 offset:17408\nM v[160:163],a[24:27],a[48:51],v[160:163]\nbuffer_load_dwordx4 v21,s[32:35],s47 offen offset:0 lds\n" \
"M v[164:167],a[28:31],a[48:51],v[164:167]\ns_add_u32 m0,s22,896\nds_read_b64_tr_b16 a[120:121],v9 offset:17920\nM v[172:175],a[28:31],a[52:55],v[172:175]\nds_read_b64_tr_b16 a[122:123],v10 offset:17920\nM v[168:171],a[24:27],a[52:55],v[168:171]\nds_read_b64_tr_b16 a[124:125],v11 offset:17920\nds_read_b64_tr_b16 a[126:127],v12 offset:17920\n" \
"M v[184:187],a[24:27],a[60:63],v[184:187]\nbuffer_load_dwordx4 v21,s[32:35],s47 offen offset:128 lds\nM v[188:191],a[28:31],a[60:63],v[188:191]\ns_add_u32 m0,s22,2048\nM v[180:183],a[28:31],a[56:59],v[180:183]\nM v[176:179],a[24:27],a[56:59],v[176:179]\nM v[64:67],a[16:19],a[32:35],v[64:67]\nM v[68:71],a[20:23],a[32:35],v[68:71]\n" \
"buffer_load_dwordx4 v21,s[92:95],s47 offen offset:0 lds\nM v[76:79],a[20:23],a[36:39],v[76:79]\ns_add_u32 m0,s22,2944\nM v[72:75],a[16:19],a[36:39],v[72:75]\nM v[88:91],a[16:19],a[44:47],v[88:91]\nM v[92:95],a[20:23],a[44:47],v[92:95]\nbuffer_load_dwordx4 v21,s[92:95],s47 offen offset:128 lds\nM v[84:87],a[20:23],a[40:43],v[84:87]\n" \
"s_add_u32 m0,s22,16384\nM v[80:83],a[16:19],a[40:43],v[80:83]\ns_mov_b32 s47,2359296\nM v[96:99],a[16:19],a[48:51],v[96:99]\nM v[100:103],a[20:23],a[48:51],v[100:103]\ns_mov_b64 exec,0xff\nglobal_load_dword v0,v1,s[84:85]\ns_mov_b64 exec,-1\n" \
"M v[108:111],a[20:23],a[52:55],v[108:111]\nM v[104:107],a[16:19],a[52:55],v[104:107]\ns_mov_b64 exec,0xff00\nglobal_load_dword v0,v1,s[84:85]\ns_mov_b64 exec,-1\nM v[120:123],a[16:19],a[60:63],v[120:123]\ns_add_u32 s84,s84,1048576\ns_addc_u32 s85,s85,0\n" \
"M v[124:127],a[20:23],a[60:63],v[124:127]\nglobal_load_dwordx4 a[230:233],v27,s[70:71] offset:0\nM v[116:119],a[20:23],a[56:59],v[116:119]\nM v[112:115],a[16:19],a[56:59],v[112:115]\nM v[192:195],a[0:3],a[32:35],v[192:195]\nM v[196:199],a[4:7],a[32:35],v[196:199]\nglobal_load_dwordx4 a[234:237],v27,s[70:71] offset:16\nM v[204:207],a[4:7],a[36:39],v[204:207]\n" \
"s_mov_b32 s70,4096\nM v[200:203],a[0:3],a[36:39],v[200:203]\ns_mov_b32 s71,0x00020000\nM v[216:219],a[0:3],a[44:47],v[216:219]\nM v[220:223],a[4:7],a[44:47],v[220:223]\nbuffer_load_dwordx2 a[192:193],v54,s[40:43],0 offen offset:0 nt\nM v[212:215],a[4:7],a[40:43],v[212:215]\nM v[208:211],a[0:3],a[40:43],v[208:211]\n" \
"M v[224:227],a[0:3],a[48:51],v[224:227]\nM v[228:231],a[4:7],a[48:51],v[228:231]\nbuffer_load_dwordx2 a[194:195],v54,s[40:43],0 offen offset:2048 nt\nM v[236:239],a[4:7],a[52:55],v[236:239]\nM v[232:235],a[0:3],a[52:55],v[232:235]\nM v[248:251],a[0:3],a[60:63],v[248:251]\nM v[252:255],a[4:7],a[60:63],v[252:255]\nbuffer_load_dwordx2 a[196:197],v55,s[40:43],0 offen offset:0 nt\n" \
"M v[244:247],a[4:7],a[56:59],v[244:247]\nM v[240:243],a[0:3],a[56:59],v[240:243]\nM a[128:131],a[8:11],a[32:35],a[128:131]\ns_waitcnt vmcnt(40)\nv_lshlrev_b32 v25,16,v46\nv_lshlrev_b32 v26,16,v30\nM a[132:135],a[12:15],a[32:35],a[132:135]\nbuffer_load_dwordx2 a[198:199],v59,s[40:43],s52 offen offset:0 nt\n" \
"M a[140:143],a[12:15],a[36:39],a[140:143]\nv_and_b32 v46,0xffff0000,v46\nv_mul_f32 v23,v26,v25\nM a[136:139],a[8:11],a[36:39],a[136:139]\nv_and_b32 v30,0xffff0000,v30\nv_lshlrev_b32 v26,16,v38\nM a[152:155],a[8:11],a[44:47],a[152:155]\nv_fmac_f32 v23,v30,v46\n" \
"v_mul_f32 v24,v26,v25\nM a[156:159],a[12:15],a[44:47],a[156:159]\nbuffer_load_dwordx2 a[214:215],v59,s[80:83],s52 offen offset:0 nt\nM a[148:151],a[12:15],a[40:43],a[148:151]\ns_waitcnt vmcnt(25) lgkmcnt(0)\nv_lshlrev_b32 v25,16,v47\nv_lshlrev_b32 v26,16,v31\nM a[144:147],a[8:11],a[40:43],a[144:147]\n" \
"s_barrier\nds_read_b64_tr_b16 a[32:33],v9 offset:32768\nM a[160:163],a[8:11],a[48:51],a[160:163]\nds_read_b64_tr_b16 a[34:35],v10 offset:32768\nds_read_b64_tr_b16 a[36:37],v11 offset:32768\nM a[164:167],a[12:15],a[48:51],a[164:167]\nbuffer_load_dwordx2 a[200:201],v59,s[40:43],s52 offen offset:2048 nt\nM a[172:175],a[12:15],a[52:55],a[172:175]\n" \
"ds_read_b64_tr_b16 a[38:39],v12 offset:32768\nds_read_b64_tr_b16 a[40:41],v9 offset:33280\nM a[168:171],a[8:11],a[52:55],a[168:171]\nds_read_b64_tr_b16 a[42:43],v10 offset:33280\nv_and_b32 v38,0xffff0000,v38\nM a[184:187],a[8:11],a[60:63],a[184:187]\nds_read_b64_tr_b16 a[44:45],v11 offset:33280\nds_read_b64_tr_b16 a[46:47],v12 offset:33280\n" \
"M a[188:191],a[12:15],a[60:63],a[188:191]\nbuffer_load_dwordx2 a[216:217],v59,s[80:83],s52 offen offset:2048 nt\nM a[180:183],a[12:15],a[56:59],a[180:183]\ns_mov_b32 s52,4096\nds_read_b64_tr_b16 a[48:49],v9 offset:33792\nds_read_b64_tr_b16 a[50:51],v10 offset:33792\nM a[176:179],a[8:11],a[56:59],a[176:179]\nds_read_b64_tr_b16 a[52:53],v11 offset:33792\n" \
"v_fmac_f32 v23,v26,v25\nM v[128:131],a[88:91],a[96:99],v[128:131]\nds_read_b64_tr_b16 a[54:55],v12 offset:33792\nds_read_b64_tr_b16 a[56:57],v9 offset:34304\nM v[132:135],a[92:95],a[96:99],v[132:135]\nbuffer_load_dwordx2 a[202:203],v59,s[40:43],s52 offen offset:0 nt\nM v[140:143],a[92:95],a[100:103],v[140:143]\nds_read_b64_tr_b16 a[58:59],v10 offset:34304\n" \
"ds_read_b64_tr_b16 a[60:61],v11 offset:34304\nM v[136:139],a[88:91],a[100:103],v[136:139]\nds_read_b64_tr_b16 a[62:63],v12 offset:34304\nv_fmac_f32 v24,v38,v46\nM v[152:155],a[88:91],a[108:111],v[152:155]\nds_read_b128 a[24:27],v7 offset:12288\nv_and_b32 v47,0xffff0000,v47\nM v[156:159],a[92:95],a[108:111],v[156:159]\n" \
"buffer_load_dwordx2 a[218:219],v59,s[80:83],s52 offen offset:0 nt\nM v[148:151],a[92:95],a[104:107],v[148:151]\nds_read_b128 a[28:31],v7 offset:14336\nds_read_b128 a[16:19],v7 offset:8192\nM v[144:147],a[88:91],a[104:107],v[144:147]\nds_read_b128 a[20:23],v7 offset:10240\nv_and_b32 v31,0xffff0000,v31\nM v[160:163],a[88:91],a[112:115],v[160:163]\n" \
"ds_read_b128 a[0:3],v7 offset:0\nv_lshlrev_b32 v26,16,v39\nM v[164:167],a[92:95],a[112:115],v[164:167]\nbuffer_load_dwordx2 a[204:205],v59,s[40:43],s52 offen offset:2048 nt\nM v[172:175],a[92:95],a[116:119],v[172:175]\nds_read_b128 a[4:7],v7 offset:2048\nds_read_b128 a[8:11],v7 offset:4096\nM v[168:171],a[88:91],a[116:119],v[168:171]\n" \
"ds_read_b128 a[12:15],v7 offset:6144\nv_fmac_f32 v23,v31,v47\nM v[184:187],a[88:91],a[124:127],v[184:187]\nv_fmac_f32 v24,v26,v25\nv_lshlrev_b32 v25,16,v48\nM v[188:191],a[92:95],a[124:127],v[188:191]\nbuffer_load_dwordx2 a[220:221],v59,s[80:83],s52 offen offset:2048 nt\nM v[180:183],a[92:95],a[120:123],v[180:183]\n" \
"s_mov_b32 s52,8192\nv_lshlrev_b32 v26,16,v32\nv_and_b32 v39,0xffff0000,v39\nM v[176:179],a[88:91],a[120:123],v[176:179]\nds_read_b128 a[88:91],v8 offset:12288\nds_read_b128 a[92:95],v8 offset:14336\nM v[64:67],a[80:83],a[96:99],v[64:67]\nv_fmac_f32 v23,v26,v25\n" \
"v_fmac_f32 v24,v39,v47\nM v[68:71],a[84:87],a[96:99],v[68:71]\nv_and_b32 v48,0xffff0000,v48\nv_and_b32 v32,0xffff0000,v32\nM v[76:79],a[84:87],a[100:103],v[76:79]\nv_lshlrev_b32 v26,16,v40\nv_fmac_f32 v23,v32,v48\nM v[72:75],a[80:83],a[100:103],v[72:75]\n" \
"v_fmac_f32 v24,v26,v25\nv_lshlrev_b32 v25,16,v49\nM v[88:91],a[80:83],a[108:111],v[88:91]\nv_lshlrev_b32 v26,16,v33\nv_and_b32 v40,0xffff0000,v40\nM v[92:95],a[84:87],a[108:111],v[92:95]\nv_fmac_f32 v23,v26,v25\nv_fmac_f32 v24,v40,v48\n" \
"M v[84:87],a[84:87],a[104:107],v[84:87]\nLG5_%=:\ns_cbranch_execz LHR0_%=\nv_and_b32 v49,0xffff0000,v49\nv_and_b32 v33,0xffff0000,v33\nM v[80:83],a[80:83],a[104:107],v[80:83]\nv_lshlrev_b32 v26,16,v41\nv_fmac_f32 v23,v33,v49\n" \
"M v[96:99],a[80:83],a[112:115],v[96:99]\nv_fmac_f32 v24,v26,v25\nv_lshlrev_b32 v25,16,v50\nM v[100:103],a[84:87],a[112:115],v[100:103]\nv_lshlrev_b32 v26,16,v34\nv_and_b32 v41,0xffff0000,v41\nM v[108:111],a[84:87],a[116:119],v[108:111]\nv_fmac_f32 v23,v26,v25\n" \
"v_fmac_f32 v24,v41,v49\nM v[104:107],a[80:83],a[116:119],v[104:107]\nv_and_b32 v50,0xffff0000,v50\nv_and_b32 v34,0xffff0000,v34\nM v[120:123],a[80:83],a[124:127],v[120:123]\nv_lshlrev_b32 v26,16,v42\nv_fmac_f32 v23,v34,v50\nM v[124:127],a[84:87],a[124:127],v[124:127]\n" \
"v_fmac_f32 v24,v26,v25\nv_lshlrev_b32 v25,16,v51\nM v[116:119],a[84:87],a[120:123],v[116:119]\nv_lshlrev_b32 v26,16,v35\nv_and_b32 v42,0xffff0000,v42\nM v[112:115],a[80:83],a[120:123],v[112:115]\nds_read_b128 a[80:83],v8 offset:8192\nds_read_b128 a[84:87],v8 offset:10240\n" \
"M v[192:195],a[64:67],a[96:99],v[192:195]\nv_fmac_f32 v23,v26,v25\nv_fmac_f32 v24,v42,v50\nM v[196:199],a[68:71],a[96:99],v[196:199]\nv_and_b32 v51,0xffff0000,v51\nv_and_b32 v35,0xffff0000,v35\nM v[204:207],a[68:71],a[100:103],v[204:207]\nv_lshlrev_b32 v26,16,v43\n" \
"v_fmac_f32 v23,v35,v51\nM v[200:203],a[64:67],a[100:103],v[200:203]\nv_fmac_f32 v24,v26,v25\nv_lshlrev_b32 v25,16,v52\nM v[216:219],a[64:67],a[108:111],v[216:219]\nv_lshlrev_b32 v26,16,v36\nv_and_b32 v43,0xffff0000,v43\nM v[220:223],a[68:71],a[108:111],v[220:223]\n" \
"v_fmac_f32 v23,v26,v25\nv_fmac_f32 v24,v43,v51\nM v[212:215],a[68:71],a[104:107],v[212:215]\nv_and_b32 v52,0xffff0000,v52\nv_and_b32 v36,0xffff0000,v36\nM v[208:211],a[64:67],a[104:107],v[208:211]\nv_lshlrev_b32 v26,16,v44\nv_fmac_f32 v23,v36,v52\n" \
"M v[224:227],a[64:67],a[112:115],v[224:227]\nv_fmac_f32 v24,v26,v25\nv_lshlrev_b32 v25,16,v53\nM v[228:231],a[68:71],a[112:115],v[228:231]\nv_lshlrev_b32 v26,16,v37\nv_and_b32 v44,0xffff0000,v44\nM v[236:239],a[68:71],a[116:119],v[236:239]\nv_fmac_f32 v23,v26,v25\n" \
"v_fmac_f32 v24,v44,v52\nM v[232:235],a[64:67],a[116:119],v[232:235]\nv_lshlrev_b32 v26,16,v45\nv_and_b32 v53,0xffff0000,v53\nM v[248:251],a[64:67],a[124:127],v[248:251]\nv_and_b32 v45,0xffff0000,v45\nv_fmac_f32 v24,v26,v25\nM v[252:255],a[68:71],a[124:127],v[252:255]\n" \
"v_fmac_f32 v24,v45,v53\nv_and_b32 v37,0xffff0000,v37\nM v[244:247],a[68:71],a[120:123],v[244:247]\nv_fmac_f32 v23,v37,v53\nM v[240:243],a[64:67],a[120:123],v[240:243]\nds_read_b128 a[64:67],v8 offset:0\nds_read_b128 a[68:71],v8 offset:2048\nM a[128:131],a[72:75],a[96:99],a[128:131]\n" \
"v_permlane32_swap_b32 v23,v24\nM a[132:135],a[76:79],a[96:99],a[132:135]\nv_add_f32 v23,v23,v24\nv_mov_b32 v24,v23\nM a[140:143],a[76:79],a[100:103],a[140:143]\ns_nop 0\nv_permlane16_swap_b32 v23,v24\nM a[136:139],a[72:75],a[100:103],a[136:139]\n" \
"v_add_f32 v23,v23,v24\ns_nop 1\nv_add_f32 v23,v23,v23 row_ror:8 row_mask:0xf bank_mask:0xf\nM a[152:155],a[72:75],a[108:111],a[152:155]\ns_nop 0\nv_add_f32 v23,v23,v23 row_ror:4 row_mask:0xf bank_mask:0xf\ns_nop 1\nv_add_f32 v23,v23,v23 row_ror:2 row_mask:0xf bank_mask:0xf\n" \
"M a[156:159],a[76:79],a[108:111],a[156:159]\ns_nop 0\nv_add_f32 v23,v23,v23 row_ror:1 row_mask:0xf bank_mask:0xf\nC v23,v23,v23\nM a[148:151],a[76:79],a[104:107],a[148:151]\ns_waitcnt vmcnt(27) lgkmcnt(14)\nv_and_b32 v23,0xffff0000,v23\nv_mul_f32 v23,0xbfb8aa3b,v23\n" \
"M a[144:147],a[72:75],a[104:107],a[144:147]\ns_barrier\nds_read_b64_tr_b16 a[96:97],v9 offset:49152\nM a[160:163],a[72:75],a[112:115],a[160:163]\nds_read_b64_tr_b16 a[98:99],v10 offset:49152\nds_read_b64_tr_b16 a[100:101],v11 offset:49152\nM a[164:167],a[76:79],a[112:115],a[164:167]\nds_read_b64_tr_b16 a[102:103],v12 offset:49152\n" \
"v_add_u32 v24,992,v4\nM a[172:175],a[76:79],a[116:119],a[172:175]\nds_read_b64_tr_b16 a[104:105],v9 offset:49664\nds_read_b64_tr_b16 a[106:107],v10 offset:49664\nM a[168:171],a[72:75],a[116:119],a[168:171]\nds_read_b64_tr_b16 a[108:109],v11 offset:49664\nM a[184:187],a[72:75],a[124:127],a[184:187]\nds_read_b64_tr_b16 a[110:111],v12 offset:49664\n" \
"M a[188:191],a[76:79],a[124:127],a[188:191]\nds_read_b64_tr_b16 a[112:113],v9 offset:50176\nds_read_b64_tr_b16 a[114:115],v10 offset:50176\nM a[180:183],a[76:79],a[120:123],a[180:183]\nds_read_b64_tr_b16 a[116:117],v11 offset:50176\nM a[176:179],a[72:75],a[120:123],a[176:179]\ns_waitcnt lgkmcnt(7)\nds_read_b128 a[72:75],v8 offset:4096\n" \
"M v[128:131],a[24:27],a[32:35],v[128:131]\nds_read_b128 a[76:79],v8 offset:6144\nM v[132:135],a[28:31],a[32:35],v[132:135]\nbuffer_load_dwordx4 v21,s[32:35],s47 offen offset:0 lds\nM v[140:143],a[28:31],a[36:39],v[140:143]\ns_add_u32 m0,s22,17280\nds_read_b64_tr_b16 a[118:119],v12 offset:50176\nM v[136:139],a[24:27],a[36:39],v[136:139]\n" \
"ds_read_b64_tr_b16 a[120:121],v9 offset:50688\nM v[152:155],a[24:27],a[44:47],v[152:155]\nds_read_b64_tr_b16 a[122:123],v10 offset:50688\nM v[156:159],a[28:31],a[44:47],v[156:159]\nbuffer_load_dwordx4 v21,s[32:35],s47 offen offset:128 lds\nM v[148:151],a[28:31],a[40:43],v[148:151]\ns_add_u32 m0,s22,18432\nds_read_b64_tr_b16 a[124:125],v11 offset:50688\n" \
"M v[144:147],a[24:27],a[40:43],v[144:147]\nds_read_b64_tr_b16 a[126:127],v12 offset:50688\nM v[160:163],a[24:27],a[48:51],v[160:163]\nv_exp_f32 v23,v23\nM v[164:167],a[28:31],a[48:51],v[164:167]\nbuffer_load_dwordx4 v21,s[92:95],s47 offen offset:0 lds\nM v[172:175],a[28:31],a[52:55],v[172:175]\ns_add_u32 m0,s22,19328\n" \
"v_add_f32 v23,1.0,v23\nM v[168:171],a[24:27],a[52:55],v[168:171]\nv_rcp_f32 v23,v23\nM v[184:187],a[24:27],a[60:63],v[184:187]\nC v23,v23,v23\nv_and_b32 v23,0xffff0000,v23\nM v[188:191],a[28:31],a[60:63],v[188:191]\nbuffer_load_dwordx4 v21,s[92:95],s47 offen offset:128 lds\n" \
"M v[180:183],a[28:31],a[56:59],v[180:183]\ns_add_u32 m0,s50,32768\nM v[176:179],a[24:27],a[56:59],v[176:179]\ns_mov_b32 s47,2621440\nM v[64:67],a[16:19],a[32:35],v[64:67]\nM v[68:71],a[20:23],a[32:35],v[68:71]\nbuffer_load_dwordx4 v13,s[28:31],s46 offen lds\nM v[76:79],a[20:23],a[36:39],v[76:79]\n" \
"s_add_u32 m0,s50,36864\nM v[72:75],a[16:19],a[36:39],v[72:75]\nM v[88:91],a[16:19],a[44:47],v[88:91]\nM v[92:95],a[20:23],a[44:47],v[92:95]\nbuffer_load_dwordx4 v14,s[28:31],s46 offen lds\nM v[84:87],a[20:23],a[40:43],v[84:87]\ns_add_u32 m0,s50,40960\nM v[80:83],a[16:19],a[40:43],v[80:83]\n" \
"M v[96:99],a[16:19],a[48:51],v[96:99]\nM v[100:103],a[20:23],a[48:51],v[100:103]\nbuffer_load_dwordx4 v15,s[28:31],s46 offen lds\nM v[108:111],a[20:23],a[52:55],v[108:111]\ns_add_u32 m0,s50,45056\nM v[104:107],a[16:19],a[52:55],v[104:107]\nM v[120:123],a[16:19],a[60:63],v[120:123]\nM v[124:127],a[20:23],a[60:63],v[124:127]\n" \
"buffer_load_dwordx4 v16,s[28:31],s46 offen lds\nM v[116:119],a[20:23],a[56:59],v[116:119]\ns_add_u32 m0,s50,49152\nM v[112:115],a[16:19],a[56:59],v[112:115]\ns_mov_b32 s46,640\nM v[192:195],a[0:3],a[32:35],v[192:195]\nM v[196:199],a[4:7],a[32:35],v[196:199]\nbuffer_load_dwordx4 v17,s[28:31],s46 offen lds\n" \
"M v[204:207],a[4:7],a[36:39],v[204:207]\ns_add_u32 m0,s50,53248\nM v[200:203],a[0:3],a[36:39],v[200:203]\nM v[216:219],a[0:3],a[44:47],v[216:219]\nM v[220:223],a[4:7],a[44:47],v[220:223]\nbuffer_load_dwordx4 v18,s[28:31],s46 offen lds\nM v[212:215],a[4:7],a[40:43],v[212:215]\ns_add_u32 m0,s50,57344\n" \
"M v[208:211],a[0:3],a[40:43],v[208:211]\nM v[224:227],a[0:3],a[48:51],v[224:227]\nM v[228:231],a[4:7],a[48:51],v[228:231]\nbuffer_load_dwordx4 v19,s[28:31],s46 offen lds\nM v[236:239],a[4:7],a[52:55],v[236:239]\ns_add_u32 m0,s50,61440\nM v[232:235],a[0:3],a[52:55],v[232:235]\nM v[248:251],a[0:3],a[60:63],v[248:251]\n" \
"M v[252:255],a[4:7],a[60:63],v[252:255]\nbuffer_load_dwordx4 v20,s[28:31],s46 offen lds\nM v[244:247],a[4:7],a[56:59],v[244:247]\ns_add_u32 m0,s22,32768\nM v[240:243],a[0:3],a[56:59],v[240:243]\ns_mov_b32 s46,768\nM a[128:131],a[8:11],a[32:35],a[128:131]\nM a[132:135],a[12:15],a[32:35],a[132:135]\n" \
"buffer_load_dwordx4 v21,s[32:35],s47 offen offset:0 lds\nM a[140:143],a[12:15],a[36:39],a[140:143]\ns_add_u32 m0,s22,33664\nM a[136:139],a[8:11],a[36:39],a[136:139]\nM a[152:155],a[8:11],a[44:47],a[152:155]\nM a[156:159],a[12:15],a[44:47],a[156:159]\nbuffer_load_dwordx4 v21,s[32:35],s47 offen offset:128 lds\nM a[148:151],a[12:15],a[40:43],a[148:151]\n" \
"s_waitcnt vmcnt(29) lgkmcnt(0)\nM a[144:147],a[8:11],a[40:43],a[144:147]\ns_add_u32 m0,s22,34816\ns_barrier\nds_read_b64_tr_b16 a[32:33],v9 offset:0\nM a[160:163],a[8:11],a[48:51],a[160:163]\nds_read_b64_tr_b16 a[34:35],v10 offset:0\nds_read_b64_tr_b16 a[36:37],v11 offset:0\n" \
"M a[164:167],a[12:15],a[48:51],a[164:167]\nbuffer_load_dwordx4 v21,s[92:95],s47 offen offset:0 lds\nM a[172:175],a[12:15],a[52:55],a[172:175]\ns_add_u32 m0,s22,35712\nds_read_b64_tr_b16 a[38:39],v12 offset:0\nM a[168:171],a[8:11],a[52:55],a[168:171]\nds_read_b64_tr_b16 a[40:41],v9 offset:512\nM a[184:187],a[8:11],a[60:63],a[184:187]\n" \
"ds_read_b64_tr_b16 a[42:43],v10 offset:512\nM a[188:191],a[12:15],a[60:63],a[188:191]\nbuffer_load_dwordx4 v21,s[92:95],s47 offen offset:128 lds\nM a[180:183],a[12:15],a[56:59],a[180:183]\ns_add_u32 m0,s22,49152\nds_read_b64_tr_b16 a[44:45],v11 offset:512\nM a[176:179],a[8:11],a[56:59],a[176:179]\ns_mov_b32 s47,2883584\n" \
"ds_read_b64_tr_b16 a[46:47],v12 offset:512\nM v[128:131],a[88:91],a[96:99],v[128:131]\nds_read_b64_tr_b16 a[48:49],v9 offset:1024\nds_read_b64_tr_b16 a[50:51],v10 offset:1024\nM v[132:135],a[92:95],a[96:99],v[132:135]\nbuffer_load_dwordx4 v21,s[32:35],s47 offen offset:0 lds\nM v[140:143],a[92:95],a[100:103],v[140:143]\ns_add_u32 m0,s22,50048\n" \
"M v[136:139],a[88:91],a[100:103],v[136:139]\nds_read_b64_tr_b16 a[52:53],v11 offset:1024\nds_read_b64_tr_b16 a[54:55],v12 offset:1024\nM v[152:155],a[88:91],a[108:111],v[152:155]\nds_read_b64_tr_b16 a[56:57],v9 offset:1536\nM v[156:159],a[92:95],a[108:111],v[156:159]\nbuffer_load_dwordx4 v21,s[32:35],s47 offen offset:128 lds\nM v[148:151],a[92:95],a[104:107],v[148:151]\n" \
"s_add_u32 m0,s22,51200\nds_read_b64_tr_b16 a[58:59],v10 offset:1536\nM v[144:147],a[88:91],a[104:107],v[144:147]\nds_read_b64_tr_b16 a[60:61],v11 offset:1536\nM v[160:163],a[88:91],a[112:115],v[160:163]\nds_read_b64_tr_b16 a[62:63],v12 offset:1536\nM v[164:167],a[92:95],a[112:115],v[164:167]\nbuffer_load_dwordx4 v21,s[92:95],s47 offen offset:0 lds\n" \
"M v[172:175],a[92:95],a[116:119],v[172:175]\ns_add_u32 m0,s22,52096\nds_read_b128 a[24:27],v5 offset:12288\nM v[168:171],a[88:91],a[116:119],v[168:171]\nds_read_b128 a[28:31],v5 offset:14336\nM v[184:187],a[88:91],a[124:127],v[184:187]\nds_read_b128 a[16:19],v5 offset:8192\nM v[188:191],a[92:95],a[124:127],v[188:191]\n" \
"buffer_store_dword v23,v61,s[36:39],0 offen sc1\nM v[180:183],a[92:95],a[120:123],v[180:183]\nds_read_b128 a[20:23],v5 offset:10240\nds_read_b128 a[0:3],v5 offset:0\nM v[176:179],a[88:91],a[120:123],v[176:179]\nbuffer_load_dwordx4 v21,s[92:95],s47 offen offset:128 lds\nM v[64:67],a[80:83],a[96:99],v[64:67]\ns_add_u32 m0,s50,65536\n" \
"v_add_u32 v23,-32,v4\nM v[68:71],a[84:87],a[96:99],v[68:71]\ns_mov_b32 s47,3145728\nds_read_b128 a[4:7],v5 offset:2048\nM v[76:79],a[84:87],a[100:103],v[76:79]\nds_read_b128 a[8:11],v5 offset:4096\nM v[72:75],a[80:83],a[100:103],v[72:75]\nds_read_b128 a[12:15],v5 offset:6144\n" \
"M v[88:91],a[80:83],a[108:111],v[88:91]\nbuffer_load_dwordx4 v13,s[28:31],s46 offen lds\nM v[92:95],a[84:87],a[108:111],v[92:95]\ns_add_u32 m0,s50,69632\nds_read_b128 a[88:91],v6 offset:12288\nM v[84:87],a[84:87],a[104:107],v[84:87]\nds_read_b128 a[92:95],v6 offset:14336\nM v[80:83],a[80:83],a[104:107],v[80:83]\n" \
"M v[96:99],a[80:83],a[112:115],v[96:99]\nbuffer_load_dwordx4 v14,s[28:31],s46 offen lds\nM v[100:103],a[84:87],a[112:115],v[100:103]\ns_add_u32 m0,s50,73728\nM v[108:111],a[84:87],a[116:119],v[108:111]\nM v[104:107],a[80:83],a[116:119],v[104:107]\nM v[120:123],a[80:83],a[124:127],v[120:123]\nM v[124:127],a[84:87],a[124:127],v[124:127]\n" \
"buffer_load_dwordx4 v15,s[28:31],s46 offen lds\nM v[116:119],a[84:87],a[120:123],v[116:119]\ns_add_u32 m0,s50,77824\nM v[112:115],a[80:83],a[120:123],v[112:115]\nds_read_b128 a[80:83],v6 offset:8192\nds_read_b128 a[84:87],v6 offset:10240\nM v[192:195],a[64:67],a[96:99],v[192:195]\nM v[196:199],a[68:71],a[96:99],v[196:199]\n" \
"buffer_load_dwordx4 v16,s[28:31],s46 offen lds\nM v[204:207],a[68:71],a[100:103],v[204:207]\ns_add_u32 m0,s50,81920\nM v[200:203],a[64:67],a[100:103],v[200:203]\ns_mov_b32 s46,768\nM v[216:219],a[64:67],a[108:111],v[216:219]\nM v[220:223],a[68:71],a[108:111],v[220:223]\nM v[212:215],a[68:71],a[104:107],v[212:215]\n" \
"buffer_load_dwordx2 a[206:207],v59,s[40:43],s52 offen offset:0 nt\nM v[208:211],a[64:67],a[104:107],v[208:211]\nM v[224:227],a[64:67],a[112:115],v[224:227]\nM v[228:231],a[68:71],a[112:115],v[228:231]\nM v[236:239],a[68:71],a[116:119],v[236:239]\nbuffer_load_dwordx2 a[222:223],v59,s[80:83],s52 offen offset:0 nt\nM v[232:235],a[64:67],a[116:119],v[232:235]\nM v[248:251],a[64:67],a[124:127],v[248:251]\n" \
"M v[252:255],a[68:71],a[124:127],v[252:255]\nM v[244:247],a[68:71],a[120:123],v[244:247]\nbuffer_load_dwordx2 a[208:209],v59,s[40:43],s52 offen offset:2048 nt\nM v[240:243],a[64:67],a[120:123],v[240:243]\nds_read_b128 a[64:67],v6 offset:0\nds_read_b128 a[68:71],v6 offset:2048\nM a[128:131],a[72:75],a[96:99],a[128:131]\nM a[132:135],a[76:79],a[96:99],a[132:135]\n" \
"M a[140:143],a[76:79],a[100:103],a[140:143]\nbuffer_load_dwordx2 a[224:225],v59,s[80:83],s52 offen offset:2048 nt\nM a[136:139],a[72:75],a[100:103],a[136:139]\ns_mov_b32 s52,12288\nM a[152:155],a[72:75],a[108:111],a[152:155]\nM a[156:159],a[76:79],a[108:111],a[156:159]\nM a[148:151],a[76:79],a[104:107],a[148:151]\ns_waitcnt vmcnt(25) lgkmcnt(14)\n" \
"M a[144:147],a[72:75],a[104:107],a[144:147]\ns_waitcnt vmcnt(29)\ns_barrier\nds_read_b64_tr_b16 a[96:97],v9 offset:16384\nM a[160:163],a[72:75],a[112:115],a[160:163]\nbuffer_load_dwordx2 a[210:211],v59,s[40:43],s52 offen offset:0 nt\nM a[164:167],a[76:79],a[112:115],a[164:167]\nds_read_b64_tr_b16 a[98:99],v10 offset:16384\n" \
"ds_read_b64_tr_b16 a[100:101],v11 offset:16384\nM a[172:175],a[76:79],a[116:119],a[172:175]\nds_read_b64_tr_b16 a[102:103],v12 offset:16384\nv_accvgpr_read_b32 v45,a231\nM a[168:171],a[72:75],a[116:119],a[168:171]\nds_read_b64_tr_b16 a[104:105],v9 offset:16896\nds_read_b64_tr_b16 a[106:107],v10 offset:16896\nM a[184:187],a[72:75],a[124:127],a[184:187]\n" \
"buffer_load_dwordx2 a[226:227],v59,s[80:83],s52 offen offset:0 nt\nM a[188:191],a[76:79],a[124:127],a[188:191]\nds_read_b64_tr_b16 a[108:109],v11 offset:16896\nds_read_b64_tr_b16 a[110:111],v12 offset:16896\nM a[180:183],a[76:79],a[120:123],a[180:183]\nds_read_b64_tr_b16 a[112:113],v9 offset:17408\nv_accvgpr_read_b32 v49,a233\nM a[176:179],a[72:75],a[120:123],a[176:179]\n" \
"s_waitcnt lgkmcnt(6)\nds_read_b128 a[72:75],v6 offset:4096\nv_lshlrev_b32 v44,16,v45\nM v[128:131],a[24:27],a[32:35],v[128:131]\nbuffer_load_dwordx2 a[212:213],v59,s[40:43],s52 offen offset:2048 nt\nM v[132:135],a[28:31],a[32:35],v[132:135]\nds_read_b128 a[76:79],v6 offset:6144\nds_read_b64_tr_b16 a[114:115],v10 offset:17408\n" \
"M v[140:143],a[28:31],a[36:39],v[140:143]\nds_read_b64_tr_b16 a[116:117],v11 offset:17408\nv_accvgpr_read_b32 v36,a192\nM v[136:139],a[24:27],a[36:39],v[136:139]\nds_read_b64_tr_b16 a[118:119],v12 offset:17408\nv_accvgpr_read_b32 v43,a230\nM v[152:155],a[24:27],a[44:47],v[152:155]\nbuffer_load_dwordx2 a[228:229],v59,s[80:83],s52 offen offset:2048 nt\n" \
"M v[156:159],a[28:31],a[44:47],v[156:159]\nds_read_b64_tr_b16 a[120:121],v9 offset:17920\nds_read_b64_tr_b16 a[122:123],v10 offset:17920\nM v[148:151],a[28:31],a[40:43],v[148:151]\nds_read_b64_tr_b16 a[124:125],v11 offset:17920\nv_accvgpr_read_b32 v37,a194\nM v[144:147],a[24:27],a[40:43],v[144:147]\nds_read_b64_tr_b16 a[126:127],v12 offset:17920\n" \
"v_lshlrev_b32 v48,16,v49\nM v[160:163],a[24:27],a[48:51],v[160:163]\ns_mov_b64 exec,0xff\nglobal_load_dword v0,v1,s[84:85]\ns_mov_b64 exec,-1\nM v[164:167],a[28:31],a[48:51],v[164:167]\nv_lshlrev_b32 v30,16,v36\nv_and_b32 v45,0xffff0000,v45\n" \
"M v[172:175],a[28:31],a[52:55],v[172:175]\ns_mov_b64 exec,0xff00\nglobal_load_dword v0,v1,s[84:85]\ns_mov_b64 exec,-1\nM v[168:171],a[24:27],a[52:55],v[168:171]\nv_accvgpr_read_b32 v47,a232\nv_lshlrev_b32 v31,16,v37\nM v[184:187],a[24:27],a[60:63],v[184:187]\n" \
"s_mov_b64 exec,0xff\nglobal_load_dword v0,v1,s[76:77]\ns_mov_b64 exec,-1\nM v[188:191],a[28:31],a[60:63],v[188:191]\nv_accvgpr_read_b32 v38,a196\nv_lshlrev_b32 v42,16,v43\nM v[180:183],a[28:31],a[56:59],v[180:183]\ns_mov_b64 exec,0xff00\n" \
"global_load_dword v0,v1,s[76:77]\ns_mov_b64 exec,-1\nM v[176:179],a[24:27],a[56:59],v[176:179]\ns_add_u32 s76,s76,2097152\ns_addc_u32 s77,s77,0\nv_and_b32 v49,0xffff0000,v49\nv_and_b32 v36,0xffff0000,v36\nM v[64:67],a[16:19],a[32:35],v[64:67]\n" \
"v_accvgpr_read_b32 v39,a198\nv_mul_f32 v25,v30,v45\nM v[68:71],a[20:23],a[32:35],v[68:71]\nv_lshlrev_b32 v46,16,v47\nv_and_b32 v37,0xffff0000,v37\nM v[76:79],a[20:23],a[36:39],v[76:79]\nv_lshlrev_b32 v32,16,v38\nv_and_b32 v43,0xffff0000,v43\n" \
"M v[72:75],a[16:19],a[36:39],v[72:75]\nv_mul_f32 v26,v36,v49\nv_lshlrev_b32 v33,16,v39\nM v[88:91],a[16:19],a[44:47],v[88:91]\nv_fmac_f32 v25,v31,v44\nv_and_b32 v47,0xffff0000,v47\nM v[92:95],a[20:23],a[44:47],v[92:95]\nv_and_b32 v38,0xffff0000,v38\n" \
"v_fmac_f32 v26,v37,v48\nM v[84:87],a[20:23],a[40:43],v[84:87]\nv_and_b32 v39,0xffff0000,v39\nv_fmac_f32 v25,v32,v43\nM v[80:83],a[16:19],a[40:43],v[80:83]\nv_fmac_f32 v26,v38,v47\nv_fmac_f32 v25,v33,v42\nM v[96:99],a[16:19],a[48:51],v[96:99]\n" \
"v_fmac_f32 v26,v39,v46\nv_accvgpr_read_b32 v28,a214\nM v[100:103],a[20:23],a[48:51],v[100:103]\nC v26,v25,v26\nv_lshlrev_b32 v27,16,v28\nM v[108:111],a[20:23],a[52:55],v[108:111]\nv_lshlrev_b32 v25,16,v26\nv_and_b32 v28,0xffff0000,v28\n" \
"M v[104:107],a[16:19],a[52:55],v[104:107]\nv_and_b32 v26,0xffff0000,v26\nv_mul_f32 v29,v25,v27\nM v[120:123],a[16:19],a[60:63],v[120:123]\nv_mul_f32 v58,v26,v28\nv_accvgpr_read_b32 v40,a200\nM v[124:127],a[20:23],a[60:63],v[124:127]\nv_mul_f32 v25,v31,v45\n" \
"v_mul_f32 v26,v37,v49\nM v[116:119],a[20:23],a[56:59],v[116:119]\nv_lshlrev_b32 v34,16,v40\nv_fmac_f32 v25,v32,v44\nM v[112:115],a[16:19],a[56:59],v[112:115]\nv_fmac_f32 v26,v38,v48\nv_fmac_f32 v25,v33,v43\nM v[192:195],a[0:3],a[32:35],v[192:195]\n" \
"v_and_b32 v40,0xffff0000,v40\nv_fmac_f32 v26,v39,v47\nM v[196:199],a[4:7],a[32:35],v[196:199]\nv_fmac_f32 v25,v34,v42\nv_fmac_f32 v26,v40,v46\nM v[204:207],a[4:7],a[36:39],v[204:207]\nv_accvgpr_read_b32 v28,a216\nC v26,v25,v26\n" \
"M v[200:203],a[0:3],a[36:39],v[200:203]\nv_lshlrev_b32 v27,16,v28\nv_lshlrev_b32 v25,16,v26\nM v[216:219],a[0:3],a[44:47],v[216:219]\nv_and_b32 v28,0xffff0000,v28\nv_and_b32 v26,0xffff0000,v26\nM v[220:223],a[4:7],a[44:47],v[220:223]\nv_mul_f32 v25,v25,v27\n" \
"v_mul_f32 v26,v26,v28\nM v[212:215],a[4:7],a[40:43],v[212:215]\nC v53,v25,v29\nC v57,v26,v58\nM v[208:211],a[0:3],a[40:43],v[208:211]\nv_accvgpr_read_b32 v41,a202\nv_lshlrev_b32 v35,16,v41\nM v[224:227],a[0:3],a[48:51],v[224:227]\n" \
"v_mul_f32 v25,v32,v45\nv_mul_f32 v26,v38,v49\nM v[228:231],a[4:7],a[48:51],v[228:231]\nv_fmac_f32 v25,v33,v44\nv_and_b32 v41,0xffff0000,v41\nM v[236:239],a[4:7],a[52:55],v[236:239]\nv_fmac_f32 v26,v39,v48\nv_fmac_f32 v25,v34,v43\n" \
"M v[232:235],a[0:3],a[52:55],v[232:235]\nv_fmac_f32 v26,v40,v47\nv_fmac_f32 v25,v35,v42\nM v[248:251],a[0:3],a[60:63],v[248:251]\nv_fmac_f32 v26,v41,v46\nv_accvgpr_read_b32 v28,a218\nM v[252:255],a[4:7],a[60:63],v[252:255]\nC v26,v25,v26\n" \
"v_lshlrev_b32 v27,16,v28\nM v[244:247],a[4:7],a[56:59],v[244:247]\nv_lshlrev_b32 v25,16,v26\nv_and_b32 v28,0xffff0000,v28\nM v[240:243],a[0:3],a[56:59],v[240:243]\nv_and_b32 v26,0xffff0000,v26\nv_mul_f32 v29,v25,v27\nM a[128:131],a[8:11],a[32:35],a[128:131]\n" \
"v_mul_f32 v58,v26,v28\nv_accvgpr_read_b32 v36,a204\nM a[132:135],a[12:15],a[32:35],a[132:135]\nv_mul_f32 v25,v33,v45\nv_lshlrev_b32 v30,16,v36\nM a[140:143],a[12:15],a[36:39],a[140:143]\nv_mul_f32 v26,v39,v49\nv_fmac_f32 v25,v34,v44\n" \
"M a[136:139],a[8:11],a[36:39],a[136:139]\nv_fmac_f32 v26,v40,v48\nv_and_b32 v36,0xffff0000,v36\nM a[152:155],a[8:11],a[44:47],a[152:155]\nbuffer_load_dwordx4 v17,s[28:31],s46 offen lds\nM a[156:159],a[12:15],a[44:47],a[156:159]\ns_add_u32 m0,s50,86016\nv_fmac_f32 v25,v35,v43\n" \
"v_fmac_f32 v26,v41,v47\nM a[148:151],a[12:15],a[40:43],a[148:151]\ns_waitcnt vmcnt(22) lgkmcnt(0)\nv_fmac_f32 v25,v30,v42\nv_fmac_f32 v26,v36,v46\nM a[144:147],a[8:11],a[40:43],a[144:147]\ns_barrier\nds_read_b64_tr_b16 a[32:33],v9 offset:32768\n" \
"M a[160:163],a[8:11],a[48:51],a[160:163]\nbuffer_load_dwordx4 v18,s[28:31],s46 offen lds\nM a[164:167],a[12:15],a[48:51],a[164:167]\ns_add_u32 m0,s50,90112\nds_read_b64_tr_b16 a[34:35],v10 offset:32768\nv_accvgpr_read_b32 v28,a220\nM a[172:175],a[12:15],a[52:55],a[172:175]\nds_read_b64_tr_b16 a[36:37],v11 offset:32768\n" \
"ds_read_b64_tr_b16 a[38:39],v12 offset:32768\nM a[168:171],a[8:11],a[52:55],a[168:171]\nds_read_b64_tr_b16 a[40:41],v9 offset:33280\nC v26,v25,v26\nM a[184:187],a[8:11],a[60:63],a[184:187]\nbuffer_load_dwordx4 v19,s[28:31],s46 offen lds\nM a[188:191],a[12:15],a[60:63],a[188:191]\ns_add_u32 m0,s50,94208\n" \
"ds_read_b64_tr_b16 a[42:43],v10 offset:33280\nv_lshlrev_b32 v27,16,v28\nM a[180:183],a[12:15],a[56:59],a[180:183]\nds_read_b64_tr_b16 a[44:45],v11 offset:33280\nv_lshlrev_b32 v25,16,v26\nM a[176:179],a[8:11],a[56:59],a[176:179]\nLG6_%=:\ns_cbranch_execz LHR0_%=\n" \
"ds_read_b64_tr_b16 a[46:47],v12 offset:33280\nv_and_b32 v28,0xffff0000,v28\nM v[128:131],a[88:91],a[96:99],v[128:131]\nbuffer_load_dwordx4 v20,s[28:31],s46 offen lds\nM v[132:135],a[92:95],a[96:99],v[132:135]\ns_add_u32 m0,s22,0\nds_read_b64_tr_b16 a[48:49],v9 offset:33792\nv_and_b32 v26,0xffff0000,v26\n" \
"M v[140:143],a[92:95],a[100:103],v[140:143]\ns_mov_b32 s46,896\nds_read_b64_tr_b16 a[50:51],v10 offset:33792\nv_mul_f32 v25,v25,v27\nM v[136:139],a[88:91],a[100:103],v[136:139]\nds_read_b64_tr_b16 a[52:53],v11 offset:33792\nds_read_b64_tr_b16 a[54:55],v12 offset:33792\nM v[152:155],a[88:91],a[108:111],v[152:155]\n" \
"buffer_load_dwordx4 v21,s[32:35],s47 offen offset:0 lds\nM v[156:159],a[92:95],a[108:111],v[156:159]\ns_add_u32 m0,s22,896\nv_mul_f32 v26,v26,v28\nC v52,v25,v29\nM v[148:151],a[92:95],a[104:107],v[148:151]\nds_read_b64_tr_b16 a[56:57],v9 offset:34304\nds_read_b64_tr_b16 a[58:59],v10 offset:34304\n" \
"M v[144:147],a[88:91],a[104:107],v[144:147]\nds_read_b64_tr_b16 a[60:61],v11 offset:34304\nC v56,v26,v58\nM v[160:163],a[88:91],a[112:115],v[160:163]\nbuffer_load_dwordx4 v21,s[32:35],s47 offen offset:128 lds\nM v[164:167],a[92:95],a[112:115],v[164:167]\ns_add_u32 m0,s22,2048\nds_read_b64_tr_b16 a[62:63],v12 offset:34304\n" \
"v_mov_b32 v59,s19\nM v[172:175],a[92:95],a[116:119],v[172:175]\nds_read_b128 a[24:27],v5 offset:45056\nM v[168:171],a[88:91],a[116:119],v[168:171]\nds_read_b128 a[28:31],v5 offset:47104\nM v[184:187],a[88:91],a[124:127],v[184:187]\nbuffer_load_dwordx4 v21,s[92:95],s47 offen offset:0 lds\nM v[188:191],a[92:95],a[124:127],v[188:191]\n" \
"s_add_u32 m0,s22,2944\nM v[180:183],a[92:95],a[120:123],v[180:183]\nds_read_b128 a[16:19],v5 offset:40960\nM v[176:179],a[88:91],a[120:123],v[176:179]\nds_read_b128 a[20:23],v5 offset:43008\nM v[64:67],a[80:83],a[96:99],v[64:67]\nbuffer_load_dwordx4 v21,s[92:95],s47 offen offset:128 lds\nM v[68:71],a[84:87],a[96:99],v[68:71]\n" \
"s_add_u32 m0,s22,16384\nds_read_b128 a[0:3],v5 offset:32768\nM v[76:79],a[84:87],a[100:103],v[76:79]\ns_mov_b32 s47,3407872\nds_read_b128 a[4:7],v5 offset:34816\nM v[72:75],a[80:83],a[100:103],v[72:75]\nds_read_b128 a[8:11],v5 offset:36864\nM v[88:91],a[80:83],a[108:111],v[88:91]\n" \
"buffer_load_dwordx4 v21,s[32:35],s47 offen offset:0 lds\nM v[92:95],a[84:87],a[108:111],v[92:95]\ns_add_u32 m0,s22,17280\nM v[84:87],a[84:87],a[104:107],v[84:87]\nds_read_b128 a[12:15],v5 offset:38912\nM v[80:83],a[80:83],a[104:107],v[80:83]\nds_read_b128 a[88:91],v6 offset:45056\nM v[96:99],a[80:83],a[112:115],v[96:99]\n" \
"buffer_load_dwordx4 v21,s[32:35],s47 offen offset:128 lds\nM v[100:103],a[84:87],a[112:115],v[100:103]\ns_add_u32 m0,s22,18432\nds_read_b128 a[92:95],v6 offset:47104\nM v[108:111],a[84:87],a[116:119],v[108:111]\nM v[104:107],a[80:83],a[116:119],v[104:107]\nM v[120:123],a[80:83],a[124:127],v[120:123]\nbuffer_load_dwordx4 v21,s[92:95],s47 offen offset:0 lds\n" \
"M v[124:127],a[84:87],a[124:127],v[124:127]\ns_add_u32 m0,s22,19328\nM v[116:119],a[84:87],a[120:123],v[116:119]\nM v[112:115],a[80:83],a[120:123],v[112:115]\nds_read_b128 a[80:83],v6 offset:40960\nds_read_b128 a[84:87],v6 offset:43008\nM v[192:195],a[64:67],a[96:99],v[192:195]\nbuffer_load_dwordx4 v21,s[92:95],s47 offen offset:128 lds\n" \
"M v[196:199],a[68:71],a[96:99],v[196:199]\ns_add_u32 m0,s50,0\nM v[204:207],a[68:71],a[100:103],v[204:207]\ns_mov_b32 s47,3670016\nM v[200:203],a[64:67],a[100:103],v[200:203]\nM v[216:219],a[64:67],a[108:111],v[216:219]\nbuffer_load_dwordx4 v13,s[28:31],s46 offen lds\nM v[220:223],a[68:71],a[108:111],v[220:223]\n" \
"s_add_u32 m0,s50,4096\nv_add_u32 v13,s60,v63\nM v[212:215],a[68:71],a[104:107],v[212:215]\nM v[208:211],a[64:67],a[104:107],v[208:211]\nM v[224:227],a[64:67],a[112:115],v[224:227]\nbuffer_load_dwordx4 v14,s[28:31],s46 offen lds\nM v[228:231],a[68:71],a[112:115],v[228:231]\ns_add_u32 m0,s50,8192\n" \
"v_add_u32 v14,0x10000,v13\nM v[236:239],a[68:71],a[116:119],v[236:239]\nM v[232:235],a[64:67],a[116:119],v[232:235]\nM v[248:251],a[64:67],a[124:127],v[248:251]\nbuffer_load_dwordx4 v15,s[28:31],s46 offen lds\nM v[252:255],a[68:71],a[124:127],v[252:255]\ns_add_u32 m0,s50,12288\nv_add_u32 v15,0x10000,v14\n" \
"M v[244:247],a[68:71],a[120:123],v[244:247]\nM v[240:243],a[64:67],a[120:123],v[240:243]\nds_read_b128 a[64:67],v6 offset:32768\nds_read_b128 a[68:71],v6 offset:34816\nM a[128:131],a[72:75],a[96:99],a[128:131]\nbuffer_load_dwordx4 v16,s[28:31],s46 offen lds\nM a[132:135],a[76:79],a[96:99],a[132:135]\ns_add_u32 m0,s50,16384\n" \
"v_add_u32 v16,0x10000,v15\nM a[140:143],a[76:79],a[100:103],a[140:143]\ns_mov_b32 s46,896\nM a[136:139],a[72:75],a[100:103],a[136:139]\nM a[152:155],a[72:75],a[108:111],a[152:155]\nbuffer_load_dwordx4 v17,s[28:31],s46 offen lds\nM a[156:159],a[76:79],a[108:111],a[156:159]\ns_add_u32 m0,s50,20480\n" \
"v_add_u32 v17,s61,v63\nv_mov_b32 v63,0x3f803f80\nM a[148:151],a[76:79],a[104:107],a[148:151]\ns_waitcnt vmcnt(33) lgkmcnt(14)\nM a[144:147],a[72:75],a[104:107],a[144:147]\ns_barrier\nds_read_b64_tr_b16 a[96:97],v9 offset:49152\nM a[160:163],a[72:75],a[112:115],a[160:163]\n" \
"ds_read_b64_tr_b16 a[98:99],v10 offset:49152\nds_read_b64_tr_b16 a[100:101],v11 offset:49152\nM a[164:167],a[76:79],a[112:115],a[164:167]\nbuffer_load_dwordx4 v18,s[28:31],s46 offen lds\nM a[172:175],a[76:79],a[116:119],a[172:175]\ns_add_u32 m0,s50,24576\nds_read_b64_tr_b16 a[102:103],v12 offset:49152\nv_add_u32 v18,0x10000,v17\n" \
"M a[168:171],a[72:75],a[116:119],a[168:171]\nds_read_b64_tr_b16 a[104:105],v9 offset:49664\nM a[184:187],a[72:75],a[124:127],a[184:187]\nds_read_b64_tr_b16 a[106:107],v10 offset:49664\nM a[188:191],a[76:79],a[124:127],a[188:191]\nbuffer_load_dwordx4 v19,s[28:31],s46 offen lds\nM a[180:183],a[76:79],a[120:123],a[180:183]\ns_add_u32 m0,s50,28672\n" \
"ds_read_b64_tr_b16 a[108:109],v11 offset:49664\nv_add_u32 v19,0x10000,v18\nM a[176:179],a[72:75],a[120:123],a[176:179]\ns_waitcnt lgkmcnt(4)\nds_read_b128 a[72:75],v6 offset:36864\nM v[128:131],a[24:27],a[32:35],v[128:131]\nds_read_b128 a[76:79],v6 offset:38912\nM v[132:135],a[28:31],a[32:35],v[132:135]\n" \
"ds_read_b64_tr_b16 a[110:111],v12 offset:49664\nM v[140:143],a[28:31],a[36:39],v[140:143]\nbuffer_load_dwordx4 v20,s[28:31],s46 offen lds\nM v[136:139],a[24:27],a[36:39],v[136:139]\ns_add_u32 m0,s22,32768\nds_read_b64_tr_b16 a[112:113],v9 offset:50176\nv_add_u32 v20,0x10000,v19\nM v[152:155],a[24:27],a[44:47],v[152:155]\n" \
"s_mov_b64 s[28:29],s[64:65]\nds_read_b64_tr_b16 a[114:115],v10 offset:50176\nM v[156:159],a[28:31],a[44:47],v[156:159]\ns_and_b32 s29,s29,0xffff\nds_read_b64_tr_b16 a[116:117],v11 offset:50176\nds_read_b64_tr_b16 a[118:119],v12 offset:50176\nM v[148:151],a[28:31],a[40:43],v[148:151]\nbuffer_load_dwordx4 v21,s[32:35],s47 offen offset:0 lds\n" \
"M v[144:147],a[24:27],a[40:43],v[144:147]\ns_add_u32 m0,s22,33664\nM v[160:163],a[24:27],a[48:51],v[160:163]\ns_mov_b32 s30,4194304\nds_read_b64_tr_b16 a[120:121],v9 offset:50688\nds_read_b64_tr_b16 a[122:123],v10 offset:50688\nM v[164:167],a[28:31],a[48:51],v[164:167]\ns_mov_b32 s46,0\n" \
"ds_read_b64_tr_b16 a[124:125],v11 offset:50688\nM v[172:175],a[28:31],a[52:55],v[172:175]\nds_read_b64_tr_b16 a[126:127],v12 offset:50688\nM v[168:171],a[24:27],a[52:55],v[168:171]\nbuffer_load_dwordx4 v21,s[32:35],s47 offen offset:128 lds\nM v[184:187],a[24:27],a[60:63],v[184:187]\ns_add_u32 m0,s22,34816\nM v[188:191],a[28:31],a[60:63],v[188:191]\n" \
"M v[180:183],a[28:31],a[56:59],v[180:183]\nM v[176:179],a[24:27],a[56:59],v[176:179]\nbuffer_load_dwordx4 v21,s[92:95],s47 offen offset:0 lds\nM v[64:67],a[16:19],a[32:35],v[64:67]\ns_add_u32 m0,s22,35712\nM v[68:71],a[20:23],a[32:35],v[68:71]\nM v[76:79],a[20:23],a[36:39],v[76:79]\nM v[72:75],a[16:19],a[36:39],v[72:75]\n" \
"M v[88:91],a[16:19],a[44:47],v[88:91]\nbuffer_load_dwordx4 v21,s[92:95],s47 offen offset:128 lds\nM v[92:95],a[20:23],a[44:47],v[92:95]\ns_add_u32 m0,s22,49152\nM v[84:87],a[20:23],a[40:43],v[84:87]\ns_mov_b32 s47,3932160\nM v[80:83],a[16:19],a[40:43],v[80:83]\nM v[96:99],a[16:19],a[48:51],v[96:99]\n" \
"M v[100:103],a[20:23],a[48:51],v[100:103]\nM v[108:111],a[20:23],a[52:55],v[108:111]\nM v[104:107],a[16:19],a[52:55],v[104:107]\nM v[120:123],a[16:19],a[60:63],v[120:123]\nM v[124:127],a[20:23],a[60:63],v[124:127]\nM v[116:119],a[20:23],a[56:59],v[116:119]\nM v[112:115],a[16:19],a[56:59],v[112:115]\nM v[192:195],a[0:3],a[32:35],v[192:195]\n" \
"s_mov_b64 exec,0xff\nglobal_load_dword v0,v2,s[88:89]\ns_mov_b64 exec,-1\nM v[196:199],a[4:7],a[32:35],v[196:199]\nM v[204:207],a[4:7],a[36:39],v[204:207]\ns_mov_b64 exec,0xff00\nglobal_load_dword v0,v2,s[88:89]\ns_mov_b64 exec,-1\n" \
"M v[200:203],a[0:3],a[36:39],v[200:203]\ns_add_u32 s88,s88,2048\ns_addc_u32 s89,s89,0\nM v[216:219],a[0:3],a[44:47],v[216:219]\ns_mov_b64 exec,0xff\nglobal_load_dword v0,v2,s[88:89]\ns_mov_b64 exec,-1\nM v[220:223],a[4:7],a[44:47],v[220:223]\n" \
"M v[212:215],a[4:7],a[40:43],v[212:215]\ns_mov_b64 exec,0xff00\nglobal_load_dword v0,v2,s[88:89]\ns_mov_b64 exec,-1\nM v[208:211],a[0:3],a[40:43],v[208:211]\nM v[224:227],a[0:3],a[48:51],v[224:227]\ns_mov_b64 exec,0xff\nglobal_load_dword v0,v1,s[76:77]\n" \
"s_mov_b64 exec,-1\nM v[228:231],a[4:7],a[48:51],v[228:231]\nM v[236:239],a[4:7],a[52:55],v[236:239]\ns_mov_b64 exec,0xff00\nglobal_load_dword v0,v1,s[76:77]\ns_mov_b64 exec,-1\nM v[232:235],a[0:3],a[52:55],v[232:235]\ns_add_u32 s76,s76,2097152\n" \
"s_addc_u32 s77,s77,0\nM v[248:251],a[0:3],a[60:63],v[248:251]\nM v[252:255],a[4:7],a[60:63],v[252:255]\nM v[244:247],a[4:7],a[56:59],v[244:247]\ns_waitcnt vmcnt(34)\nv_accvgpr_read_b32 v37,a206\nv_mul_f32 v25,v34,v45\nM v[240:243],a[0:3],a[56:59],v[240:243]\n" \
"s_mov_b32 s98,79691776\nv_mul_f32 v26,v40,v49\nv_lshlrev_b32 v31,16,v37\nM a[128:131],a[8:11],a[32:35],a[128:131]\ns_mov_b32 s99,79695872\nv_fmac_f32 v25,v35,v44\nv_fmac_f32 v26,v41,v48\nM a[132:135],a[12:15],a[32:35],a[132:135]\n" \
"v_and_b32 v37,0xffff0000,v37\nv_fmac_f32 v25,v30,v43\nM a[140:143],a[12:15],a[36:39],a[140:143]\nv_fmac_f32 v26,v36,v47\nv_fmac_f32 v25,v31,v42\nM a[136:139],a[8:11],a[36:39],a[136:139]\nv_fmac_f32 v26,v37,v46\nv_accvgpr_read_b32 v28,a222\n" \
"M a[152:155],a[8:11],a[44:47],a[152:155]\nC v26,v25,v26\nv_lshlrev_b32 v27,16,v28\nM a[156:159],a[12:15],a[44:47],a[156:159]\nv_lshlrev_b32 v25,16,v26\nv_and_b32 v28,0xffff0000,v28\nM a[148:151],a[12:15],a[40:43],a[148:151]\ns_waitcnt vmcnt(22) lgkmcnt(0)\n" \
"v_and_b32 v26,0xffff0000,v26\nv_mul_f32 v29,v25,v27\nM a[144:147],a[8:11],a[40:43],a[144:147]\ns_barrier\nv_mul_f32 v58,v26,v28\nM a[160:163],a[8:11],a[48:51],a[160:163]\nv_accvgpr_read_b32 v38,a208\nv_mul_f32 v25,v35,v45\n" \
"M a[164:167],a[12:15],a[48:51],a[164:167]\nv_lshlrev_b32 v32,16,v38\nv_mul_f32 v26,v41,v49\nM a[172:175],a[12:15],a[52:55],a[172:175]\nv_fmac_f32 v25,v30,v44\nv_and_b32 v38,0xffff0000,v38\nM a[168:171],a[8:11],a[52:55],a[168:171]\nv_fmac_f32 v26,v36,v48\n" \
"v_fmac_f32 v25,v31,v43\nM a[184:187],a[8:11],a[60:63],a[184:187]\nds_read_b64_tr_b16 a[32:33],v9 offset:0\nds_read_b64_tr_b16 a[34:35],v10 offset:0\nM a[188:191],a[12:15],a[60:63],a[188:191]\nds_read_b64_tr_b16 a[36:37],v11 offset:0\nv_fmac_f32 v26,v37,v47\nM a[180:183],a[12:15],a[56:59],a[180:183]\n" \
"ds_read_b64_tr_b16 a[38:39],v12 offset:0\nds_read_b64_tr_b16 a[40:41],v9 offset:512\nM a[176:179],a[8:11],a[56:59],a[176:179]\nds_read_b64_tr_b16 a[42:43],v10 offset:512\nv_fmac_f32 v25,v32,v42\nM v[128:131],a[88:91],a[96:99],v[128:131]\nv_fmac_f32 v26,v38,v46\nv_accvgpr_read_b32 v28,a224\n" \
"M v[132:135],a[92:95],a[96:99],v[132:135]\nds_read_b64_tr_b16 a[44:45],v11 offset:512\nC v26,v25,v26\nM v[140:143],a[92:95],a[100:103],v[140:143]\nds_read_b64_tr_b16 a[46:47],v12 offset:512\nv_lshlrev_b32 v27,16,v28\nM v[136:139],a[88:91],a[100:103],v[136:139]\nds_read_b64_tr_b16 a[48:49],v9 offset:1024\n" \
"ds_read_b64_tr_b16 a[50:51],v10 offset:1024\nM v[152:155],a[88:91],a[108:111],v[152:155]\nds_read_b64_tr_b16 a[52:53],v11 offset:1024\nv_lshlrev_b32 v25,16,v26\nM v[156:159],a[92:95],a[108:111],v[156:159]\nds_read_b64_tr_b16 a[54:55],v12 offset:1024\nds_read_b64_tr_b16 a[56:57],v9 offset:1536\nM v[148:151],a[92:95],a[104:107],v[148:151]\n" \
"ds_read_b64_tr_b16 a[58:59],v10 offset:1536\nv_and_b32 v28,0xffff0000,v28\nM v[144:147],a[88:91],a[104:107],v[144:147]\nds_read_b64_tr_b16 a[60:61],v11 offset:1536\nv_and_b32 v26,0xffff0000,v26\nM v[160:163],a[88:91],a[112:115],v[160:163]\nds_read_b64_tr_b16 a[62:63],v12 offset:1536\nv_mul_f32 v25,v25,v27\n" \
"M v[164:167],a[92:95],a[112:115],v[164:167]\nds_read_b128 a[24:27],v7 offset:12288\nv_mul_f32 v26,v26,v28\nM v[172:175],a[92:95],a[116:119],v[172:175]\nds_read_b128 a[28:31],v7 offset:14336\nC v51,v25,v29\nM v[168:171],a[88:91],a[116:119],v[168:171]\nC v55,v26,v58\n" \
"v_accvgpr_read_b32 v39,a210\nM v[184:187],a[88:91],a[124:127],v[184:187]\nv_mul_f32 v25,v30,v45\nv_mul_f32 v26,v36,v49\nM v[188:191],a[92:95],a[124:127],v[188:191]\nv_lshlrev_b32 v33,16,v39\nv_fmac_f32 v25,v31,v44\nM v[180:183],a[92:95],a[120:123],v[180:183]\n" \
"v_fmac_f32 v26,v37,v48\nv_and_b32 v39,0xffff0000,v39\nM v[176:179],a[88:91],a[120:123],v[176:179]\nv_fmac_f32 v25,v32,v43\nv_fmac_f32 v26,v38,v47\nM v[64:67],a[80:83],a[96:99],v[64:67]\nv_fmac_f32 v25,v33,v42\nv_fmac_f32 v26,v39,v46\n" \
"M v[68:71],a[84:87],a[96:99],v[68:71]\nv_accvgpr_read_b32 v28,a226\nC v26,v25,v26\nM v[76:79],a[84:87],a[100:103],v[76:79]\nv_lshlrev_b32 v27,16,v28\nv_lshlrev_b32 v25,16,v26\nM v[72:75],a[80:83],a[100:103],v[72:75]\nv_and_b32 v28,0xffff0000,v28\n" \
"v_and_b32 v26,0xffff0000,v26\nM v[88:91],a[80:83],a[108:111],v[88:91]\nv_mul_f32 v29,v25,v27\nv_mul_f32 v58,v26,v28\nM v[92:95],a[84:87],a[108:111],v[92:95]\nv_accvgpr_read_b32 v40,a212\nv_lshlrev_b32 v34,16,v40\nM v[84:87],a[84:87],a[104:107],v[84:87]\n" \
"v_mul_f32 v25,v31,v45\nv_mul_f32 v26,v37,v49\nM v[80:83],a[80:83],a[104:107],v[80:83]\nds_read_b128 a[16:19],v7 offset:8192\nv_fmac_f32 v25,v32,v44\nM v[96:99],a[80:83],a[112:115],v[96:99]\nds_read_b128 a[20:23],v7 offset:10240\nv_and_b32 v40,0xffff0000,v40\n" \
"M v[100:103],a[84:87],a[112:115],v[100:103]\nv_fmac_f32 v26,v38,v48\nv_fmac_f32 v25,v33,v43\nM v[108:111],a[84:87],a[116:119],v[108:111]\nv_fmac_f32 v26,v39,v47\nv_fmac_f32 v25,v34,v42\nM v[104:107],a[80:83],a[116:119],v[104:107]\nv_fmac_f32 v26,v40,v46\n" \
"v_accvgpr_read_b32 v28,a228\nM v[120:123],a[80:83],a[124:127],v[120:123]\nC v26,v25,v26\nv_lshlrev_b32 v27,16,v28\nM v[124:127],a[84:87],a[124:127],v[124:127]\nv_lshlrev_b32 v25,16,v26\nv_mul_f32 v25,v25,v27\nM v[116:119],a[84:87],a[120:123],v[116:119]\n" \
"C v50,v25,v29\nv_accvgpr_read_b32 v45,a235\nM v[112:115],a[80:83],a[120:123],v[112:115]\nv_and_b32 v28,0xffff0000,v28\nv_and_b32 v26,0xffff0000,v26\nM v[192:195],a[64:67],a[96:99],v[192:195]\nv_lshlrev_b32 v44,16,v45\nv_accvgpr_read_b32 v49,a237\n" \
"M v[196:199],a[68:71],a[96:99],v[196:199]\nds_read_b128 a[0:3],v7 offset:0\nv_accvgpr_read_b32 v36,a193\nM v[204:207],a[68:71],a[100:103],v[204:207]\nds_read_b128 a[4:7],v7 offset:2048\nv_accvgpr_read_b32 v37,a195\nM v[200:203],a[64:67],a[100:103],v[200:203]\nv_mul_f32 v26,v26,v28\n" \
"v_accvgpr_read_b32 v43,a234\nM v[216:219],a[64:67],a[108:111],v[216:219]\nv_accvgpr_read_b32 v47,a236\nv_lshlrev_b32 v48,16,v49\nM v[220:223],a[68:71],a[108:111],v[220:223]\nv_and_b32 v45,0xffff0000,v45\nv_lshlrev_b32 v30,16,v36\nM v[212:215],a[68:71],a[104:107],v[212:215]\n" \
"buffer_store_dwordx4 v[50:53],v60,s[36:39],s98 offen offset:0 sc1\nM v[208:211],a[64:67],a[104:107],v[208:211]\ns_mov_b32 s98,79699968\nv_lshlrev_b32 v31,16,v37\nv_accvgpr_read_b32 v38,a197\nM v[224:227],a[64:67],a[112:115],v[224:227]\nC v54,v26,v58\nv_accvgpr_read_b32 v39,a199\n" \
"M v[228:231],a[68:71],a[112:115],v[228:231]\nv_lshlrev_b32 v42,16,v43\nv_and_b32 v49,0xffff0000,v49\nM v[236:239],a[68:71],a[116:119],v[236:239]\nv_lshlrev_b32 v46,16,v47\nv_and_b32 v36,0xffff0000,v36\nM v[232:235],a[64:67],a[116:119],v[232:235]\nv_lshlrev_b32 v32,16,v38\n" \
"v_mul_f32 v25,v30,v45\nM v[248:251],a[64:67],a[124:127],v[248:251]\nds_read_b128 a[8:11],v7 offset:4096\nds_read_b128 a[12:15],v7 offset:6144\nM v[252:255],a[68:71],a[124:127],v[252:255]\nv_and_b32 v37,0xffff0000,v37\nv_lshlrev_b32 v33,16,v39\nM v[244:247],a[68:71],a[120:123],v[244:247]\n" \
"v_and_b32 v43,0xffff0000,v43\nv_mul_f32 v26,v36,v49\nM v[240:243],a[64:67],a[120:123],v[240:243]\nds_read_b128 a[64:67],v8 offset:0\nds_read_b128 a[68:71],v8 offset:2048\nM a[128:131],a[72:75],a[96:99],a[128:131]\nv_and_b32 v47,0xffff0000,v47\nv_and_b32 v38,0xffff0000,v38\n" \
"M a[132:135],a[76:79],a[96:99],a[132:135]\nds_read_b128 a[80:83],v8 offset:8192\nv_fmac_f32 v25,v31,v44\nM a[140:143],a[76:79],a[100:103],a[140:143]\nv_and_b32 v39,0xffff0000,v39\nv_fmac_f32 v26,v37,v48\nM a[136:139],a[72:75],a[100:103],a[136:139]\nv_fmac_f32 v25,v32,v43\n" \
"v_fmac_f32 v26,v38,v47\nM a[152:155],a[72:75],a[108:111],a[152:155]\nv_fmac_f32 v25,v33,v42\nv_fmac_f32 v26,v39,v46\nM a[156:159],a[76:79],a[108:111],a[156:159]\nds_read_b128 a[84:87],v8 offset:10240\nv_accvgpr_read_b32 v28,a215\nM a[148:151],a[76:79],a[104:107],a[148:151]\n" \
"s_waitcnt vmcnt(19) lgkmcnt(12)\nds_read_b128 a[88:91],v8 offset:12288\nds_read_b128 a[92:95],v8 offset:14336\nM a[144:147],a[72:75],a[104:107],a[144:147]\ns_barrier\nds_read_b64_tr_b16 a[96:97],v9 offset:16384\nM a[160:163],a[72:75],a[112:115],a[160:163]\nds_read_b64_tr_b16 a[98:99],v10 offset:16384\n" \
"C v26,v25,v26\nM a[164:167],a[76:79],a[112:115],a[164:167]\nds_read_b64_tr_b16 a[100:101],v11 offset:16384\nv_lshlrev_b32 v27,16,v28\nM a[172:175],a[76:79],a[116:119],a[172:175]\nds_read_b64_tr_b16 a[102:103],v12 offset:16384\nds_read_b64_tr_b16 a[104:105],v9 offset:16896\nM a[168:171],a[72:75],a[116:119],a[168:171]\n" \
"ds_read_b64_tr_b16 a[106:107],v10 offset:16896\nv_lshlrev_b32 v25,16,v26\nM a[184:187],a[72:75],a[124:127],a[184:187]\nds_read_b64_tr_b16 a[108:109],v11 offset:16896\nv_and_b32 v28,0xffff0000,v28\nM a[188:191],a[76:79],a[124:127],a[188:191]\ns_waitcnt lgkmcnt(6)\nds_read_b64_tr_b16 a[110:111],v12 offset:16896\n" \
"ds_read_b64_tr_b16 a[112:113],v9 offset:17408\nM a[180:183],a[76:79],a[120:123],a[180:183]\nbuffer_store_dwordx4 v[54:57],v60,s[36:39],s99 offen offset:0 sc1\nM a[176:179],a[72:75],a[120:123],a[176:179]\ns_mov_b32 s99,79704064\nds_read_b128 a[72:75],v8 offset:4096\nds_read_b128 a[76:79],v8 offset:6144\nM v[128:131],a[24:27],a[32:35],v[128:131]\n" \
"ds_read_b64_tr_b16 a[114:115],v10 offset:17408\nv_and_b32 v26,0xffff0000,v26\nM v[132:135],a[28:31],a[32:35],v[132:135]\nds_read_b64_tr_b16 a[116:117],v11 offset:17408\nv_mul_f32 v29,v25,v27\nM v[140:143],a[28:31],a[36:39],v[140:143]\nds_read_b64_tr_b16 a[118:119],v12 offset:17408\nv_mul_f32 v58,v26,v28\n" \
"M v[136:139],a[24:27],a[36:39],v[136:139]\nds_read_b64_tr_b16 a[120:121],v9 offset:17920\nds_read_b64_tr_b16 a[122:123],v10 offset:17920\nM v[152:155],a[24:27],a[44:47],v[152:155]\nds_read_b64_tr_b16 a[124:125],v11 offset:17920\nv_accvgpr_read_b32 v40,a201\nM v[156:159],a[28:31],a[44:47],v[156:159]\nds_read_b64_tr_b16 a[126:127],v12 offset:17920\n" \
"v_lshlrev_b32 v34,16,v40\nM v[148:151],a[28:31],a[40:43],v[148:151]\nv_mul_f32 v25,v31,v45\nv_mul_f32 v26,v37,v49\nM v[144:147],a[24:27],a[40:43],v[144:147]\nv_fmac_f32 v25,v32,v44\nv_and_b32 v40,0xffff0000,v40\nM v[160:163],a[24:27],a[48:51],v[160:163]\n" \
"v_fmac_f32 v26,v38,v48\nv_fmac_f32 v25,v33,v43\nM v[164:167],a[28:31],a[48:51],v[164:167]\nv_fmac_f32 v26,v39,v47\nv_fmac_f32 v25,v34,v42\nM v[172:175],a[28:31],a[52:55],v[172:175]\nv_fmac_f32 v26,v40,v46\nv_accvgpr_read_b32 v28,a217\n" \
"M v[168:171],a[24:27],a[52:55],v[168:171]\nC v26,v25,v26\nv_lshlrev_b32 v27,16,v28\nM v[184:187],a[24:27],a[60:63],v[184:187]\nv_lshlrev_b32 v25,16,v26\nv_and_b32 v28,0xffff0000,v28\nM v[188:191],a[28:31],a[60:63],v[188:191]\nv_and_b32 v26,0xffff0000,v26\n" \
"v_mul_f32 v25,v25,v27\nM v[180:183],a[28:31],a[56:59],v[180:183]\nv_mul_f32 v26,v26,v28\nC v53,v25,v29\nM v[176:179],a[24:27],a[56:59],v[176:179]\nC v57,v26,v58\nv_accvgpr_read_b32 v41,a203\nM v[64:67],a[16:19],a[32:35],v[64:67]\n" \
"v_mul_f32 v25,v32,v45\nv_lshlrev_b32 v35,16,v41\nM v[68:71],a[20:23],a[32:35],v[68:71]\nv_mul_f32 v26,v38,v49\nv_fmac_f32 v25,v33,v44\nM v[76:79],a[20:23],a[36:39],v[76:79]\nv_fmac_f32 v26,v39,v48\nv_and_b32 v41,0xffff0000,v41\n" \
"M v[72:75],a[16:19],a[36:39],v[72:75]\nv_fmac_f32 v25,v34,v43\nv_fmac_f32 v26,v40,v47\nM v[88:91],a[16:19],a[44:47],v[88:91]\nv_fmac_f32 v25,v35,v42\nv_fmac_f32 v26,v41,v46\nM v[92:95],a[20:23],a[44:47],v[92:95]\nv_accvgpr_read_b32 v28,a219\n" \
"C v26,v25,v26\nM v[84:87],a[20:23],a[40:43],v[84:87]\ns_mov_b64 exec,0xff\nglobal_load_dword v0,v2,s[100:101]\ns_mov_b64 exec,-1\nM v[80:83],a[16:19],a[40:43],v[80:83]\nv_lshlrev_b32 v27,16,v28\nv_lshlrev_b32 v25,16,v26\n" \
"M v[96:99],a[16:19],a[48:51],v[96:99]\nbuffer_load_dwordx4 v21,s[32:35],s47 offen offset:0 lds\nM v[100:103],a[20:23],a[48:51],v[100:103]\nLG7_%=:\ns_cbranch_execz LHR0_%=\ns_add_u32 m0,s22,50048\nv_and_b32 v28,0xffff0000,v28\nv_and_b32 v26,0xffff0000,v26\n" \
"M v[108:111],a[20:23],a[52:55],v[108:111]\nv_mul_f32 v29,v25,v27\nv_mul_f32 v58,v26,v28\nM v[104:107],a[16:19],a[52:55],v[104:107]\nv_accvgpr_read_b32 v36,a205\nv_lshlrev_b32 v30,16,v36\nM v[120:123],a[16:19],a[60:63],v[120:123]\nbuffer_load_dwordx4 v21,s[32:35],s47 offen offset:128 lds\n" \
"M v[124:127],a[20:23],a[60:63],v[124:127]\ns_add_u32 m0,s22,51200\nv_mul_f32 v25,v33,v45\nv_mul_f32 v26,v39,v49\nM v[116:119],a[20:23],a[56:59],v[116:119]\ns_mov_b64 s[32:33],s[44:45]\nv_fmac_f32 v25,v34,v44\nv_and_b32 v36,0xffff0000,v36\n" \
"M v[112:115],a[16:19],a[56:59],v[112:115]\ns_mov_b32 s34,s49\nv_fmac_f32 v26,v40,v48\nv_fmac_f32 v25,v35,v43\nM v[192:195],a[0:3],a[32:35],v[192:195]\nbuffer_load_dwordx4 v21,s[92:95],s47 offen offset:0 lds\nM v[196:199],a[4:7],a[32:35],v[196:199]\ns_add_u32 m0,s22,52096\n" \
"v_fmac_f32 v26,v41,v47\nv_fmac_f32 v25,v30,v42\nM v[204:207],a[4:7],a[36:39],v[204:207]\nv_fmac_f32 v26,v36,v46\nv_accvgpr_read_b32 v28,a221\nM v[200:203],a[0:3],a[36:39],v[200:203]\nC v26,v25,v26\nv_lshlrev_b32 v27,16,v28\n" \
"M v[216:219],a[0:3],a[44:47],v[216:219]\nbuffer_load_dwordx4 v21,s[92:95],s47 offen offset:128 lds\nM v[220:223],a[4:7],a[44:47],v[220:223]\ns_add_u32 m0,s50,32768\nv_lshlrev_b32 v25,16,v26\nv_and_b32 v28,0xffff0000,v28\nM v[212:215],a[4:7],a[40:43],v[212:215]\ns_mov_b32 s47,0\n" \
"v_and_b32 v26,0xffff0000,v26\nv_mul_f32 v25,v25,v27\nM v[208:211],a[0:3],a[40:43],v[208:211]\nv_mul_f32 v26,v26,v28\nC v52,v25,v29\nM v[224:227],a[0:3],a[48:51],v[224:227]\nbuffer_load_dwordx4 v13,s[28:31],s46 offen lds\nM v[228:231],a[4:7],a[48:51],v[228:231]\n" \
"s_add_u32 m0,s50,36864\nC v56,v26,v58\nv_accvgpr_read_b32 v37,a207\nM v[236:239],a[4:7],a[52:55],v[236:239]\nv_mul_f32 v25,v34,v45\nv_lshlrev_b32 v31,16,v37\nM v[232:235],a[0:3],a[52:55],v[232:235]\nv_mul_f32 v26,v40,v49\n" \
"v_fmac_f32 v25,v35,v44\nM v[248:251],a[0:3],a[60:63],v[248:251]\nbuffer_load_dwordx4 v14,s[28:31],s46 offen lds\nM v[252:255],a[4:7],a[60:63],v[252:255]\ns_add_u32 m0,s50,40960\nv_fmac_f32 v26,v41,v48\nv_and_b32 v37,0xffff0000,v37\nM v[244:247],a[4:7],a[56:59],v[244:247]\n" \
"v_fmac_f32 v25,v30,v43\nv_fmac_f32 v26,v36,v47\nM v[240:243],a[0:3],a[56:59],v[240:243]\nv_fmac_f32 v25,v31,v42\nv_fmac_f32 v26,v37,v46\nM a[128:131],a[8:11],a[32:35],a[128:131]\nbuffer_load_dwordx4 v15,s[28:31],s46 offen lds\nM a[132:135],a[12:15],a[32:35],a[132:135]\n" \
"s_add_u32 m0,s50,45056\nv_accvgpr_read_b32 v28,a223\nC v26,v25,v26\nM a[140:143],a[12:15],a[36:39],a[140:143]\nv_lshlrev_b32 v27,16,v28\nv_lshlrev_b32 v25,16,v26\nM a[136:139],a[8:11],a[36:39],a[136:139]\nv_and_b32 v28,0xffff0000,v28\n" \
"v_and_b32 v26,0xffff0000,v26\nM a[152:155],a[8:11],a[44:47],a[152:155]\nbuffer_load_dwordx4 v16,s[28:31],s46 offen lds\nM a[156:159],a[12:15],a[44:47],a[156:159]\ns_add_u32 m0,s50,49152\nv_mul_f32 v29,v25,v27\nv_mul_f32 v58,v26,v28\nM a[148:151],a[12:15],a[40:43],a[148:151]\n" \
"s_waitcnt vmcnt(17) lgkmcnt(0)\nv_accvgpr_read_b32 v38,a209\nv_mul_f32 v25,v35,v45\nM a[144:147],a[8:11],a[40:43],a[144:147]\ns_mov_b32 s46,0\ns_barrier\nds_read_b64_tr_b16 a[32:33],v9 offset:32768\nM a[160:163],a[8:11],a[48:51],a[160:163]\n" \
"buffer_load_dwordx4 v17,s[28:31],s46 offen lds\nM a[164:167],a[12:15],a[48:51],a[164:167]\ns_add_u32 m0,s50,53248\nds_read_b64_tr_b16 a[34:35],v10 offset:32768\nv_mul_f32 v26,v41,v49\nM a[172:175],a[12:15],a[52:55],a[172:175]\nds_read_b64_tr_b16 a[36:37],v11 offset:32768\nds_read_b64_tr_b16 a[38:39],v12 offset:32768\n" \
"M a[168:171],a[8:11],a[52:55],a[168:171]\nds_read_b64_tr_b16 a[40:41],v9 offset:33280\nv_lshlrev_b32 v32,16,v38\nM a[184:187],a[8:11],a[60:63],a[184:187]\nbuffer_load_dwordx4 v18,s[28:31],s46 offen lds\nM a[188:191],a[12:15],a[60:63],a[188:191]\ns_add_u32 m0,s50,57344\nds_read_b64_tr_b16 a[42:43],v10 offset:33280\n" \
"v_fmac_f32 v25,v30,v44\nM a[180:183],a[12:15],a[56:59],a[180:183]\nds_read_b64_tr_b16 a[44:45],v11 offset:33280\nv_fmac_f32 v26,v36,v48\nM a[176:179],a[8:11],a[56:59],a[176:179]\nds_read_b64_tr_b16 a[46:47],v12 offset:33280\nv_and_b32 v38,0xffff0000,v38\nM v[128:131],a[88:91],a[96:99],v[128:131]\n" \
"buffer_load_dwordx4 v19,s[28:31],s46 offen lds\nM v[132:135],a[92:95],a[96:99],v[132:135]\ns_add_u32 m0,s50,61440\nds_read_b64_tr_b16 a[48:49],v9 offset:33792\nv_fmac_f32 v25,v31,v43\nM v[140:143],a[92:95],a[100:103],v[140:143]\nds_read_b64_tr_b16 a[50:51],v10 offset:33792\nv_fmac_f32 v26,v37,v47\n" \
"M v[136:139],a[88:91],a[100:103],v[136:139]\nds_read_b64_tr_b16 a[52:53],v11 offset:33792\nds_read_b64_tr_b16 a[54:55],v12 offset:33792\nM v[152:155],a[88:91],a[108:111],v[152:155]\nbuffer_load_dwordx4 v20,s[28:31],s46 offen lds\nM v[156:159],a[92:95],a[108:111],v[156:159]\ns_add_u32 m0,s51,0\nv_fmac_f32 v25,v32,v42\n" \
"v_fmac_f32 v26,v38,v46\nM v[148:151],a[92:95],a[104:107],v[148:151]\ns_mov_b32 s46,128\nds_read_b64_tr_b16 a[56:57],v9 offset:34304\nds_read_b64_tr_b16 a[58:59],v10 offset:34304\nM v[144:147],a[88:91],a[104:107],v[144:147]\nds_read_b64_tr_b16 a[60:61],v11 offset:34304\nv_accvgpr_read_b32 v28,a225\n" \
"M v[160:163],a[88:91],a[112:115],v[160:163]\nbuffer_load_dwordx4 v21,s[32:35],s47 offen offset:0 lds\nM v[164:167],a[92:95],a[112:115],v[164:167]\ns_add_u32 m0,s51,896\nds_read_b64_tr_b16 a[62:63],v12 offset:34304\nC v26,v25,v26\nM v[172:175],a[92:95],a[116:119],v[172:175]\nds_read_b128 a[24:27],v5 offset:12288\n" \
"v_lshlrev_b32 v27,16,v28\nM v[168:171],a[88:91],a[116:119],v[168:171]\nds_read_b128 a[28:31],v5 offset:14336\nv_lshlrev_b32 v25,16,v26\nM v[184:187],a[88:91],a[124:127],v[184:187]\nbuffer_load_dwordx4 v21,s[32:35],s47 offen offset:128 lds\nM v[188:191],a[92:95],a[124:127],v[188:191]\ns_add_u32 m0,s51,8192\n" \
"v_and_b32 v28,0xffff0000,v28\nv_and_b32 v26,0xffff0000,v26\nM v[180:183],a[92:95],a[120:123],v[180:183]\ns_mov_b32 s47,262144\nv_mul_f32 v25,v25,v27\nv_mul_f32 v26,v26,v28\nM v[176:179],a[88:91],a[120:123],v[176:179]\nC v51,v25,v29\n" \
"C v55,v26,v58\nM v[64:67],a[80:83],a[96:99],v[64:67]\nbuffer_load_dwordx4 v21,s[32:35],s47 offen offset:0 lds\nM v[68:71],a[84:87],a[96:99],v[68:71]\ns_add_u32 m0,s51,9088\nv_accvgpr_read_b32 v39,a211\nv_mul_f32 v25,v30,v45\nM v[76:79],a[84:87],a[100:103],v[76:79]\n" \
"v_lshlrev_b32 v33,16,v39\nv_mul_f32 v26,v36,v49\nM v[72:75],a[80:83],a[100:103],v[72:75]\nv_fmac_f32 v25,v31,v44\nv_fmac_f32 v26,v37,v48\nM v[88:91],a[80:83],a[108:111],v[88:91]\nbuffer_load_dwordx4 v21,s[32:35],s47 offen offset:128 lds\nM v[92:95],a[84:87],a[108:111],v[92:95]\n" \
"s_add_u32 m0,s51,16384\nv_and_b32 v39,0xffff0000,v39\nv_fmac_f32 v25,v32,v43\nM v[84:87],a[84:87],a[104:107],v[84:87]\ns_mov_b32 s47,524288\nv_fmac_f32 v26,v38,v47\nv_fmac_f32 v25,v33,v42\nM v[80:83],a[80:83],a[104:107],v[80:83]\n" \
"v_fmac_f32 v26,v39,v46\nv_accvgpr_read_b32 v28,a227\nM v[96:99],a[80:83],a[112:115],v[96:99]\nbuffer_load_dwordx4 v21,s[32:35],s47 offen offset:0 lds\nM v[100:103],a[84:87],a[112:115],v[100:103]\ns_add_u32 m0,s51,17280\nC v26,v25,v26\nv_lshlrev_b32 v27,16,v28\n" \
"M v[108:111],a[84:87],a[116:119],v[108:111]\nds_read_b128 a[16:19],v5 offset:8192\nv_lshlrev_b32 v25,16,v26\nM v[104:107],a[80:83],a[116:119],v[104:107]\nds_read_b128 a[20:23],v5 offset:10240\nv_and_b32 v28,0xffff0000,v28\nM v[120:123],a[80:83],a[124:127],v[120:123]\nbuffer_load_dwordx4 v21,s[32:35],s47 offen offset:128 lds\n" \
"M v[124:127],a[84:87],a[124:127],v[124:127]\ns_add_u32 m0,s51,24576\nv_and_b32 v26,0xffff0000,v26\nv_mul_f32 v29,v25,v27\nM v[116:119],a[84:87],a[120:123],v[116:119]\ns_mov_b32 s47,786432\nv_mul_f32 v58,v26,v28\nv_accvgpr_read_b32 v40,a213\n" \
"M v[112:115],a[80:83],a[120:123],v[112:115]\nv_lshlrev_b32 v34,16,v40\nv_mul_f32 v25,v31,v45\nM v[192:195],a[64:67],a[96:99],v[192:195]\ns_mov_b64 exec,0xff\nglobal_load_dword v0,v1,s[76:77]\ns_mov_b64 exec,-1\nM v[196:199],a[68:71],a[96:99],v[196:199]\n" \
"v_mul_f32 v26,v37,v49\nv_fmac_f32 v25,v32,v44\nM v[204:207],a[68:71],a[100:103],v[204:207]\ns_mov_b64 exec,0xff00\nglobal_load_dword v0,v1,s[76:77]\ns_mov_b64 exec,-1\nM v[200:203],a[64:67],a[100:103],v[200:203]\ns_add_u32 s76,s76,2097152\n" \
"s_addc_u32 s77,s77,0\nv_and_b32 v40,0xffff0000,v40\nv_fmac_f32 v26,v38,v48\nM v[216:219],a[64:67],a[108:111],v[216:219]\ns_mov_b64 exec,0xff\nglobal_load_dword v0,v1,s[76:77]\ns_mov_b64 exec,-1\nM v[220:223],a[68:71],a[108:111],v[220:223]\n" \
"v_fmac_f32 v25,v33,v43\nv_fmac_f32 v26,v39,v47\nM v[212:215],a[68:71],a[104:107],v[212:215]\ns_mov_b64 exec,0xff00\nglobal_load_dword v0,v1,s[76:77]\ns_mov_b64 exec,-1\nM v[208:211],a[64:67],a[104:107],v[208:211]\ns_mov_b32 s76,0xf149f2ca\n" \
"v_fmac_f32 v25,v34,v42\nv_fmac_f32 v26,v40,v46\nM v[224:227],a[64:67],a[112:115],v[224:227]\nv_accvgpr_read_b32 v28,a229\nC v26,v25,v26\nM v[228:231],a[68:71],a[112:115],v[228:231]\nv_lshlrev_b32 v27,16,v28\nv_lshlrev_b32 v25,16,v26\n" \
"M v[236:239],a[68:71],a[116:119],v[236:239]\nv_mul_f32 v25,v25,v27\nC v50,v25,v29\nM v[232:235],a[64:67],a[116:119],v[232:235]\nds_read_b128 a[0:3],v5 offset:0\nds_read_b128 a[4:7],v5 offset:2048\nM v[248:251],a[64:67],a[124:127],v[248:251]\nds_read_b128 a[8:11],v5 offset:4096\n" \
"v_and_b32 v28,0xffff0000,v28\nM v[252:255],a[68:71],a[124:127],v[252:255]\nds_read_b128 a[12:15],v5 offset:6144\nv_and_b32 v26,0xffff0000,v26\nM v[244:247],a[68:71],a[120:123],v[244:247]\nv_mul_f32 v26,v26,v28\nC v54,v26,v58\nM v[240:243],a[64:67],a[120:123],v[240:243]\n" \
"ds_read_b128 a[64:67],v6 offset:0\nds_read_b128 a[68:71],v6 offset:2048\nM a[128:131],a[72:75],a[96:99],a[128:131]\nds_read_b128 a[80:83],v6 offset:8192\nv_add_u32 v5,s54,v5\nM a[132:135],a[76:79],a[96:99],a[132:135]\nds_read_b128 a[84:87],v6 offset:10240\nv_add_u32 v7,s54,v7\n" \
"M a[140:143],a[76:79],a[100:103],a[140:143]\nds_read_b128 a[88:91],v6 offset:12288\nv_add_u32 v8,s54,v8\nM a[136:139],a[72:75],a[100:103],a[136:139]\nds_read_b128 a[92:95],v6 offset:14336\nv_mov_b32 v58,s18\nM a[152:155],a[72:75],a[108:111],a[152:155]\nv_add_u32 v25,-16,v4\n" \
"v_mov_b32 v29,s62\nM a[156:159],a[76:79],a[108:111],a[156:159]\ns_and_b32 s62,%1,7\nv_mov_b32 v28,0\nv_add_u32 v26,-512,v4\nM a[148:151],a[76:79],a[104:107],a[148:151]\ns_waitcnt vmcnt(18) lgkmcnt(14)\nv_add_u32 v27,512,v4\n" \
"M a[144:147],a[72:75],a[104:107],a[144:147]\ns_lshl_b32 s62,s62,8\ns_barrier\nds_read_b64_tr_b16 a[96:97],v9 offset:49152\nM a[160:163],a[72:75],a[112:115],a[160:163]\ns_add_u32 s62,s62,94380032\nds_read_b64_tr_b16 a[98:99],v10 offset:49152\nds_read_b64_tr_b16 a[100:101],v11 offset:49152\n" \
"M a[164:167],a[76:79],a[112:115],a[164:167]\nds_read_b64_tr_b16 a[102:103],v12 offset:49152\nM a[172:175],a[76:79],a[116:119],a[172:175]\nds_read_b64_tr_b16 a[104:105],v9 offset:49664\nds_read_b64_tr_b16 a[106:107],v10 offset:49664\nM a[168:171],a[72:75],a[116:119],a[168:171]\nds_read_b64_tr_b16 a[108:109],v11 offset:49664\nM a[184:187],a[72:75],a[124:127],a[184:187]\n" \
"ds_read_b64_tr_b16 a[110:111],v12 offset:49664\nM a[188:191],a[76:79],a[124:127],a[188:191]\nds_read_b64_tr_b16 a[112:113],v9 offset:50176\nds_read_b64_tr_b16 a[114:115],v10 offset:50176\nM a[180:183],a[76:79],a[120:123],a[180:183]\nds_read_b64_tr_b16 a[116:117],v11 offset:50176\nM a[176:179],a[72:75],a[120:123],a[176:179]\ns_waitcnt lgkmcnt(7)\n" \
"ds_read_b128 a[72:75],v6 offset:4096\nM v[128:131],a[24:27],a[32:35],v[128:131]\nds_read_b128 a[76:79],v6 offset:6144\nv_add_u32 v6,s54,v6\nM v[132:135],a[28:31],a[32:35],v[132:135]\nds_read_b64_tr_b16 a[118:119],v12 offset:50176\nM v[140:143],a[28:31],a[36:39],v[140:143]\nds_read_b64_tr_b16 a[120:121],v9 offset:50688\n" \
"ds_read_b64_tr_b16 a[122:123],v10 offset:50688\nM v[136:139],a[24:27],a[36:39],v[136:139]\nds_read_b64_tr_b16 a[124:125],v11 offset:50688\nv_sub_u32 v9,v9,v62\nM v[152:155],a[24:27],a[44:47],v[152:155]\nds_read_b64_tr_b16 a[126:127],v12 offset:50688\nv_sub_u32 v10,v10,v62\nM v[156:159],a[28:31],a[44:47],v[156:159]\n" \
"v_sub_u32 v11,v11,v62\nv_sub_u32 v12,v12,v62\nM v[148:151],a[28:31],a[40:43],v[148:151]\nM v[144:147],a[24:27],a[40:43],v[144:147]\nM v[160:163],a[24:27],a[48:51],v[160:163]\nM v[164:167],a[28:31],a[48:51],v[164:167]\nM v[172:175],a[28:31],a[52:55],v[172:175]\nM v[168:171],a[24:27],a[52:55],v[168:171]\n" \
"M v[184:187],a[24:27],a[60:63],v[184:187]\nM v[188:191],a[28:31],a[60:63],v[188:191]\nM v[180:183],a[28:31],a[56:59],v[180:183]\nbuffer_load_dwordx4 v21,s[32:35],s47 offen offset:0 lds\nM v[176:179],a[24:27],a[56:59],v[176:179]\ns_add_u32 m0,s51,25472\nM v[64:67],a[16:19],a[32:35],v[64:67]\nM v[68:71],a[20:23],a[32:35],v[68:71]\n" \
"M v[76:79],a[20:23],a[36:39],v[76:79]\nbuffer_store_dwordx4 v[50:53],v60,s[36:39],s98 offen offset:0 sc1\nM v[72:75],a[16:19],a[36:39],v[72:75]\nM v[88:91],a[16:19],a[44:47],v[88:91]\nM v[92:95],a[20:23],a[44:47],v[92:95]\nM v[84:87],a[20:23],a[40:43],v[84:87]\nM v[80:83],a[16:19],a[40:43],v[80:83]\nM v[96:99],a[16:19],a[48:51],v[96:99]\n" \
"M v[100:103],a[20:23],a[48:51],v[100:103]\nM v[108:111],a[20:23],a[52:55],v[108:111]\nM v[104:107],a[16:19],a[52:55],v[104:107]\nM v[120:123],a[16:19],a[60:63],v[120:123]\nM v[124:127],a[20:23],a[60:63],v[124:127]\nM v[116:119],a[20:23],a[56:59],v[116:119]\nM v[112:115],a[16:19],a[56:59],v[112:115]\nM v[192:195],a[0:3],a[32:35],v[192:195]\n" \
"M v[196:199],a[4:7],a[32:35],v[196:199]\nM v[204:207],a[4:7],a[36:39],v[204:207]\nM v[200:203],a[0:3],a[36:39],v[200:203]\nM v[216:219],a[0:3],a[44:47],v[216:219]\nM v[220:223],a[4:7],a[44:47],v[220:223]\nM v[212:215],a[4:7],a[40:43],v[212:215]\nM v[208:211],a[0:3],a[40:43],v[208:211]\nM v[224:227],a[0:3],a[48:51],v[224:227]\n" \
"M v[228:231],a[4:7],a[48:51],v[228:231]\nM v[236:239],a[4:7],a[52:55],v[236:239]\nbuffer_store_dwordx4 v[54:57],v60,s[36:39],s99 offen offset:0 sc1\nM v[232:235],a[0:3],a[52:55],v[232:235]\ns_nop 0\nv_lshlrev_b32 v56,23,v4\nv_bfe_u32 v57,v4,4,5\nM v[248:251],a[0:3],a[60:63],v[248:251]\n" \
"v_lshl_add_u32 v57,v57,3,s62\nv_add_u32 v55,s56,v4\nM v[252:255],a[4:7],a[60:63],v[252:255]\nM v[244:247],a[4:7],a[56:59],v[244:247]\nM v[240:243],a[0:3],a[56:59],v[240:243]\nM a[128:131],a[8:11],a[32:35],a[128:131]\nM a[132:135],a[12:15],a[32:35],a[132:135]\nM a[140:143],a[12:15],a[36:39],a[140:143]\n" \
"M a[136:139],a[8:11],a[36:39],a[136:139]\nM a[152:155],a[8:11],a[44:47],a[152:155]\nM a[156:159],a[12:15],a[44:47],a[156:159]\nM a[148:151],a[12:15],a[40:43],a[148:151]\ns_waitcnt vmcnt(9) lgkmcnt(0)\nM a[144:147],a[8:11],a[40:43],a[144:147]\ns_barrier\nds_read_b64_tr_b16 a[0:1],v9 offset:0\n" \
"M a[160:163],a[8:11],a[48:51],a[160:163]\nds_read_b64_tr_b16 a[2:3],v10 offset:0\nds_read_b64_tr_b16 a[4:5],v11 offset:0\nM a[164:167],a[12:15],a[48:51],a[164:167]\nds_read_b128 a[44:47],v5 offset:45056\nM a[172:175],a[12:15],a[52:55],a[172:175]\nds_read_b64_tr_b16 a[6:7],v12 offset:0\nM a[168:171],a[8:11],a[52:55],a[168:171]\n" \
"ds_read_b64_tr_b16 a[16:17],v9 offset:1024\nds_read_b64_tr_b16 a[18:19],v10 offset:1024\nM a[184:187],a[8:11],a[60:63],a[184:187]\nds_read_b64_tr_b16 a[20:21],v11 offset:1024\nM a[188:191],a[12:15],a[60:63],a[188:191]\nds_read_b64_tr_b16 a[22:23],v12 offset:1024\nM a[180:183],a[12:15],a[56:59],a[180:183]\nds_read_b64_tr_b16 a[24:25],v9 offset:1536\n" \
"ds_read_b64_tr_b16 a[26:27],v10 offset:1536\nM a[176:179],a[8:11],a[56:59],a[176:179]\nds_read_b64_tr_b16 a[8:9],v9 offset:512\nM v[128:131],a[88:91],a[96:99],v[128:131]\nds_read_b64_tr_b16 a[10:11],v10 offset:512\nM v[132:135],a[92:95],a[96:99],v[132:135]\nds_read_b64_tr_b16 a[12:13],v11 offset:512\nds_read_b64_tr_b16 a[14:15],v12 offset:512\n" \
"M v[140:143],a[92:95],a[100:103],v[140:143]\nbuffer_load_dwordx4 v21,s[32:35],s47 offen offset:128 lds\nM v[136:139],a[88:91],a[100:103],v[136:139]\ns_add_u32 m0,s50,65536\nv_mul_f32 v128,s20,v128\nv_mul_f32 v129,s20,v129\nM v[152:155],a[88:91],a[108:111],v[152:155]\ns_mov_b32 s47,1048576\n" \
"v_mul_f32 v130,s20,v130\nv_mul_f32 v131,s20,v131\nM v[156:159],a[92:95],a[108:111],v[156:159]\nv_mul_f32 v132,s20,v132\nv_mul_f32 v133,s20,v133\nM v[148:151],a[92:95],a[104:107],v[148:151]\nbuffer_load_dwordx4 v13,s[28:31],s46 offen lds\nM v[144:147],a[88:91],a[104:107],v[144:147]\n" \
"s_add_u32 m0,s50,69632\nv_mul_f32 v134,s20,v134\nv_mul_f32 v135,s20,v135\nM v[160:163],a[88:91],a[112:115],v[160:163]\nv_mul_f32 v136,s20,v136\nv_mul_f32 v137,s20,v137\nM v[164:167],a[92:95],a[112:115],v[164:167]\nv_mul_f32 v138,s20,v138\n" \
"v_mul_f32 v139,s20,v139\nM v[172:175],a[92:95],a[116:119],v[172:175]\nbuffer_load_dwordx4 v14,s[28:31],s46 offen lds\nM v[168:171],a[88:91],a[116:119],v[168:171]\ns_add_u32 m0,s50,73728\nds_read_b64_tr_b16 a[28:29],v11 offset:1536\nv_mul_f32 v140,s20,v140\nM v[184:187],a[88:91],a[124:127],v[184:187]\n" \
"ds_read_b64_tr_b16 a[30:31],v12 offset:1536\nds_read_b128 a[40:43],v5 offset:40960\nM v[188:191],a[92:95],a[124:127],v[188:191]\nv_mul_f32 v141,s20,v141\nv_mul_f32 v142,s20,v142\nM v[180:183],a[92:95],a[120:123],v[180:183]\nbuffer_load_dwordx4 v15,s[28:31],s46 offen lds\nM v[176:179],a[88:91],a[120:123],v[176:179]\n" \
"s_add_u32 m0,s50,77824\nv_mul_f32 v143,s20,v143\nv_mul_f32 v144,s20,v144\nM v[64:67],a[80:83],a[96:99],v[64:67]\nv_mul_f32 v145,s20,v145\nv_mul_f32 v146,s20,v146\nM v[68:71],a[84:87],a[96:99],v[68:71]\nds_read_b128 a[32:35],v5 offset:32768\n" \
"v_mul_f32 v147,s20,v147\nM v[76:79],a[84:87],a[100:103],v[76:79]\nbuffer_load_dwordx4 v16,s[28:31],s46 offen lds\nM v[72:75],a[80:83],a[100:103],v[72:75]\ns_add_u32 m0,s50,81920\ns_mov_b32 s46,128\nv_mul_f32 v64,s20,v64\nv_mul_f32 v65,s20,v65\n" \
"M v[88:91],a[80:83],a[108:111],v[88:91]\nv_mul_f32 v66,s20,v66\nv_mul_f32 v67,s20,v67\nM v[92:95],a[84:87],a[108:111],v[92:95]\nC v64,v64,v65\nC v65,v66,v67\nM v[84:87],a[84:87],a[104:107],v[84:87]\nbuffer_load_dwordx4 v17,s[28:31],s46 offen lds\n" \
"M v[80:83],a[80:83],a[104:107],v[80:83]\ns_add_u32 m0,s50,86016\nv_mul_f32 v68,s20,v68\nv_mul_f32 v69,s20,v69\nM v[96:99],a[80:83],a[112:115],v[96:99]\nv_mul_f32 v148,s20,v148\nv_mul_f32 v149,s20,v149\nM v[100:103],a[84:87],a[112:115],v[100:103]\n" \
"v_mul_f32 v150,s20,v150\nv_mul_f32 v151,s20,v151\nM v[108:111],a[84:87],a[116:119],v[108:111]\nbuffer_load_dwordx4 v18,s[28:31],s46 offen lds\nM v[104:107],a[80:83],a[116:119],v[104:107]\ns_add_u32 m0,s50,90112\nv_mul_f32 v70,s20,v70\nv_mul_f32 v152,s20,v152\n" \
"M v[120:123],a[80:83],a[124:127],v[120:123]\nv_mul_f32 v71,s20,v71\nv_mul_f32 v153,s20,v153\nM v[124:127],a[84:87],a[124:127],v[124:127]\nv_mul_f32 v154,s20,v154\nv_mul_f32 v155,s20,v155\nM v[116:119],a[84:87],a[120:123],v[116:119]\nbuffer_load_dwordx4 v19,s[28:31],s46 offen lds\n" \
"M v[112:115],a[80:83],a[120:123],v[112:115]\ns_add_u32 m0,s50,94208\nv_mul_f32 v156,s20,v156\nv_mul_f32 v157,s20,v157\nM v[192:195],a[64:67],a[96:99],v[192:195]\nv_mul_f32 v158,s20,v158\nv_mul_f32 v159,s20,v159\nM v[196:199],a[68:71],a[96:99],v[196:199]\n" \
"v_mul_f32 v160,s20,v160\nv_mul_f32 v161,s20,v161\nM v[204:207],a[68:71],a[100:103],v[204:207]\nbuffer_load_dwordx4 v20,s[28:31],s46 offen lds\nM v[200:203],a[64:67],a[100:103],v[200:203]\ns_add_u32 m0,s51,32768\nds_read_b128 a[36:39],v5 offset:36864\nv_mul_f32 v162,s20,v162\n" \
"M v[216:219],a[64:67],a[108:111],v[216:219]\ns_mov_b32 s46,256\nv_mul_f32 v163,s20,v163\nC v66,v68,v69\nM v[220:223],a[68:71],a[108:111],v[220:223]\nv_mul_f32 v72,s20,v72\nv_mul_f32 v164,s20,v164\nM v[212:215],a[68:71],a[104:107],v[212:215]\n" \
"buffer_load_dwordx4 v21,s[32:35],s47 offen offset:0 lds\nM v[208:211],a[64:67],a[104:107],v[208:211]\ns_add_u32 m0,s51,33664\nv_mul_f32 v73,s20,v73\nC v30,v128,v129\nM v[224:227],a[64:67],a[112:115],v[224:227]\nv_mul_f32 v165,s20,v165\nC v31,v130,v131\n" \
"M v[228:231],a[68:71],a[112:115],v[228:231]\nv_mul_f32 v166,s20,v166\nC v32,v132,v133\nM v[236:239],a[68:71],a[116:119],v[236:239]\nbuffer_load_dwordx4 v21,s[32:35],s47 offen offset:128 lds\nM v[232:235],a[64:67],a[116:119],v[232:235]\ns_add_u32 m0,s51,40960\nv_mul_f32 v167,s20,v167\n" \
"C v33,v134,v135\nM v[248:251],a[64:67],a[124:127],v[248:251]\ns_mov_b32 s47,1310720\nv_mul_f32 v168,s20,v168\nC v34,v136,v137\nM v[252:255],a[68:71],a[124:127],v[252:255]\nv_mul_f32 v169,s20,v169\nC v35,v138,v139\n" \
"M v[244:247],a[68:71],a[120:123],v[244:247]\nbuffer_load_dwordx4 v21,s[32:35],s47 offen offset:0 lds\nM v[240:243],a[64:67],a[120:123],v[240:243]\ns_add_u32 m0,s51,41856\nds_read_b64_tr_b16 a[48:49],v9 offset:8192\nv_mul_f32 v170,s20,v170\nM a[128:131],a[72:75],a[96:99],a[128:131]\nds_read_b64_tr_b16 a[50:51],v10 offset:8192\n" \
"ds_read_b128 a[80:83],v6 offset:32768\nM a[132:135],a[76:79],a[96:99],a[132:135]\nds_read_b128 a[84:87],v6 offset:36864\nv_mul_f32 v171,s20,v171\nM a[140:143],a[76:79],a[100:103],a[140:143]\nbuffer_load_dwordx4 v21,s[32:35],s47 offen offset:128 lds\nM a[136:139],a[72:75],a[100:103],a[136:139]\ns_add_u32 m0,s50,0\n" \
"v_mul_f32 v74,s20,v74\nC v36,v140,v141\nM a[152:155],a[72:75],a[108:111],a[152:155]\ns_mov_b32 s47,1572864\nds_read_b64_tr_b16 a[52:53],v11 offset:8192\nv_mul_f32 v75,s20,v75\nM a[156:159],a[76:79],a[108:111],a[156:159]\nds_read_b64_tr_b16 a[54:55],v12 offset:8192\n" \
"ds_read_b64_tr_b16 a[56:57],v9 offset:8704\nM a[148:151],a[76:79],a[104:107],a[148:151]\nbuffer_load_dwordx4 v13,s[28:31],s46 offen lds\nM a[144:147],a[72:75],a[104:107],a[144:147]\ns_nop 0\ns_add_u32 m0,s50,4096\nC v37,v142,v143\nC v38,v144,v145\n" \
"M a[160:163],a[72:75],a[112:115],a[160:163]\nds_read_b64_tr_b16 a[58:59],v10 offset:8704\nds_read_b64_tr_b16 a[60:61],v11 offset:8704\nM a[164:167],a[76:79],a[112:115],a[164:167]\nds_read_b64_tr_b16 a[62:63],v12 offset:8704\nC v39,v146,v147\nM a[172:175],a[76:79],a[116:119],a[172:175]\nbuffer_load_dwordx4 v14,s[28:31],s46 offen lds\n" \
"M a[168:171],a[72:75],a[116:119],a[168:171]\ns_add_u32 m0,s50,8192\nds_read_b64_tr_b16 a[64:65],v9 offset:9216\nv_mul_f32 v172,s20,v172\nM a[184:187],a[72:75],a[124:127],a[184:187]\nds_read_b64_tr_b16 a[66:67],v10 offset:9216\nv_mul_f32 v173,s20,v173\nM a[188:191],a[76:79],a[124:127],a[188:191]\n" \
"s_waitcnt lgkmcnt(5)\nds_read_b64_tr_b16 a[68:69],v11 offset:9216\nv_mul_f32 v174,s20,v174\nM a[180:183],a[76:79],a[120:123],a[180:183]\nbuffer_load_dwordx4 v15,s[28:31],s46 offen lds\nM a[176:179],a[72:75],a[120:123],a[176:179]\nLC_%=:\ns_add_u32 m0,s50,12288\n" \
"ds_read_b64_tr_b16 a[70:71],v12 offset:9216\nC v40,v148,v149\nM a[192:195],a[0:3],a[44:47],0\nds_read_b64_tr_b16 a[72:73],v9 offset:9728\nv_mul_f32 v175,s20,v175\nM a[196:199],a[4:7],a[44:47],0\nds_read_b64_tr_b16 a[74:75],v10 offset:9728\nds_read_b64_tr_b16 a[76:77],v11 offset:9728\n" \
"M a[200:203],a[8:11],a[44:47],0\nbuffer_load_dwordx4 v16,s[28:31],s46 offen lds\nM a[204:207],a[12:15],a[44:47],0\ns_mov_b32 s46,256\nC v41,v150,v151\nC v67,v70,v71\nM a[208:211],a[16:19],a[44:47],0\ns_add_u32 m0,s50,16384\n" \
"ds_read_b64_tr_b16 a[78:79],v12 offset:9728\nds_read_b128 a[88:91],v6 offset:40960\nM a[212:215],a[20:23],a[44:47],0\nds_read_b128 a[92:95],v6 offset:45056\nv_mul_f32 v76,s20,v76\nM a[216:219],a[24:27],a[44:47],0\nbuffer_load_dwordx4 v17,s[28:31],s46 offen lds\nM a[220:223],a[28:31],a[44:47],0\n" \
"s_add_u32 m0,s50,20480\nv_mul_f32 v77,s20,v77\nC v42,v152,v153\nM a[224:227],a[0:3],a[40:43],0\nC v43,v154,v155\nC v44,v156,v157\nM a[228:231],a[4:7],a[40:43],0\nC v45,v158,v159\n" \
"C v46,v160,v161\nM a[232:235],a[8:11],a[40:43],0\nbuffer_load_dwordx4 v18,s[28:31],s46 offen lds\nM a[236:239],a[12:15],a[40:43],0\ns_add_u32 m0,s50,24576\nC v47,v162,v163\nC v68,v72,v73\nM a[240:243],a[16:19],a[40:43],0\n" \
"v_mul_f32 v176,s20,v176\nC v48,v164,v165\nM a[244:247],a[20:23],a[40:43],0\nv_mul_f32 v177,s20,v177\nC v49,v166,v167\nM a[248:251],a[24:27],a[40:43],0\nbuffer_load_dwordx4 v19,s[28:31],s46 offen lds\nM a[252:255],a[28:31],a[40:43],0\n" \
"s_add_u32 m0,s50,28672\nv_mul_f32 v178,s20,v178\nC v50,v168,v169\nM v[128:131],a[0:3],a[32:35],0\nv_mul_f32 v179,s20,v179\nC v51,v170,v171\nM v[132:135],a[4:7],a[32:35],0\nC v69,v74,v75\n" \
"v_mul_f32 v180,s20,v180\nM v[136:139],a[8:11],a[32:35],0\nbuffer_load_dwordx4 v20,s[28:31],s46 offen lds\nM v[140:143],a[12:15],a[32:35],0\ns_add_u32 m0,s51,49152\nv_mul_f32 v181,s20,v181\nC v52,v172,v173\nM v[144:147],a[16:19],a[32:35],0\n" \
"s_mov_b32 s46,384\nC v53,v174,v175\nC v70,v76,v77\nM v[148:151],a[20:23],a[32:35],0\nv_mul_f32 v182,s20,v182\nv_mul_f32 v183,s20,v183\nM v[152:155],a[24:27],a[32:35],0\nbuffer_load_dwordx4 v21,s[32:35],s47 offen offset:0 lds\n" \
"M v[156:159],a[28:31],a[32:35],0\ns_add_u32 m0,s51,50048\nv_mul_f32 v184,s20,v184\nv_mul_f32 v78,s20,v78\nM v[160:163],a[0:3],a[36:39],0\ns_waitcnt vmcnt(13) lgkmcnt(0)\nv_mul_f32 v185,s20,v185\nv_mul_f32 v79,s20,v79\n" \
"M v[164:167],a[4:7],a[36:39],0\ns_barrier\nC v72,v176,v177\nM v[168:171],a[8:11],a[36:39],0\nbuffer_store_dwordx2 v[58:59],v56,s[36:39],s23 offen sc1\nM v[172:175],a[12:15],a[36:39],0\nC v73,v178,v179\nv_mul_f32 v186,s20,v186\n" \
"M v[176:179],a[16:19],a[36:39],0\nC v74,v180,v181\nC v75,v182,v183\nv_mul_f32 v187,s20,v187\nM v[180:183],a[20:23],a[36:39],0\nC v76,v184,v185\nC v77,v186,v187\nM v[184:187],a[24:27],a[36:39],0\n" \
"C v71,v78,v79\nv_mul_f32 v188,s20,v188\nv_mul_f32 v189,s20,v189\nv_mul_f32 v190,s20,v190\nv_mul_f32 v191,s20,v191\nC v78,v188,v189\nC v79,v190,v191\nM v[188:191],a[28:31],a[36:39],0\n" \
"buffer_load_dwordx4 v21,s[32:35],s47 offen offset:128 lds\nM v[128:131],a[48:51],a[80:83],v[128:131]\ns_add_u32 m0,s51,57344\nds_read_b64_tr_b16 a[0:1],v9 offset:16384\nv_mul_f32 v80,s20,v80\nM v[160:163],a[48:51],a[84:87],v[160:163]\ns_mov_b32 s47,1835008\nds_read_b64_tr_b16 a[2:3],v10 offset:16384\n" \
"ds_read_b128 a[32:35],v7 offset:0\nM a[224:227],a[48:51],a[88:91],a[224:227]\nds_read_b128 a[36:39],v7 offset:4096\nv_mul_f32 v81,s20,v81\nM a[192:195],a[48:51],a[92:95],a[192:195]\nbuffer_load_dwordx4 v21,s[32:35],s47 offen offset:0 lds\nM a[196:199],a[52:55],a[92:95],a[196:199]\ns_add_u32 m0,s51,58240\n" \
"v_mul_f32 v112,s20,v112\nv_mul_f32 v82,s20,v82\nM a[228:231],a[52:55],a[88:91],a[228:231]\nds_read_b128 a[40:43],v7 offset:8192\nv_mul_f32 v113,s20,v113\nM v[164:167],a[52:55],a[84:87],v[164:167]\nds_read_b128 a[44:47],v7 offset:12288\nv_mul_f32 v83,s20,v83\n" \
"M v[132:135],a[52:55],a[80:83],v[132:135]\nbuffer_load_dwordx4 v21,s[32:35],s47 offen offset:128 lds\nM v[136:139],a[56:59],a[80:83],v[136:139]\ns_add_u32 m0,s50,32768\nds_read_b64_tr_b16 a[4:5],v11 offset:16384\nC v80,v80,v81\nM v[168:171],a[56:59],a[84:87],v[168:171]\ns_mov_b32 s47,2097152\n" \
"ds_read_b64_tr_b16 a[6:7],v12 offset:16384\nv_mul_f32 v114,s20,v114\nM a[232:235],a[56:59],a[88:91],a[232:235]\nds_read_b64_tr_b16 a[8:9],v9 offset:16896\nv_mul_f32 v115,s20,v115\nM a[200:203],a[56:59],a[92:95],a[200:203]\nbuffer_load_dwordx4 v13,s[28:31],s46 offen lds\nM a[204:207],a[60:63],a[92:95],a[204:207]\n" \
"s_add_u32 m0,s50,36864\nds_read_b64_tr_b16 a[10:11],v10 offset:16896\nC v112,v112,v113\nM a[236:239],a[60:63],a[88:91],a[236:239]\nds_read_b64_tr_b16 a[12:13],v11 offset:16896\nC v81,v82,v83\nM v[172:175],a[60:63],a[84:87],v[172:175]\nds_read_b64_tr_b16 a[14:15],v12 offset:16896\n" \
"ds_read_b64_tr_b16 a[16:17],v9 offset:17408\nM v[140:143],a[60:63],a[80:83],v[140:143]\nbuffer_load_dwordx4 v14,s[28:31],s46 offen lds\nM v[144:147],a[64:67],a[80:83],v[144:147]\ns_add_u32 m0,s50,40960\nv_mul_f32 v84,s20,v84\nv_mul_f32 v85,s20,v85\nM v[176:179],a[64:67],a[84:87],v[176:179]\n" \
"ds_read_b64_tr_b16 a[18:19],v10 offset:17408\nds_read_b64_tr_b16 a[20:21],v11 offset:17408\nM a[240:243],a[64:67],a[88:91],a[240:243]\nds_read_b64_tr_b16 a[22:23],v12 offset:17408\nC v113,v114,v115\nM a[208:211],a[64:67],a[92:95],a[208:211]\nbuffer_load_dwordx4 v15,s[28:31],s46 offen lds\nM a[212:215],a[68:71],a[92:95],a[212:215]\n" \
"s_add_u32 m0,s50,45056\nds_read_b64_tr_b16 a[24:25],v9 offset:17920\nv_mul_f32 v118,s20,v118\nM a[244:247],a[68:71],a[88:91],a[244:247]\nds_read_b64_tr_b16 a[26:27],v10 offset:17920\nv_mul_f32 v119,s20,v119\nM v[180:183],a[68:71],a[84:87],v[180:183]\nds_read_b64_tr_b16 a[28:29],v11 offset:17920\n" \
"v_mul_f32 v86,s20,v86\nM v[148:151],a[68:71],a[80:83],v[148:151]\nbuffer_load_dwordx4 v16,s[28:31],s46 offen lds\nM v[152:155],a[72:75],a[80:83],v[152:155]\ns_add_u32 m0,s50,49152\nds_read_b64_tr_b16 a[30:31],v12 offset:17920\nv_mul_f32 v87,s20,v87\nM v[184:187],a[72:75],a[84:87],v[184:187]\n" \
"s_mov_b32 s46,384\nds_read_b64_tr_b16 a[48:49],v9 offset:24576\nC v82,v84,v85\nM a[248:251],a[72:75],a[88:91],a[248:251]\nds_read_b64_tr_b16 a[50:51],v10 offset:24576\nds_read_b64_tr_b16 a[52:53],v11 offset:24576\nM a[216:219],a[72:75],a[92:95],a[216:219]\nbuffer_load_dwordx4 v17,s[28:31],s46 offen lds\n" \
"M a[220:223],a[76:79],a[92:95],a[220:223]\ns_add_u32 m0,s50,53248\nv_mul_f32 v88,s20,v88\nv_mul_f32 v89,s20,v89\nM a[252:255],a[76:79],a[88:91],a[252:255]\nds_read_b64_tr_b16 a[54:55],v12 offset:24576\nds_read_b64_tr_b16 a[56:57],v9 offset:25088\nM v[188:191],a[76:79],a[84:87],v[188:191]\n" \
"s_waitcnt lgkmcnt(5)\nds_read_b128 a[84:87],v8 offset:4096\nv_mul_f32 v90,s20,v90\nM v[156:159],a[76:79],a[80:83],v[156:159]\nbuffer_load_dwordx4 v18,s[28:31],s46 offen lds\nM v[128:131],a[0:3],a[32:35],v[128:131]\ns_add_u32 m0,s50,57344\nv_mul_f32 v91,s20,v91\n" \
"C v115,v118,v119\nM v[160:163],a[0:3],a[36:39],v[160:163]\nds_read_b128 a[80:83],v8 offset:0\nv_mul_f32 v124,s20,v124\nM a[224:227],a[0:3],a[40:43],a[224:227]\nds_read_b64_tr_b16 a[58:59],v10 offset:25088\nds_read_b64_tr_b16 a[60:61],v11 offset:25088\nM a[192:195],a[0:3],a[44:47],a[192:195]\n" \
"buffer_load_dwordx4 v19,s[28:31],s46 offen lds\nM a[196:199],a[4:7],a[44:47],a[196:199]\ns_add_u32 m0,s50,61440\nC v83,v86,v87\nv_mul_f32 v125,s20,v125\nM a[228:231],a[4:7],a[40:43],a[228:231]\nds_read_b64_tr_b16 a[62:63],v12 offset:25088\nds_read_b64_tr_b16 a[64:65],v9 offset:25600\n" \
"M v[164:167],a[4:7],a[36:39],v[164:167]\nds_read_b64_tr_b16 a[66:67],v10 offset:25600\nv_mul_f32 v92,s20,v92\nM v[132:135],a[4:7],a[32:35],v[132:135]\nbuffer_load_dwordx4 v20,s[28:31],s46 offen lds\nM v[136:139],a[8:11],a[32:35],v[136:139]\ns_add_u32 m0,s51,0\nds_read_b64_tr_b16 a[68:69],v11 offset:25600\n" \
"v_mul_f32 v93,s20,v93\nM v[168:171],a[8:11],a[36:39],v[168:171]\ns_mov_b32 s46,512\nds_read_b64_tr_b16 a[70:71],v12 offset:25600\nC v84,v88,v89\nM a[232:235],a[8:11],a[40:43],a[232:235]\nds_read_b64_tr_b16 a[72:73],v9 offset:26112\nC v85,v90,v91\n" \
"M a[200:203],a[8:11],a[44:47],a[200:203]\nbuffer_load_dwordx4 v21,s[32:35],s47 offen offset:0 lds\nM a[204:207],a[12:15],a[44:47],a[204:207]\ns_add_u32 m0,s51,896\nds_read_b64_tr_b16 a[74:75],v10 offset:26112\nv_mul_f32 v94,s20,v94\nM a[236:239],a[12:15],a[40:43],a[236:239]\nds_read_b64_tr_b16 a[76:77],v11 offset:26112\n" \
"v_mul_f32 v95,s20,v95\nM v[172:175],a[12:15],a[36:39],v[172:175]\nds_read_b64_tr_b16 a[78:79],v12 offset:26112\nds_read_b128 a[88:91],v8 offset:8192\nM v[140:143],a[12:15],a[32:35],v[140:143]\nbuffer_load_dwordx4 v21,s[32:35],s47 offen offset:128 lds\nM v[144:147],a[16:19],a[32:35],v[144:147]\ns_add_u32 m0,s51,8192\n" \
"C v118,v124,v125\nv_mul_f32 v96,s20,v96\nM v[176:179],a[16:19],a[36:39],v[176:179]\ns_mov_b32 s47,2359296\nds_read_b128 a[92:95],v8 offset:12288\nv_mul_f32 v97,s20,v97\nM a[240:243],a[16:19],a[40:43],a[240:243]\nC v86,v92,v93\n" \
"v_mov_b32 v88,v30\nM a[208:211],a[16:19],a[44:47],a[208:211]\nbuffer_load_dwordx4 v21,s[32:35],s47 offen offset:0 lds\nM a[212:215],a[20:23],a[44:47],a[212:215]\ns_add_u32 m0,s51,9088\nv_mov_b32 v89,v31\nv_mov_b32 v90,v32\nM a[244:247],a[20:23],a[40:43],a[244:247]\n" \
"v_mov_b32 v91,v33\nC v87,v94,v95\nM v[180:183],a[20:23],a[36:39],v[180:183]\nv_mov_b32 v124,v50\nv_mul_f32 v98,s20,v98\nM v[148:151],a[20:23],a[32:35],v[148:151]\nbuffer_store_dwordx4 v[64:67],v4,s[36:39],s96 offen offset:0 sc1\nM v[152:155],a[24:27],a[32:35],v[152:155]\n" \
"s_waitcnt vmcnt(17) lgkmcnt(0)\nv_mul_f32 v99,s20,v99\nC v96,v96,v97\nM v[184:187],a[24:27],a[36:39],v[184:187]\ns_barrier\nds_read_b64_tr_b16 a[0:1],v9 offset:32768\nM a[248:251],a[24:27],a[40:43],a[248:251]\nds_read_b64_tr_b16 a[2:3],v10 offset:32768\n" \
"ds_read_b64_tr_b16 a[4:5],v11 offset:32768\nM a[216:219],a[24:27],a[44:47],a[216:219]\nbuffer_load_dwordx4 v21,s[32:35],s47 offen offset:128 lds\nM a[220:223],a[28:31],a[44:47],a[220:223]\ns_add_u32 m0,s50,65536\nds_read_b128 a[44:47],v5 offset:12288\nv_mov_b32 v92,v34\nM a[252:255],a[28:31],a[40:43],a[252:255]\n" \
"s_mov_b32 s47,2621440\nds_read_b128 a[40:43],v5 offset:8192\nv_mov_b32 v93,v35\nM v[188:191],a[28:31],a[36:39],v[188:191]\nds_read_b128 a[36:39],v5 offset:4096\nv_mov_b32 v94,v36\nM v[156:159],a[28:31],a[32:35],v[156:159]\nbuffer_load_dwordx4 v13,s[28:31],s46 offen lds\n" \
"M v[128:131],a[48:51],a[80:83],v[128:131]\ns_add_u32 m0,s50,69632\nv_mov_b32 v95,v37\nC v97,v98,v99\nM v[160:163],a[48:51],a[84:87],v[160:163]\nds_read_b128 a[32:35],v5 offset:0\nv_mul_f32 v100,s20,v100\nM a[224:227],a[48:51],a[88:91],a[224:227]\n" \
"ds_read_b64_tr_b16 a[6:7],v12 offset:32768\nv_mul_f32 v101,s20,v101\nM a[192:195],a[48:51],a[92:95],a[192:195]\nbuffer_load_dwordx4 v14,s[28:31],s46 offen lds\nM a[196:199],a[52:55],a[92:95],a[196:199]\ns_add_u32 m0,s50,73728\nds_read_b64_tr_b16 a[8:9],v9 offset:33280\nv_mov_b32 v125,v51\n" \
"M a[228:231],a[52:55],a[88:91],a[228:231]\nds_read_b64_tr_b16 a[10:11],v10 offset:33280\nC v98,v100,v101\nM v[164:167],a[52:55],a[84:87],v[164:167]\nds_read_b64_tr_b16 a[12:13],v11 offset:33280\nds_read_b64_tr_b16 a[14:15],v12 offset:33280\nM v[132:135],a[52:55],a[80:83],v[132:135]\nbuffer_load_dwordx4 v15,s[28:31],s46 offen lds\n" \
"M v[136:139],a[56:59],a[80:83],v[136:139]\ns_add_u32 m0,s50,77824\nv_mul_f32 v104,s20,v104\nv_mul_f32 v105,s20,v105\nM v[168:171],a[56:59],a[84:87],v[168:171]\nds_read_b64_tr_b16 a[16:17],v9 offset:33792\nds_read_b64_tr_b16 a[18:19],v10 offset:33792\nM a[232:235],a[56:59],a[88:91],a[232:235]\n" \
"ds_read_b64_tr_b16 a[20:21],v11 offset:33792\nv_mul_f32 v106,s20,v106\nM a[200:203],a[56:59],a[92:95],a[200:203]\nbuffer_load_dwordx4 v16,s[28:31],s46 offen lds\nM a[204:207],a[60:63],a[92:95],a[204:207]\ns_mov_b32 s46,512\nds_read_b64_tr_b16 a[22:23],v12 offset:33792\nv_mul_f32 v107,s20,v107\n" \
"M a[236:239],a[60:63],a[88:91],a[236:239]\ns_add_u32 m0,s50,81920\nds_read_b64_tr_b16 a[24:25],v9 offset:34304\nC v100,v104,v105\nM v[172:175],a[60:63],a[84:87],v[172:175]\nds_read_b64_tr_b16 a[26:27],v10 offset:34304\nC v101,v106,v107\nM v[140:143],a[60:63],a[80:83],v[140:143]\n" \
"buffer_load_dwordx4 v17,s[28:31],s46 offen lds\nM v[144:147],a[64:67],a[80:83],v[144:147]\ns_add_u32 m0,s50,86016\nds_read_b64_tr_b16 a[28:29],v11 offset:34304\nv_mov_b32 v104,v38\nM v[176:179],a[64:67],a[84:87],v[176:179]\nds_read_b64_tr_b16 a[30:31],v12 offset:34304\nv_mov_b32 v105,v39\n" \
"M a[240:243],a[64:67],a[88:91],a[240:243]\nds_read_b64_tr_b16 a[48:49],v9 offset:40960\nds_read_b64_tr_b16 a[50:51],v10 offset:40960\nM a[208:211],a[64:67],a[92:95],a[208:211]\nbuffer_load_dwordx4 v18,s[28:31],s46 offen lds\nM a[212:215],a[68:71],a[92:95],a[212:215]\ns_add_u32 m0,s50,90112\nv_mov_b32 v106,v40\n" \
"v_mov_b32 v107,v41\nM a[244:247],a[68:71],a[88:91],a[244:247]\nds_read_b64_tr_b16 a[52:53],v11 offset:40960\nds_read_b64_tr_b16 a[54:55],v12 offset:40960\nM v[180:183],a[68:71],a[84:87],v[180:183]\nds_read_b64_tr_b16 a[56:57],v9 offset:41472\nv_mul_f32 v126,s20,v126\nM v[148:151],a[68:71],a[80:83],v[148:151]\n" \
"buffer_load_dwordx4 v19,s[28:31],s46 offen lds\nM v[152:155],a[72:75],a[80:83],v[152:155]\ns_add_u32 m0,s50,94208\nds_read_b64_tr_b16 a[58:59],v10 offset:41472\nv_mul_f32 v127,s20,v127\nM v[184:187],a[72:75],a[84:87],v[184:187]\nds_read_b64_tr_b16 a[60:61],v11 offset:41472\nC v119,v126,v127\n" \
"M a[248:251],a[72:75],a[88:91],a[248:251]\nds_read_b64_tr_b16 a[62:63],v12 offset:41472\nv_mul_f32 v102,s20,v102\nM a[216:219],a[72:75],a[92:95],a[216:219]\nbuffer_load_dwordx4 v20,s[28:31],s46 offen lds\nM a[220:223],a[76:79],a[92:95],a[220:223]\ns_add_u32 m0,s51,16384\nds_read_b64_tr_b16 a[64:65],v9 offset:41984\n" \
"v_mul_f32 v103,s20,v103\nM a[252:255],a[76:79],a[88:91],a[252:255]\ns_mov_b32 s46,640\nds_read_b64_tr_b16 a[66:67],v10 offset:41984\nv_mov_b32 v126,v52\nM v[188:191],a[76:79],a[84:87],v[188:191]\ns_waitcnt lgkmcnt(4)\nds_read_b128 a[84:87],v6 offset:4096\n" \
"C v99,v102,v103\nM v[156:159],a[76:79],a[80:83],v[156:159]\nbuffer_load_dwordx4 v21,s[32:35],s47 offen offset:0 lds\nM v[128:131],a[0:3],a[32:35],v[128:131]\ns_add_u32 m0,s51,17280\nds_read_b128 a[80:83],v6 offset:0\nv_mul_f32 v108,s20,v108\nM v[160:163],a[0:3],a[36:39],v[160:163]\n" \
"ds_read_b64_tr_b16 a[68:69],v11 offset:41984\nv_mul_f32 v109,s20,v109\nM a[224:227],a[0:3],a[40:43],a[224:227]\nds_read_b64_tr_b16 a[70:71],v12 offset:41984\nv_mul_f32 v110,s20,v110\nM a[192:195],a[0:3],a[44:47],a[192:195]\nbuffer_load_dwordx4 v21,s[32:35],s47 offen offset:128 lds\nM a[196:199],a[4:7],a[44:47],a[196:199]\n" \
"s_add_u32 m0,s51,24576\nds_read_b64_tr_b16 a[72:73],v9 offset:42496\nv_mul_f32 v111,s20,v111\nM a[228:231],a[4:7],a[40:43],a[228:231]\ns_mov_b32 s47,2883584\nds_read_b64_tr_b16 a[74:75],v10 offset:42496\nC v102,v108,v109\nM v[164:167],a[4:7],a[36:39],v[164:167]\n" \
"ds_read_b64_tr_b16 a[76:77],v11 offset:42496\nds_read_b64_tr_b16 a[78:79],v12 offset:42496\nM v[132:135],a[4:7],a[32:35],v[132:135]\nbuffer_load_dwordx4 v21,s[32:35],s47 offen offset:0 lds\nM v[136:139],a[8:11],a[32:35],v[136:139]\ns_add_u32 m0,s51,25472\nC v103,v110,v111\nv_mov_b32 v108,v42\n" \
"M v[168:171],a[8:11],a[36:39],v[168:171]\nds_read_b128 a[88:91],v6 offset:8192\nv_mov_b32 v109,v43\nM a[232:235],a[8:11],a[40:43],a[232:235]\nds_read_b128 a[92:95],v6 offset:12288\nv_mov_b32 v110,v44\nM a[200:203],a[8:11],a[44:47],a[200:203]\nbuffer_load_dwordx4 v21,s[32:35],s47 offen offset:128 lds\n" \
"M a[204:207],a[12:15],a[44:47],a[204:207]\ns_add_u32 m0,s50,0\nv_mov_b32 v111,v45\nv_mul_f32 v116,s20,v116\nM a[236:239],a[12:15],a[40:43],a[236:239]\ns_mov_b32 s47,3145728\nv_mul_f32 v117,s20,v117\nC v114,v116,v117\n" \
"M v[172:175],a[12:15],a[36:39],v[172:175]\nv_mov_b32 v127,v53\nv_mul_f32 v120,s20,v120\nM v[140:143],a[12:15],a[32:35],v[140:143]\nbuffer_load_dwordx4 v[30:33],v23,s[68:71],0 offen offset:0\nM v[144:147],a[16:19],a[32:35],v[144:147]\nv_mul_f32 v121,s20,v121\nv_mul_f32 v122,s20,v122\n" \
"M v[176:179],a[16:19],a[36:39],v[176:179]\nv_mul_f32 v123,s20,v123\nC v116,v120,v121\nM a[240:243],a[16:19],a[40:43],a[240:243]\nC v117,v122,v123\nv_mov_b32 v120,v46\nM a[208:211],a[16:19],a[44:47],a[208:211]\nv_mov_b32 v121,v47\n" \
"v_mov_b32 v122,v48\nM a[212:215],a[20:23],a[44:47],a[212:215]\nv_mov_b32 v123,v49\nv_mul_f32 v192,s20,v192\nM a[244:247],a[20:23],a[40:43],a[244:247]\nbuffer_load_dword v50,v25,s[68:71],0 offen offset:0\nM v[180:183],a[20:23],a[36:39],v[180:183]\nv_mul_f32 v193,s20,v193\n" \
"v_mul_f32 v194,s20,v194\nM v[148:151],a[20:23],a[32:35],v[148:151]\nv_mul_f32 v195,s20,v195\nC v192,v192,v193\nM v[152:155],a[24:27],a[32:35],v[152:155]\ns_waitcnt vmcnt(19) lgkmcnt(0)\nC v193,v194,v195\nv_mul_f32 v196,s20,v196\n" \
"M v[184:187],a[24:27],a[36:39],v[184:187]\ns_barrier\nds_read_b64_tr_b16 a[0:1],v9 offset:49152\nM a[248:251],a[24:27],a[40:43],a[248:251]\nds_read_b64_tr_b16 a[2:3],v10 offset:49152\nds_read_b64_tr_b16 a[4:5],v11 offset:49152\nM a[216:219],a[24:27],a[44:47],a[216:219]\nbuffer_load_dwordx4 v[34:37],v24,s[68:71],0 offen offset:0\n" \
"M a[220:223],a[28:31],a[44:47],a[220:223]\nds_read_b128 a[44:47],v5 offset:45056\nds_read_b64_tr_b16 a[6:7],v12 offset:49152\nM a[252:255],a[28:31],a[40:43],a[252:255]\nds_read_b128 a[40:43],v5 offset:40960\nv_mul_f32 v197,s20,v197\nM v[188:191],a[28:31],a[36:39],v[188:191]\nds_read_b128 a[36:39],v5 offset:36864\n" \
"v_mul_f32 v198,s20,v198\nM v[156:159],a[28:31],a[32:35],v[156:159]\nds_read_b128 a[32:35],v5 offset:32768\nv_mul_f32 v199,s20,v199\nM v[128:131],a[48:51],a[80:83],v[128:131]\nds_read_b64_tr_b16 a[8:9],v9 offset:49664\nC v194,v196,v197\nM v[160:163],a[48:51],a[84:87],v[160:163]\n" \
"buffer_load_dwordx2 v[58:59],v57,s[36:39],0 offen sc1\nM a[224:227],a[48:51],a[88:91],a[224:227]\nds_read_b64_tr_b16 a[10:11],v10 offset:49664\nds_read_b64_tr_b16 a[12:13],v11 offset:49664\nM a[192:195],a[48:51],a[92:95],a[192:195]\nbuffer_store_dwordx4 v[68:71],v4,s[36:39],s96 offen offset:1024 sc1\nM a[196:199],a[52:55],a[92:95],a[196:199]\nds_read_b64_tr_b16 a[14:15],v12 offset:49664\n" \
"ds_read_b64_tr_b16 a[16:17],v9 offset:50176\nM a[228:231],a[52:55],a[88:91],a[228:231]\nds_read_b64_tr_b16 a[18:19],v10 offset:50176\nv_mul_f32 v200,s20,v200\nM v[164:167],a[52:55],a[84:87],v[164:167]\nds_read_b64_tr_b16 a[20:21],v11 offset:50176\nds_read_b64_tr_b16 a[22:23],v12 offset:50176\nM v[132:135],a[52:55],a[80:83],v[132:135]\n" \
"buffer_load_dword v51,v24,s[68:71],0 offen offset:16\nM v[136:139],a[56:59],a[80:83],v[136:139]\nds_read_b64_tr_b16 a[24:25],v9 offset:50688\nds_read_b64_tr_b16 a[26:27],v10 offset:50688\nM v[168:171],a[56:59],a[84:87],v[168:171]\nds_read_b64_tr_b16 a[28:29],v11 offset:50688\nv_mul_f32 v201,s20,v201\nM a[232:235],a[56:59],a[88:91],a[232:235]\n" \
"ds_read_b64_tr_b16 a[30:31],v12 offset:50688\nds_read_b64_tr_b16 a[48:49],v9 offset:57344\nM a[200:203],a[56:59],a[92:95],a[200:203]\nds_read_b64_tr_b16 a[50:51],v10 offset:57344\nv_mul_f32 v202,s20,v202\nM a[204:207],a[60:63],a[92:95],a[204:207]\nds_read_b64_tr_b16 a[52:53],v11 offset:57344\nv_mul_f32 v203,s20,v203\n" \
"M a[236:239],a[60:63],a[88:91],a[236:239]\nbuffer_load_dwordx4 v[38:41],v24,s[68:71],0 offen offset:1024\nM v[172:175],a[60:63],a[84:87],v[172:175]\nds_read_b64_tr_b16 a[54:55],v12 offset:57344\nds_read_b64_tr_b16 a[56:57],v9 offset:57856\nM v[140:143],a[60:63],a[80:83],v[140:143]\nds_read_b64_tr_b16 a[58:59],v10 offset:57856\nC v195,v198,v199\n" \
"M v[144:147],a[64:67],a[80:83],v[144:147]\ns_nop 0\nds_read_b64_tr_b16 a[60:61],v11 offset:57856\nds_read_b64_tr_b16 a[62:63],v12 offset:57856\nM v[176:179],a[64:67],a[84:87],v[176:179]\nv_mul_f32 v204,s20,v204\nv_mul_f32 v205,s20,v205\nM a[240:243],a[64:67],a[88:91],a[240:243]\n" \
"C v196,v200,v201\nv_mul_f32 v208,s20,v208\nM a[208:211],a[64:67],a[92:95],a[208:211]\nbuffer_load_dwordx4 v13,s[28:31],s46 offen lds\nM a[212:215],a[68:71],a[92:95],a[212:215]\ns_add_u32 m0,s50,4096\nds_read_b64_tr_b16 a[64:65],v9 offset:58368\nv_mul_f32 v209,s20,v209\n" \
"M a[244:247],a[68:71],a[88:91],a[244:247]\nds_read_b64_tr_b16 a[66:67],v10 offset:58368\nv_mul_f32 v210,s20,v210\nM v[180:183],a[68:71],a[84:87],v[180:183]\nC v197,v202,v203\nv_mul_f32 v211,s20,v211\nM v[148:151],a[68:71],a[80:83],v[148:151]\nbuffer_load_dwordx4 v14,s[28:31],s46 offen lds\n" \
"M v[152:155],a[72:75],a[80:83],v[152:155]\ns_add_u32 m0,s50,8192\nds_read_b64_tr_b16 a[68:69],v11 offset:58368\nv_mul_f32 v212,s20,v212\nM v[184:187],a[72:75],a[84:87],v[184:187]\nds_read_b64_tr_b16 a[70:71],v12 offset:58368\nv_mul_f32 v213,s20,v213\nM a[248:251],a[72:75],a[88:91],a[248:251]\n" \
"v_mul_f32 v214,s20,v214\nv_mul_f32 v215,s20,v215\nM a[216:219],a[72:75],a[92:95],a[216:219]\nbuffer_load_dwordx4 v15,s[28:31],s46 offen lds\nM a[220:223],a[76:79],a[92:95],a[220:223]\ns_add_u32 m0,s50,12288\nds_read_b64_tr_b16 a[72:73],v9 offset:58880\nv_mul_f32 v206,s20,v206\n" \
"M a[252:255],a[76:79],a[88:91],a[252:255]\nds_read_b64_tr_b16 a[74:75],v10 offset:58880\nds_read_b128 a[88:91],v6 offset:40960\nM v[188:191],a[76:79],a[84:87],v[188:191]\ns_waitcnt lgkmcnt(4)\nds_read_b128 a[84:87],v6 offset:36864\nv_mul_f32 v207,s20,v207\nM v[156:159],a[76:79],a[80:83],v[156:159]\n" \
"buffer_load_dwordx4 v16,s[28:31],s46 offen lds\nM v[128:131],a[0:3],a[32:35],v[128:131]\ns_add_u32 m0,s50,16384\nC v198,v204,v205\nv_mul_f32 v216,s20,v216\nM v[160:163],a[0:3],a[36:39],v[160:163]\ns_mov_b32 s46,640\nds_read_b128 a[80:83],v6 offset:32768\n" \
"v_mul_f32 v217,s20,v217\nM a[224:227],a[0:3],a[40:43],a[224:227]\nds_read_b64_tr_b16 a[76:77],v11 offset:58880\nv_mul_f32 v218,s20,v218\nM a[192:195],a[0:3],a[44:47],a[192:195]\nbuffer_load_dword v52,v24,s[68:71],0 offen offset:1040\nM a[196:199],a[4:7],a[44:47],a[196:199]\nds_read_b64_tr_b16 a[78:79],v12 offset:58880\n" \
"ds_read_b128 a[92:95],v6 offset:45056\nM a[228:231],a[4:7],a[40:43],a[228:231]\nv_mul_f32 v219,s20,v219\nC v200,v208,v209\nM v[164:167],a[4:7],a[36:39],v[164:167]\nv_mul_f32 v224,s20,v224\nv_mul_f32 v225,s20,v225\nM v[132:135],a[4:7],a[32:35],v[132:135]\n" \
"v_mul_f32 v226,s20,v226\nv_mul_f32 v227,s20,v227\nM v[136:139],a[8:11],a[32:35],v[136:139]\nC v201,v210,v211\nv_mul_f32 v228,s20,v228\nM v[168:171],a[8:11],a[36:39],v[168:171]\nbuffer_load_dwordx4 v17,s[28:31],s46 offen lds\nM a[232:235],a[8:11],a[40:43],a[232:235]\n" \
"s_add_u32 m0,s50,20480\nv_mul_f32 v229,s20,v229\nv_mul_f32 v230,s20,v230\nM a[200:203],a[8:11],a[44:47],a[200:203]\nv_mul_f32 v231,s20,v231\nC v202,v212,v213\nM a[204:207],a[12:15],a[44:47],a[204:207]\nv_mul_f32 v232,s20,v232\n" \
"v_mul_f32 v233,s20,v233\nM a[236:239],a[12:15],a[40:43],a[236:239]\nbuffer_load_dwordx4 v[42:45],v24,s[68:71],0 offen offset:2048\nM v[172:175],a[12:15],a[36:39],v[172:175]\nv_mul_f32 v234,s20,v234\nv_mul_f32 v235,s20,v235\nM v[140:143],a[12:15],a[32:35],v[140:143]\nC v203,v214,v215\n" \
"v_mul_f32 v236,s20,v236\nM v[144:147],a[16:19],a[32:35],v[144:147]\nv_mul_f32 v237,s20,v237\nv_mul_f32 v238,s20,v238\nM v[176:179],a[16:19],a[36:39],v[176:179]\nv_mul_f32 v239,s20,v239\nC v199,v206,v207\nM a[240:243],a[16:19],a[40:43],a[240:243]\n" \
"C v204,v216,v217\nv_mul_f32 v220,s20,v220\nM a[208:211],a[16:19],a[44:47],a[208:211]\ns_waitcnt vmcnt(10)\nbuffer_store_dwordx4 v[80:83],v4,s[36:39],s96 offen offset:2048 sc1\nM a[212:215],a[20:23],a[44:47],a[212:215]\nv_cmp_gt_u64 s[98:99],s[14:15],v[58:59]\nv_cmp_le_u64 s[18:19],s[16:17],v[58:59]\n" \
"M a[244:247],a[20:23],a[40:43],a[244:247]\ns_or_b64 s[98:99],s[98:99],s[18:19]\ns_cbranch_scc1 LPU_%=\nLPUR_%=:\nv_mul_f32 v221,s20,v221\nv_mul_f32 v240,s20,v240\nM v[180:183],a[20:23],a[36:39],v[180:183]\nv_mul_f32 v241,s20,v241\n" \
"v_mul_f32 v242,s20,v242\nM v[148:151],a[20:23],a[32:35],v[148:151]\nbuffer_load_dword v53,v24,s[68:71],0 offen offset:2064\nM v[152:155],a[24:27],a[32:35],v[152:155]\ns_waitcnt vmcnt(20) lgkmcnt(0)\nv_mul_f32 v243,s20,v243\nv_mul_f32 v222,s20,v222\nM v[184:187],a[24:27],a[36:39],v[184:187]\n" \
"s_barrier\nds_read_b64_tr_b16 a[0:1],v9 offset:0\nM a[248:251],a[24:27],a[40:43],a[248:251]\nds_read_b64_tr_b16 a[2:3],v10 offset:0\nds_read_b64_tr_b16 a[4:5],v11 offset:0\nM a[216:219],a[24:27],a[44:47],a[216:219]\nds_read_b64_tr_b16 a[6:7],v12 offset:0\nv_mul_f32 v223,s20,v223\n" \
"M a[220:223],a[28:31],a[44:47],a[220:223]\nds_read_b128 a[44:47],v7 offset:12288\nC v205,v218,v219\nM a[252:255],a[28:31],a[40:43],a[252:255]\nbuffer_load_dwordx4 v[46:49],v24,s[68:71],0 offen offset:3072\nM v[188:191],a[28:31],a[36:39],v[188:191]\nds_read_b128 a[36:39],v7 offset:4096\nds_read_b128 a[40:43],v7 offset:8192\n" \
"M v[156:159],a[28:31],a[32:35],v[156:159]\nds_read_b128 a[32:35],v7 offset:0\nC v208,v224,v225\nM v[128:131],a[48:51],a[80:83],v[128:131]\nds_read_b64_tr_b16 a[8:9],v9 offset:512\nC v209,v226,v227\nM v[160:163],a[48:51],a[84:87],v[160:163]\nds_read_b64_tr_b16 a[10:11],v10 offset:512\n" \
"v_mul_f32 v244,s20,v244\nM a[224:227],a[48:51],a[88:91],a[224:227]\nds_read_b64_tr_b16 a[12:13],v11 offset:512\nv_mul_f32 v245,s20,v245\nM a[192:195],a[48:51],a[92:95],a[192:195]\nbuffer_load_dword v54,v24,s[68:71],0 offen offset:3088\nM a[196:199],a[52:55],a[92:95],a[196:199]\nds_read_b64_tr_b16 a[14:15],v12 offset:512\n" \
"ds_read_b64_tr_b16 a[16:17],v9 offset:1024\nM a[228:231],a[52:55],a[88:91],a[228:231]\nds_read_b64_tr_b16 a[18:19],v10 offset:1024\nv_mul_f32 v246,s20,v246\nM v[164:167],a[52:55],a[84:87],v[164:167]\nds_read_b64_tr_b16 a[20:21],v11 offset:1024\nds_read_b64_tr_b16 a[22:23],v12 offset:1024\nM v[132:135],a[52:55],a[80:83],v[132:135]\n" \
"ds_read_b64_tr_b16 a[24:25],v9 offset:1536\nv_mul_f32 v247,s20,v247\nM v[136:139],a[56:59],a[80:83],v[136:139]\nds_read_b64_tr_b16 a[26:27],v10 offset:1536\nC v210,v228,v229\nM v[168:171],a[56:59],a[84:87],v[168:171]\nglobal_load_short_d16_hi v28,v29,s[12:13]\nM a[232:235],a[56:59],a[88:91],a[232:235]\n" \
"ds_read_b64_tr_b16 a[28:29],v11 offset:1536\nds_read_b64_tr_b16 a[30:31],v12 offset:1536\nM a[200:203],a[56:59],a[92:95],a[200:203]\nds_read_b64_tr_b16 a[48:49],v9 offset:8192\nC v211,v230,v231\nM a[204:207],a[60:63],a[92:95],a[204:207]\nds_read_b64_tr_b16 a[50:51],v10 offset:8192\nds_read_b64_tr_b16 a[52:53],v11 offset:8192\n" \
"M a[236:239],a[60:63],a[88:91],a[236:239]\nds_read_b64_tr_b16 a[54:55],v12 offset:8192\nC v212,v232,v233\nM v[172:175],a[60:63],a[84:87],v[172:175]\nds_read_b64_tr_b16 a[56:57],v9 offset:8704\nC v213,v234,v235\nM v[140:143],a[60:63],a[80:83],v[140:143]\nbuffer_load_dwordx4 v18,s[28:31],s46 offen lds\n" \
"M v[144:147],a[64:67],a[80:83],v[144:147]\ns_add_u32 m0,s50,24576\nds_read_b64_tr_b16 a[58:59],v10 offset:8704\nC v214,v236,v237\nM v[176:179],a[64:67],a[84:87],v[176:179]\nds_read_b64_tr_b16 a[60:61],v11 offset:8704\nC v215,v238,v239\nM a[240:243],a[64:67],a[88:91],a[240:243]\n" \
"ds_read_b64_tr_b16 a[62:63],v12 offset:8704\nC v206,v220,v221\nM a[208:211],a[64:67],a[92:95],a[208:211]\nbuffer_load_dwordx4 v19,s[28:31],s46 offen lds\nM a[212:215],a[68:71],a[92:95],a[212:215]\ns_add_u32 m0,s50,28672\nds_read_b64_tr_b16 a[64:65],v9 offset:9216\nC v216,v240,v241\n" \
"M a[244:247],a[68:71],a[88:91],a[244:247]\nds_read_b64_tr_b16 a[66:67],v10 offset:9216\nC v217,v242,v243\nM v[180:183],a[68:71],a[84:87],v[180:183]\nv_mul_f32 v248,s20,v248\nv_mul_f32 v249,s20,v249\nM v[148:151],a[68:71],a[80:83],v[148:151]\nbuffer_load_dwordx4 v20,s[28:31],s46 offen lds\n" \
"M v[152:155],a[72:75],a[80:83],v[152:155]\ns_add_u32 m0,s56,0\nds_read_b64_tr_b16 a[68:69],v11 offset:9216\nv_mul_f32 v250,s20,v250\nM v[184:187],a[72:75],a[84:87],v[184:187]\ns_mov_b32 s46,768\nds_read_b64_tr_b16 a[70:71],v12 offset:9216\nC v207,v222,v223\n" \
"M a[248:251],a[72:75],a[88:91],a[248:251]\nv_mul_f32 v251,s20,v251\nC v218,v244,v245\nM a[216:219],a[72:75],a[92:95],a[216:219]\nbuffer_load_dwordx4 v23,s[68:71],0 offen offset:0 lds\nM a[220:223],a[76:79],a[92:95],a[220:223]\ns_add_u32 m0,s56,1024\nds_read_b64_tr_b16 a[72:73],v9 offset:9728\n" \
"C v219,v246,v247\nM a[252:255],a[76:79],a[88:91],a[252:255]\nds_read_b64_tr_b16 a[74:75],v10 offset:9728\nds_read_b128 a[88:91],v8 offset:8192\nM v[188:191],a[76:79],a[84:87],v[188:191]\ns_waitcnt lgkmcnt(4)\nds_read_b128 a[84:87],v8 offset:4096\nv_mul_f32 v252,s20,v252\n" \
"M v[156:159],a[76:79],a[80:83],v[156:159]\nbuffer_load_dwordx4 v24,s[68:71],0 offen offset:0 lds\nM v[128:131],a[0:3],a[32:35],v[128:131]\ns_add_u32 m0,s56,1024\nv_mul_f32 v253,s20,v253\nv_mul_f32 v254,s20,v254\nM v[160:163],a[0:3],a[36:39],v[160:163]\nds_read_b128 a[80:83],v8 offset:0\n" \
"v_mul_f32 v255,s20,v255\nM a[224:227],a[0:3],a[40:43],a[224:227]\nds_read_b64_tr_b16 a[76:77],v11 offset:9728\nC v220,v248,v249\nM a[192:195],a[0:3],a[44:47],a[192:195]\nbuffer_load_dwordx4 v24,s[68:71],0 offen offset:1024 lds\nM a[196:199],a[4:7],a[44:47],a[196:199]\ns_add_u32 m0,s56,1024\n" \
"ds_read_b64_tr_b16 a[78:79],v12 offset:9728\nC v221,v250,v251\nM a[228:231],a[4:7],a[40:43],a[228:231]\nds_read_b128 a[92:95],v8 offset:12288\nC v222,v252,v253\nM v[164:167],a[4:7],a[36:39],v[164:167]\nC v223,v254,v255\nM v[132:135],a[4:7],a[32:35],v[132:135]\n" \
"buffer_load_dwordx4 v24,s[68:71],0 offen offset:2048 lds\nM v[136:139],a[8:11],a[32:35],v[136:139]\ns_add_u32 m0,s56,1024\nM v[168:171],a[8:11],a[36:39],v[168:171]\nM a[232:235],a[8:11],a[40:43],a[232:235]\nM a[200:203],a[8:11],a[44:47],a[200:203]\nbuffer_load_dwordx4 v24,s[68:71],0 offen offset:3072 lds\nM a[204:207],a[12:15],a[44:47],a[204:207]\n" \
"s_add_u32 m0,s56,10304\nM a[236:239],a[12:15],a[40:43],a[236:239]\nM v[172:175],a[12:15],a[36:39],v[172:175]\nM v[140:143],a[12:15],a[32:35],v[140:143]\nM v[144:147],a[16:19],a[32:35],v[144:147]\nbuffer_store_dwordx4 v[84:87],v4,s[36:39],s96 offen offset:3072 sc1\nM v[176:179],a[16:19],a[36:39],v[176:179]\ns_add_u32 s96,s57,36864\n" \
"M a[240:243],a[16:19],a[40:43],a[240:243]\nM a[208:211],a[16:19],a[44:47],a[208:211]\nM a[212:215],a[20:23],a[44:47],a[212:215]\ns_mov_b64 exec,0xff\nglobal_load_dword v0,v2,s[100:101]\ns_mov_b64 exec,-1\nM a[244:247],a[20:23],a[40:43],a[244:247]\nM v[180:183],a[20:23],a[36:39],v[180:183]\n" \
"buffer_load_dwordx4 v26,s[72:75],0 offen offset:0 sc1 lds\nM v[148:151],a[20:23],a[32:35],v[148:151]\ns_add_u32 m0,s56,11328\nM v[152:155],a[24:27],a[32:35],v[152:155]\ns_waitcnt vmcnt(8) lgkmcnt(0)\nM v[184:187],a[24:27],a[36:39],v[184:187]\ns_barrier\nds_read_b64_tr_b16 a[0:1],v9 offset:16384\n" \
"M a[248:251],a[24:27],a[40:43],a[248:251]\nbuffer_load_dwordx4 v27,s[72:75],0 offen offset:0 sc1 lds\nM a[216:219],a[24:27],a[44:47],a[216:219]\ns_add_u32 m0,s56,11328\nds_read_b64_tr_b16 a[2:3],v10 offset:16384\nM a[220:223],a[28:31],a[44:47],a[220:223]\nds_read_b64_tr_b16 a[4:5],v11 offset:16384\nds_read_b128 a[44:47],v5 offset:12288\n" \
"M a[252:255],a[28:31],a[40:43],a[252:255]\nds_read_b128 a[40:43],v5 offset:8192\nM v[188:191],a[28:31],a[36:39],v[188:191]\nbuffer_load_dwordx4 v27,s[72:75],0 offen offset:1024 sc1 lds\nM v[156:159],a[28:31],a[32:35],v[156:159]\ns_add_u32 m0,s56,11328\nM v[128:131],a[48:51],a[80:83],v[128:131]\nds_read_b128 a[32:35],v5 offset:0\n" \
"M v[160:163],a[48:51],a[84:87],v[160:163]\nds_read_b128 a[36:39],v5 offset:4096\nM a[224:227],a[48:51],a[88:91],a[224:227]\nds_read_b64_tr_b16 a[6:7],v12 offset:16384\nM a[192:195],a[48:51],a[92:95],a[192:195]\nbuffer_load_dwordx4 v27,s[72:75],0 offen offset:2048 sc1 lds\nM a[196:199],a[52:55],a[92:95],a[196:199]\ns_add_u32 m0,s56,11328\n" \
"ds_read_b64_tr_b16 a[8:9],v9 offset:16896\nM a[228:231],a[52:55],a[88:91],a[228:231]\nds_read_b64_tr_b16 a[10:11],v10 offset:16896\nM v[164:167],a[52:55],a[84:87],v[164:167]\nds_read_b64_tr_b16 a[12:13],v11 offset:16896\nds_read_b64_tr_b16 a[14:15],v12 offset:16896\nM v[132:135],a[52:55],a[80:83],v[132:135]\nbuffer_load_dwordx4 v27,s[72:75],0 offen offset:3072 sc1 lds\n" \
"M v[136:139],a[56:59],a[80:83],v[136:139]\ns_add_u32 m0,s51,0\nM v[168:171],a[56:59],a[84:87],v[168:171]\ns_mov_b32 s74,0\nds_read_b64_tr_b16 a[16:17],v9 offset:17408\nds_read_b64_tr_b16 a[18:19],v10 offset:17408\nM a[232:235],a[56:59],a[88:91],a[232:235]\nds_read_b64_tr_b16 a[20:21],v11 offset:17408\n" \
"M a[200:203],a[56:59],a[92:95],a[200:203]\nds_read_b64_tr_b16 a[22:23],v12 offset:17408\nM a[204:207],a[60:63],a[92:95],a[204:207]\nbuffer_load_dwordx4 v21,s[32:35],s47 offen offset:0 lds\nM a[236:239],a[60:63],a[88:91],a[236:239]\ns_add_u32 m0,s51,896\nds_read_b64_tr_b16 a[24:25],v9 offset:17920\nM v[172:175],a[60:63],a[84:87],v[172:175]\n" \
"ds_read_b64_tr_b16 a[26:27],v10 offset:17920\nM v[140:143],a[60:63],a[80:83],v[140:143]\nds_read_b64_tr_b16 a[28:29],v11 offset:17920\nds_read_b64_tr_b16 a[30:31],v12 offset:17920\nM v[144:147],a[64:67],a[80:83],v[144:147]\nbuffer_load_dwordx4 v21,s[32:35],s47 offen offset:128 lds\nM v[176:179],a[64:67],a[84:87],v[176:179]\ns_add_u32 m0,s51,8192\n" \
"M a[240:243],a[64:67],a[88:91],a[240:243]\ns_mov_b32 s47,3407872\nds_read_b64_tr_b16 a[48:49],v9 offset:24576\nds_read_b64_tr_b16 a[50:51],v10 offset:24576\nM a[208:211],a[64:67],a[92:95],a[208:211]\nds_read_b64_tr_b16 a[52:53],v11 offset:24576\nM a[212:215],a[68:71],a[92:95],a[212:215]\nds_read_b64_tr_b16 a[54:55],v12 offset:24576\n" \
"M a[244:247],a[68:71],a[88:91],a[244:247]\ns_waitcnt vmcnt(17)\nv_alignbyte_b32 v30,v31,v30,2\nv_alignbyte_b32 v31,v32,v31,2\nM v[180:183],a[68:71],a[84:87],v[180:183]\nv_alignbyte_b32 v32,v33,v32,2\nv_alignbyte_b32 v33,v50,v33,2\nM v[148:151],a[68:71],a[80:83],v[148:151]\n" \
"ds_write_b128 v55,v[30:33] offset:5184\nM v[152:155],a[72:75],a[80:83],v[152:155]\nv_alignbyte_b32 v34,v35,v34,2\nv_alignbyte_b32 v35,v36,v35,2\nM v[184:187],a[72:75],a[84:87],v[184:187]\nv_alignbyte_b32 v36,v37,v36,2\nv_alignbyte_b32 v37,v51,v37,2\nM a[248:251],a[72:75],a[88:91],a[248:251]\n" \
"v_lshrrev_b32 v30,4,v4\nv_alignbyte_b32 v46,v47,v46,2\nM a[216:219],a[72:75],a[92:95],a[216:219]\nds_write_b128 v55,v[34:37] offset:6208\nM a[220:223],a[76:79],a[92:95],a[220:223]\nv_and_b32 v31,15,v30\nv_alignbyte_b32 v47,v48,v47,2\nM a[252:255],a[76:79],a[88:91],a[252:255]\n" \
"v_add_u32 v33,1,v31\nv_alignbyte_b32 v48,v49,v48,2\nM v[188:191],a[76:79],a[84:87],v[188:191]\nv_and_b32 v34,1,v33\nv_alignbyte_b32 v49,v54,v49,2\nM v[156:159],a[76:79],a[80:83],v[156:159]\ns_waitcnt lgkmcnt(1)\nv_lshrrev_b32 v32,4,v30\n" \
"v_and_b32 v33,30,v33\nM v[128:131],a[0:3],a[32:35],v[128:131]\nv_mul_u32_u24 v34,5184,v34\nv_lshl_add_u32 v33,v33,1,v34\nM v[160:163],a[0:3],a[36:39],v[160:163]\nds_write_b128 v55,v[46:49] offset:9280\nM a[224:227],a[0:3],a[40:43],a[224:227]\nv_lshl_add_u32 v33,v32,4,v33\n" \
"v_add_u32 v46,s56,v33\nM a[192:195],a[0:3],a[44:47],a[192:195]\nv_lshlrev_b32 v33,1,v31\nv_sub_u32 v33,v33,v32\nM a[196:199],a[4:7],a[44:47],a[196:199]\nds_read_b128 a[84:87],v6 offset:4096\nv_lshlrev_b32 v33,4,v33\nv_add_u32 v33,10320,v33\n" \
"M a[228:231],a[4:7],a[40:43],a[228:231]\nds_read_b128 a[80:83],v6 offset:0\nv_add_u32 v50,s56,v33\nv_bfe_u32 v33,v4,6,3\nM v[164:167],a[4:7],a[36:39],v[164:167]\nds_read_b64_tr_b16 a[56:57],v9 offset:25088\nv_lshlrev_b32 v60,17,v33\nv_and_b32 v33,16,v4\n" \
"M v[132:135],a[4:7],a[32:35],v[132:135]\nv_lshl_add_u32 v60,v33,2,v60\nv_and_b32 v33,32,v4\nv_alignbyte_b32 v38,v39,v38,2\nM v[136:139],a[8:11],a[32:35],v[136:139]\nv_add_u32 v60,v60,v33\nv_lshrrev_b32 v33,9,v4\nv_alignbyte_b32 v39,v40,v39,2\n" \
"M v[168:171],a[8:11],a[36:39],v[168:171]\nv_lshl_add_u32 v60,v33,4,v60\nv_lshl_add_u32 v33,v32,3,v31\nv_alignbyte_b32 v40,v41,v40,2\nM a[232:235],a[8:11],a[40:43],a[232:235]\nv_sub_u32 v33,15,v33\nv_and_b32 v34,16,v4\nv_alignbyte_b32 v41,v52,v41,2\n" \
"M a[200:203],a[8:11],a[44:47],a[200:203]\nv_lshrrev_b32 v33,1,v33\nv_cmp_eq_u32 vcc,0,v33\nv_alignbyte_b32 v42,v43,v42,2\nM a[204:207],a[12:15],a[44:47],a[204:207]\nv_lshrrev_b32 v34,v34,v28\nv_alignbyte_b32 v43,v44,v43,2\nv_alignbyte_b32 v44,v45,v44,2\n" \
"M a[236:239],a[12:15],a[40:43],a[236:239]\nv_cndmask_b32 v52,0,v34,vcc\nv_cmp_eq_u32 vcc,1,v33\nv_alignbyte_b32 v45,v53,v45,2\nM v[172:175],a[12:15],a[36:39],v[172:175]\nds_read_b64_tr_b16 a[58:59],v10 offset:25088\nds_read_b64_tr_b16 a[60:61],v11 offset:25088\nM v[140:143],a[12:15],a[32:35],v[140:143]\n" \
"ds_read_b64_tr_b16 a[62:63],v12 offset:25088\nv_cndmask_b32 v53,0,v34,vcc\nM v[144:147],a[16:19],a[32:35],v[144:147]\nbuffer_store_dwordx4 v[96:99],v4,s[36:39],s96 offen offset:0 sc1\nds_read_b64_tr_b16 a[64:65],v9 offset:25600\nM v[176:179],a[16:19],a[36:39],v[176:179]\nds_read_b64_tr_b16 a[66:67],v10 offset:25600\nds_read_b64_tr_b16 a[68:69],v11 offset:25600\n" \
"M a[240:243],a[16:19],a[40:43],a[240:243]\nds_read_b64_tr_b16 a[70:71],v12 offset:25600\nds_read_b64_tr_b16 a[72:73],v9 offset:26112\nds_read_b64_tr_b16 a[74:75],v10 offset:26112\nds_read_b64_tr_b16 a[76:77],v11 offset:26112\nds_read_b64_tr_b16 a[78:79],v12 offset:26112\nds_read_b128 a[88:91],v6 offset:8192\nds_read_b128 a[92:95],v6 offset:12288\n" \
"M a[208:211],a[16:19],a[44:47],a[208:211]\nv_cmp_eq_u32 vcc,2,v33\nv_add_u32 v47,1024,v46\nM a[212:215],a[20:23],a[44:47],a[212:215]\nv_cndmask_b32 v54,0,v34,vcc\nv_cmp_eq_u32 vcc,3,v33\nM a[244:247],a[20:23],a[40:43],a[244:247]\nds_write_b128 v55,v[38:41] offset:7232\n" \
"M v[180:183],a[20:23],a[36:39],v[180:183]\nv_add_u32 v48,2048,v46\nv_add_u32 v49,3072,v46\nM v[148:151],a[20:23],a[32:35],v[148:151]\nbuffer_load_dwordx4 v21,s[32:35],s47 offen offset:0 lds\nM v[152:155],a[24:27],a[32:35],v[152:155]\ns_waitcnt lgkmcnt(1)\nv_accvgpr_read_b32 v30,a128\n" \
"v_accvgpr_read_b32 v31,a129\nM v[184:187],a[24:27],a[36:39],v[184:187]\ns_add_u32 m0,s51,9088\ns_barrier\nv_accvgpr_read_b32 v32,a130\nM a[248:251],a[24:27],a[40:43],a[248:251]\nds_write_b128 v55,v[42:45] offset:8256\nM a[216:219],a[24:27],a[44:47],a[216:219]\n" \
"v_cndmask_b32 v55,0,v34,vcc\nv_accvgpr_read_b32 v33,a131\nM a[220:223],a[28:31],a[44:47],a[220:223]\nbuffer_load_dwordx4 v21,s[32:35],s47 offen offset:128 lds\nM a[252:255],a[28:31],a[40:43],a[252:255]\ns_add_u32 m0,s50,65536\nv_mul_f32 v30,s20,v30\nv_mul_f32 v31,s20,v31\n" \
"M v[188:191],a[28:31],a[36:39],v[188:191]\ns_mov_b32 s47,3670016\nv_mul_f32 v32,s20,v32\nv_mul_f32 v33,s20,v33\nM v[156:159],a[28:31],a[32:35],v[156:159]\nC v30,v30,v31\nv_accvgpr_read_b32 v34,a132\nM v[128:131],a[48:51],a[80:83],v[128:131]\n" \
"buffer_load_dwordx4 v13,s[28:31],s46 offen lds\nM v[160:163],a[48:51],a[84:87],v[160:163]\ns_waitcnt vmcnt(6) lgkmcnt(0)\nds_read_b128 a[32:35],v50 offset:512\nv_accvgpr_read_b32 v35,a133\nM a[224:227],a[48:51],a[88:91],a[224:227]\ns_add_u32 m0,s50,69632\nds_read_b128 a[36:39],v50 offset:1024\n" \
"v_accvgpr_read_b32 v36,a134\nM a[192:195],a[48:51],a[92:95],a[192:195]\nds_read_b128 a[40:43],v50 offset:1536\nv_accvgpr_read_b32 v37,a135\nM a[196:199],a[52:55],a[92:95],a[196:199]\nds_read_b128 a[44:47],v50 offset:2048\nv_accvgpr_read_b32 v38,a136\nM a[228:231],a[52:55],a[88:91],a[228:231]\n" \
"buffer_store_dwordx4 v[100:103],v4,s[36:39],s96 offen offset:1024 sc1\nM v[164:167],a[52:55],a[84:87],v[164:167]\nds_read_b128 a[48:51],v50 offset:2560\nds_read2_b32 a[0:1],v46 offset0:0 offset1:1\nM v[132:135],a[52:55],a[80:83],v[132:135]\nds_read_b128 a[52:55],v50 offset:3072\nC v31,v32,v33\nM v[136:139],a[56:59],a[80:83],v[136:139]\n" \
"ds_read2_b32 a[2:3],v46 offset0:2 offset1:3\nv_accvgpr_read_b32 v39,a137\nM v[168:171],a[56:59],a[84:87],v[168:171]\nds_read2_b32 a[4:5],v46 offset0:128 offset1:129\nv_mul_f32 v34,s20,v34\nM a[232:235],a[56:59],a[88:91],a[232:235]\nds_read2_b32 a[6:7],v46 offset0:130 offset1:131\nds_read2_b32 a[8:9],v47 offset0:0 offset1:1\n" \
"M a[200:203],a[56:59],a[92:95],a[200:203]\nds_read_b128 a[56:59],v50 offset:3584\nv_mul_f32 v35,s20,v35\nM a[204:207],a[60:63],a[92:95],a[204:207]\nds_read2_b32 a[10:11],v47 offset0:2 offset1:3\nv_mul_f32 v36,s20,v36\nM a[236:239],a[60:63],a[88:91],a[236:239]\nds_read2_b32 a[12:13],v47 offset0:128 offset1:129\n" \
"v_mul_f32 v37,s20,v37\nM v[172:175],a[60:63],a[84:87],v[172:175]\nds_read2_b32 a[14:15],v47 offset0:130 offset1:131\nds_read2_b32 a[16:17],v48 offset0:0 offset1:1\nM v[140:143],a[60:63],a[80:83],v[140:143]\nds_read_b128 a[60:63],v50 offset:4096\nv_accvgpr_read_b32 v40,a138\nM v[144:147],a[64:67],a[80:83],v[144:147]\n" \
"ds_read2_b32 a[18:19],v48 offset0:2 offset1:3\nv_accvgpr_read_b32 v41,a139\nM v[176:179],a[64:67],a[84:87],v[176:179]\nds_read2_b32 a[20:21],v48 offset0:128 offset1:129\nv_mul_f32 v38,s20,v38\nM a[240:243],a[64:67],a[88:91],a[240:243]\nbuffer_store_dwordx4 v[112:115],v4,s[36:39],s96 offen offset:2048 sc1\nM a[208:211],a[64:67],a[92:95],a[208:211]\n" \
"ds_read2_b32 a[22:23],v48 offset0:130 offset1:131\nds_read2_b32 a[24:25],v49 offset0:0 offset1:1\nM a[212:215],a[68:71],a[92:95],a[212:215]\nds_read2_b32 a[26:27],v49 offset0:2 offset1:3\nv_mul_f32 v39,s20,v39\nM a[244:247],a[68:71],a[88:91],a[244:247]\nds_read2_b32 a[28:29],v49 offset0:128 offset1:129\nds_read2_b32 a[30:31],v49 offset0:130 offset1:131\n" \
"M v[180:183],a[68:71],a[84:87],v[180:183]\ns_nop 0\nds_read_b128 a[96:99],v50 offset:448\nC v32,v34,v35\nM v[148:151],a[68:71],a[80:83],v[148:151]\nds_read2_b32 a[64:65],v46 offset0:16 offset1:17\nC v33,v36,v37\nM v[152:155],a[72:75],a[80:83],v[152:155]\n" \
"ds_read2_b32 a[66:67],v46 offset0:18 offset1:19\nv_accvgpr_write_b32 a128,v30\nM v[184:187],a[72:75],a[84:87],v[184:187]\nds_read_b128 a[100:103],v50 offset:960\nv_accvgpr_write_b32 a129,v31\nM a[248:251],a[72:75],a[88:91],a[248:251]\nds_read2_b32 a[68:69],v46 offset0:144 offset1:145\nds_read2_b32 a[70:71],v46 offset0:146 offset1:147\n" \
"M a[216:219],a[72:75],a[92:95],a[216:219]\nds_read_b128 a[104:107],v50 offset:1472\nv_mul_f32 v40,s20,v40\nM a[220:223],a[76:79],a[92:95],a[220:223]\nds_read2_b32 a[72:73],v47 offset0:16 offset1:17\nv_mul_f32 v41,s20,v41\nM a[252:255],a[76:79],a[88:91],a[252:255]\nds_read2_b32 a[74:75],v47 offset0:18 offset1:19\n" \
"C v38,v38,v39\nM v[188:191],a[76:79],a[84:87],v[188:191]\ns_waitcnt lgkmcnt(6)\nds_read_b128 a[108:111],v50 offset:1984\nv_accvgpr_read_b32 v42,a140\nM v[156:159],a[76:79],a[80:83],v[156:159]\nbuffer_store_dwordx4 v[116:119],v4,s[36:39],s96 offen offset:3072 sc1\nM v[224:227],v[52:55],a[32:35],0\n" \
"s_add_u32 s96,s57,0\nds_read2_b32 a[76:77],v47 offset0:144 offset1:145\nds_read2_b32 a[78:79],v47 offset0:146 offset1:147\nM v[228:231],v[52:55],a[36:39],0\nds_read_b128 a[112:115],v50 offset:2496\nv_accvgpr_read_b32 v43,a141\nM v[232:235],v[52:55],a[40:43],0\nds_read2_b32 a[80:81],v48 offset0:16 offset1:17\n" \
"v_accvgpr_read_b32 v44,a142\nM v[236:239],v[52:55],a[44:47],0\nds_read2_b32 a[82:83],v48 offset0:18 offset1:19\nds_read_b128 a[116:119],v50 offset:3008\nM v[240:243],v[52:55],a[48:51],0\nds_read2_b32 a[84:85],v48 offset0:144 offset1:145\nv_accvgpr_read_b32 v45,a143\nM v[244:247],v[52:55],a[52:55],0\n" \
"ds_read2_b32 a[86:87],v48 offset0:146 offset1:147\nv_accvgpr_write_b32 a130,v32\nM v[248:251],v[52:55],a[56:59],0\nds_read_b128 a[120:123],v50 offset:3520\nv_accvgpr_write_b32 a131,v33\nM v[252:255],v[52:55],a[60:63],0\nds_read2_b32 a[88:89],v49 offset0:16 offset1:17\nv_accvgpr_read_b32 v30,a144\n" \
"M v[224:227],a[0:3],a[32:35],v[224:227]\nds_read2_b32 a[90:91],v49 offset0:18 offset1:19\nds_read_b128 a[124:127],v50 offset:4032\nM v[228:231],a[0:3],a[36:39],v[228:231]\nds_read2_b32 a[92:93],v49 offset0:144 offset1:145\nv_accvgpr_read_b32 v31,a145\nM v[228:231],a[4:7],a[32:35],v[228:231]\nds_read2_b32 a[94:95],v49 offset0:146 offset1:147\n" \
"C v39,v40,v41\nM v[232:235],a[0:3],a[40:43],v[232:235]\nbuffer_load_dwordx4 v14,s[28:31],s46 offen lds\nM v[232:235],a[4:7],a[36:39],v[232:235]\ns_add_u32 m0,s50,73728\nv_mul_f32 v42,s20,v42\nv_mul_f32 v43,s20,v43\nM v[232:235],a[8:11],a[32:35],v[232:235]\n" \
"v_mul_f32 v44,s20,v44\nv_mul_f32 v45,s20,v45\nM v[236:239],a[0:3],a[44:47],v[236:239]\nv_accvgpr_read_b32 v32,a146\nv_accvgpr_read_b32 v33,a147\nM v[236:239],a[4:7],a[40:43],v[236:239]\nv_mul_f32 v30,s20,v30\nv_mul_f32 v31,s20,v31\n" \
"M v[236:239],a[8:11],a[36:39],v[236:239]\nbuffer_store_dwordx4 v[192:195],v4,s[36:39],s96 offen offset:0 sc1\nM v[236:239],a[12:15],a[32:35],v[236:239]\nC v40,v42,v43\nC v41,v44,v45\nM v[240:243],a[0:3],a[48:51],v[240:243]\nv_accvgpr_write_b32 a132,v38\nv_accvgpr_write_b32 a133,v39\n" \
"M v[240:243],a[4:7],a[44:47],v[240:243]\nv_mul_f32 v32,s20,v32\nv_mul_f32 v33,s20,v33\nM v[240:243],a[8:11],a[40:43],v[240:243]\nbuffer_load_dwordx4 v15,s[28:31],s46 offen lds\nM v[240:243],a[12:15],a[36:39],v[240:243]\ns_add_u32 m0,s50,77824\nC v30,v30,v31\n" \
"v_accvgpr_write_b32 a134,v40\nM v[240:243],a[16:19],a[32:35],v[240:243]\nv_accvgpr_write_b32 a135,v41\nv_accvgpr_read_b32 v34,a148\nM v[244:247],a[0:3],a[52:55],v[244:247]\nv_accvgpr_read_b32 v35,a149\nv_accvgpr_read_b32 v36,a150\nM v[244:247],a[4:7],a[48:51],v[244:247]\n" \
"buffer_load_dwordx4 v16,s[28:31],s46 offen lds\nM v[244:247],a[8:11],a[44:47],v[244:247]\ns_add_u32 m0,s50,81920\nv_accvgpr_read_b32 v37,a151\nv_accvgpr_read_b32 v38,a152\nM v[244:247],a[12:15],a[40:43],v[244:247]\ns_mov_b32 s46,768\nC v31,v32,v33\n" \
"v_accvgpr_read_b32 v39,a153\nM v[244:247],a[16:19],a[36:39],v[244:247]\nv_mul_f32 v34,s20,v34\nv_mul_f32 v35,s20,v35\nM v[244:247],a[20:23],a[32:35],v[244:247]\nbuffer_load_dwordx4 v17,s[28:31],s46 offen lds\nM v[248:251],a[0:3],a[56:59],v[248:251]\ns_add_u32 m0,s50,86016\n" \
"v_mul_f32 v36,s20,v36\nv_mul_f32 v37,s20,v37\nM v[248:251],a[4:7],a[52:55],v[248:251]\nv_accvgpr_read_b32 v40,a154\nv_accvgpr_read_b32 v41,a155\nM v[248:251],a[8:11],a[48:51],v[248:251]\nv_mul_f32 v38,s20,v38\nv_mul_f32 v39,s20,v39\n" \
"M v[248:251],a[12:15],a[44:47],v[248:251]\nbuffer_load_dwordx4 v18,s[28:31],s46 offen lds\nM v[248:251],a[16:19],a[40:43],v[248:251]\ns_add_u32 m0,s50,90112\nC v32,v34,v35\nC v33,v36,v37\nM v[248:251],a[20:23],a[36:39],v[248:251]\nv_mul_f32 v40,s20,v40\n" \
"v_mul_f32 v41,s20,v41\nM v[248:251],a[24:27],a[32:35],v[248:251]\nC v38,v38,v39\nv_accvgpr_read_b32 v42,a156\nM v[252:255],a[0:3],a[60:63],v[252:255]\nds_read2_b32 a[0:1],v46 offset0:32 offset1:33\nds_read2_b32 a[2:3],v46 offset0:34 offset1:35\nM v[252:255],a[4:7],a[56:59],v[252:255]\n" \
"buffer_store_dwordx4 v[196:199],v4,s[36:39],s96 offen offset:1024 sc1\nM v[252:255],a[8:11],a[52:55],v[252:255]\nds_read2_b32 a[4:5],v46 offset0:160 offset1:161\nds_read2_b32 a[6:7],v46 offset0:162 offset1:163\nM v[252:255],a[12:15],a[48:51],v[252:255]\nds_read2_b32 a[8:9],v47 offset0:32 offset1:33\nv_accvgpr_write_b32 a136,v30\nM v[252:255],a[16:19],a[44:47],v[252:255]\n" \
"ds_read2_b32 a[10:11],v47 offset0:34 offset1:35\nds_read_b128 a[44:47],v50 offset:1920\nM v[252:255],a[20:23],a[40:43],v[252:255]\nbuffer_load_dwordx4 v19,s[28:31],s46 offen lds\nM v[252:255],a[24:27],a[36:39],v[252:255]\ns_add_u32 m0,s50,94208\nv_accvgpr_read_b32 v43,a157\nv_accvgpr_write_b32 a137,v31\n" \
"M v[252:255],a[28:31],a[32:35],v[252:255]\nds_read_b128 a[32:35],v50 offset:384\nv_accvgpr_write_b32 a138,v32\nM v[224:227],a[64:67],a[96:99],v[224:227]\ns_waitcnt lgkmcnt(4)\nds_read_b128 a[36:39],v50 offset:896\nv_accvgpr_write_b32 a139,v33\nM v[228:231],a[64:67],a[100:103],v[228:231]\n" \
"buffer_load_dwordx4 v20,s[28:31],s46 offen lds\nM v[228:231],a[68:71],a[96:99],v[228:231]\ns_add_u32 m0,s51,16384\nds_read_b128 a[40:43],v50 offset:1408\nv_accvgpr_read_b32 v44,a158\nM v[232:235],a[64:67],a[104:107],v[232:235]\ns_mov_b32 s46,896\nds_read2_b32 a[12:13],v47 offset0:160 offset1:161\n" \
"C v39,v40,v41\nM v[232:235],a[68:71],a[100:103],v[232:235]\nds_read2_b32 a[14:15],v47 offset0:162 offset1:163\nv_accvgpr_read_b32 v45,a159\nM v[232:235],a[72:75],a[96:99],v[232:235]\nbuffer_load_dwordx4 v21,s[32:35],s47 offen offset:0 lds\nM v[236:239],a[64:67],a[108:111],v[236:239]\ns_add_u32 m0,s51,17280\n" \
"ds_read_b128 a[48:51],v50 offset:2432\nv_mul_f32 v42,s20,v42\nM v[236:239],a[68:71],a[104:107],v[236:239]\nds_read2_b32 a[16:17],v48 offset0:32 offset1:33\nv_mul_f32 v43,s20,v43\nM v[236:239],a[72:75],a[100:103],v[236:239]\nds_read2_b32 a[18:19],v48 offset0:34 offset1:35\nv_mul_f32 v44,s20,v44\n" \
"M v[236:239],a[76:79],a[96:99],v[236:239]\nbuffer_load_dwordx4 v21,s[32:35],s47 offen offset:128 lds\nM v[240:243],a[64:67],a[112:115],v[240:243]\ns_add_u32 m0,s51,24576\nds_read_b128 a[52:55],v50 offset:2944\nv_mul_f32 v45,s20,v45\nM v[240:243],a[68:71],a[108:111],v[240:243]\ns_mov_b32 s47,3932160\n" \
"ds_read2_b32 a[20:21],v48 offset0:160 offset1:161\nv_accvgpr_read_b32 v30,a160\nM v[240:243],a[72:75],a[104:107],v[240:243]\nds_read2_b32 a[22:23],v48 offset0:162 offset1:163\nv_accvgpr_read_b32 v31,a161\nM v[240:243],a[76:79],a[100:103],v[240:243]\nds_read_b128 a[56:59],v50 offset:3456\nC v40,v42,v43\n" \
"M v[240:243],a[80:83],a[96:99],v[240:243]\nbuffer_store_dwordx4 v[200:203],v4,s[36:39],s96 offen offset:2048 sc1\nM v[244:247],a[64:67],a[116:119],v[244:247]\nds_read2_b32 a[24:25],v49 offset0:32 offset1:33\nds_read2_b32 a[26:27],v49 offset0:34 offset1:35\nM v[244:247],a[68:71],a[112:115],v[244:247]\nds_read_b128 a[60:63],v50 offset:3968\nC v41,v44,v45\n" \
"M v[244:247],a[72:75],a[108:111],v[244:247]\nds_read2_b32 a[28:29],v49 offset0:160 offset1:161\nv_accvgpr_read_b32 v32,a162\nM v[244:247],a[76:79],a[104:107],v[244:247]\nbuffer_load_dwordx4 v21,s[32:35],s47 offen offset:0 lds\nM v[244:247],a[80:83],a[100:103],v[244:247]\ns_add_u32 m0,s51,25472\nds_read2_b32 a[30:31],v49 offset0:162 offset1:163\n" \
"v_accvgpr_read_b32 v33,a163\nM v[244:247],a[84:87],a[96:99],v[244:247]\nv_mul_f32 v30,s20,v30\nv_accvgpr_write_b32 a140,v38\nM v[248:251],a[64:67],a[120:123],v[248:251]\nv_mul_f32 v31,s20,v31\nv_accvgpr_write_b32 a141,v39\nM v[248:251],a[68:71],a[116:119],v[248:251]\n" \
"buffer_load_dwordx4 v21,s[32:35],s47 offen offset:128 lds\nM v[248:251],a[72:75],a[112:115],v[248:251]\ns_add_u32 m0,s50,0\nv_accvgpr_write_b32 a142,v40\nv_accvgpr_write_b32 a143,v41\nM v[248:251],a[76:79],a[108:111],v[248:251]\ns_mov_b32 s47,4194304\nv_mul_f32 v32,s20,v32\n" \
"v_mul_f32 v33,s20,v33\nM v[248:251],a[80:83],a[104:107],v[248:251]\nC v30,v30,v31\nv_accvgpr_read_b32 v34,a164\nM v[248:251],a[84:87],a[100:103],v[248:251]\nbuffer_load_dwordx4 v13,s[28:31],s46 offen lds\nM v[248:251],a[88:91],a[96:99],v[248:251]\ns_add_u32 m0,s50,4096\n" \
"v_accvgpr_read_b32 v35,a165\nv_accvgpr_read_b32 v36,a166\nM v[252:255],a[64:67],a[124:127],v[252:255]\nds_read2_b32 a[64:65],v46 offset0:48 offset1:49\nds_read2_b32 a[66:67],v46 offset0:50 offset1:51\nM v[252:255],a[68:71],a[120:123],v[252:255]\nds_read2_b32 a[68:69],v46 offset0:176 offset1:177\nv_accvgpr_read_b32 v37,a167\n" \
"M v[252:255],a[72:75],a[116:119],v[252:255]\nds_read2_b32 a[70:71],v46 offset0:178 offset1:179\nds_read2_b32 a[72:73],v47 offset0:48 offset1:49\nM v[252:255],a[76:79],a[112:115],v[252:255]\nbuffer_store_dwordx4 v[204:207],v4,s[36:39],s96 offen offset:3072 sc1\nM v[252:255],a[80:83],a[108:111],v[252:255]\ns_add_u32 s96,s57,4096\nds_read2_b32 a[74:75],v47 offset0:50 offset1:51\n" \
"ds_read_b128 a[108:111],v50 offset:1856\nM v[252:255],a[84:87],a[104:107],v[252:255]\nds_read_b128 a[104:107],v50 offset:1344\nC v31,v32,v33\nM v[252:255],a[88:91],a[100:103],v[252:255]\ns_waitcnt lgkmcnt(6)\nds_read_b128 a[100:103],v50 offset:832\nv_mul_f32 v34,s20,v34\n" \
"M v[252:255],a[92:95],a[96:99],v[252:255]\nbuffer_load_dwordx4 v14,s[28:31],s46 offen lds\nM v[224:227],a[0:3],a[32:35],v[224:227]\ns_add_u32 m0,s50,8192\nv_mul_f32 v35,s20,v35\nv_mul_f32 v36,s20,v36\nM v[228:231],a[0:3],a[36:39],v[228:231]\nds_read_b128 a[96:99],v50 offset:320\n" \
"v_mul_f32 v37,s20,v37\nM v[228:231],a[4:7],a[32:35],v[228:231]\nds_read2_b32 a[76:77],v47 offset0:176 offset1:177\nds_read2_b32 a[78:79],v47 offset0:178 offset1:179\nM v[232:235],a[0:3],a[40:43],v[232:235]\nds_read_b128 a[112:115],v50 offset:2368\nv_accvgpr_read_b32 v38,a168\nM v[232:235],a[4:7],a[36:39],v[232:235]\n" \
"buffer_store_dwordx4 v[208:211],v4,s[36:39],s96 offen offset:0 sc1\nM v[232:235],a[8:11],a[32:35],v[232:235]\nds_read2_b32 a[80:81],v48 offset0:48 offset1:49\nds_read2_b32 a[82:83],v48 offset0:50 offset1:51\nM v[236:239],a[0:3],a[44:47],v[236:239]\nds_read_b128 a[116:119],v50 offset:2880\nv_accvgpr_read_b32 v39,a169\nM v[236:239],a[4:7],a[40:43],v[236:239]\n" \
"ds_read2_b32 a[84:85],v48 offset0:176 offset1:177\nC v32,v34,v35\nM v[236:239],a[8:11],a[36:39],v[236:239]\nbuffer_load_dwordx4 v15,s[28:31],s46 offen lds\nM v[236:239],a[12:15],a[32:35],v[236:239]\ns_add_u32 m0,s50,12288\nds_read2_b32 a[86:87],v48 offset0:178 offset1:179\nC v33,v36,v37\n" \
"M v[240:243],a[0:3],a[48:51],v[240:243]\nds_read_b128 a[120:123],v50 offset:3392\nv_accvgpr_read_b32 v40,a170\nM v[240:243],a[4:7],a[44:47],v[240:243]\nds_read2_b32 a[88:89],v49 offset0:48 offset1:49\nv_accvgpr_read_b32 v41,a171\nM v[240:243],a[8:11],a[40:43],v[240:243]\nds_read2_b32 a[90:91],v49 offset0:50 offset1:51\n" \
"ds_read_b128 a[124:127],v50 offset:3904\nM v[240:243],a[12:15],a[36:39],v[240:243]\nbuffer_store_dwordx4 v[212:215],v4,s[36:39],s96 offen offset:1024 sc1\nM v[240:243],a[16:19],a[32:35],v[240:243]\nds_read2_b32 a[92:93],v49 offset0:176 offset1:177\nds_read2_b32 a[94:95],v49 offset0:178 offset1:179\nM v[244:247],a[0:3],a[52:55],v[244:247]\nv_mul_f32 v38,s20,v38\n" \
"v_mul_f32 v39,s20,v39\nM v[244:247],a[4:7],a[48:51],v[244:247]\nv_accvgpr_write_b32 a144,v30\nv_accvgpr_write_b32 a145,v31\nM v[244:247],a[8:11],a[44:47],v[244:247]\nbuffer_load_dwordx4 v16,s[28:31],s46 offen lds\nM v[244:247],a[12:15],a[40:43],v[244:247]\ns_add_u32 m0,s50,16384\n" \
"v_accvgpr_write_b32 a146,v32\nv_accvgpr_write_b32 a147,v33\nM v[244:247],a[16:19],a[36:39],v[244:247]\ns_mov_b32 s46,896\nv_mul_f32 v40,s20,v40\nv_mul_f32 v41,s20,v41\nM v[244:247],a[20:23],a[32:35],v[244:247]\nC v38,v38,v39\n" \
"v_accvgpr_read_b32 v42,a172\nM v[248:251],a[0:3],a[56:59],v[248:251]\nv_accvgpr_read_b32 v43,a173\nv_accvgpr_read_b32 v44,a174\nM v[248:251],a[4:7],a[52:55],v[248:251]\nbuffer_store_dwordx4 v[216:219],v4,s[36:39],s96 offen offset:2048 sc1\nM v[248:251],a[8:11],a[48:51],v[248:251]\nC v39,v40,v41\n" \
"v_accvgpr_read_b32 v45,a175\nM v[248:251],a[12:15],a[44:47],v[248:251]\nv_mul_f32 v42,s20,v42\nv_mul_f32 v43,s20,v43\nM v[248:251],a[16:19],a[40:43],v[248:251]\nv_mul_f32 v44,s20,v44\nv_mul_f32 v45,s20,v45\nM v[248:251],a[20:23],a[36:39],v[248:251]\n" \
"buffer_load_dwordx4 v17,s[28:31],s46 offen lds\nM v[248:251],a[24:27],a[32:35],v[248:251]\ns_add_u32 m0,s50,20480\nC v40,v42,v43\nC v41,v44,v45\nM v[252:255],a[0:3],a[60:63],v[252:255]\nds_read2_b32 a[0:1],v46 offset0:64 offset1:65\nds_read2_b32 a[2:3],v46 offset0:66 offset1:67\n" \
"M v[252:255],a[4:7],a[56:59],v[252:255]\nds_read2_b32 a[4:5],v46 offset0:192 offset1:193\nv_accvgpr_write_b32 a148,v38\nM v[252:255],a[8:11],a[52:55],v[252:255]\nds_read2_b32 a[6:7],v46 offset0:194 offset1:195\nds_read2_b32 a[8:9],v47 offset0:64 offset1:65\nM v[252:255],a[12:15],a[48:51],v[252:255]\nbuffer_store_dwordx4 v[220:223],v4,s[36:39],s96 offen offset:3072 sc1\n" \
"M v[252:255],a[16:19],a[44:47],v[252:255]\ns_add_u32 s96,s57,16384\nds_read2_b32 a[10:11],v47 offset0:66 offset1:67\nds_read_b128 a[44:47],v50 offset:1792\nM v[252:255],a[20:23],a[40:43],v[252:255]\nds_read_b128 a[40:43],v50 offset:1280\nv_accvgpr_write_b32 a149,v39\nM v[252:255],a[24:27],a[36:39],v[252:255]\n" \
"s_waitcnt lgkmcnt(6)\nds_read_b128 a[36:39],v50 offset:768\nv_accvgpr_write_b32 a150,v40\nM v[252:255],a[28:31],a[32:35],v[252:255]\nbuffer_load_dwordx4 v18,s[28:31],s46 offen lds\nM v[224:227],a[64:67],a[96:99],v[224:227]\ns_add_u32 m0,s50,24576\nv_accvgpr_write_b32 a151,v41\n" \
"v_accvgpr_read_b32 v30,a176\nM v[228:231],a[64:67],a[100:103],v[228:231]\nds_read_b128 a[32:35],v50 offset:256\nv_accvgpr_read_b32 v31,a177\nM v[228:231],a[68:71],a[96:99],v[228:231]\nds_read2_b32 a[12:13],v47 offset0:192 offset1:193\nds_read2_b32 a[14:15],v47 offset0:194 offset1:195\nM v[232:235],a[64:67],a[104:107],v[232:235]\n" \
"ds_read_b128 a[48:51],v50 offset:2304\nv_accvgpr_read_b32 v32,a178\nM v[232:235],a[68:71],a[100:103],v[232:235]\nbuffer_store_dwordx4 a[128:131],v4,s[36:39],s96 offen offset:0 sc1\nM v[232:235],a[72:75],a[96:99],v[232:235]\nds_read2_b32 a[16:17],v48 offset0:64 offset1:65\nds_read2_b32 a[18:19],v48 offset0:66 offset1:67\nM v[236:239],a[64:67],a[108:111],v[236:239]\n" \
"ds_read_b128 a[52:55],v50 offset:2816\nv_accvgpr_read_b32 v33,a179\nM v[236:239],a[68:71],a[104:107],v[236:239]\nds_read2_b32 a[20:21],v48 offset0:192 offset1:193\nv_mul_f32 v30,s20,v30\nM v[236:239],a[72:75],a[100:103],v[236:239]\nds_read2_b32 a[22:23],v48 offset0:194 offset1:195\nds_read_b128 a[56:59],v50 offset:3328\n" \
"M v[236:239],a[76:79],a[96:99],v[236:239]\nds_read2_b32 a[24:25],v49 offset0:64 offset1:65\nv_mul_f32 v31,s20,v31\nM v[240:243],a[64:67],a[112:115],v[240:243]\nds_read2_b32 a[26:27],v49 offset0:66 offset1:67\nv_mul_f32 v32,s20,v32\nM v[240:243],a[68:71],a[108:111],v[240:243]\nds_read_b128 a[60:63],v50 offset:3840\n" \
"v_mul_f32 v33,s20,v33\nM v[240:243],a[72:75],a[104:107],v[240:243]\nds_read2_b32 a[28:29],v49 offset0:192 offset1:193\nC v30,v30,v31\nM v[240:243],a[76:79],a[100:103],v[240:243]\nds_read2_b32 a[30:31],v49 offset0:194 offset1:195\nv_accvgpr_read_b32 v34,a180\nM v[240:243],a[80:83],a[96:99],v[240:243]\n" \
"v_accvgpr_read_b32 v35,a181\nv_accvgpr_read_b32 v36,a182\nM v[244:247],a[64:67],a[116:119],v[244:247]\nC v31,v32,v33\nv_accvgpr_read_b32 v37,a183\nM v[244:247],a[68:71],a[112:115],v[244:247]\nv_mul_f32 v34,s20,v34\nv_mul_f32 v35,s20,v35\n" \
"M v[244:247],a[72:75],a[108:111],v[244:247]\nbuffer_store_dwordx4 a[132:135],v4,s[36:39],s96 offen offset:1024 sc1\nM v[244:247],a[76:79],a[104:107],v[244:247]\nv_mul_f32 v36,s20,v36\nv_mul_f32 v37,s20,v37\nM v[244:247],a[80:83],a[100:103],v[244:247]\nC v32,v34,v35\nC v33,v36,v37\n" \
"M v[244:247],a[84:87],a[96:99],v[244:247]\nv_accvgpr_write_b32 a152,v30\nv_accvgpr_write_b32 a153,v31\nM v[248:251],a[64:67],a[120:123],v[248:251]\nv_accvgpr_write_b32 a154,v32\nv_accvgpr_write_b32 a155,v33\nM v[248:251],a[68:71],a[116:119],v[248:251]\nv_accvgpr_read_b32 v38,a184\n" \
"v_accvgpr_read_b32 v39,a185\nM v[248:251],a[72:75],a[112:115],v[248:251]\nv_accvgpr_read_b32 v40,a186\nv_accvgpr_read_b32 v41,a187\nM v[248:251],a[76:79],a[108:111],v[248:251]\nv_mul_f32 v38,s20,v38\nv_mul_f32 v39,s20,v39\nM v[248:251],a[80:83],a[104:107],v[248:251]\n" \
"v_mul_f32 v40,s20,v40\nv_mul_f32 v41,s20,v41\nM v[248:251],a[84:87],a[100:103],v[248:251]\nC v38,v38,v39\nv_accvgpr_read_b32 v42,a188\nM v[248:251],a[88:91],a[96:99],v[248:251]\nv_accvgpr_read_b32 v43,a189\nv_accvgpr_read_b32 v44,a190\n" \
"M v[252:255],a[64:67],a[124:127],v[252:255]\nds_read2_b32 a[64:65],v46 offset0:80 offset1:81\nds_read2_b32 a[66:67],v46 offset0:82 offset1:83\nM v[252:255],a[68:71],a[120:123],v[252:255]\nds_read2_b32 a[68:69],v46 offset0:208 offset1:209\nC v39,v40,v41\nM v[252:255],a[72:75],a[116:119],v[252:255]\nbuffer_store_dwordx4 a[136:139],v4,s[36:39],s96 offen offset:2048 sc1\n" \
"M v[252:255],a[76:79],a[112:115],v[252:255]\nds_read2_b32 a[70:71],v46 offset0:210 offset1:211\nds_read2_b32 a[72:73],v47 offset0:80 offset1:81\nM v[252:255],a[80:83],a[108:111],v[252:255]\nds_read2_b32 a[74:75],v47 offset0:82 offset1:83\nv_accvgpr_read_b32 v45,a191\nM v[252:255],a[84:87],a[104:107],v[252:255]\nds_read_b128 a[104:107],v50 offset:1216\n" \
"v_mul_f32 v42,s20,v42\nM v[252:255],a[88:91],a[100:103],v[252:255]\nds_read_b128 a[100:103],v50 offset:704\nv_mul_f32 v43,s20,v43\nM v[252:255],a[92:95],a[96:99],v[252:255]\ns_waitcnt lgkmcnt(5)\nds_read_b128 a[96:99],v50 offset:192\nv_mul_f32 v44,s20,v44\n" \
"M v[224:227],a[0:3],a[32:35],v[224:227]\nds_read_b128 a[108:111],v50 offset:1728\nv_mul_f32 v45,s20,v45\nM v[228:231],a[0:3],a[36:39],v[228:231]\nds_read2_b32 a[76:77],v47 offset0:208 offset1:209\nds_read2_b32 a[78:79],v47 offset0:210 offset1:211\nM v[228:231],a[4:7],a[32:35],v[228:231]\nds_read_b128 a[112:115],v50 offset:2240\n" \
"C v40,v42,v43\nM v[232:235],a[0:3],a[40:43],v[232:235]\nds_read2_b32 a[80:81],v48 offset0:80 offset1:81\nC v41,v44,v45\nM v[232:235],a[4:7],a[36:39],v[232:235]\nds_read2_b32 a[82:83],v48 offset0:82 offset1:83\nv_accvgpr_write_b32 a156,v38\nM v[232:235],a[8:11],a[32:35],v[232:235]\n" \
"ds_read_b128 a[116:119],v50 offset:2752\nv_accvgpr_write_b32 a157,v39\nM v[236:239],a[0:3],a[44:47],v[236:239]\nds_read2_b32 a[84:85],v48 offset0:208 offset1:209\nds_read2_b32 a[86:87],v48 offset0:210 offset1:211\nM v[236:239],a[4:7],a[40:43],v[236:239]\nbuffer_store_dwordx4 a[140:143],v4,s[36:39],s96 offen offset:3072 sc1\nM v[236:239],a[8:11],a[36:39],v[236:239]\n" \
"s_add_u32 s96,s57,20480\nds_read_b128 a[120:123],v50 offset:3264\nds_read2_b32 a[88:89],v49 offset0:80 offset1:81\nM v[236:239],a[12:15],a[32:35],v[236:239]\nds_read2_b32 a[90:91],v49 offset0:82 offset1:83\nv_accvgpr_write_b32 a158,v40\nM v[240:243],a[0:3],a[48:51],v[240:243]\nds_read_b128 a[124:127],v50 offset:3776\n" \
"v_accvgpr_write_b32 a159,v41\nM v[240:243],a[4:7],a[44:47],v[240:243]\nbuffer_load_dwordx4 v19,s[28:31],s46 offen lds\nM v[240:243],a[8:11],a[40:43],v[240:243]\ns_add_u32 m0,s50,28672\nds_read2_b32 a[92:93],v49 offset0:208 offset1:209\nM v[240:243],a[12:15],a[36:39],v[240:243]\nds_read2_b32 a[94:95],v49 offset0:210 offset1:211\n" \
"M v[240:243],a[16:19],a[32:35],v[240:243]\nM v[244:247],a[0:3],a[52:55],v[244:247]\nM v[244:247],a[4:7],a[48:51],v[244:247]\nbuffer_store_dwordx4 a[144:147],v4,s[36:39],s96 offen offset:0 sc1\nM v[244:247],a[8:11],a[44:47],v[244:247]\nM v[244:247],a[12:15],a[40:43],v[244:247]\nM v[244:247],a[16:19],a[36:39],v[244:247]\nM v[244:247],a[20:23],a[32:35],v[244:247]\n" \
"M v[248:251],a[0:3],a[56:59],v[248:251]\nM v[248:251],a[4:7],a[52:55],v[248:251]\nM v[248:251],a[8:11],a[48:51],v[248:251]\nM v[248:251],a[12:15],a[44:47],v[248:251]\nM v[248:251],a[16:19],a[40:43],v[248:251]\nM v[248:251],a[20:23],a[36:39],v[248:251]\nM v[248:251],a[24:27],a[32:35],v[248:251]\ns_nop 0\n" \
"M v[252:255],a[0:3],a[60:63],v[252:255]\nds_read2_b32 a[0:1],v46 offset0:96 offset1:97\nds_read2_b32 a[2:3],v46 offset0:98 offset1:99\nM v[252:255],a[4:7],a[56:59],v[252:255]\nbuffer_store_dwordx4 a[148:151],v4,s[36:39],s96 offen offset:1024 sc1\nM v[252:255],a[8:11],a[52:55],v[252:255]\nds_read2_b32 a[4:5],v46 offset0:224 offset1:225\nds_read2_b32 a[6:7],v46 offset0:226 offset1:227\n" \
"M v[252:255],a[12:15],a[48:51],v[252:255]\nds_read2_b32 a[8:9],v47 offset0:96 offset1:97\nM v[252:255],a[16:19],a[44:47],v[252:255]\nds_read_b128 a[44:47],v50 offset:1664\nM v[252:255],a[20:23],a[40:43],v[252:255]\nds_read_b128 a[40:43],v50 offset:1152\nM v[252:255],a[24:27],a[36:39],v[252:255]\nds_read_b128 a[36:39],v50 offset:640\n" \
"M v[252:255],a[28:31],a[32:35],v[252:255]\ns_waitcnt lgkmcnt(6)\nds_read_b128 a[32:35],v50 offset:128\nM v[224:227],a[64:67],a[96:99],v[224:227]\nds_read2_b32 a[10:11],v47 offset0:98 offset1:99\nds_read2_b32 a[12:13],v47 offset0:224 offset1:225\nM v[228:231],a[64:67],a[100:103],v[228:231]\nds_read2_b32 a[14:15],v47 offset0:226 offset1:227\n" \
"M v[228:231],a[68:71],a[96:99],v[228:231]\nds_read_b128 a[48:51],v50 offset:2176\nM v[232:235],a[64:67],a[104:107],v[232:235]\nds_read2_b32 a[16:17],v48 offset0:96 offset1:97\nM v[232:235],a[68:71],a[100:103],v[232:235]\nds_read2_b32 a[18:19],v48 offset0:98 offset1:99\nds_read_b128 a[52:55],v50 offset:2688\nM v[232:235],a[72:75],a[96:99],v[232:235]\n" \
"ds_read2_b32 a[20:21],v48 offset0:224 offset1:225\nM v[236:239],a[64:67],a[108:111],v[236:239]\nbuffer_store_dwordx4 a[152:155],v4,s[36:39],s96 offen offset:2048 sc1\nM v[236:239],a[68:71],a[104:107],v[236:239]\nds_read2_b32 a[22:23],v48 offset0:226 offset1:227\nds_read_b128 a[56:59],v50 offset:3200\nM v[236:239],a[72:75],a[100:103],v[236:239]\nds_read2_b32 a[24:25],v49 offset0:96 offset1:97\n" \
"M v[236:239],a[76:79],a[96:99],v[236:239]\nds_read2_b32 a[26:27],v49 offset0:98 offset1:99\nM v[240:243],a[64:67],a[112:115],v[240:243]\nds_read_b128 a[60:63],v50 offset:3712\nM v[240:243],a[68:71],a[108:111],v[240:243]\nds_read2_b32 a[28:29],v49 offset0:224 offset1:225\nds_read2_b32 a[30:31],v49 offset0:226 offset1:227\nM v[240:243],a[72:75],a[104:107],v[240:243]\n" \
"M v[240:243],a[76:79],a[100:103],v[240:243]\nM v[240:243],a[80:83],a[96:99],v[240:243]\nM v[244:247],a[64:67],a[116:119],v[244:247]\nM v[244:247],a[68:71],a[112:115],v[244:247]\nM v[244:247],a[72:75],a[108:111],v[244:247]\nM v[244:247],a[76:79],a[104:107],v[244:247]\nM v[244:247],a[80:83],a[100:103],v[244:247]\nbuffer_store_dwordx4 a[156:159],v4,s[36:39],s96 offen offset:3072 sc1\n" \
"M v[244:247],a[84:87],a[96:99],v[244:247]\ns_add_u32 s96,s57,49152\nM v[248:251],a[64:67],a[120:123],v[248:251]\nM v[248:251],a[68:71],a[116:119],v[248:251]\nM v[248:251],a[72:75],a[112:115],v[248:251]\nM v[248:251],a[76:79],a[108:111],v[248:251]\nM v[248:251],a[80:83],a[104:107],v[248:251]\nM v[248:251],a[84:87],a[100:103],v[248:251]\n" \
"M v[248:251],a[88:91],a[96:99],v[248:251]\nM v[252:255],a[64:67],a[124:127],v[252:255]\nds_read2_b32 a[64:65],v46 offset0:112 offset1:113\nds_read2_b32 a[66:67],v46 offset0:114 offset1:115\nM v[252:255],a[68:71],a[120:123],v[252:255]\nds_read2_b32 a[68:69],v46 offset0:240 offset1:241\nM v[252:255],a[72:75],a[116:119],v[252:255]\nbuffer_load_dwordx4 v20,s[28:31],s46 offen lds\n" \
"M v[252:255],a[76:79],a[112:115],v[252:255]\ns_add_u32 m0,s51,32768\nds_read2_b32 a[70:71],v46 offset0:242 offset1:243\nM v[252:255],a[80:83],a[108:111],v[252:255]\ns_mov_b32 s46,1024\nds_read2_b32 a[72:73],v47 offset0:112 offset1:113\nM v[252:255],a[84:87],a[104:107],v[252:255]\nds_read_b128 a[104:107],v50 offset:1088\n" \
"M v[252:255],a[88:91],a[100:103],v[252:255]\ns_waitcnt lgkmcnt(4)\nds_read_b128 a[100:103],v50 offset:576\nM v[252:255],a[92:95],a[96:99],v[252:255]\nbuffer_store_dwordx4 v[88:91],v4,s[36:39],s96 offen offset:0 sc1\nM v[224:227],a[0:3],a[32:35],v[224:227]\nds_read_b128 a[96:99],v50 offset:64\nds_read2_b32 a[74:75],v47 offset0:114 offset1:115\n" \
"M v[228:231],a[0:3],a[36:39],v[228:231]\nds_read_b128 a[108:111],v50 offset:1600\nM v[228:231],a[4:7],a[32:35],v[228:231]\nds_read2_b32 a[76:77],v47 offset0:240 offset1:241\nM v[232:235],a[0:3],a[40:43],v[232:235]\nds_read2_b32 a[78:79],v47 offset0:242 offset1:243\nM v[232:235],a[4:7],a[36:39],v[232:235]\nds_read_b128 a[112:115],v50 offset:2112\n" \
"M v[232:235],a[8:11],a[32:35],v[232:235]\nds_read2_b32 a[80:81],v48 offset0:112 offset1:113\nds_read2_b32 a[82:83],v48 offset0:114 offset1:115\nM v[236:239],a[0:3],a[44:47],v[236:239]\nds_read_b128 a[116:119],v50 offset:2624\nM v[236:239],a[4:7],a[40:43],v[236:239]\nds_read2_b32 a[84:85],v48 offset0:240 offset1:241\nM v[236:239],a[8:11],a[36:39],v[236:239]\n" \
"ds_read2_b32 a[86:87],v48 offset0:242 offset1:243\nM v[236:239],a[12:15],a[32:35],v[236:239]\nds_read_b128 a[120:123],v50 offset:3136\nM v[240:243],a[0:3],a[48:51],v[240:243]\nds_read2_b32 a[88:89],v49 offset0:112 offset1:113\nds_read2_b32 a[90:91],v49 offset0:114 offset1:115\nM v[240:243],a[4:7],a[44:47],v[240:243]\nds_read_b128 a[124:127],v50 offset:3648\n" \
"M v[240:243],a[8:11],a[40:43],v[240:243]\nds_read2_b32 a[92:93],v49 offset0:240 offset1:241\nM v[240:243],a[12:15],a[36:39],v[240:243]\nds_read2_b32 a[94:95],v49 offset0:242 offset1:243\nM v[240:243],a[16:19],a[32:35],v[240:243]\nM v[244:247],a[0:3],a[52:55],v[244:247]\nM v[244:247],a[4:7],a[48:51],v[244:247]\nM v[244:247],a[8:11],a[44:47],v[244:247]\n" \
"M v[244:247],a[12:15],a[40:43],v[244:247]\nM v[244:247],a[16:19],a[36:39],v[244:247]\nM v[244:247],a[20:23],a[32:35],v[244:247]\nM v[248:251],a[0:3],a[56:59],v[248:251]\nM v[248:251],a[4:7],a[52:55],v[248:251]\nM v[248:251],a[8:11],a[48:51],v[248:251]\nM v[248:251],a[12:15],a[44:47],v[248:251]\nM v[248:251],a[16:19],a[40:43],v[248:251]\n" \
"M v[248:251],a[20:23],a[36:39],v[248:251]\nM v[248:251],a[24:27],a[32:35],v[248:251]\nM v[252:255],a[0:3],a[60:63],v[252:255]\nM v[252:255],a[4:7],a[56:59],v[252:255]\nM v[252:255],a[8:11],a[52:55],v[252:255]\nM v[252:255],a[12:15],a[48:51],v[252:255]\nM v[252:255],a[16:19],a[44:47],v[252:255]\nM v[252:255],a[20:23],a[40:43],v[252:255]\n" \
"M v[252:255],a[24:27],a[36:39],v[252:255]\nM v[252:255],a[28:31],a[32:35],v[252:255]\ns_waitcnt lgkmcnt(0)\nM v[224:227],a[64:67],a[96:99],v[224:227]\ns_waitcnt vmcnt(27) lgkmcnt(0)\nM v[228:231],a[64:67],a[100:103],v[228:231]\ns_barrier\nds_read_b64_tr_b16 a[0:1],v9 offset:0\n" \
"M v[228:231],a[68:71],a[96:99],v[228:231]\nbuffer_load_dwordx4 v21,s[32:35],s47 offen offset:0 lds\nM v[232:235],a[64:67],a[104:107],v[232:235]\ns_add_u32 m0,s51,33664\nC v224,v224,v225\nC v225,v226,v227\nM v[232:235],a[68:71],a[100:103],v[232:235]\nC v226,v228,v229\n" \
"C v227,v230,v231\nM v[232:235],a[72:75],a[96:99],v[232:235]\nv_permlane16_swap_b32 v224,v226\nM v[236:239],a[64:67],a[108:111],v[236:239]\nbuffer_load_dwordx4 v21,s[32:35],s47 offen offset:128 lds\nM v[236:239],a[68:71],a[104:107],v[236:239]\ns_mov_b32 s47,4456448\nds_read_b64_tr_b16 a[2:3],v10 offset:0\n" \
"C v232,v232,v233\nM v[236:239],a[72:75],a[100:103],v[236:239]\ns_add_u32 m0,s51,40960\nv_permlane16_swap_b32 v225,v227\nM v[236:239],a[76:79],a[96:99],v[236:239]\nds_read_b128 a[32:35],v7 offset:0\nds_read_b128 a[36:39],v7 offset:4096\nM v[240:243],a[64:67],a[112:115],v[240:243]\n" \
"buffer_load_dwordx4 v21,s[32:35],s47 offen offset:0 lds\nM v[240:243],a[68:71],a[108:111],v[240:243]\ns_add_u32 m0,s51,41856\nC v233,v234,v235\nC v234,v236,v237\nM v[240:243],a[72:75],a[104:107],v[240:243]\nds_read_b128 a[40:43],v7 offset:8192\nC v235,v238,v239\n" \
"M v[240:243],a[76:79],a[100:103],v[240:243]\nds_read_b64_tr_b16 a[4:5],v11 offset:0\nM v[240:243],a[80:83],a[96:99],v[240:243]\nbuffer_load_dwordx4 v21,s[32:35],s47 offen offset:128 lds\nM v[244:247],a[64:67],a[116:119],v[244:247]\ns_add_u32 m0,s50,32768\nv_permlane16_swap_b32 v232,v234\nM v[244:247],a[68:71],a[112:115],v[244:247]\n" \
"s_mov_b32 s47,4718592\nv_permlane16_swap_b32 v233,v235\nM v[244:247],a[72:75],a[108:111],v[244:247]\nds_read_b128 a[44:47],v7 offset:12288\nds_read_b64_tr_b16 a[6:7],v12 offset:0\nM v[244:247],a[76:79],a[104:107],v[244:247]\nbuffer_load_dwordx4 v13,s[28:31],s46 offen lds\nM v[244:247],a[80:83],a[100:103],v[244:247]\n" \
"s_add_u32 m0,s50,36864\nC v240,v240,v241\nC v241,v242,v243\nM v[244:247],a[84:87],a[96:99],v[244:247]\nds_read_b64_tr_b16 a[8:9],v9 offset:512\nds_read_b64_tr_b16 a[10:11],v10 offset:512\nM v[248:251],a[64:67],a[120:123],v[248:251]\nds_read_b64_tr_b16 a[12:13],v11 offset:512\n" \
"M v[248:251],a[68:71],a[116:119],v[248:251]\nbuffer_load_dwordx4 v14,s[28:31],s46 offen lds\nM v[248:251],a[72:75],a[112:115],v[248:251]\ns_add_u32 m0,s50,40960\nds_read_b64_tr_b16 a[14:15],v12 offset:512\nC v242,v244,v245\nM v[248:251],a[76:79],a[108:111],v[248:251]\nds_read_b64_tr_b16 a[16:17],v9 offset:1024\n" \
"C v243,v246,v247\nM v[248:251],a[80:83],a[104:107],v[248:251]\nds_read_b64_tr_b16 a[18:19],v10 offset:1024\nM v[248:251],a[84:87],a[100:103],v[248:251]\nbuffer_load_dwordx4 v15,s[28:31],s46 offen lds\nM v[248:251],a[88:91],a[96:99],v[248:251]\ns_add_u32 m0,s50,45056\nds_read_b64_tr_b16 a[20:21],v11 offset:1024\n" \
"M v[252:255],a[64:67],a[124:127],v[252:255]\nds_read_b64_tr_b16 a[22:23],v12 offset:1024\nM v[252:255],a[68:71],a[120:123],v[252:255]\nds_read_b64_tr_b16 a[24:25],v9 offset:1536\nds_read_b64_tr_b16 a[26:27],v10 offset:1536\nM v[252:255],a[72:75],a[116:119],v[252:255]\nbuffer_load_dwordx4 v16,s[28:31],s46 offen lds\nM v[252:255],a[76:79],a[112:115],v[252:255]\n" \
"s_mov_b32 s46,1024\nv_permlane16_swap_b32 v240,v242\nM v[252:255],a[80:83],a[108:111],v[252:255]\ns_add_u32 m0,s50,49152\nds_read_b64_tr_b16 a[28:29],v11 offset:1536\nds_read_b64_tr_b16 a[30:31],v12 offset:1536\nM v[252:255],a[84:87],a[104:107],v[252:255]\ns_waitcnt lgkmcnt(5)\n" \
"ds_read_b64_tr_b16 a[48:49],v9 offset:8192\nC v248,v248,v249\nM v[252:255],a[88:91],a[100:103],v[252:255]\nbuffer_load_dwordx4 v17,s[28:31],s46 offen lds\nM v[252:255],a[92:95],a[96:99],v[252:255]\ns_add_u32 m0,s50,53248\nds_read_b64_tr_b16 a[50:51],v10 offset:8192\nC v249,v250,v251\n" \
"M v[128:131],a[0:3],a[32:35],v[128:131]\ns_load_dwordx2 s[84:85],%0,0x50\nds_read_b128 a[80:83],v8 offset:0\nM v[160:163],a[0:3],a[36:39],v[160:163]\ns_load_dwordx2 s[88:89],%0,0x60\nds_read_b128 a[84:87],v8 offset:4096\nC v250,v252,v253\nM a[224:227],a[0:3],a[40:43],a[224:227]\n" \
"buffer_load_dwordx4 v18,s[28:31],s46 offen lds\nM a[192:195],a[0:3],a[44:47],a[192:195]\ns_add_u32 m0,s50,57344\nv_permlane16_swap_b32 v241,v243\nM a[196:199],a[4:7],a[44:47],a[196:199]\ns_load_dwordx2 s[12:13],%0,0x0\nds_read_b64_tr_b16 a[52:53],v11 offset:8192\nds_read_b64_tr_b16 a[54:55],v12 offset:8192\n" \
"M a[228:231],a[4:7],a[40:43],a[228:231]\ns_memrealtime s[10:11]\nds_read_b64_tr_b16 a[56:57],v9 offset:8704\nC v251,v254,v255\nM v[164:167],a[4:7],a[36:39],v[164:167]\nbuffer_load_dwordx4 v19,s[28:31],s46 offen lds\nM v[132:135],a[4:7],a[32:35],v[132:135]\ns_add_u32 m0,s50,61440\n" \
"ds_read_b64_tr_b16 a[58:59],v10 offset:8704\nM v[136:139],a[8:11],a[32:35],v[136:139]\nds_read_b64_tr_b16 a[60:61],v11 offset:8704\nM v[168:171],a[8:11],a[36:39],v[168:171]\nds_read_b64_tr_b16 a[62:63],v12 offset:8704\nM a[232:235],a[8:11],a[40:43],a[232:235]\nbuffer_load_dwordx4 v20,s[28:31],s46 offen lds\nM a[200:203],a[8:11],a[44:47],a[200:203]\n" \
"s_add_u32 m0,s51,49152\nds_read_b64_tr_b16 a[64:65],v9 offset:9216\nM a[204:207],a[12:15],a[44:47],a[204:207]\ns_mov_b32 s46,1152\nds_read_b64_tr_b16 a[66:67],v10 offset:9216\nM a[236:239],a[12:15],a[40:43],a[236:239]\nds_read_b64_tr_b16 a[68:69],v11 offset:9216\nds_read_b64_tr_b16 a[70:71],v12 offset:9216\n" \
"M v[172:175],a[12:15],a[36:39],v[172:175]\nbuffer_load_dwordx4 v21,s[32:35],s47 offen offset:0 lds\nM v[140:143],a[12:15],a[32:35],v[140:143]\ns_add_u32 m0,s51,50048\nv_permlane16_swap_b32 v248,v250\nM v[144:147],a[16:19],a[32:35],v[144:147]\nds_read_b64_tr_b16 a[72:73],v9 offset:9728\nds_read_b64_tr_b16 a[74:75],v10 offset:9728\n" \
"M v[176:179],a[16:19],a[36:39],v[176:179]\nds_read_b64_tr_b16 a[76:77],v11 offset:9728\nv_mov_b32 v58,s10\nM a[240:243],a[16:19],a[40:43],a[240:243]\ns_waitcnt lgkmcnt(5)\nds_read_b64_tr_b16 a[78:79],v12 offset:9728\nds_read_b128 a[88:91],v8 offset:8192\nds_read_b128 a[92:95],v8 offset:12288\n" \
"M a[208:211],a[16:19],a[44:47],a[208:211]\nbuffer_load_dwordx4 v21,s[32:35],s47 offen offset:128 lds\nM a[212:215],a[20:23],a[44:47],a[212:215]\ns_add_u32 m0,s51,57344\nv_permlane16_swap_b32 v249,v251\nM a[244:247],a[20:23],a[40:43],a[244:247]\ns_mov_b32 s47,4980736\nv_mov_b32 v59,s11\n" \
"M v[180:183],a[20:23],a[36:39],v[180:183]\nM v[148:151],a[20:23],a[32:35],v[148:151]\nbuffer_load_dwordx4 v21,s[32:35],s47 offen offset:0 lds\nM v[152:155],a[24:27],a[32:35],v[152:155]\ns_waitcnt vmcnt(16) lgkmcnt(0)\nM v[184:187],a[24:27],a[36:39],v[184:187]\ns_add_u32 m0,s51,58240\ns_barrier\n" \
"ds_read_b64_tr_b16 a[0:1],v9 offset:16384\nM a[248:251],a[24:27],a[40:43],a[248:251]\ns_lshl_b32 s62,s6,20\nds_read_b64_tr_b16 a[2:3],v10 offset:16384\nds_read_b64_tr_b16 a[4:5],v11 offset:16384\nM a[216:219],a[24:27],a[44:47],a[216:219]\nbuffer_store_dwordx4 v[224:227],v60,s[36:39],s21 offen offset:0 sc1\nM a[220:223],a[28:31],a[44:47],a[220:223]\n" \
"s_add_u32 s21,s21,1048576\nds_read_b128 a[44:47],v5 offset:12288\nds_read_b64_tr_b16 a[6:7],v12 offset:16384\nM a[252:255],a[28:31],a[40:43],a[252:255]\ns_add_u32 s62,s62,16777216\nds_read_b128 a[40:43],v5 offset:8192\nv_bfe_u32 v57,v4,4,3\nM v[188:191],a[28:31],a[36:39],v[188:191]\n" \
"s_add_u32 s40,s4,s62\ns_addc_u32 s41,s5,0\nds_read_b128 a[36:39],v5 offset:4096\nv_bfe_u32 v61,v4,4,4\nM v[156:159],a[28:31],a[32:35],v[156:159]\ns_lshl_b32 s62,s7,16\nds_read_b128 a[32:35],v5 offset:0\nv_mov_b32 v24,0\n" \
"M v[128:131],a[48:51],a[80:83],v[128:131]\ns_sub_u32 s63,983040,s62\nds_read_b64_tr_b16 a[8:9],v9 offset:16896\nv_mov_b32 v25,0\nM v[160:163],a[48:51],a[84:87],v[160:163]\ns_cmp_lt_u32 %2,2\ns_cselect_b32 s44,s63,s62\ns_cselect_b32 s42,1048576,s63\n" \
"ds_read_b64_tr_b16 a[10:11],v10 offset:16896\nv_mov_b32 v26,0\nM a[224:227],a[48:51],a[88:91],a[224:227]\ns_and_b32 s63,%2,1\nds_read_b64_tr_b16 a[12:13],v11 offset:16896\nds_read_b64_tr_b16 a[14:15],v12 offset:16896\nM a[192:195],a[48:51],a[92:95],a[192:195]\ns_lshl_b32 s63,s63,12\n" \
"ds_read_b64_tr_b16 a[16:17],v9 offset:17408\nv_add_u32 v22,s63,v4\nM a[196:199],a[52:55],a[92:95],a[196:199]\ns_and_b32 s41,s41,0xffff\nds_read_b64_tr_b16 a[18:19],v10 offset:17408\nv_mov_b32 v27,0\nM a[228:231],a[52:55],a[88:91],a[228:231]\ns_add_u32 s61,s44,32768\n" \
"ds_read_b64_tr_b16 a[20:21],v11 offset:17408\nds_read_b64_tr_b16 a[22:23],v12 offset:17408\nM v[164:167],a[52:55],a[84:87],v[164:167]\ns_lshl_b32 s62,s6,6\nds_read_b64_tr_b16 a[24:25],v9 offset:17920\nM v[132:135],a[52:55],a[80:83],v[132:135]\ns_add_u32 s62,s62,94375936\nds_read_b64_tr_b16 a[26:27],v10 offset:17920\n" \
"v_lshl_add_u32 v57,v57,3,s62\nM v[136:139],a[56:59],a[80:83],v[136:139]\ns_lshl_b32 s62,s7,1\nds_read_b64_tr_b16 a[28:29],v11 offset:17920\nds_read_b64_tr_b16 a[30:31],v12 offset:17920\nM v[168:171],a[56:59],a[84:87],v[168:171]\ns_mov_b32 s4,s62\nds_read_b64_tr_b16 a[48:49],v9 offset:24576\n" \
"M a[232:235],a[56:59],a[88:91],a[232:235]\ns_cmp_lt_u32 %2,2\ns_cselect_b32 s62,s62,50\ns_cselect_b32 s54,8192,0\nds_read_b64_tr_b16 a[50:51],v10 offset:24576\nM a[200:203],a[56:59],a[92:95],a[200:203]\nbuffer_load_dwordx4 v21,s[32:35],s47 offen offset:128 lds\nM a[204:207],a[60:63],a[92:95],a[204:207]\n" \
"s_add_u32 m0,s50,65536\nds_read_b64_tr_b16 a[52:53],v11 offset:24576\nM a[236:239],a[60:63],a[88:91],a[236:239]\ns_mov_b32 s47,5242880\nds_read_b64_tr_b16 a[54:55],v12 offset:24576\nM v[172:175],a[60:63],a[84:87],v[172:175]\ns_sub_u32 s55,s62,1\nds_read_b64_tr_b16 a[56:57],v9 offset:25088\n" \
"ds_read_b64_tr_b16 a[58:59],v10 offset:25088\nM v[140:143],a[60:63],a[80:83],v[140:143]\nbuffer_load_dwordx4 v13,s[28:31],s46 offen lds\nM v[144:147],a[64:67],a[80:83],v[144:147]\ns_add_u32 m0,s50,69632\nM v[176:179],a[64:67],a[84:87],v[176:179]\ns_lshl_b32 s70,s62,1\nds_read_b64_tr_b16 a[60:61],v11 offset:25088\n" \
"ds_read_b64_tr_b16 a[62:63],v12 offset:25088\nM a[240:243],a[64:67],a[88:91],a[240:243]\ns_cmp_eq_u32 s62,0\ns_cselect_b64 s[66:67],-1,0\nM a[208:211],a[64:67],a[92:95],a[208:211]\nbuffer_load_dwordx4 v14,s[28:31],s46 offen lds\nM a[212:215],a[68:71],a[92:95],a[212:215]\ns_add_u32 m0,s50,73728\n" \
"ds_read_b64_tr_b16 a[64:65],v9 offset:25600\nM a[244:247],a[68:71],a[88:91],a[244:247]\ns_lshl_b32 s62,s7,2\nds_read_b64_tr_b16 a[66:67],v10 offset:25600\nM v[180:183],a[68:71],a[84:87],v[180:183]\ns_sub_u32 s63,60,s62\nM v[148:151],a[68:71],a[80:83],v[148:151]\nbuffer_load_dwordx4 v15,s[28:31],s46 offen lds\n" \
"M v[152:155],a[72:75],a[80:83],v[152:155]\ns_add_u32 m0,s50,77824\nds_read_b64_tr_b16 a[68:69],v11 offset:25600\nM v[184:187],a[72:75],a[84:87],v[184:187]\ns_cmp_lt_u32 %2,2\ns_cselect_b32 s5,s62,s63\nds_read_b64_tr_b16 a[70:71],v12 offset:25600\nM a[248:251],a[72:75],a[88:91],a[248:251]\n" \
"s_and_b32 s63,%2,1\nM a[216:219],a[72:75],a[92:95],a[216:219]\nbuffer_load_dwordx4 v16,s[28:31],s46 offen lds\nM a[220:223],a[76:79],a[92:95],a[220:223]\ns_mov_b32 s46,1152\nds_read_b64_tr_b16 a[72:73],v9 offset:26112\nM a[252:255],a[76:79],a[88:91],a[252:255]\ns_add_u32 m0,s50,81920\n" \
"ds_read_b64_tr_b16 a[74:75],v10 offset:26112\nds_read_b128 a[88:91],v6 offset:8192\nM v[188:191],a[76:79],a[84:87],v[188:191]\ns_add_u32 s69,s62,s63\nds_read_b128 a[84:87],v6 offset:4096\nM v[156:159],a[76:79],a[80:83],v[156:159]\ns_waitcnt lgkmcnt(4)\nbuffer_load_dwordx4 v17,s[28:31],s46 offen lds\n" \
"M v[128:131],a[0:3],a[32:35],v[128:131]\ns_add_u32 m0,s50,86016\nM v[160:163],a[0:3],a[36:39],v[160:163]\ns_add_u32 s68,s69,s62\nds_read_b128 a[80:83],v6 offset:0\nM a[224:227],a[0:3],a[40:43],a[224:227]\ns_bfe_u32 s62,%1,0x20001\nds_read_b64_tr_b16 a[76:77],v11 offset:26112\n" \
"M a[192:195],a[0:3],a[44:47],a[192:195]\nbuffer_store_dwordx4 v[232:235],v60,s[36:39],s21 offen offset:0 sc1\nM a[196:199],a[4:7],a[44:47],a[196:199]\ns_add_u32 s21,s21,1048576\nds_read_b64_tr_b16 a[78:79],v12 offset:26112\nds_read_b128 a[92:95],v6 offset:12288\nM a[228:231],a[4:7],a[40:43],a[228:231]\ns_lshr_b32 s63,%1,5\n" \
"M v[164:167],a[4:7],a[36:39],v[164:167]\ns_lshl_b32 s62,s62,21\nM v[132:135],a[4:7],a[32:35],v[132:135]\ns_lshl_b32 s63,s63,8\nM v[136:139],a[8:11],a[32:35],v[136:139]\ns_add_u32 s62,s62,s63\nM v[168:171],a[8:11],a[36:39],v[168:171]\ns_sub_u32 s86,8388608,s62\n" \
"M a[232:235],a[8:11],a[40:43],a[232:235]\ns_add_u32 s84,s84,s62\ns_addc_u32 s85,s85,0\nM a[200:203],a[8:11],a[44:47],a[200:203]\ns_and_b32 s63,%2,1\nM a[204:207],a[12:15],a[44:47],a[204:207]\ns_lshl_b32 s62,s63,4\nM a[236:239],a[12:15],a[40:43],a[236:239]\n" \
"s_and_b32 s63,%2,1\nM v[172:175],a[12:15],a[36:39],v[172:175]\ns_lshl_b32 s63,s63,14\nM v[140:143],a[12:15],a[32:35],v[140:143]\ns_add_u32 s63,s63,0x20000\nM v[144:147],a[16:19],a[32:35],v[144:147]\ns_cmp_lt_u32 %2,2\ns_cselect_b32 s65,0,-4096\n" \
"M v[176:179],a[16:19],a[36:39],v[176:179]\ns_cmp_lt_u32 %2,2\ns_cselect_b32 s49,1,-1\ns_cselect_b32 s45,-1,60\nM a[240:243],a[16:19],a[40:43],a[240:243]\ns_lshl_b32 s45,s45,14\nM a[208:211],a[16:19],a[44:47],a[208:211]\nbuffer_load_dwordx4 v18,s[28:31],s46 offen lds\n" \
"M a[212:215],a[20:23],a[44:47],a[212:215]\ns_add_u32 m0,s50,90112\nM a[244:247],a[20:23],a[40:43],a[244:247]\ns_lshl_b32 s49,s49,14\nM v[180:183],a[20:23],a[36:39],v[180:183]\ns_add_u32 s48,s45,8192\nM v[148:151],a[20:23],a[32:35],v[148:151]\nbuffer_load_dwordx4 v19,s[28:31],s46 offen lds\n" \
"M v[152:155],a[24:27],a[32:35],v[152:155]\ns_waitcnt vmcnt(13) lgkmcnt(0)\nM v[184:187],a[24:27],a[36:39],v[184:187]\ns_add_u32 m0,s50,94208\ns_barrier\nds_read_b64_tr_b16 a[0:1],v9 offset:32768\nM a[248:251],a[24:27],a[40:43],a[248:251]\ns_add_u32 s45,s45,s49\n" \
"ds_read_b64_tr_b16 a[2:3],v10 offset:32768\nds_read_b64_tr_b16 a[4:5],v11 offset:32768\nM a[216:219],a[24:27],a[44:47],a[216:219]\nbuffer_load_dwordx4 v20,s[28:31],s46 offen lds\nM a[220:223],a[28:31],a[44:47],a[220:223]\ns_add_u32 m0,s51,0\nds_read_b128 a[44:47],v5 offset:45056\nM a[252:255],a[28:31],a[40:43],a[252:255]\n" \
"s_mov_b32 s46,1280\nds_read_b128 a[40:43],v5 offset:40960\nM v[188:191],a[28:31],a[36:39],v[188:191]\ns_add_u32 s48,s48,s49\nds_read_b128 a[36:39],v5 offset:36864\nM v[156:159],a[28:31],a[32:35],v[156:159]\nbuffer_load_dwordx4 v21,s[32:35],s47 offen offset:0 lds\nM v[128:131],a[48:51],a[80:83],v[128:131]\n" \
"s_add_u32 m0,s51,896\nM v[160:163],a[48:51],a[84:87],v[160:163]\ns_mov_b32 s52,-1\nds_read_b128 a[32:35],v5 offset:32768\nM a[224:227],a[48:51],a[88:91],a[224:227]\ns_min_u32 s56,s4,8\nds_read_b64_tr_b16 a[6:7],v12 offset:32768\nM a[192:195],a[48:51],a[92:95],a[192:195]\n" \
"buffer_load_dwordx4 v21,s[32:35],s47 offen offset:128 lds\nM a[196:199],a[52:55],a[92:95],a[196:199]\ns_add_u32 m0,s51,8192\nds_read_b64_tr_b16 a[8:9],v9 offset:33280\nM a[228:231],a[52:55],a[88:91],a[228:231]\ns_mov_b32 s47,5505024\nds_read_b64_tr_b16 a[10:11],v10 offset:33280\nM v[164:167],a[52:55],a[84:87],v[164:167]\n" \
"s_mov_b32 s87,0x00020000\nds_read_b64_tr_b16 a[12:13],v11 offset:33280\nds_read_b64_tr_b16 a[14:15],v12 offset:33280\nM v[132:135],a[52:55],a[80:83],v[132:135]\nbuffer_load_dwordx4 v21,s[32:35],s47 offen offset:0 lds\nM v[136:139],a[56:59],a[80:83],v[136:139]\ns_add_u32 m0,s51,9088\nM v[168:171],a[56:59],a[84:87],v[168:171]\n" \
"s_and_b32 s85,s85,0xffff\nds_read_b64_tr_b16 a[16:17],v9 offset:33792\nds_read_b64_tr_b16 a[18:19],v10 offset:33792\nM a[232:235],a[56:59],a[88:91],a[232:235]\ns_nop 0\ns_lshl_b32 s72,%1,2\nds_read_b64_tr_b16 a[20:21],v11 offset:33792\nM a[200:203],a[56:59],a[92:95],a[200:203]\n" \
"buffer_load_dwordx4 v21,s[32:35],s47 offen offset:128 lds\nM a[204:207],a[60:63],a[92:95],a[204:207]\ns_add_u32 m0,s50,0\nds_read_b64_tr_b16 a[22:23],v12 offset:33792\nM a[236:239],a[60:63],a[88:91],a[236:239]\ns_mov_b32 s47,5767168\nds_read_b64_tr_b16 a[24:25],v9 offset:34304\nM v[172:175],a[60:63],a[84:87],v[172:175]\n" \
"s_add_u32 s72,s72,%2\nds_read_b64_tr_b16 a[26:27],v10 offset:34304\nM v[140:143],a[60:63],a[80:83],v[140:143]\nbuffer_load_dwordx4 v13,s[28:31],s46 offen lds\nM v[144:147],a[64:67],a[80:83],v[144:147]\ns_add_u32 m0,s50,4096\nds_read_b64_tr_b16 a[28:29],v11 offset:34304\nM v[176:179],a[64:67],a[84:87],v[176:179]\n" \
"s_mul_i32 s72,s72,12288\nds_read_b64_tr_b16 a[30:31],v12 offset:34304\nM a[240:243],a[64:67],a[88:91],a[240:243]\ns_add_u32 s72,s72,109060096\nds_read_b64_tr_b16 a[48:49],v9 offset:40960\nds_read_b64_tr_b16 a[50:51],v10 offset:40960\nM a[208:211],a[64:67],a[92:95],a[208:211]\nbuffer_load_dwordx4 v14,s[28:31],s46 offen lds\n" \
"M a[212:215],a[68:71],a[92:95],a[212:215]\ns_add_u32 m0,s50,8192\nM a[244:247],a[68:71],a[88:91],a[244:247]\ns_lshl_b32 s68,s68,5\nds_read_b64_tr_b16 a[52:53],v11 offset:40960\nds_read_b64_tr_b16 a[54:55],v12 offset:40960\nM v[180:183],a[68:71],a[84:87],v[180:183]\ns_add_u32 s68,s68,s6\n" \
"ds_read_b64_tr_b16 a[56:57],v9 offset:41472\nM v[148:151],a[68:71],a[80:83],v[148:151]\nbuffer_load_dwordx4 v15,s[28:31],s46 offen lds\nM v[152:155],a[72:75],a[80:83],v[152:155]\ns_add_u32 m0,s50,12288\nds_read_b64_tr_b16 a[58:59],v10 offset:41472\nM v[184:187],a[72:75],a[84:87],v[184:187]\ns_lshl_b32 s68,s68,12\n" \
"ds_read_b64_tr_b16 a[60:61],v11 offset:41472\nM a[248:251],a[72:75],a[88:91],a[248:251]\ns_add_u32 s68,s68,50331648\nds_read_b64_tr_b16 a[62:63],v12 offset:41472\nM a[216:219],a[72:75],a[92:95],a[216:219]\nbuffer_load_dwordx4 v16,s[28:31],s46 offen lds\nM a[220:223],a[76:79],a[92:95],a[220:223]\ns_mov_b32 s46,1280\n" \
"ds_read_b64_tr_b16 a[64:65],v9 offset:41984\nM a[252:255],a[76:79],a[88:91],a[252:255]\ns_add_u32 m0,s50,16384\nds_read_b64_tr_b16 a[66:67],v10 offset:41984\nM v[188:191],a[76:79],a[84:87],v[188:191]\ns_lshl_b32 s69,s69,5\nds_read_b128 a[84:87],v6 offset:36864\nM v[156:159],a[76:79],a[80:83],v[156:159]\n" \
"s_waitcnt lgkmcnt(4)\nbuffer_store_dwordx4 v[240:243],v60,s[36:39],s21 offen offset:0 sc1\nM v[128:131],a[0:3],a[32:35],v[128:131]\ns_add_u32 s21,s21,1048576\nds_read_b128 a[80:83],v6 offset:32768\nds_read_b64_tr_b16 a[68:69],v11 offset:41984\nM v[160:163],a[0:3],a[36:39],v[160:163]\ns_add_u32 s69,s69,s6\n" \
"ds_read_b64_tr_b16 a[70:71],v12 offset:41984\nM a[224:227],a[0:3],a[40:43],a[224:227]\ns_lshl_b32 s69,s69,3\nds_read_b64_tr_b16 a[72:73],v9 offset:42496\nM a[192:195],a[0:3],a[44:47],a[192:195]\ns_add_u32 s69,s69,94388224\nds_read_b64_tr_b16 a[74:75],v10 offset:42496\nds_read_b64_tr_b16 a[76:77],v11 offset:42496\n" \
"M a[196:199],a[4:7],a[44:47],a[196:199]\ns_and_b32 s89,s89,0xffff\nds_read_b64_tr_b16 a[78:79],v12 offset:42496\nM a[228:231],a[4:7],a[40:43],a[228:231]\ns_mov_b32 s90,4194304\nds_read_b128 a[88:91],v6 offset:40960\nM v[164:167],a[4:7],a[36:39],v[164:167]\ns_mov_b32 s91,0x00020000\n" \
"ds_read_b128 a[92:95],v6 offset:45056\nM v[132:135],a[4:7],a[32:35],v[132:135]\ns_lshl_b32 s71,s6,20\nM v[136:139],a[8:11],a[32:35],v[136:139]\ns_mov_b32 s75,0\nM v[168:171],a[8:11],a[36:39],v[168:171]\ns_add_u32 s71,s71,16777216\nM a[232:235],a[8:11],a[40:43],a[232:235]\n" \
"M a[200:203],a[8:11],a[44:47],a[200:203]\nM a[204:207],a[12:15],a[44:47],a[204:207]\nM a[236:239],a[12:15],a[40:43],a[236:239]\nM v[172:175],a[12:15],a[36:39],v[172:175]\nM v[140:143],a[12:15],a[32:35],v[140:143]\nbuffer_load_dwordx4 v17,s[28:31],s46 offen lds\nM v[144:147],a[16:19],a[32:35],v[144:147]\ns_add_u32 m0,s50,20480\n" \
"M v[176:179],a[16:19],a[36:39],v[176:179]\nM a[240:243],a[16:19],a[40:43],a[240:243]\nM a[208:211],a[16:19],a[44:47],a[208:211]\nbuffer_load_dwordx4 v18,s[28:31],s46 offen lds\nM a[212:215],a[20:23],a[44:47],a[212:215]\ns_add_u32 m0,s50,24576\nM a[244:247],a[20:23],a[40:43],a[244:247]\nM v[180:183],a[20:23],a[36:39],v[180:183]\n" \
"M v[148:151],a[20:23],a[32:35],v[148:151]\nbuffer_load_dwordx4 v19,s[28:31],s46 offen lds\nM v[152:155],a[24:27],a[32:35],v[152:155]\ns_waitcnt vmcnt(12) lgkmcnt(0)\nM v[184:187],a[24:27],a[36:39],v[184:187]\ns_add_u32 m0,s50,28672\ns_barrier\nds_read_b64_tr_b16 a[0:1],v9 offset:49152\n" \
"M a[248:251],a[24:27],a[40:43],a[248:251]\nds_read_b64_tr_b16 a[2:3],v10 offset:49152\nds_read_b64_tr_b16 a[4:5],v11 offset:49152\nM a[216:219],a[24:27],a[44:47],a[216:219]\nbuffer_load_dwordx4 v20,s[28:31],s46 offen lds\nM a[220:223],a[28:31],a[44:47],a[220:223]\ns_add_u32 m0,s51,16384\nds_read_b128 a[44:47],v7 offset:12288\n" \
"M a[252:255],a[28:31],a[40:43],a[252:255]\ns_mov_b32 s46,1408\nds_read_b128 a[40:43],v7 offset:8192\nM v[188:191],a[28:31],a[36:39],v[188:191]\nds_read_b128 a[36:39],v7 offset:4096\nM v[156:159],a[28:31],a[32:35],v[156:159]\nbuffer_load_dwordx4 v21,s[32:35],s47 offen offset:0 lds\nM v[128:131],a[48:51],a[80:83],v[128:131]\n" \
"s_add_u32 m0,s51,17280\nM v[160:163],a[48:51],a[84:87],v[160:163]\nds_read_b128 a[32:35],v7 offset:0\nM a[224:227],a[48:51],a[88:91],a[224:227]\nds_read_b64_tr_b16 a[6:7],v12 offset:49152\nM a[192:195],a[48:51],a[92:95],a[192:195]\nbuffer_load_dwordx4 v21,s[32:35],s47 offen offset:128 lds\nM a[196:199],a[52:55],a[92:95],a[196:199]\n" \
"s_add_u32 m0,s51,24576\nds_read_b64_tr_b16 a[8:9],v9 offset:49664\nM a[228:231],a[52:55],a[88:91],a[228:231]\ns_mov_b32 s47,6029312\nds_read_b64_tr_b16 a[10:11],v10 offset:49664\nM v[164:167],a[52:55],a[84:87],v[164:167]\nds_read_b64_tr_b16 a[12:13],v11 offset:49664\nds_read_b64_tr_b16 a[14:15],v12 offset:49664\n" \
"M v[132:135],a[52:55],a[80:83],v[132:135]\nbuffer_load_dwordx4 v21,s[32:35],s47 offen offset:0 lds\nM v[136:139],a[56:59],a[80:83],v[136:139]\ns_add_u32 m0,s51,25472\nM v[168:171],a[56:59],a[84:87],v[168:171]\nds_read_b64_tr_b16 a[16:17],v9 offset:50176\nds_read_b64_tr_b16 a[18:19],v10 offset:50176\nM a[232:235],a[56:59],a[88:91],a[232:235]\n" \
"ds_read_b64_tr_b16 a[20:21],v11 offset:50176\nM a[200:203],a[56:59],a[92:95],a[200:203]\nbuffer_store_dwordx4 v[92:95],v4,s[36:39],s96 offen offset:1024 sc1\nM a[204:207],a[60:63],a[92:95],a[204:207]\nds_read_b64_tr_b16 a[22:23],v12 offset:50176\nds_read_b64_tr_b16 a[24:25],v9 offset:50688\nM a[236:239],a[60:63],a[88:91],a[236:239]\nds_read_b64_tr_b16 a[26:27],v10 offset:50688\n" \
"M v[172:175],a[60:63],a[84:87],v[172:175]\nds_read_b64_tr_b16 a[28:29],v11 offset:50688\nds_read_b64_tr_b16 a[30:31],v12 offset:50688\nM v[140:143],a[60:63],a[80:83],v[140:143]\nbuffer_load_dwordx4 v21,s[32:35],s47 offen offset:128 lds\nM v[144:147],a[64:67],a[80:83],v[144:147]\ns_add_u32 m0,s50,32768\nM v[176:179],a[64:67],a[84:87],v[176:179]\n" \
"s_mov_b32 s47,6291456\nds_read_b64_tr_b16 a[48:49],v9 offset:57344\nds_read_b64_tr_b16 a[50:51],v10 offset:57344\nM a[240:243],a[64:67],a[88:91],a[240:243]\nds_read_b64_tr_b16 a[52:53],v11 offset:57344\nM a[208:211],a[64:67],a[92:95],a[208:211]\nbuffer_load_dwordx4 v13,s[28:31],s46 offen lds\nM a[212:215],a[68:71],a[92:95],a[212:215]\n" \
"s_add_u32 m0,s50,36864\nds_read_b64_tr_b16 a[54:55],v12 offset:57344\nM a[244:247],a[68:71],a[88:91],a[244:247]\nds_read_b64_tr_b16 a[56:57],v9 offset:57856\nM v[180:183],a[68:71],a[84:87],v[180:183]\nds_read_b64_tr_b16 a[58:59],v10 offset:57856\nM v[148:151],a[68:71],a[80:83],v[148:151]\nbuffer_load_dwordx4 v14,s[28:31],s46 offen lds\n" \
"M v[152:155],a[72:75],a[80:83],v[152:155]\ns_add_u32 m0,s50,40960\nds_read_b64_tr_b16 a[60:61],v11 offset:57856\nM v[184:187],a[72:75],a[84:87],v[184:187]\nds_read_b64_tr_b16 a[62:63],v12 offset:57856\nM a[248:251],a[72:75],a[88:91],a[248:251]\nds_read_b64_tr_b16 a[64:65],v9 offset:58368\nds_read_b64_tr_b16 a[66:67],v10 offset:58368\n" \
"M a[216:219],a[72:75],a[92:95],a[216:219]\nbuffer_load_dwordx4 v15,s[28:31],s46 offen lds\nM a[220:223],a[76:79],a[92:95],a[220:223]\ns_add_u32 m0,s50,45056\nM a[252:255],a[76:79],a[88:91],a[252:255]\nds_read_b64_tr_b16 a[68:69],v11 offset:58368\nds_read_b64_tr_b16 a[70:71],v12 offset:58368\nM v[188:191],a[76:79],a[84:87],v[188:191]\n" \
"s_waitcnt lgkmcnt(5)\nds_read_b128 a[84:87],v8 offset:4096\nM v[156:159],a[76:79],a[80:83],v[156:159]\nbuffer_load_dwordx4 v16,s[28:31],s46 offen lds\nM v[128:131],a[0:3],a[32:35],v[128:131]\ns_mov_b32 s46,1408\nM v[160:163],a[0:3],a[36:39],v[160:163]\ns_add_u32 m0,s50,49152\n" \
"ds_read_b128 a[80:83],v8 offset:0\nM a[224:227],a[0:3],a[40:43],a[224:227]\nds_read_b64_tr_b16 a[72:73],v9 offset:58880\nds_read_b64_tr_b16 a[74:75],v10 offset:58880\nM a[192:195],a[0:3],a[44:47],a[192:195]\nbuffer_load_dwordx4 v17,s[28:31],s46 offen lds\nM a[196:199],a[4:7],a[44:47],a[196:199]\ns_add_u32 m0,s50,53248\n" \
"M a[228:231],a[4:7],a[40:43],a[228:231]\nds_read_b64_tr_b16 a[76:77],v11 offset:58880\nds_read_b64_tr_b16 a[78:79],v12 offset:58880\nM v[164:167],a[4:7],a[36:39],v[164:167]\nds_read_b128 a[88:91],v8 offset:8192\nM v[132:135],a[4:7],a[32:35],v[132:135]\nbuffer_load_dwordx4 v18,s[28:31],s46 offen lds\nM v[136:139],a[8:11],a[32:35],v[136:139]\n" \
"s_add_u32 m0,s50,57344\nM v[168:171],a[8:11],a[36:39],v[168:171]\nds_read_b128 a[92:95],v8 offset:12288\nM a[232:235],a[8:11],a[40:43],a[232:235]\nM a[200:203],a[8:11],a[44:47],a[200:203]\nbuffer_load_dwordx4 v19,s[28:31],s46 offen lds\nM a[204:207],a[12:15],a[44:47],a[204:207]\ns_add_u32 m0,s50,61440\n" \
"M a[236:239],a[12:15],a[40:43],a[236:239]\nM v[172:175],a[12:15],a[36:39],v[172:175]\nM v[140:143],a[12:15],a[32:35],v[140:143]\nbuffer_load_dwordx4 v20,s[28:31],s46 offen lds\nM v[144:147],a[16:19],a[32:35],v[144:147]\ns_add_u32 m0,s51,32768\nM v[176:179],a[16:19],a[36:39],v[176:179]\ns_mov_b32 s46,1536\n" \
"M a[240:243],a[16:19],a[40:43],a[240:243]\nM a[208:211],a[16:19],a[44:47],a[208:211]\nbuffer_load_dwordx4 v21,s[32:35],s47 offen offset:0 lds\nM a[212:215],a[20:23],a[44:47],a[212:215]\ns_add_u32 m0,s51,33664\nM a[244:247],a[20:23],a[40:43],a[244:247]\nM v[180:183],a[20:23],a[36:39],v[180:183]\nM v[148:151],a[20:23],a[32:35],v[148:151]\n" \
"buffer_store_dwordx4 v[248:251],v60,s[36:39],s21 offen offset:0 sc1\nM v[152:155],a[24:27],a[32:35],v[152:155]\ns_waitcnt vmcnt(15) lgkmcnt(0)\nv_lshrrev_b32 v60,8,v4\nv_lshlrev_b32 v60,2,v60\nM v[184:187],a[24:27],a[36:39],v[184:187]\ns_barrier\nds_read_b64_tr_b16 a[0:1],v9 offset:0\n" \
"M a[248:251],a[24:27],a[40:43],a[248:251]\nds_read_b64_tr_b16 a[2:3],v10 offset:0\nds_read_b64_tr_b16 a[4:5],v11 offset:0\nM a[216:219],a[24:27],a[44:47],a[216:219]\nds_read_b64_tr_b16 a[6:7],v12 offset:0\nv_sub_u32 v60,v60,v61\nM a[220:223],a[28:31],a[44:47],a[220:223]\nds_read_b128 a[44:47],v5 offset:12288\n" \
"v_subrev_u32 v60,s62,v60\nM a[252:255],a[28:31],a[40:43],a[252:255]\ns_xor_b32 s62,s22,0x2000\nds_read_b128 a[40:43],v5 offset:8192\nv_mov_b32 v61,0xff800000\nM v[188:191],a[28:31],a[36:39],v[188:191]\ns_add_u32 s62,s62,84992\nds_read_b128 a[36:39],v5 offset:4096\n" \
"v_add_u32 v23,s62,v4\nM v[156:159],a[28:31],a[32:35],v[156:159]\ns_and_b32 s62,%2,1\nds_read_b128 a[32:35],v5 offset:0\nv_cmp_lt_i32 vcc,0,v60\nM v[128:131],a[48:51],a[80:83],v[128:131]\ns_lshl_b32 s62,s62,15\nds_read_b64_tr_b16 a[8:9],v9 offset:512\n" \
"ds_read_b64_tr_b16 a[10:11],v10 offset:512\nM v[160:163],a[48:51],a[84:87],v[160:163]\ns_add_u32 s62,s62,s54\nds_read_b64_tr_b16 a[12:13],v11 offset:512\nv_cndmask_b32 v48,0,v61,vcc\nM a[224:227],a[48:51],a[88:91],a[224:227]\ns_add_u32 s62,s62,32768\nds_read_b64_tr_b16 a[14:15],v12 offset:512\n" \
"v_add_u32 v1,s62,v4\nM a[192:195],a[48:51],a[92:95],a[192:195]\nds_read_b64_tr_b16 a[16:17],v9 offset:1024\nds_read_b64_tr_b16 a[18:19],v10 offset:1024\nM a[196:199],a[52:55],a[92:95],a[196:199]\nds_read_b64_tr_b16 a[20:21],v11 offset:1024\nv_cmp_lt_i32 vcc,-1,v60\nM a[228:231],a[52:55],a[88:91],a[228:231]\n" \
"ds_read_b64_tr_b16 a[22:23],v12 offset:1024\nv_cndmask_b32 v49,0,v61,vcc\nM v[164:167],a[52:55],a[84:87],v[164:167]\nds_read_b64_tr_b16 a[24:25],v9 offset:1536\nds_read_b64_tr_b16 a[26:27],v10 offset:1536\nM v[132:135],a[52:55],a[80:83],v[132:135]\nbuffer_load_dwordx4 v21,s[32:35],s47 offen offset:128 lds\nM v[136:139],a[56:59],a[80:83],v[136:139]\n" \
"s_add_u32 m0,s51,40960\nv_cmp_lt_i32 vcc,-2,v60\nM v[168:171],a[56:59],a[84:87],v[168:171]\ns_mov_b32 s47,6553600\nds_read_b64_tr_b16 a[28:29],v11 offset:1536\nds_read_b64_tr_b16 a[30:31],v12 offset:1536\nM a[232:235],a[56:59],a[88:91],a[232:235]\nds_read_b64_tr_b16 a[48:49],v9 offset:8192\n" \
"v_cndmask_b32 v50,0,v61,vcc\nM a[200:203],a[56:59],a[92:95],a[200:203]\ns_mov_b64 exec,0xff\nglobal_load_dword v0,v2,s[100:101]\ns_mov_b64 exec,-1\nM a[204:207],a[60:63],a[92:95],a[204:207]\nds_read_b64_tr_b16 a[50:51],v10 offset:8192\nds_read_b64_tr_b16 a[52:53],v11 offset:8192\n" \
"M a[236:239],a[60:63],a[88:91],a[236:239]\nbuffer_load_dwordx4 v21,s[32:35],s47 offen offset:0 lds\nM v[172:175],a[60:63],a[84:87],v[172:175]\ns_add_u32 m0,s51,41856\nds_read_b64_tr_b16 a[54:55],v12 offset:8192\nv_cmp_lt_i32 vcc,-3,v60\nM v[140:143],a[60:63],a[80:83],v[140:143]\nds_read_b64_tr_b16 a[56:57],v9 offset:8704\n" \
"v_cndmask_b32 v51,0,v61,vcc\nM v[144:147],a[64:67],a[80:83],v[144:147]\nds_read_b64_tr_b16 a[58:59],v10 offset:8704\nv_cmp_lt_i32 vcc,-16,v60\nM v[176:179],a[64:67],a[84:87],v[176:179]\nbuffer_store_dwordx4 v[104:107],v4,s[36:39],s96 offen offset:2048 sc1\nM a[240:243],a[64:67],a[88:91],a[240:243]\nds_read_b64_tr_b16 a[60:61],v11 offset:8704\n" \
"ds_read_b64_tr_b16 a[62:63],v12 offset:8704\nM a[208:211],a[64:67],a[92:95],a[208:211]\nds_read_b64_tr_b16 a[64:65],v9 offset:9216\nv_cndmask_b32 v52,0,v61,vcc\nM a[212:215],a[68:71],a[92:95],a[212:215]\nds_read_b64_tr_b16 a[66:67],v10 offset:9216\nv_cmp_lt_i32 vcc,-17,v60\nM a[244:247],a[68:71],a[88:91],a[244:247]\n" \
"buffer_load_dwordx4 v21,s[32:35],s47 offen offset:128 lds\nM v[180:183],a[68:71],a[84:87],v[180:183]\ns_add_u32 m0,s50,65536\nv_cndmask_b32 v53,0,v61,vcc\nv_cmp_lt_i32 vcc,-18,v60\nM v[148:151],a[68:71],a[80:83],v[148:151]\ns_mov_b32 s47,6815744\nds_read_b64_tr_b16 a[68:69],v11 offset:9216\n" \
"ds_read_b64_tr_b16 a[70:71],v12 offset:9216\nM v[152:155],a[72:75],a[80:83],v[152:155]\nv_cndmask_b32 v54,0,v61,vcc\nv_cmp_lt_i32 vcc,-19,v60\nM v[184:187],a[72:75],a[84:87],v[184:187]\nbuffer_load_dwordx4 v13,s[28:31],s46 offen lds\nM a[248:251],a[72:75],a[88:91],a[248:251]\ns_add_u32 m0,s50,69632\n" \
"v_cndmask_b32 v55,0,v61,vcc\nM a[216:219],a[72:75],a[92:95],a[216:219]\nds_read_b64_tr_b16 a[72:73],v9 offset:9728\nds_read_b64_tr_b16 a[74:75],v10 offset:9728\nM a[220:223],a[76:79],a[92:95],a[220:223]\nds_read_b128 a[92:95],v6 offset:12288\nM a[252:255],a[76:79],a[88:91],a[252:255]\nbuffer_load_dwordx4 v14,s[28:31],s46 offen lds\n" \
"M v[188:191],a[76:79],a[84:87],v[188:191]\ns_add_u32 m0,s50,73728\nds_read_b128 a[84:87],v6 offset:4096\nM v[156:159],a[76:79],a[80:83],v[156:159]\ns_waitcnt lgkmcnt(4)\nds_read_b128 a[80:83],v6 offset:0\nM v[128:131],a[0:3],a[32:35],v[128:131]\nds_read_b64_tr_b16 a[76:77],v11 offset:9728\n" \
"M v[160:163],a[0:3],a[36:39],v[160:163]\nbuffer_load_dwordx4 v15,s[28:31],s46 offen lds\nM a[224:227],a[0:3],a[40:43],a[224:227]\ns_add_u32 m0,s50,77824\nM a[192:195],a[0:3],a[44:47],a[192:195]\nds_read_b64_tr_b16 a[78:79],v12 offset:9728\nds_read_b128 a[88:91],v6 offset:8192\nM a[196:199],a[4:7],a[44:47],a[196:199]\n" \
"M a[228:231],a[4:7],a[40:43],a[228:231]\nbuffer_load_dwordx4 v16,s[28:31],s46 offen lds\nM v[164:167],a[4:7],a[36:39],v[164:167]\ns_add_u32 m0,s50,81920\nM v[132:135],a[4:7],a[32:35],v[132:135]\ns_mov_b32 s46,1536\nM v[136:139],a[8:11],a[32:35],v[136:139]\nM v[168:171],a[8:11],a[36:39],v[168:171]\n" \
"buffer_load_dwordx4 v17,s[28:31],s46 offen lds\nM a[232:235],a[8:11],a[40:43],a[232:235]\ns_add_u32 m0,s50,86016\nM a[200:203],a[8:11],a[44:47],a[200:203]\nM a[204:207],a[12:15],a[44:47],a[204:207]\nM a[236:239],a[12:15],a[40:43],a[236:239]\nbuffer_load_dwordx4 v18,s[28:31],s46 offen lds\nM v[172:175],a[12:15],a[36:39],v[172:175]\n" \
"s_add_u32 m0,s50,90112\nM v[140:143],a[12:15],a[32:35],v[140:143]\nM v[144:147],a[16:19],a[32:35],v[144:147]\nM v[176:179],a[16:19],a[36:39],v[176:179]\nbuffer_load_dwordx4 v19,s[28:31],s46 offen lds\nM a[240:243],a[16:19],a[40:43],a[240:243]\ns_add_u32 m0,s50,94208\nM a[208:211],a[16:19],a[44:47],a[208:211]\n" \
"M a[212:215],a[20:23],a[44:47],a[212:215]\nM a[244:247],a[20:23],a[40:43],a[244:247]\nbuffer_load_dwordx4 v20,s[28:31],s46 offen lds\nM v[180:183],a[20:23],a[36:39],v[180:183]\ns_add_u32 m0,s51,49152\nM v[148:151],a[20:23],a[32:35],v[148:151]\ns_mov_b32 s46,1664\nM v[152:155],a[24:27],a[32:35],v[152:155]\n" \
"s_waitcnt vmcnt(15) lgkmcnt(0)\nM v[184:187],a[24:27],a[36:39],v[184:187]\ns_barrier\nbuffer_store_dwordx4 v[108:111],v4,s[36:39],s96 offen offset:3072 sc1\nM a[248:251],a[24:27],a[40:43],a[248:251]\ns_add_u32 s96,s57,53248\nds_read_b64_tr_b16 a[0:1],v9 offset:16384\nds_read_b64_tr_b16 a[2:3],v10 offset:16384\n" \
"M a[216:219],a[24:27],a[44:47],a[216:219]\nds_read_b64_tr_b16 a[4:5],v11 offset:16384\nM a[220:223],a[28:31],a[44:47],a[220:223]\nds_read_b128 a[44:47],v5 offset:45056\nM a[252:255],a[28:31],a[40:43],a[252:255]\nbuffer_load_dwordx4 v21,s[32:35],s47 offen offset:0 lds\nM v[188:191],a[28:31],a[36:39],v[188:191]\ns_add_u32 m0,s51,50048\n" \
"ds_read_b128 a[36:39],v5 offset:36864\nM v[156:159],a[28:31],a[32:35],v[156:159]\nds_read_b128 a[32:35],v5 offset:32768\nM v[128:131],a[48:51],a[80:83],v[128:131]\nds_read_b128 a[40:43],v5 offset:40960\nM v[160:163],a[48:51],a[84:87],v[160:163]\nbuffer_load_dwordx4 v21,s[32:35],s47 offen offset:128 lds\nM a[224:227],a[48:51],a[88:91],a[224:227]\n" \
"s_add_u32 m0,s51,57344\nM a[192:195],a[48:51],a[92:95],a[192:195]\ns_mov_b32 s47,7077888\nds_read_b64_tr_b16 a[6:7],v12 offset:16384\nds_read_b64_tr_b16 a[8:9],v9 offset:16896\nM a[196:199],a[52:55],a[92:95],a[196:199]\nds_read_b64_tr_b16 a[10:11],v10 offset:16896\nM a[228:231],a[52:55],a[88:91],a[228:231]\n" \
"buffer_load_dwordx4 v21,s[32:35],s47 offen offset:0 lds\nM v[164:167],a[52:55],a[84:87],v[164:167]\ns_add_u32 m0,s51,58240\nds_read_b64_tr_b16 a[12:13],v11 offset:16896\nM v[132:135],a[52:55],a[80:83],v[132:135]\nds_read_b64_tr_b16 a[14:15],v12 offset:16896\nM v[136:139],a[56:59],a[80:83],v[136:139]\nds_read_b64_tr_b16 a[16:17],v9 offset:17408\n" \
"M v[168:171],a[56:59],a[84:87],v[168:171]\nbuffer_load_dwordx4 v21,s[32:35],s47 offen offset:128 lds\nM a[232:235],a[56:59],a[88:91],a[232:235]\ns_add_u32 m0,s50,0\nds_read_b64_tr_b16 a[18:19],v10 offset:17408\nM a[200:203],a[56:59],a[92:95],a[200:203]\ns_mov_b32 s47,7340032\nds_read_b64_tr_b16 a[20:21],v11 offset:17408\n" \
"M a[204:207],a[60:63],a[92:95],a[204:207]\nds_read_b64_tr_b16 a[22:23],v12 offset:17408\nds_read_b64_tr_b16 a[24:25],v9 offset:17920\nM a[236:239],a[60:63],a[88:91],a[236:239]\nbuffer_load_dwordx4 v13,s[28:31],s46 offen lds\nM v[172:175],a[60:63],a[84:87],v[172:175]\ns_add_u32 m0,s50,4096\nM v[140:143],a[60:63],a[80:83],v[140:143]\n" \
"ds_read_b64_tr_b16 a[26:27],v10 offset:17920\nds_read_b64_tr_b16 a[28:29],v11 offset:17920\nM v[144:147],a[64:67],a[80:83],v[144:147]\nds_read_b64_tr_b16 a[30:31],v12 offset:17920\nM v[176:179],a[64:67],a[84:87],v[176:179]\nbuffer_load_dwordx4 v14,s[28:31],s46 offen lds\nM a[240:243],a[64:67],a[88:91],a[240:243]\ns_add_u32 m0,s50,8192\n" \
"ds_read_b64_tr_b16 a[48:49],v9 offset:24576\nM a[208:211],a[64:67],a[92:95],a[208:211]\nds_read_b64_tr_b16 a[50:51],v10 offset:24576\nM a[212:215],a[68:71],a[92:95],a[212:215]\nds_read_b64_tr_b16 a[52:53],v11 offset:24576\nM a[244:247],a[68:71],a[88:91],a[244:247]\nbuffer_load_dwordx4 v15,s[28:31],s46 offen lds\nM v[180:183],a[68:71],a[84:87],v[180:183]\n" \
"s_add_u32 m0,s50,12288\nds_read_b64_tr_b16 a[54:55],v12 offset:24576\nM v[148:151],a[68:71],a[80:83],v[148:151]\nds_read_b64_tr_b16 a[56:57],v9 offset:25088\nM v[152:155],a[72:75],a[80:83],v[152:155]\nds_read_b64_tr_b16 a[58:59],v10 offset:25088\nds_read_b64_tr_b16 a[60:61],v11 offset:25088\nM v[184:187],a[72:75],a[84:87],v[184:187]\n" \
"buffer_load_dwordx4 v16,s[28:31],s46 offen lds\nM a[248:251],a[72:75],a[88:91],a[248:251]\ns_mov_b32 s46,1664\nM a[216:219],a[72:75],a[92:95],a[216:219]\ns_add_u32 m0,s50,16384\nds_read_b64_tr_b16 a[62:63],v12 offset:25088\nds_read_b64_tr_b16 a[64:65],v9 offset:25600\nM a[220:223],a[76:79],a[92:95],a[220:223]\n" \
"ds_read_b64_tr_b16 a[66:67],v10 offset:25600\nM a[252:255],a[76:79],a[88:91],a[252:255]\nbuffer_load_dwordx4 v17,s[28:31],s46 offen lds\nM v[188:191],a[76:79],a[84:87],v[188:191]\ns_add_u32 m0,s50,20480\nds_read_b128 a[84:87],v6 offset:36864\nM v[156:159],a[76:79],a[80:83],v[156:159]\ns_waitcnt lgkmcnt(4)\n" \
"ds_read_b128 a[80:83],v6 offset:32768\nM v[128:131],a[0:3],a[32:35],v[128:131]\nds_read_b64_tr_b16 a[68:69],v11 offset:25600\nM v[160:163],a[0:3],a[36:39],v[160:163]\nbuffer_load_dwordx4 v18,s[28:31],s46 offen lds\nM a[224:227],a[0:3],a[40:43],a[224:227]\ns_add_u32 m0,s50,24576\nM a[192:195],a[0:3],a[44:47],a[192:195]\n" \
"ds_read_b64_tr_b16 a[70:71],v12 offset:25600\nds_read_b64_tr_b16 a[72:73],v9 offset:26112\nM a[196:199],a[4:7],a[44:47],a[196:199]\nds_read_b64_tr_b16 a[74:75],v10 offset:26112\nM a[228:231],a[4:7],a[40:43],a[228:231]\nbuffer_load_dwordx4 v19,s[28:31],s46 offen lds\nM v[164:167],a[4:7],a[36:39],v[164:167]\ns_add_u32 m0,s50,28672\n" \
"ds_read_b64_tr_b16 a[76:77],v11 offset:26112\nM v[132:135],a[4:7],a[32:35],v[132:135]\nds_read_b64_tr_b16 a[78:79],v12 offset:26112\nM v[136:139],a[8:11],a[32:35],v[136:139]\ns_nop 0\nds_read_b128 a[88:91],v6 offset:40960\nM v[168:171],a[8:11],a[36:39],v[168:171]\nbuffer_load_dwordx4 v20,s[28:31],s46 offen lds\n" \
"M a[232:235],a[8:11],a[40:43],a[232:235]\ns_add_u32 m0,s51,0\nds_read_b128 a[92:95],v6 offset:45056\nM a[200:203],a[8:11],a[44:47],a[200:203]\ns_mov_b32 s46,1792\nM a[204:207],a[12:15],a[44:47],a[204:207]\nM a[236:239],a[12:15],a[40:43],a[236:239]\nbuffer_load_dwordx4 v21,s[32:35],s47 offen offset:0 lds\n" \
"M v[172:175],a[12:15],a[36:39],v[172:175]\ns_add_u32 m0,s51,896\nM v[140:143],a[12:15],a[32:35],v[140:143]\nM v[144:147],a[16:19],a[32:35],v[144:147]\nM v[176:179],a[16:19],a[36:39],v[176:179]\nbuffer_load_dwordx4 v21,s[32:35],s47 offen offset:128 lds\nM a[240:243],a[16:19],a[40:43],a[240:243]\ns_add_u32 m0,s51,8192\n" \
"M a[208:211],a[16:19],a[44:47],a[208:211]\ns_mov_b32 s47,7602176\nM a[212:215],a[20:23],a[44:47],a[212:215]\nM a[244:247],a[20:23],a[40:43],a[244:247]\nbuffer_load_dwordx4 v21,s[32:35],s47 offen offset:0 lds\nM v[180:183],a[20:23],a[36:39],v[180:183]\ns_add_u32 m0,s51,9088\nM v[148:151],a[20:23],a[32:35],v[148:151]\n" \
"M v[152:155],a[24:27],a[32:35],v[152:155]\ns_waitcnt vmcnt(16) lgkmcnt(0)\nM v[184:187],a[24:27],a[36:39],v[184:187]\nbuffer_store_dwordx4 v[120:123],v4,s[36:39],s96 offen offset:0 sc1\ns_barrier\nM a[248:251],a[24:27],a[40:43],a[248:251]\nds_read_b64_tr_b16 a[0:1],v9 offset:32768\nds_read_b64_tr_b16 a[2:3],v10 offset:32768\n" \
"M a[216:219],a[24:27],a[44:47],a[216:219]\nds_read_b64_tr_b16 a[4:5],v11 offset:32768\nM a[220:223],a[28:31],a[44:47],a[220:223]\nds_read_b128 a[44:47],v7 offset:12288\nM a[252:255],a[28:31],a[40:43],a[252:255]\nbuffer_load_dwordx4 v21,s[32:35],s47 offen offset:128 lds\nM v[188:191],a[28:31],a[36:39],v[188:191]\ns_add_u32 m0,s50,32768\n" \
"ds_read_b128 a[36:39],v7 offset:4096\nM v[156:159],a[28:31],a[32:35],v[156:159]\ns_mov_b32 s47,7864320\nds_read_b128 a[32:35],v7 offset:0\nM v[128:131],a[48:51],a[80:83],v[128:131]\nds_read_b128 a[40:43],v7 offset:8192\nM v[160:163],a[48:51],a[84:87],v[160:163]\nbuffer_load_dwordx4 v13,s[28:31],s46 offen lds\n" \
"M a[224:227],a[48:51],a[88:91],a[224:227]\ns_add_u32 m0,s50,36864\nM a[192:195],a[48:51],a[92:95],a[192:195]\nds_read_b64_tr_b16 a[6:7],v12 offset:32768\nds_read_b64_tr_b16 a[8:9],v9 offset:33280\nM a[196:199],a[52:55],a[92:95],a[196:199]\nds_read_b64_tr_b16 a[10:11],v10 offset:33280\nM a[228:231],a[52:55],a[88:91],a[228:231]\n" \
"buffer_load_dwordx4 v14,s[28:31],s46 offen lds\nM v[164:167],a[52:55],a[84:87],v[164:167]\ns_add_u32 m0,s50,40960\nds_read_b64_tr_b16 a[12:13],v11 offset:33280\nM v[132:135],a[52:55],a[80:83],v[132:135]\nds_read_b64_tr_b16 a[14:15],v12 offset:33280\nM v[136:139],a[56:59],a[80:83],v[136:139]\nds_read_b64_tr_b16 a[16:17],v9 offset:33792\n" \
"M v[168:171],a[56:59],a[84:87],v[168:171]\nbuffer_load_dwordx4 v15,s[28:31],s46 offen lds\nM a[232:235],a[56:59],a[88:91],a[232:235]\ns_add_u32 m0,s50,45056\nds_read_b64_tr_b16 a[18:19],v10 offset:33792\nM a[200:203],a[56:59],a[92:95],a[200:203]\nds_read_b64_tr_b16 a[20:21],v11 offset:33792\nM a[204:207],a[60:63],a[92:95],a[204:207]\n" \
"ds_read_b64_tr_b16 a[22:23],v12 offset:33792\nds_read_b64_tr_b16 a[24:25],v9 offset:34304\nM a[236:239],a[60:63],a[88:91],a[236:239]\nbuffer_load_dwordx4 v16,s[28:31],s46 offen lds\nM v[172:175],a[60:63],a[84:87],v[172:175]\ns_add_u32 m0,s50,49152\nM v[140:143],a[60:63],a[80:83],v[140:143]\ns_mov_b32 s46,1792\n" \
"ds_read_b64_tr_b16 a[26:27],v10 offset:34304\nds_read_b64_tr_b16 a[28:29],v11 offset:34304\nM v[144:147],a[64:67],a[80:83],v[144:147]\nds_read_b64_tr_b16 a[30:31],v12 offset:34304\nM v[176:179],a[64:67],a[84:87],v[176:179]\nbuffer_load_dwordx4 v17,s[28:31],s46 offen lds\nM a[240:243],a[64:67],a[88:91],a[240:243]\ns_add_u32 m0,s50,53248\n" \
"ds_read_b64_tr_b16 a[48:49],v9 offset:40960\nM a[208:211],a[64:67],a[92:95],a[208:211]\nds_read_b64_tr_b16 a[50:51],v10 offset:40960\nM a[212:215],a[68:71],a[92:95],a[212:215]\nds_read_b64_tr_b16 a[52:53],v11 offset:40960\nM a[244:247],a[68:71],a[88:91],a[244:247]\nbuffer_load_dwordx4 v18,s[28:31],s46 offen lds\nM v[180:183],a[68:71],a[84:87],v[180:183]\n" \
"s_add_u32 m0,s50,57344\nds_read_b64_tr_b16 a[54:55],v12 offset:40960\nM v[148:151],a[68:71],a[80:83],v[148:151]\nds_read_b64_tr_b16 a[56:57],v9 offset:41472\nM v[152:155],a[72:75],a[80:83],v[152:155]\nds_read_b64_tr_b16 a[58:59],v10 offset:41472\nds_read_b64_tr_b16 a[60:61],v11 offset:41472\nM v[184:187],a[72:75],a[84:87],v[184:187]\n" \
"buffer_load_dwordx4 v19,s[28:31],s46 offen lds\nM a[248:251],a[72:75],a[88:91],a[248:251]\ns_add_u32 m0,s50,61440\nM a[216:219],a[72:75],a[92:95],a[216:219]\nds_read_b64_tr_b16 a[62:63],v12 offset:41472\nds_read_b64_tr_b16 a[64:65],v9 offset:41984\nM a[220:223],a[76:79],a[92:95],a[220:223]\ns_waitcnt lgkmcnt(5)\n" \
"ds_read_b64_tr_b16 a[66:67],v10 offset:41984\nM a[252:255],a[76:79],a[88:91],a[252:255]\nbuffer_load_dwordx4 v20,s[28:31],s46 offen lds\nM v[188:191],a[76:79],a[84:87],v[188:191]\ns_add_u32 m0,s51,16384\nds_read_b128 a[84:87],v8 offset:4096\nM v[156:159],a[76:79],a[80:83],v[156:159]\ns_mov_b32 s46,1920\n" \
"ds_read_b128 a[80:83],v8 offset:0\nM v[128:131],a[0:3],a[32:35],v[128:131]\nds_read_b64_tr_b16 a[68:69],v11 offset:41984\nM v[160:163],a[0:3],a[36:39],v[160:163]\nbuffer_load_dwordx4 v21,s[32:35],s47 offen offset:0 lds\nM a[224:227],a[0:3],a[40:43],a[224:227]\ns_add_u32 m0,s51,17280\nM a[192:195],a[0:3],a[44:47],a[192:195]\n" \
"ds_read_b64_tr_b16 a[70:71],v12 offset:41984\nds_read_b64_tr_b16 a[72:73],v9 offset:42496\nM a[196:199],a[4:7],a[44:47],a[196:199]\nds_read_b64_tr_b16 a[74:75],v10 offset:42496\nM a[228:231],a[4:7],a[40:43],a[228:231]\nbuffer_load_dwordx4 v21,s[32:35],s47 offen offset:128 lds\nM v[164:167],a[4:7],a[36:39],v[164:167]\ns_add_u32 m0,s51,24576\n" \
"ds_read_b64_tr_b16 a[76:77],v11 offset:42496\nM v[132:135],a[4:7],a[32:35],v[132:135]\ns_mov_b32 s47,8126464\nds_read_b64_tr_b16 a[78:79],v12 offset:42496\nM v[136:139],a[8:11],a[32:35],v[136:139]\nds_read_b128 a[88:91],v8 offset:8192\nM v[168:171],a[8:11],a[36:39],v[168:171]\nbuffer_load_dwordx4 v21,s[32:35],s47 offen offset:0 lds\n" \
"M a[232:235],a[8:11],a[40:43],a[232:235]\ns_add_u32 m0,s51,25472\nds_read_b128 a[92:95],v8 offset:12288\nM a[200:203],a[8:11],a[44:47],a[200:203]\nM a[204:207],a[12:15],a[44:47],a[204:207]\nM a[236:239],a[12:15],a[40:43],a[236:239]\nM v[172:175],a[12:15],a[36:39],v[172:175]\nbuffer_load_dwordx4 v21,s[32:35],s47 offen offset:128 lds\n" \
"M v[140:143],a[12:15],a[32:35],v[140:143]\ns_add_u32 m0,s50,65536\nv_add_u32 v21,s63,v4\nM v[144:147],a[16:19],a[32:35],v[144:147]\ns_add_u32 s63,s22,16384\nv_add_u32 v62,s63,v4\nM v[176:179],a[16:19],a[36:39],v[176:179]\ns_cmp_lt_u32 %2,2\n" \
"s_cselect_b64 s[62:63],-1,0\nM a[240:243],a[16:19],a[40:43],a[240:243]\nbuffer_store_dwordx4 v[124:127],v4,s[36:39],s96 offen offset:1024 sc1\nM a[208:211],a[16:19],a[44:47],a[208:211]\nM a[212:215],a[20:23],a[44:47],a[212:215]\nM a[244:247],a[20:23],a[40:43],a[244:247]\nM v[180:183],a[20:23],a[36:39],v[180:183]\nM v[148:151],a[20:23],a[32:35],v[148:151]\n" \
"M v[152:155],a[24:27],a[32:35],v[152:155]\ns_waitcnt vmcnt(18) lgkmcnt(0)\nM v[184:187],a[24:27],a[36:39],v[184:187]\ns_barrier\nds_read_b64_tr_b16 a[0:1],v9 offset:49152\nM a[248:251],a[24:27],a[40:43],a[248:251]\nbuffer_load_dwordx4 v13,s[28:31],s46 offen lds\nM a[216:219],a[24:27],a[44:47],a[216:219]\n" \
"s_add_u32 m0,s50,69632\nds_read_b64_tr_b16 a[2:3],v10 offset:49152\nv_add_u32 v13,s54,v4\nM a[220:223],a[28:31],a[44:47],a[220:223]\nds_read_b128 a[44:47],v5 offset:12288\nM a[252:255],a[28:31],a[40:43],a[252:255]\nds_read_b128 a[40:43],v5 offset:8192\nM v[188:191],a[28:31],a[36:39],v[188:191]\n" \
"buffer_load_dwordx4 v14,s[28:31],s46 offen lds\nM v[156:159],a[28:31],a[32:35],v[156:159]\ns_add_u32 m0,s50,73728\nds_read_b128 a[32:35],v5 offset:0\nv_add_u32 v14,0x10000,v13\nM v[128:131],a[48:51],a[80:83],v[128:131]\nds_read_b128 a[36:39],v5 offset:4096\nM v[160:163],a[48:51],a[84:87],v[160:163]\n" \
"ds_read_b64_tr_b16 a[4:5],v11 offset:49152\nM a[224:227],a[48:51],a[88:91],a[224:227]\nbuffer_load_dwordx4 v15,s[28:31],s46 offen lds\nM a[192:195],a[48:51],a[92:95],a[192:195]\ns_add_u32 m0,s50,77824\nds_read_b64_tr_b16 a[6:7],v12 offset:49152\nM a[196:199],a[52:55],a[92:95],a[196:199]\nds_read_b64_tr_b16 a[8:9],v9 offset:49664\n" \
"M a[228:231],a[52:55],a[88:91],a[228:231]\nds_read_b64_tr_b16 a[10:11],v10 offset:49664\nM v[164:167],a[52:55],a[84:87],v[164:167]\nbuffer_load_dwordx4 v16,s[28:31],s46 offen lds\nM v[132:135],a[52:55],a[80:83],v[132:135]\ns_add_u32 m0,s50,81920\nds_read_b64_tr_b16 a[12:13],v11 offset:49664\nM v[136:139],a[56:59],a[80:83],v[136:139]\n" \
"s_mov_b32 s46,1920\nds_read_b64_tr_b16 a[14:15],v12 offset:49664\nM v[168:171],a[56:59],a[84:87],v[168:171]\nds_read_b64_tr_b16 a[16:17],v9 offset:50176\nds_read_b64_tr_b16 a[18:19],v10 offset:50176\nM a[232:235],a[56:59],a[88:91],a[232:235]\nbuffer_store_dwordx4 v[72:75],v4,s[36:39],s96 offen offset:2048 sc1\nM a[200:203],a[56:59],a[92:95],a[200:203]\n" \
"ds_read_b64_tr_b16 a[20:21],v11 offset:50176\nds_read_b64_tr_b16 a[22:23],v12 offset:50176\nM a[204:207],a[60:63],a[92:95],a[204:207]\nds_read_b64_tr_b16 a[24:25],v9 offset:50688\nM a[236:239],a[60:63],a[88:91],a[236:239]\nds_read_b64_tr_b16 a[26:27],v10 offset:50688\nds_read_b64_tr_b16 a[28:29],v11 offset:50688\nM v[172:175],a[60:63],a[84:87],v[172:175]\n" \
"buffer_load_dwordx4 v17,s[28:31],s46 offen lds\nM v[140:143],a[60:63],a[80:83],v[140:143]\ns_add_u32 m0,s50,86016\nM v[144:147],a[64:67],a[80:83],v[144:147]\nds_read_b64_tr_b16 a[30:31],v12 offset:50688\nds_read_b64_tr_b16 a[48:49],v9 offset:57344\nM v[176:179],a[64:67],a[84:87],v[176:179]\nds_read_b64_tr_b16 a[50:51],v10 offset:57344\n" \
"M a[240:243],a[64:67],a[88:91],a[240:243]\nbuffer_load_dwordx4 v18,s[28:31],s46 offen lds\nM a[208:211],a[64:67],a[92:95],a[208:211]\ns_add_u32 m0,s50,90112\nds_read_b64_tr_b16 a[52:53],v11 offset:57344\nM a[212:215],a[68:71],a[92:95],a[212:215]\nds_read_b64_tr_b16 a[54:55],v12 offset:57344\nM a[244:247],a[68:71],a[88:91],a[244:247]\n" \
"ds_read_b64_tr_b16 a[56:57],v9 offset:57856\nM v[180:183],a[68:71],a[84:87],v[180:183]\nbuffer_load_dwordx4 v19,s[28:31],s46 offen lds\nM v[148:151],a[68:71],a[80:83],v[148:151]\ns_add_u32 m0,s50,94208\nds_read_b64_tr_b16 a[58:59],v10 offset:57856\nM v[152:155],a[72:75],a[80:83],v[152:155]\nds_read_b64_tr_b16 a[60:61],v11 offset:57856\n" \
"M v[184:187],a[72:75],a[84:87],v[184:187]\nds_read_b64_tr_b16 a[62:63],v12 offset:57856\nds_read_b64_tr_b16 a[64:65],v9 offset:58368\nM a[248:251],a[72:75],a[88:91],a[248:251]\nbuffer_load_dwordx4 v20,s[28:31],s46 offen lds\nM a[216:219],a[72:75],a[92:95],a[216:219]\ns_add_u32 m0,s22,49152\nv_add_u32 v20,12288,v21\n" \
"v_cndmask_b32 v20,v20,v62,s[62:63]\nM a[220:223],a[76:79],a[92:95],a[220:223]\ns_lshr_b32 s62,%1,3\nds_read_b64_tr_b16 a[66:67],v10 offset:58368\nds_read_b64_tr_b16 a[68:69],v11 offset:58368\nM a[252:255],a[76:79],a[88:91],a[252:255]\ns_lshl_b32 s62,s62,2\nds_read_b64_tr_b16 a[70:71],v12 offset:58368\n" \
"M v[188:191],a[76:79],a[84:87],v[188:191]\nbuffer_store_dwordx4 v[76:79],v4,s[36:39],s96 offen offset:3072 sc1\nM v[156:159],a[76:79],a[80:83],v[156:159]\ns_waitcnt lgkmcnt(3)\ns_add_u32 s62,s62,%2\nds_read_b128 a[80:83],v6 offset:0\nds_read_b128 a[84:87],v6 offset:4096\nM v[128:131],a[0:3],a[32:35],v[128:131]\n" \
"s_lshr_b32 s63,%1,5\nds_read_b64_tr_b16 a[72:73],v9 offset:58880\nM v[160:163],a[0:3],a[36:39],v[160:163]\ns_lshl_b32 s62,s62,14\nds_read_b64_tr_b16 a[74:75],v10 offset:58880\nM a[224:227],a[0:3],a[40:43],a[224:227]\ns_lshl_b32 s63,s63,8\nds_read_b64_tr_b16 a[76:77],v11 offset:58880\n" \
"M a[192:195],a[0:3],a[44:47],a[192:195]\ns_sub_u32 s62,s62,s63\nds_read_b64_tr_b16 a[78:79],v12 offset:58880\nds_read_b128 a[88:91],v6 offset:8192\nM a[196:199],a[4:7],a[44:47],a[196:199]\ns_mov_b32 s46,0\nds_read_b128 a[92:95],v6 offset:12288\nM a[228:231],a[4:7],a[40:43],a[228:231]\n" \
"M v[164:167],a[4:7],a[36:39],v[164:167]\nM v[132:135],a[4:7],a[32:35],v[132:135]\nM v[136:139],a[8:11],a[32:35],v[136:139]\nM v[168:171],a[8:11],a[36:39],v[168:171]\nM a[232:235],a[8:11],a[40:43],a[232:235]\nM a[200:203],a[8:11],a[44:47],a[200:203]\nM a[204:207],a[12:15],a[44:47],a[204:207]\nM a[236:239],a[12:15],a[40:43],a[236:239]\n" \
"M v[172:175],a[12:15],a[36:39],v[172:175]\nM v[140:143],a[12:15],a[32:35],v[140:143]\nM v[144:147],a[16:19],a[32:35],v[144:147]\nM v[176:179],a[16:19],a[36:39],v[176:179]\nM a[240:243],a[16:19],a[40:43],a[240:243]\nM a[208:211],a[16:19],a[44:47],a[208:211]\nM a[212:215],a[20:23],a[44:47],a[212:215]\nM a[244:247],a[20:23],a[40:43],a[244:247]\n" \
"M v[180:183],a[20:23],a[36:39],v[180:183]\nM v[148:151],a[20:23],a[32:35],v[148:151]\nM v[152:155],a[24:27],a[32:35],v[152:155]\ns_waitcnt vmcnt(15) lgkmcnt(0)\nM v[184:187],a[24:27],a[36:39],v[184:187]\ns_barrier\nds_read_b64_tr_b16 a[0:1],v9 offset:0\nM a[248:251],a[24:27],a[40:43],a[248:251]\n" \
"buffer_load_dwordx4 v22,s[40:43],s61 offen offset:0 sc1 lds\nM a[216:219],a[24:27],a[44:47],a[216:219]\nds_read_b64_tr_b16 a[2:3],v10 offset:0\nM a[220:223],a[28:31],a[44:47],a[220:223]\nds_read_b64_tr_b16 a[4:5],v11 offset:0\nds_read_b64_tr_b16 a[6:7],v12 offset:0\nM a[252:255],a[28:31],a[40:43],a[252:255]\nds_read_b64_tr_b16 a[8:9],v9 offset:512\n" \
"M v[188:191],a[28:31],a[36:39],v[188:191]\nbuffer_load_dwordx4 v22,s[40:43],s61 offen offset:1024 sc1 lds\nM v[156:159],a[28:31],a[32:35],v[156:159]\nds_read_b128 a[32:35],v5 offset:45056\nM v[128:131],a[48:51],a[80:83],v[128:131]\nds_read_b64_tr_b16 a[10:11],v10 offset:512\nM v[160:163],a[48:51],a[84:87],v[160:163]\nds_read_b64_tr_b16 a[12:13],v11 offset:512\n" \
"M a[224:227],a[48:51],a[88:91],a[224:227]\nbuffer_load_dwordx4 v22,s[40:43],s61 offen offset:2048 sc1 lds\nM a[192:195],a[48:51],a[92:95],a[192:195]\nds_read_b64_tr_b16 a[14:15],v12 offset:512\nM a[196:199],a[52:55],a[92:95],a[196:199]\nds_read_b64_tr_b16 a[16:17],v9 offset:1024\nM a[228:231],a[52:55],a[88:91],a[228:231]\nds_read_b64_tr_b16 a[18:19],v10 offset:1024\n" \
"M v[164:167],a[52:55],a[84:87],v[164:167]\nbuffer_load_dwordx4 v22,s[40:43],s61 offen offset:3072 sc1 lds\nM v[132:135],a[52:55],a[80:83],v[132:135]\ns_add_u32 m0,s22,81920\nds_read_b64_tr_b16 a[20:21],v11 offset:1024\nM v[136:139],a[56:59],a[80:83],v[136:139]\ns_add_u32 s61,s44,40960\nds_read_b64_tr_b16 a[22:23],v12 offset:1024\n" \
"M v[168:171],a[56:59],a[84:87],v[168:171]\nds_read_b64_tr_b16 a[24:25],v9 offset:1536\nds_read_b64_tr_b16 a[26:27],v10 offset:1536\nM a[232:235],a[56:59],a[88:91],a[232:235]\nbuffer_load_dwordx4 v22,s[40:43],s61 offen offset:0 sc1 lds\nM a[200:203],a[56:59],a[92:95],a[200:203]\nM a[204:207],a[60:63],a[92:95],a[204:207]\nds_read_b64_tr_b16 a[28:29],v11 offset:1536\n" \
"ds_read_b64_tr_b16 a[30:31],v12 offset:1536\nM a[236:239],a[60:63],a[88:91],a[236:239]\nds_read_b128 a[44:47],v6 offset:45056\nM v[172:175],a[60:63],a[84:87],v[172:175]\nbuffer_load_dwordx4 v22,s[40:43],s61 offen offset:1024 sc1 lds\nM v[140:143],a[60:63],a[80:83],v[140:143]\nM v[144:147],a[64:67],a[80:83],v[144:147]\nds_read_b64_tr_b16 a[48:49],v9 offset:8192\n" \
"ds_read_b64_tr_b16 a[50:51],v10 offset:8192\nM v[176:179],a[64:67],a[84:87],v[176:179]\nds_read_b64_tr_b16 a[52:53],v11 offset:8192\nM a[240:243],a[64:67],a[88:91],a[240:243]\ns_waitcnt vmcnt(7)\nds_read_b64_tr_b16 a[54:55],v12 offset:8192\nM a[208:211],a[64:67],a[92:95],a[208:211]\ns_barrier\n" \
"ds_read_b128 a[36:39],v7 offset:12288\nM a[212:215],a[68:71],a[92:95],a[212:215]\nbuffer_load_dwordx4 v22,s[40:43],s61 offen offset:2048 sc1 lds\nM a[244:247],a[68:71],a[88:91],a[244:247]\nds_read_b64_tr_b16 a[96:97],v9 offset:16384\nM v[180:183],a[68:71],a[84:87],v[180:183]\nds_read_b64_tr_b16 a[98:99],v10 offset:16384\nM v[148:151],a[68:71],a[80:83],v[148:151]\n" \
"ds_read_b64_tr_b16 a[100:101],v11 offset:16384\nds_read_b64_tr_b16 a[102:103],v12 offset:16384\nM v[152:155],a[72:75],a[80:83],v[152:155]\nds_read_b64_tr_b16 a[104:105],v9 offset:16896\nM v[184:187],a[72:75],a[84:87],v[184:187]\nds_read_b64_tr_b16 a[106:107],v10 offset:16896\nM a[248:251],a[72:75],a[88:91],a[248:251]\nds_read_b64_tr_b16 a[108:109],v11 offset:16896\n" \
"ds_read_b64_tr_b16 a[110:111],v12 offset:16896\nM a[216:219],a[72:75],a[92:95],a[216:219]\nds_read_b64_tr_b16 a[112:113],v9 offset:17408\nM a[220:223],a[76:79],a[92:95],a[220:223]\nds_read_b64_tr_b16 a[114:115],v10 offset:17408\nM a[252:255],a[76:79],a[88:91],a[252:255]\nds_read_b64_tr_b16 a[116:117],v11 offset:17408\nds_read_b64_tr_b16 a[118:119],v12 offset:17408\n" \
"M v[188:191],a[76:79],a[84:87],v[188:191]\nds_read_b64_tr_b16 a[120:121],v9 offset:17920\nM v[156:159],a[76:79],a[80:83],v[156:159]\ns_waitcnt lgkmcnt(7)\nds_read_b64_tr_b16 a[122:123],v10 offset:17920\nM a[192:195],a[0:3],a[32:35],a[192:195]\nds_read_b64_tr_b16 a[124:125],v11 offset:17920\nds_read_b64_tr_b16 a[126:127],v12 offset:17920\n" \
"M a[196:199],a[4:7],a[32:35],a[196:199]\nds_read_b128 a[40:43],v8 offset:12288\nM a[200:203],a[8:11],a[32:35],a[200:203]\nds_read_b64_tr_b16 a[128:129],v9 offset:24576\nM a[204:207],a[12:15],a[32:35],a[204:207]\nds_read_b64_tr_b16 a[130:131],v10 offset:24576\nM a[208:211],a[16:19],a[32:35],a[208:211]\nds_read_b64_tr_b16 a[132:133],v11 offset:24576\n" \
"ds_read_b64_tr_b16 a[134:135],v12 offset:24576\nM a[212:215],a[20:23],a[32:35],a[212:215]\nds_read_b64_tr_b16 a[136:137],v9 offset:25088\nM a[216:219],a[24:27],a[32:35],a[216:219]\nds_read_b64_tr_b16 a[138:139],v10 offset:25088\nM a[220:223],a[28:31],a[32:35],a[220:223]\nds_read_b64_tr_b16 a[140:141],v11 offset:25088\nds_read_b64_tr_b16 a[142:143],v12 offset:25088\n" \
"M a[192:195],a[96:99],a[36:39],a[192:195]\nds_read_b64_tr_b16 a[144:145],v9 offset:25600\nM a[196:199],a[100:103],a[36:39],a[196:199]\nds_read_b64_tr_b16 a[146:147],v10 offset:25600\nM a[200:203],a[104:107],a[36:39],a[200:203]\ns_waitcnt lgkmcnt(6)\nds_read_b64_tr_b16 a[148:149],v11 offset:25600\nds_read_b64_tr_b16 a[150:151],v12 offset:25600\n" \
"M a[204:207],a[108:111],a[36:39],a[204:207]\nds_read_b64_tr_b16 a[152:153],v9 offset:26112\nds_read_b64_tr_b16 a[154:155],v10 offset:26112\nM a[208:211],a[112:115],a[36:39],a[208:211]\nds_read_b64_tr_b16 a[156:157],v11 offset:26112\nds_read_b64_tr_b16 a[158:159],v12 offset:26112\nM a[212:215],a[116:119],a[36:39],a[212:215]\nbuffer_load_dwordx4 v22,s[40:43],s61 offen offset:3072 sc1 lds\n" \
"M a[216:219],a[120:123],a[36:39],a[216:219]\ns_add_u32 m0,s22,98304\nM a[220:223],a[124:127],a[36:39],a[220:223]\ns_add_u32 s61,s44,16384\nds_read_b64_tr_b16 a[56:57],v9 offset:8704\nds_read_b64_tr_b16 a[58:59],v10 offset:8704\nM a[192:195],a[128:131],a[40:43],a[192:195]\nds_read_b64_tr_b16 a[60:61],v11 offset:8704\n" \
"ds_read_b64_tr_b16 a[62:63],v12 offset:8704\nM a[196:199],a[132:135],a[40:43],a[196:199]\ns_waitcnt lgkmcnt(6)\nds_read_b64_tr_b16 a[64:65],v9 offset:9216\nds_read_b64_tr_b16 a[66:67],v10 offset:9216\nM a[200:203],a[136:139],a[40:43],a[200:203]\nds_read_b128 a[80:83],v5 offset:40960\nds_read_b128 v[240:243],v5 offset:36864\n" \
"ds_read_b128 v[224:227],v5 offset:32768\nds_read_b128 a[84:87],v7 offset:8192\nds_read_b128 v[244:247],v7 offset:4096\nds_read_b128 v[228:231],v7 offset:0\nds_read_b128 a[88:91],v8 offset:8192\nds_read_b128 v[248:251],v8 offset:4096\nds_read_b128 v[232:235],v8 offset:0\nds_read_b64_tr_b16 a[68:69],v11 offset:9216\n" \
"ds_read_b64_tr_b16 a[70:71],v12 offset:9216\nds_read_b64_tr_b16 a[72:73],v9 offset:9728\nds_read_b64_tr_b16 a[74:75],v10 offset:9728\nds_read_b64_tr_b16 a[76:77],v11 offset:9728\nds_read_b64_tr_b16 a[78:79],v12 offset:9728\nds_read_b128 a[92:95],v6 offset:40960\nds_read_b128 v[252:255],v6 offset:36864\nds_read_b128 v[236:239],v6 offset:32768\n" \
"M a[204:207],a[140:143],a[40:43],a[204:207]\nM a[208:211],a[144:147],a[40:43],a[208:211]\ns_mov_b64 exec,0\ns_cmp_eq_u32 %2,0\ns_cbranch_scc1 LG27_%=\ns_cmp_eq_u32 %2,1\ns_cbranch_scc1 LG28_%=\ns_cmp_eq_u32 %2,2\n" \
"s_cbranch_scc1 LG14_%=\ns_cmp_eq_u32 %2,3\ns_cbranch_scc1 LG15_%=\nLHR1_%=:\ns_mov_b64 exec,-1\nM a[212:215],a[148:151],a[40:43],a[212:215]\nds_write_b128 v1,v[88:91] offset:0\nM a[216:219],a[152:155],a[40:43],a[216:219]\n" \
"s_waitcnt lgkmcnt(15)\nM a[220:223],a[156:159],a[40:43],a[220:223]\nM a[192:195],a[48:51],a[44:47],a[192:195]\ns_waitcnt vmcnt(8) lgkmcnt(1)\nM a[196:199],a[52:55],a[44:47],a[196:199]\ns_barrier\nM a[200:203],a[56:59],a[44:47],a[200:203]\nbuffer_store_dwordx2 v[58:59],v56,s[36:39],s24 offen sc1\n" \
"M a[204:207],a[60:63],a[44:47],a[204:207]\nds_write_b128 v1,v[92:95] offset:1024\nM a[208:211],a[64:67],a[44:47],a[208:211]\nbuffer_load_dwordx4 v22,s[40:43],s61 offen offset:0 sc1 lds\nM a[212:215],a[68:71],a[44:47],a[212:215]\nv_accvgpr_read_b32 v5,a192\nv_accvgpr_read_b32 v10,a193\nv_accvgpr_read_b32 v11,a194\n" \
"M a[216:219],a[72:75],a[44:47],a[216:219]\nv_accvgpr_read_b32 v12,a195\nC v6,v5,v10\nC v7,v11,v12\nM a[220:223],a[76:79],a[44:47],a[220:223]\nds_write_b128 v1,v[104:107] offset:2048\nv_accvgpr_read_b32 v5,a196\nM a[224:227],a[0:3],a[80:83],a[224:227]\n" \
"buffer_load_dwordx4 v22,s[40:43],s61 offen offset:1024 sc1 lds\nv_accvgpr_read_b32 v10,a197\nM a[224:227],a[96:99],a[84:87],a[224:227]\nv_accvgpr_read_b32 v11,a198\nv_accvgpr_read_b32 v12,a199\nC v8,v5,v10\nM a[224:227],a[128:131],a[88:91],a[224:227]\nC v9,v11,v12\n" \
"v_accvgpr_read_b32 v32,a200\nv_accvgpr_read_b32 v33,a201\nM a[224:227],a[48:51],a[92:95],a[224:227]\nds_write_b128 v20,v[6:9] offset:0\nv_accvgpr_read_b32 v34,a202\nM a[228:231],a[52:55],a[92:95],a[228:231]\nbuffer_load_dwordx4 v22,s[40:43],s61 offen offset:2048 sc1 lds\nv_accvgpr_read_b32 v35,a203\n" \
"M a[228:231],a[132:135],a[88:91],a[228:231]\nC v36,v32,v33\nC v37,v34,v35\nv_accvgpr_read_b32 v32,a204\nM a[228:231],a[100:103],a[84:87],a[228:231]\nv_accvgpr_read_b32 v33,a205\nv_accvgpr_read_b32 v34,a206\nv_accvgpr_read_b32 v35,a207\n" \
"M a[228:231],a[4:7],a[80:83],a[228:231]\nLG14_%=:\ns_cbranch_execz LHR1_%=\nC v38,v32,v33\nC v39,v34,v35\nv_accvgpr_read_b32 v5,a208\nM a[232:235],a[8:11],a[80:83],a[232:235]\nbuffer_load_dwordx4 v22,s[40:43],s61 offen offset:3072 sc1 lds\n" \
"v_accvgpr_read_b32 v10,a209\nM a[232:235],a[104:107],a[84:87],a[232:235]\ns_add_u32 m0,s22,114688\nv_accvgpr_read_b32 v11,a210\nv_accvgpr_read_b32 v12,a211\nC v6,v5,v10\nM a[232:235],a[136:139],a[88:91],a[232:235]\ns_add_u32 s61,s44,0\n" \
"ds_write_b128 v1,v[108:111] offset:3072\nC v7,v11,v12\nM a[232:235],a[56:59],a[92:95],a[232:235]\nv_accvgpr_read_b32 v5,a212\nv_accvgpr_read_b32 v10,a213\nv_accvgpr_read_b32 v11,a214\nM a[236:239],a[60:63],a[92:95],a[236:239]\nbuffer_load_dwordx4 v22,s[40:43],s61 offen offset:0 sc1 lds\n" \
"v_accvgpr_read_b32 v12,a215\nM a[236:239],a[140:143],a[88:91],a[236:239]\nC v8,v5,v10\nC v9,v11,v12\nv_accvgpr_read_b32 v32,a216\nM a[236:239],a[108:111],a[84:87],a[236:239]\nv_accvgpr_read_b32 v33,a217\nv_accvgpr_read_b32 v34,a218\n" \
"v_accvgpr_read_b32 v35,a219\nM a[236:239],a[12:15],a[80:83],a[236:239]\nds_write_b128 v20,v[36:39] offset:1024\nC v36,v32,v33\nM a[240:243],a[16:19],a[80:83],a[240:243]\nbuffer_load_dwordx4 v22,s[40:43],s61 offen offset:1024 sc1 lds\nC v37,v34,v35\nM a[240:243],a[112:115],a[84:87],a[240:243]\n" \
"v_accvgpr_read_b32 v32,a220\nv_accvgpr_read_b32 v33,a221\nv_accvgpr_read_b32 v34,a222\nM a[240:243],a[144:147],a[88:91],a[240:243]\nv_accvgpr_read_b32 v35,a223\nC v38,v32,v33\nC v39,v34,v35\nM a[240:243],a[64:67],a[92:95],a[240:243]\n" \
"ds_write_b128 v20,v[6:9] offset:2048\nM a[244:247],a[68:71],a[92:95],a[244:247]\nv_accvgpr_read_b32 v5,a224\nv_accvgpr_read_b32 v10,a225\nM a[244:247],a[148:151],a[88:91],a[244:247]\nv_accvgpr_read_b32 v11,a226\nv_accvgpr_read_b32 v12,a227\nM a[244:247],a[116:119],a[84:87],a[244:247]\n" \
"ds_write_b128 v1,v[120:123] offset:4096\nds_write_b128 v1,v[124:127] offset:5120\nds_write_b128 v1,v[72:75] offset:6144\nds_write_b128 v1,v[76:79] offset:7168\nM a[244:247],a[20:23],a[80:83],a[244:247]\nC v6,v5,v10\nC v7,v11,v12\nM a[248:251],a[24:27],a[80:83],a[248:251]\n" \
"v_accvgpr_read_b32 v32,a232\nv_accvgpr_read_b32 v33,a233\nM a[248:251],a[120:123],a[84:87],a[248:251]\nv_accvgpr_read_b32 v34,a234\nv_accvgpr_read_b32 v35,a235\nM a[248:251],a[152:155],a[88:91],a[248:251]\nbuffer_load_dwordx4 v22,s[40:43],s61 offen offset:2048 sc1 lds\nM a[248:251],a[72:75],a[92:95],a[248:251]\n" \
"v_accvgpr_read_b32 v5,a228\nv_accvgpr_read_b32 v10,a229\nM a[252:255],a[76:79],a[92:95],a[252:255]\ns_waitcnt lgkmcnt(0)\nv_accvgpr_read_b32 v11,a230\nv_accvgpr_read_b32 v12,a231\nM a[252:255],a[156:159],a[88:91],a[252:255]\ns_barrier\n" \
"C v8,v5,v10\nM a[252:255],a[124:127],a[84:87],a[252:255]\nds_write_b128 v20,v[36:39] offset:3072\nM a[252:255],a[28:31],a[80:83],a[252:255]\nC v36,v32,v33\nC v37,v34,v35\nM v[160:163],a[0:3],v[240:243],v[160:163]\nbuffer_load_dwordx4 v22,s[40:43],s61 offen offset:3072 sc1 lds\n" \
"M v[160:163],a[96:99],v[244:247],v[160:163]\ns_add_u32 m0,s22,0\nC v9,v11,v12\nv_accvgpr_read_b32 v32,a236\nM v[164:167],a[100:103],v[244:247],v[164:167]\ns_add_u32 s61,s44,24576\nds_read_b128 a[176:179],v20 offset:0\nv_accvgpr_read_b32 v33,a237\n" \
"M v[164:167],a[4:7],v[240:243],v[164:167]\nds_read_b128 a[180:183],v20 offset:1024\nv_accvgpr_read_b32 v34,a238\nM v[168:171],a[8:11],v[240:243],v[168:171]\nds_read_b128 a[184:187],v20 offset:2048\nv_accvgpr_read_b32 v35,a239\nM v[168:171],a[104:107],v[244:247],v[168:171]\nds_read_b128 a[192:195],v13 offset:32768\n" \
"C v38,v32,v33\nM v[172:175],a[108:111],v[244:247],v[172:175]\nds_read_b128 a[196:199],v13 offset:34816\nC v39,v34,v35\nM v[172:175],a[12:15],v[240:243],v[172:175]\nds_read_b128 a[200:203],v13 offset:36864\nv_accvgpr_read_b32 v5,a240\nM v[176:179],a[16:19],v[240:243],v[176:179]\n" \
"ds_read_b128 a[188:191],v20 offset:3072\nds_read_b128 a[204:207],v13 offset:38912\nv_add_u32 v20,s65,v20\nM v[176:179],a[112:115],v[244:247],v[176:179]\nds_read_b128 a[208:211],v13 offset:33792\nv_accvgpr_read_b32 v10,a241\nM v[180:183],a[116:119],v[244:247],v[180:183]\nds_read_b128 a[212:215],v13 offset:35840\n" \
"v_accvgpr_read_b32 v11,a242\nM v[180:183],a[20:23],v[240:243],v[180:183]\nds_read_b128 a[216:219],v13 offset:37888\nv_accvgpr_read_b32 v12,a243\nM v[184:187],a[24:27],v[240:243],v[184:187]\nds_read_b128 a[220:223],v13 offset:39936\nv_accvgpr_read_b32 v32,a248\nM v[184:187],a[120:123],v[244:247],v[184:187]\n" \
"buffer_load_dwordx4 v22,s[40:43],s61 offen offset:0 sc1 lds\nM v[188:191],a[124:127],v[244:247],v[188:191]\nv_accvgpr_read_b32 v33,a249\nv_accvgpr_read_b32 v34,a250\nM v[188:191],a[28:31],v[240:243],v[188:191]\ns_waitcnt lgkmcnt(3)\nv_accvgpr_read_b32 v35,a251\nM v[88:91],a[192:195],a[176:179],v[48:51]\n" \
"ds_write_b128 v20,v[6:9] offset:0\nC v6,v5,v10\nM v[88:91],a[196:199],a[180:183],v[88:91]\nC v7,v11,v12\nv_accvgpr_read_b32 v5,a244\nM v[88:91],a[200:203],a[184:187],v[88:91]\nbuffer_load_dwordx4 v22,s[40:43],s61 offen offset:1024 sc1 lds\nM v[88:91],a[204:207],a[188:191],v[88:91]\n" \
"v_accvgpr_read_b32 v10,a245\nv_accvgpr_read_b32 v11,a246\nM v[92:95],a[208:211],a[176:179],v[52:55]\ns_waitcnt lgkmcnt(1)\nv_accvgpr_read_b32 v12,a247\nC v8,v5,v10\nM v[92:95],a[212:215],a[180:183],v[92:95]\nds_write_b128 v20,v[36:39] offset:1024\n" \
"C v36,v32,v33\nM v[92:95],a[216:219],a[184:187],v[92:95]\nC v37,v34,v35\nC v9,v11,v12\nM v[92:95],a[220:223],a[188:191],v[92:95]\nbuffer_load_dwordx4 v22,s[40:43],s61 offen offset:2048 sc1 lds\nM v[160:163],a[128:131],v[248:251],v[160:163]\nv_accvgpr_read_b32 v32,a252\n" \
"v_accvgpr_read_b32 v33,a253\nM v[160:163],a[48:51],v[252:255],v[160:163]\nv_accvgpr_read_b32 v34,a254\nv_accvgpr_read_b32 v35,a255\nM v[164:167],a[52:55],v[252:255],v[164:167]\nv_max3_f32 v60,v88,v89,v90\nC v38,v32,v33\nM v[164:167],a[132:135],v[248:251],v[164:167]\n" \
"C v39,v34,v35\nv_max3_f32 v61,v91,v92,v93\nM v[168:171],a[136:139],v[248:251],v[168:171]\nds_write_b128 v20,v[6:9] offset:2048\nv_max3_f32 v60,v60,v94,v95\nM v[168:171],a[56:59],v[252:255],v[168:171]\nv_max3_f32 v60,v60,v61,s76\nv_mov_b32 v61,v60\n" \
"M v[172:175],a[60:63],v[252:255],v[172:175]\ns_nop 0\nv_permlane32_swap_b32 v60,v61\nv_max_f32 v60,v60,v61\nM v[172:175],a[140:143],v[248:251],v[172:175]\nv_mov_b32 v61,v60\nC v160,v160,v161\nM v[176:179],a[144:147],v[248:251],v[176:179]\n" \
"s_waitcnt vmcnt(12)\nds_write_b128 v20,v[36:39] offset:3072\nM v[176:179],a[64:67],v[252:255],v[176:179]\ns_barrier\nC v161,v162,v163\nM v[180:183],a[68:71],v[252:255],v[180:183]\nv_permlane16_swap_b32 v60,v61\nv_min_f32 v44,-v60,-v61\n" \
"M v[180:183],a[148:151],v[248:251],v[180:183]\nds_read_b128 a[160:163],v20 offset:0\nv_add_f32 v88,v88,v44\nM v[184:187],a[152:155],v[248:251],v[184:187]\nds_read_b128 a[164:167],v20 offset:1024\nv_exp_f32 v88,v88\nM v[184:187],a[72:75],v[252:255],v[184:187]\nds_read_b128 a[168:171],v20 offset:2048\n" \
"v_add_f32 v89,v89,v44\nM v[188:191],a[76:79],v[252:255],v[188:191]\nds_read_b128 a[172:175],v20 offset:3072\nv_add_f32 v90,v90,v44\nM v[188:191],a[156:159],v[248:251],v[188:191]\nv_exp_f32 v89,v89\nM v[128:131],a[128:131],v[232:235],v[128:131]\nbuffer_load_dwordx4 v22,s[40:43],s61 offen offset:3072 sc1 lds\n" \
"M v[128:131],a[0:3],v[224:227],v[128:131]\ns_add_u32 m0,s22,32768\nds_read_b128 v[192:195],v13 offset:49152\nC v168,v168,v169\nM v[132:135],a[4:7],v[224:227],v[132:135]\ns_add_u32 s61,s44,8192\nds_read_b128 v[196:199],v13 offset:51200\nC v176,v176,v177\n" \
"M v[132:135],a[132:135],v[232:235],v[132:135]\nds_read_b128 v[200:203],v13 offset:53248\nv_add_u32 v20,s65,v20\nM v[136:139],a[136:139],v[232:235],v[136:139]\nds_read_b128 v[204:207],v13 offset:55296\nC v162,v164,v165\nM v[136:139],a[8:11],v[224:227],v[136:139]\nds_read_b128 v[208:211],v13 offset:50176\n" \
"C v163,v166,v167\nM v[140:143],a[12:15],v[224:227],v[140:143]\nds_read_b128 v[212:215],v13 offset:52224\nC v169,v170,v171\nM v[140:143],a[140:143],v[232:235],v[140:143]\nds_read_b128 v[216:219],v13 offset:54272\nv_add_f32 v91,v91,v44\nM v[144:147],a[144:147],v[232:235],v[144:147]\n" \
"v_exp_f32 v90,v90\nM v[144:147],a[16:19],v[224:227],v[144:147]\nds_read_b128 v[220:223],v13 offset:56320\nC v177,v178,v179\nM v[148:151],a[20:23],v[224:227],v[148:151]\nds_write_b128 v20,v[160:163] offset:0\nM v[148:151],a[148:151],v[232:235],v[148:151]\nC v170,v172,v173\n" \
"C v171,v174,v175\nM v[152:155],a[152:155],v[232:235],v[152:155]\nC v178,v180,v181\nC v179,v182,v183\nM v[152:155],a[24:27],v[224:227],v[152:155]\nv_exp_f32 v91,v91\nM v[156:159],a[28:31],v[224:227],v[156:159]\nv_add_f32 v92,v92,v44\n" \
"v_min_f32 v45,-v60,-v61\nM v[156:159],a[156:159],v[232:235],v[156:159]\ns_waitcnt lgkmcnt(1)\nds_write_b128 v20,v[168:171] offset:1024\nM v[112:115],v[192:195],a[160:163],v[48:51]\nC v184,v184,v185\nv_min_f32 v46,-v60,-v61\nM v[112:115],v[196:199],a[164:167],v[112:115]\n" \
"v_min_f32 v47,-v60,-v61\nC v185,v186,v187\nM v[112:115],v[200:203],a[168:171],v[112:115]\nv_exp_f32 v92,v92\nM v[112:115],v[204:207],a[172:175],v[112:115]\nds_write_b128 v20,v[176:179] offset:2048\nM v[116:119],v[208:211],a[160:163],v[52:55]\nv_min_f32 v29,-v60,-v61\n" \
"v_add_f32 v93,v93,v44\nM v[116:119],v[212:215],a[164:167],v[116:119]\nC v186,v188,v189\nC v187,v190,v191\nM v[116:119],v[216:219],a[168:171],v[116:119]\nds_read_b128 a[192:195],v14 offset:0\nv_max3_f32 v60,v112,v113,v114\nM v[116:119],v[220:223],a[172:175],v[116:119]\n" \
"ds_read_b128 a[196:199],v14 offset:1024\nv_add_f32 v94,v94,v44\nM v[120:123],v[192:195],a[176:179],v[44:47]\nv_exp_f32 v93,v93\nM v[120:123],v[196:199],a[180:183],v[120:123]\nds_write_b128 v20,v[184:187] offset:3072\nM v[120:123],v[200:203],a[184:187],v[120:123]\nv_max3_f32 v61,v115,v116,v117\n" \
"v_max3_f32 v60,v60,v118,v119\nM v[120:123],v[204:207],a[188:191],v[120:123]\nv_max3_f32 v60,v60,v61,s76\nv_mov_b32 v61,v60\nM v[124:127],v[208:211],a[176:179],v[44:47]\nds_read_b128 a[200:203],v14 offset:2048\nv_add_f32 v95,v95,v44\nM v[124:127],v[212:215],a[180:183],v[124:127]\n" \
"v_permlane32_swap_b32 v60,v61\nv_max_f32 v60,v60,v61\nM v[124:127],v[216:219],a[184:187],v[124:127]\nds_read_b128 a[204:207],v14 offset:3072\nv_mov_b32 v61,v60\nv_exp_f32 v94,v94\nM v[124:127],v[220:223],a[188:191],v[124:127]\nv_permlane16_swap_b32 v60,v61\n" \
"v_min_f32 v40,-v60,-v61\nM v[128:131],a[96:99],v[228:231],v[128:131]\nv_add_f32 v112,v112,v40\nv_add_f32 v113,v113,v40\nv_add_f32 v114,v114,v40\nM v[128:131],a[48:51],v[236:239],v[128:131]\nv_exp_f32 v112,v112\nv_add_f32 v115,v115,v40\n" \
"M v[132:135],a[52:55],v[236:239],v[132:135]\ns_waitcnt vmcnt(8) lgkmcnt(15)\nv_exp_f32 v113,v113\nv_add_f32 v116,v116,v40\nM v[132:135],a[100:103],v[228:231],v[132:135]\ns_barrier\nv_add_f32 v117,v117,v40\nv_exp_f32 v114,v114\n" \
"M v[136:139],a[104:107],v[228:231],v[136:139]\nbuffer_load_dwordx2 v[58:59],v57,s[36:39],0 offen sc1\nv_add_f32 v118,v118,v40\nM v[136:139],a[56:59],v[236:239],v[136:139]\nv_exp_f32 v115,v115\nv_exp_f32 v95,v95\nM v[140:143],a[60:63],v[236:239],v[140:143]\nds_read_b128 a[208:211],v14 offset:4096\n" \
"v_exp_f32 v116,v116\nM v[140:143],a[108:111],v[228:231],v[140:143]\nds_read_b128 a[212:215],v14 offset:5120\nv_exp_f32 v117,v117\nM v[144:147],a[112:115],v[228:231],v[144:147]\nds_read_b128 a[216:219],v14 offset:6144\nv_add_f32 v119,v119,v40\nv_exp_f32 v118,v118\n" \
"M v[144:147],a[64:67],v[236:239],v[144:147]\nds_read_b128 a[220:223],v14 offset:7168\nC v88,v88,v89\nv_exp_f32 v119,v119\nM v[148:151],a[68:71],v[236:239],v[148:151]\nds_read_b128 a[144:147],v20 offset:0\nC v89,v90,v91\nv_exp_f32 v120,v120\n" \
"M v[148:151],a[116:119],v[228:231],v[148:151]\nv_exp_f32 v121,v121\nv_xor_b32 v13,0x10000,v13\nM v[152:155],a[120:123],v[228:231],v[152:155]\nds_read_b128 a[148:151],v20 offset:1024\nds_read_b128 a[152:155],v20 offset:2048\nv_exp_f32 v122,v122\nM v[152:155],a[72:75],v[236:239],v[152:155]\n" \
"C v128,v128,v129\nC v90,v92,v93\nC v91,v94,v95\nM v[156:159],a[76:79],v[236:239],v[156:159]\nds_read_b128 a[0:3],v13 offset:32768\nds_read_b128 a[156:159],v20 offset:3072\nC v136,v136,v137\nM v[156:159],a[124:127],v[228:231],v[156:159]\n" \
"s_waitcnt lgkmcnt(5)\nds_read_b128 a[4:7],v13 offset:34816\nC v129,v130,v131\nC v144,v144,v145\nM v[224:227],a[192:195],v[88:91],0\nds_read_b128 a[8:11],v13 offset:36864\nC v137,v138,v139\nv_exp_f32 v123,v123\n" \
"M v[228:231],a[196:199],v[88:91],0\nds_read_b128 a[12:15],v13 offset:38912\nv_add_u32 v20,s65,v20\nM v[232:235],a[200:203],v[88:91],0\nds_read_b128 a[16:19],v13 offset:33792\nC v130,v132,v133\nC v131,v134,v135\nM v[236:239],a[204:207],v[88:91],0\n" \
"ds_read_b128 a[20:23],v13 offset:35840\nC v145,v146,v147\nM v[240:243],a[208:211],v[88:91],0\nds_read_b128 a[24:27],v13 offset:37888\nC v138,v140,v141\nM v[244:247],a[212:215],v[88:91],0\nds_read_b128 a[28:31],v13 offset:39936\nC v152,v152,v153\n" \
"M v[248:251],a[216:219],v[88:91],0\ns_waitcnt lgkmcnt(5)\nv_exp_f32 v124,v124\nM v[252:255],a[220:223],v[88:91],0\nds_write_b128 v20,v[128:131] offset:0\ns_waitcnt vmcnt(0)\nv_cmp_gt_u64 s[98:99],s[14:15],v[58:59]\nv_cmp_le_u64 s[18:19],s[16:17],v[58:59]\n" \
"s_or_b64 s[98:99],s[98:99],s[18:19]\ns_cbranch_scc1 LPK_%=\nLPKR_%=:\nv_and_b32 v58,0xf0,v4\nv_lshl_add_u32 v57,v58,3,s62\ns_mov_b32 s62,0x11111111\ns_mov_b32 s63,s62\nv_cndmask_b32 v62,0,v63,s[62:63]\n" \
"v_accvgpr_write_b32 a224,v62\nv_accvgpr_write_b32 a225,v62\nv_accvgpr_write_b32 a226,v62\nv_accvgpr_write_b32 a227,v62\ns_mov_b32 s62,0x22222222\ns_mov_b32 s63,s62\nv_cndmask_b32 v62,0,v63,s[62:63]\nv_accvgpr_write_b32 a228,v62\n" \
"v_accvgpr_write_b32 a229,v62\nv_accvgpr_write_b32 a230,v62\nv_accvgpr_write_b32 a231,v62\ns_mov_b32 s62,0x44444444\ns_mov_b32 s63,s62\nv_cndmask_b32 v62,0,v63,s[62:63]\nv_accvgpr_write_b32 a232,v62\nv_accvgpr_write_b32 a233,v62\n" \
"v_accvgpr_write_b32 a234,v62\nv_accvgpr_write_b32 a235,v62\ns_mov_b32 s62,0x88888888\ns_mov_b32 s63,s62\nv_cndmask_b32 v62,0,v63,s[62:63]\nv_accvgpr_write_b32 a236,v62\nv_accvgpr_write_b32 a237,v62\nv_accvgpr_write_b32 a238,v62\n" \
"v_accvgpr_write_b32 a239,v62\ns_nop 1\nM v[16:19],a[236:239],v[88:91],v[24:27]\ns_waitcnt vmcnt(5) lgkmcnt(13)\nC v112,v112,v113\nC v139,v142,v143\nM v[72:75],a[0:3],a[144:147],v[48:51]\ns_barrier\n" \
"C v146,v148,v149\nM v[72:75],a[4:7],a[148:151],v[72:75]\nC v147,v150,v151\nv_min_f32 v41,-v60,-v61\nM v[72:75],a[8:11],a[152:155],v[72:75]\ns_waitcnt lgkmcnt(1)\nC v153,v154,v155\nv_min_f32 v42,-v60,-v61\n" \
"M v[72:75],a[12:15],a[156:159],v[72:75]\nv_min_f32 v43,-v60,-v61\nv_min_f32 v28,-v60,-v61\nM v[76:79],a[16:19],a[144:147],v[52:55]\nds_write_b128 v20,v[136:139] offset:1024\nM v[76:79],a[20:23],a[148:151],v[76:79]\nbuffer_load_dwordx4 v22,s[40:43],s45 offen offset:0 sc1 lds\nM v[76:79],a[24:27],a[152:155],v[76:79]\n" \
"v_exp_f32 v125,v125\nM v[76:79],a[28:31],a[156:159],v[76:79]\nds_read_b128 a[96:99],v14 offset:16384\nC v113,v114,v115\nC v154,v156,v157\nM v[80:83],a[0:3],a[160:163],v[40:43]\nC v155,v158,v159\nv_max3_f32 v60,v72,v73,v74\n" \
"M v[80:83],a[4:7],a[164:167],v[80:83]\nds_write_b128 v20,v[144:147] offset:2048\nM v[80:83],a[8:11],a[168:171],v[80:83]\nv_exp_f32 v126,v126\nM v[80:83],a[12:15],a[172:175],v[80:83]\nC v114,v116,v117\nC v120,v120,v121\nM v[88:91],a[0:3],a[176:179],v[44:47]\n" \
"ds_read_b128 a[100:103],v14 offset:17408\nC v115,v118,v119\nv_max3_f32 v61,v75,v76,v77\nM v[88:91],a[4:7],a[180:183],v[88:91]\nv_max3_f32 v60,v60,v78,v79\nv_max3_f32 v60,v60,v61,s76\nv_mov_b32 v61,v60\nM v[88:91],a[8:11],a[184:187],v[88:91]\n" \
"ds_write_b128 v20,v[152:155] offset:3072\nC v121,v122,v123\nM v[24:27],a[232:235],v[112:115],v[16:19]\nv_exp_f32 v127,v127\nM v[88:91],a[12:15],a[188:191],v[88:91]\nC v122,v124,v125\nC v123,v126,v127\nM v[84:87],a[16:19],a[160:163],v[40:43]\n" \
"v_permlane32_swap_b32 v60,v61\nv_max_f32 v60,v60,v61\nM v[24:27],a[236:239],v[120:123],v[24:27]\nds_read_b128 a[104:107],v14 offset:18432\nds_read_b128 a[108:111],v14 offset:19456\nv_mov_b32 v61,v60\nM v[84:87],a[20:23],a[164:167],v[84:87]\nbuffer_load_dwordx4 v22,s[40:43],s45 offen offset:1024 sc1 lds\n" \
"M v[84:87],a[24:27],a[168:171],v[84:87]\nv_permlane16_swap_b32 v60,v61\nv_min_f32 v36,-v60,-v61\nM v[84:87],a[28:31],a[172:175],v[84:87]\nv_max3_f32 v15,v24,v25,v26\nv_max_f32 v15,v15,v27\nv_cmp_nge_f32 vcc,0x53800000,v15\nM v[92:95],a[16:19],a[176:179],v[44:47]\n" \
"v_add_f32 v72,v72,v36\nv_add_f32 v73,v73,v36\nv_add_f32 v74,v74,v36\nM v[92:95],a[20:23],a[180:183],v[92:95]\nbuffer_load_dwordx4 v22,s[40:43],s45 offen offset:2048 sc1 lds\nds_read_b128 a[128:131],v20 offset:0\nds_read_b128 a[132:135],v20 offset:1024\nds_read_b128 a[136:139],v20 offset:2048\n" \
"ds_read_b128 a[140:143],v20 offset:3072\nds_read_b128 a[112:115],v14 offset:20480\ns_cbranch_vccnz Lcs1_%=\nLcr2_%=:\nM v[92:95],a[24:27],a[184:187],v[92:95]\nv_exp_f32 v72,v72\nv_add_f32 v75,v75,v36\nM v[92:95],a[28:31],a[188:191],v[92:95]\n" \
"s_waitcnt lgkmcnt(5)\nds_read_b128 a[116:119],v14 offset:21504\nv_add_f32 v76,v76,v36\nv_exp_f32 v73,v73\nM v[192:195],a[96:99],v[112:115],0\nds_read_b128 a[120:123],v14 offset:22528\nds_read_b128 a[124:127],v14 offset:23552\nv_exp_f32 v74,v74\n" \
"M v[224:227],a[96:99],v[120:123],v[224:227]\nbuffer_load_dwordx4 v22,s[40:43],s45 offen offset:3072 sc1 lds\nv_exp_f32 v75,v75\nM v[228:231],a[100:103],v[120:123],v[228:231]\ns_add_u32 s45,s45,s49\ns_add_u32 m0,s22,49152\nv_exp_f32 v76,v76\nv_add_f32 v77,v77,v36\n" \
"M v[196:199],a[100:103],v[112:115],0\ns_waitcnt lgkmcnt(4)\nv_exp_f32 v77,v77\nv_add_f32 v78,v78,v36\nM v[200:203],a[104:107],v[112:115],0\nds_read_b128 a[48:51],v13 offset:49152\nv_exp_f32 v78,v78\nM v[232:235],a[104:107],v[120:123],v[232:235]\n" \
"buffer_load_dwordx4 v22,s[40:43],s45 offen offset:0 sc1 lds\nv_add_f32 v79,v79,v36\nM v[236:239],a[108:111],v[120:123],v[236:239]\ns_waitcnt vmcnt(6) lgkmcnt(1)\nds_read_b128 a[52:55],v13 offset:51200\nv_min_f32 v37,-v60,-v61\nv_exp_f32 v79,v79\nM v[204:207],a[108:111],v[112:115],0\n" \
"s_barrier\nds_read_b128 a[56:59],v13 offset:53248\nv_exp_f32 v80,v80\nM v[208:211],a[112:115],v[112:115],0\nds_read_b128 a[60:63],v13 offset:55296\nv_min_f32 v38,-v60,-v61\nv_exp_f32 v81,v81\nM v[240:243],a[112:115],v[120:123],v[240:243]\n" \
"buffer_load_dwordx4 v22,s[40:43],s45 offen offset:1024 sc1 lds\nds_read_b128 a[64:67],v13 offset:50176\nM v[244:247],a[116:119],v[120:123],v[244:247]\nds_read_b128 a[68:71],v13 offset:52224\nv_min_f32 v39,-v60,-v61\nv_exp_f32 v82,v82\nM v[212:215],a[116:119],v[112:115],0\nds_read_b128 a[72:75],v13 offset:54272\n" \
"v_min_f32 v31,-v60,-v61\nv_exp_f32 v83,v83\nM v[216:219],a[120:123],v[112:115],0\nds_read_b128 a[76:79],v13 offset:56320\nC v72,v72,v73\nv_exp_f32 v84,v84\nM v[248:251],a[120:123],v[120:123],v[248:251]\nbuffer_load_dwordx4 v22,s[40:43],s45 offen offset:2048 sc1 lds\n" \
"M v[252:255],a[124:127],v[120:123],v[252:255]\nv_exp_f32 v85,v85\nM v[220:223],a[124:127],v[112:115],0\ns_waitcnt lgkmcnt(3)\nv_exp_f32 v86,v86\nM v[96:99],a[48:51],a[128:131],v[48:51]\nv_exp_f32 v87,v87\nM v[96:99],a[52:55],a[132:135],v[96:99]\n" \
"buffer_load_dwordx4 v22,s[40:43],s45 offen offset:3072 sc1 lds\nM v[96:99],a[56:59],a[136:139],v[96:99]\ns_add_u32 m0,s22,65536\ns_add_u32 s45,s45,s49\nv_exp_f32 v88,v88\nM v[96:99],a[60:63],a[140:143],v[96:99]\nLG15_%=:\ns_cbranch_execz LHR1_%=\n" \
"v_exp_f32 v89,v89\nM v[100:103],a[64:67],a[128:131],v[52:55]\ns_waitcnt lgkmcnt(0)\nv_exp_f32 v90,v90\nM v[100:103],a[68:71],a[132:135],v[100:103]\nbuffer_load_dwordx4 v22,s[40:43],s48 offen offset:0 sc1 lds\nM v[100:103],a[72:75],a[136:139],v[100:103]\nv_exp_f32 v91,v91\n" \
"M v[100:103],a[76:79],a[140:143],v[100:103]\nv_exp_f32 v92,v92\nM v[104:107],a[48:51],a[144:147],v[36:39]\nv_max3_f32 v60,v96,v97,v98\nC v73,v74,v75\nM v[104:107],a[52:55],a[148:151],v[104:107]\nbuffer_load_dwordx4 v22,s[40:43],s48 offen offset:1024 sc1 lds\nM v[104:107],a[56:59],a[152:155],v[104:107]\n" \
"v_max3_f32 v61,v99,v100,v101\nv_max3_f32 v60,v60,v102,v103\nM v[104:107],a[60:63],a[156:159],v[104:107]\nv_max3_f32 v60,v60,v61,s76\nv_mov_b32 v61,v60\nM v[112:115],a[48:51],a[160:163],v[40:43]\nv_exp_f32 v93,v93\nM v[112:115],a[52:55],a[164:167],v[112:115]\n" \
"buffer_load_dwordx4 v22,s[40:43],s48 offen offset:2048 sc1 lds\nM v[112:115],a[56:59],a[168:171],v[112:115]\nv_permlane32_swap_b32 v60,v61\nM v[112:115],a[60:63],a[172:175],v[112:115]\nC v80,v80,v81\nC v88,v88,v89\nM v[120:123],a[48:51],a[176:179],v[44:47]\nv_max_f32 v60,v60,v61\n" \
"C v74,v76,v77\nM v[120:123],a[52:55],a[180:183],v[120:123]\nbuffer_load_dwordx4 v22,s[40:43],s48 offen offset:3072 sc1 lds\nv_exp_f32 v94,v94\nM v[120:123],a[56:59],a[184:187],v[120:123]\ns_add_u32 m0,s22,98304\nC v75,v78,v79\nC v81,v82,v83\n" \
"M v[120:123],a[60:63],a[188:191],v[120:123]\ns_add_u32 s48,s48,s49\nv_mov_b32 v61,v60\nC v89,v90,v91\nM v[108:111],a[64:67],a[144:147],v[36:39]\nv_xor_b32 v14,0x10000,v14\nC v82,v84,v85\nM v[16:19],a[228:231],v[72:75],v[24:27]\n" \
"buffer_load_dwordx4 v22,s[40:43],s45 offen offset:0 sc1 lds\nv_exp_f32 v95,v95\nM v[108:111],a[68:71],a[148:151],v[108:111]\nds_read_b128 a[0:3],v14 offset:0\nC v83,v86,v87\nM v[108:111],a[72:75],a[152:155],v[108:111]\nv_permlane16_swap_b32 v60,v61\nM v[16:19],a[232:235],v[80:83],v[16:19]\n" \
"ds_read_b128 a[4:7],v14 offset:1024\nC v90,v92,v93\nM v[108:111],a[76:79],a[156:159],v[108:111]\nbuffer_load_dwordx4 v22,s[40:43],s45 offen offset:1024 sc1 lds\nC v91,v94,v95\nM v[116:119],a[64:67],a[160:163],v[40:43]\nds_read_b128 a[8:11],v14 offset:2048\nv_min_f32 v32,-v60,-v61\n" \
"M v[16:19],a[236:239],v[88:91],v[16:19]\nv_xor_b32 v13,s54,v13\nv_min_f32 v33,-v60,-v61\nM v[116:119],a[68:71],a[164:167],v[116:119]\nds_read_b128 a[12:15],v14 offset:3072\nv_min_f32 v34,-v60,-v61\nM v[116:119],a[72:75],a[168:171],v[116:119]\nbuffer_load_dwordx4 v22,s[40:43],s45 offen offset:2048 sc1 lds\n" \
"v_min_f32 v35,-v60,-v61\nM v[116:119],a[76:79],a[172:175],v[116:119]\nv_max3_f32 v15,v16,v17,v18\nv_max_f32 v15,v15,v19\nv_cmp_nge_f32 vcc,0x53800000,v15\nM v[124:127],a[64:67],a[176:179],v[44:47]\ns_waitcnt lgkmcnt(2)\nv_min_f32 v30,-v60,-v61\n" \
"v_add_f32 v96,v96,v32\nv_add_f32 v97,v97,v32\nM v[124:127],a[68:71],a[180:183],v[124:127]\ns_cbranch_vccnz Lcs3_%=\nLcr4_%=:\nv_add_f32 v98,v98,v32\nv_add_f32 v99,v99,v32\nv_add_f32 v100,v100,v32\n" \
"v_add_f32 v101,v101,v32\nv_add_f32 v102,v102,v32\nv_add_f32 v103,v103,v32\nM v[124:127],a[72:75],a[184:187],v[124:127]\ns_cmp_eq_u32 s52,s55\nbuffer_load_dwordx4 v22,s[40:43],s45 offen offset:3072 sc1 lds\nds_write_b128 v23,v[28:31] offset:0\nds_read_b128 a[16:19],v14 offset:4096\n" \
"ds_read_b128 a[20:23],v14 offset:5120\nds_read_b128 a[24:27],v14 offset:6144\nds_read_b128 a[28:31],v14 offset:7168\nM v[124:127],a[76:79],a[188:191],v[124:127]\ns_cbranch_scc1 LH1_%=\nLH1R0_%=:\ns_add_u32 m0,s22,16384\nv_exp_f32 v96,v96\n" \
"M v[160:163],a[0:3],v[72:75],0\ns_add_u32 s45,s45,s49\nv_exp_f32 v97,v97\nM v[192:195],a[0:3],v[80:83],v[192:195]\nv_exp_f32 v98,v98\nM v[224:227],a[0:3],v[88:91],v[224:227]\nbuffer_load_dwordx4 v22,s[40:43],s61 offen offset:0 sc1 lds\nM v[228:231],a[4:7],v[88:91],v[228:231]\n" \
"v_exp_f32 v99,v99\nM v[196:199],a[4:7],v[80:83],v[196:199]\ns_waitcnt vmcnt(13) lgkmcnt(0)\nv_exp_f32 v100,v100\nM v[164:167],a[4:7],v[72:75],0\ns_barrier\nv_xor_b32 v59,0x2000,v23\nM v[168:171],a[8:11],v[72:75],0\n" \
"buffer_load_dwordx4 v22,s[40:43],s61 offen offset:1024 sc1 lds\nM v[200:203],a[8:11],v[80:83],v[200:203]\nds_read_b128 v[28:31],v59 offset:0\nv_xor_b32 v13,0x10000,v13\nv_exp_f32 v101,v101\nM v[232:235],a[8:11],v[88:91],v[232:235]\nv_exp_f32 v102,v102\nM v[236:239],a[12:15],v[88:91],v[236:239]\n" \
"v_exp_f32 v103,v103\nM v[204:207],a[12:15],v[80:83],v[204:207]\nbuffer_load_dwordx4 v22,s[40:43],s61 offen offset:2048 sc1 lds\nv_exp_f32 v104,v104\nM v[172:175],a[12:15],v[72:75],0\nv_exp_f32 v105,v105\nM v[176:179],a[16:19],v[72:75],0\nv_exp_f32 v106,v106\n" \
"M v[208:211],a[16:19],v[80:83],v[208:211]\ns_waitcnt lgkmcnt(0)\nds_read_b128 a[192:195],v13 offset:32768\nds_read_b128 a[196:199],v13 offset:34816\nv_exp_f32 v107,v107\nM v[240:243],a[16:19],v[88:91],v[240:243]\nbuffer_load_dwordx4 v22,s[40:43],s61 offen offset:3072 sc1 lds\nv_exp_f32 v108,v108\n" \
"M v[244:247],a[20:23],v[88:91],v[244:247]\ns_add_u32 m0,s22,81920\nv_exp_f32 v109,v109\nM v[212:215],a[20:23],v[80:83],v[212:215]\nds_read_b128 a[200:203],v13 offset:36864\nC v96,v96,v97\nv_exp_f32 v110,v110\nM v[180:183],a[20:23],v[72:75],0\n" \
"ds_read_b128 a[204:207],v13 offset:38912\nC v97,v98,v99\nv_exp_f32 v111,v111\nM v[184:187],a[24:27],v[72:75],0\nv_exp_f32 v112,v112\nM v[216:219],a[24:27],v[80:83],v[216:219]\nv_exp_f32 v113,v113\nM v[248:251],a[24:27],v[88:91],v[248:251]\n" \
"v_exp_f32 v114,v114\nM v[252:255],a[28:31],v[88:91],v[252:255]\nbuffer_load_dwordx4 v22,s[40:43],s48 offen offset:0 sc1 lds\nM v[220:223],a[28:31],v[80:83],v[220:223]\nv_cndmask_b32 v32,v32,v28,s[66:67]\nv_cndmask_b32 v33,v33,v28,s[66:67]\nv_cndmask_b32 v34,v34,v28,s[66:67]\nv_cndmask_b32 v35,v35,v28,s[66:67]\n" \
"M v[188:191],a[28:31],v[72:75],0\ns_waitcnt lgkmcnt(0)\nds_read_b128 a[208:211],v13 offset:33792\nds_read_b128 a[212:215],v13 offset:35840\nM v[64:67],a[192:195],a[128:131],v[32:35]\nC v98,v100,v101\nC v99,v102,v103\nv_exp_f32 v115,v115\n" \
"M v[64:67],a[196:199],a[132:135],v[64:67]\nds_read_b128 a[216:219],v13 offset:37888\nC v104,v104,v105\nM v[24:27],a[224:227],v[96:99],v[16:19]\nds_read_b128 a[220:223],v13 offset:39936\nC v105,v106,v107\nM v[64:67],a[200:203],a[136:139],v[64:67]\nv_cndmask_b32 v36,v36,v29,s[66:67]\n" \
"v_cndmask_b32 v37,v37,v29,s[66:67]\nv_cndmask_b32 v38,v38,v29,s[66:67]\nv_cndmask_b32 v39,v39,v29,s[66:67]\nM v[64:67],a[204:207],a[140:143],v[64:67]\nv_exp_f32 v116,v116\nM v[72:75],a[192:195],a[144:147],v[36:39]\nC v106,v108,v109\nC v107,v110,v111\n" \
"M v[72:75],a[196:199],a[148:151],v[72:75]\nbuffer_load_dwordx4 v22,s[40:43],s48 offen offset:1024 sc1 lds\nM v[24:27],a[228:231],v[104:107],v[24:27]\nv_exp_f32 v117,v117\nM v[72:75],a[200:203],a[152:155],v[72:75]\nv_cndmask_b32 v40,v40,v30,s[66:67]\nv_cndmask_b32 v41,v41,v30,s[66:67]\nv_cndmask_b32 v42,v42,v30,s[66:67]\n" \
"v_cndmask_b32 v43,v43,v30,s[66:67]\nM v[72:75],a[204:207],a[156:159],v[72:75]\ns_waitcnt vmcnt(2)\nv_exp_f32 v118,v118\nM v[80:83],a[192:195],a[160:163],v[40:43]\ns_barrier\nv_cndmask_b32 v44,v44,v31,s[66:67]\nM v[80:83],a[196:199],a[164:167],v[80:83]\n" \
"buffer_load_dwordx4 v22,s[40:43],s48 offen offset:2048 sc1 lds\nM v[80:83],a[200:203],a[168:171],v[80:83]\nv_cndmask_b32 v45,v45,v31,s[66:67]\nv_cndmask_b32 v46,v46,v31,s[66:67]\nv_cndmask_b32 v47,v47,v31,s[66:67]\nM v[80:83],a[204:207],a[172:175],v[80:83]\nv_exp_f32 v119,v119\nM v[88:91],a[192:195],a[176:179],v[44:47]\n" \
"C v112,v112,v113\nC v113,v114,v115\nM v[88:91],a[196:199],a[180:183],v[88:91]\nbuffer_load_dwordx4 v22,s[40:43],s48 offen offset:3072 sc1 lds\nM v[88:91],a[200:203],a[184:187],v[88:91]\ns_add_u32 m0,s22,114688\nC v114,v116,v117\nC v115,v118,v119\n" \
"M v[88:91],a[204:207],a[188:191],v[88:91]\ns_add_u32 s48,s48,s49\nv_exp_f32 v120,v120\nM v[24:27],a[232:235],v[112:115],v[24:27]\ns_waitcnt lgkmcnt(0)\nv_exp_f32 v121,v121\nM v[68:71],a[208:211],a[128:131],v[32:35]\nv_exp_f32 v122,v122\n" \
"M v[68:71],a[212:215],a[132:135],v[68:71]\nv_exp_f32 v123,v123\nM v[68:71],a[216:219],a[136:139],v[68:71]\nv_exp_f32 v124,v124\nM v[68:71],a[220:223],a[140:143],v[68:71]\nv_exp_f32 v125,v125\nM v[76:79],a[208:211],a[144:147],v[36:39]\nv_exp_f32 v126,v126\n" \
"M v[76:79],a[212:215],a[148:151],v[76:79]\nv_exp_f32 v127,v127\nM v[76:79],a[216:219],a[152:155],v[76:79]\nds_read_b128 a[192:195],v14 offset:16384\nC v120,v120,v121\nC v121,v122,v123\nC v122,v124,v125\nC v123,v126,v127\n" \
"M v[76:79],a[220:223],a[156:159],v[76:79]\nbuffer_load_dwordx4 v22,s[40:43],s45 offen offset:0 sc1 lds\nM v[24:27],a[236:239],v[120:123],v[24:27]\nds_read_b128 a[196:199],v14 offset:17408\nv_lshrrev_b32 v58,8,v4\nM v[84:87],a[208:211],a[160:163],v[40:43]\nds_read_b128 a[200:203],v14 offset:18432\nv_lshl_add_u32 v57,v58,27,v57\n" \
"M v[84:87],a[212:215],a[164:167],v[84:87]\nds_read_b128 a[204:207],v14 offset:19456\nv_max3_f32 v15,v24,v25,v26\nM v[84:87],a[216:219],a[168:171],v[84:87]\nbuffer_load_dwordx4 v22,s[40:43],s45 offen offset:1024 sc1 lds\nM v[84:87],a[220:223],a[172:175],v[84:87]\nv_max_f32 v15,v15,v27\nv_cmp_nge_f32 vcc,0x53800000,v15\n" \
"M v[92:95],a[208:211],a[176:179],v[44:47]\nds_read_b128 a[208:211],v14 offset:20480\nM v[92:95],a[212:215],a[180:183],v[92:95]\ns_cbranch_vccnz Lcs5_%=\nLcr6_%=:\nds_read_b128 a[212:215],v14 offset:21504\nM v[92:95],a[216:219],a[184:187],v[92:95]\nbuffer_load_dwordx4 v22,s[40:43],s45 offen offset:2048 sc1 lds\n" \
"M v[92:95],a[220:223],a[188:191],v[92:95]\ns_waitcnt lgkmcnt(2)\nds_read_b128 a[216:219],v14 offset:22528\nM v[128:131],a[192:195],v[96:99],0\nds_read_b128 a[220:223],v14 offset:23552\nv_xor_b32 v14,s54,v14\nM v[160:163],a[192:195],v[104:107],v[160:163]\nv_exp_f32 v64,v64\n" \
"M v[192:195],a[192:195],v[112:115],v[192:195]\nbuffer_load_dwordx4 v22,s[40:43],s45 offen offset:3072 sc1 lds\nM v[224:227],a[192:195],v[120:123],v[224:227]\ns_add_u32 m0,s22,0\nv_exp_f32 v65,v65\nM v[228:231],a[196:199],v[120:123],v[228:231]\ns_add_u32 s45,s45,s49\nv_exp_f32 v66,v66\n" \
"M v[196:199],a[196:199],v[112:115],v[196:199]\nv_exp_f32 v67,v67\nM v[164:167],a[196:199],v[104:107],v[164:167]\ns_waitcnt vmcnt(16) lgkmcnt(0)\nv_exp_f32 v68,v68\nM v[132:135],a[196:199],v[96:99],0\ns_barrier\nds_read_b128 a[192:195],v13 offset:49152\n" \
"M v[136:139],a[200:203],v[96:99],0\nbuffer_load_dwordx4 v22,s[40:43],s48 offen offset:0 sc1 lds\nM v[168:171],a[200:203],v[104:107],v[168:171]\nv_exp_f32 v69,v69\nM v[200:203],a[200:203],v[112:115],v[200:203]\nv_exp_f32 v70,v70\nM v[232:235],a[200:203],v[120:123],v[232:235]\nv_exp_f32 v71,v71\n" \
"M v[236:239],a[204:207],v[120:123],v[236:239]\nbuffer_load_dwordx4 v22,s[40:43],s48 offen offset:1024 sc1 lds\nM v[204:207],a[204:207],v[112:115],v[204:207]\nds_read_b128 a[196:199],v13 offset:51200\nC v64,v64,v65\nM v[172:175],a[204:207],v[104:107],v[172:175]\nv_exp_f32 v72,v72\nM v[140:143],a[204:207],v[96:99],0\n" \
"ds_read_b128 a[200:203],v13 offset:53248\nds_read_b128 a[204:207],v13 offset:55296\nM v[144:147],a[208:211],v[96:99],0\nbuffer_load_dwordx4 v22,s[40:43],s48 offen offset:2048 sc1 lds\nM v[176:179],a[208:211],v[104:107],v[176:179]\nv_exp_f32 v73,v73\nM v[208:211],a[208:211],v[112:115],v[208:211]\nv_exp_f32 v74,v74\n" \
"M v[240:243],a[208:211],v[120:123],v[240:243]\nv_exp_f32 v75,v75\nM v[244:247],a[212:215],v[120:123],v[244:247]\nbuffer_load_dwordx4 v22,s[40:43],s48 offen offset:3072 sc1 lds\nM v[212:215],a[212:215],v[112:115],v[212:215]\ns_add_u32 m0,s53,32768\nv_exp_f32 v76,v76\nM v[180:183],a[212:215],v[104:107],v[180:183]\n" \
"s_add_u32 s52,s52,1\nv_exp_f32 v77,v77\nM v[148:151],a[212:215],v[96:99],0\ns_add_u32 s48,s48,s49\nv_exp_f32 v78,v78\nM v[152:155],a[216:219],v[96:99],0\nbuffer_load_dwordx4 v22,s[40:43],s45 offen offset:0 sc1 lds\nM v[184:187],a[216:219],v[104:107],v[184:187]\n" \
"v_exp_f32 v79,v79\nM v[216:219],a[216:219],v[112:115],v[216:219]\nv_exp_f32 v80,v80\nM v[248:251],a[216:219],v[120:123],v[248:251]\nC v65,v66,v67\nC v66,v68,v69\nM v[252:255],a[220:223],v[120:123],v[252:255]\nbuffer_load_dwordx4 v22,s[40:43],s45 offen offset:1024 sc1 lds\n" \
"M v[220:223],a[220:223],v[112:115],v[220:223]\ns_waitcnt lgkmcnt(0)\nv_exp_f32 v81,v81\nM v[188:191],a[220:223],v[104:107],v[188:191]\ns_cmp_eq_u32 s52,s56\nC v67,v70,v71\nC v72,v72,v73\nM v[156:159],a[220:223],v[96:99],0\n" \
"s_cbranch_scc1 LEV_%=\nLEVR0_%=:\nds_read_b128 a[208:211],v13 offset:50176\nds_read_b128 a[212:215],v13 offset:52224\nM v[96:99],a[192:195],a[128:131],v[32:35]\nbuffer_load_dwordx4 v22,s[40:43],s45 offen offset:2048 sc1 lds\nM v[96:99],a[196:199],a[132:135],v[96:99]\nv_exp_f32 v82,v82\n" \
"M v[16:19],a[224:227],v[64:67],v[24:27]\nds_read_b128 a[216:219],v13 offset:54272\nC v73,v74,v75\nM v[96:99],a[200:203],a[136:139],v[96:99]\nds_read_b128 a[220:223],v13 offset:56320\nC v74,v76,v77\nM v[96:99],a[204:207],a[140:143],v[96:99]\nv_exp_f32 v83,v83\n" \
"M v[104:107],a[192:195],a[144:147],v[36:39]\nC v75,v78,v79\nC v80,v80,v81\nM v[104:107],a[196:199],a[148:151],v[104:107]\nv_exp_f32 v84,v84\nM v[16:19],a[228:231],v[72:75],v[16:19]\nbuffer_load_dwordx4 v22,s[40:43],s45 offen offset:3072 sc1 lds\nM v[104:107],a[200:203],a[152:155],v[104:107]\n" \
"s_add_u32 m0,s53,16384\nv_exp_f32 v85,v85\nM v[104:107],a[204:207],a[156:159],v[104:107]\ns_add_u32 s45,s45,s49\nv_exp_f32 v86,v86\nM v[112:115],a[192:195],a[160:163],v[40:43]\nv_exp_f32 v87,v87\nM v[112:115],a[196:199],a[164:167],v[112:115]\n" \
"buffer_load_dwordx4 v22,s[40:43],s48 offen offset:0 sc1 lds\nM v[112:115],a[200:203],a[168:171],v[112:115]\nv_exp_f32 v88,v88\nM v[112:115],a[204:207],a[172:175],v[112:115]\nC v81,v82,v83\nC v82,v84,v85\nM v[120:123],a[192:195],a[176:179],v[44:47]\nv_exp_f32 v89,v89\n" \
"M v[120:123],a[196:199],a[180:183],v[120:123]\nbuffer_load_dwordx4 v22,s[40:43],s48 offen offset:1024 sc1 lds\nM v[120:123],a[200:203],a[184:187],v[120:123]\nC v83,v86,v87\nC v88,v88,v89\nM v[120:123],a[204:207],a[188:191],v[120:123]\nv_exp_f32 v90,v90\nM v[16:19],a[232:235],v[80:83],v[16:19]\n" \
"s_waitcnt lgkmcnt(0)\nv_exp_f32 v91,v91\nM v[100:103],a[208:211],a[128:131],v[32:35]\nbuffer_load_dwordx4 v22,s[40:43],s48 offen offset:2048 sc1 lds\nM v[100:103],a[212:215],a[132:135],v[100:103]\nv_exp_f32 v92,v92\nM v[100:103],a[216:219],a[136:139],v[100:103]\nv_exp_f32 v93,v93\n" \
"M v[100:103],a[220:223],a[140:143],v[100:103]\nv_exp_f32 v94,v94\nM v[108:111],a[208:211],a[144:147],v[36:39]\nbuffer_load_dwordx4 v22,s[40:43],s48 offen offset:3072 sc1 lds\nM v[108:111],a[212:215],a[148:151],v[108:111]\ns_add_u32 m0,s53,49152\nv_exp_f32 v95,v95\nv_xor_b32 v14,0x10000,v14\n" \
"M v[108:111],a[216:219],a[152:155],v[108:111]\ns_xor_b32 s53,s53,0x10000\nds_read_b128 a[192:195],v14 offset:0\nC v89,v90,v91\nC v90,v92,v93\nC v91,v94,v95\nM v[108:111],a[220:223],a[156:159],v[108:111]\ns_add_u32 s48,s48,s49\n" \
"ds_read_b128 a[196:199],v14 offset:1024\nds_read_b128 a[200:203],v14 offset:2048\nM v[16:19],a[236:239],v[88:91],v[16:19]\nds_read_b128 a[204:207],v14 offset:3072\nM v[116:119],a[208:211],a[160:163],v[40:43]\nM v[116:119],a[212:215],a[164:167],v[116:119]\nv_max3_f32 v15,v16,v17,v18\nv_max_f32 v15,v15,v19\n" \
"M v[116:119],a[216:219],a[168:171],v[116:119]\nv_cmp_nge_f32 vcc,0x53800000,v15\nM v[116:119],a[220:223],a[172:175],v[116:119]\nM v[124:127],a[208:211],a[176:179],v[44:47]\ns_waitcnt lgkmcnt(1)\nds_read_b128 a[208:211],v14 offset:4096\nM v[124:127],a[212:215],a[180:183],v[124:127]\ns_cbranch_vccnz Lcs7_%=\n" \
"Lcr8_%=:\nds_read_b128 a[212:215],v14 offset:5120\nM v[124:127],a[216:219],a[184:187],v[124:127]\ns_cmp_eq_u32 s52,s55\nds_read_b128 a[216:219],v14 offset:6144\nM v[124:127],a[220:223],a[188:191],v[124:127]\ns_cbranch_scc1 LH1_%=\nLH1R1_%=:\n" \
"ds_read_b128 a[220:223],v14 offset:7168\nv_xor_b32 v13,0x10000,v13\nM v[128:131],a[192:195],v[64:67],v[128:131]\nv_exp_f32 v96,v96\nM v[160:163],a[192:195],v[72:75],v[160:163]\nv_exp_f32 v97,v97\nM v[192:195],a[192:195],v[80:83],v[192:195]\nv_exp_f32 v98,v98\n" \
"M v[224:227],a[192:195],v[88:91],v[224:227]\nv_exp_f32 v99,v99\nM v[228:231],a[196:199],v[88:91],v[228:231]\nv_exp_f32 v100,v100\nM v[196:199],a[196:199],v[80:83],v[196:199]\nv_exp_f32 v101,v101\nM v[164:167],a[196:199],v[72:75],v[164:167]\ns_waitcnt vmcnt(16) lgkmcnt(0)\n" \
"v_exp_f32 v102,v102\nM v[132:135],a[196:199],v[64:67],v[132:135]\ns_barrier\nds_read_b128 a[192:195],v13 offset:32768\nM v[136:139],a[200:203],v[64:67],v[136:139]\nbuffer_load_dwordx4 v22,s[40:43],s45 offen offset:0 sc1 lds\nM v[168:171],a[200:203],v[72:75],v[168:171]\nv_exp_f32 v103,v103\n" \
"M v[200:203],a[200:203],v[80:83],v[200:203]\nv_exp_f32 v104,v104\nM v[232:235],a[200:203],v[88:91],v[232:235]\nv_exp_f32 v105,v105\nM v[236:239],a[204:207],v[88:91],v[236:239]\nbuffer_load_dwordx4 v22,s[40:43],s45 offen offset:1024 sc1 lds\nM v[204:207],a[204:207],v[80:83],v[204:207]\nds_read_b128 a[196:199],v13 offset:34816\n" \
"C v96,v96,v97\nM v[172:175],a[204:207],v[72:75],v[172:175]\nv_exp_f32 v106,v106\nM v[140:143],a[204:207],v[64:67],v[140:143]\nds_read_b128 a[200:203],v13 offset:36864\nds_read_b128 a[204:207],v13 offset:38912\nM v[144:147],a[208:211],v[64:67],v[144:147]\nbuffer_load_dwordx4 v22,s[40:43],s45 offen offset:2048 sc1 lds\n" \
"M v[176:179],a[208:211],v[72:75],v[176:179]\nv_exp_f32 v107,v107\nM v[208:211],a[208:211],v[80:83],v[208:211]\nv_exp_f32 v108,v108\nM v[240:243],a[208:211],v[88:91],v[240:243]\nv_exp_f32 v109,v109\nM v[244:247],a[212:215],v[88:91],v[244:247]\nbuffer_load_dwordx4 v22,s[40:43],s45 offen offset:3072 sc1 lds\n" \
"M v[212:215],a[212:215],v[80:83],v[212:215]\ns_add_u32 m0,s53,0\nv_exp_f32 v110,v110\nM v[180:183],a[212:215],v[72:75],v[180:183]\ns_add_u32 s45,s45,s49\nv_exp_f32 v111,v111\nM v[148:151],a[212:215],v[64:67],v[148:151]\nv_exp_f32 v112,v112\n" \
"M v[152:155],a[216:219],v[64:67],v[152:155]\nbuffer_load_dwordx4 v22,s[40:43],s48 offen offset:0 sc1 lds\nM v[184:187],a[216:219],v[72:75],v[184:187]\nv_exp_f32 v113,v113\nM v[216:219],a[216:219],v[80:83],v[216:219]\nC v97,v98,v99\nC v98,v100,v101\nM v[248:251],a[216:219],v[88:91],v[248:251]\n" \
"v_exp_f32 v114,v114\nM v[252:255],a[220:223],v[88:91],v[252:255]\nbuffer_load_dwordx4 v22,s[40:43],s48 offen offset:1024 sc1 lds\nM v[220:223],a[220:223],v[80:83],v[220:223]\nC v99,v102,v103\nC v104,v104,v105\nM v[188:191],a[220:223],v[72:75],v[188:191]\nv_exp_f32 v115,v115\n" \
"M v[156:159],a[220:223],v[64:67],v[156:159]\ns_waitcnt lgkmcnt(0)\nv_exp_f32 v116,v116\nM v[64:67],a[192:195],a[128:131],v[32:35]\nbuffer_load_dwordx4 v22,s[40:43],s48 offen offset:2048 sc1 lds\nM v[64:67],a[196:199],a[132:135],v[64:67]\nds_read_b128 a[208:211],v13 offset:33792\nC v105,v106,v107\n" \
"M v[24:27],a[224:227],v[96:99],v[16:19]\nds_read_b128 a[212:215],v13 offset:35840\nC v106,v108,v109\nM v[64:67],a[200:203],a[136:139],v[64:67]\nds_read_b128 a[216:219],v13 offset:37888\nC v107,v110,v111\nM v[64:67],a[204:207],a[140:143],v[64:67]\nbuffer_load_dwordx4 v22,s[40:43],s48 offen offset:3072 sc1 lds\n" \
"M v[72:75],a[192:195],a[144:147],v[36:39]\ns_add_u32 m0,s53,32768\nds_read_b128 a[220:223],v13 offset:39936\nC v112,v112,v113\nM v[72:75],a[196:199],a[148:151],v[72:75]\ns_add_u32 s48,s48,s49\nv_exp_f32 v117,v117\nM v[24:27],a[228:231],v[104:107],v[24:27]\n" \
"v_exp_f32 v118,v118\nM v[72:75],a[200:203],a[152:155],v[72:75]\nv_exp_f32 v119,v119\nM v[72:75],a[204:207],a[156:159],v[72:75]\nv_exp_f32 v120,v120\nM v[80:83],a[192:195],a[160:163],v[40:43]\nv_exp_f32 v121,v121\nM v[80:83],a[196:199],a[164:167],v[80:83]\n" \
"C v113,v114,v115\nC v114,v116,v117\nM v[80:83],a[200:203],a[168:171],v[80:83]\nv_exp_f32 v122,v122\nM v[80:83],a[204:207],a[172:175],v[80:83]\nC v115,v118,v119\nC v120,v120,v121\nM v[88:91],a[192:195],a[176:179],v[44:47]\n" \
"v_exp_f32 v123,v123\nM v[88:91],a[196:199],a[180:183],v[88:91]\nv_exp_f32 v124,v124\nM v[88:91],a[200:203],a[184:187],v[88:91]\nv_exp_f32 v125,v125\nM v[88:91],a[204:207],a[188:191],v[88:91]\nds_read_b128 a[192:195],v14 offset:16384\nC v121,v122,v123\n" \
"M v[24:27],a[232:235],v[112:115],v[24:27]\ns_waitcnt lgkmcnt(1)\nv_exp_f32 v126,v126\nM v[68:71],a[208:211],a[128:131],v[32:35]\nds_read_b128 a[196:199],v14 offset:17408\nC v122,v124,v125\nM v[68:71],a[212:215],a[132:135],v[68:71]\nv_exp_f32 v127,v127\n" \
"M v[68:71],a[216:219],a[136:139],v[68:71]\nds_read_b128 a[200:203],v14 offset:18432\nC v123,v126,v127\nM v[68:71],a[220:223],a[140:143],v[68:71]\nds_read_b128 a[204:207],v14 offset:19456\nM v[76:79],a[208:211],a[144:147],v[36:39]\nM v[76:79],a[212:215],a[148:151],v[76:79]\nM v[76:79],a[216:219],a[152:155],v[76:79]\n" \
"M v[76:79],a[220:223],a[156:159],v[76:79]\nM v[24:27],a[236:239],v[120:123],v[24:27]\nM v[84:87],a[208:211],a[160:163],v[40:43]\ns_mov_b64 exec,0xff\nglobal_load_dword v0,v2,s[100:101]\ns_mov_b64 exec,-1\nM v[84:87],a[212:215],a[164:167],v[84:87]\nv_max3_f32 v15,v24,v25,v26\n" \
"v_max_f32 v15,v15,v27\nM v[84:87],a[216:219],a[168:171],v[84:87]\nv_cmp_nge_f32 vcc,0x53800000,v15\nM v[84:87],a[220:223],a[172:175],v[84:87]\nM v[92:95],a[208:211],a[176:179],v[44:47]\nds_read_b128 a[208:211],v14 offset:20480\nM v[92:95],a[212:215],a[180:183],v[92:95]\ns_cbranch_vccnz Lcs9_%=\n" \
"Lcr10_%=:\nds_read_b128 a[212:215],v14 offset:21504\nM v[92:95],a[216:219],a[184:187],v[92:95]\nLG16_%=:\ns_cbranch_execz LHR2_%=\nds_read_b128 a[216:219],v14 offset:22528\nM v[92:95],a[220:223],a[188:191],v[92:95]\ns_waitcnt lgkmcnt(3)\n" \
"ds_read_b128 a[220:223],v14 offset:23552\nM v[128:131],a[192:195],v[96:99],v[128:131]\nv_exp_f32 v64,v64\nM v[160:163],a[192:195],v[104:107],v[160:163]\nv_exp_f32 v65,v65\nM v[192:195],a[192:195],v[112:115],v[192:195]\nbuffer_load_dword v58,v57,s[84:87],s74 offen\nM v[224:227],a[192:195],v[120:123],v[224:227]\n" \
"s_add_u32 s74,s74,2048\nv_exp_f32 v66,v66\nM v[228:231],a[196:199],v[120:123],v[228:231]\nv_exp_f32 v67,v67\nM v[196:199],a[196:199],v[112:115],v[196:199]\nv_exp_f32 v68,v68\nM v[164:167],a[196:199],v[104:107],v[164:167]\ns_waitcnt vmcnt(18) lgkmcnt(0)\n" \
"v_exp_f32 v69,v69\nM v[132:135],a[196:199],v[96:99],v[132:135]\ns_barrier\nds_read_b128 a[192:195],v13 offset:49152\nM v[136:139],a[200:203],v[96:99],v[136:139]\nbuffer_load_dwordx4 v22,s[40:43],s45 offen offset:0 sc1 lds\nM v[168:171],a[200:203],v[104:107],v[168:171]\nv_exp_f32 v70,v70\n" \
"M v[200:203],a[200:203],v[112:115],v[200:203]\nv_exp_f32 v71,v71\nM v[232:235],a[200:203],v[120:123],v[232:235]\nv_exp_f32 v72,v72\nM v[236:239],a[204:207],v[120:123],v[236:239]\nbuffer_load_dwordx4 v22,s[40:43],s45 offen offset:1024 sc1 lds\nM v[204:207],a[204:207],v[112:115],v[204:207]\nds_read_b128 a[196:199],v13 offset:51200\n" \
"C v64,v64,v65\nM v[172:175],a[204:207],v[104:107],v[172:175]\nv_exp_f32 v73,v73\nM v[140:143],a[204:207],v[96:99],v[140:143]\nds_read_b128 a[200:203],v13 offset:53248\nds_read_b128 a[204:207],v13 offset:55296\nM v[144:147],a[208:211],v[96:99],v[144:147]\nbuffer_load_dwordx4 v22,s[40:43],s45 offen offset:2048 sc1 lds\n" \
"M v[176:179],a[208:211],v[104:107],v[176:179]\nv_exp_f32 v74,v74\nM v[208:211],a[208:211],v[112:115],v[208:211]\nv_exp_f32 v75,v75\nM v[240:243],a[208:211],v[120:123],v[240:243]\nv_exp_f32 v76,v76\nM v[244:247],a[212:215],v[120:123],v[244:247]\nbuffer_load_dwordx4 v22,s[40:43],s45 offen offset:3072 sc1 lds\n" \
"M v[212:215],a[212:215],v[112:115],v[212:215]\ns_add_u32 m0,s53,16384\nv_exp_f32 v77,v77\nM v[180:183],a[212:215],v[104:107],v[180:183]\ns_add_u32 s52,s52,1\nv_exp_f32 v78,v78\nM v[148:151],a[212:215],v[96:99],v[148:151]\ns_add_u32 s45,s45,s49\n" \
"v_exp_f32 v79,v79\nM v[152:155],a[216:219],v[96:99],v[152:155]\nbuffer_load_dwordx4 v22,s[40:43],s48 offen offset:0 sc1 lds\nM v[184:187],a[216:219],v[104:107],v[184:187]\nv_exp_f32 v80,v80\nM v[216:219],a[216:219],v[112:115],v[216:219]\nC v65,v66,v67\nC v66,v68,v69\n" \
"M v[248:251],a[216:219],v[120:123],v[248:251]\nv_exp_f32 v81,v81\nM v[252:255],a[220:223],v[120:123],v[252:255]\nbuffer_load_dwordx4 v22,s[40:43],s48 offen offset:1024 sc1 lds\nM v[220:223],a[220:223],v[112:115],v[220:223]\ns_waitcnt lgkmcnt(0)\nC v67,v70,v71\nC v72,v72,v73\n" \
"M v[188:191],a[220:223],v[104:107],v[188:191]\ns_cmp_eq_u32 s52,s56\nv_exp_f32 v82,v82\nM v[156:159],a[220:223],v[96:99],v[156:159]\ns_cbranch_scc1 LEV_%=\nLEVR1_%=:\nds_read_b128 a[208:211],v13 offset:50176\nds_read_b128 a[212:215],v13 offset:52224\n" \
"M v[96:99],a[192:195],a[128:131],v[32:35]\nbuffer_load_dwordx4 v22,s[40:43],s48 offen offset:2048 sc1 lds\nM v[96:99],a[196:199],a[132:135],v[96:99]\nv_exp_f32 v83,v83\nM v[16:19],a[224:227],v[64:67],v[24:27]\nds_read_b128 a[216:219],v13 offset:54272\nC v73,v74,v75\nM v[96:99],a[200:203],a[136:139],v[96:99]\n" \
"ds_read_b128 a[220:223],v13 offset:56320\nC v74,v76,v77\nM v[96:99],a[204:207],a[140:143],v[96:99]\nbuffer_load_dwordx4 v22,s[40:43],s48 offen offset:3072 sc1 lds\nM v[104:107],a[192:195],a[144:147],v[36:39]\ns_add_u32 m0,s53,49152\nC v75,v78,v79\nC v80,v80,v81\n" \
"M v[104:107],a[196:199],a[148:151],v[104:107]\ns_xor_b32 s53,s53,0x10000\nv_exp_f32 v84,v84\nM v[16:19],a[228:231],v[72:75],v[16:19]\ns_add_u32 s48,s48,s49\nv_exp_f32 v85,v85\nM v[104:107],a[200:203],a[152:155],v[104:107]\nv_exp_f32 v86,v86\n" \
"M v[104:107],a[204:207],a[156:159],v[104:107]\nv_exp_f32 v87,v87\nM v[112:115],a[192:195],a[160:163],v[40:43]\nv_exp_f32 v88,v88\nM v[112:115],a[196:199],a[164:167],v[112:115]\nC v81,v82,v83\nC v82,v84,v85\nM v[112:115],a[200:203],a[168:171],v[112:115]\n" \
"v_exp_f32 v89,v89\nM v[112:115],a[204:207],a[172:175],v[112:115]\nC v83,v86,v87\nC v88,v88,v89\nM v[120:123],a[192:195],a[176:179],v[44:47]\nv_exp_f32 v90,v90\nM v[120:123],a[196:199],a[180:183],v[120:123]\nv_exp_f32 v91,v91\n" \
"M v[120:123],a[200:203],a[184:187],v[120:123]\nv_exp_f32 v92,v92\nM v[120:123],a[204:207],a[188:191],v[120:123]\nv_exp_f32 v93,v93\nM v[16:19],a[232:235],v[80:83],v[16:19]\ns_waitcnt lgkmcnt(0)\nv_exp_f32 v94,v94\nM v[100:103],a[208:211],a[128:131],v[32:35]\n" \
"C v89,v90,v91\nC v90,v92,v93\nM v[100:103],a[212:215],a[132:135],v[100:103]\nv_exp_f32 v95,v95\nM v[100:103],a[216:219],a[136:139],v[100:103]\nv_xor_b32 v14,0x10000,v14\nC v91,v94,v95\nM v[100:103],a[220:223],a[140:143],v[100:103]\n" \
"ds_read_b128 a[192:195],v14 offset:0\nds_read_b128 a[196:199],v14 offset:1024\nM v[108:111],a[208:211],a[144:147],v[36:39]\nds_read_b128 a[200:203],v14 offset:2048\nM v[108:111],a[212:215],a[148:151],v[108:111]\nds_read_b128 a[204:207],v14 offset:3072\nM v[108:111],a[216:219],a[152:155],v[108:111]\nM v[108:111],a[220:223],a[156:159],v[108:111]\n" \
"M v[16:19],a[236:239],v[88:91],v[16:19]\nM v[116:119],a[208:211],a[160:163],v[40:43]\nM v[116:119],a[212:215],a[164:167],v[116:119]\nv_max3_f32 v15,v16,v17,v18\nv_max_f32 v15,v15,v19\nM v[116:119],a[216:219],a[168:171],v[116:119]\nv_cmp_nge_f32 vcc,0x53800000,v15\nM v[116:119],a[220:223],a[172:175],v[116:119]\n" \
"M v[124:127],a[208:211],a[176:179],v[44:47]\ns_waitcnt lgkmcnt(0)\nds_read_b128 a[208:211],v14 offset:4096\nM v[124:127],a[212:215],a[180:183],v[124:127]\ns_cbranch_vccnz Lcs11_%=\nLcr12_%=:\nds_read_b128 a[212:215],v14 offset:5120\nM v[124:127],a[216:219],a[184:187],v[124:127]\n" \
"s_cmp_eq_u32 s52,s55\nds_read_b128 a[216:219],v14 offset:6144\nM v[124:127],a[220:223],a[188:191],v[124:127]\ns_cbranch_scc1 LH1_%=\nLH1R2_%=:\nds_read_b128 a[220:223],v14 offset:7168\nv_xor_b32 v13,0x10000,v13\nM v[128:131],a[192:195],v[64:67],v[128:131]\n" \
"v_exp_f32 v96,v96\nM v[160:163],a[192:195],v[72:75],v[160:163]\nv_exp_f32 v97,v97\nM v[192:195],a[192:195],v[80:83],v[192:195]\nv_exp_f32 v98,v98\nM v[224:227],a[192:195],v[88:91],v[224:227]\nv_exp_f32 v99,v99\nM v[228:231],a[196:199],v[88:91],v[228:231]\n" \
"v_exp_f32 v100,v100\nM v[196:199],a[196:199],v[80:83],v[196:199]\nv_exp_f32 v101,v101\nM v[164:167],a[196:199],v[72:75],v[164:167]\ns_waitcnt vmcnt(18) lgkmcnt(0)\nv_exp_f32 v102,v102\nM v[132:135],a[196:199],v[64:67],v[132:135]\ns_barrier\n" \
"ds_read_b128 a[192:195],v13 offset:32768\nM v[136:139],a[200:203],v[64:67],v[136:139]\nbuffer_load_dwordx4 v22,s[40:43],s45 offen offset:0 sc1 lds\nM v[168:171],a[200:203],v[72:75],v[168:171]\nv_exp_f32 v103,v103\nM v[200:203],a[200:203],v[80:83],v[200:203]\nv_exp_f32 v104,v104\nM v[232:235],a[200:203],v[88:91],v[232:235]\n" \
"v_exp_f32 v105,v105\nM v[236:239],a[204:207],v[88:91],v[236:239]\nbuffer_load_dwordx4 v22,s[40:43],s45 offen offset:1024 sc1 lds\nM v[204:207],a[204:207],v[80:83],v[204:207]\nds_read_b128 a[196:199],v13 offset:34816\nC v96,v96,v97\nM v[172:175],a[204:207],v[72:75],v[172:175]\nv_exp_f32 v106,v106\n" \
"M v[140:143],a[204:207],v[64:67],v[140:143]\nds_read_b128 a[200:203],v13 offset:36864\nds_read_b128 a[204:207],v13 offset:38912\nM v[144:147],a[208:211],v[64:67],v[144:147]\nbuffer_load_dwordx4 v22,s[40:43],s45 offen offset:2048 sc1 lds\nM v[176:179],a[208:211],v[72:75],v[176:179]\nv_exp_f32 v107,v107\nM v[208:211],a[208:211],v[80:83],v[208:211]\n" \
"v_exp_f32 v108,v108\nM v[240:243],a[208:211],v[88:91],v[240:243]\nv_exp_f32 v109,v109\nM v[244:247],a[212:215],v[88:91],v[244:247]\nbuffer_load_dwordx4 v22,s[40:43],s45 offen offset:3072 sc1 lds\nM v[212:215],a[212:215],v[80:83],v[212:215]\ns_add_u32 m0,s53,0\nv_exp_f32 v110,v110\n" \
"M v[180:183],a[212:215],v[72:75],v[180:183]\ns_add_u32 s45,s45,s49\nv_exp_f32 v111,v111\nM v[148:151],a[212:215],v[64:67],v[148:151]\nv_exp_f32 v112,v112\nM v[152:155],a[216:219],v[64:67],v[152:155]\nbuffer_load_dwordx4 v22,s[40:43],s48 offen offset:0 sc1 lds\nM v[184:187],a[216:219],v[72:75],v[184:187]\n" \
"C v97,v98,v99\nC v98,v100,v101\nM v[216:219],a[216:219],v[80:83],v[216:219]\nv_exp_f32 v113,v113\nM v[248:251],a[216:219],v[88:91],v[248:251]\nC v99,v102,v103\nC v104,v104,v105\nM v[252:255],a[220:223],v[88:91],v[252:255]\n" \
"buffer_load_dwordx4 v22,s[40:43],s48 offen offset:1024 sc1 lds\nM v[220:223],a[220:223],v[80:83],v[220:223]\nv_exp_f32 v114,v114\nM v[188:191],a[220:223],v[72:75],v[188:191]\nv_exp_f32 v115,v115\nM v[156:159],a[220:223],v[64:67],v[156:159]\ns_waitcnt lgkmcnt(0)\nds_read_b128 a[208:211],v13 offset:33792\n" \
"ds_read_b128 a[212:215],v13 offset:35840\nM v[64:67],a[192:195],a[128:131],v[32:35]\nbuffer_load_dwordx4 v22,s[40:43],s48 offen offset:2048 sc1 lds\nM v[64:67],a[196:199],a[132:135],v[64:67]\nC v105,v106,v107\nC v106,v108,v109\nM v[24:27],a[224:227],v[96:99],v[16:19]\nds_read_b128 a[216:219],v13 offset:37888\n" \
"C v107,v110,v111\nM v[64:67],a[200:203],a[136:139],v[64:67]\nds_read_b128 a[220:223],v13 offset:39936\nC v112,v112,v113\nM v[64:67],a[204:207],a[140:143],v[64:67]\nbuffer_load_dwordx4 v22,s[40:43],s48 offen offset:3072 sc1 lds\nM v[72:75],a[192:195],a[144:147],v[36:39]\ns_add_u32 m0,s53,32768\n" \
"v_exp_f32 v116,v116\nM v[72:75],a[196:199],a[148:151],v[72:75]\ns_add_u32 s48,s48,s49\nv_exp_f32 v117,v117\nM v[24:27],a[228:231],v[104:107],v[24:27]\nv_exp_f32 v118,v118\nM v[72:75],a[200:203],a[152:155],v[72:75]\nv_exp_f32 v119,v119\n" \
"M v[72:75],a[204:207],a[156:159],v[72:75]\nv_exp_f32 v120,v120\nM v[80:83],a[192:195],a[160:163],v[40:43]\nC v113,v114,v115\nC v114,v116,v117\nM v[80:83],a[196:199],a[164:167],v[80:83]\nv_exp_f32 v121,v121\nM v[80:83],a[200:203],a[168:171],v[80:83]\n" \
"C v115,v118,v119\nC v120,v120,v121\nM v[80:83],a[204:207],a[172:175],v[80:83]\nv_exp_f32 v122,v122\nM v[88:91],a[192:195],a[176:179],v[44:47]\nv_exp_f32 v123,v123\nM v[88:91],a[196:199],a[180:183],v[88:91]\nv_exp_f32 v124,v124\n" \
"M v[88:91],a[200:203],a[184:187],v[88:91]\nv_exp_f32 v125,v125\nM v[88:91],a[204:207],a[188:191],v[88:91]\nds_read_b128 a[192:195],v14 offset:16384\nC v121,v122,v123\nM v[24:27],a[232:235],v[112:115],v[24:27]\ns_waitcnt lgkmcnt(1)\nv_exp_f32 v126,v126\n" \
"M v[68:71],a[208:211],a[128:131],v[32:35]\nds_read_b128 a[196:199],v14 offset:17408\nC v122,v124,v125\nM v[68:71],a[212:215],a[132:135],v[68:71]\nv_exp_f32 v127,v127\nM v[68:71],a[216:219],a[136:139],v[68:71]\nds_read_b128 a[200:203],v14 offset:18432\nC v123,v126,v127\n" \
"M v[68:71],a[220:223],a[140:143],v[68:71]\nds_read_b128 a[204:207],v14 offset:19456\nM v[76:79],a[208:211],a[144:147],v[36:39]\nM v[76:79],a[212:215],a[148:151],v[76:79]\nM v[76:79],a[216:219],a[152:155],v[76:79]\nM v[76:79],a[220:223],a[156:159],v[76:79]\nM v[24:27],a[236:239],v[120:123],v[24:27]\nM v[84:87],a[208:211],a[160:163],v[40:43]\n" \
"s_mov_b64 exec,0xff\nglobal_load_dword v0,v2,s[100:101]\ns_mov_b64 exec,-1\nM v[84:87],a[212:215],a[164:167],v[84:87]\nv_max3_f32 v15,v24,v25,v26\nv_max_f32 v15,v15,v27\nM v[84:87],a[216:219],a[168:171],v[84:87]\nv_cmp_nge_f32 vcc,0x53800000,v15\n" \
"M v[84:87],a[220:223],a[172:175],v[84:87]\nM v[92:95],a[208:211],a[176:179],v[44:47]\nds_read_b128 a[208:211],v14 offset:20480\nM v[92:95],a[212:215],a[180:183],v[92:95]\ns_cbranch_vccnz Lcs13_%=\nLcr14_%=:\nds_read_b128 a[212:215],v14 offset:21504\nM v[92:95],a[216:219],a[184:187],v[92:95]\n" \
"ds_read_b128 a[216:219],v14 offset:22528\nM v[92:95],a[220:223],a[188:191],v[92:95]\ns_waitcnt lgkmcnt(3)\nds_read_b128 a[220:223],v14 offset:23552\nM v[128:131],a[192:195],v[96:99],v[128:131]\nv_exp_f32 v64,v64\nM v[160:163],a[192:195],v[104:107],v[160:163]\nv_exp_f32 v65,v65\n" \
"M v[192:195],a[192:195],v[112:115],v[192:195]\nbuffer_load_dword v58,v57,s[84:87],s74 offen\nM v[224:227],a[192:195],v[120:123],v[224:227]\ns_add_u32 s74,s74,2048\nv_exp_f32 v66,v66\nM v[228:231],a[196:199],v[120:123],v[228:231]\nv_exp_f32 v67,v67\nM v[196:199],a[196:199],v[112:115],v[196:199]\n" \
"v_exp_f32 v68,v68\nM v[164:167],a[196:199],v[104:107],v[164:167]\ns_waitcnt vmcnt(20) lgkmcnt(0)\nv_exp_f32 v69,v69\nM v[132:135],a[196:199],v[96:99],v[132:135]\ns_barrier\nds_read_b128 a[192:195],v13 offset:49152\nM v[136:139],a[200:203],v[96:99],v[136:139]\n" \
"buffer_load_dwordx4 v22,s[40:43],s45 offen offset:0 sc1 lds\nM v[168:171],a[200:203],v[104:107],v[168:171]\nv_exp_f32 v70,v70\nM v[200:203],a[200:203],v[112:115],v[200:203]\nv_exp_f32 v71,v71\nM v[232:235],a[200:203],v[120:123],v[232:235]\nv_exp_f32 v72,v72\nM v[236:239],a[204:207],v[120:123],v[236:239]\n" \
"buffer_load_dwordx4 v22,s[40:43],s45 offen offset:1024 sc1 lds\nM v[204:207],a[204:207],v[112:115],v[204:207]\nds_read_b128 a[196:199],v13 offset:51200\nC v64,v64,v65\nM v[172:175],a[204:207],v[104:107],v[172:175]\nv_exp_f32 v73,v73\nM v[140:143],a[204:207],v[96:99],v[140:143]\nds_read_b128 a[200:203],v13 offset:53248\n" \
"ds_read_b128 a[204:207],v13 offset:55296\nM v[144:147],a[208:211],v[96:99],v[144:147]\nbuffer_load_dwordx4 v22,s[40:43],s45 offen offset:2048 sc1 lds\nM v[176:179],a[208:211],v[104:107],v[176:179]\nv_exp_f32 v74,v74\nM v[208:211],a[208:211],v[112:115],v[208:211]\nv_exp_f32 v75,v75\nM v[240:243],a[208:211],v[120:123],v[240:243]\n" \
"v_exp_f32 v76,v76\nM v[244:247],a[212:215],v[120:123],v[244:247]\nbuffer_load_dwordx4 v22,s[40:43],s45 offen offset:3072 sc1 lds\nM v[212:215],a[212:215],v[112:115],v[212:215]\ns_add_u32 m0,s53,16384\nv_exp_f32 v77,v77\nM v[180:183],a[212:215],v[104:107],v[180:183]\ns_add_u32 s52,s52,1\n" \
"v_exp_f32 v78,v78\nM v[148:151],a[212:215],v[96:99],v[148:151]\ns_add_u32 s45,s45,s49\nv_exp_f32 v79,v79\nM v[152:155],a[216:219],v[96:99],v[152:155]\nbuffer_load_dwordx4 v22,s[40:43],s48 offen offset:0 sc1 lds\nM v[184:187],a[216:219],v[104:107],v[184:187]\nv_exp_f32 v80,v80\n" \
"M v[216:219],a[216:219],v[112:115],v[216:219]\nC v65,v66,v67\nC v66,v68,v69\nM v[248:251],a[216:219],v[120:123],v[248:251]\nv_exp_f32 v81,v81\nM v[252:255],a[220:223],v[120:123],v[252:255]\nbuffer_load_dwordx4 v22,s[40:43],s48 offen offset:1024 sc1 lds\nM v[220:223],a[220:223],v[112:115],v[220:223]\n" \
"s_waitcnt lgkmcnt(0)\nC v67,v70,v71\nC v72,v72,v73\nM v[188:191],a[220:223],v[104:107],v[188:191]\ns_cmp_eq_u32 s52,s56\nv_exp_f32 v82,v82\nM v[156:159],a[220:223],v[96:99],v[156:159]\ns_cbranch_scc0 LEVR1_%=\n" \
"s_branch LEV_%=\nLEVR8_%=:\nds_read_b128 a[208:211],v13 offset:50176\nds_read_b128 a[212:215],v13 offset:52224\nM v[96:99],a[192:195],a[128:131],v[32:35]\nbuffer_load_dwordx4 v22,s[40:43],s48 offen offset:2048 sc1 lds\nM v[96:99],a[196:199],a[132:135],v[96:99]\ns_lshl_b32 s58,%2,2\n" \
"v_exp_f32 v83,v83\nM v[16:19],a[224:227],v[64:67],v[24:27]\ns_lshl_b32 s59,%2,1\nds_read_b128 a[216:219],v13 offset:54272\nC v73,v74,v75\nM v[96:99],a[200:203],a[136:139],v[96:99]\nds_read_b128 a[220:223],v13 offset:56320\nC v74,v76,v77\n" \
"M v[96:99],a[204:207],a[140:143],v[96:99]\nbuffer_load_dwordx4 v22,s[40:43],s48 offen offset:3072 sc1 lds\nM v[104:107],a[192:195],a[144:147],v[36:39]\ns_add_u32 m0,s22,49152\nC v75,v78,v79\nC v80,v80,v81\nM v[104:107],a[196:199],a[148:151],v[104:107]\ns_add_u32 s48,s48,s49\n" \
"v_exp_f32 v84,v84\nM v[16:19],a[228:231],v[72:75],v[16:19]\nv_exp_f32 v85,v85\nM v[104:107],a[200:203],a[152:155],v[104:107]\nv_exp_f32 v86,v86\nM v[104:107],a[204:207],a[156:159],v[104:107]\nv_exp_f32 v87,v87\nM v[112:115],a[192:195],a[160:163],v[40:43]\n" \
"v_exp_f32 v88,v88\nM v[112:115],a[196:199],a[164:167],v[112:115]\nC v81,v82,v83\nC v82,v84,v85\nM v[112:115],a[200:203],a[168:171],v[112:115]\nv_exp_f32 v89,v89\nM v[112:115],a[204:207],a[172:175],v[112:115]\nC v83,v86,v87\n" \
"C v88,v88,v89\nM v[120:123],a[192:195],a[176:179],v[44:47]\nv_exp_f32 v90,v90\nM v[120:123],a[196:199],a[180:183],v[120:123]\nv_exp_f32 v91,v91\nM v[120:123],a[200:203],a[184:187],v[120:123]\nv_exp_f32 v92,v92\nM v[120:123],a[204:207],a[188:191],v[120:123]\n" \
"v_exp_f32 v93,v93\nM v[16:19],a[232:235],v[80:83],v[16:19]\ns_waitcnt lgkmcnt(0)\nv_exp_f32 v94,v94\nM v[100:103],a[208:211],a[128:131],v[32:35]\nC v89,v90,v91\nv_xor_b32 v14,0x10000,v14\nM v[100:103],a[212:215],a[132:135],v[100:103]\n" \
"ds_read_b128 a[192:195],v14 offset:0\nds_read_b128 a[196:199],v14 offset:1024\nM v[100:103],a[216:219],a[136:139],v[100:103]\nv_exp_f32 v95,v95\nM v[100:103],a[220:223],a[140:143],v[100:103]\nC v90,v92,v93\nC v91,v94,v95\nM v[108:111],a[208:211],a[144:147],v[36:39]\n" \
"ds_read_b128 a[200:203],v14 offset:2048\nv_bfe_u32 v52,v4,6,2\nM v[108:111],a[212:215],a[148:151],v[108:111]\nv_add_u32 v52,s58,v52\nv_lshrrev_b32 v5,1,v4\nM v[108:111],a[216:219],a[152:155],v[108:111]\ns_lshl_b32 s58,%2,9\nds_read_b128 a[204:207],v14 offset:3072\n" \
"v_xor_b32 v13,0x10000,v13\nM v[108:111],a[220:223],a[156:159],v[108:111]\ns_add_u32 s58,s58,94375936\nv_add_u32 v5,s58,v5\nv_bfe_u32 v53,v4,8,1\nM v[16:19],a[236:239],v[88:91],v[16:19]\ns_lshl_b32 s58,%2,9\nv_lshl_add_u32 v52,v53,4,v52\n" \
"v_add_u32 v53,s59,v53\nM v[116:119],a[208:211],a[160:163],v[40:43]\nv_lshrrev_b32 v54,4,v4\nv_xor_b32 v53,v53,v54\nM v[116:119],a[212:215],a[164:167],v[116:119]\nbuffer_load_dwordx2 v[10:11],v5,s[36:39],0 offen sc1\nM v[116:119],a[216:219],a[168:171],v[116:119]\nv_max3_f32 v15,v16,v17,v18\n" \
"v_max_f32 v15,v15,v19\nM v[116:119],a[220:223],a[172:175],v[116:119]\nv_cmp_nge_f32 vcc,0x53800000,v15\nv_lshlrev_b32 v52,11,v52\nM v[124:127],a[208:211],a[176:179],v[44:47]\nds_read_b128 a[208:211],v14 offset:4096\nv_and_b32 v53,3,v53\nM v[124:127],a[212:215],a[180:183],v[124:127]\n" \
"s_cbranch_vccnz Lcs39_%=\nLcr40_%=:\nds_read_b128 a[212:215],v14 offset:5120\nv_lshrrev_b32 v54,9,v4\nM v[124:127],a[216:219],a[184:187],v[124:127]\nds_read_b128 a[216:219],v14 offset:6144\nv_lshl_add_u32 v53,v54,2,v53\nM v[124:127],a[220:223],a[188:191],v[124:127]\n" \
"s_waitcnt lgkmcnt(3)\nds_read_b128 a[220:223],v14 offset:7168\nv_lshl_add_u32 v1,v53,4,v52\nM v[128:131],a[192:195],v[64:67],v[128:131]\nv_exp_f32 v96,v96\nM v[160:163],a[192:195],v[72:75],v[160:163]\nv_exp_f32 v97,v97\nM v[192:195],a[192:195],v[80:83],v[192:195]\n" \
"v_exp_f32 v98,v98\nM v[224:227],a[192:195],v[88:91],v[224:227]\nv_exp_f32 v99,v99\nM v[228:231],a[196:199],v[88:91],v[228:231]\nv_exp_f32 v100,v100\nM v[196:199],a[196:199],v[80:83],v[196:199]\nv_exp_f32 v101,v101\nM v[164:167],a[196:199],v[72:75],v[164:167]\n" \
"s_waitcnt vmcnt(19) lgkmcnt(0)\nv_exp_f32 v102,v102\nM v[132:135],a[196:199],v[64:67],v[132:135]\ns_barrier\nds_read_b128 a[192:195],v13 offset:32768\nM v[136:139],a[200:203],v[64:67],v[136:139]\nbuffer_load_dwordx4 v1,s[84:87],s46 offen offset:0 lds\nM v[168:171],a[200:203],v[72:75],v[168:171]\n" \
"s_add_u32 m0,s22,50048\nv_exp_f32 v103,v103\nM v[200:203],a[200:203],v[80:83],v[200:203]\nv_exp_f32 v104,v104\nM v[232:235],a[200:203],v[88:91],v[232:235]\nv_exp_f32 v105,v105\nM v[236:239],a[204:207],v[88:91],v[236:239]\nbuffer_load_dwordx4 v1,s[84:87],s46 offen offset:128 lds\n" \
"M v[204:207],a[204:207],v[80:83],v[204:207]\ns_add_u32 m0,s22,51200\nv_exp_f32 v106,v106\nM v[172:175],a[204:207],v[72:75],v[172:175]\ns_mov_b32 s46,65536\nds_read_b128 a[196:199],v13 offset:34816\nds_read_b128 a[200:203],v13 offset:36864\nM v[140:143],a[204:207],v[64:67],v[140:143]\n" \
"v_exp_f32 v107,v107\nM v[144:147],a[208:211],v[64:67],v[144:147]\nbuffer_load_dwordx4 v1,s[84:87],s46 offen offset:0 lds\nM v[176:179],a[208:211],v[72:75],v[176:179]\ns_add_u32 m0,s22,52096\nds_read_b128 a[204:207],v13 offset:38912\nC v96,v96,v97\nM v[208:211],a[208:211],v[80:83],v[208:211]\n" \
"v_exp_f32 v108,v108\nM v[240:243],a[208:211],v[88:91],v[240:243]\nv_exp_f32 v109,v109\nM v[244:247],a[212:215],v[88:91],v[244:247]\nbuffer_load_dwordx4 v1,s[84:87],s46 offen offset:128 lds\nM v[212:215],a[212:215],v[80:83],v[212:215]\ns_add_u32 m0,s22,65536\nv_exp_f32 v110,v110\n" \
"M v[180:183],a[212:215],v[72:75],v[180:183]\ns_mov_b32 s46,131072\nv_exp_f32 v111,v111\nM v[148:151],a[212:215],v[64:67],v[148:151]\ns_and_b32 s61,%1,1\nv_exp_f32 v112,v112\nM v[152:155],a[216:219],v[64:67],v[152:155]\nbuffer_load_dwordx4 v1,s[84:87],s46 offen offset:0 lds\n" \
"M v[184:187],a[216:219],v[72:75],v[184:187]\ns_add_u32 m0,s22,66432\nv_exp_f32 v113,v113\nM v[216:219],a[216:219],v[80:83],v[216:219]\ns_lshl_b32 s61,s61,2\nv_exp_f32 v114,v114\nM v[248:251],a[216:219],v[88:91],v[248:251]\ns_bfe_u32 s60,%1,0x20001\n" \
"C v97,v98,v99\nC v98,v100,v101\nM v[252:255],a[220:223],v[88:91],v[252:255]\nbuffer_load_dwordx4 v1,s[84:87],s46 offen offset:128 lds\nM v[220:223],a[220:223],v[80:83],v[220:223]\ns_add_u32 m0,s22,67584\nv_exp_f32 v115,v115\nM v[188:191],a[220:223],v[72:75],v[188:191]\n" \
"s_mov_b32 s46,196608\nC v99,v102,v103\nC v104,v104,v105\nM v[156:159],a[220:223],v[64:67],v[156:159]\ns_cmp_eq_u32 s60,0\ns_cselect_b64 s[96:97],-1,0\ns_waitcnt lgkmcnt(0)\nv_exp_f32 v116,v116\n" \
"M v[64:67],a[192:195],a[128:131],v[32:35]\nbuffer_load_dwordx4 v1,s[84:87],s46 offen offset:0 lds\nM v[64:67],a[196:199],a[132:135],v[64:67]\ns_waitcnt vmcnt(7)\nv_cmp_gt_u64 s[98:99],s[14:15],v[10:11]\nv_cmp_le_u64 s[18:19],s[16:17],v[10:11]\nM v[24:27],a[224:227],v[96:99],v[16:19]\ns_or_b64 s[98:99],s[98:99],s[18:19]\n" \
"s_cbranch_scc1 LPZ_%=\nLPZR_%=:\nds_read_b128 a[208:211],v13 offset:33792\nds_read_b128 a[212:215],v13 offset:35840\nM v[64:67],a[200:203],a[136:139],v[64:67]\nLG17_%=:\ns_cbranch_execz LHR2_%=\ns_add_u32 m0,s22,68480\n" \
"ds_read_b128 a[216:219],v13 offset:37888\nC v105,v106,v107\nM v[64:67],a[204:207],a[140:143],v[64:67]\nbuffer_load_dwordx4 v1,s[84:87],s46 offen offset:128 lds\nM v[72:75],a[192:195],a[144:147],v[36:39]\ns_add_u32 m0,s22,98304\nC v106,v108,v109\nC v107,v110,v111\n" \
"v_exp_f32 v117,v117\nM v[72:75],a[196:199],a[148:151],v[72:75]\ns_mov_b32 s46,262144\nds_read_b128 a[220:223],v13 offset:39936\nC v112,v112,v113\nM v[24:27],a[228:231],v[104:107],v[24:27]\ns_add_u32 s14,s14,3000\ns_addc_u32 s15,s15,0\n" \
"v_exp_f32 v118,v118\nM v[72:75],a[200:203],a[152:155],v[72:75]\ns_sub_u32 s73,s72,2048\nv_exp_f32 v119,v119\nM v[72:75],a[204:207],a[156:159],v[72:75]\ns_lshl_b32 s95,%1,2\nv_exp_f32 v120,v120\nM v[80:83],a[192:195],a[160:163],v[40:43]\n" \
"s_add_u32 s95,s95,%2\nv_exp_f32 v121,v121\nM v[80:83],a[196:199],a[164:167],v[80:83]\ns_lshl_b32 s95,s95,5\nC v113,v114,v115\nC v114,v116,v117\nM v[80:83],a[200:203],a[168:171],v[80:83]\ns_add_u32 s95,s95,94437376\n" \
"v_exp_f32 v122,v122\nM v[80:83],a[204:207],a[172:175],v[80:83]\nC v115,v118,v119\nC v120,v120,v121\nM v[88:91],a[192:195],a[176:179],v[44:47]\nv_exp_f32 v123,v123\nM v[88:91],a[196:199],a[180:183],v[88:91]\nv_exp_f32 v124,v124\n" \
"M v[88:91],a[200:203],a[184:187],v[88:91]\nv_exp_f32 v125,v125\nM v[88:91],a[204:207],a[188:191],v[88:91]\nds_read_b128 a[192:195],v14 offset:16384\nC v121,v122,v123\nM v[24:27],a[232:235],v[112:115],v[24:27]\ns_waitcnt lgkmcnt(1)\nv_exp_f32 v126,v126\n" \
"M v[68:71],a[208:211],a[128:131],v[32:35]\nds_read_b128 a[196:199],v14 offset:17408\nC v122,v124,v125\nM v[68:71],a[212:215],a[132:135],v[68:71]\nv_exp_f32 v127,v127\nM v[68:71],a[216:219],a[136:139],v[68:71]\nds_read_b128 a[200:203],v14 offset:18432\nC v123,v126,v127\n" \
"M v[68:71],a[220:223],a[140:143],v[68:71]\nds_read_b128 a[204:207],v14 offset:19456\nv_and_b32 v52,0xc0,v4\nM v[76:79],a[208:211],a[144:147],v[36:39]\nv_bfe_u32 v53,v4,4,1\nv_lshl_add_u32 v52,v53,3,v52\nM v[76:79],a[212:215],a[148:151],v[76:79]\nv_lshrrev_b32 v54,7,v4\n" \
"v_bfe_u32 v53,v4,5,1\nM v[76:79],a[216:219],a[152:155],v[76:79]\nv_and_b32 v54,2,v54\nv_xor_b32 v53,v53,v54\nM v[76:79],a[220:223],a[156:159],v[76:79]\nv_lshl_add_u32 v52,v53,4,v52\nv_lshrrev_b32 v53,8,v4\nM v[24:27],a[236:239],v[120:123],v[24:27]\n" \
"v_lshl_add_u32 v52,v53,12,v52\nv_add_u32 v6,s58,v52\nM v[84:87],a[208:211],a[160:163],v[40:43]\ns_bfe_u32 s58,%1,0x20003\nv_xor_b32 v7,0x110,v6\nv_xor_b32 v8,32,v6\nM v[84:87],a[212:215],a[164:167],v[84:87]\ns_add_u32 s61,s61,s58\n" \
"v_max3_f32 v15,v24,v25,v26\nv_max_f32 v15,v15,v27\nM v[84:87],a[216:219],a[168:171],v[84:87]\ns_and_b32 s58,s61,6\nv_cmp_nge_f32 vcc,0x53800000,v15\nv_xor_b32 v9,0x130,v6\nM v[84:87],a[220:223],a[172:175],v[84:87]\ns_add_u32 s58,s58,s61\n" \
"v_bfe_u32 v52,v4,7,2\nv_mov_b32 v48,-1\nM v[92:95],a[208:211],a[176:179],v[44:47]\ns_lshl_b32 s58,s58,2\nds_read_b128 a[208:211],v14 offset:20480\nv_mov_b32 v49,-1\nM v[92:95],a[212:215],a[180:183],v[92:95]\ns_cbranch_vccnz Lcs41_%=\n" \
"Lcr42_%=:\nds_read_b128 a[212:215],v14 offset:21504\nv_mov_b32 v50,-1\nM v[92:95],a[216:219],a[184:187],v[92:95]\ns_add_u32 s58,s58,s60\nds_read_b128 a[216:219],v14 offset:22528\nv_mov_b32 v51,-1\nM v[92:95],a[220:223],a[188:191],v[92:95]\n" \
"s_waitcnt lgkmcnt(3)\nbuffer_store_dwordx4 v[48:51],v4,s[36:39],s72 offen offset:0 sc1\nM v[128:131],a[192:195],v[96:99],v[128:131]\ns_lshl_b32 s58,s58,15\nds_read_b128 a[220:223],v14 offset:23552\nv_mov_b32 v53,s11\nM v[160:163],a[192:195],v[104:107],v[160:163]\ns_add_u32 s28,s58,50331648\n" \
"v_exp_f32 v64,v64\nM v[192:195],a[192:195],v[112:115],v[192:195]\ns_add_u32 s29,s58,52428800\nv_exp_f32 v65,v65\nM v[224:227],a[192:195],v[120:123],v[224:227]\ns_add_u32 s30,s58,54525952\nv_exp_f32 v66,v66\nM v[228:231],a[196:199],v[120:123],v[228:231]\n" \
"s_add_u32 s31,s58,56623104\nv_exp_f32 v67,v67\nM v[196:199],a[196:199],v[112:115],v[196:199]\ns_lshr_b32 s58,%2,1\nv_exp_f32 v68,v68\nM v[164:167],a[196:199],v[104:107],v[164:167]\ns_waitcnt vmcnt(20) lgkmcnt(0)\nv_exp_f32 v69,v69\n" \
"M v[132:135],a[196:199],v[96:99],v[132:135]\ns_lshl_b32 s58,s58,18\ns_barrier\nds_read_b128 a[192:195],v13 offset:49152\nM v[136:139],a[200:203],v[96:99],v[136:139]\nbuffer_store_dwordx4 v[48:51],v4,s[36:39],s72 offen offset:1024 sc1\nM v[168:171],a[200:203],v[104:107],v[168:171]\ns_add_u32 s47,s28,0\n" \
"v_exp_f32 v70,v70\nM v[200:203],a[200:203],v[112:115],v[200:203]\nv_exp_f32 v71,v71\nM v[232:235],a[200:203],v[120:123],v[232:235]\nv_exp_f32 v72,v72\nM v[236:239],a[204:207],v[120:123],v[236:239]\nv_exp_f32 v73,v73\nM v[204:207],a[204:207],v[112:115],v[204:207]\n" \
"v_exp_f32 v74,v74\nM v[172:175],a[204:207],v[104:107],v[172:175]\nds_read_b128 a[196:199],v13 offset:51200\nds_read_b128 a[200:203],v13 offset:53248\nM v[140:143],a[204:207],v[96:99],v[140:143]\nv_exp_f32 v75,v75\nM v[144:147],a[208:211],v[96:99],v[144:147]\nds_read_b128 a[204:207],v13 offset:55296\n" \
"C v64,v64,v65\nM v[176:179],a[208:211],v[104:107],v[176:179]\nv_exp_f32 v76,v76\nM v[208:211],a[208:211],v[112:115],v[208:211]\nv_exp_f32 v77,v77\nM v[240:243],a[208:211],v[120:123],v[240:243]\nbuffer_load_dwordx4 v22,s[40:43],s45 offen offset:0 sc1 lds\nM v[244:247],a[212:215],v[120:123],v[244:247]\n" \
"v_exp_f32 v78,v78\nM v[212:215],a[212:215],v[112:115],v[212:215]\nv_exp_f32 v79,v79\nM v[180:183],a[212:215],v[104:107],v[180:183]\ns_add_u32 s52,s52,1\nv_exp_f32 v80,v80\nM v[148:151],a[212:215],v[96:99],v[148:151]\nv_exp_f32 v81,v81\n" \
"M v[152:155],a[216:219],v[96:99],v[152:155]\nbuffer_load_dwordx4 v22,s[40:43],s45 offen offset:1024 sc1 lds\nM v[184:187],a[216:219],v[104:107],v[184:187]\nC v65,v66,v67\nC v66,v68,v69\nM v[216:219],a[216:219],v[112:115],v[216:219]\nv_exp_f32 v82,v82\nM v[248:251],a[216:219],v[120:123],v[248:251]\n" \
"C v67,v70,v71\nC v72,v72,v73\nM v[252:255],a[220:223],v[120:123],v[252:255]\nbuffer_load_dwordx4 v22,s[40:43],s45 offen offset:2048 sc1 lds\nM v[220:223],a[220:223],v[112:115],v[220:223]\ns_waitcnt lgkmcnt(0)\nv_exp_f32 v83,v83\nM v[188:191],a[220:223],v[104:107],v[188:191]\n" \
"s_cmp_eq_u32 s52,s56\nv_exp_f32 v84,v84\nM v[156:159],a[220:223],v[96:99],v[156:159]\ns_cbranch_scc1 LEV_%=\nLEVR9_%=:\nC v73,v74,v75\nC v74,v76,v77\nM v[96:99],a[192:195],a[128:131],v[32:35]\n" \
"ds_read_b128 a[208:211],v13 offset:50176\nds_read_b128 a[212:215],v13 offset:52224\nM v[96:99],a[196:199],a[132:135],v[96:99]\nbuffer_load_dwordx4 v22,s[40:43],s45 offen offset:3072 sc1 lds\nM v[16:19],a[224:227],v[64:67],v[24:27]\ns_add_u32 m0,s22,81920\nv_exp_f32 v85,v85\nM v[96:99],a[200:203],a[136:139],v[96:99]\n" \
"s_add_u32 s45,s45,s49\nds_read_b128 a[216:219],v13 offset:54272\nC v75,v78,v79\nM v[96:99],a[204:207],a[140:143],v[96:99]\nds_read_b128 a[220:223],v13 offset:56320\nC v80,v80,v81\nM v[104:107],a[192:195],a[144:147],v[36:39]\nbuffer_load_dwordx4 v22,s[40:43],s48 offen offset:0 sc1 lds\n" \
"M v[104:107],a[196:199],a[148:151],v[104:107]\nv_exp_f32 v86,v86\nM v[16:19],a[228:231],v[72:75],v[16:19]\nv_exp_f32 v87,v87\nM v[104:107],a[200:203],a[152:155],v[104:107]\nv_exp_f32 v88,v88\nM v[104:107],a[204:207],a[156:159],v[104:107]\nC v81,v82,v83\n" \
"C v82,v84,v85\nM v[112:115],a[192:195],a[160:163],v[40:43]\nbuffer_store_dwordx4 v[48:51],v4,s[36:39],s72 offen offset:2048 sc1\nM v[112:115],a[196:199],a[164:167],v[112:115]\nv_exp_f32 v89,v89\nM v[112:115],a[200:203],a[168:171],v[112:115]\nC v83,v86,v87\nC v88,v88,v89\n" \
"M v[112:115],a[204:207],a[172:175],v[112:115]\nv_exp_f32 v90,v90\nM v[120:123],a[192:195],a[176:179],v[44:47]\nv_exp_f32 v91,v91\nM v[120:123],a[196:199],a[180:183],v[120:123]\nv_exp_f32 v92,v92\nM v[120:123],a[200:203],a[184:187],v[120:123]\nv_exp_f32 v93,v93\n" \
"M v[120:123],a[204:207],a[188:191],v[120:123]\nv_exp_f32 v94,v94\nM v[16:19],a[232:235],v[80:83],v[16:19]\ns_waitcnt lgkmcnt(0)\nC v89,v90,v91\nC v90,v92,v93\nM v[100:103],a[208:211],a[128:131],v[32:35]\nv_exp_f32 v95,v95\n" \
"M v[100:103],a[212:215],a[132:135],v[100:103]\nv_xor_b32 v14,0x10000,v14\nC v91,v94,v95\nM v[100:103],a[216:219],a[136:139],v[100:103]\nds_read_b128 a[192:195],v14 offset:0\nds_read_b128 a[196:199],v14 offset:1024\nM v[100:103],a[220:223],a[140:143],v[100:103]\nds_read_b128 a[200:203],v14 offset:2048\n" \
"v_add_u32 v2,s58,v22\nM v[108:111],a[208:211],a[144:147],v[36:39]\nbuffer_store_dwordx4 v[48:51],v4,s[36:39],s72 offen offset:3072 sc1\nM v[108:111],a[212:215],a[148:151],v[108:111]\ns_lshl_b32 s58,s61,2\nds_read_b128 a[204:207],v14 offset:3072\nM v[108:111],a[216:219],a[152:155],v[108:111]\ns_add_u32 s58,s58,s60\n" \
"M v[108:111],a[220:223],a[156:159],v[108:111]\ns_lshl_b32 s58,s58,6\nM v[16:19],a[236:239],v[88:91],v[16:19]\ns_add_u32 s58,s58,94388224\nv_lshl_add_u32 v5,v52,11,s58\nv_bfe_u32 v52,v4,4,3\nM v[116:119],a[208:211],a[160:163],v[40:43]\ns_lshl_b32 s58,%2,13\n" \
"v_lshl_add_u32 v5,v52,3,v5\nv_mov_b32 v52,s10\nM v[116:119],a[212:215],a[164:167],v[116:119]\ns_add_u32 s58,s58,0x20000\nv_max3_f32 v15,v16,v17,v18\nv_max_f32 v15,v15,v19\nM v[116:119],a[216:219],a[168:171],v[116:119]\nv_cmp_nge_f32 vcc,0x53800000,v15\n" \
"v_add_u32 v12,s58,v4\nM v[116:119],a[220:223],a[172:175],v[116:119]\ns_lshl_b32 s58,s7,1\nv_xor_b32 v57,0x4000,v12\nM v[124:127],a[208:211],a[176:179],v[44:47]\ns_sub_u32 s58,15,s58\nds_read_b128 a[208:211],v14 offset:4096\nM v[124:127],a[212:215],a[180:183],v[124:127]\n" \
"s_cbranch_vccnz Lcs43_%=\nLcr44_%=:\nds_read_b128 a[212:215],v14 offset:5120\nM v[124:127],a[216:219],a[184:187],v[124:127]\ns_cmp_eq_u32 s52,s55\nds_read_b128 a[216:219],v14 offset:6144\nM v[124:127],a[220:223],a[188:191],v[124:127]\ns_cbranch_scc1 LH1_%=\n" \
"LH1R10_%=:\ns_waitcnt lgkmcnt(3)\nds_read_b128 a[220:223],v14 offset:7168\nv_xor_b32 v13,0x10000,v13\nM v[128:131],a[192:195],v[64:67],v[128:131]\nbuffer_store_dwordx4 v[48:51],v4,s[36:39],s73 offen offset:0 sc1\nM v[160:163],a[192:195],v[72:75],v[160:163]\ns_lshl_b32 s59,s58,20\n" \
"v_exp_f32 v96,v96\nM v[192:195],a[192:195],v[80:83],v[192:195]\ns_add_u32 s51,s68,s59\nv_exp_f32 v97,v97\nM v[224:227],a[192:195],v[88:91],v[224:227]\ns_lshl_b32 s59,s54,6\nv_exp_f32 v98,v98\nM v[228:231],a[196:199],v[88:91],v[228:231]\n" \
"buffer_load_dwordx4 v22,s[40:43],s48 offen offset:1024 sc1 lds\nM v[196:199],a[196:199],v[80:83],v[196:199]\ns_add_u32 s51,s51,s59\nv_exp_f32 v99,v99\nM v[164:167],a[196:199],v[72:75],v[164:167]\ns_waitcnt vmcnt(15) lgkmcnt(0)\nv_exp_f32 v100,v100\nM v[132:135],a[196:199],v[64:67],v[132:135]\n" \
"s_add_u32 s62,s51,0\ns_barrier\nds_read_b128 a[192:195],v13 offset:32768\nv_exp_f32 v101,v101\nM v[136:139],a[200:203],v[64:67],v[136:139]\ns_lshl_b32 s59,s58,10\nv_exp_f32 v102,v102\nM v[168:171],a[200:203],v[72:75],v[168:171]\n" \
"s_add_u32 s50,s69,s59\nv_exp_f32 v103,v103\nM v[200:203],a[200:203],v[80:83],v[200:203]\nbuffer_load_dwordx4 v22,s[40:43],s48 offen offset:2048 sc1 lds\nv_exp_f32 v104,v104\nM v[232:235],a[200:203],v[88:91],v[232:235]\ns_lshr_b32 s59,s54,4\nv_exp_f32 v105,v105\n" \
"M v[236:239],a[204:207],v[88:91],v[236:239]\ns_add_u32 s50,s50,s59\nv_exp_f32 v106,v106\nM v[204:207],a[204:207],v[80:83],v[204:207]\nv_exp_f32 v107,v107\nM v[172:175],a[204:207],v[72:75],v[172:175]\nbuffer_load_dwordx4 v22,s[40:43],s48 offen offset:3072 sc1 lds\nv_exp_f32 v108,v108\n" \
"M v[140:143],a[204:207],v[64:67],v[140:143]\ns_add_u32 m0,s22,114688\nv_exp_f32 v109,v109\nM v[144:147],a[208:211],v[64:67],v[144:147]\ns_add_u32 s48,s48,s49\nv_exp_f32 v110,v110\nM v[176:179],a[208:211],v[72:75],v[176:179]\nv_exp_f32 v111,v111\n" \
"M v[208:211],a[208:211],v[80:83],v[208:211]\nv_exp_f32 v112,v112\nM v[240:243],a[208:211],v[88:91],v[240:243]\nv_exp_f32 v113,v113\nM v[244:247],a[212:215],v[88:91],v[244:247]\nv_exp_f32 v114,v114\nM v[212:215],a[212:215],v[80:83],v[212:215]\nv_exp_f32 v115,v115\n" \
"M v[180:183],a[212:215],v[72:75],v[180:183]\nds_read_b128 a[196:199],v13 offset:34816\nds_read_b128 a[200:203],v13 offset:36864\nv_exp_f32 v116,v116\nM v[148:151],a[212:215],v[64:67],v[148:151]\nv_exp_f32 v117,v117\nM v[152:155],a[216:219],v[64:67],v[152:155]\nbuffer_load_dwordx4 v1,s[84:87],s46 offen offset:0 lds\n" \
"M v[184:187],a[216:219],v[72:75],v[184:187]\ns_add_u32 m0,s22,115584\nds_read_b128 a[204:207],v13 offset:38912\nC v96,v96,v97\nv_exp_f32 v118,v118\nM v[216:219],a[216:219],v[80:83],v[216:219]\nv_exp_f32 v119,v119\nM v[248:251],a[216:219],v[88:91],v[248:251]\n" \
"C v97,v98,v99\nC v104,v104,v105\nM v[252:255],a[220:223],v[88:91],v[252:255]\nbuffer_load_dwordx4 v1,s[84:87],s46 offen offset:128 lds\nM v[220:223],a[220:223],v[80:83],v[220:223]\ns_add_u32 m0,s22,116736\nds_read_b128 a[208:211],v13 offset:33792\nC v98,v100,v101\n" \
"M v[188:191],a[220:223],v[72:75],v[188:191]\ns_mov_b32 s46,327680\nds_read_b128 a[212:215],v13 offset:35840\nC v99,v102,v103\nM v[156:159],a[220:223],v[64:67],v[156:159]\ns_waitcnt lgkmcnt(2)\nv_exp_f32 v120,v120\nM v[64:67],a[192:195],a[128:131],v[32:35]\n" \
"buffer_load_dwordx4 v1,s[84:87],s46 offen offset:0 lds\nM v[64:67],a[196:199],a[132:135],v[64:67]\ns_add_u32 m0,s22,117632\nds_read_b128 a[216:219],v13 offset:37888\nC v105,v106,v107\nM v[24:27],a[224:227],v[96:99],v[16:19]\nds_read_b128 a[220:223],v13 offset:39936\nC v106,v108,v109\n" \
"M v[64:67],a[200:203],a[136:139],v[64:67]\nv_exp_f32 v121,v121\nM v[64:67],a[204:207],a[140:143],v[64:67]\nbuffer_load_dwordx4 v1,s[84:87],s46 offen offset:128 lds\nM v[72:75],a[192:195],a[144:147],v[36:39]\ns_add_u32 m0,s22,0\nC v107,v110,v111\nC v112,v112,v113\n" \
"M v[72:75],a[196:199],a[148:151],v[72:75]\ns_mov_b32 s46,393216\nv_exp_f32 v122,v122\nM v[24:27],a[228:231],v[104:107],v[24:27]\nv_exp_f32 v123,v123\nM v[72:75],a[200:203],a[152:155],v[72:75]\nbuffer_load_dwordx4 v22,s[40:43],s45 offen offset:0 sc1 lds\nM v[72:75],a[204:207],a[156:159],v[72:75]\n" \
"v_exp_f32 v124,v124\nM v[80:83],a[192:195],a[160:163],v[40:43]\nC v113,v114,v115\nC v114,v116,v117\nM v[80:83],a[196:199],a[164:167],v[80:83]\nC v115,v118,v119\nC v120,v120,v121\nM v[80:83],a[200:203],a[168:171],v[80:83]\n" \
"buffer_load_dwordx4 v22,s[40:43],s45 offen offset:1024 sc1 lds\nM v[80:83],a[204:207],a[172:175],v[80:83]\nv_exp_f32 v125,v125\nM v[88:91],a[192:195],a[176:179],v[44:47]\nds_read_b128 a[192:195],v14 offset:16384\nC v121,v122,v123\nM v[88:91],a[196:199],a[180:183],v[88:91]\nv_exp_f32 v126,v126\n" \
"M v[88:91],a[200:203],a[184:187],v[88:91]\nbuffer_load_dwordx4 v22,s[40:43],s45 offen offset:2048 sc1 lds\nM v[88:91],a[204:207],a[188:191],v[88:91]\nds_read_b128 a[196:199],v14 offset:17408\nC v122,v124,v125\nM v[24:27],a[232:235],v[112:115],v[24:27]\ns_waitcnt lgkmcnt(2)\nds_read_b128 a[200:203],v14 offset:18432\n" \
"v_xor_b32 v13,0x10000,v13\nM v[68:71],a[208:211],a[128:131],v[32:35]\nds_read_b64_tr_b16 a[0:1],v6 offset:49152\nM v[68:71],a[212:215],a[132:135],v[68:71]\nbuffer_load_dwordx4 v22,s[40:43],s45 offen offset:3072 sc1 lds\nM v[68:71],a[216:219],a[136:139],v[68:71]\ns_add_u32 m0,s22,32768\nds_read_b64_tr_b16 a[2:3],v7 offset:49152\n" \
"M v[68:71],a[220:223],a[140:143],v[68:71]\ns_add_u32 s45,s45,s49\nds_read_b64_tr_b16 a[4:5],v8 offset:49152\nM v[76:79],a[208:211],a[144:147],v[36:39]\nv_exp_f32 v127,v127\nM v[76:79],a[212:215],a[148:151],v[76:79]\nbuffer_store_dwordx4 v[48:51],v4,s[36:39],s73 offen offset:1024 sc1\nM v[76:79],a[216:219],a[152:155],v[76:79]\n" \
"ds_read_b64_tr_b16 a[6:7],v9 offset:49152\nC v123,v126,v127\nM v[76:79],a[220:223],a[156:159],v[76:79]\nds_read_b64_tr_b16 a[8:9],v6 offset:51200\nds_read_b64_tr_b16 a[10:11],v7 offset:51200\nM v[24:27],a[236:239],v[120:123],v[24:27]\nds_read_b64_tr_b16 a[12:13],v8 offset:51200\nv_xor_b32 v6,0x10000,v6\n" \
"M v[84:87],a[208:211],a[160:163],v[40:43]\nds_read_b64_tr_b16 a[14:15],v9 offset:51200\nds_read_b128 a[204:207],v14 offset:19456\nM v[84:87],a[212:215],a[164:167],v[84:87]\nv_max3_f32 v15,v24,v25,v26\nv_max_f32 v15,v15,v27\nM v[84:87],a[216:219],a[168:171],v[84:87]\nv_cmp_nge_f32 vcc,0x53800000,v15\n" \
"v_xor_b32 v7,0x10000,v7\nM v[84:87],a[220:223],a[172:175],v[84:87]\nv_xor_b32 v8,0x10000,v8\nv_xor_b32 v9,0x10000,v9\nM v[92:95],a[208:211],a[176:179],v[44:47]\nds_read_b128 a[208:211],v14 offset:20480\nM v[92:95],a[212:215],a[180:183],v[92:95]\ns_cbranch_vccnz Lcs45_%=\n" \
"Lcr46_%=:\nds_read_b128 a[212:215],v14 offset:21504\nM v[92:95],a[216:219],a[184:187],v[92:95]\nds_read_b128 a[216:219],v14 offset:22528\nM v[92:95],a[220:223],a[188:191],v[92:95]\ns_waitcnt lgkmcnt(3)\nds_read_b128 a[220:223],v14 offset:23552\nM v[128:131],a[192:195],v[96:99],v[128:131]\n" \
"v_exp_f32 v64,v64\nM v[160:163],a[192:195],v[104:107],v[160:163]\nv_exp_f32 v65,v65\nM v[192:195],a[192:195],v[112:115],v[192:195]\nv_exp_f32 v66,v66\nM v[224:227],a[192:195],v[120:123],v[224:227]\nv_exp_f32 v67,v67\nM v[228:231],a[196:199],v[120:123],v[228:231]\n" \
"v_exp_f32 v68,v68\nM v[196:199],a[196:199],v[112:115],v[196:199]\nv_exp_f32 v69,v69\nM v[164:167],a[196:199],v[104:107],v[164:167]\ns_waitcnt vmcnt(9) lgkmcnt(0)\nv_exp_f32 v70,v70\nM v[132:135],a[196:199],v[96:99],v[132:135]\ns_barrier\n" \
"buffer_load_dwordx4 v22,s[40:43],s48 offen offset:0 sc1 lds\nM v[136:139],a[200:203],v[96:99],v[136:139]\nv_exp_f32 v71,v71\nM v[168:171],a[200:203],v[104:107],v[168:171]\nv_exp_f32 v72,v72\nM v[200:203],a[200:203],v[112:115],v[200:203]\nv_exp_f32 v73,v73\nM v[232:235],a[200:203],v[120:123],v[232:235]\n" \
"buffer_load_dwordx4 v22,s[40:43],s48 offen offset:1024 sc1 lds\nM v[236:239],a[204:207],v[120:123],v[236:239]\nds_read_b128 a[192:195],v13 offset:32768\nC v64,v64,v65\nM v[204:207],a[204:207],v[112:115],v[204:207]\nv_exp_f32 v74,v74\nM v[172:175],a[204:207],v[104:107],v[172:175]\nv_exp_f32 v75,v75\n" \
"M v[140:143],a[204:207],v[96:99],v[140:143]\nbuffer_load_dwordx4 v22,s[40:43],s48 offen offset:2048 sc1 lds\nv_exp_f32 v76,v76\nM v[144:147],a[208:211],v[96:99],v[144:147]\nv_exp_f32 v77,v77\nM v[176:179],a[208:211],v[104:107],v[176:179]\nds_read_b128 a[196:199],v13 offset:34816\nds_read_b128 a[200:203],v13 offset:36864\n" \
"M v[208:211],a[208:211],v[112:115],v[208:211]\nv_exp_f32 v78,v78\nM v[240:243],a[208:211],v[120:123],v[240:243]\nbuffer_load_dwordx4 v22,s[40:43],s48 offen offset:3072 sc1 lds\nv_exp_f32 v79,v79\nM v[244:247],a[212:215],v[120:123],v[244:247]\ns_add_u32 m0,s22,16384\nv_exp_f32 v80,v80\n" \
"M v[212:215],a[212:215],v[112:115],v[212:215]\ns_add_u32 s48,s48,s49\nds_read_b128 a[204:207],v13 offset:38912\nC v65,v66,v67\nM v[180:183],a[212:215],v[104:107],v[180:183]\ns_add_u32 s52,s52,1\nv_exp_f32 v81,v81\nM v[148:151],a[212:215],v[96:99],v[148:151]\n" \
"buffer_load_dwordx4 v1,s[84:87],s46 offen offset:0 lds\nM v[152:155],a[216:219],v[96:99],v[152:155]\ns_add_u32 m0,s22,17280\nv_exp_f32 v82,v82\nM v[184:187],a[216:219],v[104:107],v[184:187]\nv_exp_f32 v83,v83\nM v[216:219],a[216:219],v[112:115],v[216:219]\nv_exp_f32 v84,v84\n" \
"M v[248:251],a[216:219],v[120:123],v[248:251]\nbuffer_load_dwordx4 v1,s[84:87],s46 offen offset:128 lds\nM v[252:255],a[220:223],v[120:123],v[252:255]\ns_add_u32 m0,s22,18432\nv_exp_f32 v85,v85\nM v[220:223],a[220:223],v[112:115],v[220:223]\ns_mov_b32 s46,458752\nv_exp_f32 v86,v86\n" \
"M v[188:191],a[220:223],v[104:107],v[188:191]\ns_cmp_eq_u32 s52,s56\nC v66,v68,v69\nC v67,v70,v71\nM v[156:159],a[220:223],v[96:99],v[156:159]\ns_waitcnt lgkmcnt(0)\nbuffer_load_dwordx4 v1,s[84:87],s46 offen offset:0 lds\nM v[96:99],a[192:195],a[128:131],v[32:35]\n" \
"s_cbranch_scc1 LEV_%=\nLEVR10_%=:\nds_read_b128 a[208:211],v13 offset:33792\nC v72,v72,v73\nM v[96:99],a[196:199],a[132:135],v[96:99]\ns_add_u32 m0,s22,19328\nds_read_b128 a[212:215],v13 offset:35840\nC v73,v74,v75\n" \
"M v[16:19],a[224:227],v[64:67],v[24:27]\nds_read_b128 a[216:219],v13 offset:37888\nC v74,v76,v77\nM v[96:99],a[200:203],a[136:139],v[96:99]\nv_exp_f32 v87,v87\nM v[96:99],a[204:207],a[140:143],v[96:99]\nbuffer_load_dwordx4 v1,s[84:87],s46 offen offset:128 lds\nM v[104:107],a[192:195],a[144:147],v[36:39]\n" \
"s_add_u32 m0,s22,49152\nds_read_b128 a[220:223],v13 offset:39936\nC v75,v78,v79\nv_exp_f32 v88,v88\nM v[104:107],a[196:199],a[148:151],v[104:107]\ns_mov_b32 s46,524288\nv_exp_f32 v89,v89\nM v[16:19],a[228:231],v[72:75],v[16:19]\n" \
"v_exp_f32 v90,v90\nM v[104:107],a[200:203],a[152:155],v[104:107]\nbuffer_load_dwordx4 v22,s[40:43],s45 offen offset:0 sc1 lds\nM v[104:107],a[204:207],a[156:159],v[104:107]\nv_exp_f32 v91,v91\nM v[112:115],a[192:195],a[160:163],v[40:43]\nv_exp_f32 v92,v92\nM v[112:115],a[196:199],a[164:167],v[112:115]\n" \
"C v80,v80,v81\nC v81,v82,v83\nM v[112:115],a[200:203],a[168:171],v[112:115]\nbuffer_load_dwordx4 v22,s[40:43],s45 offen offset:1024 sc1 lds\nM v[112:115],a[204:207],a[172:175],v[112:115]\nv_exp_f32 v93,v93\nM v[120:123],a[192:195],a[176:179],v[44:47]\nC v82,v84,v85\n" \
"C v83,v86,v87\nM v[120:123],a[196:199],a[180:183],v[120:123]\nv_exp_f32 v94,v94\nM v[120:123],a[200:203],a[184:187],v[120:123]\nbuffer_load_dwordx4 v22,s[40:43],s45 offen offset:2048 sc1 lds\nM v[120:123],a[204:207],a[188:191],v[120:123]\nLG18_%=:\ns_cbranch_execz LHR2_%=\n" \
"C v88,v88,v89\nC v89,v90,v91\nM v[16:19],a[232:235],v[80:83],v[16:19]\ns_waitcnt lgkmcnt(0)\nds_read_b64_tr_b16 a[16:17],v6 offset:0\nds_read_b64_tr_b16 a[18:19],v7 offset:0\nM v[100:103],a[208:211],a[128:131],v[32:35]\nds_read_b64_tr_b16 a[20:21],v8 offset:0\n" \
"C v90,v92,v93\nM v[100:103],a[212:215],a[132:135],v[100:103]\nbuffer_load_dwordx4 v22,s[40:43],s45 offen offset:3072 sc1 lds\nM v[100:103],a[216:219],a[136:139],v[100:103]\ns_add_u32 m0,s22,65536\nv_exp_f32 v95,v95\nM v[100:103],a[220:223],a[140:143],v[100:103]\ns_add_u32 s45,s45,s49\n" \
"ds_read_b64_tr_b16 a[22:23],v9 offset:0\nds_read_b64_tr_b16 a[24:25],v6 offset:2048\nM v[108:111],a[208:211],a[144:147],v[36:39]\nds_read_b64_tr_b16 a[26:27],v7 offset:2048\nv_xor_b32 v14,0x10000,v14\nM v[108:111],a[212:215],a[148:151],v[108:111]\nds_read_b128 a[192:195],v14 offset:16384\nC v91,v94,v95\n" \
"M v[108:111],a[216:219],a[152:155],v[108:111]\nds_read_b128 a[196:199],v14 offset:17408\nv_xor_b32 v13,0x10000,v13\nM v[108:111],a[220:223],a[156:159],v[108:111]\nds_read_b128 a[200:203],v14 offset:18432\nM v[16:19],a[236:239],v[88:91],v[16:19]\nds_read_b64_tr_b16 a[28:29],v8 offset:2048\nds_read_b64_tr_b16 a[30:31],v9 offset:2048\n" \
"M v[116:119],a[208:211],a[160:163],v[40:43]\nds_read_b128 a[204:207],v14 offset:19456\nM v[116:119],a[212:215],a[164:167],v[116:119]\nv_max3_f32 v15,v16,v17,v18\nv_max_f32 v15,v15,v19\nM v[116:119],a[216:219],a[168:171],v[116:119]\nv_cmp_nge_f32 vcc,0x53800000,v15\nM v[116:119],a[220:223],a[172:175],v[116:119]\n" \
"M v[124:127],a[208:211],a[176:179],v[44:47]\nds_read_b128 a[208:211],v14 offset:20480\nM v[124:127],a[212:215],a[180:183],v[124:127]\ns_cbranch_vccnz Lcs47_%=\nLcr48_%=:\nds_read_b128 a[212:215],v14 offset:21504\nM v[124:127],a[216:219],a[184:187],v[124:127]\nds_read_b128 a[216:219],v14 offset:22528\n" \
"M v[124:127],a[220:223],a[188:191],v[124:127]\ns_waitcnt lgkmcnt(3)\nds_read_b128 a[220:223],v14 offset:23552\nv_xor_b32 v14,0x10000,v14\nM v[128:131],a[192:195],v[64:67],v[128:131]\nv_exp_f32 v96,v96\nM v[160:163],a[192:195],v[72:75],v[160:163]\nv_exp_f32 v97,v97\n" \
"M v[192:195],a[192:195],v[80:83],v[192:195]\nv_exp_f32 v98,v98\nM v[224:227],a[192:195],v[88:91],v[224:227]\nv_exp_f32 v99,v99\nM v[228:231],a[196:199],v[88:91],v[228:231]\nv_exp_f32 v100,v100\nM v[196:199],a[196:199],v[80:83],v[196:199]\nv_exp_f32 v101,v101\n" \
"M v[164:167],a[196:199],v[72:75],v[164:167]\ns_waitcnt vmcnt(8) lgkmcnt(0)\nv_exp_f32 v102,v102\nM v[132:135],a[196:199],v[64:67],v[132:135]\ns_barrier\nbuffer_load_dwordx4 v22,s[40:43],s48 offen offset:0 sc1 lds\nM v[136:139],a[200:203],v[64:67],v[136:139]\nv_exp_f32 v103,v103\n" \
"M v[168:171],a[200:203],v[72:75],v[168:171]\nv_exp_f32 v104,v104\nM v[200:203],a[200:203],v[80:83],v[200:203]\nv_exp_f32 v105,v105\nM v[232:235],a[200:203],v[88:91],v[232:235]\nbuffer_load_dwordx4 v22,s[40:43],s48 offen offset:1024 sc1 lds\nM v[236:239],a[204:207],v[88:91],v[236:239]\nds_read_b128 a[192:195],v13 offset:0\n" \
"C v96,v96,v97\nM v[204:207],a[204:207],v[80:83],v[204:207]\nv_exp_f32 v106,v106\nM v[172:175],a[204:207],v[72:75],v[172:175]\nds_read_b128 a[196:199],v13 offset:2048\nds_read_b128 a[200:203],v13 offset:4096\nM v[140:143],a[204:207],v[64:67],v[140:143]\nbuffer_load_dwordx4 v22,s[40:43],s48 offen offset:2048 sc1 lds\n" \
"M v[144:147],a[208:211],v[64:67],v[144:147]\nv_exp_f32 v107,v107\nM v[176:179],a[208:211],v[72:75],v[176:179]\nds_read_b128 a[204:207],v13 offset:6144\nC v97,v98,v99\nM v[208:211],a[208:211],v[80:83],v[208:211]\nv_exp_f32 v108,v108\nM v[240:243],a[208:211],v[88:91],v[240:243]\n" \
"buffer_load_dwordx4 v22,s[40:43],s48 offen offset:3072 sc1 lds\nv_exp_f32 v109,v109\nM v[244:247],a[212:215],v[88:91],v[244:247]\ns_add_u32 m0,s22,98304\nv_exp_f32 v110,v110\nM v[212:215],a[212:215],v[80:83],v[212:215]\ns_add_u32 s48,s48,s49\nv_exp_f32 v111,v111\n" \
"M v[180:183],a[212:215],v[72:75],v[180:183]\nv_exp_f32 v112,v112\nM v[148:151],a[212:215],v[64:67],v[148:151]\nbuffer_load_dwordx4 v1,s[84:87],s46 offen offset:0 lds\nM v[152:155],a[216:219],v[64:67],v[152:155]\ns_add_u32 m0,s22,99200\nv_exp_f32 v113,v113\nM v[184:187],a[216:219],v[72:75],v[184:187]\n" \
"v_exp_f32 v114,v114\nM v[216:219],a[216:219],v[80:83],v[216:219]\nv_exp_f32 v115,v115\nM v[248:251],a[216:219],v[88:91],v[248:251]\nbuffer_load_dwordx4 v1,s[84:87],s46 offen offset:128 lds\nM v[252:255],a[220:223],v[88:91],v[252:255]\ns_add_u32 m0,s22,100352\nv_exp_f32 v116,v116\n" \
"M v[220:223],a[220:223],v[80:83],v[220:223]\ns_mov_b32 s46,589824\nC v98,v100,v101\nC v99,v102,v103\nM v[188:191],a[220:223],v[72:75],v[188:191]\ns_waitcnt lgkmcnt(0)\nv_exp_f32 v117,v117\nM v[156:159],a[220:223],v[64:67],v[156:159]\n" \
"buffer_load_dwordx4 v1,s[84:87],s46 offen offset:0 lds\nM v[64:67],a[192:195],a[128:131],v[32:35]\ns_add_u32 m0,s22,101248\nds_read_b128 a[208:211],v13 offset:1024\nC v104,v104,v105\nM v[64:67],a[196:199],a[132:135],v[64:67]\nds_read_b128 a[212:215],v13 offset:3072\nC v105,v106,v107\n" \
"M v[24:27],a[224:227],v[96:99],v[16:19]\nds_read_b128 a[216:219],v13 offset:5120\nC v106,v108,v109\nM v[64:67],a[200:203],a[136:139],v[64:67]\nbuffer_load_dwordx4 v1,s[84:87],s46 offen offset:128 lds\nM v[64:67],a[204:207],a[140:143],v[64:67]\ns_add_u32 m0,s22,81920\nds_read_b128 a[220:223],v13 offset:7168\n" \
"C v107,v110,v111\nM v[72:75],a[192:195],a[144:147],v[36:39]\ns_mov_b32 s46,655360\nv_exp_f32 v118,v118\nM v[72:75],a[196:199],a[148:151],v[72:75]\nv_exp_f32 v119,v119\nM v[24:27],a[228:231],v[104:107],v[24:27]\nbuffer_load_dwordx4 v22,s[40:43],s45 offen offset:0 sc1 lds\n" \
"M v[72:75],a[200:203],a[152:155],v[72:75]\ns_waitcnt vmcnt(21)\nv_exp_f32 v120,v120\nM v[72:75],a[204:207],a[156:159],v[72:75]\nv_exp_f32 v121,v121\nM v[80:83],a[192:195],a[160:163],v[40:43]\nv_exp_f32 v122,v122\nM v[80:83],a[196:199],a[164:167],v[80:83]\n" \
"buffer_store_dwordx2 v[52:53],v56,s[36:39],s95 offen offset:24 sc1\nM v[80:83],a[200:203],a[168:171],v[80:83]\nC v112,v112,v113\nC v113,v114,v115\nM v[80:83],a[204:207],a[172:175],v[80:83]\nbuffer_load_dwordx4 v22,s[40:43],s45 offen offset:1024 sc1 lds\nM v[88:91],a[192:195],a[176:179],v[44:47]\nds_read_b128 a[192:195],v14 offset:32768\n" \
"C v114,v116,v117\nM v[88:91],a[196:199],a[180:183],v[88:91]\nv_exp_f32 v123,v123\nM v[88:91],a[200:203],a[184:187],v[88:91]\nds_read_b128 a[196:199],v14 offset:33792\nC v115,v118,v119\nM v[88:91],a[204:207],a[188:191],v[88:91]\nbuffer_load_dwordx4 v22,s[40:43],s45 offen offset:2048 sc1 lds\n" \
"M v[24:27],a[232:235],v[112:115],v[24:27]\ns_waitcnt lgkmcnt(2)\nds_read_b128 a[200:203],v14 offset:34816\nC v120,v120,v121\nM v[68:71],a[208:211],a[128:131],v[32:35]\nv_exp_f32 v124,v124\nM v[68:71],a[212:215],a[132:135],v[68:71]\nv_exp_f32 v125,v125\n" \
"M v[68:71],a[216:219],a[136:139],v[68:71]\nv_exp_f32 v126,v126\nM v[68:71],a[220:223],a[140:143],v[68:71]\nbuffer_load_dwordx4 v22,s[40:43],s45 offen offset:3072 sc1 lds\nM v[76:79],a[208:211],a[144:147],v[36:39]\ns_add_u32 m0,s22,114688\nds_read_b64_tr_b16 a[32:33],v6 offset:49152\nC v121,v122,v123\n" \
"M v[76:79],a[212:215],a[148:151],v[76:79]\ns_add_u32 s45,s45,s49\nv_exp_f32 v127,v127\nM v[76:79],a[216:219],a[152:155],v[76:79]\nC v122,v124,v125\nC v123,v126,v127\nM v[76:79],a[220:223],a[156:159],v[76:79]\nds_read_b64_tr_b16 a[34:35],v7 offset:49152\n" \
"ds_read_b64_tr_b16 a[36:37],v8 offset:49152\nM v[24:27],a[236:239],v[120:123],v[24:27]\nds_read_b64_tr_b16 a[38:39],v9 offset:49152\nM v[84:87],a[208:211],a[160:163],v[40:43]\nds_read_b64_tr_b16 a[40:41],v6 offset:51200\nds_read_b64_tr_b16 a[42:43],v7 offset:51200\nM v[84:87],a[212:215],a[164:167],v[84:87]\nds_read_b64_tr_b16 a[44:45],v8 offset:51200\n" \
"v_max3_f32 v15,v24,v25,v26\nM v[84:87],a[216:219],a[168:171],v[84:87]\nds_read_b64_tr_b16 a[46:47],v9 offset:51200\nv_max_f32 v15,v15,v27\nM v[84:87],a[220:223],a[172:175],v[84:87]\nds_read_b128 a[204:207],v14 offset:35840\nv_cmp_nge_f32 vcc,0x53800000,v15\nM v[92:95],a[208:211],a[176:179],v[44:47]\n" \
"ds_read_b128 a[208:211],v14 offset:36864\nv_xor_b32 v6,0x10000,v6\nM v[92:95],a[212:215],a[180:183],v[92:95]\ns_cbranch_vccnz Lcs49_%=\nLcr50_%=:\nds_read_b128 a[212:215],v14 offset:37888\nv_xor_b32 v7,0x10000,v7\nM v[92:95],a[216:219],a[184:187],v[92:95]\n" \
"ds_read_b128 a[216:219],v14 offset:38912\nv_xor_b32 v8,0x10000,v8\nM v[92:95],a[220:223],a[188:191],v[92:95]\ns_waitcnt lgkmcnt(5)\nds_read_b128 a[220:223],v14 offset:39936\nv_xor_b32 v9,0x10000,v9\nM v[128:131],a[192:195],v[96:99],v[128:131]\nv_exp_f32 v64,v64\n" \
"M v[160:163],a[192:195],v[104:107],v[160:163]\nv_exp_f32 v65,v65\nM v[192:195],a[192:195],v[112:115],v[192:195]\nv_exp_f32 v66,v66\nM v[224:227],a[192:195],v[120:123],v[224:227]\nv_exp_f32 v67,v67\nM v[228:231],a[196:199],v[120:123],v[228:231]\nv_exp_f32 v68,v68\n" \
"M v[196:199],a[196:199],v[112:115],v[196:199]\nv_exp_f32 v69,v69\nM v[164:167],a[196:199],v[104:107],v[164:167]\ns_waitcnt vmcnt(9) lgkmcnt(0)\nv_exp_f32 v70,v70\nM v[132:135],a[196:199],v[96:99],v[132:135]\ns_barrier\nbuffer_load_dwordx4 v22,s[40:43],s48 offen offset:0 sc1 lds\n" \
"M v[136:139],a[200:203],v[96:99],v[136:139]\nv_exp_f32 v71,v71\nM v[168:171],a[200:203],v[104:107],v[168:171]\nv_exp_f32 v72,v72\nM v[200:203],a[200:203],v[112:115],v[200:203]\nv_exp_f32 v73,v73\nM v[232:235],a[200:203],v[120:123],v[232:235]\nbuffer_load_dwordx4 v22,s[40:43],s48 offen offset:1024 sc1 lds\n" \
"M v[236:239],a[204:207],v[120:123],v[236:239]\nds_read_b128 a[192:195],v13 offset:49152\nC v64,v64,v65\nM v[204:207],a[204:207],v[112:115],v[204:207]\nv_exp_f32 v74,v74\nM v[172:175],a[204:207],v[104:107],v[172:175]\nds_read_b128 a[196:199],v13 offset:51200\nds_read_b128 a[200:203],v13 offset:53248\n" \
"M v[140:143],a[204:207],v[96:99],v[140:143]\nbuffer_load_dwordx4 v22,s[40:43],s48 offen offset:2048 sc1 lds\nv_exp_f32 v75,v75\nM v[144:147],a[208:211],v[96:99],v[144:147]\nv_exp_f32 v76,v76\nM v[176:179],a[208:211],v[104:107],v[176:179]\nds_read_b128 a[204:207],v13 offset:55296\nC v65,v66,v67\n" \
"M v[208:211],a[208:211],v[112:115],v[208:211]\nv_exp_f32 v77,v77\nM v[240:243],a[208:211],v[120:123],v[240:243]\nbuffer_load_dwordx4 v22,s[40:43],s48 offen offset:3072 sc1 lds\nv_exp_f32 v78,v78\nM v[244:247],a[212:215],v[120:123],v[244:247]\ns_add_u32 m0,s22,0\nv_exp_f32 v79,v79\n" \
"M v[212:215],a[212:215],v[112:115],v[212:215]\ns_add_u32 s48,s48,s49\nv_exp_f32 v80,v80\nM v[180:183],a[212:215],v[104:107],v[180:183]\ns_add_u32 s52,s52,1\nv_exp_f32 v81,v81\nM v[148:151],a[212:215],v[96:99],v[148:151]\nbuffer_load_dwordx4 v1,s[84:87],s46 offen offset:0 lds\n" \
"M v[152:155],a[216:219],v[96:99],v[152:155]\ns_add_u32 m0,s22,896\nv_exp_f32 v82,v82\nM v[184:187],a[216:219],v[104:107],v[184:187]\nv_exp_f32 v83,v83\nM v[216:219],a[216:219],v[112:115],v[216:219]\nv_exp_f32 v84,v84\nM v[248:251],a[216:219],v[120:123],v[248:251]\n" \
"buffer_load_dwordx4 v1,s[84:87],s46 offen offset:128 lds\nM v[252:255],a[220:223],v[120:123],v[252:255]\ns_add_u32 m0,s22,2048\nv_exp_f32 v85,v85\nM v[220:223],a[220:223],v[112:115],v[220:223]\ns_mov_b32 s46,720896\nv_exp_f32 v86,v86\nM v[188:191],a[220:223],v[104:107],v[188:191]\n" \
"s_cmp_eq_u32 s52,s56\nC v66,v68,v69\nC v67,v70,v71\nM v[156:159],a[220:223],v[96:99],v[156:159]\ns_waitcnt lgkmcnt(0)\nbuffer_load_dwordx4 v1,s[84:87],s46 offen offset:0 lds\nM v[96:99],a[192:195],a[128:131],v[32:35]\ns_cbranch_scc1 LEV_%=\n" \
"LEVR11_%=:\nds_read_b128 a[208:211],v13 offset:50176\nC v72,v72,v73\nM v[96:99],a[196:199],a[132:135],v[96:99]\ns_add_u32 m0,s22,2944\nds_read_b128 a[212:215],v13 offset:52224\nC v73,v74,v75\nM v[16:19],a[224:227],v[64:67],v[24:27]\n" \
"ds_read_b128 a[216:219],v13 offset:54272\nC v74,v76,v77\nM v[96:99],a[200:203],a[136:139],v[96:99]\nv_exp_f32 v87,v87\nM v[96:99],a[204:207],a[140:143],v[96:99]\nbuffer_load_dwordx4 v1,s[84:87],s46 offen offset:128 lds\nM v[104:107],a[192:195],a[144:147],v[36:39]\ns_add_u32 m0,s22,32768\n" \
"ds_read_b128 a[220:223],v13 offset:56320\nC v75,v78,v79\nv_exp_f32 v88,v88\nM v[104:107],a[196:199],a[148:151],v[104:107]\ns_mov_b32 s46,786432\nv_exp_f32 v89,v89\nM v[16:19],a[228:231],v[72:75],v[16:19]\nv_exp_f32 v90,v90\n" \
"M v[104:107],a[200:203],a[152:155],v[104:107]\nbuffer_load_dwordx4 v22,s[40:43],s45 offen offset:0 sc1 lds\nM v[104:107],a[204:207],a[156:159],v[104:107]\nv_exp_f32 v91,v91\nM v[112:115],a[192:195],a[160:163],v[40:43]\nv_exp_f32 v92,v92\nM v[112:115],a[196:199],a[164:167],v[112:115]\nC v80,v80,v81\n" \
"C v81,v82,v83\nM v[112:115],a[200:203],a[168:171],v[112:115]\nbuffer_load_dwordx4 v22,s[40:43],s45 offen offset:1024 sc1 lds\nM v[112:115],a[204:207],a[172:175],v[112:115]\nv_exp_f32 v93,v93\nM v[120:123],a[192:195],a[176:179],v[44:47]\nC v82,v84,v85\nC v83,v86,v87\n" \
"M v[120:123],a[196:199],a[180:183],v[120:123]\nv_exp_f32 v94,v94\nM v[120:123],a[200:203],a[184:187],v[120:123]\nbuffer_load_dwordx4 v22,s[40:43],s45 offen offset:2048 sc1 lds\nM v[120:123],a[204:207],a[188:191],v[120:123]\nC v88,v88,v89\nC v89,v90,v91\nM v[16:19],a[232:235],v[80:83],v[16:19]\n" \
"s_waitcnt lgkmcnt(0)\nds_read_b64_tr_b16 a[48:49],v6 offset:16384\nds_read_b64_tr_b16 a[50:51],v7 offset:16384\nM v[100:103],a[208:211],a[128:131],v[32:35]\nds_read_b64_tr_b16 a[52:53],v8 offset:16384\nC v90,v92,v93\nM v[100:103],a[212:215],a[132:135],v[100:103]\nbuffer_load_dwordx4 v22,s[40:43],s45 offen offset:3072 sc1 lds\n" \
"M v[100:103],a[216:219],a[136:139],v[100:103]\ns_add_u32 m0,s22,16384\nv_exp_f32 v95,v95\nM v[100:103],a[220:223],a[140:143],v[100:103]\ns_add_u32 s45,s45,s49\nds_read_b64_tr_b16 a[54:55],v9 offset:16384\nds_read_b64_tr_b16 a[56:57],v6 offset:18432\nM v[108:111],a[208:211],a[144:147],v[36:39]\n" \
"ds_read_b64_tr_b16 a[58:59],v7 offset:18432\nv_xor_b32 v14,0x10000,v14\nM v[108:111],a[212:215],a[148:151],v[108:111]\nds_read_b128 a[192:195],v14 offset:0\nC v91,v94,v95\nM v[108:111],a[216:219],a[152:155],v[108:111]\nds_read_b128 a[196:199],v14 offset:1024\nv_xor_b32 v6,0x10000,v6\n" \
"M v[108:111],a[220:223],a[156:159],v[108:111]\nds_read_b128 a[200:203],v14 offset:2048\nv_xor_b32 v7,0x10000,v7\nM v[16:19],a[236:239],v[88:91],v[16:19]\nds_read_b64_tr_b16 a[60:61],v8 offset:18432\nds_read_b64_tr_b16 a[62:63],v9 offset:18432\nM v[116:119],a[208:211],a[160:163],v[40:43]\nds_read_b128 a[204:207],v14 offset:3072\n" \
"v_xor_b32 v8,0x10000,v8\nM v[116:119],a[212:215],a[164:167],v[116:119]\nv_max3_f32 v15,v16,v17,v18\nv_max_f32 v15,v15,v19\nM v[116:119],a[216:219],a[168:171],v[116:119]\nv_cmp_nge_f32 vcc,0x53800000,v15\nv_xor_b32 v9,0x10000,v9\nM v[116:119],a[220:223],a[172:175],v[116:119]\n" \
"M v[124:127],a[208:211],a[176:179],v[44:47]\ns_waitcnt lgkmcnt(3)\nds_read_b128 a[208:211],v14 offset:4096\nM v[124:127],a[212:215],a[180:183],v[124:127]\ns_cbranch_vccnz Lcs51_%=\nLcr52_%=:\nds_read_b128 a[212:215],v14 offset:5120\nM v[124:127],a[216:219],a[184:187],v[124:127]\n" \
"s_cmp_eq_u32 s52,s55\nds_read_b128 a[216:219],v14 offset:6144\nM v[124:127],a[220:223],a[188:191],v[124:127]\ns_cbranch_scc1 LH1_%=\nLH1R12_%=:\nds_read_b128 a[220:223],v14 offset:7168\nv_xor_b32 v13,0x10000,v13\nM v[128:131],a[192:195],v[64:67],v[128:131]\n" \
"v_exp_f32 v96,v96\nM v[160:163],a[192:195],v[72:75],v[160:163]\nv_exp_f32 v97,v97\nM v[192:195],a[192:195],v[80:83],v[192:195]\nv_exp_f32 v98,v98\nM v[224:227],a[192:195],v[88:91],v[224:227]\nv_exp_f32 v99,v99\nM v[228:231],a[196:199],v[88:91],v[228:231]\n" \
"v_exp_f32 v100,v100\nM v[196:199],a[196:199],v[80:83],v[196:199]\nv_exp_f32 v101,v101\nM v[164:167],a[196:199],v[72:75],v[164:167]\ns_waitcnt vmcnt(8) lgkmcnt(0)\nv_exp_f32 v102,v102\nM v[132:135],a[196:199],v[64:67],v[132:135]\ns_barrier\n" \
"buffer_load_dwordx4 v22,s[40:43],s48 offen offset:0 sc1 lds\nM v[136:139],a[200:203],v[64:67],v[136:139]\nv_exp_f32 v103,v103\nM v[168:171],a[200:203],v[72:75],v[168:171]\nv_exp_f32 v104,v104\nM v[200:203],a[200:203],v[80:83],v[200:203]\nv_exp_f32 v105,v105\nM v[232:235],a[200:203],v[88:91],v[232:235]\n" \
"buffer_load_dwordx4 v22,s[40:43],s48 offen offset:1024 sc1 lds\nM v[236:239],a[204:207],v[88:91],v[236:239]\nds_read_b128 a[192:195],v13 offset:16384\nC v96,v96,v97\nM v[204:207],a[204:207],v[80:83],v[204:207]\nv_exp_f32 v106,v106\nM v[172:175],a[204:207],v[72:75],v[172:175]\nv_exp_f32 v107,v107\n" \
"M v[140:143],a[204:207],v[64:67],v[140:143]\nbuffer_load_dwordx4 v22,s[40:43],s48 offen offset:2048 sc1 lds\nv_exp_f32 v108,v108\nM v[144:147],a[208:211],v[64:67],v[144:147]\nv_exp_f32 v109,v109\nM v[176:179],a[208:211],v[72:75],v[176:179]\nds_read_b128 a[196:199],v13 offset:18432\nds_read_b128 a[200:203],v13 offset:20480\n" \
"M v[208:211],a[208:211],v[80:83],v[208:211]\nv_exp_f32 v110,v110\nM v[240:243],a[208:211],v[88:91],v[240:243]\nbuffer_load_dwordx4 v22,s[40:43],s48 offen offset:3072 sc1 lds\nv_exp_f32 v111,v111\nM v[244:247],a[212:215],v[88:91],v[244:247]\ns_add_u32 m0,s22,49152\nv_exp_f32 v112,v112\n" \
"M v[212:215],a[212:215],v[80:83],v[212:215]\ns_add_u32 s48,s48,s49\nds_read_b128 a[204:207],v13 offset:22528\nC v97,v98,v99\nM v[180:183],a[212:215],v[72:75],v[180:183]\nv_exp_f32 v113,v113\nM v[148:151],a[212:215],v[64:67],v[148:151]\nbuffer_load_dwordx4 v1,s[84:87],s46 offen offset:0 lds\n" \
"M v[152:155],a[216:219],v[64:67],v[152:155]\ns_add_u32 m0,s22,50048\nv_exp_f32 v114,v114\nM v[184:187],a[216:219],v[72:75],v[184:187]\nv_exp_f32 v115,v115\nM v[216:219],a[216:219],v[80:83],v[216:219]\nv_exp_f32 v116,v116\nM v[248:251],a[216:219],v[88:91],v[248:251]\n" \
"buffer_load_dwordx4 v1,s[84:87],s46 offen offset:128 lds\nM v[252:255],a[220:223],v[88:91],v[252:255]\ns_add_u32 m0,s22,51200\nv_exp_f32 v117,v117\nM v[220:223],a[220:223],v[80:83],v[220:223]\ns_mov_b32 s46,851968\nv_exp_f32 v118,v118\nM v[188:191],a[220:223],v[72:75],v[188:191]\n" \
"s_waitcnt lgkmcnt(0)\nC v98,v100,v101\nC v99,v102,v103\nM v[156:159],a[220:223],v[64:67],v[156:159]\nbuffer_load_dwordx4 v1,s[84:87],s46 offen offset:0 lds\nM v[64:67],a[192:195],a[128:131],v[32:35]\ns_add_u32 m0,s22,52096\nds_read_b128 a[208:211],v13 offset:17408\n" \
"C v104,v104,v105\nM v[64:67],a[196:199],a[132:135],v[64:67]\nds_read_b128 a[212:215],v13 offset:19456\nC v105,v106,v107\nM v[24:27],a[224:227],v[96:99],v[16:19]\nds_read_b128 a[216:219],v13 offset:21504\nC v106,v108,v109\nM v[64:67],a[200:203],a[136:139],v[64:67]\n" \
"v_exp_f32 v119,v119\nM v[64:67],a[204:207],a[140:143],v[64:67]\nbuffer_load_dwordx4 v1,s[84:87],s46 offen offset:128 lds\nM v[72:75],a[192:195],a[144:147],v[36:39]\ns_add_u32 m0,s22,65536\nds_read_b128 a[220:223],v13 offset:23552\nC v107,v110,v111\nv_exp_f32 v120,v120\n" \
"M v[72:75],a[196:199],a[148:151],v[72:75]\ns_mov_b32 s46,917504\nv_exp_f32 v121,v121\nM v[24:27],a[228:231],v[104:107],v[24:27]\nv_exp_f32 v122,v122\nM v[72:75],a[200:203],a[152:155],v[72:75]\nbuffer_load_dwordx4 v22,s[40:43],s45 offen offset:0 sc1 lds\nM v[72:75],a[204:207],a[156:159],v[72:75]\n" \
"v_exp_f32 v123,v123\nM v[80:83],a[192:195],a[160:163],v[40:43]\nv_exp_f32 v124,v124\nM v[80:83],a[196:199],a[164:167],v[80:83]\nC v112,v112,v113\nC v113,v114,v115\nM v[80:83],a[200:203],a[168:171],v[80:83]\nbuffer_load_dwordx4 v22,s[40:43],s45 offen offset:1024 sc1 lds\n" \
"M v[80:83],a[204:207],a[172:175],v[80:83]\nv_exp_f32 v125,v125\nM v[88:91],a[192:195],a[176:179],v[44:47]\nds_read_b128 a[192:195],v14 offset:49152\nC v114,v116,v117\nM v[88:91],a[196:199],a[180:183],v[88:91]\nC v115,v118,v119\nC v120,v120,v121\n" \
"M v[88:91],a[200:203],a[184:187],v[88:91]\nbuffer_load_dwordx4 v22,s[40:43],s45 offen offset:2048 sc1 lds\nM v[88:91],a[204:207],a[188:191],v[88:91]\nds_read_b128 a[196:199],v14 offset:50176\nC v121,v122,v123\nM v[24:27],a[232:235],v[112:115],v[24:27]\ns_waitcnt lgkmcnt(2)\nds_read_b128 a[200:203],v14 offset:51200\n" \
"C v122,v124,v125\nM v[68:71],a[208:211],a[128:131],v[32:35]\nv_exp_f32 v126,v126\nM v[68:71],a[212:215],a[132:135],v[68:71]\nbuffer_load_dwordx4 v22,s[40:43],s45 offen offset:3072 sc1 lds\nM v[68:71],a[216:219],a[136:139],v[68:71]\ns_add_u32 m0,s22,98304\nv_exp_f32 v127,v127\n" \
"M v[68:71],a[220:223],a[140:143],v[68:71]\ns_add_u32 s45,s45,s49\nds_read_b64_tr_b16 a[64:65],v6 offset:32768\nds_read_b64_tr_b16 a[66:67],v7 offset:32768\nM v[76:79],a[208:211],a[144:147],v[36:39]\nds_read_b64_tr_b16 a[68:69],v8 offset:32768\nC v123,v126,v127\nM v[76:79],a[212:215],a[148:151],v[76:79]\n" \
"ds_read_b64_tr_b16 a[70:71],v9 offset:32768\nds_read_b64_tr_b16 a[72:73],v6 offset:34816\nM v[76:79],a[216:219],a[152:155],v[76:79]\nds_read_b64_tr_b16 a[74:75],v7 offset:34816\nv_xor_b32 v13,0x10000,v13\nM v[76:79],a[220:223],a[156:159],v[76:79]\nds_read_b64_tr_b16 a[76:77],v8 offset:34816\nv_xor_b32 v6,0x10000,v6\n" \
"M v[24:27],a[236:239],v[120:123],v[24:27]\nds_read_b64_tr_b16 a[78:79],v9 offset:34816\nds_read_b128 a[204:207],v14 offset:52224\nM v[84:87],a[208:211],a[160:163],v[40:43]\nv_xor_b32 v7,0x10000,v7\nv_xor_b32 v8,0x10000,v8\nM v[84:87],a[212:215],a[164:167],v[84:87]\nv_max3_f32 v15,v24,v25,v26\n" \
"v_max_f32 v15,v15,v27\nM v[84:87],a[216:219],a[168:171],v[84:87]\nv_cmp_nge_f32 vcc,0x53800000,v15\nv_xor_b32 v9,0x10000,v9\nM v[84:87],a[220:223],a[172:175],v[84:87]\nM v[92:95],a[208:211],a[176:179],v[44:47]\nds_read_b128 a[208:211],v14 offset:53248\nM v[92:95],a[212:215],a[180:183],v[92:95]\n" \
"LG19_%=:\ns_cbranch_execz LHR2_%=\ns_cbranch_vccnz Lcs53_%=\nLcr54_%=:\nds_read_b128 a[212:215],v14 offset:54272\nM v[92:95],a[216:219],a[184:187],v[92:95]\nds_read_b128 a[216:219],v14 offset:55296\nM v[92:95],a[220:223],a[188:191],v[92:95]\n" \
"s_waitcnt lgkmcnt(3)\nds_read_b128 a[220:223],v14 offset:56320\nM v[128:131],a[192:195],v[96:99],v[128:131]\nv_exp_f32 v64,v64\nM v[160:163],a[192:195],v[104:107],v[160:163]\nv_exp_f32 v65,v65\nM v[192:195],a[192:195],v[112:115],v[192:195]\nv_exp_f32 v66,v66\n" \
"M v[224:227],a[192:195],v[120:123],v[224:227]\nv_exp_f32 v67,v67\nM v[228:231],a[196:199],v[120:123],v[228:231]\nv_exp_f32 v68,v68\nM v[196:199],a[196:199],v[112:115],v[196:199]\nv_exp_f32 v69,v69\nM v[164:167],a[196:199],v[104:107],v[164:167]\ns_waitcnt vmcnt(8) lgkmcnt(0)\n" \
"v_exp_f32 v70,v70\nM v[132:135],a[196:199],v[96:99],v[132:135]\ns_barrier\nbuffer_load_dwordx4 v22,s[40:43],s48 offen offset:0 sc1 lds\nM v[136:139],a[200:203],v[96:99],v[136:139]\nv_exp_f32 v71,v71\nM v[168:171],a[200:203],v[104:107],v[168:171]\nv_exp_f32 v72,v72\n" \
"M v[200:203],a[200:203],v[112:115],v[200:203]\nv_exp_f32 v73,v73\nM v[232:235],a[200:203],v[120:123],v[232:235]\nbuffer_load_dwordx4 v22,s[40:43],s48 offen offset:1024 sc1 lds\nv_exp_f32 v74,v74\nM v[236:239],a[204:207],v[120:123],v[236:239]\nds_read_b128 a[192:195],v13 offset:32768\nC v64,v64,v65\n" \
"M v[204:207],a[204:207],v[112:115],v[204:207]\nv_exp_f32 v75,v75\nM v[172:175],a[204:207],v[104:107],v[172:175]\nv_exp_f32 v76,v76\nM v[140:143],a[204:207],v[96:99],v[140:143]\nbuffer_load_dwordx4 v22,s[40:43],s48 offen offset:2048 sc1 lds\nv_exp_f32 v77,v77\nM v[144:147],a[208:211],v[96:99],v[144:147]\n" \
"v_exp_f32 v78,v78\nM v[176:179],a[208:211],v[104:107],v[176:179]\nds_read_b128 a[196:199],v13 offset:34816\nds_read_b128 a[200:203],v13 offset:36864\nM v[208:211],a[208:211],v[112:115],v[208:211]\nv_exp_f32 v79,v79\nM v[240:243],a[208:211],v[120:123],v[240:243]\nbuffer_load_dwordx4 v22,s[40:43],s48 offen offset:3072 sc1 lds\n" \
"v_exp_f32 v80,v80\nM v[244:247],a[212:215],v[120:123],v[244:247]\ns_add_u32 m0,s22,81920\nv_exp_f32 v81,v81\nM v[212:215],a[212:215],v[112:115],v[212:215]\ns_add_u32 s48,s48,s49\nds_read_b128 a[204:207],v13 offset:38912\nC v65,v66,v67\n" \
"M v[180:183],a[212:215],v[104:107],v[180:183]\ns_add_u32 s52,s52,1\nv_exp_f32 v82,v82\nM v[148:151],a[212:215],v[96:99],v[148:151]\nbuffer_load_dwordx4 v1,s[84:87],s46 offen offset:0 lds\nM v[152:155],a[216:219],v[96:99],v[152:155]\ns_add_u32 m0,s22,82816\nv_exp_f32 v83,v83\n" \
"M v[184:187],a[216:219],v[104:107],v[184:187]\nv_exp_f32 v84,v84\nM v[216:219],a[216:219],v[112:115],v[216:219]\nv_exp_f32 v85,v85\nM v[248:251],a[216:219],v[120:123],v[248:251]\nbuffer_load_dwordx4 v1,s[84:87],s46 offen offset:128 lds\nM v[252:255],a[220:223],v[120:123],v[252:255]\ns_add_u32 m0,s22,83968\n" \
"v_exp_f32 v86,v86\nM v[220:223],a[220:223],v[112:115],v[220:223]\ns_mov_b32 s46,983040\nv_exp_f32 v87,v87\nM v[188:191],a[220:223],v[104:107],v[188:191]\ns_cmp_eq_u32 s52,s56\nds_read_b128 a[208:211],v13 offset:33792\nC v66,v68,v69\n" \
"M v[156:159],a[220:223],v[96:99],v[156:159]\ns_waitcnt lgkmcnt(1)\nbuffer_load_dwordx4 v1,s[84:87],s46 offen offset:0 lds\nM v[96:99],a[192:195],a[128:131],v[32:35]\ns_cbranch_scc1 LEV_%=\nLEVR12_%=:\nC v67,v70,v71\nC v72,v72,v73\n" \
"M v[96:99],a[196:199],a[132:135],v[96:99]\ns_add_u32 m0,s22,84864\nds_read_b128 a[212:215],v13 offset:35840\nds_read_b128 a[216:219],v13 offset:37888\nM v[16:19],a[224:227],v[64:67],v[24:27]\nv_exp_f32 v88,v88\nM v[96:99],a[200:203],a[136:139],v[96:99]\nbuffer_load_dwordx4 v1,s[84:87],s46 offen offset:128 lds\n" \
"M v[96:99],a[204:207],a[140:143],v[96:99]\ns_add_u32 m0,s22,114688\nds_read_b128 a[220:223],v13 offset:39936\nC v73,v74,v75\nM v[104:107],a[192:195],a[144:147],v[36:39]\ns_mov_b32 s46,1048576\nC v74,v76,v77\nC v75,v78,v79\n" \
"v_exp_f32 v89,v89\nM v[104:107],a[196:199],a[148:151],v[104:107]\nv_exp_f32 v90,v90\nM v[16:19],a[228:231],v[72:75],v[16:19]\nbuffer_load_dwordx4 v22,s[40:43],s45 offen offset:0 sc1 lds\nM v[104:107],a[200:203],a[152:155],v[104:107]\nv_exp_f32 v91,v91\nM v[104:107],a[204:207],a[156:159],v[104:107]\n" \
"v_exp_f32 v92,v92\nM v[112:115],a[192:195],a[160:163],v[40:43]\nC v80,v80,v81\nC v81,v82,v83\nM v[112:115],a[196:199],a[164:167],v[112:115]\nbuffer_load_dwordx4 v22,s[40:43],s45 offen offset:1024 sc1 lds\nM v[112:115],a[200:203],a[168:171],v[112:115]\nv_exp_f32 v93,v93\n" \
"M v[112:115],a[204:207],a[172:175],v[112:115]\nC v82,v84,v85\nC v83,v86,v87\nM v[120:123],a[192:195],a[176:179],v[44:47]\nv_exp_f32 v94,v94\nM v[120:123],a[196:199],a[180:183],v[120:123]\nbuffer_load_dwordx4 v22,s[40:43],s45 offen offset:2048 sc1 lds\nM v[120:123],a[200:203],a[184:187],v[120:123]\n" \
"C v88,v88,v89\nC v89,v90,v91\nM v[120:123],a[204:207],a[188:191],v[120:123]\nds_read_b64_tr_b16 a[80:81],v6 offset:0\nv_xor_b32 v14,0x10000,v14\nM v[16:19],a[232:235],v[80:83],v[16:19]\ns_waitcnt lgkmcnt(1)\nds_read_b128 a[192:195],v14 offset:16384\n" \
"ds_read_b128 a[196:199],v14 offset:17408\nM v[100:103],a[208:211],a[128:131],v[32:35]\nbuffer_load_dwordx4 v22,s[40:43],s45 offen offset:3072 sc1 lds\nM v[100:103],a[212:215],a[132:135],v[100:103]\ns_add_u32 m0,s22,0\nv_exp_f32 v95,v95\nM v[100:103],a[216:219],a[136:139],v[100:103]\ns_add_u32 s45,s45,s49\n" \
"ds_read_b128 a[200:203],v14 offset:18432\nC v90,v92,v93\nM v[100:103],a[220:223],a[140:143],v[100:103]\nds_read_b64_tr_b16 a[82:83],v7 offset:0\nC v91,v94,v95\nM v[108:111],a[208:211],a[144:147],v[36:39]\nds_read_b64_tr_b16 a[84:85],v8 offset:0\nds_read_b64_tr_b16 a[86:87],v9 offset:0\n" \
"M v[108:111],a[212:215],a[148:151],v[108:111]\nds_read_b64_tr_b16 a[88:89],v6 offset:2048\nv_xor_b32 v13,0x10000,v13\nM v[108:111],a[216:219],a[152:155],v[108:111]\nds_read_b64_tr_b16 a[90:91],v7 offset:2048\nM v[108:111],a[220:223],a[156:159],v[108:111]\nds_read_b64_tr_b16 a[92:93],v8 offset:2048\nds_read_b64_tr_b16 a[94:95],v9 offset:2048\n" \
"M v[16:19],a[236:239],v[88:91],v[16:19]\nds_read_b128 a[204:207],v14 offset:19456\nM v[116:119],a[208:211],a[160:163],v[40:43]\nM v[116:119],a[212:215],a[164:167],v[116:119]\nv_max3_f32 v15,v16,v17,v18\nv_max_f32 v15,v15,v19\nM v[116:119],a[216:219],a[168:171],v[116:119]\nv_cmp_nge_f32 vcc,0x53800000,v15\n" \
"M v[116:119],a[220:223],a[172:175],v[116:119]\nM v[124:127],a[208:211],a[176:179],v[44:47]\nds_read_b128 a[208:211],v14 offset:20480\nM v[124:127],a[212:215],a[180:183],v[124:127]\ns_cbranch_vccnz Lcs55_%=\nLcr56_%=:\nds_read_b128 a[212:215],v14 offset:21504\nM v[124:127],a[216:219],a[184:187],v[124:127]\n" \
"ds_read_b128 a[216:219],v14 offset:22528\nM v[124:127],a[220:223],a[188:191],v[124:127]\ns_waitcnt lgkmcnt(3)\nds_read_b128 a[220:223],v14 offset:23552\nv_xor_b32 v14,0x10000,v14\nM v[128:131],a[192:195],v[64:67],v[128:131]\nv_exp_f32 v96,v96\nM v[160:163],a[192:195],v[72:75],v[160:163]\n" \
"v_exp_f32 v97,v97\nM v[192:195],a[192:195],v[80:83],v[192:195]\nv_exp_f32 v98,v98\nM v[224:227],a[192:195],v[88:91],v[224:227]\nv_exp_f32 v99,v99\nM v[228:231],a[196:199],v[88:91],v[228:231]\nv_exp_f32 v100,v100\nM v[196:199],a[196:199],v[80:83],v[196:199]\n" \
"v_exp_f32 v101,v101\nM v[164:167],a[196:199],v[72:75],v[164:167]\ns_waitcnt vmcnt(8) lgkmcnt(0)\nv_exp_f32 v102,v102\nM v[132:135],a[196:199],v[64:67],v[132:135]\ns_barrier\nbuffer_load_dwordx4 v22,s[40:43],s48 offen offset:0 sc1 lds\nM v[136:139],a[200:203],v[64:67],v[136:139]\n" \
"v_exp_f32 v103,v103\nM v[168:171],a[200:203],v[72:75],v[168:171]\nv_exp_f32 v104,v104\nM v[200:203],a[200:203],v[80:83],v[200:203]\nv_exp_f32 v105,v105\nM v[232:235],a[200:203],v[88:91],v[232:235]\nbuffer_load_dwordx4 v22,s[40:43],s48 offen offset:1024 sc1 lds\nM v[236:239],a[204:207],v[88:91],v[236:239]\n" \
"ds_read_b128 a[192:195],v13 offset:0\nC v96,v96,v97\nM v[204:207],a[204:207],v[80:83],v[204:207]\nv_exp_f32 v106,v106\nM v[172:175],a[204:207],v[72:75],v[172:175]\nv_exp_f32 v107,v107\nM v[140:143],a[204:207],v[64:67],v[140:143]\nbuffer_load_dwordx4 v22,s[40:43],s48 offen offset:2048 sc1 lds\n" \
"v_exp_f32 v108,v108\nM v[144:147],a[208:211],v[64:67],v[144:147]\nv_exp_f32 v109,v109\nM v[176:179],a[208:211],v[72:75],v[176:179]\nds_read_b128 a[196:199],v13 offset:2048\nds_read_b128 a[200:203],v13 offset:4096\nM v[208:211],a[208:211],v[80:83],v[208:211]\nv_exp_f32 v110,v110\n" \
"M v[240:243],a[208:211],v[88:91],v[240:243]\nbuffer_load_dwordx4 v22,s[40:43],s48 offen offset:3072 sc1 lds\nv_exp_f32 v111,v111\nM v[244:247],a[212:215],v[88:91],v[244:247]\ns_add_u32 m0,s22,32768\nv_exp_f32 v112,v112\nM v[212:215],a[212:215],v[80:83],v[212:215]\ns_add_u32 s48,s48,s49\n" \
"ds_read_b128 a[204:207],v13 offset:6144\nC v97,v98,v99\nM v[180:183],a[212:215],v[72:75],v[180:183]\nv_exp_f32 v113,v113\nM v[148:151],a[212:215],v[64:67],v[148:151]\nbuffer_load_dwordx4 v22,s[40:43],s45 offen offset:0 sc1 lds\nM v[152:155],a[216:219],v[64:67],v[152:155]\nv_exp_f32 v114,v114\n" \
"M v[184:187],a[216:219],v[72:75],v[184:187]\nv_exp_f32 v115,v115\nM v[216:219],a[216:219],v[80:83],v[216:219]\nv_exp_f32 v116,v116\nM v[248:251],a[216:219],v[88:91],v[248:251]\nbuffer_load_dwordx4 v22,s[40:43],s45 offen offset:1024 sc1 lds\nM v[252:255],a[220:223],v[88:91],v[252:255]\nv_exp_f32 v117,v117\n" \
"M v[220:223],a[220:223],v[80:83],v[220:223]\nC v98,v100,v101\nC v99,v102,v103\nM v[188:191],a[220:223],v[72:75],v[188:191]\ns_waitcnt lgkmcnt(0)\nv_exp_f32 v118,v118\nM v[156:159],a[220:223],v[64:67],v[156:159]\nbuffer_load_dwordx4 v22,s[40:43],s45 offen offset:2048 sc1 lds\n" \
"M v[64:67],a[192:195],a[128:131],v[32:35]\nds_read_b128 a[208:211],v13 offset:1024\nC v104,v104,v105\nM v[64:67],a[196:199],a[132:135],v[64:67]\nds_read_b128 a[212:215],v13 offset:3072\nC v105,v106,v107\nM v[24:27],a[224:227],v[96:99],v[16:19]\nds_read_b128 a[216:219],v13 offset:5120\n" \
"C v106,v108,v109\nM v[64:67],a[200:203],a[136:139],v[64:67]\nbuffer_load_dwordx2 v[10:11],v5,s[36:39],0 offen sc1\nM v[64:67],a[204:207],a[140:143],v[64:67]\nds_read_b128 a[220:223],v13 offset:7168\nC v107,v110,v111\nM v[72:75],a[192:195],a[144:147],v[36:39]\nbuffer_load_dwordx4 v22,s[40:43],s45 offen offset:3072 sc1 lds\n" \
"M v[72:75],a[196:199],a[148:151],v[72:75]\ns_add_u32 m0,s22,16384\nv_exp_f32 v119,v119\nM v[24:27],a[228:231],v[104:107],v[24:27]\ns_add_u32 s45,s45,s49\nv_exp_f32 v120,v120\nM v[72:75],a[200:203],a[152:155],v[72:75]\nv_exp_f32 v121,v121\n" \
"M v[72:75],a[204:207],a[156:159],v[72:75]\nbuffer_load_dwordx4 v22,s[40:43],s48 offen offset:0 sc1 lds\nM v[80:83],a[192:195],a[160:163],v[40:43]\nv_exp_f32 v122,v122\nM v[80:83],a[196:199],a[164:167],v[80:83]\nv_exp_f32 v123,v123\nM v[80:83],a[200:203],a[168:171],v[80:83]\nC v112,v112,v113\n" \
"C v113,v114,v115\nM v[80:83],a[204:207],a[172:175],v[80:83]\nbuffer_load_dwordx4 v22,s[40:43],s48 offen offset:1024 sc1 lds\nM v[88:91],a[192:195],a[176:179],v[44:47]\nds_read_b128 a[192:195],v14 offset:32768\nC v114,v116,v117\nM v[88:91],a[196:199],a[180:183],v[88:91]\nds_read_b128 a[196:199],v14 offset:33792\n" \
"C v115,v118,v119\nM v[88:91],a[200:203],a[184:187],v[88:91]\nv_exp_f32 v124,v124\nM v[88:91],a[204:207],a[188:191],v[88:91]\nbuffer_load_dwordx4 v22,s[40:43],s48 offen offset:2048 sc1 lds\nM v[24:27],a[232:235],v[112:115],v[24:27]\ns_waitcnt lgkmcnt(2)\nds_read_b64_tr_b16 a[96:97],v6 offset:49152\n" \
"C v120,v120,v121\nM v[68:71],a[208:211],a[128:131],v[32:35]\nv_exp_f32 v125,v125\nM v[68:71],a[212:215],a[132:135],v[68:71]\nds_read_b128 a[200:203],v14 offset:34816\nds_read_b64_tr_b16 a[98:99],v7 offset:49152\nM v[68:71],a[216:219],a[136:139],v[68:71]\nbuffer_load_dwordx4 v22,s[40:43],s48 offen offset:3072 sc1 lds\n" \
"M v[68:71],a[220:223],a[140:143],v[68:71]\ns_add_u32 m0,s22,49152\nv_exp_f32 v126,v126\nM v[76:79],a[208:211],a[144:147],v[36:39]\ns_add_u32 s48,s48,s49\nds_read_b64_tr_b16 a[100:101],v8 offset:49152\nds_read_b64_tr_b16 a[102:103],v9 offset:49152\nM v[76:79],a[212:215],a[148:151],v[76:79]\n" \
"v_exp_f32 v127,v127\nM v[76:79],a[216:219],a[152:155],v[76:79]\nC v121,v122,v123\nC v122,v124,v125\nC v123,v126,v127\nM v[76:79],a[220:223],a[156:159],v[76:79]\nds_read_b64_tr_b16 a[104:105],v6 offset:51200\nds_read_b64_tr_b16 a[106:107],v7 offset:51200\n" \
"M v[24:27],a[236:239],v[120:123],v[24:27]\nds_read_b64_tr_b16 a[108:109],v8 offset:51200\nv_xor_b32 v6,0x10000,v6\nM v[84:87],a[208:211],a[160:163],v[40:43]\nds_read_b64_tr_b16 a[110:111],v9 offset:51200\nds_read_b128 a[204:207],v14 offset:35840\nM v[84:87],a[212:215],a[164:167],v[84:87]\nv_max3_f32 v15,v24,v25,v26\n" \
"v_max_f32 v15,v15,v27\nM v[84:87],a[216:219],a[168:171],v[84:87]\nv_cmp_nge_f32 vcc,0x53800000,v15\nv_xor_b32 v7,0x10000,v7\nM v[84:87],a[220:223],a[172:175],v[84:87]\nv_xor_b32 v8,0x10000,v8\nv_xor_b32 v9,0x10000,v9\nM v[92:95],a[208:211],a[176:179],v[44:47]\n" \
"ds_read_b128 a[208:211],v14 offset:36864\nM v[92:95],a[212:215],a[180:183],v[92:95]\ns_cbranch_vccnz Lcs57_%=\nLcr58_%=:\nds_read_b128 a[212:215],v14 offset:37888\nM v[92:95],a[216:219],a[184:187],v[92:95]\nds_read_b128 a[216:219],v14 offset:38912\nM v[92:95],a[220:223],a[188:191],v[92:95]\n" \
"s_waitcnt lgkmcnt(3)\nds_read_b128 a[220:223],v14 offset:39936\nM v[128:131],a[192:195],v[96:99],v[128:131]\nv_exp_f32 v64,v64\nM v[160:163],a[192:195],v[104:107],v[160:163]\nv_exp_f32 v65,v65\nM v[192:195],a[192:195],v[112:115],v[192:195]\nv_exp_f32 v66,v66\n" \
"M v[224:227],a[192:195],v[120:123],v[224:227]\nv_exp_f32 v67,v67\nM v[228:231],a[196:199],v[120:123],v[228:231]\nv_exp_f32 v68,v68\nM v[196:199],a[196:199],v[112:115],v[196:199]\nv_exp_f32 v69,v69\nM v[164:167],a[196:199],v[104:107],v[164:167]\ns_waitcnt vmcnt(9) lgkmcnt(0)\n" \
"v_exp_f32 v70,v70\nM v[132:135],a[196:199],v[96:99],v[132:135]\ns_barrier\nds_read_b128 a[192:195],v13 offset:49152\nM v[136:139],a[200:203],v[96:99],v[136:139]\nv_exp_f32 v71,v71\nM v[168:171],a[200:203],v[104:107],v[168:171]\nv_exp_f32 v72,v72\n" \
"M v[200:203],a[200:203],v[112:115],v[200:203]\nv_exp_f32 v73,v73\nM v[232:235],a[200:203],v[120:123],v[232:235]\nbuffer_load_dwordx4 v22,s[40:43],s45 offen offset:0 sc1 lds\nM v[236:239],a[204:207],v[120:123],v[236:239]\nv_exp_f32 v74,v74\nM v[204:207],a[204:207],v[112:115],v[204:207]\nv_exp_f32 v75,v75\n" \
"M v[172:175],a[204:207],v[104:107],v[172:175]\nv_exp_f32 v76,v76\nM v[140:143],a[204:207],v[96:99],v[140:143]\nbuffer_load_dwordx4 v22,s[40:43],s45 offen offset:1024 sc1 lds\nM v[144:147],a[208:211],v[96:99],v[144:147]\nv_exp_f32 v77,v77\nM v[176:179],a[208:211],v[104:107],v[176:179]\ns_waitcnt vmcnt(7)\n" \
"v_cmp_gt_u64 s[98:99],s[14:15],v[10:11]\nv_cmp_le_u64 s[18:19],s[16:17],v[10:11]\nv_exp_f32 v78,v78\nM v[208:211],a[208:211],v[112:115],v[208:211]\ns_or_b64 s[98:99],s[98:99],s[18:19]\ns_and_b32 s98,s98,0xffff\ns_cbranch_scc1 LPE_%=\nLPER_%=:\n" \
"v_exp_f32 v79,v79\nM v[240:243],a[208:211],v[120:123],v[240:243]\nbuffer_load_dwordx4 v22,s[40:43],s45 offen offset:2048 sc1 lds\nM v[244:247],a[212:215],v[120:123],v[244:247]\nds_read_b128 a[196:199],v13 offset:51200\nC v64,v64,v65\nM v[212:215],a[212:215],v[112:115],v[212:215]\nv_exp_f32 v80,v80\n" \
"M v[180:183],a[212:215],v[104:107],v[180:183]\ns_add_u32 s52,s52,1\nv_exp_f32 v81,v81\nM v[148:151],a[212:215],v[96:99],v[148:151]\nbuffer_load_dwordx4 v22,s[40:43],s45 offen offset:3072 sc1 lds\nM v[152:155],a[216:219],v[96:99],v[152:155]\ns_add_u32 m0,s22,65536\nds_read_b128 a[200:203],v13 offset:53248\n" \
"C v65,v66,v67\nv_exp_f32 v82,v82\nM v[184:187],a[216:219],v[104:107],v[184:187]\nds_read_b128 a[204:207],v13 offset:55296\nC v66,v68,v69\nM v[216:219],a[216:219],v[112:115],v[216:219]\nv_exp_f32 v83,v83\nM v[248:251],a[216:219],v[120:123],v[248:251]\n" \
"buffer_load_dwordx4 v22,s[40:43],s48 offen offset:0 sc1 lds\nM v[252:255],a[220:223],v[120:123],v[252:255]\nv_exp_f32 v84,v84\nM v[220:223],a[220:223],v[112:115],v[220:223]\ns_waitcnt lgkmcnt(2)\nv_exp_f32 v85,v85\nM v[188:191],a[220:223],v[104:107],v[188:191]\ns_cmp_eq_u32 s52,s56\n" \
"C v72,v72,v73\nC v67,v70,v71\nM v[156:159],a[220:223],v[96:99],v[156:159]\nbuffer_load_dwordx4 v22,s[40:43],s48 offen offset:1024 sc1 lds\nM v[96:99],a[192:195],a[128:131],v[32:35]\ns_cbranch_scc1 LEV_%=\nLEVR13_%=:\nds_read_b128 a[208:211],v13 offset:50176\n" \
"C v73,v74,v75\nM v[96:99],a[196:199],a[132:135],v[96:99]\nds_read_b128 a[212:215],v13 offset:52224\nC v74,v76,v77\nM v[16:19],a[224:227],v[64:67],v[24:27]\ns_waitcnt lgkmcnt(2)\nds_read_b128 a[216:219],v13 offset:54272\nC v75,v78,v79\n" \
"M v[96:99],a[200:203],a[136:139],v[96:99]\nbuffer_load_dwordx4 v22,s[40:43],s48 offen offset:2048 sc1 lds\nv_exp_f32 v86,v86\nM v[96:99],a[204:207],a[140:143],v[96:99]\nds_read_b128 a[220:223],v13 offset:56320\nC v80,v80,v81\nM v[104:107],a[192:195],a[144:147],v[36:39]\nv_exp_f32 v87,v87\n" \
"M v[104:107],a[196:199],a[148:151],v[104:107]\nv_exp_f32 v88,v88\nM v[16:19],a[228:231],v[72:75],v[16:19]\nbuffer_load_dwordx4 v22,s[40:43],s48 offen offset:3072 sc1 lds\nM v[104:107],a[200:203],a[152:155],v[104:107]\ns_add_u32 m0,s22,98304\nv_exp_f32 v89,v89\nM v[104:107],a[204:207],a[156:159],v[104:107]\n" \
"s_add_u32 s48,s48,s49\nv_exp_f32 v90,v90\nM v[112:115],a[192:195],a[160:163],v[40:43]\nC v81,v82,v83\nC v82,v84,v85\nM v[112:115],a[196:199],a[164:167],v[112:115]\nbuffer_load_dwordx4 v1,s[84:87],s46 offen offset:0 lds\nM v[112:115],a[200:203],a[168:171],v[112:115]\n" \
"s_add_u32 m0,s22,99200\nv_exp_f32 v91,v91\nM v[112:115],a[204:207],a[172:175],v[112:115]\nC v83,v86,v87\nC v88,v88,v89\nM v[120:123],a[192:195],a[176:179],v[44:47]\nv_exp_f32 v92,v92\nM v[120:123],a[196:199],a[180:183],v[120:123]\n" \
"buffer_load_dwordx4 v1,s[84:87],s46 offen offset:128 lds\nM v[120:123],a[200:203],a[184:187],v[120:123]\ns_add_u32 m0,s22,100352\nv_exp_f32 v93,v93\nM v[120:123],a[204:207],a[188:191],v[120:123]\ns_mov_b32 s46,1114112\nv_exp_f32 v94,v94\nM v[16:19],a[232:235],v[80:83],v[16:19]\n" \
"s_waitcnt lgkmcnt(0)\nC v89,v90,v91\nC v90,v92,v93\nM v[100:103],a[208:211],a[128:131],v[32:35]\nbuffer_load_dwordx4 v1,s[84:87],s46 offen offset:0 lds\nM v[100:103],a[212:215],a[132:135],v[100:103]\ns_add_u32 m0,s22,101248\nds_read_b64_tr_b16 a[112:113],v6 offset:16384\n" \
"v_xor_b32 v14,0x10000,v14\nM v[100:103],a[216:219],a[136:139],v[100:103]\nds_read_b128 a[192:195],v14 offset:0\nM v[100:103],a[220:223],a[140:143],v[100:103]\nds_read_b128 a[196:199],v14 offset:1024\nM v[108:111],a[208:211],a[144:147],v[36:39]\nbuffer_load_dwordx4 v1,s[84:87],s46 offen offset:128 lds\nM v[108:111],a[212:215],a[148:151],v[108:111]\n" \
"s_add_u32 m0,s22,81920\nv_exp_f32 v95,v95\nM v[108:111],a[216:219],a[152:155],v[108:111]\ns_mov_b32 s46,1179648\nds_read_b128 a[200:203],v14 offset:2048\nC v91,v94,v95\nM v[108:111],a[220:223],a[156:159],v[108:111]\nds_read_b64_tr_b16 a[114:115],v7 offset:16384\n" \
"ds_read_b64_tr_b16 a[116:117],v8 offset:16384\nM v[16:19],a[236:239],v[88:91],v[16:19]\nds_read_b64_tr_b16 a[118:119],v9 offset:16384\nM v[116:119],a[208:211],a[160:163],v[40:43]\nds_read_b64_tr_b16 a[120:121],v6 offset:18432\nds_read_b64_tr_b16 a[122:123],v7 offset:18432\nM v[116:119],a[212:215],a[164:167],v[116:119]\nds_read_b64_tr_b16 a[124:125],v8 offset:18432\n" \
"v_max3_f32 v15,v16,v17,v18\nM v[116:119],a[216:219],a[168:171],v[116:119]\nds_read_b64_tr_b16 a[126:127],v9 offset:18432\nv_max_f32 v15,v15,v19\nM v[116:119],a[220:223],a[172:175],v[116:119]\nds_read_b128 a[204:207],v14 offset:3072\nv_cmp_nge_f32 vcc,0x53800000,v15\nM v[124:127],a[208:211],a[176:179],v[44:47]\n" \
"s_waitcnt lgkmcnt(6)\nds_read_b128 a[208:211],v14 offset:4096\nM v[124:127],a[212:215],a[180:183],v[124:127]\ns_cbranch_vccnz Lcs59_%=\nLcr60_%=:\nds_read_b128 a[212:215],v14 offset:5120\nM v[124:127],a[216:219],a[184:187],v[124:127]\ns_cmp_eq_u32 s52,s55\n" \
"ds_read_b128 a[216:219],v14 offset:6144\nM v[124:127],a[220:223],a[188:191],v[124:127]\ns_cbranch_scc1 LH1_%=\nLH1R14_%=:\nds_read_b128 a[220:223],v14 offset:7168\nv_xor_b32 v13,0x10000,v13\nM v[128:131],a[192:195],v[64:67],v[128:131]\nv_exp_f32 v96,v96\n" \
"M v[160:163],a[192:195],v[72:75],v[160:163]\nv_exp_f32 v97,v97\nM v[192:195],a[192:195],v[80:83],v[192:195]\nv_exp_f32 v98,v98\nM v[224:227],a[192:195],v[88:91],v[224:227]\nv_exp_f32 v99,v99\nM v[228:231],a[196:199],v[88:91],v[228:231]\nv_exp_f32 v100,v100\n" \
"M v[196:199],a[196:199],v[80:83],v[196:199]\nv_exp_f32 v101,v101\nM v[164:167],a[196:199],v[72:75],v[164:167]\ns_waitcnt vmcnt(12) lgkmcnt(0)\nv_exp_f32 v102,v102\nM v[132:135],a[196:199],v[64:67],v[132:135]\ns_barrier\nds_read_b128 a[192:195],v13 offset:32768\n" \
"M v[136:139],a[200:203],v[64:67],v[136:139]\nbuffer_load_dwordx4 v1,s[84:87],s46 offen offset:0 lds\nM v[168:171],a[200:203],v[72:75],v[168:171]\ns_add_u32 m0,s22,82816\nv_exp_f32 v103,v103\nM v[200:203],a[200:203],v[80:83],v[200:203]\nv_exp_f32 v104,v104\nM v[232:235],a[200:203],v[88:91],v[232:235]\n" \
"v_exp_f32 v105,v105\nM v[236:239],a[204:207],v[88:91],v[236:239]\nbuffer_load_dwordx4 v1,s[84:87],s46 offen offset:128 lds\nM v[204:207],a[204:207],v[80:83],v[204:207]\ns_mov_b32 s46,1245184\nds_read_b128 a[196:199],v13 offset:34816\nC v96,v96,v97\nM v[172:175],a[204:207],v[72:75],v[172:175]\n" \
"s_add_u32 m0,s22,83968\nv_exp_f32 v106,v106\nM v[140:143],a[204:207],v[64:67],v[140:143]\nds_read_b128 a[200:203],v13 offset:36864\nds_read_b128 a[204:207],v13 offset:38912\nM v[144:147],a[208:211],v[64:67],v[144:147]\nbuffer_load_dwordx4 v1,s[84:87],s46 offen offset:0 lds\nM v[176:179],a[208:211],v[72:75],v[176:179]\n" \
"s_add_u32 m0,s22,84864\nv_exp_f32 v107,v107\nM v[208:211],a[208:211],v[80:83],v[208:211]\nv_exp_f32 v108,v108\nM v[240:243],a[208:211],v[88:91],v[240:243]\nLG20_%=:\ns_cbranch_execz LG22_%=\nv_exp_f32 v109,v109\n" \
"M v[244:247],a[212:215],v[88:91],v[244:247]\nbuffer_load_dwordx4 v1,s[84:87],s46 offen offset:128 lds\nM v[212:215],a[212:215],v[80:83],v[212:215]\ns_add_u32 m0,s22,114688\nv_exp_f32 v110,v110\nM v[180:183],a[212:215],v[72:75],v[180:183]\ns_mov_b32 s46,1310720\nv_exp_f32 v111,v111\n" \
"M v[148:151],a[212:215],v[64:67],v[148:151]\nv_exp_f32 v112,v112\nM v[152:155],a[216:219],v[64:67],v[152:155]\nbuffer_load_dwordx4 v22,s[40:43],s48 offen offset:0 sc1 lds\nM v[184:187],a[216:219],v[72:75],v[184:187]\nC v97,v98,v99\nC v98,v100,v101\nM v[216:219],a[216:219],v[80:83],v[216:219]\n" \
"v_exp_f32 v113,v113\nM v[248:251],a[216:219],v[88:91],v[248:251]\nC v99,v102,v103\nC v104,v104,v105\nM v[252:255],a[220:223],v[88:91],v[252:255]\nbuffer_load_dwordx4 v22,s[40:43],s48 offen offset:1024 sc1 lds\nM v[220:223],a[220:223],v[80:83],v[220:223]\nv_exp_f32 v114,v114\n" \
"M v[188:191],a[220:223],v[72:75],v[188:191]\nC v105,v106,v107\nC v106,v108,v109\nM v[156:159],a[220:223],v[64:67],v[156:159]\ns_waitcnt lgkmcnt(0)\nv_exp_f32 v115,v115\nM v[64:67],a[192:195],a[128:131],v[32:35]\nbuffer_load_dwordx4 v22,s[40:43],s48 offen offset:2048 sc1 lds\n" \
"M v[64:67],a[196:199],a[132:135],v[64:67]\nds_read_b128 a[208:211],v13 offset:33792\nC v107,v110,v111\nM v[24:27],a[224:227],v[96:99],v[16:19]\nds_read_b128 a[212:215],v13 offset:35840\nC v112,v112,v113\nM v[64:67],a[200:203],a[136:139],v[64:67]\nds_read_b128 a[216:219],v13 offset:37888\n" \
"C v113,v114,v115\nM v[64:67],a[204:207],a[140:143],v[64:67]\nbuffer_load_dwordx4 v22,s[40:43],s48 offen offset:3072 sc1 lds\nM v[72:75],a[192:195],a[144:147],v[36:39]\ns_add_u32 m0,s22,0\nds_read_b128 a[220:223],v13 offset:39936\nM v[72:75],a[196:199],a[148:151],v[72:75]\nv_exp_f32 v116,v116\n" \
"M v[24:27],a[228:231],v[104:107],v[24:27]\nv_exp_f32 v117,v117\nM v[72:75],a[200:203],a[152:155],v[72:75]\nbuffer_load_dwordx4 v2,s[36:39],s47 offen offset:0 sc1 lds\nM v[72:75],a[204:207],a[156:159],v[72:75]\nv_exp_f32 v118,v118\nM v[80:83],a[192:195],a[160:163],v[40:43]\nv_exp_f32 v119,v119\n" \
"M v[80:83],a[196:199],a[164:167],v[80:83]\nv_exp_f32 v120,v120\nM v[80:83],a[200:203],a[168:171],v[80:83]\nbuffer_load_dwordx4 v2,s[36:39],s47 offen offset:1024 sc1 lds\nM v[80:83],a[204:207],a[172:175],v[80:83]\nv_exp_f32 v121,v121\nM v[88:91],a[192:195],a[176:179],v[44:47]\nC v114,v116,v117\n" \
"C v115,v118,v119\nM v[88:91],a[196:199],a[180:183],v[88:91]\nv_exp_f32 v122,v122\nM v[88:91],a[200:203],a[184:187],v[88:91]\nbuffer_load_dwordx4 v2,s[36:39],s47 offen offset:2048 sc1 lds\nM v[88:91],a[204:207],a[188:191],v[88:91]\nds_read_b128 a[192:195],v14 offset:16384\nC v120,v120,v121\n" \
"M v[24:27],a[232:235],v[112:115],v[24:27]\ns_waitcnt lgkmcnt(1)\nds_read_b128 a[196:199],v14 offset:17408\nM v[68:71],a[208:211],a[128:131],v[32:35]\nv_exp_f32 v123,v123\nM v[68:71],a[212:215],a[132:135],v[68:71]\nbuffer_load_dwordx4 v2,s[36:39],s47 offen offset:3072 sc1 lds\nM v[68:71],a[216:219],a[136:139],v[68:71]\n" \
"s_add_u32 m0,s22,32768\nv_exp_f32 v124,v124\nM v[68:71],a[220:223],a[140:143],v[68:71]\ns_add_u32 s47,s28,8192\nv_exp_f32 v125,v125\nM v[76:79],a[208:211],a[144:147],v[36:39]\nv_exp_f32 v126,v126\nM v[76:79],a[212:215],a[148:151],v[76:79]\n" \
"v_exp_f32 v127,v127\nM v[76:79],a[216:219],a[152:155],v[76:79]\nC v121,v122,v123\nC v122,v124,v125\nC v123,v126,v127\nM v[76:79],a[220:223],a[156:159],v[76:79]\nds_read_b128 a[200:203],v14 offset:18432\nds_read_b128 a[204:207],v14 offset:19456\n" \
"M v[24:27],a[236:239],v[120:123],v[24:27]\nM v[84:87],a[208:211],a[160:163],v[40:43]\nM v[84:87],a[212:215],a[164:167],v[84:87]\nv_max3_f32 v15,v24,v25,v26\nv_max_f32 v15,v15,v27\nM v[84:87],a[216:219],a[168:171],v[84:87]\nv_cmp_nge_f32 vcc,0x53800000,v15\nM v[84:87],a[220:223],a[172:175],v[84:87]\n" \
"M v[92:95],a[208:211],a[176:179],v[44:47]\nds_read_b128 a[208:211],v14 offset:20480\nM v[92:95],a[212:215],a[180:183],v[92:95]\ns_cbranch_vccnz Lcs61_%=\nLcr62_%=:\nds_read_b128 a[212:215],v14 offset:21504\nM v[92:95],a[216:219],a[184:187],v[92:95]\nds_read_b128 a[216:219],v14 offset:22528\n" \
"M v[92:95],a[220:223],a[188:191],v[92:95]\ns_waitcnt lgkmcnt(3)\nds_read_b128 a[220:223],v14 offset:23552\nM v[128:131],a[192:195],v[96:99],v[128:131]\nv_exp_f32 v64,v64\nM v[160:163],a[192:195],v[104:107],v[160:163]\nv_exp_f32 v65,v65\nM v[192:195],a[192:195],v[112:115],v[192:195]\n" \
"v_exp_f32 v66,v66\nM v[224:227],a[192:195],v[120:123],v[224:227]\nv_exp_f32 v67,v67\nM v[228:231],a[196:199],v[120:123],v[228:231]\nv_exp_f32 v68,v68\nM v[196:199],a[196:199],v[112:115],v[196:199]\nv_exp_f32 v69,v69\nM v[164:167],a[196:199],v[104:107],v[164:167]\n" \
"s_waitcnt vmcnt(16) lgkmcnt(0)\nv_exp_f32 v70,v70\nM v[132:135],a[196:199],v[96:99],v[132:135]\ns_barrier\nds_read_b128 a[192:195],v13 offset:49152\nM v[136:139],a[200:203],v[96:99],v[136:139]\nbuffer_load_dwordx4 v1,s[84:87],s46 offen offset:0 lds\nM v[168:171],a[200:203],v[104:107],v[168:171]\n" \
"s_add_u32 m0,s22,33664\nv_exp_f32 v71,v71\nM v[200:203],a[200:203],v[112:115],v[200:203]\nv_exp_f32 v72,v72\nM v[232:235],a[200:203],v[120:123],v[232:235]\nv_exp_f32 v73,v73\nM v[236:239],a[204:207],v[120:123],v[236:239]\nbuffer_load_dwordx4 v1,s[84:87],s46 offen offset:128 lds\n" \
"M v[204:207],a[204:207],v[112:115],v[204:207]\ns_add_u32 m0,s22,34816\nds_read_b128 a[196:199],v13 offset:51200\nC v64,v64,v65\nM v[172:175],a[204:207],v[104:107],v[172:175]\ns_mov_b32 s46,1376256\nv_exp_f32 v74,v74\nM v[140:143],a[204:207],v[96:99],v[140:143]\n" \
"ds_read_b128 a[200:203],v13 offset:53248\nds_read_b128 a[204:207],v13 offset:55296\nM v[144:147],a[208:211],v[96:99],v[144:147]\nbuffer_load_dwordx4 v1,s[84:87],s46 offen offset:0 lds\nM v[176:179],a[208:211],v[104:107],v[176:179]\ns_add_u32 m0,s22,35712\nv_exp_f32 v75,v75\nM v[208:211],a[208:211],v[112:115],v[208:211]\n" \
"v_exp_f32 v76,v76\nM v[240:243],a[208:211],v[120:123],v[240:243]\nv_exp_f32 v77,v77\nM v[244:247],a[212:215],v[120:123],v[244:247]\nbuffer_load_dwordx4 v1,s[84:87],s46 offen offset:128 lds\nM v[212:215],a[212:215],v[112:115],v[212:215]\ns_add_u32 m0,s22,16384\nv_exp_f32 v78,v78\n" \
"M v[180:183],a[212:215],v[104:107],v[180:183]\ns_add_u32 s52,s52,1\nv_exp_f32 v79,v79\nM v[148:151],a[212:215],v[96:99],v[148:151]\ns_mov_b32 s46,1441792\nv_exp_f32 v80,v80\nM v[152:155],a[216:219],v[96:99],v[152:155]\nbuffer_load_dwordx4 v1,s[84:87],s46 offen offset:0 lds\n" \
"M v[184:187],a[216:219],v[104:107],v[184:187]\ns_add_u32 m0,s22,17280\nv_exp_f32 v81,v81\nM v[216:219],a[216:219],v[112:115],v[216:219]\nv_exp_f32 v82,v82\nM v[248:251],a[216:219],v[120:123],v[248:251]\ns_waitcnt lgkmcnt(0)\nC v65,v66,v67\n" \
"C v66,v68,v69\nM v[252:255],a[220:223],v[120:123],v[252:255]\nbuffer_load_dwordx4 v1,s[84:87],s46 offen offset:128 lds\nM v[220:223],a[220:223],v[112:115],v[220:223]\ns_add_u32 m0,s22,18432\nv_exp_f32 v83,v83\nM v[188:191],a[220:223],v[104:107],v[188:191]\ns_cmp_eq_u32 s52,s56\n" \
"C v67,v70,v71\nC v72,v72,v73\nM v[156:159],a[220:223],v[96:99],v[156:159]\ns_cbranch_scc1 LEV_%=\nLEVR14_%=:\nv_exp_f32 v84,v84\nM v[96:99],a[192:195],a[128:131],v[32:35]\nbuffer_load_dwordx2 v[10:11],v5,s[36:39],0 offen sc1\n" \
"M v[96:99],a[196:199],a[132:135],v[96:99]\ns_mov_b32 s46,1507328\nds_read_b128 a[208:211],v13 offset:50176\nds_read_b128 a[212:215],v13 offset:52224\nM v[16:19],a[224:227],v[64:67],v[24:27]\nbuffer_load_dwordx4 v1,s[84:87],s46 offen offset:0 lds\nM v[96:99],a[200:203],a[136:139],v[96:99]\ns_add_u32 m0,s22,19328\n" \
"C v73,v74,v75\nC v74,v76,v77\nM v[96:99],a[204:207],a[140:143],v[96:99]\nds_read_b128 a[216:219],v13 offset:54272\nC v75,v78,v79\nM v[104:107],a[192:195],a[144:147],v[36:39]\nds_read_b128 a[220:223],v13 offset:56320\nC v80,v80,v81\n" \
"M v[104:107],a[196:199],a[148:151],v[104:107]\nv_exp_f32 v85,v85\nM v[16:19],a[228:231],v[72:75],v[16:19]\nbuffer_load_dwordx4 v1,s[84:87],s46 offen offset:128 lds\nM v[104:107],a[200:203],a[152:155],v[104:107]\ns_add_u32 m0,s22,49152\nv_exp_f32 v86,v86\nM v[104:107],a[204:207],a[156:159],v[104:107]\n" \
"s_mov_b32 s46,1966080\nv_exp_f32 v87,v87\nM v[112:115],a[192:195],a[160:163],v[40:43]\nv_exp_f32 v88,v88\nM v[112:115],a[196:199],a[164:167],v[112:115]\nC v81,v82,v83\nC v82,v84,v85\nM v[112:115],a[200:203],a[168:171],v[112:115]\n" \
"v_exp_f32 v89,v89\nM v[112:115],a[204:207],a[172:175],v[112:115]\nC v83,v86,v87\nC v88,v88,v89\nM v[120:123],a[192:195],a[176:179],v[44:47]\nv_exp_f32 v90,v90\nM v[120:123],a[196:199],a[180:183],v[120:123]\nv_exp_f32 v91,v91\n" \
"M v[120:123],a[200:203],a[184:187],v[120:123]\nv_exp_f32 v92,v92\nM v[120:123],a[204:207],a[188:191],v[120:123]\nv_exp_f32 v93,v93\nM v[16:19],a[232:235],v[80:83],v[16:19]\ns_waitcnt lgkmcnt(0)\nv_exp_f32 v94,v94\nM v[100:103],a[208:211],a[128:131],v[32:35]\n" \
"C v89,v90,v91\nC v90,v92,v93\nM v[100:103],a[212:215],a[132:135],v[100:103]\nv_exp_f32 v95,v95\nM v[100:103],a[216:219],a[136:139],v[100:103]\nv_xor_b32 v14,0x10000,v14\nC v91,v94,v95\nM v[100:103],a[220:223],a[140:143],v[100:103]\n" \
"ds_read_b128 a[192:195],v14 offset:0\nds_read_b128 a[196:199],v14 offset:1024\nM v[108:111],a[208:211],a[144:147],v[36:39]\nds_read_b128 a[200:203],v14 offset:2048\nv_add_u32 v20,0x10000,v4\nM v[108:111],a[212:215],a[148:151],v[108:111]\nds_read_b128 a[204:207],v14 offset:3072\nv_add_u32 v23,0x20000,v4\n" \
"M v[108:111],a[216:219],a[152:155],v[108:111]\nM v[108:111],a[220:223],a[156:159],v[108:111]\nM v[16:19],a[236:239],v[88:91],v[16:19]\nM v[116:119],a[208:211],a[160:163],v[40:43]\nM v[116:119],a[212:215],a[164:167],v[116:119]\nv_max3_f32 v15,v16,v17,v18\nv_max_f32 v15,v15,v19\nM v[116:119],a[216:219],a[168:171],v[116:119]\n" \
"v_cmp_nge_f32 vcc,0x53800000,v15\nM v[116:119],a[220:223],a[172:175],v[116:119]\nM v[124:127],a[208:211],a[176:179],v[44:47]\nds_read_b128 a[208:211],v14 offset:4096\nM v[124:127],a[212:215],a[180:183],v[124:127]\ns_cbranch_vccnz Lcs63_%=\nLcr64_%=:\nds_read_b128 a[212:215],v14 offset:5120\n" \
"v_max3_f32 v60,v96,v97,v98\nM v[124:127],a[216:219],a[184:187],v[124:127]\ns_waitcnt lgkmcnt(2)\nds_read_b128 a[216:219],v14 offset:6144\nv_max3_f32 v61,v99,v104,v105\nM v[124:127],a[220:223],a[188:191],v[124:127]\nds_read_b128 a[220:223],v14 offset:7168\nv_max3_f32 v60,v60,v106,v107\n" \
"v_max3_f32 v61,v61,v112,v113\nv_max3_f32 v60,v60,v114,v115\nv_max3_f32 v61,v61,v120,v121\nv_max3_f32 v60,v60,v122,v123\nv_max3_f32 v61,v61,v100,v101\nv_max3_f32 v60,v60,v102,v103\nv_max3_f32 v61,v61,v108,v109\nv_max3_f32 v60,v60,v110,v111\n" \
"v_max3_f32 v61,v61,v116,v117\nv_max3_f32 v60,v60,v118,v119\nv_max3_f32 v61,v61,v124,v125\nv_max3_f32 v61,v61,v126,v127\nv_max_f32 v60,v60,v61\nv_cmp_nge_f32 vcc,0x41f00000,v60\ns_cbranch_vccnz LRS_%=\nLH1R15_%=:\n" \
"M v[128:131],a[192:195],v[64:67],v[128:131]\nv_exp_f32 v96,v96\nM v[160:163],a[192:195],v[72:75],v[160:163]\nv_exp_f32 v97,v97\nM v[192:195],a[192:195],v[80:83],v[192:195]\nv_exp_f32 v98,v98\nM v[224:227],a[192:195],v[88:91],v[224:227]\nv_exp_f32 v99,v99\n" \
"M v[228:231],a[196:199],v[88:91],v[228:231]\nv_exp_f32 v100,v100\nM v[196:199],a[196:199],v[80:83],v[196:199]\nv_exp_f32 v101,v101\nM v[164:167],a[196:199],v[72:75],v[164:167]\ns_waitcnt vmcnt(9) lgkmcnt(0)\nv_exp_f32 v102,v102\nM v[132:135],a[196:199],v[64:67],v[132:135]\n" \
"s_barrier\nbuffer_load_dwordx4 v1,s[84:87],s46 offen offset:0 lds\nM v[136:139],a[200:203],v[64:67],v[136:139]\ns_add_u32 m0,s22,50048\nds_read_b128 v[76:79],v4 offset:0\nC v96,v96,v97\nM v[168:171],a[200:203],v[72:75],v[168:171]\nv_exp_f32 v103,v103\n" \
"M v[200:203],a[200:203],v[80:83],v[200:203]\nv_exp_f32 v104,v104\nM v[232:235],a[200:203],v[88:91],v[232:235]\nbuffer_load_dwordx4 v1,s[84:87],s46 offen offset:128 lds\nM v[236:239],a[204:207],v[88:91],v[236:239]\ns_mov_b32 s46,2031616\ns_add_u32 m0,s22,51200\nds_read_b128 v[84:87],v4 offset:1024\n" \
"C v97,v98,v99\nM v[204:207],a[204:207],v[80:83],v[204:207]\nds_read_b128 v[92:95],v4 offset:2048\nC v98,v100,v101\nM v[172:175],a[204:207],v[72:75],v[172:175]\ns_waitcnt vmcnt(4)\nv_cmp_gt_u64 s[98:99],s[14:15],v[10:11]\nv_cmp_le_u64 s[18:19],s[16:17],v[10:11]\n" \
"M v[140:143],a[204:207],v[64:67],v[140:143]\nbuffer_load_dwordx4 v1,s[84:87],s46 offen offset:0 lds\nv_exp_f32 v105,v105\nM v[144:147],a[208:211],v[64:67],v[144:147]\ns_add_u32 m0,s22,52096\nv_exp_f32 v106,v106\nM v[176:179],a[208:211],v[72:75],v[176:179]\ns_or_b64 s[98:99],s[98:99],s[18:19]\n" \
"s_and_b32 s98,s98,0xff0000\ns_cbranch_scc1 LPF_%=\nLPFR_%=:\nv_exp_f32 v107,v107\nM v[208:211],a[208:211],v[80:83],v[208:211]\nv_exp_f32 v108,v108\nM v[240:243],a[208:211],v[88:91],v[240:243]\nbuffer_load_dwordx4 v1,s[84:87],s46 offen offset:128 lds\n" \
"v_exp_f32 v109,v109\nM v[244:247],a[212:215],v[88:91],v[244:247]\ns_add_u32 m0,s22,65536\nv_exp_f32 v110,v110\nM v[212:215],a[212:215],v[80:83],v[212:215]\ns_mov_b32 s46,1572864\nv_exp_f32 v111,v111\nM v[180:183],a[212:215],v[72:75],v[180:183]\n" \
"v_exp_f32 v112,v112\nM v[148:151],a[212:215],v[64:67],v[148:151]\nbuffer_load_dwordx4 v2,s[36:39],s47 offen offset:0 sc1 lds\nv_exp_f32 v113,v113\nM v[152:155],a[216:219],v[64:67],v[152:155]\nds_read_b128 a[192:195],v14 offset:49152\nv_exp_f32 v114,v114\nM v[184:187],a[216:219],v[72:75],v[184:187]\n" \
"ds_read_b128 a[196:199],v14 offset:50176\nv_exp_f32 v115,v115\nM v[216:219],a[216:219],v[80:83],v[216:219]\nds_read_b128 a[200:203],v14 offset:51200\nC v104,v104,v105\nv_exp_f32 v116,v116\nM v[248:251],a[216:219],v[88:91],v[248:251]\nbuffer_load_dwordx4 v2,s[36:39],s47 offen offset:1024 sc1 lds\n" \
"v_exp_f32 v117,v117\nM v[252:255],a[220:223],v[88:91],v[252:255]\nds_read_b128 a[204:207],v14 offset:52224\nC v99,v102,v103\nv_exp_f32 v118,v118\nM v[220:223],a[220:223],v[80:83],v[220:223]\nds_read_b128 a[208:211],v14 offset:53248\nC v105,v106,v107\n" \
"v_exp_f32 v119,v119\nM v[188:191],a[220:223],v[72:75],v[188:191]\ns_waitcnt lgkmcnt(4)\nds_read_b128 v[72:75],v4 offset:3072\nC v112,v112,v113\nv_exp_f32 v120,v120\nM v[156:159],a[220:223],v[64:67],v[156:159]\nbuffer_load_dwordx4 v2,s[36:39],s47 offen offset:2048 sc1 lds\n" \
"v_exp_f32 v121,v121\nM v[48:51],a[0:3],v[76:79],0\nv_exp_f32 v122,v122\nM v[48:51],a[8:11],v[84:87],v[48:51]\nds_read_b128 v[80:83],v4 offset:8192\nC v106,v108,v109\nv_exp_f32 v123,v123\nM v[48:51],a[16:19],v[92:95],v[48:51]\n" \
"s_waitcnt lgkmcnt(1)\nds_read_b128 v[88:91],v4 offset:9216\nC v107,v110,v111\nM v[48:51],a[24:27],v[72:75],v[48:51]\nbuffer_load_dwordx4 v2,s[36:39],s47 offen offset:3072 sc1 lds\nM v[52:55],a[28:31],v[72:75],0\ns_add_u32 m0,s22,98304\nds_read_b128 a[212:215],v14 offset:54272\n" \
"C v113,v114,v115\nM v[52:55],a[20:23],v[92:95],v[52:55]\ns_add_u32 s47,s29,0\nds_read_b128 v[72:75],v4 offset:10240\nds_read_b128 v[92:95],v4 offset:11264\nM v[52:55],a[12:15],v[84:87],v[52:55]\nv_exp_f32 v124,v124\nM v[52:55],a[4:7],v[76:79],v[52:55]\n" \
"s_waitcnt lgkmcnt(4)\nds_read_b128 a[216:219],v14 offset:55296\nC v120,v120,v121\nM v[64:67],a[0:3],v[80:83],0\ns_waitcnt lgkmcnt(4)\nds_read_b64_tr_b16 a[128:129],v6 offset:32768\nC v121,v122,v123\nM v[64:67],a[8:11],v[88:91],v[64:67]\n" \
"s_waitcnt lgkmcnt(3)\nds_read_b64_tr_b16 a[130:131],v7 offset:32768\nds_read_b64_tr_b16 a[132:133],v8 offset:32768\nds_read_b64_tr_b16 a[134:135],v9 offset:32768\nds_read_b128 a[220:223],v14 offset:56320\nds_read_b64_tr_b16 a[136:137],v6 offset:34816\nds_read_b64_tr_b16 a[138:139],v7 offset:34816\nds_read_b64_tr_b16 a[140:141],v8 offset:34816\n" \
"ds_read_b64_tr_b16 a[142:143],v9 offset:34816\nds_read_b64_tr_b16 a[144:145],v6 offset:16384\nds_read_b64_tr_b16 a[146:147],v7 offset:16384\nds_read_b64_tr_b16 a[148:149],v8 offset:16384\nds_read_b64_tr_b16 a[150:151],v9 offset:16384\nds_read_b64_tr_b16 a[152:153],v6 offset:18432\nds_read_b64_tr_b16 a[154:155],v7 offset:18432\nds_read_b64_tr_b16 a[156:157],v8 offset:18432\n" \
"ds_read_b64_tr_b16 a[158:159],v9 offset:18432\nM v[64:67],a[16:19],v[72:75],v[64:67]\ns_waitcnt lgkmcnt(15)\nds_read_b128 v[84:87],v4 offset:4096\nv_xor_b32 v6,0x10000,v6\nM v[64:67],a[24:27],v[92:95],v[64:67]\nds_read_b128 v[76:79],v4 offset:5120\nv_xor_b32 v7,0x10000,v7\n" \
"M v[68:71],a[28:31],v[92:95],0\nds_read_b128 v[92:95],v4 offset:6144\nv_xor_b32 v8,0x10000,v8\nM v[68:71],a[20:23],v[72:75],v[68:71]\nds_read_b128 v[72:75],v4 offset:7168\nv_xor_b32 v9,0x10000,v9\nv_exp_f32 v125,v125\nM v[68:71],a[12:15],v[88:91],v[68:71]\n" \
"s_waitcnt lgkmcnt(3)\nv_exp_f32 v126,v126\nM v[68:71],a[4:7],v[80:83],v[68:71]\ns_waitcnt vmcnt(8) lgkmcnt(4)\nds_read_b128 v[88:91],v4 offset:12288\nC v114,v116,v117\nM v[24:27],a[224:227],v[96:99],v[16:19]\ns_barrier\n" \
"buffer_load_dwordx4 v2,s[36:39],s47 offen offset:0 sc1 lds\nM v[48:51],a[32:35],v[84:87],v[48:51]\ns_waitcnt lgkmcnt(3)\nds_read_b128 v[80:83],v4 offset:13312\nC v115,v118,v119\nM v[48:51],a[40:43],v[76:79],v[48:51]\ns_waitcnt lgkmcnt(3)\nds_read_b64_tr_b16 a[160:161],v6 offset:32768\n" \
"C v122,v124,v125\nM v[48:51],a[48:51],v[92:95],v[48:51]\ns_waitcnt lgkmcnt(3)\nds_read_b64_tr_b16 a[162:163],v7 offset:32768\nM v[48:51],a[56:59],v[72:75],v[48:51]\nbuffer_load_dwordx4 v2,s[36:39],s47 offen offset:1024 sc1 lds\nM v[52:55],a[60:63],v[72:75],v[52:55]\nds_read_b128 v[72:75],v4 offset:14336\n" \
"M v[52:55],a[52:55],v[92:95],v[52:55]\nds_read_b128 v[92:95],v4 offset:15360\nM v[52:55],a[44:47],v[76:79],v[52:55]\ns_waitcnt lgkmcnt(4)\nv_exp_f32 v127,v127\nM v[52:55],a[36:39],v[84:87],v[52:55]\nbuffer_load_dwordx4 v2,s[36:39],s47 offen offset:2048 sc1 lds\nM v[24:27],a[228:231],v[104:107],v[24:27]\n" \
"s_add_u32 s52,s52,1\nds_read_b64_tr_b16 a[164:165],v8 offset:32768\nC v123,v126,v127\nM v[64:67],a[32:35],v[88:91],v[64:67]\ns_waitcnt lgkmcnt(1)\nds_read_b64_tr_b16 a[166:167],v9 offset:32768\nds_read_b64_tr_b16 a[168:169],v6 offset:34816\nM v[64:67],a[40:43],v[80:83],v[64:67]\n" \
"s_waitcnt lgkmcnt(4)\nds_read_b64_tr_b16 a[170:171],v7 offset:34816\nM v[64:67],a[48:51],v[72:75],v[64:67]\nbuffer_load_dwordx4 v2,s[36:39],s47 offen offset:3072 sc1 lds\nM v[64:67],a[56:59],v[92:95],v[64:67]\ns_add_u32 s47,s29,8192\nds_read_b64_tr_b16 a[172:173],v8 offset:34816\nM v[68:71],a[60:63],v[92:95],v[68:71]\n" \
"s_add_u32 m0,s22,81920\nds_read_b64_tr_b16 a[174:175],v9 offset:34816\nM v[68:71],a[52:55],v[72:75],v[68:71]\nds_read_b64_tr_b16 a[176:177],v6 offset:16384\nM v[68:71],a[44:47],v[80:83],v[68:71]\ns_cmp_eq_u32 s52,s56\nds_read_b64_tr_b16 a[178:179],v7 offset:16384\nds_read_b64_tr_b16 a[180:181],v8 offset:16384\n" \
"M v[68:71],a[36:39],v[88:91],v[68:71]\nbuffer_load_dwordx4 v2,s[36:39],s47 offen offset:0 sc1 lds\nM v[24:27],a[232:235],v[112:115],v[24:27]\ns_cbranch_scc1 LEV_%=\nLEVR15_%=:\nds_read_b64_tr_b16 a[182:183],v9 offset:16384\nds_read_b64_tr_b16 a[184:185],v6 offset:18432\nds_read_b64_tr_b16 a[186:187],v7 offset:18432\n" \
"ds_read_b64_tr_b16 a[188:189],v8 offset:18432\nds_read_b64_tr_b16 a[190:191],v9 offset:18432\nds_write2_b32 v57,v40,v44 offset1:1\nM v[24:27],a[236:239],v[120:123],v[24:27]\nM v[192:195],a[192:195],v[112:115],v[192:195]\nM v[196:199],a[196:199],v[112:115],v[196:199]\nds_write_b64 v57,v[26:27] offset:8\nM v[200:203],a[200:203],v[112:115],v[200:203]\n" \
"buffer_load_dwordx4 v2,s[36:39],s47 offen offset:1024 sc1 lds\nM v[204:207],a[204:207],v[112:115],v[204:207]\nC v192,v192,v193\nC v193,v194,v195\nM v[208:211],a[208:211],v[112:115],v[208:211]\ns_waitcnt vmcnt(6) lgkmcnt(0)\nC v194,v196,v197\nC v195,v198,v199\n" \
"M v[212:215],a[212:215],v[112:115],v[212:215]\ns_barrier\nds_read_b128 v[28:31],v12 offset:0\nM v[216:219],a[216:219],v[112:115],v[216:219]\nds_write_b128 v12,v[192:195] offset:0\nM v[220:223],a[220:223],v[112:115],v[220:223]\nbuffer_load_dwordx4 v2,s[36:39],s47 offen offset:2048 sc1 lds\nM v[224:227],a[192:195],v[120:123],v[224:227]\n" \
"C v200,v200,v201\nC v201,v202,v203\nM v[228:231],a[196:199],v[120:123],v[228:231]\nds_read_b64_tr_b16 a[240:241],v6 offset:49152\nC v202,v204,v205\nM v[232:235],a[200:203],v[120:123],v[232:235]\nds_read_b64_tr_b16 a[242:243],v7 offset:49152\nC v203,v206,v207\n" \
"M v[236:239],a[204:207],v[120:123],v[236:239]\nbuffer_load_dwordx4 v2,s[36:39],s47 offen offset:3072 sc1 lds\nM v[240:243],a[208:211],v[120:123],v[240:243]\ns_add_u32 m0,s22,114688\nC v208,v208,v209\nC v209,v210,v211\nM v[244:247],a[212:215],v[120:123],v[244:247]\ns_add_u32 s47,s30,0\n" \
"ds_write_b128 v12,v[200:203] offset:1024\nM v[248:251],a[216:219],v[120:123],v[248:251]\nC v210,v212,v213\nC v211,v214,v215\nM v[252:255],a[220:223],v[120:123],v[252:255]\nbuffer_load_dwordx4 v2,s[36:39],s47 offen offset:0 sc1 lds\nM v[128:131],a[192:195],v[96:99],v[128:131]\nC v216,v216,v217\n" \
"C v217,v218,v219\nM v[132:135],a[196:199],v[96:99],v[132:135]\nC v218,v220,v221\nC v219,v222,v223\nM v[136:139],a[200:203],v[96:99],v[136:139]\nLG21_%=:\ns_cbranch_execz LG23_%=\nds_write_b128 v12,v[208:211] offset:2048\n" \
"M v[140:143],a[204:207],v[96:99],v[140:143]\nbuffer_load_dwordx4 v2,s[36:39],s47 offen offset:1024 sc1 lds\nM v[144:147],a[208:211],v[96:99],v[144:147]\nC v224,v224,v225\nC v232,v232,v233\nM v[148:151],a[212:215],v[96:99],v[148:151]\nC v225,v226,v227\nC v233,v234,v235\n" \
"M v[152:155],a[216:219],v[96:99],v[152:155]\nds_write_b128 v12,v[216:219] offset:3072\nM v[156:159],a[220:223],v[96:99],v[156:159]\nbuffer_load_dwordx4 v2,s[36:39],s47 offen offset:2048 sc1 lds\nM v[160:163],a[192:195],v[104:107],v[160:163]\nC v226,v228,v229\nC v227,v230,v231\nM v[164:167],a[196:199],v[104:107],v[164:167]\n" \
"ds_read_b128 v[76:79],v20 offset:0\nC v234,v236,v237\nM v[168:171],a[200:203],v[104:107],v[168:171]\nds_read_b128 v[84:87],v20 offset:1024\nC v235,v238,v239\nM v[172:175],a[204:207],v[104:107],v[172:175]\nds_read_b128 v[92:95],v20 offset:2048\nC v240,v240,v241\n" \
"M v[176:179],a[208:211],v[104:107],v[176:179]\ns_waitcnt lgkmcnt(3)\nds_read_b128 v[72:75],v20 offset:3072\nv_min_f32 v15,v32,v28\nM v[180:183],a[212:215],v[104:107],v[180:183]\nbuffer_load_dwordx4 v2,s[36:39],s47 offen offset:3072 sc1 lds\nM v[184:187],a[216:219],v[104:107],v[184:187]\ns_add_u32 m0,s22,0\n" \
"C v248,v248,v249\nC v241,v242,v243\nM v[188:191],a[220:223],v[104:107],v[188:191]\ns_waitcnt lgkmcnt(3)\ns_add_u32 s47,s30,8192\nC v249,v250,v251\nv_sub_f32 v16,v15,v32\nM v[48:51],a[64:67],v[76:79],v[48:51]\n" \
"s_waitcnt lgkmcnt(2)\nds_write_b128 v12,v[224:227] offset:4096\nM v[48:51],a[72:75],v[84:87],v[48:51]\ns_waitcnt lgkmcnt(2)\nds_read_b128 v[80:83],v20 offset:8192\nC v242,v244,v245\nM v[48:51],a[80:83],v[92:95],v[48:51]\ns_waitcnt lgkmcnt(2)\n" \
"ds_read_b128 v[88:91],v20 offset:9216\nC v243,v246,v247\nM v[48:51],a[88:91],v[72:75],v[48:51]\nds_read_b128 v[60:63],v20 offset:10240\nC v250,v252,v253\nM v[52:55],a[92:95],v[72:75],v[52:55]\nds_read_b128 v[72:75],v20 offset:11264\nC v251,v254,v255\n" \
"M v[52:55],a[84:87],v[92:95],v[52:55]\nbuffer_load_dwordx4 v2,s[36:39],s47 offen offset:0 sc1 lds\nM v[52:55],a[76:79],v[84:87],v[52:55]\nv_exp_f32 v16,v16\nM v[52:55],a[68:71],v[76:79],v[52:55]\ns_waitcnt lgkmcnt(3)\nv_sub_f32 v18,v15,v28\nv_mul_f32 v15,v24,v16\n" \
"M v[64:67],a[64:67],v[80:83],v[64:67]\ns_waitcnt lgkmcnt(2)\nds_write_b128 v12,v[232:235] offset:5120\nM v[64:67],a[72:75],v[88:91],v[64:67]\ns_waitcnt lgkmcnt(2)\nds_read_b128 v[92:95],v20 offset:4096\nv_mov_b32 v28,s10\nM v[64:67],a[80:83],v[60:63],v[64:67]\n" \
"s_waitcnt lgkmcnt(2)\nds_read_b128 v[84:87],v20 offset:5120\nv_exp_f32 v18,v18\nM v[64:67],a[88:91],v[72:75],v[64:67]\nds_write_b128 v12,v[240:243] offset:6144\nds_write_b128 v12,v[248:251] offset:7168\nds_read_b64_tr_b16 a[244:245],v8 offset:49152\nds_read_b64_tr_b16 a[246:247],v9 offset:49152\n" \
"ds_read_b64_tr_b16 a[248:249],v6 offset:51200\nds_read_b64_tr_b16 a[250:251],v7 offset:51200\nds_read_b64_tr_b16 a[252:253],v8 offset:51200\nds_read_b64_tr_b16 a[254:255],v9 offset:51200\nds_read_b128 v[76:79],v20 offset:6144\nM v[68:71],a[92:95],v[72:75],v[68:71]\nds_read_b128 v[72:75],v20 offset:7168\nv_fmac_f32 v15,v30,v18\n" \
"v_rcp_f32 v15,v15\nM v[68:71],a[84:87],v[60:63],v[68:71]\nbuffer_load_dwordx4 v2,s[36:39],s47 offen offset:1024 sc1 lds\nM v[68:71],a[76:79],v[88:91],v[68:71]\nv_mul_f32 v16,v16,v15\nv_mul_f32 v18,v18,v15\nM v[68:71],a[68:71],v[80:83],v[68:71]\ns_waitcnt lgkmcnt(11)\n" \
"ds_read_b128 v[60:63],v20 offset:12288\nv_mul_f32 v128,v16,v128\nM v[48:51],a[96:99],v[92:95],v[48:51]\ns_waitcnt lgkmcnt(11)\nds_read_b128 v[88:91],v20 offset:13312\nv_mul_f32 v129,v16,v129\nM v[48:51],a[104:107],v[84:87],v[48:51]\ns_waitcnt vmcnt(10) lgkmcnt(4)\n" \
"s_waitcnt lgkmcnt(3)\nds_read_b128 v[80:83],v20 offset:14336\nv_mul_f32 v130,v16,v130\nM v[48:51],a[112:115],v[76:79],v[48:51]\ns_barrier\ns_waitcnt lgkmcnt(3)\nv_mul_f32 v131,v16,v131\nv_mul_f32 v132,v16,v132\n" \
"M v[48:51],a[120:123],v[72:75],v[48:51]\nbuffer_load_dwordx4 v2,s[36:39],s47 offen offset:2048 sc1 lds\nv_mul_f32 v133,v16,v133\nM v[52:55],a[124:127],v[72:75],v[52:55]\nds_read_b128 v[72:75],v20 offset:15360\nv_mul_f32 v134,v16,v134\nv_mul_f32 v135,v16,v135\nM v[52:55],a[116:119],v[76:79],v[52:55]\n" \
"s_waitcnt lgkmcnt(1)\nds_read_b128 v[96:99],v57 offset:0\nv_mul_f32 v136,v16,v136\nv_mul_f32 v137,v16,v137\nM v[52:55],a[108:111],v[84:87],v[52:55]\ns_waitcnt lgkmcnt(3)\nds_read_b128 v[100:103],v57 offset:1024\nv_mul_f32 v138,v16,v138\n" \
"v_mul_f32 v139,v16,v139\nM v[52:55],a[100:103],v[92:95],v[52:55]\nbuffer_load_dwordx4 v2,s[36:39],s47 offen offset:3072 sc1 lds\nv_mul_f32 v140,v16,v140\nM v[64:67],a[96:99],v[60:63],v[64:67]\ns_add_u32 m0,s22,32768\nds_read_b128 v[104:107],v57 offset:2048\nv_mul_f32 v141,v16,v141\n" \
"v_mul_f32 v142,v16,v142\nM v[64:67],a[104:107],v[88:91],v[64:67]\ns_add_u32 s47,s28,16384\nds_read_b128 v[76:79],v20 offset:32768\nv_mul_f32 v143,v16,v143\nM v[64:67],a[112:115],v[80:83],v[64:67]\ns_waitcnt lgkmcnt(4)\nds_read_b128 v[84:87],v20 offset:33792\n" \
"v_min_f32 v15,v36,v29\nM v[64:67],a[120:123],v[72:75],v[64:67]\nds_read_b128 v[92:95],v20 offset:34816\nv_mul_f32 v144,v16,v144\nM v[68:71],a[124:127],v[72:75],v[68:71]\ns_waitcnt lgkmcnt(4)\nds_read_b128 v[72:75],v20 offset:35840\nv_mul_f32 v145,v16,v145\n" \
"v_lshlrev_b32 v228,16,v96\nM v[68:71],a[116:119],v[80:83],v[68:71]\nbuffer_load_dwordx4 v2,s[36:39],s47 offen offset:0 sc1 lds\nv_and_b32 v96,0xffff0000,v96\nM v[68:71],a[108:111],v[88:91],v[68:71]\nv_fmac_f32 v128,v228,v18\nv_fmac_f32 v129,v96,v18\nv_lshlrev_b32 v229,16,v97\n" \
"M v[68:71],a[100:103],v[60:63],v[68:71]\ns_waitcnt lgkmcnt(3)\nds_read_b128 v[108:111],v57 offset:3072\nv_and_b32 v97,0xffff0000,v97\nv_fmac_f32 v130,v229,v18\nM v[32:35],a[0:3],v[76:79],0\ns_waitcnt lgkmcnt(3)\nv_fmac_f32 v131,v97,v18\n" \
"v_lshlrev_b32 v230,16,v98\nv_and_b32 v98,0xffff0000,v98\nM v[32:35],a[8:11],v[84:87],v[32:35]\ns_waitcnt lgkmcnt(2)\nds_read_b128 v[80:83],v20 offset:40960\nv_fmac_f32 v132,v230,v18\nv_fmac_f32 v133,v98,v18\nM v[32:35],a[16:19],v[92:95],v[32:35]\n" \
"s_waitcnt lgkmcnt(2)\nds_read_b128 v[88:91],v20 offset:41984\nv_lshlrev_b32 v231,16,v99\nv_and_b32 v99,0xffff0000,v99\nM v[32:35],a[24:27],v[72:75],v[32:35]\nds_read_b128 v[60:63],v20 offset:43008\nv_sub_f32 v17,v15,v36\nv_fmac_f32 v134,v231,v18\n" \
"M v[36:39],a[28:31],v[72:75],0\nds_read_b128 v[72:75],v20 offset:44032\nv_fmac_f32 v135,v99,v18\nC v128,v128,v129\nM v[36:39],a[20:23],v[92:95],v[36:39]\nbuffer_load_dwordx4 v2,s[36:39],s47 offen offset:1024 sc1 lds\nC v129,v130,v131\nM v[36:39],a[12:15],v[84:87],v[36:39]\n" \
"C v130,v132,v133\nC v131,v134,v135\nv_lshlrev_b32 v236,16,v100\nM v[36:39],a[4:7],v[76:79],v[36:39]\ns_waitcnt lgkmcnt(3)\nv_and_b32 v100,0xffff0000,v100\nv_fmac_f32 v136,v236,v18\nv_fmac_f32 v137,v100,v18\n" \
"M v[40:43],a[0:3],v[80:83],0\ns_waitcnt lgkmcnt(2)\nv_lshlrev_b32 v237,16,v101\nv_and_b32 v101,0xffff0000,v101\nv_fmac_f32 v138,v237,v18\nM v[40:43],a[8:11],v[88:91],v[40:43]\ns_waitcnt lgkmcnt(1)\nds_read_b128 v[92:95],v20 offset:36864\n" \
"v_fmac_f32 v139,v101,v18\nv_lshlrev_b32 v238,16,v102\nM v[40:43],a[16:19],v[60:63],v[40:43]\ns_waitcnt lgkmcnt(1)\nds_read_b128 v[84:87],v20 offset:37888\nv_and_b32 v102,0xffff0000,v102\nv_fmac_f32 v140,v238,v18\nM v[40:43],a[24:27],v[72:75],v[40:43]\n" \
"buffer_store_dwordx4 v[128:131],v4,s[36:39],s62 offen offset:0 sc1\nds_read_b128 v[112:115],v57 offset:4096\nds_read_b128 v[116:119],v57 offset:5120\nds_read_b128 v[120:123],v57 offset:6144\nds_read_b128 v[124:127],v57 offset:7168\nds_read_b128 v[76:79],v20 offset:38912\nM v[44:47],a[28:31],v[72:75],0\nds_read_b128 v[72:75],v20 offset:39936\n" \
"v_fmac_f32 v141,v102,v18\nv_lshlrev_b32 v239,16,v103\nM v[44:47],a[20:23],v[60:63],v[44:47]\nv_and_b32 v103,0xffff0000,v103\nv_fmac_f32 v142,v239,v18\nv_fmac_f32 v143,v103,v18\nM v[44:47],a[12:15],v[88:91],v[44:47]\ns_waitcnt lgkmcnt(7)\n" \
"C v136,v136,v137\nC v137,v138,v139\nv_lshlrev_b32 v244,16,v104\nM v[44:47],a[4:7],v[80:83],v[44:47]\nbuffer_load_dwordx4 v2,s[36:39],s47 offen offset:2048 sc1 lds\nv_and_b32 v104,0xffff0000,v104\nM v[32:35],a[32:35],v[92:95],v[32:35]\ns_waitcnt lgkmcnt(6)\n" \
"v_lshlrev_b32 v245,16,v105\nC v138,v140,v141\nC v139,v142,v143\nM v[32:35],a[40:43],v[84:87],v[32:35]\ns_waitcnt vmcnt(12) lgkmcnt(2)\ns_waitcnt lgkmcnt(1)\nds_read_b128 v[60:63],v20 offset:45056\nv_lshlrev_b32 v246,16,v106\n" \
"v_mul_f32 v146,v16,v146\nM v[32:35],a[48:51],v[76:79],v[32:35]\ns_barrier\ns_waitcnt lgkmcnt(1)\nds_read_b128 v[88:91],v20 offset:46080\nv_mul_f32 v147,v16,v147\nM v[32:35],a[56:59],v[72:75],v[32:35]\nds_read_b128 v[80:83],v20 offset:47104\n" \
"v_fmac_f32 v144,v244,v18\nv_and_b32 v105,0xffff0000,v105\nM v[36:39],a[60:63],v[72:75],v[36:39]\nds_read_b128 v[72:75],v20 offset:48128\nv_fmac_f32 v145,v104,v18\nv_lshlrev_b32 v247,16,v107\nM v[36:39],a[52:55],v[76:79],v[36:39]\nbuffer_load_dwordx4 v2,s[36:39],s47 offen offset:3072 sc1 lds\n" \
"v_mul_f32 v148,v16,v148\nM v[36:39],a[44:47],v[84:87],v[36:39]\ns_add_u32 m0,s22,16384\nv_mul_f32 v149,v16,v149\nv_mul_f32 v150,v16,v150\nv_and_b32 v106,0xffff0000,v106\nM v[36:39],a[36:39],v[92:95],v[36:39]\ns_waitcnt lgkmcnt(3)\n" \
"s_add_u32 s47,s29,16384\nv_fmac_f32 v146,v245,v18\nv_fmac_f32 v147,v105,v18\nC v144,v144,v145\nM v[40:43],a[32:35],v[60:63],v[40:43]\ns_waitcnt lgkmcnt(2)\nv_mul_f32 v151,v16,v151\nv_and_b32 v107,0xffff0000,v107\n" \
"v_fmac_f32 v148,v246,v18\nM v[40:43],a[40:43],v[88:91],v[40:43]\ns_waitcnt lgkmcnt(1)\nds_read_b128 v[76:79],v20 offset:16384\nv_fmac_f32 v149,v106,v18\nv_fmac_f32 v150,v247,v18\nM v[40:43],a[48:51],v[80:83],v[40:43]\ns_waitcnt lgkmcnt(1)\n" \
"ds_read_b128 v[84:87],v20 offset:17408\nC v145,v146,v147\nv_fmac_f32 v151,v107,v18\nM v[40:43],a[56:59],v[72:75],v[40:43]\nbuffer_store_dwordx4 v[136:139],v4,s[36:39],s62 offen offset:1024 sc1\nds_read_b128 v[92:95],v20 offset:18432\nM v[44:47],a[60:63],v[72:75],v[44:47]\nds_read_b128 v[72:75],v20 offset:19456\n" \
"v_sub_f32 v19,v15,v29\nC v146,v148,v149\nM v[44:47],a[52:55],v[80:83],v[44:47]\nv_exp_f32 v17,v17\nv_lshlrev_b32 v252,16,v108\nM v[44:47],a[44:47],v[88:91],v[44:47]\ns_waitcnt lgkmcnt(3)\nC v147,v150,v151\n" \
"v_lshlrev_b32 v253,16,v109\nv_mul_f32 v152,v16,v152\nM v[44:47],a[36:39],v[60:63],v[44:47]\nbuffer_load_dwordx4 v2,s[36:39],s47 offen offset:0 sc1 lds\nv_mul_f32 v153,v16,v153\nM v[32:35],a[64:67],v[76:79],v[32:35]\ns_waitcnt lgkmcnt(2)\nv_exp_f32 v19,v19\n" \
"v_and_b32 v108,0xffff0000,v108\nM v[32:35],a[72:75],v[84:87],v[32:35]\ns_waitcnt lgkmcnt(1)\nds_read_b128 v[80:83],v20 offset:24576\nv_mul_f32 v15,v25,v17\nv_mul_f32 v154,v16,v154\nM v[32:35],a[80:83],v[92:95],v[32:35]\ns_waitcnt lgkmcnt(1)\n" \
"ds_read_b128 v[88:91],v20 offset:25600\nv_lshlrev_b32 v254,16,v110\nv_mul_f32 v155,v16,v155\nM v[32:35],a[88:91],v[72:75],v[32:35]\nds_read_b128 v[60:63],v20 offset:26624\nv_and_b32 v109,0xffff0000,v109\nv_fmac_f32 v152,v252,v18\nM v[36:39],a[92:95],v[72:75],v[36:39]\n" \
"ds_read_b128 v[72:75],v20 offset:27648\nv_fmac_f32 v153,v108,v18\nv_lshlrev_b32 v255,16,v111\nM v[36:39],a[84:87],v[92:95],v[36:39]\nbuffer_load_dwordx4 v2,s[36:39],s47 offen offset:1024 sc1 lds\nv_fmac_f32 v15,v31,v19\nM v[36:39],a[76:79],v[84:87],v[36:39]\nv_mul_f32 v156,v16,v156\n" \
"v_mul_f32 v157,v16,v157\nv_and_b32 v110,0xffff0000,v110\nM v[36:39],a[68:71],v[76:79],v[36:39]\ns_waitcnt lgkmcnt(3)\nv_fmac_f32 v154,v253,v18\nv_fmac_f32 v155,v109,v18\nv_mul_f32 v158,v16,v158\nM v[40:43],a[64:67],v[80:83],v[40:43]\n" \
"s_waitcnt lgkmcnt(2)\nC v152,v152,v153\nv_mul_f32 v159,v16,v159\nv_and_b32 v111,0xffff0000,v111\nM v[40:43],a[72:75],v[88:91],v[40:43]\ns_waitcnt lgkmcnt(1)\nds_read_b128 v[92:95],v20 offset:20480\nv_fmac_f32 v156,v254,v18\n" \
"v_rcp_f32 v15,v15\nM v[40:43],a[80:83],v[60:63],v[40:43]\ns_waitcnt lgkmcnt(1)\nds_read_b128 v[84:87],v20 offset:21504\nv_fmac_f32 v157,v110,v18\nC v153,v154,v155\nM v[40:43],a[88:91],v[72:75],v[40:43]\nbuffer_store_dwordx4 v[144:147],v4,s[36:39],s62 offen offset:2048 sc1\n" \
"ds_read_b128 v[76:79],v20 offset:22528\nM v[44:47],a[92:95],v[72:75],v[44:47]\nds_read_b128 v[72:75],v20 offset:23552\nv_fmac_f32 v158,v255,v18\nv_fmac_f32 v159,v111,v18\nM v[44:47],a[84:87],v[60:63],v[44:47]\nv_mul_f32 v17,v17,v15\nC v154,v156,v157\n" \
"C v155,v158,v159\nM v[44:47],a[76:79],v[88:91],v[44:47]\ns_waitcnt lgkmcnt(3)\nv_lshlrev_b32 v228,16,v112\nv_mul_f32 v19,v19,v15\nv_lshlrev_b32 v229,16,v113\nM v[44:47],a[68:71],v[80:83],v[44:47]\nbuffer_load_dwordx2 v[10:11],v5,s[36:39],0 offen sc1\n" \
"v_mul_f32 v160,v17,v160\nM v[32:35],a[96:99],v[92:95],v[32:35]\ns_waitcnt lgkmcnt(2)\nv_mul_f32 v161,v17,v161\nv_and_b32 v112,0xffff0000,v112\nv_mul_f32 v162,v17,v162\nM v[32:35],a[104:107],v[84:87],v[32:35]\ns_waitcnt vmcnt(14) lgkmcnt(12)\n" \
"s_waitcnt lgkmcnt(1)\nds_read_b128 v[60:63],v20 offset:28672\nv_lshlrev_b32 v230,16,v114\nv_mul_f32 v163,v17,v163\nM v[32:35],a[112:115],v[76:79],v[32:35]\ns_barrier\ns_waitcnt lgkmcnt(1)\nds_read_b128 v[88:91],v20 offset:29696\n" \
"v_and_b32 v113,0xffff0000,v113\nM v[32:35],a[120:123],v[72:75],v[32:35]\nds_read_b128 v[80:83],v20 offset:30720\nv_fmac_f32 v160,v228,v19\nv_fmac_f32 v161,v112,v19\nM v[36:39],a[124:127],v[72:75],v[36:39]\nds_read_b128 v[72:75],v20 offset:31744\nv_mul_f32 v164,v17,v164\n" \
"v_mul_f32 v165,v17,v165\nM v[36:39],a[116:119],v[76:79],v[36:39]\nbuffer_load_dwordx4 v2,s[36:39],s47 offen offset:2048 sc1 lds\nv_and_b32 v114,0xffff0000,v114\nM v[36:39],a[108:111],v[84:87],v[36:39]\nv_lshlrev_b32 v231,16,v115\nv_mul_f32 v166,v17,v166\nv_fmac_f32 v162,v229,v19\n" \
"M v[36:39],a[100:103],v[92:95],v[36:39]\ns_waitcnt lgkmcnt(3)\nv_fmac_f32 v163,v113,v19\nC v160,v160,v161\nv_mul_f32 v167,v17,v167\nM v[40:43],a[96:99],v[60:63],v[40:43]\ns_waitcnt lgkmcnt(2)\nv_fmac_f32 v164,v230,v19\n" \
"v_fmac_f32 v165,v114,v19\nv_and_b32 v115,0xffff0000,v115\nM v[40:43],a[104:107],v[88:91],v[40:43]\ns_waitcnt lgkmcnt(1)\nds_read_b128 v[76:79],v20 offset:49152\nv_fmac_f32 v166,v231,v19\nC v161,v162,v163\nM v[40:43],a[112:115],v[80:83],v[40:43]\n" \
"s_waitcnt lgkmcnt(1)\nds_read_b128 v[84:87],v20 offset:50176\nv_fmac_f32 v167,v115,v19\nC v162,v164,v165\nM v[40:43],a[120:123],v[72:75],v[40:43]\nbuffer_store_dwordx4 v[152:155],v4,s[36:39],s62 offen offset:3072 sc1\nds_read_b128 v[92:95],v20 offset:51200\nM v[44:47],a[124:127],v[72:75],v[44:47]\n" \
"s_add_u32 s62,s51,262144\nds_read_b128 v[72:75],v20 offset:52224\nC v163,v166,v167\nv_lshlrev_b32 v236,16,v116\nM v[44:47],a[116:119],v[80:83],v[44:47]\ns_lshl_b32 s59,s61,20\nv_mul_f32 v168,v17,v168\nv_mul_f32 v169,v17,v169\n" \
"v_and_b32 v116,0xffff0000,v116\nM v[44:47],a[108:111],v[88:91],v[44:47]\ns_lshl_b32 s58,s60,15\nv_lshlrev_b32 v237,16,v117\nv_lshlrev_b32 v238,16,v118\nM v[44:47],a[100:103],v[60:63],v[44:47]\ns_waitcnt lgkmcnt(3)\nbuffer_load_dwordx4 v2,s[36:39],s47 offen offset:3072 sc1 lds\n" \
"v_mul_f32 v170,v17,v170\nM v[192:195],a[0:3],v[76:79],0\ns_waitcnt lgkmcnt(2)\ns_add_u32 m0,s22,49152\nv_lshlrev_b32 v239,16,v119\nv_mul_f32 v171,v17,v171\nv_fmac_f32 v168,v236,v19\nM v[192:195],a[8:11],v[84:87],v[192:195]\n" \
"s_waitcnt lgkmcnt(1)\ns_add_u32 s47,s30,16384\nds_read_b128 v[80:83],v20 offset:57344\nv_fmac_f32 v169,v116,v19\nv_and_b32 v117,0xffff0000,v117\nM v[192:195],a[16:19],v[92:95],v[192:195]\ns_waitcnt lgkmcnt(1)\ns_add_u32 s94,s59,s58\n" \
"ds_read_b128 v[88:91],v20 offset:58368\nds_read_b128 v[60:63],v20 offset:59392\nv_mul_f32 v172,v17,v172\nM v[192:195],a[24:27],v[72:75],v[192:195]\nbuffer_load_dwordx4 v2,s[36:39],s47 offen offset:0 sc1 lds\nv_mul_f32 v173,v17,v173\nM v[196:199],a[28:31],v[72:75],0\ns_add_u32 s58,s60,1\n" \
"ds_read_b128 v[72:75],v20 offset:60416\nv_and_b32 v118,0xffff0000,v118\nv_mul_f32 v174,v17,v174\nM v[196:199],a[20:23],v[92:95],v[196:199]\ns_and_b32 s58,s58,3\nv_mul_f32 v175,v17,v175\nv_fmac_f32 v170,v237,v19\nv_and_b32 v119,0xffff0000,v119\n" \
"M v[196:199],a[12:15],v[84:87],v[196:199]\ns_and_b32 s59,s58,2\nv_fmac_f32 v171,v117,v19\nC v168,v168,v169\nM v[196:199],a[4:7],v[76:79],v[196:199]\ns_waitcnt lgkmcnt(3)\ns_add_u32 s59,s59,s58\nv_fmac_f32 v172,v238,v19\n" \
"v_fmac_f32 v173,v118,v19\nM v[200:203],a[0:3],v[80:83],0\ns_waitcnt lgkmcnt(1)\ns_add_u32 s94,s94,58720256\nv_fmac_f32 v174,v239,v19\nv_fmac_f32 v175,v119,v19\nM v[200:203],a[8:11],v[88:91],v[200:203]\ns_lshl_b32 s59,s59,17\n" \
"ds_read_b128 v[92:95],v20 offset:53248\nds_read_b128 v[84:87],v20 offset:54272\nM v[200:203],a[16:19],v[60:63],v[200:203]\ns_waitcnt lgkmcnt(2)\nbuffer_load_dwordx4 v2,s[36:39],s47 offen offset:1024 sc1 lds\nC v169,v170,v171\nM v[200:203],a[24:27],v[72:75],v[200:203]\ns_add_u32 s32,s94,s59\n" \
"ds_read_b128 v[76:79],v20 offset:55296\nC v170,v172,v173\nC v171,v174,v175\nM v[204:207],a[28:31],v[72:75],0\ns_lshl_b32 s58,s58,1\nds_read_b128 v[72:75],v20 offset:56320\nv_lshlrev_b32 v244,16,v120\nM v[204:207],a[20:23],v[60:63],v[204:207]\n" \
"s_and_b32 s59,%1,0xf9\nv_lshlrev_b32 v245,16,v121\nv_mul_f32 v176,v17,v176\nM v[204:207],a[12:15],v[88:91],v[204:207]\nbuffer_store_dwordx4 v[160:163],v4,s[36:39],s62 offen offset:0 sc1\nM v[204:207],a[4:7],v[80:83],v[204:207]\ns_waitcnt lgkmcnt(2)\ns_or_b32 s58,s58,s59\n" \
"ds_read_b128 v[60:63],v20 offset:61440\nv_mul_f32 v177,v17,v177\nM v[192:195],a[32:35],v[92:95],v[192:195]\ns_lshl_b32 s58,s58,2\nds_read_b128 v[88:91],v20 offset:62464\nv_and_b32 v120,0xffff0000,v120\nM v[192:195],a[40:43],v[84:87],v[192:195]\ns_waitcnt vmcnt(16) lgkmcnt(14)\n" \
"s_waitcnt lgkmcnt(3)\nds_read_b128 v[80:83],v20 offset:63488\nv_lshlrev_b32 v246,16,v122\nM v[192:195],a[48:51],v[76:79],v[192:195]\ns_add_u32 s58,s58,%2\ns_barrier\ns_waitcnt lgkmcnt(3)\nv_mul_f32 v178,v17,v178\n" \
"v_lshlrev_b32 v247,16,v123\nM v[192:195],a[56:59],v[72:75],v[192:195]\ns_mul_i32 s58,s58,12288\nv_mul_f32 v179,v17,v179\nv_and_b32 v121,0xffff0000,v121\nM v[196:199],a[60:63],v[72:75],v[196:199]\ns_add_u32 s44,s58,109058048\nds_read_b128 v[72:75],v20 offset:64512\n" \
"v_fmac_f32 v176,v244,v19\nv_fmac_f32 v177,v120,v19\nM v[196:199],a[52:55],v[76:79],v[196:199]\ns_add_u32 s58,s60,2\nv_mul_f32 v180,v17,v180\nv_mul_f32 v181,v17,v181\nM v[196:199],a[44:47],v[84:87],v[196:199]\ns_and_b32 s58,s58,3\n" \
"v_mul_f32 v182,v17,v182\nv_and_b32 v122,0xffff0000,v122\nM v[196:199],a[36:39],v[92:95],v[196:199]\ns_waitcnt lgkmcnt(1)\ns_and_b32 s59,s58,2\nv_mul_f32 v183,v17,v183\nv_and_b32 v123,0xffff0000,v123\nM v[200:203],a[32:35],v[60:63],v[200:203]\n" \
"s_add_u32 s59,s59,s58\nds_read_b128 v[76:79],v4 offset:0\nv_fmac_f32 v178,v245,v19\nv_fmac_f32 v179,v121,v19\nM v[200:203],a[40:43],v[88:91],v[200:203]\ns_lshl_b32 s59,s59,17\nds_read_b128 v[84:87],v4 offset:1024\nC v176,v176,v177\n" \
"M v[200:203],a[48:51],v[80:83],v[200:203]\ns_waitcnt lgkmcnt(2)\ns_add_u32 s33,s94,s59\nv_fmac_f32 v180,v246,v19\nv_fmac_f32 v181,v122,v19\nM v[200:203],a[56:59],v[72:75],v[200:203]\nbuffer_store_dwordx4 v[168:171],v4,s[36:39],s62 offen offset:1024 sc1\nds_read_b128 v[92:95],v4 offset:2048\n" \
"M v[204:207],a[60:63],v[72:75],v[204:207]\ns_lshl_b32 s58,s58,1\nds_read_b128 v[72:75],v4 offset:3072\nv_fmac_f32 v182,v247,v19\nv_fmac_f32 v183,v123,v19\nM v[204:207],a[52:55],v[80:83],v[204:207]\ns_and_b32 s59,%1,0xf9\nds_read_b128 v[80:83],v4 offset:8192\n" \
"C v177,v178,v179\nM v[204:207],a[44:47],v[88:91],v[204:207]\ns_or_b32 s58,s58,s59\nC v178,v180,v181\nC v179,v182,v183\nM v[204:207],a[36:39],v[60:63],v[204:207]\ns_waitcnt lgkmcnt(3)\nbuffer_load_dwordx4 v2,s[36:39],s47 offen offset:2048 sc1 lds\n" \
"v_lshlrev_b32 v252,16,v124\nM v[192:195],a[64:67],v[76:79],v[192:195]\ns_lshl_b32 s58,s58,2\nds_read_b128 v[88:91],v4 offset:9216\nv_mul_f32 v184,v17,v184\nM v[192:195],a[72:75],v[84:87],v[192:195]\ns_waitcnt lgkmcnt(3)\ns_add_u32 s58,s58,%2\n" \
"ds_read_b128 v[60:63],v4 offset:10240\nv_mul_f32 v185,v17,v185\nM v[192:195],a[80:83],v[92:95],v[192:195]\ns_waitcnt lgkmcnt(3)\ns_mul_i32 s58,s58,12288\nv_lshlrev_b32 v253,16,v125\nv_and_b32 v124,0xffff0000,v124\nM v[192:195],a[88:91],v[72:75],v[192:195]\n" \
"s_add_u32 s45,s58,109060096\nv_lshlrev_b32 v254,16,v126\nv_mul_f32 v186,v17,v186\nM v[196:199],a[92:95],v[72:75],v[196:199]\ns_add_u32 s58,s60,3\nds_read_b128 v[72:75],v4 offset:11264\nv_mul_f32 v187,v17,v187\nM v[196:199],a[84:87],v[92:95],v[196:199]\n" \
"buffer_load_dwordx4 v2,s[36:39],s47 offen offset:3072 sc1 lds\nM v[196:199],a[76:79],v[84:87],v[196:199]\ns_add_u32 m0,s22,131072\nds_read_b128 v[92:95],v4 offset:4096\nv_fmac_f32 v184,v252,v19\nM v[196:199],a[68:71],v[76:79],v[196:199]\ns_waitcnt lgkmcnt(2)\ns_add_u32 s47,s31,0\n" \
"ds_read_b128 v[84:87],v4 offset:5120\nv_and_b32 v125,0xffff0000,v125\nM v[200:203],a[64:67],v[80:83],v[200:203]\ns_and_b32 s58,s58,3\nds_read_b128 v[76:79],v4 offset:6144\nv_fmac_f32 v185,v124,v19\nM v[200:203],a[72:75],v[88:91],v[200:203]\ns_and_b32 s59,s58,2\n" \
"v_mul_f32 v188,v17,v188\nv_mul_f32 v189,v17,v189\nM v[200:203],a[80:83],v[60:63],v[200:203]\ns_waitcnt lgkmcnt(3)\ns_add_u32 s59,s59,s58\nv_and_b32 v126,0xffff0000,v126\nv_fmac_f32 v186,v253,v19\nM v[200:203],a[88:91],v[72:75],v[200:203]\n" \
"buffer_store_dwordx4 v[176:179],v4,s[36:39],s62 offen offset:2048 sc1\nv_fmac_f32 v187,v125,v19\nM v[204:207],a[92:95],v[72:75],v[204:207]\ns_lshl_b32 s59,s59,17\nds_read_b128 v[72:75],v4 offset:7168\nv_lshlrev_b32 v255,16,v127\nC v184,v184,v185\nM v[204:207],a[84:87],v[60:63],v[204:207]\n" \
"s_add_u32 s34,s94,s59\nds_read_b128 v[60:63],v4 offset:12288\nv_mul_f32 v190,v17,v190\nM v[204:207],a[76:79],v[88:91],v[204:207]\nLG22_%=:\ns_cbranch_execz LG24_%=\ns_lshl_b32 s58,s58,1\nds_read_b128 v[88:91],v4 offset:13312\n" \
"v_mul_f32 v191,v17,v191\nM v[204:207],a[68:71],v[80:83],v[204:207]\ns_waitcnt lgkmcnt(3)\ns_and_b32 s59,%1,0xf9\nv_fmac_f32 v188,v254,v19\nv_fmac_f32 v189,v126,v19\nM v[192:195],a[96:99],v[92:95],v[192:195]\ns_or_b32 s58,s58,s59\n" \
"v_and_b32 v127,0xffff0000,v127\nC v185,v186,v187\nM v[192:195],a[104:107],v[84:87],v[192:195]\ns_waitcnt vmcnt(15) lgkmcnt(14)\nds_read_b128 v[80:83],v4 offset:14336\nv_fmac_f32 v190,v255,v19\nM v[192:195],a[112:115],v[76:79],v[192:195]\ns_waitcnt vmcnt(10)\n" \
"s_barrier\ns_waitcnt lgkmcnt(3)\nv_cmp_gt_u64 s[98:99],s[14:15],v[10:11]\nv_cmp_le_u64 s[18:19],s[16:17],v[10:11]\nM v[192:195],a[120:123],v[72:75],v[192:195]\ns_or_b64 s[98:99],s[98:99],s[18:19]\ns_and_b32 s98,s98,0xff000000\ns_cbranch_scc1 LPM_%=\n" \
"LPMR_%=:\nbuffer_load_dwordx4 v2,s[36:39],s47 offen offset:0 sc1 lds\nv_fmac_f32 v191,v127,v19\nM v[196:199],a[124:127],v[72:75],v[196:199]\ns_lshl_b32 s58,s58,2\nds_read_b128 v[72:75],v4 offset:15360\nC v186,v188,v189\nC v187,v190,v191\n" \
"M v[196:199],a[116:119],v[76:79],v[196:199]\ns_add_u32 s58,s58,%2\nds_read_b128 v[76:79],v4 offset:32768\nv_mov_b32 v29,s11\nM v[196:199],a[108:111],v[84:87],v[196:199]\ns_mul_i32 s58,s58,12288\nds_read_b128 v[84:87],v4 offset:33792\nv_xor_b32 v6,0x20000,v6\n" \
"M v[196:199],a[100:103],v[92:95],v[196:199]\ns_waitcnt lgkmcnt(3)\nbuffer_load_dwordx4 v2,s[36:39],s47 offen offset:1024 sc1 lds\nM v[200:203],a[96:99],v[60:63],v[200:203]\ns_add_u32 s48,s58,109062144\nds_read_b128 v[92:95],v4 offset:34816\nv_xor_b32 v7,0x20000,v7\nM v[200:203],a[104:107],v[88:91],v[200:203]\n" \
"s_add_u32 s58,s60,4\nv_xor_b32 v8,0x20000,v8\nv_xor_b32 v9,0x20000,v9\nM v[200:203],a[112:115],v[80:83],v[200:203]\ns_waitcnt lgkmcnt(3)\ns_and_b32 s58,s58,3\nv_and_b32 v30,0xf0,v4\nv_lshrrev_b32 v15,2,v30\n" \
"M v[200:203],a[120:123],v[72:75],v[200:203]\nbuffer_store_dwordx4 v[184:187],v4,s[36:39],s62 offen offset:3072 sc1\nM v[204:207],a[124:127],v[72:75],v[204:207]\ns_and_b32 s59,s58,2\nds_read_b128 v[72:75],v4 offset:35840\nv_lshlrev_b32 v13,7,v30\nM v[204:207],a[116:119],v[80:83],v[204:207]\ns_add_u32 s59,s59,s58\n" \
"ds_read_b128 v[80:83],v4 offset:40960\nv_lshl_add_u32 v114,v30,2,s22\nM v[204:207],a[108:111],v[88:91],v[204:207]\ns_and_b32 s58,%1,0xf9\nds_read_b128 v[88:91],v4 offset:41984\nv_bfe_u32 v30,v4,8,1\nM v[204:207],a[100:103],v[60:63],v[204:207]\ns_waitcnt lgkmcnt(3)\n" \
"buffer_load_dwordx4 v2,s[36:39],s47 offen offset:2048 sc1 lds\nM v[48:51],a[128:131],v[76:79],v[48:51]\ns_lshl_b32 s59,s59,17\nds_read_b128 v[60:63],v4 offset:43008\nv_lshrrev_b32 v31,9,v4\nM v[48:51],a[136:139],v[84:87],v[48:51]\ns_lshl_b32 s58,s58,2\nv_lshl_add_u32 v13,v30,5,v13\n" \
"v_lshl_add_u32 v114,v30,3,v114\nM v[48:51],a[144:147],v[92:95],v[48:51]\ns_waitcnt lgkmcnt(3)\ns_add_u32 s35,s94,s59\nv_lshl_add_u32 v13,v31,4,v13\nv_lshl_add_u32 v114,v31,4,v114\nM v[48:51],a[152:155],v[72:75],v[48:51]\nbuffer_load_dwordx4 v2,s[36:39],s47 offen offset:3072 sc1 lds\n" \
"M v[52:55],a[156:159],v[72:75],v[52:55]\ns_add_u32 m0,s22,147456\nds_read_b128 v[72:75],v4 offset:44032\nv_and_b32 v30,0x3c0,v4\nM v[52:55],a[148:151],v[92:95],v[52:55]\ns_add_u32 s47,s31,8192\nds_read_b128 v[92:95],v4 offset:36864\nv_and_b32 v31,0x30,v4\n" \
"M v[52:55],a[140:143],v[84:87],v[52:55]\ns_add_u32 s58,s58,%2\nds_read_b128 v[84:87],v4 offset:37888\nv_lshl_add_u32 v112,v30,5,v31\nM v[52:55],a[132:135],v[76:79],v[52:55]\ns_waitcnt lgkmcnt(3)\nbuffer_load_dwordx4 v1,s[84:87],s46 offen offset:0 lds\nM v[64:67],a[128:131],v[80:83],v[64:67]\n" \
"s_add_u32 m0,s22,148352\nds_read_b128 v[76:79],v4 offset:38912\nv_lshl_add_u32 v113,v30,1,v31\nM v[64:67],a[136:139],v[88:91],v[64:67]\ns_sub_u32 s59,3,s60\nv_lshrrev_b32 v31,1,v31\nv_add3_u32 v115,v30,v31,s22\nM v[64:67],a[144:147],v[60:63],v[64:67]\n" \
"s_waitcnt lgkmcnt(3)\ns_lshl_b32 s95,s58,5\nv_bfe_u32 v30,v4,7,2\nv_xor_b32 v114,0x20000,v114\nM v[64:67],a[152:155],v[72:75],v[64:67]\nbuffer_load_dwordx4 v1,s[84:87],s46 offen offset:128 lds\nM v[68:71],a[156:159],v[72:75],v[68:71]\ns_mov_b32 s46,1638400\n" \
"s_add_u32 m0,s22,149504\nds_read_b128 v[72:75],v4 offset:39936\nv_xor_b32 v115,0x20000,v115\nM v[68:71],a[148:151],v[60:63],v[68:71]\ns_and_b32 s59,s59,3\nds_read_b128 v[60:63],v4 offset:45056\nv_lshrrev_b32 v190,4,v4\nM v[68:71],a[140:143],v[88:91],v[68:71]\n" \
"s_mul_i32 s58,s58,12288\nds_read_b128 v[88:91],v4 offset:46080\nM v[68:71],a[132:135],v[80:83],v[68:71]\ns_waitcnt lgkmcnt(3)\nbuffer_load_dwordx4 v1,s[84:87],s46 offen offset:0 lds\nM v[48:51],a[160:163],v[92:95],v[48:51]\ns_add_u32 m0,s22,150400\nds_read_b128 v[80:83],v4 offset:47104\n" \
"M v[48:51],a[168:171],v[84:87],v[48:51]\ns_waitcnt vmcnt(15) lgkmcnt(15)\nM v[48:51],a[176:179],v[76:79],v[48:51]\ns_lshl_b32 s59,s59,11\ns_barrier\ns_waitcnt lgkmcnt(3)\nds_read_b128 v[176:179],v4 offset:27648\nM v[48:51],a[184:187],v[72:75],v[48:51]\n" \
"buffer_load_dwordx4 v1,s[84:87],s46 offen offset:128 lds\nM v[52:55],a[188:191],v[72:75],v[52:55]\ns_add_u32 m0,s22,65536\ns_mov_b32 s46,1703936\nds_read_b128 v[72:75],v4 offset:48128\nM v[52:55],a[180:183],v[76:79],v[52:55]\ns_add_u32 s59,s59,s58\nds_read_b128 v[76:79],v4 offset:16384\n" \
"M v[52:55],a[172:175],v[84:87],v[52:55]\ns_add_u32 s59,s59,109051904\nds_read_b128 v[84:87],v4 offset:17408\nM v[52:55],a[164:167],v[92:95],v[52:55]\ns_waitcnt lgkmcnt(4)\nbuffer_load_dwordx4 v1,s[84:87],s46 offen offset:0 lds\nM v[64:67],a[160:163],v[60:63],v[64:67]\ns_add_u32 m0,s22,66432\n" \
"ds_read_b128 v[92:95],v4 offset:18432\nM v[64:67],a[168:171],v[88:91],v[64:67]\ns_cmp_eq_u32 s60,0\ns_cselect_b32 s40,0x40000000,s59\nds_read_b128 v[180:183],v4 offset:20480\nM v[64:67],a[176:179],v[80:83],v[64:67]\ns_waitcnt lgkmcnt(4)\ns_sub_u32 s59,4,s60\n" \
"M v[64:67],a[184:187],v[72:75],v[64:67]\nbuffer_load_dwordx4 v1,s[84:87],s46 offen offset:128 lds\nM v[68:71],a[188:191],v[72:75],v[68:71]\ns_add_u32 m0,s22,67584\ns_mov_b32 s46,1769472\nds_read_b128 v[72:75],v4 offset:19456\nM v[68:71],a[180:183],v[80:83],v[68:71]\ns_and_b32 s59,s59,3\n" \
"ds_read_b128 v[80:83],v4 offset:24576\nM v[68:71],a[172:175],v[88:91],v[68:71]\ns_lshl_b32 s59,s59,11\nds_read_b128 v[88:91],v4 offset:25600\nM v[68:71],a[164:167],v[60:63],v[68:71]\ns_waitcnt lgkmcnt(3)\nbuffer_load_dwordx4 v1,s[84:87],s46 offen offset:0 lds\nM v[32:35],a[128:131],v[76:79],v[32:35]\n" \
"s_add_u32 m0,s22,68480\nds_read_b128 v[60:63],v4 offset:26624\nM v[32:35],a[136:139],v[84:87],v[32:35]\ns_add_u32 s59,s59,s58\nM v[32:35],a[144:147],v[92:95],v[32:35]\ns_waitcnt lgkmcnt(3)\ns_add_u32 s59,s59,109150208\nM v[32:35],a[152:155],v[72:75],v[32:35]\n" \
"buffer_load_dwordx4 v1,s[84:87],s46 offen offset:128 lds\nM v[36:39],a[156:159],v[72:75],v[36:39]\ns_add_u32 m0,s22,98304\nds_read_b128 v[72:75],v4 offset:21504\nM v[36:39],a[148:151],v[92:95],v[36:39]\ns_mov_b32 s46,1835008\nds_read_b128 v[92:95],v4 offset:22528\nM v[36:39],a[140:143],v[84:87],v[36:39]\n" \
"s_cmp_eq_u32 s60,1\ns_cselect_b32 s41,0x40000000,s59\nds_read_b128 v[84:87],v4 offset:23552\nM v[36:39],a[132:135],v[76:79],v[36:39]\ns_waitcnt lgkmcnt(3)\nbuffer_load_dwordx4 v2,s[36:39],s47 offen offset:0 sc1 lds\nM v[40:43],a[128:131],v[80:83],v[40:43]\ns_sub_u32 s59,5,s60\n" \
"ds_read_b128 v[76:79],v4 offset:28672\nM v[40:43],a[136:139],v[88:91],v[40:43]\ns_and_b32 s59,s59,3\nM v[40:43],a[144:147],v[60:63],v[40:43]\ns_lshl_b32 s59,s59,11\nM v[40:43],a[152:155],v[176:179],v[40:43]\nbuffer_load_dwordx4 v2,s[36:39],s47 offen offset:1024 sc1 lds\nM v[44:47],a[156:159],v[176:179],v[44:47]\n" \
"s_add_u32 s59,s59,s58\nds_read_b128 v[176:179],v4 offset:29696\nM v[44:47],a[148:151],v[60:63],v[44:47]\ns_waitcnt vmcnt(16) lgkmcnt(13)\nds_read_b128 v[60:63],v4 offset:30720\nM v[44:47],a[140:143],v[88:91],v[44:47]\ns_add_u32 s59,s59,109248512\ns_barrier\n" \
"ds_read_b128 v[88:91],v4 offset:31744\nM v[44:47],a[132:135],v[80:83],v[44:47]\nbuffer_load_dwordx4 v2,s[36:39],s47 offen offset:2048 sc1 lds\nM v[32:35],a[160:163],v[180:183],v[32:35]\ns_cmp_eq_u32 s60,2\ns_cselect_b32 s42,0x40000000,s59\ns_waitcnt lgkmcnt(3)\nds_read_b128 v[80:83],v4 offset:49152\n" \
"M v[32:35],a[168:171],v[72:75],v[32:35]\ns_sub_u32 s59,6,s60\nM v[32:35],a[176:179],v[92:95],v[32:35]\ns_and_b32 s59,s59,3\nM v[32:35],a[184:187],v[84:87],v[32:35]\nbuffer_load_dwordx4 v2,s[36:39],s47 offen offset:3072 sc1 lds\nM v[36:39],a[188:191],v[84:87],v[36:39]\ns_add_u32 m0,s22,81920\n" \
"s_add_u32 s47,s31,16384\nds_read_b128 v[84:87],v4 offset:50176\nM v[36:39],a[180:183],v[92:95],v[36:39]\ns_lshl_b32 s59,s59,11\nds_read_b128 v[92:95],v4 offset:51200\nM v[36:39],a[172:175],v[72:75],v[36:39]\ns_add_u32 s59,s59,s58\nds_read_b128 v[72:75],v4 offset:52224\n" \
"M v[36:39],a[164:167],v[180:183],v[36:39]\nbuffer_load_dwordx4 v2,s[36:39],s47 offen offset:0 sc1 lds\nM v[40:43],a[160:163],v[76:79],v[40:43]\ns_waitcnt lgkmcnt(3)\ns_add_u32 s59,s59,109346816\nds_read_b128 v[180:183],v4 offset:57344\nM v[40:43],a[168:171],v[176:179],v[40:43]\ns_and_b32 s58,s61,6\n" \
"M v[40:43],a[176:179],v[60:63],v[40:43]\ns_cmp_eq_u32 s60,3\ns_cselect_b32 s43,0x40000000,s59\nM v[40:43],a[184:187],v[88:91],v[40:43]\nbuffer_load_dwordx4 v2,s[36:39],s47 offen offset:1024 sc1 lds\nM v[44:47],a[188:191],v[88:91],v[44:47]\ns_add_u32 s58,s58,s61\nds_read_b128 v[88:91],v4 offset:58368\n" \
"M v[44:47],a[180:183],v[60:63],v[44:47]\ns_lshl_b32 s59,s60,4\nds_read_b128 v[60:63],v4 offset:59392\nM v[44:47],a[172:175],v[176:179],v[44:47]\ns_add_u32 s92,s58,s59\nds_read_b128 v[176:179],v4 offset:60416\nM v[44:47],a[164:167],v[76:79],v[44:47]\nbuffer_load_dwordx4 v2,s[36:39],s47 offen offset:2048 sc1 lds\n" \
"M v[192:195],a[128:131],v[80:83],v[192:195]\ns_waitcnt lgkmcnt(3)\ns_and_b32 s58,s60,2\nds_read_b128 v[76:79],v4 offset:53248\nM v[192:195],a[136:139],v[84:87],v[192:195]\ns_add_u32 s58,s58,s60\nM v[192:195],a[144:147],v[92:95],v[192:195]\ns_lshl_b32 s59,s61,3\n" \
"M v[192:195],a[152:155],v[72:75],v[192:195]\nbuffer_load_dwordx4 v2,s[36:39],s47 offen offset:3072 sc1 lds\nM v[196:199],a[156:159],v[72:75],v[196:199]\ns_add_u32 m0,s22,114688\nds_read_b128 v[72:75],v4 offset:54272\nM v[196:199],a[148:151],v[92:95],v[196:199]\ns_add_u32 s47,s28,24576\nds_read_b128 v[92:95],v4 offset:55296\n" \
"M v[196:199],a[140:143],v[84:87],v[196:199]\ns_lshr_b32 s63,%1,5\nds_read_b128 v[84:87],v4 offset:56320\nM v[196:199],a[132:135],v[80:83],v[196:199]\nbuffer_load_dwordx4 v1,s[84:87],s46 offen offset:0 lds\nM v[200:203],a[128:131],v[180:183],v[200:203]\ns_waitcnt lgkmcnt(3)\ns_add_u32 m0,s22,115584\n" \
"ds_read_b128 v[80:83],v4 offset:61440\nM v[200:203],a[136:139],v[88:91],v[200:203]\ns_add_u32 s58,s58,s59\nM v[200:203],a[144:147],v[60:63],v[200:203]\ns_lshl_b32 s63,s63,2\nM v[200:203],a[152:155],v[176:179],v[200:203]\nbuffer_load_dwordx4 v1,s[84:87],s46 offen offset:128 lds\nM v[204:207],a[156:159],v[176:179],v[204:207]\n" \
"s_add_u32 m0,s22,116736\nds_read_b128 v[176:179],v4 offset:62464\nM v[204:207],a[148:151],v[60:63],v[204:207]\ns_waitcnt vmcnt(18) lgkmcnt(14)\nds_read_b128 v[60:63],v4 offset:63488\nM v[204:207],a[140:143],v[88:91],v[204:207]\ns_mov_b32 s46,1900544\ns_barrier\n" \
"ds_read_b128 v[88:91],v4 offset:64512\nM v[204:207],a[132:135],v[180:183],v[204:207]\nbuffer_load_dwordx4 v1,s[84:87],s46 offen offset:0 lds\nM v[192:195],a[160:163],v[76:79],v[192:195]\ns_waitcnt lgkmcnt(3)\ns_add_u32 m0,s22,117632\nds_read_b128 v[180:183],v23 offset:0\nM v[192:195],a[168:171],v[72:75],v[192:195]\n" \
"s_add_u32 s93,s58,64\nM v[192:195],a[176:179],v[92:95],v[192:195]\ns_add_u32 s63,s63,%2\nM v[192:195],a[184:187],v[84:87],v[192:195]\ns_waitcnt vmcnt(21)\nM v[196:199],a[188:191],v[84:87],v[196:199]\nbuffer_store_dwordx2 v[28:29],v56,s[36:39],s50 offen offset:0 sc1\nM v[196:199],a[180:183],v[92:95],v[196:199]\n" \
"s_lshl_b32 s58,s92,15\nds_read_b128 v[84:87],v23 offset:1024\nds_read_b128 v[92:95],v23 offset:2048\nM v[196:199],a[172:175],v[72:75],v[196:199]\nbuffer_load_dwordx4 v1,s[84:87],s46 offen offset:128 lds\nM v[196:199],a[164:167],v[76:79],v[196:199]\ns_add_u32 m0,s22,0\nM v[200:203],a[160:163],v[80:83],v[200:203]\n" \
"s_waitcnt lgkmcnt(2)\ns_lshl_b32 s59,s63,6\nds_read_b128 v[72:75],v23 offset:3072\nM v[200:203],a[168:171],v[176:179],v[200:203]\ns_add_u32 s80,s58,s59\nds_read_b128 v[76:79],v23 offset:8192\nM v[200:203],a[176:179],v[60:63],v[200:203]\nbuffer_load_dwordx4 v2,s[36:39],s47 offen offset:0 sc1 lds\n" \
"M v[200:203],a[184:187],v[88:91],v[200:203]\ns_and_b32 s58,s58,0x3e0000\nM v[204:207],a[188:191],v[88:91],v[204:207]\ns_lshl_b32 s59,s63,12\nds_read_b128 v[88:91],v23 offset:9216\nM v[204:207],a[180:183],v[60:63],v[204:207]\ns_add_u32 s58,s58,s59\nds_read_b128 v[60:63],v23 offset:10240\n" \
"M v[204:207],a[172:175],v[176:179],v[204:207]\nbuffer_load_dwordx4 v2,s[36:39],s47 offen offset:1024 sc1 lds\nM v[204:207],a[164:167],v[80:83],v[204:207]\ns_and_b32 s59,s92,1\nds_read_b128 v[176:179],v23 offset:11264\nM v[208:211],a[0:3],v[180:183],0\ns_waitcnt lgkmcnt(3)\ns_lshl_b32 s59,s59,6\n" \
"ds_read_b128 v[80:83],v23 offset:4096\nM v[208:211],a[8:11],v[84:87],v[208:211]\ns_add_u32 s58,s58,s59\nM v[208:211],a[16:19],v[92:95],v[208:211]\nbuffer_load_dwordx4 v2,s[36:39],s47 offen offset:2048 sc1 lds\nM v[208:211],a[24:27],v[72:75],v[208:211]\ns_add_u32 s82,s58,67108864\nM v[212:215],a[28:31],v[72:75],0\n" \
"s_lshl_b32 s58,s93,15\nds_read_b128 v[72:75],v23 offset:5120\nM v[212:215],a[20:23],v[92:95],v[212:215]\ns_lshl_b32 s59,s63,6\nds_read_b128 v[92:95],v23 offset:6144\nM v[212:215],a[12:15],v[84:87],v[212:215]\nbuffer_load_dwordx4 v2,s[36:39],s47 offen offset:3072 sc1 lds\nM v[212:215],a[4:7],v[180:183],v[212:215]\n" \
"s_add_u32 m0,s22,32768\ns_add_u32 s47,s29,24576\nds_read_b128 v[84:87],v23 offset:7168\nM v[216:219],a[0:3],v[76:79],0\ns_waitcnt lgkmcnt(3)\ns_add_u32 s81,s58,s59\nds_read_b128 v[180:183],v23 offset:12288\nM v[216:219],a[8:11],v[88:91],v[216:219]\n" \
"s_and_b32 s58,s58,0x3e0000\nM v[216:219],a[16:19],v[60:63],v[216:219]\nbuffer_load_dwordx4 v2,s[36:39],s47 offen offset:0 sc1 lds\nM v[216:219],a[24:27],v[176:179],v[216:219]\ns_lshl_b32 s59,s63,12\nM v[220:223],a[28:31],v[176:179],0\ns_add_u32 s58,s58,s59\nds_read_b128 v[176:179],v23 offset:13312\n" \
"M v[220:223],a[20:23],v[60:63],v[220:223]\ns_waitcnt vmcnt(10) lgkmcnt(14)\nds_read_b128 v[60:63],v23 offset:14336\nM v[220:223],a[12:15],v[88:91],v[220:223]\nbuffer_load_dwordx4 v2,s[36:39],s47 offen offset:1024 sc1 lds\ns_barrier\nM v[220:223],a[4:7],v[76:79],v[220:223]\ns_and_b32 s59,s93,1\n" \
"ds_read_b128 v[88:91],v23 offset:15360\nM v[208:211],a[32:35],v[80:83],v[208:211]\ns_waitcnt lgkmcnt(3)\ns_lshl_b32 s59,s59,6\nds_read_b128 v[76:79],v20 offset:32768\nM v[208:211],a[40:43],v[72:75],v[208:211]\ns_add_u32 s58,s58,s59\nds_read_b64_tr_b16 a[192:193],v6 offset:16384\n" \
"M v[208:211],a[48:51],v[92:95],v[208:211]\nbuffer_load_dwordx4 v2,s[36:39],s47 offen offset:2048 sc1 lds\nM v[208:211],a[56:59],v[84:87],v[208:211]\ns_add_u32 s83,s58,67108864\nds_read_b64_tr_b16 a[194:195],v7 offset:16384\nM v[212:215],a[60:63],v[84:87],v[212:215]\ns_lshl_b32 s58,s61,10\nds_read_b128 v[84:87],v20 offset:33792\n" \
"M v[212:215],a[52:55],v[92:95],v[212:215]\ns_lshl_b32 s59,s60,6\nds_read_b128 v[92:95],v20 offset:34816\nM v[212:215],a[44:47],v[72:75],v[212:215]\nbuffer_load_dwordx4 v2,s[36:39],s47 offen offset:3072 sc1 lds\nM v[212:215],a[36:39],v[80:83],v[212:215]\ns_add_u32 m0,s22,16384\nds_read_b128 v[72:75],v20 offset:35840\n" \
"M v[216:219],a[32:35],v[180:183],v[216:219]\ns_waitcnt lgkmcnt(4)\ns_add_u32 s47,s30,24576\nds_read_b128 v[80:83],v20 offset:40960\nM v[216:219],a[40:43],v[176:179],v[216:219]\ns_add_u32 s58,s58,s59\nds_read_b64_tr_b16 a[200:201],v6 offset:18432\nv_xor_b32 v6,0x30000,v6\n" \
"M v[216:219],a[48:51],v[60:63],v[216:219]\nbuffer_load_dwordx4 v2,s[36:39],s47 offen offset:0 sc1 lds\nM v[216:219],a[56:59],v[88:91],v[216:219]\ns_add_u32 s95,s95,94437376\nM v[220:223],a[60:63],v[88:91],v[220:223]\ns_add_u32 s58,s58,94396416\nds_read_b128 v[88:91],v20 offset:41984\nv_lshl_add_u32 v58,v30,8,s58\n" \
"M v[220:223],a[52:55],v[60:63],v[220:223]\ns_mov_b64 s[64:65],s[12:13]\nds_read_b128 v[60:63],v20 offset:43008\nv_bfe_u32 v30,v4,4,3\nM v[220:223],a[44:47],v[176:179],v[220:223]\nbuffer_load_dwordx4 v2,s[36:39],s47 offen offset:1024 sc1 lds\nM v[220:223],a[36:39],v[180:183],v[220:223]\ns_add_u32 s58,s95,24\n" \
"ds_read_b128 v[176:179],v20 offset:44032\nv_lshl_add_u32 v58,v30,3,v58\nM v[208:211],a[64:67],v[76:79],v[208:211]\ns_waitcnt lgkmcnt(3)\ns_and_b32 s65,s65,0xffff\nds_read_b128 v[180:183],v20 offset:36864\nv_and_b32 v30,0x30,v4\nM v[208:211],a[72:75],v[84:87],v[208:211]\n" \
"s_mov_b32 s66,4194304\nds_read_b64_tr_b16 a[196:197],v8 offset:16384\ns_mov_b32 exec_lo,0\nv_lshl_add_u32 v58,v30,4,s58\ns_mov_b32 exec_lo,-1\ns_nop 1\nM v[208:211],a[80:83],v[92:95],v[208:211]\ns_mov_b32 s67,0x00020000\n" \
"ds_read_b64_tr_b16 a[202:203],v7 offset:18432\nv_xor_b32 v7,0x30000,v7\nM v[208:211],a[88:91],v[72:75],v[208:211]\nbuffer_load_dwordx4 v2,s[36:39],s47 offen offset:2048 sc1 lds\nM v[212:215],a[92:95],v[72:75],v[212:215]\ns_mov_b32 s58,0x300003\nds_read_b128 v[72:75],v20 offset:37888\nv_and_b32 v30,16,v4\n" \
"M v[212:215],a[84:87],v[92:95],v[212:215]\ns_mov_b32 s59,0x30000300\nds_read_b128 v[92:95],v20 offset:38912\nv_mov_b32 v31,0x3f80\nM v[212:215],a[76:79],v[84:87],v[212:215]\ns_lshl_b32 s92,s92,6\nds_read_b128 v[84:87],v20 offset:39936\nv_lshlrev_b32 v30,v30,v31\n" \
"M v[212:215],a[68:71],v[76:79],v[212:215]\nbuffer_load_dwordx4 v2,s[36:39],s47 offen offset:3072 sc1 lds\nM v[216:219],a[64:67],v[80:83],v[216:219]\ns_waitcnt lgkmcnt(3)\ns_add_u32 m0,s22,49152\nv_cndmask_b32 v186,0,v30,s[58:59]\nv_mov_b32 v188,0\nM v[216:219],a[72:75],v[88:91],v[216:219]\n" \
"s_add_u32 s47,s31,24576\nds_read_b128 v[76:79],v20 offset:45056\nv_mov_b32 v189,0\nM v[216:219],a[80:83],v[60:63],v[216:219]\ns_mov_b32 s58,0xc0000c\nds_read_b64_tr_b16 a[198:199],v9 offset:16384\nds_read_b64_tr_b16 a[204:205],v8 offset:18432\nM v[216:219],a[88:91],v[176:179],v[216:219]\n" \
"s_mov_b32 s59,0xc0000c00\nds_read_b64_tr_b16 a[206:207],v9 offset:18432\nv_xor_b32 v8,0x30000,v8\nM v[220:223],a[92:95],v[176:179],v[220:223]\nbuffer_load_dwordx4 v2,s[36:39],s47 offen offset:0 sc1 lds\nM v[220:223],a[84:87],v[60:63],v[220:223]\ns_add_u32 s92,s92,71303168\nds_read_b128 v[176:179],v20 offset:46080\n" \
"v_xor_b32 v9,0x30000,v9\nM v[220:223],a[76:79],v[88:91],v[220:223]\ns_lshl_b32 s93,s93,6\nds_read_b128 v[60:63],v20 offset:47104\nv_cndmask_b32 v187,0,v30,s[58:59]\nM v[220:223],a[68:71],v[80:83],v[220:223]\ns_add_u32 s58,s60,1\nds_read_b128 v[88:91],v20 offset:48128\n" \
"v_add_u32 v191,s58,v190\nM v[208:211],a[96:99],v[180:183],v[208:211]\ns_waitcnt lgkmcnt(4)\nbuffer_load_dwordx4 v2,s[36:39],s47 offen offset:1024 sc1 lds\nM v[208:211],a[104:107],v[72:75],v[208:211]\ns_add_u32 s93,s93,71303168\nv_and_b32 v191,3,v191\nv_lshlrev_b32 v191,8,v191\n" \
"M v[208:211],a[112:115],v[92:95],v[208:211]\ns_lshl_b32 s58,s60,8\nds_read_b128 v[80:83],v20 offset:16384\nv_lshl_add_u32 v191,v190,3,v191\nM v[208:211],a[120:123],v[84:87],v[208:211]\ns_add_u32 s58,s58,s95\nds_read_b64_tr_b16 a[208:209],v6 offset:0\nv_add_u32 v190,1,v190\n" \
"M v[212:215],a[124:127],v[84:87],v[212:215]\ns_sub_u32 s49,s72,8192\nds_read_b128 v[84:87],v20 offset:17408\nv_lshrrev_b32 v190,2,v190\nM v[212:215],a[116:119],v[92:95],v[212:215]\nbuffer_load_dwordx4 v2,s[36:39],s47 offen offset:2048 sc1 lds\nM v[212:215],a[108:111],v[72:75],v[212:215]\ns_add_u32 s63,s49,0\n" \
"ds_read_b128 v[92:95],v20 offset:18432\nv_lshl_add_u32 v191,v190,27,v191\nM v[212:215],a[100:103],v[180:183],v[212:215]\ns_add_u32 s59,s80,65536\nds_read_b128 v[72:75],v20 offset:19456\nv_bfe_u32 v190,v4,4,2\nM v[216:219],a[96:99],v[76:79],v[216:219]\ns_waitcnt lgkmcnt(4)\n" \
"ds_read_b128 v[180:183],v20 offset:24576\nv_lshl_add_u32 v190,v190,3,s58\nM v[216:219],a[104:107],v[176:179],v[216:219]\nbuffer_load_dwordx4 v2,s[36:39],s47 offen offset:3072 sc1 lds\nM v[216:219],a[112:115],v[60:63],v[216:219]\ns_add_u32 m0,s22,131072\nv_mov_b32 v184,0\nv_mov_b32 v185,0\n" \
"M v[216:219],a[120:123],v[88:91],v[216:219]\ns_add_u32 s47,s80,0\nds_read_b64_tr_b16 a[210:211],v7 offset:0\nds_read_b64_tr_b16 a[212:213],v8 offset:0\nM v[220:223],a[124:127],v[88:91],v[220:223]\ns_add_u32 s58,s80,0\nds_read_b128 v[88:91],v20 offset:25600\nM v[220:223],a[116:119],v[60:63],v[220:223]\n" \
"ds_read_b128 v[60:63],v20 offset:26624\nM v[220:223],a[108:111],v[176:179],v[220:223]\nds_read_b128 v[176:179],v20 offset:27648\nM v[220:223],a[100:103],v[76:79],v[220:223]\nds_read_b64_tr_b16 a[214:215],v9 offset:0\nM v[208:211],a[128:131],v[80:83],v[208:211]\ns_waitcnt lgkmcnt(6)\nds_read_b64_tr_b16 a[216:217],v6 offset:2048\n" \
"M v[208:211],a[136:139],v[84:87],v[208:211]\nds_read_b64_tr_b16 a[218:219],v7 offset:2048\nds_read_b64_tr_b16 a[220:221],v8 offset:2048\nM v[208:211],a[144:147],v[92:95],v[208:211]\nds_read_b64_tr_b16 a[222:223],v9 offset:2048\nM v[208:211],a[152:155],v[72:75],v[208:211]\nds_read_b128 v[76:79],v20 offset:20480\nM v[212:215],a[156:159],v[72:75],v[212:215]\n" \
"ds_read_b128 v[72:75],v20 offset:21504\nM v[212:215],a[148:151],v[92:95],v[212:215]\nds_read_b128 v[92:95],v20 offset:22528\nM v[212:215],a[140:143],v[84:87],v[212:215]\nds_read_b128 v[84:87],v20 offset:23552\nM v[212:215],a[132:135],v[80:83],v[212:215]\nds_read_b128 v[80:83],v20 offset:28672\nM v[216:219],a[128:131],v[180:183],v[216:219]\n" \
"LG23_%=:\ns_cbranch_execz LG25_%=\ns_waitcnt lgkmcnt(5)\nM v[216:219],a[136:139],v[88:91],v[216:219]\nM v[216:219],a[144:147],v[60:63],v[216:219]\nM v[216:219],a[152:155],v[176:179],v[216:219]\nM v[220:223],a[156:159],v[176:179],v[220:223]\ns_waitcnt lgkmcnt(1)\n" \
"ds_read_b128 v[176:179],v20 offset:29696\nM v[220:223],a[148:151],v[60:63],v[220:223]\ns_waitcnt vmcnt(8) lgkmcnt(6)\nds_read_b128 v[60:63],v20 offset:30720\nM v[220:223],a[140:143],v[88:91],v[220:223]\ns_barrier\nds_read_b128 v[88:91],v20 offset:31744\nM v[220:223],a[132:135],v[180:183],v[220:223]\n" \
"buffer_load_dwordx4 v112,s[64:67],s47 offen offset:0 lds\nM v[208:211],a[160:163],v[76:79],v[208:211]\ns_add_u32 m0,s22,132096\nds_read_b128 v[180:183],v4 offset:0\nM v[208:211],a[168:171],v[72:75],v[208:211]\ns_add_u32 s47,s80,65536\nds_read_b64_tr_b16 a[224:225],v6 offset:49152\nM v[208:211],a[176:179],v[92:95],v[208:211]\n" \
"ds_read_b64_tr_b16 a[226:227],v7 offset:49152\nds_read_b64_tr_b16 a[232:233],v6 offset:51200\nM v[208:211],a[184:187],v[84:87],v[208:211]\nbuffer_load_dwordx4 v112,s[64:67],s47 offen offset:0 lds\nM v[212:215],a[188:191],v[84:87],v[212:215]\ns_add_u32 m0,s22,133120\nM v[212:215],a[180:183],v[92:95],v[212:215]\ns_add_u32 s47,s81,0\n" \
"ds_read_b128 v[84:87],v4 offset:1024\nM v[212:215],a[172:175],v[72:75],v[212:215]\ns_waitcnt lgkmcnt(4)\nds_read_b128 v[92:95],v4 offset:2048\nM v[212:215],a[164:167],v[76:79],v[212:215]\nbuffer_load_dwordx4 v112,s[64:67],s47 offen offset:0 lds\nM v[216:219],a[160:163],v[80:83],v[216:219]\ns_add_u32 m0,s22,134144\n" \
"ds_read_b128 v[72:75],v4 offset:3072\nM v[216:219],a[168:171],v[176:179],v[216:219]\ns_add_u32 s47,s81,65536\nds_read_b128 v[76:79],v4 offset:8192\nM v[216:219],a[176:179],v[60:63],v[216:219]\nds_read_b64_tr_b16 a[234:235],v7 offset:51200\nM v[216:219],a[184:187],v[88:91],v[216:219]\nbuffer_load_dwordx4 v112,s[64:67],s47 offen offset:0 lds\n" \
"M v[220:223],a[188:191],v[88:91],v[220:223]\ns_add_u32 m0,s22,147456\nds_read_b128 v[88:91],v4 offset:9216\nM v[220:223],a[180:183],v[60:63],v[220:223]\ns_add_u32 s47,s32,0\nds_read_b128 v[60:63],v4 offset:10240\nM v[220:223],a[172:175],v[176:179],v[220:223]\ns_waitcnt lgkmcnt(4)\n" \
"ds_read_b128 v[176:179],v4 offset:11264\nM v[220:223],a[164:167],v[80:83],v[220:223]\nbuffer_load_dwordx4 v113,s[36:39],s82 offen offset:0 sc1 lds\nM v[48:51],a[192:195],v[180:183],v[48:51]\ns_add_u32 m0,s22,146432\nM v[48:51],a[200:203],v[84:87],v[48:51]\nds_read_b128 v[80:83],v4 offset:4096\nM v[48:51],a[208:211],v[92:95],v[48:51]\n" \
"ds_read_b64_tr_b16 a[236:237],v8 offset:51200\nM v[48:51],a[216:219],v[72:75],v[48:51]\nbuffer_load_dwordx4 v113,s[36:39],s82 offen offset:2048 sc1 lds\nM v[52:55],a[220:223],v[72:75],v[52:55]\ns_add_u32 m0,s22,149504\nds_read_b128 v[72:75],v4 offset:5120\nM v[52:55],a[212:215],v[92:95],v[52:55]\nds_read_b64_tr_b16 a[238:239],v9 offset:51200\n" \
"M v[52:55],a[204:207],v[84:87],v[52:55]\ns_waitcnt lgkmcnt(4)\nds_read_b64_tr_b16 a[228:229],v8 offset:49152\nM v[52:55],a[196:199],v[180:183],v[52:55]\nbuffer_load_dwordx2 v[10:11],v58,s[36:39],0 offen sc1\nM v[64:67],a[192:195],v[76:79],v[64:67]\nds_read_b128 v[92:95],v4 offset:6144\nds_read_b64_tr_b16 a[230:231],v9 offset:49152\n" \
"M v[64:67],a[200:203],v[88:91],v[64:67]\nbuffer_load_dwordx4 v113,s[36:39],s83 offen offset:0 sc1 lds\nM v[64:67],a[208:211],v[60:63],v[64:67]\ns_add_u32 m0,s22,148480\nM v[64:67],a[216:219],v[176:179],v[64:67]\nds_read_b128 v[84:87],v4 offset:7168\nM v[68:71],a[220:223],v[176:179],v[68:71]\nds_read_b128 v[180:183],v4 offset:12288\n" \
"M v[68:71],a[212:215],v[60:63],v[68:71]\nbuffer_load_dwordx4 v113,s[36:39],s83 offen offset:2048 sc1 lds\nM v[68:71],a[204:207],v[88:91],v[68:71]\ns_add_u32 m0,s22,65536\nds_read_b128 v[176:179],v4 offset:13312\nM v[68:71],a[196:199],v[76:79],v[68:71]\ns_waitcnt lgkmcnt(3)\nds_read_b128 v[60:63],v4 offset:14336\n" \
"M v[48:51],a[224:227],v[80:83],v[48:51]\nds_read_b128 v[88:91],v4 offset:15360\nM v[48:51],a[232:235],v[72:75],v[48:51]\nbuffer_load_dword v16,v15,s[36:39],s92 offen offset:0 sc1\nM v[48:51],a[240:243],v[92:95],v[48:51]\ns_waitcnt lgkmcnt(3)\nds_read_b128 v[76:79],v4 offset:32768\nM v[48:51],a[248:251],v[84:87],v[48:51]\n" \
"buffer_load_dword v17,v15,s[36:39],s92 offen offset:128 sc1\nM v[52:55],a[252:255],v[84:87],v[52:55]\nds_read_b128 v[84:87],v4 offset:33792\nM v[52:55],a[244:247],v[92:95],v[52:55]\nbuffer_load_dword v18,v15,s[36:39],s93 offen offset:0 sc1\nM v[52:55],a[236:239],v[72:75],v[52:55]\nds_read_b128 v[92:95],v4 offset:34816\nds_read_b128 v[72:75],v4 offset:35840\n" \
"M v[52:55],a[228:231],v[80:83],v[52:55]\nbuffer_load_dword v19,v15,s[36:39],s93 offen offset:128 sc1\nM v[64:67],a[224:227],v[180:183],v[64:67]\ns_waitcnt lgkmcnt(3)\nds_read_b128 v[80:83],v4 offset:40960\nC v48,v48,v49\nM v[64:67],a[232:235],v[176:179],v[64:67]\nC v49,v50,v51\n" \
"C v50,v52,v53\nM v[64:67],a[240:243],v[60:63],v[64:67]\nC v51,v54,v55\nv_cndmask_b32 v120,v120,v48,s[96:97]\nM v[64:67],a[248:251],v[88:91],v[64:67]\nv_cndmask_b32 v121,v121,v49,s[96:97]\nv_cndmask_b32 v122,v122,v50,s[96:97]\nM v[68:71],a[252:255],v[88:91],v[68:71]\n" \
"ds_read_b128 v[88:91],v4 offset:41984\nv_cndmask_b32 v123,v123,v51,s[96:97]\nM v[68:71],a[244:247],v[60:63],v[68:71]\nds_read_b128 v[60:63],v4 offset:43008\nC v64,v64,v65\nM v[68:71],a[236:239],v[176:179],v[68:71]\nds_read_b128 v[176:179],v4 offset:44032\nC v65,v66,v67\n" \
"M v[68:71],a[228:231],v[180:183],v[68:71]\nds_read_b128 v[180:183],v4 offset:36864\nv_cndmask_b32 v124,v124,v64,s[96:97]\nM v[32:35],a[192:195],v[76:79],v[32:35]\ns_waitcnt lgkmcnt(4)\nv_cndmask_b32 v125,v125,v65,s[96:97]\nM v[32:35],a[200:203],v[84:87],v[32:35]\nC v66,v68,v69\n" \
"C v67,v70,v71\nM v[32:35],a[208:211],v[92:95],v[32:35]\nv_cndmask_b32 v126,v126,v66,s[96:97]\nv_cndmask_b32 v127,v127,v67,s[96:97]\nM v[32:35],a[216:219],v[72:75],v[32:35]\ns_cmp_eq_u32 s60,1\ns_cselect_b64 s[96:97],-1,0\nM v[36:39],a[220:223],v[72:75],v[36:39]\n" \
"ds_read_b128 v[72:75],v4 offset:37888\nM v[36:39],a[212:215],v[92:95],v[36:39]\nds_read_b128 v[92:95],v4 offset:38912\nM v[36:39],a[204:207],v[84:87],v[36:39]\nds_read_b128 v[84:87],v4 offset:39936\nM v[36:39],a[196:199],v[76:79],v[36:39]\nds_read_b128 v[76:79],v4 offset:45056\nM v[40:43],a[192:195],v[80:83],v[40:43]\n" \
"s_waitcnt lgkmcnt(4)\nM v[40:43],a[200:203],v[88:91],v[40:43]\nM v[40:43],a[208:211],v[60:63],v[40:43]\nM v[40:43],a[216:219],v[176:179],v[40:43]\nM v[44:47],a[220:223],v[176:179],v[44:47]\nds_read_b128 v[176:179],v4 offset:46080\nM v[44:47],a[212:215],v[60:63],v[44:47]\ns_waitcnt vmcnt(13) lgkmcnt(14)\n" \
"ds_read_b128 v[60:63],v4 offset:47104\nM v[44:47],a[204:207],v[88:91],v[44:47]\ns_barrier\nds_read_b128 v[88:91],v4 offset:48128\nM v[44:47],a[196:199],v[80:83],v[44:47]\nds_read_b128 v[80:83],v4 offset:16384\nM v[32:35],a[224:227],v[180:183],v[32:35]\ns_waitcnt lgkmcnt(4)\n" \
"M v[32:35],a[232:235],v[72:75],v[32:35]\nM v[32:35],a[240:243],v[92:95],v[32:35]\nM v[32:35],a[248:251],v[84:87],v[32:35]\nM v[36:39],a[252:255],v[84:87],v[36:39]\ns_waitcnt vmcnt(6)\nv_cmp_gt_u64 s[98:99],s[14:15],v[10:11]\nv_cmp_le_u64 s[18:19],s[16:17],v[10:11]\nM v[36:39],a[244:247],v[92:95],v[36:39]\n" \
"s_or_b64 s[98:99],s[98:99],s[18:19]\ns_cbranch_scc1 LPL_%=\nLPLR_%=:\nbuffer_load_dwordx4 v2,s[36:39],s47 offen offset:0 sc1 lds\nM v[36:39],a[236:239],v[72:75],v[36:39]\nds_read_b128 v[84:87],v4 offset:17408\nC v32,v32,v33\nM v[36:39],a[228:231],v[180:183],v[36:39]\n" \
"ds_read_b128 v[92:95],v4 offset:18432\nC v33,v34,v35\nM v[40:43],a[224:227],v[76:79],v[40:43]\ns_waitcnt lgkmcnt(2)\nds_read_b128 v[72:75],v4 offset:19456\nv_cndmask_b32 v120,v120,v32,s[96:97]\nM v[40:43],a[232:235],v[176:179],v[40:43]\nbuffer_load_dwordx4 v2,s[36:39],s47 offen offset:1024 sc1 lds\n" \
"M v[40:43],a[240:243],v[60:63],v[40:43]\nds_read_b128 v[180:183],v4 offset:24576\nC v34,v36,v37\nM v[40:43],a[248:251],v[88:91],v[40:43]\nC v35,v38,v39\nv_cndmask_b32 v121,v121,v33,s[96:97]\nM v[44:47],a[252:255],v[88:91],v[44:47]\nds_read_b128 v[88:91],v4 offset:25600\n" \
"v_cndmask_b32 v122,v122,v34,s[96:97]\nM v[44:47],a[244:247],v[60:63],v[44:47]\nbuffer_load_dwordx4 v2,s[36:39],s47 offen offset:2048 sc1 lds\nM v[44:47],a[236:239],v[176:179],v[44:47]\nds_read_b128 v[60:63],v4 offset:26624\nC v40,v40,v41\nM v[44:47],a[228:231],v[76:79],v[44:47]\nds_read_b128 v[176:179],v4 offset:27648\n" \
"C v41,v42,v43\nM v[192:195],a[192:195],v[80:83],v[192:195]\ns_waitcnt lgkmcnt(3)\nds_read_b128 v[76:79],v4 offset:20480\nv_cndmask_b32 v123,v123,v35,s[96:97]\nM v[192:195],a[200:203],v[84:87],v[192:195]\nbuffer_load_dwordx4 v2,s[36:39],s47 offen offset:3072 sc1 lds\nM v[192:195],a[208:211],v[92:95],v[192:195]\n" \
"s_add_u32 m0,s22,98304\ns_add_u32 s47,s32,8192\nC v42,v44,v45\nC v43,v46,v47\nM v[192:195],a[216:219],v[72:75],v[192:195]\nv_cndmask_b32 v124,v124,v40,s[96:97]\nv_cndmask_b32 v125,v125,v41,s[96:97]\nM v[196:199],a[220:223],v[72:75],v[196:199]\n" \
"ds_read_b128 v[72:75],v4 offset:21504\nv_cndmask_b32 v126,v126,v42,s[96:97]\nM v[196:199],a[212:215],v[92:95],v[196:199]\nbuffer_load_dwordx4 v2,s[36:39],s47 offen offset:0 sc1 lds\nM v[196:199],a[204:207],v[84:87],v[196:199]\nds_read_b128 v[92:95],v4 offset:22528\nv_cndmask_b32 v127,v127,v43,s[96:97]\nM v[196:199],a[196:199],v[80:83],v[196:199]\n" \
"s_cmp_eq_u32 s60,2\ns_cselect_b64 s[96:97],-1,0\nds_read_b128 v[84:87],v4 offset:23552\nM v[200:203],a[192:195],v[180:183],v[200:203]\ns_waitcnt lgkmcnt(3)\nds_read_b128 v[80:83],v4 offset:28672\nM v[200:203],a[200:203],v[88:91],v[200:203]\nbuffer_load_dwordx4 v2,s[36:39],s47 offen offset:1024 sc1 lds\n" \
"M v[200:203],a[208:211],v[60:63],v[200:203]\nM v[200:203],a[216:219],v[176:179],v[200:203]\nM v[204:207],a[220:223],v[176:179],v[204:207]\nds_read_b128 v[176:179],v4 offset:29696\nM v[204:207],a[212:215],v[60:63],v[204:207]\nbuffer_load_dwordx4 v2,s[36:39],s47 offen offset:2048 sc1 lds\nM v[204:207],a[204:207],v[88:91],v[204:207]\nds_read_b128 v[60:63],v4 offset:30720\n" \
"M v[204:207],a[196:199],v[180:183],v[204:207]\nds_read_b128 v[88:91],v4 offset:31744\nM v[192:195],a[224:227],v[76:79],v[192:195]\ns_waitcnt lgkmcnt(3)\nds_read_b128 v[180:183],v4 offset:49152\nM v[192:195],a[232:235],v[72:75],v[192:195]\nbuffer_load_dwordx4 v2,s[36:39],s47 offen offset:3072 sc1 lds\nM v[192:195],a[240:243],v[92:95],v[192:195]\n" \
"s_add_u32 m0,s22,81920\nM v[192:195],a[248:251],v[84:87],v[192:195]\ns_add_u32 s47,s32,16384\nM v[196:199],a[252:255],v[84:87],v[196:199]\ns_waitcnt vmcnt(8)\nds_read_b128 v[84:87],v4 offset:50176\nv_sub_f32 v24,1.0,v16\nM v[196:199],a[244:247],v[92:95],v[196:199]\n" \
"buffer_store_dwordx4 v[48:51],v4,s[36:39],s40 offen offset:0 sc1\nM v[196:199],a[236:239],v[72:75],v[196:199]\nds_read_b128 v[92:95],v4 offset:51200\nds_read_b128 v[72:75],v4 offset:52224\nM v[196:199],a[228:231],v[76:79],v[196:199]\nds_read_b128 v[76:79],v4 offset:57344\nC v192,v192,v193\nM v[200:203],a[224:227],v[80:83],v[200:203]\n" \
"s_waitcnt lgkmcnt(4)\nC v193,v194,v195\nv_cndmask_b32 v120,v120,v192,s[96:97]\nM v[200:203],a[232:235],v[176:179],v[200:203]\nbuffer_load_dwordx4 v2,s[36:39],s47 offen offset:0 sc1 lds\nM v[200:203],a[240:243],v[60:63],v[200:203]\nC v194,v196,v197\nC v195,v198,v199\n" \
"M v[200:203],a[248:251],v[88:91],v[200:203]\nv_cndmask_b32 v121,v121,v193,s[96:97]\nv_cndmask_b32 v122,v122,v194,s[96:97]\nM v[204:207],a[252:255],v[88:91],v[204:207]\nds_read_b128 v[88:91],v4 offset:58368\nv_cndmask_b32 v123,v123,v195,s[96:97]\nM v[204:207],a[244:247],v[60:63],v[204:207]\nbuffer_load_dwordx4 v2,s[36:39],s47 offen offset:1024 sc1 lds\n" \
"M v[204:207],a[236:239],v[176:179],v[204:207]\nds_read_b128 v[60:63],v4 offset:59392\nC v200,v200,v201\nM v[204:207],a[228:231],v[80:83],v[204:207]\nds_read_b128 v[176:179],v4 offset:60416\nC v201,v202,v203\nM v[208:211],a[192:195],v[180:183],v[208:211]\ns_waitcnt lgkmcnt(3)\n" \
"ds_read_b128 v[80:83],v4 offset:53248\nv_cndmask_b32 v124,v124,v200,s[96:97]\nM v[208:211],a[200:203],v[84:87],v[208:211]\nbuffer_load_dwordx4 v2,s[36:39],s47 offen offset:2048 sc1 lds\nM v[208:211],a[208:211],v[92:95],v[208:211]\nC v202,v204,v205\nC v203,v206,v207\nM v[208:211],a[216:219],v[72:75],v[208:211]\n" \
"v_cndmask_b32 v125,v125,v201,s[96:97]\nv_cndmask_b32 v126,v126,v202,s[96:97]\nM v[212:215],a[220:223],v[72:75],v[212:215]\nds_read_b128 v[72:75],v4 offset:54272\nv_cndmask_b32 v127,v127,v203,s[96:97]\nM v[212:215],a[212:215],v[92:95],v[212:215]\nbuffer_store_dwordx4 v[64:67],v4,s[36:39],s40 offen offset:1024 sc1\nM v[212:215],a[204:207],v[84:87],v[212:215]\n" \
"s_cmp_eq_u32 s60,3\ns_cselect_b64 s[96:97],-1,0\nds_read_b128 v[92:95],v4 offset:55296\nds_read_b128 v[84:87],v4 offset:56320\nM v[212:215],a[196:199],v[180:183],v[212:215]\nds_read_b128 v[180:183],v4 offset:61440\nC v24,v24,v24\nM v[216:219],a[192:195],v[76:79],v[216:219]\n" \
"s_waitcnt lgkmcnt(4)\nv_and_b32 v24,0xffff0000,v24\nv_sub_f32 v25,1.0,v17\nM v[216:219],a[200:203],v[88:91],v[216:219]\nbuffer_load_dwordx4 v2,s[36:39],s47 offen offset:3072 sc1 lds\nM v[216:219],a[208:211],v[60:63],v[216:219]\ns_add_u32 m0,s22,114688\ns_add_u32 s47,s32,24576\n" \
"C v25,v25,v25\nv_and_b32 v25,0xffff0000,v25\nM v[216:219],a[216:219],v[176:179],v[216:219]\nv_sub_f32 v26,1.0,v18\nv_sub_f32 v27,1.0,v19\nM v[220:223],a[220:223],v[176:179],v[220:223]\nds_read_b128 v[176:179],v4 offset:62464\nC v26,v26,v26\n" \
"M v[220:223],a[212:215],v[60:63],v[220:223]\ns_waitcnt vmcnt(6) lgkmcnt(14)\nbuffer_load_dwordx4 v2,s[36:39],s47 offen offset:0 sc1 lds\nM v[220:223],a[204:207],v[88:91],v[220:223]\ns_barrier\nds_read_b128 v[60:63],v4 offset:63488\nM v[220:223],a[196:199],v[76:79],v[220:223]\nds_read_b128 v[88:91],v4 offset:64512\n" \
"C v27,v27,v27\nM v[208:211],a[224:227],v[80:83],v[208:211]\ns_waitcnt lgkmcnt(3)\nds_read_b128 v[76:79],v20 offset:0\nv_and_b32 v26,0xffff0000,v26\nM v[208:211],a[232:235],v[72:75],v[208:211]\nbuffer_load_dwordx4 v2,s[36:39],s47 offen offset:1024 sc1 lds\nM v[208:211],a[240:243],v[92:95],v[208:211]\n" \
"ds_read_b64 v[160:161],v114 offset:0\nv_and_b32 v27,0xffff0000,v27\nM v[208:211],a[248:251],v[84:87],v[208:211]\nds_read_b64_tr_b16 v[96:97],v115 offset:16384\nM v[212:215],a[252:255],v[84:87],v[212:215]\nds_read_b128 v[84:87],v20 offset:1024\nM v[212:215],a[244:247],v[92:95],v[212:215]\nbuffer_load_dwordx4 v2,s[36:39],s47 offen offset:2048 sc1 lds\n" \
"M v[212:215],a[236:239],v[72:75],v[212:215]\nds_read_b128 v[92:95],v20 offset:2048\nC v208,v208,v209\nM v[212:215],a[228:231],v[80:83],v[212:215]\nds_read_b128 v[72:75],v20 offset:3072\nC v209,v210,v211\nM v[216:219],a[224:227],v[180:183],v[216:219]\ns_waitcnt lgkmcnt(4)\n" \
"ds_read_b128 v[80:83],v20 offset:8192\nv_cndmask_b32 v120,v120,v208,s[96:97]\nM v[216:219],a[232:235],v[176:179],v[216:219]\nbuffer_store_dwordx4 v[32:35],v4,s[36:39],s41 offen offset:0 sc1\nM v[216:219],a[240:243],v[60:63],v[216:219]\nds_read_b64 v[162:163],v114 offset:32\nds_read_b64_tr_b16 v[98:99],v115 offset:17408\nM v[216:219],a[248:251],v[88:91],v[216:219]\n" \
"ds_read_b64 v[164:165],v114 offset:1024\nC v210,v212,v213\nM v[220:223],a[252:255],v[88:91],v[220:223]\nds_read_b128 v[88:91],v20 offset:9216\nC v211,v214,v215\nM v[220:223],a[244:247],v[60:63],v[220:223]\nbuffer_load_dwordx4 v2,s[36:39],s47 offen offset:3072 sc1 lds\nM v[220:223],a[236:239],v[176:179],v[220:223]\n" \
"s_add_u32 m0,s22,0\nds_read_b128 v[60:63],v20 offset:10240\nC v216,v216,v217\nM v[220:223],a[228:231],v[180:183],v[220:223]\ns_add_u32 s47,s33,0\nds_read_b128 v[176:179],v20 offset:11264\nC v217,v218,v219\nM v[224:227],a[0:3],v[76:79],0\n" \
"s_waitcnt lgkmcnt(4)\nds_read_b128 v[180:183],v20 offset:4096\nv_lshlrev_b32 v128,16,v160\nM v[224:227],a[8:11],v[84:87],v[224:227]\nbuffer_load_dwordx4 v2,s[36:39],s47 offen offset:0 sc1 lds\nM v[224:227],a[16:19],v[92:95],v[224:227]\nC v218,v220,v221\nC v219,v222,v223\n" \
"M v[224:227],a[24:27],v[72:75],v[224:227]\nds_read_b64_tr_b16 v[100:101],v115 offset:16416\nds_read_b64 v[166:167],v114 offset:1056\nM v[228:231],a[28:31],v[72:75],0\nds_read_b128 v[72:75],v20 offset:5120\nv_and_b32 v129,0xffff0000,v160\nM v[228:231],a[20:23],v[92:95],v[228:231]\nbuffer_store_dwordx4 v[40:43],v4,s[36:39],s41 offen offset:1024 sc1\n" \
"M v[228:231],a[12:15],v[84:87],v[228:231]\nds_read_b128 v[92:95],v20 offset:6144\nds_read_b128 v[84:87],v20 offset:7168\nM v[228:231],a[4:7],v[76:79],v[228:231]\nds_read_b128 v[76:79],v20 offset:12288\nv_lshlrev_b32 v130,16,v161\nM v[232:235],a[0:3],v[80:83],0\ns_waitcnt lgkmcnt(6)\n" \
"ds_read_b64_tr_b16 v[102:103],v115 offset:17440\nv_and_b32 v131,0xffff0000,v161\nM v[232:235],a[8:11],v[88:91],v[232:235]\nbuffer_load_dwordx4 v2,s[36:39],s47 offen offset:1024 sc1 lds\nM v[232:235],a[16:19],v[60:63],v[232:235]\nv_lshlrev_b32 v132,16,v162\nv_and_b32 v133,0xffff0000,v162\nM v[232:235],a[24:27],v[176:179],v[232:235]\n" \
"ds_read_b64 v[168:169],v114 offset:2048\nds_read_b64_tr_b16 v[104:105],v115 offset:18432\nM v[236:239],a[28:31],v[176:179],0\nds_read_b128 v[176:179],v20 offset:13312\nv_lshlrev_b32 v134,16,v163\nM v[236:239],a[20:23],v[60:63],v[236:239]\nbuffer_load_dwordx4 v2,s[36:39],s47 offen offset:2048 sc1 lds\nM v[236:239],a[12:15],v[88:91],v[236:239]\n" \
"v_and_b32 v135,0xffff0000,v163\nv_lshlrev_b32 v136,16,v164\nM v[236:239],a[4:7],v[80:83],v[236:239]\nds_read_b128 v[60:63],v20 offset:14336\nv_and_b32 v137,0xffff0000,v164\nM v[224:227],a[32:35],v[180:183],v[224:227]\ns_waitcnt lgkmcnt(4)\nds_read_b128 v[88:91],v20 offset:15360\n" \
"v_lshlrev_b32 v138,16,v165\nM v[224:227],a[40:43],v[72:75],v[224:227]\nbuffer_load_dwordx4 v2,s[36:39],s47 offen offset:3072 sc1 lds\nM v[224:227],a[48:51],v[92:95],v[224:227]\ns_add_u32 m0,s22,32768\nds_read_b128 v[80:83],v20 offset:32768\nv_and_b32 v139,0xffff0000,v165\nM v[224:227],a[56:59],v[84:87],v[224:227]\n" \
"s_add_u32 s47,s33,8192\nds_read_b64 v[170:171],v114 offset:2080\nv_lshlrev_b32 v140,16,v166\nM v[228:231],a[60:63],v[84:87],v[228:231]\nds_read_b128 v[84:87],v20 offset:33792\nv_and_b32 v141,0xffff0000,v166\nM v[228:231],a[52:55],v[92:95],v[228:231]\nbuffer_load_dwordx4 v2,s[36:39],s47 offen offset:0 sc1 lds\n" \
"M v[228:231],a[44:47],v[72:75],v[228:231]\nds_read_b128 v[92:95],v20 offset:34816\nv_lshlrev_b32 v142,16,v167\nM v[228:231],a[36:39],v[180:183],v[228:231]\nds_read_b128 v[72:75],v20 offset:35840\nv_lshlrev_b32 v160,16,v96\nM v[232:235],a[32:35],v[76:79],v[232:235]\ns_waitcnt lgkmcnt(4)\n" \
"ds_read_b128 v[180:183],v20 offset:40960\nv_and_b32 v143,0xffff0000,v167\nM v[232:235],a[40:43],v[176:179],v[232:235]\nbuffer_store_dwordx4 v[192:195],v4,s[36:39],s42 offen offset:0 sc1\nM v[232:235],a[48:51],v[60:63],v[232:235]\nds_read_b64_tr_b16 v[106:107],v115 offset:19456\nds_read_b64 v[172:173],v114 offset:3072\nM v[232:235],a[56:59],v[88:91],v[232:235]\n" \
"ds_read_b64_tr_b16 v[108:109],v115 offset:18464\nv_lshlrev_b32 v161,16,v97\nM v[236:239],a[60:63],v[88:91],v[236:239]\nds_read_b128 v[88:91],v20 offset:41984\nv_lshlrev_b32 v162,16,v98\nM v[236:239],a[52:55],v[60:63],v[236:239]\nbuffer_load_dwordx4 v2,s[36:39],s47 offen offset:1024 sc1 lds\nM v[236:239],a[44:47],v[176:179],v[236:239]\n" \
"ds_read_b128 v[60:63],v20 offset:43008\nv_lshlrev_b32 v163,16,v99\nM v[236:239],a[36:39],v[76:79],v[236:239]\nds_read_b128 v[176:179],v20 offset:44032\nv_lshlrev_b32 v164,16,v100\nM v[224:227],a[64:67],v[80:83],v[224:227]\ns_waitcnt lgkmcnt(4)\nds_read_b64 v[174:175],v114 offset:3104\n" \
"v_lshlrev_b32 v165,16,v101\nM v[224:227],a[72:75],v[84:87],v[224:227]\nds_read_b64_tr_b16 v[110:111],v115 offset:19488\nv_lshlrev_b32 v166,16,v102\nM v[224:227],a[80:83],v[92:95],v[224:227]\nbuffer_load_dwordx4 v2,s[36:39],s47 offen offset:2048 sc1 lds\nM v[224:227],a[88:91],v[72:75],v[224:227]\nds_read_b128 v[76:79],v20 offset:36864\n" \
"v_lshlrev_b32 v167,16,v103\nM v[228:231],a[92:95],v[72:75],v[228:231]\nds_read_b128 v[72:75],v20 offset:37888\nv_lshlrev_b32 v144,16,v168\nM v[228:231],a[84:87],v[92:95],v[228:231]\ns_waitcnt lgkmcnt(4)\nds_read_b128 v[92:95],v20 offset:38912\nv_and_b32 v96,0xffff0000,v96\n" \
"M v[228:231],a[76:79],v[84:87],v[228:231]\nbuffer_load_dwordx4 v2,s[36:39],s47 offen offset:3072 sc1 lds\nM v[228:231],a[68:71],v[80:83],v[228:231]\ns_add_u32 m0,s22,16384\nv_and_b32 v145,0xffff0000,v168\nv_and_b32 v97,0xffff0000,v97\nM v[232:235],a[64:67],v[180:183],v[232:235]\ns_add_u32 s47,s33,16384\n" \
"ds_read_b128 v[84:87],v20 offset:39936\nv_and_b32 v98,0xffff0000,v98\nM v[232:235],a[72:75],v[88:91],v[232:235]\nds_read_b128 v[80:83],v20 offset:45056\nv_and_b32 v99,0xffff0000,v99\nM v[232:235],a[80:83],v[60:63],v[232:235]\nbuffer_store_dwordx4 v[200:203],v4,s[36:39],s42 offen offset:1024 sc1\nM v[232:235],a[88:91],v[176:179],v[232:235]\n" \
"v_lshlrev_b32 v146,16,v169\nv_and_b32 v147,0xffff0000,v169\nM v[236:239],a[92:95],v[176:179],v[236:239]\nds_read_b128 v[176:179],v20 offset:46080\nv_lshlrev_b32 v148,16,v170\nM v[236:239],a[84:87],v[60:63],v[236:239]\ns_waitcnt vmcnt(11) lgkmcnt(6)\nds_read_b128 v[60:63],v20 offset:47104\n" \
"v_and_b32 v149,0xffff0000,v170\nM v[236:239],a[76:79],v[88:91],v[236:239]\ns_barrier\nds_read_b128 v[88:91],v20 offset:48128\nM v[236:239],a[68:71],v[180:183],v[236:239]\ns_waitcnt lgkmcnt(3)\nds_read_b128 v[180:183],v20 offset:16384\nv_lshlrev_b32 v150,16,v171\n" \
"M v[224:227],a[96:99],v[76:79],v[224:227]\nv_and_b32 v100,0xffff0000,v100\nv_and_b32 v151,0xffff0000,v171\nM v[224:227],a[104:107],v[72:75],v[224:227]\nv_and_b32 v101,0xffff0000,v101\nv_and_b32 v102,0xffff0000,v102\nM v[224:227],a[112:115],v[92:95],v[224:227]\nLG24_%=:\n" \
"s_cbranch_execz LG26_%=\nv_and_b32 v103,0xffff0000,v103\nv_lshlrev_b32 v168,16,v104\nM v[224:227],a[120:123],v[84:87],v[224:227]\nv_mul_f32 v129,v129,v96\nv_mul_f32 v131,v131,v97\nM v[228:231],a[124:127],v[84:87],v[228:231]\nds_read_b128 v[84:87],v20 offset:17408\n" \
"v_mul_f32 v133,v133,v98\nM v[228:231],a[116:119],v[92:95],v[228:231]\nds_read_b128 v[92:95],v20 offset:18432\nv_mul_f32 v135,v135,v99\nM v[228:231],a[108:111],v[72:75],v[228:231]\nds_read_b128 v[72:75],v20 offset:19456\nv_lshlrev_b32 v169,16,v105\nM v[228:231],a[100:103],v[76:79],v[228:231]\n" \
"buffer_load_dwordx4 v2,s[36:39],s47 offen offset:0 sc1 lds\nM v[232:235],a[96:99],v[80:83],v[232:235]\ns_waitcnt lgkmcnt(3)\nds_read_b128 v[76:79],v20 offset:24576\nv_lshlrev_b32 v170,16,v106\nM v[232:235],a[104:107],v[176:179],v[232:235]\nv_lshlrev_b32 v171,16,v107\nv_lshlrev_b32 v152,16,v172\n" \
"M v[232:235],a[112:115],v[60:63],v[232:235]\nv_and_b32 v153,0xffff0000,v172\nv_lshlrev_b32 v154,16,v173\nM v[232:235],a[120:123],v[88:91],v[232:235]\nv_and_b32 v155,0xffff0000,v173\nv_lshlrev_b32 v156,16,v174\nM v[236:239],a[124:127],v[88:91],v[236:239]\nbuffer_store_dwordx4 v[208:211],v4,s[36:39],s43 offen offset:0 sc1\n" \
"M v[236:239],a[116:119],v[60:63],v[236:239]\nds_read_b128 v[88:91],v20 offset:25600\nds_read_b128 v[60:63],v20 offset:26624\nM v[236:239],a[108:111],v[176:179],v[236:239]\nds_read_b128 v[176:179],v20 offset:27648\nv_mul_f32 v137,v137,v100\nM v[236:239],a[100:103],v[80:83],v[236:239]\nds_read_b128 v[80:83],v20 offset:20480\n" \
"v_and_b32 v157,0xffff0000,v174\nM v[224:227],a[128:131],v[180:183],v[224:227]\ns_waitcnt lgkmcnt(4)\nv_mul_f32 v139,v139,v101\nv_mul_f32 v141,v141,v102\nM v[224:227],a[136:139],v[84:87],v[224:227]\nv_mul_f32 v143,v143,v103\nv_and_b32 v104,0xffff0000,v104\n" \
"M v[224:227],a[144:147],v[92:95],v[224:227]\nv_and_b32 v105,0xffff0000,v105\nv_and_b32 v106,0xffff0000,v106\nM v[224:227],a[152:155],v[72:75],v[224:227]\nv_and_b32 v107,0xffff0000,v107\nv_lshlrev_b32 v172,16,v108\nM v[228:231],a[156:159],v[72:75],v[228:231]\nds_read_b128 v[72:75],v20 offset:21504\n" \
"v_lshlrev_b32 v158,16,v175\nM v[228:231],a[148:151],v[92:95],v[228:231]\nds_read_b128 v[92:95],v20 offset:22528\nv_and_b32 v159,0xffff0000,v175\nM v[228:231],a[140:143],v[84:87],v[228:231]\nds_read_b128 v[84:87],v20 offset:23552\nv_lshlrev_b32 v173,16,v109\nM v[228:231],a[132:135],v[180:183],v[228:231]\n" \
"ds_read_b128 v[180:183],v20 offset:28672\nv_lshlrev_b32 v174,16,v110\nM v[232:235],a[128:131],v[76:79],v[232:235]\ns_waitcnt lgkmcnt(4)\nv_mul_f32 v145,v145,v104\nv_mul_f32 v147,v147,v105\nM v[232:235],a[136:139],v[88:91],v[232:235]\nbuffer_load_dwordx4 v2,s[36:39],s47 offen offset:1024 sc1 lds\n" \
"M v[232:235],a[144:147],v[60:63],v[232:235]\nv_mul_f32 v149,v149,v106\nv_mul_f32 v151,v151,v107\nM v[232:235],a[152:155],v[176:179],v[232:235]\nv_and_b32 v108,0xffff0000,v108\nv_lshlrev_b32 v175,16,v111\nM v[236:239],a[156:159],v[176:179],v[236:239]\nds_read_b128 v[176:179],v20 offset:29696\n" \
"v_and_b32 v109,0xffff0000,v109\nM v[236:239],a[148:151],v[60:63],v[236:239]\nbuffer_load_dwordx4 v2,s[36:39],s47 offen offset:2048 sc1 lds\nM v[236:239],a[140:143],v[88:91],v[236:239]\nds_read_b128 v[60:63],v20 offset:30720\nv_and_b32 v110,0xffff0000,v110\nM v[236:239],a[132:135],v[76:79],v[236:239]\nds_read_b128 v[88:91],v20 offset:31744\n" \
"v_and_b32 v111,0xffff0000,v111\nM v[224:227],a[160:163],v[80:83],v[224:227]\ns_waitcnt lgkmcnt(3)\nds_read_b128 v[76:79],v20 offset:49152\nv_mul_f32 v153,v153,v108\nM v[224:227],a[168:171],v[72:75],v[224:227]\nbuffer_store_dwordx4 v[216:219],v4,s[36:39],s43 offen offset:1024 sc1\nM v[224:227],a[176:179],v[92:95],v[224:227]\n" \
"v_mul_f32 v155,v155,v109\nv_mul_f32 v157,v157,v110\nM v[224:227],a[184:187],v[84:87],v[224:227]\nv_mul_f32 v159,v159,v111\nv_cndmask_b32 v121,v121,v209,s[96:97]\nM v[228:231],a[188:191],v[84:87],v[228:231]\nds_read_b128 v[84:87],v20 offset:50176\nv_cndmask_b32 v122,v122,v210,s[96:97]\n" \
"M v[228:231],a[180:183],v[92:95],v[228:231]\nds_read_b128 v[92:95],v20 offset:51200\nv_cndmask_b32 v123,v123,v211,s[96:97]\nM v[228:231],a[172:175],v[72:75],v[228:231]\nds_read_b128 v[72:75],v20 offset:52224\nv_cndmask_b32 v124,v124,v216,s[96:97]\nM v[228:231],a[164:167],v[80:83],v[228:231]\nds_read_b128 v[80:83],v20 offset:57344\n" \
"v_cndmask_b32 v125,v125,v217,s[96:97]\nM v[232:235],a[160:163],v[180:183],v[232:235]\ns_waitcnt lgkmcnt(4)\nv_cndmask_b32 v126,v126,v218,s[96:97]\nv_cndmask_b32 v127,v127,v219,s[96:97]\nM v[232:235],a[168:171],v[176:179],v[232:235]\nv_mul_f32 v128,v128,v160\nv_mul_f32 v130,v130,v161\n" \
"M v[232:235],a[176:179],v[60:63],v[232:235]\nC v129,v128,v129\nv_mul_f32 v132,v132,v162\nM v[232:235],a[184:187],v[88:91],v[232:235]\nC v131,v130,v131\nv_mul_f32 v134,v134,v163\nM v[236:239],a[188:191],v[88:91],v[236:239]\nbuffer_load_dwordx4 v2,s[36:39],s47 offen offset:3072 sc1 lds\n" \
"M v[236:239],a[180:183],v[60:63],v[236:239]\ns_add_u32 m0,s22,49152\nds_read_b128 v[88:91],v20 offset:58368\nv_lshlrev_b32 v128,16,v129\nM v[236:239],a[172:175],v[176:179],v[236:239]\ns_add_u32 s47,s33,24576\nds_read_b128 v[60:63],v20 offset:59392\nC v133,v132,v133\n" \
"M v[236:239],a[164:167],v[180:183],v[236:239]\nds_read_b128 v[176:179],v20 offset:60416\nC v135,v134,v135\nM v[224:227],a[192:195],v[76:79],v[224:227]\ns_waitcnt lgkmcnt(3)\nds_read_b128 v[180:183],v20 offset:53248\nv_lshlrev_b32 v130,16,v131\nM v[224:227],a[200:203],v[84:87],v[224:227]\n" \
"buffer_load_dwordx4 v2,s[36:39],s47 offen offset:0 sc1 lds\nM v[224:227],a[208:211],v[92:95],v[224:227]\nv_lshlrev_b32 v132,16,v133\nv_and_b32 v129,0xffff0000,v129\nM v[224:227],a[216:219],v[72:75],v[224:227]\nv_lshlrev_b32 v134,16,v135\nv_and_b32 v133,0xffff0000,v133\nM v[228:231],a[220:223],v[72:75],v[228:231]\n" \
"ds_read_b128 v[72:75],v20 offset:54272\nv_and_b32 v131,0xffff0000,v131\nM v[228:231],a[212:215],v[92:95],v[228:231]\nbuffer_load_dwordx4 v2,s[36:39],s47 offen offset:1024 sc1 lds\nM v[228:231],a[204:207],v[84:87],v[228:231]\nds_read_b128 v[92:95],v20 offset:55296\nv_and_b32 v135,0xffff0000,v135\nM v[228:231],a[196:199],v[76:79],v[228:231]\n" \
"ds_read_b128 v[84:87],v20 offset:56320\nv_mul_f32 v128,v16,v128\nM v[232:235],a[192:195],v[80:83],v[232:235]\ns_waitcnt lgkmcnt(3)\nds_read_b128 v[76:79],v20 offset:61440\nv_mul_f32 v129,v16,v129\nM v[232:235],a[200:203],v[88:91],v[232:235]\nv_mul_f32 v132,v16,v132\n" \
"v_mul_f32 v133,v16,v133\nM v[232:235],a[208:211],v[60:63],v[232:235]\nbuffer_load_dwordx4 v2,s[36:39],s47 offen offset:2048 sc1 lds\nM v[232:235],a[216:219],v[176:179],v[232:235]\nv_mul_f32 v130,v16,v130\nv_mul_f32 v131,v16,v131\nM v[236:239],a[220:223],v[176:179],v[236:239]\ns_waitcnt lgkmcnt(2)\n" \
"ds_read_b128 v[176:179],v20 offset:62464\nv_mul_f32 v134,v16,v134\nM v[236:239],a[212:215],v[60:63],v[236:239]\ns_waitcnt vmcnt(10) lgkmcnt(14)\nds_read_b128 v[60:63],v20 offset:63488\nv_mul_f32 v135,v16,v135\nM v[236:239],a[204:207],v[88:91],v[236:239]\ns_barrier\n" \
"ds_read_b128 v[88:91],v20 offset:64512\nM v[236:239],a[196:199],v[80:83],v[236:239]\nbuffer_load_dwordx4 v2,s[36:39],s47 offen offset:3072 sc1 lds\nM v[224:227],a[224:227],v[180:183],v[224:227]\ns_add_u32 m0,s22,131072\nds_read_b128 v[80:83],v4 offset:0\nv_mul_f32 v136,v136,v164\nM v[224:227],a[232:235],v[72:75],v[224:227]\n" \
"s_add_u32 s47,s34,0\nv_mul_f32 v138,v138,v165\nv_mul_f32 v140,v140,v166\nM v[224:227],a[240:243],v[92:95],v[224:227]\ns_waitcnt lgkmcnt(3)\nC v129,v128,v129\nv_mul_f32 v142,v142,v167\nM v[224:227],a[248:251],v[84:87],v[224:227]\n" \
"buffer_load_dwordx4 v2,s[36:39],s47 offen offset:0 sc1 lds\nM v[228:231],a[252:255],v[84:87],v[228:231]\nds_read_b128 v[84:87],v4 offset:1024\nC v133,v132,v133\nM v[228:231],a[244:247],v[92:95],v[228:231]\nds_read_b128 v[92:95],v4 offset:2048\nC v224,v224,v225\nM v[228:231],a[236:239],v[72:75],v[228:231]\n" \
"ds_read_b128 v[72:75],v4 offset:3072\nC v225,v226,v227\nM v[228:231],a[228:231],v[180:183],v[228:231]\nbuffer_load_dwordx4 v2,s[36:39],s47 offen offset:1024 sc1 lds\nM v[232:235],a[224:227],v[76:79],v[232:235]\nds_read_b128 v[180:183],v4 offset:8192\nC v131,v130,v131\nM v[232:235],a[232:235],v[176:179],v[232:235]\n" \
"s_waitcnt lgkmcnt(4)\nC v226,v228,v229\nC v227,v230,v231\nM v[232:235],a[240:243],v[60:63],v[232:235]\nC v135,v134,v135\nC v137,v136,v137\nM v[232:235],a[248:251],v[88:91],v[232:235]\nC v139,v138,v139\n" \
"C v141,v140,v141\nM v[236:239],a[252:255],v[88:91],v[236:239]\nbuffer_load_dwordx4 v2,s[36:39],s47 offen offset:2048 sc1 lds\nM v[236:239],a[244:247],v[60:63],v[236:239]\nds_read_b128 v[88:91],v4 offset:9216\nC v232,v232,v233\nM v[236:239],a[236:239],v[176:179],v[236:239]\nds_read_b128 v[60:63],v4 offset:10240\n" \
"C v233,v234,v235\nM v[236:239],a[228:231],v[76:79],v[236:239]\ns_waitcnt lgkmcnt(2)\nds_read_b128 v[176:179],v4 offset:11264\nv_lshlrev_b32 v128,16,v129\nM v[240:243],a[0:3],v[80:83],0\nbuffer_load_dwordx4 v2,s[36:39],s47 offen offset:3072 sc1 lds\nM v[240:243],a[8:11],v[84:87],v[240:243]\n" \
"s_add_u32 m0,s22,147456\nds_read_b128 v[76:79],v4 offset:4096\nC v234,v236,v237\nM v[240:243],a[16:19],v[92:95],v[240:243]\ns_add_u32 s47,s34,8192\nC v235,v238,v239\nC v143,v142,v143\nM v[240:243],a[24:27],v[72:75],v[240:243]\n" \
"v_lshlrev_b32 v132,16,v133\nv_lshlrev_b32 v130,16,v131\nM v[244:247],a[28:31],v[72:75],0\nds_read_b128 v[72:75],v4 offset:5120\nv_lshlrev_b32 v134,16,v135\nM v[244:247],a[20:23],v[92:95],v[244:247]\nbuffer_load_dwordx4 v2,s[36:39],s47 offen offset:0 sc1 lds\nM v[244:247],a[12:15],v[84:87],v[244:247]\n" \
"ds_read_b128 v[92:95],v4 offset:6144\nv_lshlrev_b32 v136,16,v137\nM v[244:247],a[4:7],v[80:83],v[244:247]\nds_read_b128 v[84:87],v4 offset:7168\nv_lshlrev_b32 v140,16,v141\nM v[248:251],a[0:3],v[180:183],0\ns_waitcnt lgkmcnt(3)\nds_read_b128 v[80:83],v4 offset:12288\n" \
"v_lshlrev_b32 v138,16,v139\nM v[248:251],a[8:11],v[88:91],v[248:251]\nbuffer_store_dwordx4 v[224:227],v4,s[36:39],s44 offen offset:0 sc1\nM v[248:251],a[16:19],v[60:63],v[248:251]\nv_lshlrev_b32 v142,16,v143\nv_and_b32 v129,0xffff0000,v129\nM v[248:251],a[24:27],v[176:179],v[248:251]\nv_and_b32 v133,0xffff0000,v133\n" \
"v_and_b32 v131,0xffff0000,v131\nM v[252:255],a[28:31],v[176:179],0\nds_read_b128 v[176:179],v4 offset:13312\nv_and_b32 v135,0xffff0000,v135\nM v[252:255],a[20:23],v[60:63],v[252:255]\nds_read_b128 v[60:63],v4 offset:14336\nv_and_b32 v137,0xffff0000,v137\nM v[252:255],a[12:15],v[88:91],v[252:255]\n" \
"ds_read_b128 v[88:91],v4 offset:15360\nv_and_b32 v141,0xffff0000,v141\nM v[252:255],a[4:7],v[180:183],v[252:255]\nds_read_b128 v[180:183],v4 offset:32768\nv_and_b32 v139,0xffff0000,v139\nM v[240:243],a[32:35],v[76:79],v[240:243]\ns_waitcnt lgkmcnt(4)\nv_and_b32 v143,0xffff0000,v143\n" \
"v_mul_f32 v136,v17,v136\nM v[240:243],a[40:43],v[72:75],v[240:243]\nv_mul_f32 v137,v17,v137\nv_mul_f32 v140,v17,v140\nM v[240:243],a[48:51],v[92:95],v[240:243]\nv_mul_f32 v141,v17,v141\nv_mul_f32 v138,v17,v138\nM v[240:243],a[56:59],v[84:87],v[240:243]\n" \
"v_mul_f32 v139,v17,v139\nv_mul_f32 v142,v17,v142\nM v[244:247],a[60:63],v[84:87],v[244:247]\nbuffer_store_dwordx4 v[232:235],v4,s[36:39],s44 offen offset:1024 sc1\nM v[244:247],a[52:55],v[92:95],v[244:247]\nds_read_b128 v[84:87],v4 offset:33792\nds_read_b128 v[92:95],v4 offset:34816\nM v[244:247],a[44:47],v[72:75],v[244:247]\n" \
"ds_read_b128 v[72:75],v4 offset:35840\nv_mul_f32 v143,v17,v143\nM v[244:247],a[36:39],v[76:79],v[244:247]\nds_read_b128 v[76:79],v4 offset:40960\nv_mul_f32 v144,v144,v168\nM v[248:251],a[32:35],v[80:83],v[248:251]\ns_waitcnt lgkmcnt(4)\nv_mul_f32 v148,v148,v170\n" \
"v_mul_f32 v146,v146,v169\nM v[248:251],a[40:43],v[176:179],v[248:251]\nv_mul_f32 v150,v150,v171\nC v137,v136,v137\nM v[248:251],a[48:51],v[60:63],v[248:251]\nC v141,v140,v141\nC v139,v138,v139\nM v[248:251],a[56:59],v[88:91],v[248:251]\n" \
"C v143,v142,v143\nC v145,v144,v145\nM v[252:255],a[60:63],v[88:91],v[252:255]\nds_read_b128 v[88:91],v4 offset:41984\nC v149,v148,v149\nM v[252:255],a[52:55],v[60:63],v[252:255]\nds_read_b128 v[60:63],v4 offset:43008\nC v147,v146,v147\n" \
"M v[252:255],a[44:47],v[176:179],v[252:255]\nds_read_b128 v[176:179],v4 offset:44032\nC v151,v150,v151\nM v[252:255],a[36:39],v[80:83],v[252:255]\nds_read_b128 v[80:83],v4 offset:36864\nv_lshlrev_b32 v136,16,v137\nM v[240:243],a[64:67],v[180:183],v[240:243]\ns_waitcnt lgkmcnt(4)\n" \
"v_lshlrev_b32 v140,16,v141\nv_lshlrev_b32 v138,16,v139\nM v[240:243],a[72:75],v[84:87],v[240:243]\nbuffer_load_dwordx4 v2,s[36:39],s47 offen offset:1024 sc1 lds\nM v[240:243],a[80:83],v[92:95],v[240:243]\nv_lshlrev_b32 v142,16,v143\nv_lshlrev_b32 v144,16,v145\nM v[240:243],a[88:91],v[72:75],v[240:243]\n" \
"v_lshlrev_b32 v148,16,v149\nv_lshlrev_b32 v146,16,v147\nM v[244:247],a[92:95],v[72:75],v[244:247]\nds_read_b128 v[72:75],v4 offset:37888\nv_lshlrev_b32 v150,16,v151\nM v[244:247],a[84:87],v[92:95],v[244:247]\nds_read_b128 v[92:95],v4 offset:38912\nv_and_b32 v137,0xffff0000,v137\n" \
"M v[244:247],a[76:79],v[84:87],v[244:247]\nbuffer_load_dwordx4 v2,s[36:39],s47 offen offset:2048 sc1 lds\nM v[244:247],a[68:71],v[180:183],v[244:247]\nds_read_b128 v[84:87],v4 offset:39936\nv_and_b32 v141,0xffff0000,v141\nM v[248:251],a[64:67],v[76:79],v[248:251]\ns_waitcnt lgkmcnt(3)\nds_read_b128 v[180:183],v4 offset:45056\n" \
"v_and_b32 v139,0xffff0000,v139\nM v[248:251],a[72:75],v[88:91],v[248:251]\nv_and_b32 v143,0xffff0000,v143\nv_and_b32 v145,0xffff0000,v145\nM v[248:251],a[80:83],v[60:63],v[248:251]\nbuffer_load_dwordx4 v2,s[36:39],s47 offen offset:3072 sc1 lds\nM v[248:251],a[88:91],v[176:179],v[248:251]\ns_add_u32 m0,s22,65536\n" \
"v_and_b32 v149,0xffff0000,v149\nv_and_b32 v147,0xffff0000,v147\nM v[252:255],a[92:95],v[176:179],v[252:255]\ns_add_u32 s47,s34,16384\nds_read_b128 v[176:179],v4 offset:46080\nv_and_b32 v151,0xffff0000,v151\nM v[252:255],a[84:87],v[60:63],v[252:255]\ns_waitcnt vmcnt(10) lgkmcnt(14)\n" \
"ds_read_b128 v[60:63],v4 offset:47104\nv_mul_f32 v144,v18,v144\nM v[252:255],a[76:79],v[88:91],v[252:255]\ns_barrier\nds_read_b128 v[88:91],v4 offset:48128\nM v[252:255],a[68:71],v[76:79],v[252:255]\nbuffer_load_dwordx4 v2,s[36:39],s47 offen offset:0 sc1 lds\nM v[240:243],a[96:99],v[80:83],v[240:243]\n" \
"s_waitcnt lgkmcnt(3)\nds_read_b128 v[76:79],v4 offset:16384\nv_mul_f32 v145,v18,v145\nM v[240:243],a[104:107],v[72:75],v[240:243]\nv_mul_f32 v148,v18,v148\nv_mul_f32 v149,v18,v149\nM v[240:243],a[112:115],v[92:95],v[240:243]\nv_mul_f32 v146,v18,v146\n" \
"v_mul_f32 v147,v18,v147\nM v[240:243],a[120:123],v[84:87],v[240:243]\nbuffer_load_dwordx4 v2,s[36:39],s47 offen offset:1024 sc1 lds\nM v[244:247],a[124:127],v[84:87],v[244:247]\nds_read_b128 v[84:87],v4 offset:17408\nv_mul_f32 v150,v18,v150\nM v[244:247],a[116:119],v[92:95],v[244:247]\ns_waitcnt vmcnt(17)\n" \
"ds_read_b128 v[92:95],v4 offset:18432\nv_mul_f32 v151,v18,v151\nM v[244:247],a[108:111],v[72:75],v[244:247]\nds_read_b128 v[72:75],v4 offset:19456\nv_mul_f32 v152,v152,v172\nM v[244:247],a[100:103],v[80:83],v[244:247]\nbuffer_store_dwordx2 v[28:29],v191,s[36:39],s95 offen offset:0 sc1\nM v[248:251],a[96:99],v[180:183],v[248:251]\n" \
"s_waitcnt lgkmcnt(3)\nds_read_b128 v[80:83],v4 offset:24576\nv_mul_f32 v156,v156,v174\nM v[248:251],a[104:107],v[176:179],v[248:251]\nbuffer_load_dwordx4 v2,s[36:39],s47 offen offset:2048 sc1 lds\nM v[248:251],a[112:115],v[60:63],v[248:251]\nv_mul_f32 v154,v154,v173\nv_mul_f32 v158,v158,v175\n" \
"M v[248:251],a[120:123],v[88:91],v[248:251]\nC v145,v144,v145\nC v149,v148,v149\nM v[252:255],a[124:127],v[88:91],v[252:255]\nds_read_b128 v[88:91],v4 offset:25600\nC v147,v146,v147\nM v[252:255],a[116:119],v[60:63],v[252:255]\ns_waitcnt lgkmcnt(2)\n" \
"ds_read_b128 v[60:63],v4 offset:26624\nC v151,v150,v151\nM v[252:255],a[108:111],v[176:179],v[252:255]\nbuffer_load_dwordx4 v2,s[36:39],s47 offen offset:3072 sc1 lds\nM v[252:255],a[100:103],v[180:183],v[252:255]\ns_add_u32 m0,s22,98304\nds_read_b128 v[176:179],v4 offset:27648\nC v153,v152,v153\n" \
"M v[240:243],a[128:131],v[76:79],v[240:243]\ns_add_u32 s47,s34,24576\nds_read_b128 v[180:183],v4 offset:20480\nC v157,v156,v157\nM v[240:243],a[136:139],v[84:87],v[240:243]\nC v155,v154,v155\nC v159,v158,v159\nM v[240:243],a[144:147],v[92:95],v[240:243]\n" \
"buffer_load_dwordx4 v2,s[36:39],s47 offen offset:0 sc1 lds\nM v[240:243],a[152:155],v[72:75],v[240:243]\nv_lshlrev_b32 v144,16,v145\nv_lshlrev_b32 v148,16,v149\nM v[244:247],a[156:159],v[72:75],v[244:247]\nds_read_b128 v[72:75],v4 offset:21504\nv_lshlrev_b32 v146,16,v147\nM v[244:247],a[148:151],v[92:95],v[244:247]\n" \
"ds_read_b128 v[92:95],v4 offset:22528\nv_lshlrev_b32 v150,16,v151\nM v[244:247],a[140:143],v[84:87],v[244:247]\ns_waitcnt lgkmcnt(2)\nds_read_b128 v[84:87],v4 offset:23552\nv_lshlrev_b32 v152,16,v153\nM v[244:247],a[132:135],v[76:79],v[244:247]\nbuffer_load_dwordx4 v2,s[36:39],s47 offen offset:1024 sc1 lds\n" \
"M v[248:251],a[128:131],v[80:83],v[248:251]\nds_read_b128 v[76:79],v4 offset:28672\nv_lshlrev_b32 v156,16,v157\nM v[248:251],a[136:139],v[88:91],v[248:251]\nv_lshlrev_b32 v154,16,v155\nv_lshlrev_b32 v158,16,v159\nM v[248:251],a[144:147],v[60:63],v[248:251]\nv_and_b32 v145,0xffff0000,v145\n" \
"v_and_b32 v149,0xffff0000,v149\nM v[248:251],a[152:155],v[176:179],v[248:251]\nbuffer_load_dwordx4 v2,s[36:39],s47 offen offset:2048 sc1 lds\nM v[252:255],a[156:159],v[176:179],v[252:255]\nds_read_b128 v[176:179],v4 offset:29696\nv_and_b32 v147,0xffff0000,v147\nM v[252:255],a[148:151],v[60:63],v[252:255]\nds_read_b128 v[60:63],v4 offset:30720\n" \
"v_and_b32 v151,0xffff0000,v151\nM v[252:255],a[140:143],v[88:91],v[252:255]\nds_read_b128 v[88:91],v4 offset:31744\nv_and_b32 v153,0xffff0000,v153\nM v[252:255],a[132:135],v[80:83],v[252:255]\ns_waitcnt lgkmcnt(3)\nds_read_b128 v[80:83],v4 offset:49152\nv_and_b32 v157,0xffff0000,v157\n" \
"M v[240:243],a[160:163],v[180:183],v[240:243]\nbuffer_load_dwordx4 v2,s[36:39],s47 offen offset:3072 sc1 lds\nM v[240:243],a[168:171],v[72:75],v[240:243]\ns_add_u32 m0,s22,81920\nv_and_b32 v155,0xffff0000,v155\nv_and_b32 v159,0xffff0000,v159\nM v[240:243],a[176:179],v[92:95],v[240:243]\ns_add_u32 s47,s35,0\n" \
"v_mul_f32 v152,v19,v152\nv_mul_f32 v153,v19,v153\nM v[240:243],a[184:187],v[84:87],v[240:243]\nv_mul_f32 v156,v19,v156\nv_mul_f32 v157,v19,v157\nM v[244:247],a[188:191],v[84:87],v[244:247]\nbuffer_load_dwordx4 v2,s[36:39],s47 offen offset:0 sc1 lds\nM v[244:247],a[180:183],v[92:95],v[244:247]\n" \
"ds_read_b128 v[84:87],v4 offset:50176\nv_mul_f32 v154,v19,v154\nM v[244:247],a[172:175],v[72:75],v[244:247]\nds_read_b128 v[92:95],v4 offset:51200\nv_mul_f32 v155,v19,v155\nM v[244:247],a[164:167],v[180:183],v[244:247]\nds_read_b128 v[72:75],v4 offset:52224\nv_mul_f32 v158,v19,v158\n" \
"M v[248:251],a[160:163],v[76:79],v[248:251]\ns_waitcnt lgkmcnt(3)\nds_read_b128 v[180:183],v4 offset:57344\nv_mul_f32 v159,v19,v159\nM v[248:251],a[168:171],v[176:179],v[248:251]\nbuffer_load_dwordx4 v2,s[36:39],s47 offen offset:1024 sc1 lds\nM v[248:251],a[176:179],v[60:63],v[248:251]\nC v153,v152,v153\n" \
"C v157,v156,v157\nM v[248:251],a[184:187],v[88:91],v[248:251]\nC v155,v154,v155\nC v159,v158,v159\nM v[252:255],a[188:191],v[88:91],v[252:255]\nds_read_b128 v[88:91],v4 offset:58368\nv_lshlrev_b32 v152,16,v153\nM v[252:255],a[180:183],v[60:63],v[252:255]\n" \
"buffer_load_dwordx4 v2,s[36:39],s47 offen offset:2048 sc1 lds\nM v[252:255],a[172:175],v[176:179],v[252:255]\nds_read_b128 v[60:63],v4 offset:59392\nv_lshlrev_b32 v156,16,v157\nM v[252:255],a[164:167],v[76:79],v[252:255]\nds_read_b128 v[176:179],v4 offset:60416\nv_lshlrev_b32 v154,16,v155\nM v[240:243],a[192:195],v[80:83],v[240:243]\n" \
"s_waitcnt lgkmcnt(3)\nds_read_b128 v[76:79],v4 offset:53248\nv_lshlrev_b32 v158,16,v159\nM v[240:243],a[200:203],v[84:87],v[240:243]\nv_and_b32 v153,0xffff0000,v153\nv_and_b32 v157,0xffff0000,v157\nM v[240:243],a[208:211],v[92:95],v[240:243]\nbuffer_load_dwordx4 v2,s[36:39],s47 offen offset:3072 sc1 lds\n" \
"M v[240:243],a[216:219],v[72:75],v[240:243]\ns_add_u32 m0,s22,114688\nv_and_b32 v155,0xffff0000,v155\nv_and_b32 v159,0xffff0000,v159\nM v[244:247],a[220:223],v[72:75],v[244:247]\ns_add_u32 s47,s35,8192\nds_read_b128 v[72:75],v4 offset:54272\nM v[244:247],a[212:215],v[92:95],v[244:247]\n" \
"ds_read_b128 v[92:95],v4 offset:55296\nM v[244:247],a[204:207],v[84:87],v[244:247]\nbuffer_load_dwordx4 v2,s[36:39],s47 offen offset:0 sc1 lds\nM v[244:247],a[196:199],v[80:83],v[244:247]\nds_read_b128 v[84:87],v4 offset:56320\nM v[248:251],a[192:195],v[180:183],v[248:251]\ns_waitcnt lgkmcnt(3)\nds_read_b128 v[80:83],v4 offset:61440\n" \
"M v[248:251],a[200:203],v[88:91],v[248:251]\nM v[248:251],a[208:211],v[60:63],v[248:251]\nM v[248:251],a[216:219],v[176:179],v[248:251]\nbuffer_load_dwordx4 v2,s[36:39],s47 offen offset:1024 sc1 lds\nM v[252:255],a[220:223],v[176:179],v[252:255]\nds_read_b128 v[176:179],v4 offset:62464\nM v[252:255],a[212:215],v[60:63],v[252:255]\ns_waitcnt vmcnt(15) lgkmcnt(14)\n" \
"ds_read_b128 v[60:63],v4 offset:63488\nM v[252:255],a[204:207],v[88:91],v[252:255]\ns_barrier\nds_read_b128 v[88:91],v4 offset:64512\nM v[252:255],a[196:199],v[180:183],v[252:255]\nbuffer_load_dwordx4 v2,s[36:39],s47 offen offset:2048 sc1 lds\nM v[240:243],a[224:227],v[76:79],v[240:243]\ns_waitcnt lgkmcnt(3)\n" \
"ds_read_b128 v[180:183],v23 offset:0\nM v[240:243],a[232:235],v[72:75],v[240:243]\nM v[240:243],a[240:243],v[92:95],v[240:243]\nM v[240:243],a[248:251],v[84:87],v[240:243]\nbuffer_load_dwordx2 v[10:11],v190,s[36:39],0 offen sc1\nM v[244:247],a[252:255],v[84:87],v[244:247]\nds_read_b128 v[84:87],v23 offset:1024\nM v[244:247],a[244:247],v[92:95],v[244:247]\n" \
"buffer_load_dwordx4 v2,s[36:39],s47 offen offset:3072 sc1 lds\nM v[244:247],a[236:239],v[72:75],v[244:247]\ns_add_u32 m0,s22,0\nds_read_b128 v[92:95],v23 offset:2048\nC v240,v240,v241\nM v[244:247],a[228:231],v[76:79],v[244:247]\ns_add_u32 s47,s35,16384\nds_read_b128 v[72:75],v23 offset:3072\n" \
"C v241,v242,v243\nM v[248:251],a[224:227],v[80:83],v[248:251]\nLG25_%=:\ns_cbranch_execz LHR3_%=\ns_waitcnt lgkmcnt(3)\nds_read_b128 v[76:79],v23 offset:8192\nM v[248:251],a[232:235],v[176:179],v[248:251]\nC v242,v244,v245\n" \
"C v243,v246,v247\nM v[248:251],a[240:243],v[60:63],v[248:251]\nbuffer_store_dwordx4 v[240:243],v4,s[36:39],s45 offen offset:0 sc1\nM v[248:251],a[248:251],v[88:91],v[248:251]\nM v[252:255],a[252:255],v[88:91],v[252:255]\nds_read_b128 v[88:91],v23 offset:9216\nM v[252:255],a[244:247],v[60:63],v[252:255]\nds_read_b128 v[60:63],v23 offset:10240\n" \
"C v248,v248,v249\nM v[252:255],a[236:239],v[176:179],v[252:255]\nds_read_b128 v[176:179],v23 offset:11264\nC v249,v250,v251\nM v[252:255],a[228:231],v[80:83],v[252:255]\nds_read_b128 v[80:83],v23 offset:4096\nM v[192:195],a[0:3],v[180:183],0\ns_waitcnt lgkmcnt(4)\n" \
"M v[192:195],a[8:11],v[84:87],v[192:195]\nC v250,v252,v253\nC v251,v254,v255\nM v[192:195],a[16:19],v[92:95],v[192:195]\nM v[192:195],a[24:27],v[72:75],v[192:195]\nM v[196:199],a[28:31],v[72:75],0\nds_read_b128 v[72:75],v23 offset:5120\nM v[196:199],a[20:23],v[92:95],v[196:199]\n" \
"ds_read_b128 v[92:95],v23 offset:6144\nM v[196:199],a[12:15],v[84:87],v[196:199]\nds_read_b128 v[84:87],v23 offset:7168\nM v[196:199],a[4:7],v[180:183],v[196:199]\nbuffer_store_dwordx4 v[248:251],v4,s[36:39],s45 offen offset:1024 sc1\nM v[200:203],a[0:3],v[76:79],0\ns_waitcnt lgkmcnt(3)\nM v[200:203],a[8:11],v[88:91],v[200:203]\n" \
"M v[200:203],a[16:19],v[60:63],v[200:203]\nM v[200:203],a[24:27],v[176:179],v[200:203]\nM v[204:207],a[28:31],v[176:179],0\nM v[204:207],a[20:23],v[60:63],v[204:207]\nds_read_b128 v[60:63],v23 offset:12288\nM v[204:207],a[12:15],v[88:91],v[204:207]\nds_read_b128 v[88:91],v23 offset:13312\nM v[204:207],a[4:7],v[76:79],v[204:207]\n" \
"ds_read_b128 v[76:79],v23 offset:14336\nM v[192:195],a[32:35],v[80:83],v[192:195]\ns_waitcnt lgkmcnt(3)\nM v[192:195],a[40:43],v[72:75],v[192:195]\nM v[192:195],a[48:51],v[92:95],v[192:195]\nM v[192:195],a[56:59],v[84:87],v[192:195]\nM v[196:199],a[60:63],v[84:87],v[196:199]\nds_read_b128 v[84:87],v23 offset:15360\n" \
"M v[196:199],a[52:55],v[92:95],v[196:199]\nbuffer_load_dwordx4 v2,s[36:39],s47 offen offset:0 sc1 lds\nM v[196:199],a[44:47],v[72:75],v[196:199]\nds_read_b128 v[92:95],v23 offset:16384\nM v[196:199],a[36:39],v[80:83],v[196:199]\ns_waitcnt lgkmcnt(2)\nds_read_b128 v[72:75],v23 offset:17408\nM v[200:203],a[32:35],v[60:63],v[200:203]\n" \
"ds_read_b128 v[80:83],v23 offset:18432\nM v[200:203],a[40:43],v[88:91],v[200:203]\nbuffer_load_dwordx4 v2,s[36:39],s47 offen offset:1024 sc1 lds\nM v[200:203],a[48:51],v[76:79],v[200:203]\ns_waitcnt lgkmcnt(3)\nM v[200:203],a[56:59],v[84:87],v[200:203]\nM v[204:207],a[60:63],v[84:87],v[204:207]\nds_read_b128 v[84:87],v23 offset:19456\n" \
"M v[204:207],a[52:55],v[76:79],v[204:207]\nbuffer_load_dwordx4 v2,s[36:39],s47 offen offset:2048 sc1 lds\nM v[204:207],a[44:47],v[88:91],v[204:207]\nds_read_b128 v[76:79],v23 offset:24576\nM v[204:207],a[36:39],v[60:63],v[204:207]\ns_waitcnt lgkmcnt(2)\nds_read_b128 v[88:91],v23 offset:25600\nM v[192:195],a[64:67],v[92:95],v[192:195]\n" \
"s_waitcnt lgkmcnt(2)\nds_read_b128 v[60:63],v23 offset:26624\nM v[192:195],a[72:75],v[72:75],v[192:195]\nbuffer_load_dwordx4 v2,s[36:39],s47 offen offset:3072 sc1 lds\nM v[192:195],a[80:83],v[80:83],v[192:195]\ns_add_u32 m0,s22,32768\nM v[192:195],a[88:91],v[84:87],v[192:195]\ns_add_u32 s47,s35,24576\n" \
"M v[196:199],a[92:95],v[84:87],v[196:199]\ns_waitcnt lgkmcnt(2)\nds_read_b128 v[84:87],v23 offset:27648\nM v[196:199],a[84:87],v[80:83],v[196:199]\ns_waitcnt vmcnt(7)\nv_cmp_gt_u64 s[98:99],s[14:15],v[10:11]\nv_cmp_le_u64 s[18:19],s[16:17],v[10:11]\nM v[196:199],a[76:79],v[72:75],v[196:199]\n" \
"s_or_b64 s[98:99],s[98:99],s[18:19]\ns_and_b32 s98,s98,0x7\ns_cbranch_scc1 LPP_%=\nLPPR_%=:\nds_read_b128 v[80:83],v23 offset:20480\nds_read_b128 v[72:75],v23 offset:21504\nM v[196:199],a[68:71],v[92:95],v[196:199]\nbuffer_load_dwordx4 v[96:99],v4,s[36:39],s63 offen offset:0 sc1\n" \
"M v[200:203],a[64:67],v[76:79],v[200:203]\ns_waitcnt lgkmcnt(3)\nds_read_b128 v[92:95],v23 offset:22528\nM v[200:203],a[72:75],v[88:91],v[200:203]\nM v[200:203],a[80:83],v[60:63],v[200:203]\ns_waitcnt lgkmcnt(3)\nM v[200:203],a[88:91],v[84:87],v[200:203]\nbuffer_load_dwordx4 v[100:103],v4,s[36:39],s63 offen offset:1024 sc1\n" \
"M v[204:207],a[92:95],v[84:87],v[204:207]\nds_read_b128 v[84:87],v23 offset:23552\nM v[204:207],a[84:87],v[60:63],v[204:207]\nds_read_b128 v[60:63],v23 offset:28672\nM v[204:207],a[76:79],v[88:91],v[204:207]\ns_waitcnt lgkmcnt(2)\nds_read_b128 v[88:91],v23 offset:29696\nM v[204:207],a[68:71],v[76:79],v[204:207]\n" \
"buffer_load_dwordx4 v[104:107],v4,s[36:39],s63 offen offset:2048 sc1\nM v[192:195],a[96:99],v[80:83],v[192:195]\ns_waitcnt lgkmcnt(2)\nds_read_b128 v[76:79],v23 offset:30720\nM v[192:195],a[104:107],v[72:75],v[192:195]\ns_waitcnt vmcnt(18) lgkmcnt(15)\nM v[192:195],a[112:115],v[92:95],v[192:195]\ns_barrier\n" \
"M v[192:195],a[120:123],v[84:87],v[192:195]\nbuffer_load_dwordx4 v[108:111],v4,s[36:39],s63 offen offset:3072 sc1\nM v[196:199],a[124:127],v[84:87],v[196:199]\ns_add_u32 s63,s49,4096\nds_read_b128 v[84:87],v23 offset:31744\nM v[196:199],a[116:119],v[92:95],v[196:199]\nds_read_b128 v[92:95],v20 offset:0\nM v[196:199],a[108:111],v[72:75],v[196:199]\n" \
"ds_read_b128 v[72:75],v20 offset:1024\nM v[196:199],a[100:103],v[80:83],v[196:199]\ns_waitcnt lgkmcnt(3)\nds_read_b128 v[80:83],v20 offset:2048\nM v[200:203],a[96:99],v[60:63],v[200:203]\nbuffer_load_dwordx4 v[112:115],v4,s[36:39],s63 offen offset:0 sc1\nM v[200:203],a[104:107],v[88:91],v[200:203]\nM v[200:203],a[112:115],v[76:79],v[200:203]\n" \
"s_waitcnt lgkmcnt(3)\nM v[200:203],a[120:123],v[84:87],v[200:203]\nM v[204:207],a[124:127],v[84:87],v[204:207]\nds_read_b128 v[84:87],v20 offset:3072\nM v[204:207],a[116:119],v[76:79],v[204:207]\nbuffer_load_dwordx4 v[116:119],v4,s[36:39],s63 offen offset:1024 sc1\nM v[204:207],a[108:111],v[88:91],v[204:207]\nds_read_b128 v[76:79],v20 offset:8192\n" \
"ds_read_b128 v[88:91],v20 offset:9216\nM v[204:207],a[100:103],v[60:63],v[204:207]\ns_waitcnt lgkmcnt(3)\nds_read_b128 v[60:63],v20 offset:10240\nM v[192:195],a[128:131],v[92:95],v[192:195]\nM v[192:195],a[136:139],v[72:75],v[192:195]\nbuffer_load_dwordx4 v2,s[36:39],s47 offen offset:0 sc1 lds\nM v[192:195],a[144:147],v[80:83],v[192:195]\n" \
"s_waitcnt lgkmcnt(3)\nM v[192:195],a[152:155],v[84:87],v[192:195]\nM v[196:199],a[156:159],v[84:87],v[196:199]\nds_read_b128 v[84:87],v20 offset:11264\nM v[196:199],a[148:151],v[80:83],v[196:199]\nbuffer_load_dwordx4 v2,s[36:39],s47 offen offset:1024 sc1 lds\nM v[196:199],a[140:143],v[72:75],v[196:199]\nds_read_b128 v[80:83],v20 offset:4096\n" \
"M v[196:199],a[132:135],v[92:95],v[196:199]\ns_waitcnt lgkmcnt(2)\nds_read_b128 v[72:75],v20 offset:5120\nM v[200:203],a[128:131],v[76:79],v[200:203]\nds_read_b128 v[92:95],v20 offset:6144\nM v[200:203],a[136:139],v[88:91],v[200:203]\ns_waitcnt lgkmcnt(3)\nM v[200:203],a[144:147],v[60:63],v[200:203]\n" \
"buffer_load_dwordx4 v2,s[36:39],s47 offen offset:2048 sc1 lds\nM v[200:203],a[152:155],v[84:87],v[200:203]\nM v[204:207],a[156:159],v[84:87],v[204:207]\nds_read_b128 v[84:87],v20 offset:7168\nM v[204:207],a[148:151],v[60:63],v[204:207]\nds_read_b128 v[60:63],v20 offset:12288\nM v[204:207],a[140:143],v[88:91],v[204:207]\nbuffer_load_dwordx4 v2,s[36:39],s47 offen offset:3072 sc1 lds\n" \
"M v[204:207],a[132:135],v[76:79],v[204:207]\ns_waitcnt lgkmcnt(2)\nds_read_b128 v[88:91],v20 offset:13312\nM v[192:195],a[160:163],v[80:83],v[192:195]\nds_read_b128 v[76:79],v20 offset:14336\nM v[192:195],a[168:171],v[72:75],v[192:195]\nM v[192:195],a[176:179],v[92:95],v[192:195]\ns_waitcnt lgkmcnt(3)\n" \
"M v[192:195],a[184:187],v[84:87],v[192:195]\nM v[196:199],a[188:191],v[84:87],v[196:199]\nds_read_b128 v[84:87],v20 offset:15360\nM v[196:199],a[180:183],v[92:95],v[196:199]\nds_read_b128 v[92:95],v20 offset:32768\nM v[196:199],a[172:175],v[72:75],v[196:199]\nds_read_b128 v[72:75],v20 offset:33792\nM v[196:199],a[164:167],v[80:83],v[196:199]\n" \
"s_waitcnt lgkmcnt(3)\nds_read_b128 v[80:83],v20 offset:34816\nM v[200:203],a[160:163],v[60:63],v[200:203]\nM v[200:203],a[168:171],v[88:91],v[200:203]\nM v[200:203],a[176:179],v[76:79],v[200:203]\ns_waitcnt lgkmcnt(3)\nM v[200:203],a[184:187],v[84:87],v[200:203]\nM v[204:207],a[188:191],v[84:87],v[204:207]\n" \
"ds_read_b128 v[84:87],v20 offset:35840\nM v[204:207],a[180:183],v[76:79],v[204:207]\nds_read_b128 v[76:79],v20 offset:40960\nM v[204:207],a[172:175],v[88:91],v[204:207]\nds_read_b128 v[88:91],v20 offset:41984\nM v[204:207],a[164:167],v[60:63],v[204:207]\ns_waitcnt lgkmcnt(3)\nds_read_b128 v[60:63],v20 offset:43008\n" \
"M v[192:195],a[192:195],v[92:95],v[192:195]\nM v[192:195],a[200:203],v[72:75],v[192:195]\nM v[192:195],a[208:211],v[80:83],v[192:195]\ns_waitcnt lgkmcnt(3)\nM v[192:195],a[216:219],v[84:87],v[192:195]\nM v[196:199],a[220:223],v[84:87],v[196:199]\nds_read_b128 v[84:87],v20 offset:44032\nM v[196:199],a[212:215],v[80:83],v[196:199]\n" \
"ds_read_b128 v[80:83],v20 offset:36864\nM v[196:199],a[204:207],v[72:75],v[196:199]\nds_read_b128 v[72:75],v20 offset:37888\nM v[196:199],a[196:199],v[92:95],v[196:199]\ns_waitcnt lgkmcnt(3)\nds_read_b128 v[92:95],v20 offset:38912\nM v[200:203],a[192:195],v[76:79],v[200:203]\nM v[200:203],a[200:203],v[88:91],v[200:203]\n" \
"M v[200:203],a[208:211],v[60:63],v[200:203]\ns_waitcnt lgkmcnt(3)\nM v[200:203],a[216:219],v[84:87],v[200:203]\ns_add_u32 s63,s49,4096\nM v[204:207],a[220:223],v[84:87],v[204:207]\nds_read_b128 v[84:87],v20 offset:39936\nM v[204:207],a[212:215],v[60:63],v[204:207]\nbuffer_load_dwordx4 v[176:179],v4,s[36:39],s63 offen offset:2048 sc1\n" \
"M v[204:207],a[204:207],v[88:91],v[204:207]\ns_waitcnt lgkmcnt(1)\nds_read_b128 v[60:63],v20 offset:45056\nds_read_b128 v[88:91],v20 offset:46080\nM v[204:207],a[196:199],v[76:79],v[204:207]\ns_waitcnt vmcnt(5)\nds_read_b128 v[76:79],v20 offset:47104\nM v[192:195],a[224:227],v[80:83],v[192:195]\n" \
"s_waitcnt lgkmcnt(3)\nM v[192:195],a[232:235],v[72:75],v[192:195]\ns_waitcnt vmcnt(17) lgkmcnt(15)\nM v[192:195],a[240:243],v[92:95],v[192:195]\ns_barrier\nM v[192:195],a[248:251],v[84:87],v[192:195]\nbuffer_load_dwordx4 v[180:183],v4,s[36:39],s63 offen offset:3072 sc1\nM v[196:199],a[252:255],v[84:87],v[196:199]\n" \
"s_add_u32 s63,s49,8192\nds_read_b128 v[84:87],v20 offset:48128\nM v[196:199],a[244:247],v[92:95],v[196:199]\nC v192,v192,v193\nC v193,v194,v195\nM v[196:199],a[236:239],v[72:75],v[196:199]\ns_waitcnt lgkmcnt(1)\nds_read_b128 v[92:95],v20 offset:16384\n" \
"ds_read_b128 v[72:75],v20 offset:17408\nM v[196:199],a[228:231],v[80:83],v[196:199]\nbuffer_load_dwordx4 v[32:35],v4,s[36:39],s63 offen offset:0 sc1\nM v[200:203],a[224:227],v[60:63],v[200:203]\nds_read_b128 v[80:83],v20 offset:18432\nM v[200:203],a[232:235],v[88:91],v[200:203]\nC v194,v196,v197\nC v195,v198,v199\n" \
"M v[200:203],a[240:243],v[76:79],v[200:203]\ns_waitcnt lgkmcnt(3)\nM v[200:203],a[248:251],v[84:87],v[200:203]\nbuffer_load_dwordx4 v[36:39],v4,s[36:39],s63 offen offset:1024 sc1\nM v[204:207],a[252:255],v[84:87],v[204:207]\nds_read_b128 v[84:87],v20 offset:19456\nM v[204:207],a[244:247],v[76:79],v[204:207]\nds_read_b128 v[76:79],v20 offset:24576\n" \
"C v200,v200,v201\nM v[204:207],a[236:239],v[88:91],v[204:207]\nds_read_b128 v[88:91],v20 offset:25600\nC v201,v202,v203\nM v[204:207],a[228:231],v[60:63],v[204:207]\nbuffer_store_dwordx4 v[192:195],v4,s[36:39],s48 offen offset:0 sc1\nM v[48:51],v[186:189],v[120:123],0\nds_read_b128 v[60:63],v20 offset:26624\n" \
"M v[52:55],v[184:187],v[120:123],0\nC v202,v204,v205\nC v203,v206,v207\nM v[64:67],v[186:189],v[124:127],0\nM v[68:71],v[184:187],v[124:127],0\nM v[48:51],v[186:189],v[96:99],v[48:51]\nM v[52:55],v[184:187],v[96:99],v[52:55]\nM v[64:67],v[186:189],v[100:103],v[64:67]\n" \
"M v[68:71],v[184:187],v[100:103],v[68:71]\nM v[48:51],v[186:189],v[104:107],v[48:51]\nbuffer_store_dwordx4 v[200:203],v4,s[36:39],s48 offen offset:1024 sc1\nM v[52:55],v[184:187],v[104:107],v[52:55]\nM v[64:67],v[186:189],v[108:111],v[64:67]\nM v[68:71],v[184:187],v[108:111],v[68:71]\nM v[48:51],v[186:189],v[112:115],v[48:51]\nM v[52:55],v[184:187],v[112:115],v[52:55]\n" \
"M v[64:67],v[186:189],v[116:119],v[64:67]\nC v160,v48,v49\nC v161,v50,v51\nM v[68:71],v[184:187],v[116:119],v[68:71]\ns_waitcnt lgkmcnt(0)\nC v162,v52,v53\nC v163,v54,v55\nM v[208:211],a[0:3],v[92:95],0\n" \
"v_lshlrev_b32 v48,16,v160\nv_lshlrev_b32 v52,16,v162\nM v[208:211],a[8:11],v[72:75],v[208:211]\nv_and_b32 v49,0xffff0000,v160\nv_and_b32 v53,0xffff0000,v162\nM v[208:211],a[16:19],v[80:83],v[208:211]\nv_lshlrev_b32 v50,16,v161\nv_lshlrev_b32 v54,16,v163\n" \
"M v[208:211],a[24:27],v[84:87],v[208:211]\nv_and_b32 v51,0xffff0000,v161\nv_and_b32 v55,0xffff0000,v163\nM v[212:215],a[28:31],v[84:87],0\nds_read_b128 v[84:87],v20 offset:27648\nv_mul_f32 v48,v24,v48\nM v[212:215],a[20:23],v[80:83],v[212:215]\nds_read_b128 v[80:83],v20 offset:20480\n" \
"v_mul_f32 v52,v24,v52\nM v[212:215],a[12:15],v[72:75],v[212:215]\nds_read_b128 v[72:75],v20 offset:21504\nv_mul_f32 v49,v24,v49\nM v[212:215],a[4:7],v[92:95],v[212:215]\nds_read_b128 v[92:95],v20 offset:22528\nv_mul_f32 v53,v24,v53\nM v[216:219],a[0:3],v[76:79],0\n" \
"v_mul_f32 v50,v24,v50\nv_mul_f32 v51,v24,v51\nM v[216:219],a[8:11],v[88:91],v[216:219]\nv_mul_f32 v54,v24,v54\nv_mul_f32 v55,v24,v55\nM v[216:219],a[16:19],v[60:63],v[216:219]\ns_waitcnt lgkmcnt(3)\nC v160,v48,v49\n" \
"C v162,v52,v53\nM v[216:219],a[24:27],v[84:87],v[216:219]\nC v161,v50,v51\nC v163,v54,v55\nM v[220:223],a[28:31],v[84:87],0\nds_read_b128 v[84:87],v20 offset:23552\nv_lshlrev_b32 v48,16,v160\nM v[220:223],a[20:23],v[60:63],v[220:223]\n" \
"ds_read_b128 v[60:63],v20 offset:28672\nv_lshlrev_b32 v52,16,v162\nM v[220:223],a[12:15],v[88:91],v[220:223]\nds_read_b128 v[88:91],v20 offset:29696\nv_and_b32 v49,0xffff0000,v160\nM v[220:223],a[4:7],v[76:79],v[220:223]\ns_waitcnt lgkmcnt(3)\nds_read_b128 v[76:79],v20 offset:30720\n" \
"v_lshlrev_b32 v50,16,v161\nM v[208:211],a[32:35],v[80:83],v[208:211]\nv_and_b32 v53,0xffff0000,v162\nv_lshlrev_b32 v54,16,v163\nM v[208:211],a[40:43],v[72:75],v[208:211]\nv_and_b32 v51,0xffff0000,v161\nv_and_b32 v55,0xffff0000,v163\nM v[208:211],a[48:51],v[92:95],v[208:211]\n" \
"s_waitcnt lgkmcnt(3)\nC v164,v64,v65\nC v166,v68,v69\nM v[208:211],a[56:59],v[84:87],v[208:211]\nC v165,v66,v67\nC v167,v70,v71\nM v[212:215],a[60:63],v[84:87],v[212:215]\ns_waitcnt vmcnt(2)\n" \
"ds_read_b128 v[84:87],v20 offset:31744\nv_max_u32 v10,v176,v177\nM v[212:215],a[52:55],v[92:95],v[212:215]\nds_read_b128 v[92:95],v20 offset:49152\nv_max3_u32 v10,v10,v178,v179\nM v[212:215],a[44:47],v[72:75],v[212:215]\nds_read_b128 v[72:75],v20 offset:50176\nv_max3_u32 v10,v10,v180,v181\n" \
"M v[212:215],a[36:39],v[80:83],v[212:215]\ns_waitcnt lgkmcnt(3)\nds_read_b128 v[80:83],v20 offset:51200\nv_max3_u32 v10,v10,v182,v183\nM v[216:219],a[32:35],v[60:63],v[216:219]\nv_max3_u32 v10,v10,v32,v33\nv_max3_u32 v10,v10,v34,v35\nM v[216:219],a[40:43],v[88:91],v[216:219]\n" \
"v_max3_u32 v10,v10,v36,v37\nv_max3_u32 v10,v10,v38,v39\nM v[216:219],a[48:51],v[76:79],v[216:219]\ns_waitcnt lgkmcnt(3)\nv_cmp_eq_u32 vcc,-1,v10\nv_add_f32 v48,v48,v128\nM v[216:219],a[56:59],v[84:87],v[216:219]\nv_add_f32 v52,v52,v132\n" \
"v_add_f32 v49,v49,v129\nM v[220:223],a[60:63],v[84:87],v[220:223]\ns_cbranch_vccnz LIN_%=\nLINR_%=:\nds_read_b128 v[84:87],v20 offset:52224\nv_add_f32 v50,v50,v130\nM v[220:223],a[52:55],v[76:79],v[220:223]\nds_read_b128 v[76:79],v20 offset:57344\n" \
"v_add_f32 v53,v53,v133\nM v[220:223],a[44:47],v[88:91],v[220:223]\nds_read_b128 v[88:91],v20 offset:58368\nv_add_f32 v54,v54,v134\nM v[220:223],a[36:39],v[60:63],v[220:223]\ns_waitcnt lgkmcnt(3)\nds_read_b128 v[60:63],v20 offset:59392\nv_add_f32 v51,v51,v131\n" \
"M v[208:211],a[64:67],v[92:95],v[208:211]\nv_add_f32 v55,v55,v135\nv_lshlrev_b32 v64,16,v164\nM v[208:211],a[72:75],v[72:75],v[208:211]\nv_lshlrev_b32 v68,16,v166\nv_and_b32 v65,0xffff0000,v164\nM v[208:211],a[80:83],v[80:83],v[208:211]\ns_waitcnt lgkmcnt(3)\n" \
"v_and_b32 v69,0xffff0000,v166\nv_lshlrev_b32 v66,16,v165\nM v[208:211],a[88:91],v[84:87],v[208:211]\nv_lshlrev_b32 v70,16,v167\nv_and_b32 v67,0xffff0000,v165\nM v[212:215],a[92:95],v[84:87],v[212:215]\nds_read_b128 v[84:87],v20 offset:60416\nv_and_b32 v71,0xffff0000,v167\n" \
"M v[212:215],a[84:87],v[80:83],v[212:215]\nds_read_b128 v[80:83],v20 offset:53248\nC v160,v48,v49\nM v[212:215],a[76:79],v[72:75],v[212:215]\nds_read_b128 v[72:75],v20 offset:54272\nC v162,v52,v53\nM v[212:215],a[68:71],v[92:95],v[212:215]\ns_waitcnt lgkmcnt(3)\n" \
"ds_read_b128 v[92:95],v20 offset:55296\nC v161,v50,v51\nM v[216:219],a[64:67],v[76:79],v[216:219]\nC v163,v54,v55\nv_mul_f32 v64,v25,v64\nM v[216:219],a[72:75],v[88:91],v[216:219]\nv_mul_f32 v68,v25,v68\nv_mul_f32 v65,v25,v65\n" \
"M v[216:219],a[80:83],v[60:63],v[216:219]\ns_waitcnt lgkmcnt(3)\nv_mul_f32 v69,v25,v69\nv_mul_f32 v66,v25,v66\nM v[216:219],a[88:91],v[84:87],v[216:219]\nv_mul_f32 v70,v25,v70\nv_mul_f32 v67,v25,v67\nM v[220:223],a[92:95],v[84:87],v[220:223]\n" \
"ds_read_b128 v[84:87],v20 offset:56320\nv_mul_f32 v71,v25,v71\nM v[220:223],a[84:87],v[60:63],v[220:223]\nds_read_b128 v[60:63],v20 offset:61440\nC v164,v64,v65\nM v[220:223],a[76:79],v[88:91],v[220:223]\nds_read_b128 v[88:91],v20 offset:62464\nC v166,v68,v69\n" \
"M v[220:223],a[68:71],v[76:79],v[220:223]\ns_waitcnt lgkmcnt(3)\nds_read_b128 v[76:79],v20 offset:63488\nC v165,v66,v67\nM v[208:211],a[96:99],v[80:83],v[208:211]\ns_waitcnt lgkmcnt(3)\nv_permlane16_swap_b32 v160,v162\nM v[208:211],a[104:107],v[72:75],v[208:211]\n" \
"s_waitcnt vmcnt(6) lgkmcnt(15)\nv_permlane16_swap_b32 v161,v163\nM v[208:211],a[112:115],v[92:95],v[208:211]\ns_barrier\nC v167,v70,v71\nM v[208:211],a[120:123],v[84:87],v[208:211]\nv_lshlrev_b32 v64,16,v164\nv_lshlrev_b32 v68,16,v166\n" \
"M v[212:215],a[124:127],v[84:87],v[212:215]\nds_read_b128 v[84:87],v20 offset:64512\nv_and_b32 v65,0xffff0000,v164\nM v[212:215],a[116:119],v[92:95],v[212:215]\nds_read_b128 v[92:95],v4 offset:0\nv_and_b32 v69,0xffff0000,v166\nM v[212:215],a[108:111],v[72:75],v[212:215]\nds_read_b128 v[72:75],v4 offset:1024\n" \
"v_lshlrev_b32 v66,16,v165\nM v[212:215],a[100:103],v[80:83],v[212:215]\ns_waitcnt lgkmcnt(3)\nds_read_b128 v[80:83],v4 offset:2048\nv_lshlrev_b32 v70,16,v167\nM v[216:219],a[96:99],v[60:63],v[216:219]\nv_and_b32 v67,0xffff0000,v165\nv_and_b32 v71,0xffff0000,v167\n" \
"M v[216:219],a[104:107],v[88:91],v[216:219]\nv_add_f32 v64,v64,v136\nv_add_f32 v68,v68,v140\nM v[216:219],a[112:115],v[76:79],v[216:219]\ns_waitcnt lgkmcnt(3)\nv_add_f32 v65,v65,v137\nv_add_f32 v69,v69,v141\nM v[216:219],a[120:123],v[84:87],v[216:219]\n" \
"v_add_f32 v66,v66,v138\nv_add_f32 v70,v70,v142\nM v[220:223],a[124:127],v[84:87],v[220:223]\nds_read_b128 v[84:87],v4 offset:3072\nv_add_f32 v67,v67,v139\nM v[220:223],a[116:119],v[76:79],v[220:223]\nds_read_b128 v[76:79],v4 offset:8192\nv_add_f32 v71,v71,v143\n" \
"M v[220:223],a[108:111],v[88:91],v[220:223]\nds_read_b128 v[88:91],v4 offset:9216\nC v164,v64,v65\nM v[220:223],a[100:103],v[60:63],v[220:223]\ns_waitcnt lgkmcnt(3)\nds_read_b128 v[60:63],v4 offset:10240\nC v166,v68,v69\nM v[208:211],a[128:131],v[92:95],v[208:211]\n" \
"C v165,v66,v67\nC v167,v70,v71\nM v[208:211],a[136:139],v[72:75],v[208:211]\nv_permlane16_swap_b32 v164,v166\nM v[208:211],a[144:147],v[80:83],v[208:211]\ns_waitcnt lgkmcnt(3)\nv_permlane16_swap_b32 v165,v167\nM v[208:211],a[152:155],v[84:87],v[208:211]\n" \
"M v[212:215],a[156:159],v[84:87],v[212:215]\nds_read_b128 v[84:87],v4 offset:11264\nM v[212:215],a[148:151],v[80:83],v[212:215]\nds_read_b128 v[80:83],v4 offset:4096\nM v[212:215],a[140:143],v[72:75],v[212:215]\nds_read_b128 v[72:75],v4 offset:5120\nM v[212:215],a[132:135],v[92:95],v[212:215]\ns_waitcnt lgkmcnt(3)\n" \
"ds_read_b128 v[92:95],v4 offset:6144\nM v[216:219],a[128:131],v[76:79],v[216:219]\nM v[216:219],a[136:139],v[88:91],v[216:219]\nM v[216:219],a[144:147],v[60:63],v[216:219]\ns_waitcnt lgkmcnt(3)\nM v[216:219],a[152:155],v[84:87],v[216:219]\nM v[220:223],a[156:159],v[84:87],v[220:223]\nds_read_b128 v[84:87],v4 offset:7168\n" \
"M v[220:223],a[148:151],v[60:63],v[220:223]\nds_read_b128 v[60:63],v4 offset:12288\nM v[220:223],a[140:143],v[88:91],v[220:223]\nds_read_b128 v[88:91],v4 offset:13312\nM v[220:223],a[132:135],v[76:79],v[220:223]\ns_waitcnt lgkmcnt(3)\nds_read_b128 v[76:79],v4 offset:14336\nM v[208:211],a[160:163],v[80:83],v[208:211]\n" \
"M v[208:211],a[168:171],v[72:75],v[208:211]\nM v[208:211],a[176:179],v[92:95],v[208:211]\ns_waitcnt lgkmcnt(3)\nM v[208:211],a[184:187],v[84:87],v[208:211]\nM v[212:215],a[188:191],v[84:87],v[212:215]\nds_read_b128 v[84:87],v4 offset:15360\nM v[212:215],a[180:183],v[92:95],v[212:215]\ns_add_u32 s63,s49,8192\n" \
"ds_read_b128 v[92:95],v4 offset:32768\nM v[212:215],a[172:175],v[72:75],v[212:215]\nbuffer_load_dwordx4 v[40:43],v4,s[36:39],s63 offen offset:2048 sc1\nM v[212:215],a[164:167],v[80:83],v[212:215]\ns_waitcnt lgkmcnt(2)\nds_read_b128 v[72:75],v4 offset:33792\nds_read_b128 v[80:83],v4 offset:34816\nM v[216:219],a[160:163],v[60:63],v[216:219]\n" \
"M v[216:219],a[168:171],v[88:91],v[216:219]\ns_waitcnt lgkmcnt(3)\nM v[216:219],a[176:179],v[76:79],v[216:219]\nbuffer_load_dwordx4 v[44:47],v4,s[36:39],s63 offen offset:3072 sc1\nM v[216:219],a[184:187],v[84:87],v[216:219]\nM v[220:223],a[188:191],v[84:87],v[220:223]\nds_read_b128 v[84:87],v4 offset:35840\nM v[220:223],a[180:183],v[76:79],v[220:223]\n" \
"LG26_%=:\ns_cbranch_execz LHR3_%=\ns_waitcnt lgkmcnt(1)\nds_read_b128 v[76:79],v4 offset:40960\nM v[220:223],a[172:175],v[88:91],v[220:223]\nbuffer_store_dwordx4 v[160:163],v13,s[88:91],s58 offen offset:0 sc1\nM v[220:223],a[164:167],v[60:63],v[220:223]\ns_add_u32 s58,s81,0\n" \
"ds_read_b128 v[88:91],v4 offset:41984\nds_read_b128 v[60:63],v4 offset:43008\nM v[208:211],a[192:195],v[92:95],v[208:211]\nM v[208:211],a[200:203],v[72:75],v[208:211]\nM v[208:211],a[208:211],v[80:83],v[208:211]\ns_waitcnt lgkmcnt(3)\nM v[208:211],a[216:219],v[84:87],v[208:211]\nM v[212:215],a[220:223],v[84:87],v[212:215]\n" \
"ds_read_b128 v[84:87],v4 offset:44032\nM v[212:215],a[212:215],v[80:83],v[212:215]\ns_waitcnt lgkmcnt(1)\nds_read_b128 v[80:83],v4 offset:36864\nM v[212:215],a[204:207],v[72:75],v[212:215]\nbuffer_store_dwordx4 v[164:167],v13,s[88:91],s59 offen offset:0 sc1\nM v[212:215],a[196:199],v[92:95],v[212:215]\ns_add_u32 s59,s81,65536\n" \
"ds_read_b128 v[72:75],v4 offset:37888\nds_read_b128 v[92:95],v4 offset:38912\nM v[216:219],a[192:195],v[76:79],v[216:219]\nM v[216:219],a[200:203],v[88:91],v[216:219]\nM v[216:219],a[208:211],v[60:63],v[216:219]\ns_waitcnt lgkmcnt(3)\nM v[216:219],a[216:219],v[84:87],v[216:219]\nM v[220:223],a[220:223],v[84:87],v[220:223]\n" \
"ds_read_b128 v[84:87],v4 offset:39936\nM v[220:223],a[212:215],v[60:63],v[220:223]\nds_read_b128 v[60:63],v4 offset:45056\nM v[220:223],a[204:207],v[88:91],v[220:223]\nds_read_b128 v[88:91],v4 offset:46080\nM v[220:223],a[196:199],v[76:79],v[220:223]\ns_waitcnt lgkmcnt(3)\nds_read_b128 v[76:79],v4 offset:47104\n" \
"M v[208:211],a[224:227],v[80:83],v[208:211]\nM v[208:211],a[232:235],v[72:75],v[208:211]\nM v[208:211],a[240:243],v[92:95],v[208:211]\ns_waitcnt lgkmcnt(3)\nM v[208:211],a[248:251],v[84:87],v[208:211]\nM v[212:215],a[252:255],v[84:87],v[212:215]\nds_read_b128 v[84:87],v4 offset:48128\nM v[212:215],a[244:247],v[92:95],v[212:215]\n" \
"M v[212:215],a[236:239],v[72:75],v[212:215]\nM v[212:215],a[228:231],v[80:83],v[212:215]\ns_waitcnt lgkmcnt(1)\nM v[216:219],a[224:227],v[60:63],v[216:219]\nM v[216:219],a[232:235],v[88:91],v[216:219]\nM v[216:219],a[240:243],v[76:79],v[216:219]\ns_waitcnt lgkmcnt(0)\nM v[216:219],a[248:251],v[84:87],v[216:219]\n" \
"M v[220:223],a[252:255],v[84:87],v[220:223]\nM v[220:223],a[244:247],v[76:79],v[220:223]\nM v[220:223],a[236:239],v[88:91],v[220:223]\nM v[220:223],a[228:231],v[60:63],v[220:223]\nM v[208:211],v[186:189],v[176:179],v[208:211]\nM v[212:215],v[184:187],v[176:179],v[212:215]\nM v[216:219],v[186:189],v[180:183],v[216:219]\ns_waitcnt vmcnt(2)\n" \
"v_max_u32 v10,v40,v41\nv_max3_u32 v10,v10,v42,v43\nM v[220:223],v[184:187],v[180:183],v[220:223]\nv_max3_u32 v10,v10,v44,v45\nv_max3_u32 v10,v10,v46,v47\nM v[208:211],v[186:189],v[32:35],v[208:211]\nv_cmp_eq_u32 vcc,-1,v10\nM v[212:215],v[184:187],v[32:35],v[212:215]\n" \
"M v[216:219],v[186:189],v[36:39],v[216:219]\ns_cbranch_vccnz LIO_%=\nLIOR_%=:\nM v[220:223],v[184:187],v[36:39],v[220:223]\nM v[208:211],v[186:189],v[40:43],v[208:211]\nM v[212:215],v[184:187],v[40:43],v[212:215]\nM v[216:219],v[186:189],v[44:47],v[216:219]\nC v160,v208,v209\n" \
"C v161,v210,v211\nM v[220:223],v[184:187],v[44:47],v[220:223]\nC v162,v212,v213\nC v163,v214,v215\nv_lshlrev_b32 v208,16,v160\nv_lshlrev_b32 v210,16,v161\nv_lshlrev_b32 v212,16,v162\nv_lshlrev_b32 v214,16,v163\n" \
"v_and_b32 v209,0xffff0000,v160\nv_and_b32 v211,0xffff0000,v161\nv_and_b32 v213,0xffff0000,v162\nv_and_b32 v215,0xffff0000,v163\nv_pk_mul_f32 v[208:209],v[208:209],v[26:27] op_sel_hi:[1,0]\nv_pk_mul_f32 v[210:211],v[210:211],v[26:27] op_sel_hi:[1,0]\nv_pk_mul_f32 v[212:213],v[212:213],v[26:27] op_sel_hi:[1,0]\nv_pk_mul_f32 v[214:215],v[214:215],v[26:27] op_sel_hi:[1,0]\n" \
"C v160,v208,v209\nC v161,v210,v211\nC v162,v212,v213\nC v163,v214,v215\nv_lshlrev_b32 v208,16,v160\nv_lshlrev_b32 v210,16,v161\nv_lshlrev_b32 v212,16,v162\nv_lshlrev_b32 v214,16,v163\n" \
"v_and_b32 v209,0xffff0000,v160\nv_and_b32 v211,0xffff0000,v161\nv_and_b32 v213,0xffff0000,v162\nv_and_b32 v215,0xffff0000,v163\nv_pk_add_f32 v[208:209],v[208:209],v[144:145]\nv_pk_add_f32 v[210:211],v[210:211],v[146:147]\nv_pk_add_f32 v[212:213],v[212:213],v[148:149]\nv_pk_add_f32 v[214:215],v[214:215],v[150:151]\n" \
"C v160,v208,v209\nC v161,v210,v211\nC v162,v212,v213\nC v163,v214,v215\ns_nop 0\nv_permlane16_swap_b32 v160,v162\nv_permlane16_swap_b32 v161,v163\nbuffer_store_dwordx4 v[160:163],v13,s[88:91],s58 offen offset:0 sc1\n" \
"C v164,v216,v217\nC v165,v218,v219\nC v166,v220,v221\nC v167,v222,v223\nv_lshlrev_b32 v216,16,v164\nv_lshlrev_b32 v218,16,v165\nv_lshlrev_b32 v220,16,v166\nv_lshlrev_b32 v222,16,v167\n" \
"v_and_b32 v217,0xffff0000,v164\nv_and_b32 v219,0xffff0000,v165\nv_and_b32 v221,0xffff0000,v166\nv_and_b32 v223,0xffff0000,v167\nv_pk_mul_f32 v[216:217],v[216:217],v[26:27] op_sel:[0,1] op_sel_hi:[1,1]\nv_pk_mul_f32 v[218:219],v[218:219],v[26:27] op_sel:[0,1] op_sel_hi:[1,1]\nv_pk_mul_f32 v[220:221],v[220:221],v[26:27] op_sel:[0,1] op_sel_hi:[1,1]\nv_pk_mul_f32 v[222:223],v[222:223],v[26:27] op_sel:[0,1] op_sel_hi:[1,1]\n" \
"C v164,v216,v217\nC v165,v218,v219\nC v166,v220,v221\nC v167,v222,v223\nv_lshlrev_b32 v216,16,v164\nv_lshlrev_b32 v218,16,v165\nv_lshlrev_b32 v220,16,v166\nv_lshlrev_b32 v222,16,v167\n" \
"v_and_b32 v217,0xffff0000,v164\nv_and_b32 v219,0xffff0000,v165\nv_and_b32 v221,0xffff0000,v166\nv_and_b32 v223,0xffff0000,v167\nv_pk_add_f32 v[216:217],v[216:217],v[152:153]\nv_pk_add_f32 v[218:219],v[218:219],v[154:155]\nv_pk_add_f32 v[220:221],v[220:221],v[156:157]\nv_pk_add_f32 v[222:223],v[222:223],v[158:159]\n" \
"C v164,v216,v217\nC v165,v218,v219\nC v166,v220,v221\nC v167,v222,v223\ns_nop 0\nv_permlane16_swap_b32 v164,v166\nv_permlane16_swap_b32 v165,v167\nbuffer_store_dwordx4 v[164:167],v13,s[88:91],s59 offen offset:0 sc1\n" \
"s_endpgm\n.p2align 6\nLG27_%=:\ns_cbranch_execz LHR1_%=\nLH1_%=:\nds_read_b128 a[128:131],v21 offset:8192\nds_read_b128 a[132:135],v21 offset:9216\nds_read_b128 a[136:139],v21 offset:10240\n" \
"ds_read_b128 a[140:143],v21 offset:11264\nds_read_b128 a[144:147],v21 offset:12288\nds_read_b128 a[148:151],v21 offset:13312\nds_read_b128 a[152:155],v21 offset:14336\nds_read_b128 a[156:159],v21 offset:15360\nds_read_b128 a[160:163],v21 offset:0\nds_read_b128 a[164:167],v21 offset:1024\nds_read_b128 a[168:171],v21 offset:2048\n" \
"ds_read_b128 a[172:175],v21 offset:3072\nds_read_b128 a[176:179],v21 offset:4096\nds_read_b128 a[180:183],v21 offset:5120\nds_read_b128 a[184:187],v21 offset:6144\nds_read_b128 a[188:191],v21 offset:7168\nv_max3_f32 v60,v96,v97,v98\nv_max3_f32 v61,v99,v104,v105\nv_max3_f32 v60,v60,v106,v107\n" \
"v_max3_f32 v61,v61,v112,v113\nv_max3_f32 v60,v60,v114,v115\nv_max3_f32 v61,v61,v120,v121\nv_max3_f32 v60,v60,v122,v123\nv_max3_f32 v61,v61,v100,v101\nv_max3_f32 v60,v60,v102,v103\nv_max3_f32 v61,v61,v108,v109\nv_max3_f32 v60,v60,v110,v111\n" \
"v_max3_f32 v61,v61,v116,v117\nv_max3_f32 v60,v60,v118,v119\nv_max3_f32 v61,v61,v124,v125\nv_max3_f32 v61,v61,v126,v127\nv_max_f32 v60,v60,v61\ns_mov_b32 s75,1\nv_cmp_nge_f32 vcc,0x41f00000,v60\nv_xor_b32 v13,0x2000,v13\n" \
"s_cbranch_vccz LH1X_%=\nLRS_%=:\ns_nop 7\nv_max3_f32 v60,v96,v97,v98\nv_max3_f32 v61,v99,v100,v101\nv_max3_f32 v60,v60,v102,v103\nv_max3_f32 v60,v60,v61,0\nv_mov_b32 v61,v60\n" \
"s_nop 1\nv_permlane32_swap_b32 v60,v61\nv_max_f32 v60,v60,v61\nv_mov_b32 v61,v60\ns_nop 1\nv_permlane16_swap_b32 v60,v61\nv_max_f32 v60,v60,v61\nv_exp_f32 v62,-v60\n" \
"v_sub_f32 v96,v96,v60\nv_sub_f32 v97,v97,v60\nv_sub_f32 v98,v98,v60\nv_sub_f32 v99,v99,v60\nv_sub_f32 v100,v100,v60\nv_sub_f32 v101,v101,v60\nv_sub_f32 v102,v102,v60\nv_sub_f32 v103,v103,v60\n" \
"v_sub_f32 v32,v32,v60\nv_sub_f32 v33,v33,v60\nv_sub_f32 v34,v34,v60\nv_sub_f32 v35,v35,v60\nv_mul_f32 v16,v16,v62\nv_mul_f32 v128,v128,v62\nv_mul_f32 v129,v129,v62\nv_mul_f32 v130,v130,v62\n" \
"v_mul_f32 v131,v131,v62\nv_mul_f32 v132,v132,v62\nv_mul_f32 v133,v133,v62\nv_mul_f32 v134,v134,v62\nv_mul_f32 v135,v135,v62\nv_mul_f32 v136,v136,v62\nv_mul_f32 v137,v137,v62\nv_mul_f32 v138,v138,v62\n" \
"v_mul_f32 v139,v139,v62\nv_mul_f32 v140,v140,v62\nv_mul_f32 v141,v141,v62\nv_mul_f32 v142,v142,v62\nv_mul_f32 v143,v143,v62\nv_mul_f32 v144,v144,v62\nv_mul_f32 v145,v145,v62\nv_mul_f32 v146,v146,v62\n" \
"v_mul_f32 v147,v147,v62\nv_mul_f32 v148,v148,v62\nv_mul_f32 v149,v149,v62\nv_mul_f32 v150,v150,v62\nv_mul_f32 v151,v151,v62\nv_mul_f32 v152,v152,v62\nv_mul_f32 v153,v153,v62\nv_mul_f32 v154,v154,v62\n" \
"v_mul_f32 v155,v155,v62\nv_mul_f32 v156,v156,v62\nv_mul_f32 v157,v157,v62\nv_mul_f32 v158,v158,v62\nv_mul_f32 v159,v159,v62\nv_lshlrev_b32 v61,16,v64\nv_and_b32 v63,0xffff0000,v64\nv_mul_f32 v61,v61,v62\n" \
"v_mul_f32 v63,v63,v62\nC v64,v61,v63\nv_lshlrev_b32 v61,16,v65\nv_and_b32 v63,0xffff0000,v65\nv_mul_f32 v61,v61,v62\nv_mul_f32 v63,v63,v62\nC v65,v61,v63\nv_lshlrev_b32 v61,16,v66\n" \
"v_and_b32 v63,0xffff0000,v66\nv_mul_f32 v61,v61,v62\nv_mul_f32 v63,v63,v62\nC v66,v61,v63\nv_lshlrev_b32 v61,16,v67\nv_and_b32 v63,0xffff0000,v67\nv_mul_f32 v61,v61,v62\nv_mul_f32 v63,v63,v62\n" \
"C v67,v61,v63\nv_max3_f32 v60,v104,v105,v106\nv_max3_f32 v61,v107,v108,v109\nv_max3_f32 v60,v60,v110,v111\nv_max3_f32 v60,v60,v61,0\nv_mov_b32 v61,v60\ns_nop 1\nv_permlane32_swap_b32 v60,v61\n" \
"v_max_f32 v60,v60,v61\nv_mov_b32 v61,v60\ns_nop 1\nv_permlane16_swap_b32 v60,v61\nv_max_f32 v60,v60,v61\nv_exp_f32 v62,-v60\nv_sub_f32 v104,v104,v60\nv_sub_f32 v105,v105,v60\n" \
"v_sub_f32 v106,v106,v60\nv_sub_f32 v107,v107,v60\nv_sub_f32 v108,v108,v60\nv_sub_f32 v109,v109,v60\nv_sub_f32 v110,v110,v60\nv_sub_f32 v111,v111,v60\nv_sub_f32 v36,v36,v60\nv_sub_f32 v37,v37,v60\n" \
"v_sub_f32 v38,v38,v60\nv_sub_f32 v39,v39,v60\nv_mul_f32 v17,v17,v62\nv_mul_f32 v160,v160,v62\nv_mul_f32 v161,v161,v62\nv_mul_f32 v162,v162,v62\nv_mul_f32 v163,v163,v62\nv_mul_f32 v164,v164,v62\n" \
"v_mul_f32 v165,v165,v62\nv_mul_f32 v166,v166,v62\nv_mul_f32 v167,v167,v62\nv_mul_f32 v168,v168,v62\nv_mul_f32 v169,v169,v62\nv_mul_f32 v170,v170,v62\nv_mul_f32 v171,v171,v62\nv_mul_f32 v172,v172,v62\n" \
"v_mul_f32 v173,v173,v62\nv_mul_f32 v174,v174,v62\nv_mul_f32 v175,v175,v62\nv_mul_f32 v176,v176,v62\nv_mul_f32 v177,v177,v62\nv_mul_f32 v178,v178,v62\nv_mul_f32 v179,v179,v62\nv_mul_f32 v180,v180,v62\n" \
"v_mul_f32 v181,v181,v62\nv_mul_f32 v182,v182,v62\nv_mul_f32 v183,v183,v62\nv_mul_f32 v184,v184,v62\nv_mul_f32 v185,v185,v62\nv_mul_f32 v186,v186,v62\nv_mul_f32 v187,v187,v62\nv_mul_f32 v188,v188,v62\n" \
"v_mul_f32 v189,v189,v62\nv_mul_f32 v190,v190,v62\nv_mul_f32 v191,v191,v62\nv_lshlrev_b32 v61,16,v72\nv_and_b32 v63,0xffff0000,v72\nv_mul_f32 v61,v61,v62\nv_mul_f32 v63,v63,v62\nC v72,v61,v63\n" \
"v_lshlrev_b32 v61,16,v73\nv_and_b32 v63,0xffff0000,v73\nv_mul_f32 v61,v61,v62\nv_mul_f32 v63,v63,v62\nC v73,v61,v63\nv_lshlrev_b32 v61,16,v74\nv_and_b32 v63,0xffff0000,v74\nv_mul_f32 v61,v61,v62\n" \
"v_mul_f32 v63,v63,v62\nC v74,v61,v63\nv_lshlrev_b32 v61,16,v75\nv_and_b32 v63,0xffff0000,v75\nv_mul_f32 v61,v61,v62\nv_mul_f32 v63,v63,v62\nC v75,v61,v63\nv_max3_f32 v60,v112,v113,v114\n" \
"v_max3_f32 v61,v115,v116,v117\nv_max3_f32 v60,v60,v118,v119\nv_max3_f32 v60,v60,v61,0\nv_mov_b32 v61,v60\ns_nop 1\nv_permlane32_swap_b32 v60,v61\nv_max_f32 v60,v60,v61\nv_mov_b32 v61,v60\n" \
"s_nop 1\nv_permlane16_swap_b32 v60,v61\nv_max_f32 v60,v60,v61\nv_exp_f32 v62,-v60\nv_sub_f32 v112,v112,v60\nv_sub_f32 v113,v113,v60\nv_sub_f32 v114,v114,v60\nv_sub_f32 v115,v115,v60\n" \
"v_sub_f32 v116,v116,v60\nv_sub_f32 v117,v117,v60\nv_sub_f32 v118,v118,v60\nv_sub_f32 v119,v119,v60\nv_sub_f32 v40,v40,v60\nv_sub_f32 v41,v41,v60\nv_sub_f32 v42,v42,v60\nv_sub_f32 v43,v43,v60\n" \
"v_mul_f32 v18,v18,v62\nv_mul_f32 v192,v192,v62\nv_mul_f32 v193,v193,v62\nv_mul_f32 v194,v194,v62\nv_mul_f32 v195,v195,v62\nv_mul_f32 v196,v196,v62\nv_mul_f32 v197,v197,v62\nv_mul_f32 v198,v198,v62\n" \
"v_mul_f32 v199,v199,v62\nv_mul_f32 v200,v200,v62\nv_mul_f32 v201,v201,v62\nv_mul_f32 v202,v202,v62\nv_mul_f32 v203,v203,v62\nv_mul_f32 v204,v204,v62\nv_mul_f32 v205,v205,v62\nv_mul_f32 v206,v206,v62\n" \
"v_mul_f32 v207,v207,v62\nv_mul_f32 v208,v208,v62\nv_mul_f32 v209,v209,v62\nv_mul_f32 v210,v210,v62\nv_mul_f32 v211,v211,v62\nv_mul_f32 v212,v212,v62\nv_mul_f32 v213,v213,v62\nv_mul_f32 v214,v214,v62\n" \
"v_mul_f32 v215,v215,v62\nv_mul_f32 v216,v216,v62\nv_mul_f32 v217,v217,v62\nv_mul_f32 v218,v218,v62\nv_mul_f32 v219,v219,v62\nv_mul_f32 v220,v220,v62\nv_mul_f32 v221,v221,v62\nv_mul_f32 v222,v222,v62\n" \
"v_mul_f32 v223,v223,v62\nv_lshlrev_b32 v61,16,v80\nv_and_b32 v63,0xffff0000,v80\nv_mul_f32 v61,v61,v62\nv_mul_f32 v63,v63,v62\nC v80,v61,v63\nv_lshlrev_b32 v61,16,v81\nv_and_b32 v63,0xffff0000,v81\n" \
"v_mul_f32 v61,v61,v62\nv_mul_f32 v63,v63,v62\nC v81,v61,v63\nv_lshlrev_b32 v61,16,v82\nv_and_b32 v63,0xffff0000,v82\nv_mul_f32 v61,v61,v62\nv_mul_f32 v63,v63,v62\nC v82,v61,v63\n" \
"v_lshlrev_b32 v61,16,v83\nv_and_b32 v63,0xffff0000,v83\nv_mul_f32 v61,v61,v62\nv_mul_f32 v63,v63,v62\nC v83,v61,v63\nv_max3_f32 v60,v120,v121,v122\nv_max3_f32 v61,v123,v124,v125\nv_max3_f32 v60,v60,v126,v127\n" \
"v_max3_f32 v60,v60,v61,0\nv_mov_b32 v61,v60\ns_nop 1\nv_permlane32_swap_b32 v60,v61\nv_max_f32 v60,v60,v61\nv_mov_b32 v61,v60\ns_nop 1\nv_permlane16_swap_b32 v60,v61\n" \
"v_max_f32 v60,v60,v61\nv_exp_f32 v62,-v60\nv_sub_f32 v120,v120,v60\nv_sub_f32 v121,v121,v60\nv_sub_f32 v122,v122,v60\nv_sub_f32 v123,v123,v60\nv_sub_f32 v124,v124,v60\nv_sub_f32 v125,v125,v60\n" \
"v_sub_f32 v126,v126,v60\nv_sub_f32 v127,v127,v60\nv_sub_f32 v44,v44,v60\nv_sub_f32 v45,v45,v60\nv_sub_f32 v46,v46,v60\nv_sub_f32 v47,v47,v60\nv_mul_f32 v19,v19,v62\nv_mul_f32 v224,v224,v62\n" \
"v_mul_f32 v225,v225,v62\nv_mul_f32 v226,v226,v62\nv_mul_f32 v227,v227,v62\nv_mul_f32 v228,v228,v62\nv_mul_f32 v229,v229,v62\nv_mul_f32 v230,v230,v62\nv_mul_f32 v231,v231,v62\nv_mul_f32 v232,v232,v62\n" \
"v_mul_f32 v233,v233,v62\nv_mul_f32 v234,v234,v62\nv_mul_f32 v235,v235,v62\nv_mul_f32 v236,v236,v62\nv_mul_f32 v237,v237,v62\nv_mul_f32 v238,v238,v62\nv_mul_f32 v239,v239,v62\nv_mul_f32 v240,v240,v62\n" \
"v_mul_f32 v241,v241,v62\nv_mul_f32 v242,v242,v62\nv_mul_f32 v243,v243,v62\nv_mul_f32 v244,v244,v62\nv_mul_f32 v245,v245,v62\nv_mul_f32 v246,v246,v62\nv_mul_f32 v247,v247,v62\nv_mul_f32 v248,v248,v62\n" \
"v_mul_f32 v249,v249,v62\nv_mul_f32 v250,v250,v62\nv_mul_f32 v251,v251,v62\nv_mul_f32 v252,v252,v62\nv_mul_f32 v253,v253,v62\nv_mul_f32 v254,v254,v62\nv_mul_f32 v255,v255,v62\nv_lshlrev_b32 v61,16,v88\n" \
"v_and_b32 v63,0xffff0000,v88\nv_mul_f32 v61,v61,v62\nv_mul_f32 v63,v63,v62\nC v88,v61,v63\nv_lshlrev_b32 v61,16,v89\nv_and_b32 v63,0xffff0000,v89\nv_mul_f32 v61,v61,v62\nv_mul_f32 v63,v63,v62\n" \
"C v89,v61,v63\nv_lshlrev_b32 v61,16,v90\nv_and_b32 v63,0xffff0000,v90\nv_mul_f32 v61,v61,v62\nv_mul_f32 v63,v63,v62\nC v90,v61,v63\nv_lshlrev_b32 v61,16,v91\nv_and_b32 v63,0xffff0000,v91\n" \
"v_mul_f32 v61,v61,v62\nv_mul_f32 v63,v63,v62\nC v91,v61,v63\ns_cmp_eq_u32 s52,14\ns_cbranch_scc1 LH1R15_%=\nLH1X_%=:\nv_pk_mov_b32 v[32:33],v[28:29],v[28:29] op_sel:[0,0]\nv_pk_mov_b32 v[34:35],v[28:29],v[28:29] op_sel:[0,0]\n" \
"v_pk_mov_b32 v[36:37],v[28:29],v[28:29] op_sel:[1,1]\nv_pk_mov_b32 v[38:39],v[28:29],v[28:29] op_sel:[1,1]\nv_pk_mov_b32 v[40:41],v[30:31],v[30:31] op_sel:[0,0]\nv_pk_mov_b32 v[42:43],v[30:31],v[30:31] op_sel:[0,0]\nv_pk_mov_b32 v[44:45],v[30:31],v[30:31] op_sel:[1,1]\nv_pk_mov_b32 v[46:47],v[30:31],v[30:31] op_sel:[1,1]\ns_cmp_eq_u32 s52,-1\ns_cbranch_scc1 LH1R0_%=\n" \
"s_cmp_eq_u32 s52,0\ns_cbranch_scc1 LH1R1_%=\ns_cmp_eq_u32 s52,9\ns_cbranch_scc1 LH1R10_%=\ns_cmp_eq_u32 s52,11\ns_cbranch_scc1 LH1R12_%=\ns_cmp_eq_u32 s52,13\ns_cbranch_scc1 LH1R14_%=\n" \
"s_branch LH1R2_%=\n.p2align 6\nLG28_%=:\ns_cbranch_execz LHR1_%=\nLEV_%=:\ns_cmp_eq_u32 s52,s4\ns_cbranch_scc0 LEV3_%=\ns_cmp_lt_u32 %2,2\n" \
"s_cbranch_scc1 LEV2_%=\ns_mov_b64 exec,0\ns_cmp_eq_u32 %2,2\ns_cbranch_scc1 LG20_%=\ns_cmp_eq_u32 %2,3\ns_cbranch_scc1 LG21_%=\nLHR3_%=:\ns_mov_b64 exec,-1\n" \
"s_mov_b32 s4,50\ns_mov_b32 s56,50\ns_branch LEV4_%=\nLEV2_%=:\nv_rcp_f32 v60,v24\nv_rcp_f32 v61,v25\nv_rcp_f32 v62,v26\nv_rcp_f32 v63,v27\n" \
"v_xor_b32 v14,0x2000,v14\ns_mov_b32 s75,0\ns_mov_b32 s77,s68\nv_mov_b32 v24,0\nv_mov_b32 v25,0\nv_mov_b32 v26,0\nv_mov_b32 v27,0\nv_pk_mul_f32 v[128:129],v[128:129],v[60:61] op_sel_hi:[1,0]\n" \
"v_pk_mul_f32 v[130:131],v[130:131],v[60:61] op_sel_hi:[1,0]\nv_pk_mul_f32 v[132:133],v[132:133],v[60:61] op_sel_hi:[1,0]\nv_pk_mul_f32 v[134:135],v[134:135],v[60:61] op_sel_hi:[1,0]\nv_pk_mul_f32 v[136:137],v[136:137],v[60:61] op_sel_hi:[1,0]\nv_pk_mul_f32 v[138:139],v[138:139],v[60:61] op_sel_hi:[1,0]\nv_pk_mul_f32 v[140:141],v[140:141],v[60:61] op_sel_hi:[1,0]\nv_pk_mul_f32 v[142:143],v[142:143],v[60:61] op_sel_hi:[1,0]\nv_pk_mul_f32 v[144:145],v[144:145],v[60:61] op_sel_hi:[1,0]\n" \
"v_pk_mul_f32 v[146:147],v[146:147],v[60:61] op_sel_hi:[1,0]\nv_pk_mul_f32 v[148:149],v[148:149],v[60:61] op_sel_hi:[1,0]\nv_pk_mul_f32 v[150:151],v[150:151],v[60:61] op_sel_hi:[1,0]\nv_pk_mul_f32 v[152:153],v[152:153],v[60:61] op_sel_hi:[1,0]\nv_pk_mul_f32 v[154:155],v[154:155],v[60:61] op_sel_hi:[1,0]\nv_pk_mul_f32 v[156:157],v[156:157],v[60:61] op_sel_hi:[1,0]\nv_pk_mul_f32 v[158:159],v[158:159],v[60:61] op_sel_hi:[1,0]\nv_pk_mul_f32 v[160:161],v[160:161],v[60:61] op_sel:[0,1] op_sel_hi:[1,1]\n" \
"v_pk_mul_f32 v[162:163],v[162:163],v[60:61] op_sel:[0,1] op_sel_hi:[1,1]\nv_pk_mul_f32 v[164:165],v[164:165],v[60:61] op_sel:[0,1] op_sel_hi:[1,1]\nv_pk_mul_f32 v[166:167],v[166:167],v[60:61] op_sel:[0,1] op_sel_hi:[1,1]\nv_pk_mul_f32 v[168:169],v[168:169],v[60:61] op_sel:[0,1] op_sel_hi:[1,1]\nv_pk_mul_f32 v[170:171],v[170:171],v[60:61] op_sel:[0,1] op_sel_hi:[1,1]\nv_pk_mul_f32 v[172:173],v[172:173],v[60:61] op_sel:[0,1] op_sel_hi:[1,1]\nv_pk_mul_f32 v[174:175],v[174:175],v[60:61] op_sel:[0,1] op_sel_hi:[1,1]\nv_pk_mul_f32 v[176:177],v[176:177],v[60:61] op_sel:[0,1] op_sel_hi:[1,1]\n" \
"v_pk_mul_f32 v[178:179],v[178:179],v[60:61] op_sel:[0,1] op_sel_hi:[1,1]\nv_pk_mul_f32 v[180:181],v[180:181],v[60:61] op_sel:[0,1] op_sel_hi:[1,1]\nv_pk_mul_f32 v[182:183],v[182:183],v[60:61] op_sel:[0,1] op_sel_hi:[1,1]\nv_pk_mul_f32 v[184:185],v[184:185],v[60:61] op_sel:[0,1] op_sel_hi:[1,1]\nv_pk_mul_f32 v[186:187],v[186:187],v[60:61] op_sel:[0,1] op_sel_hi:[1,1]\nv_pk_mul_f32 v[188:189],v[188:189],v[60:61] op_sel:[0,1] op_sel_hi:[1,1]\nv_pk_mul_f32 v[190:191],v[190:191],v[60:61] op_sel:[0,1] op_sel_hi:[1,1]\nv_pk_mul_f32 v[192:193],v[192:193],v[62:63] op_sel_hi:[1,0]\n" \
"v_pk_mul_f32 v[194:195],v[194:195],v[62:63] op_sel_hi:[1,0]\nv_pk_mul_f32 v[196:197],v[196:197],v[62:63] op_sel_hi:[1,0]\nv_pk_mul_f32 v[198:199],v[198:199],v[62:63] op_sel_hi:[1,0]\nv_pk_mul_f32 v[200:201],v[200:201],v[62:63] op_sel_hi:[1,0]\nv_pk_mul_f32 v[202:203],v[202:203],v[62:63] op_sel_hi:[1,0]\nv_pk_mul_f32 v[204:205],v[204:205],v[62:63] op_sel_hi:[1,0]\nv_pk_mul_f32 v[206:207],v[206:207],v[62:63] op_sel_hi:[1,0]\nv_pk_mul_f32 v[208:209],v[208:209],v[62:63] op_sel_hi:[1,0]\n" \
"v_pk_mul_f32 v[210:211],v[210:211],v[62:63] op_sel_hi:[1,0]\nv_pk_mul_f32 v[212:213],v[212:213],v[62:63] op_sel_hi:[1,0]\nv_pk_mul_f32 v[214:215],v[214:215],v[62:63] op_sel_hi:[1,0]\nv_pk_mul_f32 v[216:217],v[216:217],v[62:63] op_sel_hi:[1,0]\nv_pk_mul_f32 v[218:219],v[218:219],v[62:63] op_sel_hi:[1,0]\nv_pk_mul_f32 v[220:221],v[220:221],v[62:63] op_sel_hi:[1,0]\nv_pk_mul_f32 v[222:223],v[222:223],v[62:63] op_sel_hi:[1,0]\nv_pk_mul_f32 v[224:225],v[224:225],v[62:63] op_sel:[0,1] op_sel_hi:[1,1]\n" \
"v_pk_mul_f32 v[226:227],v[226:227],v[62:63] op_sel:[0,1] op_sel_hi:[1,1]\nv_pk_mul_f32 v[228:229],v[228:229],v[62:63] op_sel:[0,1] op_sel_hi:[1,1]\nv_pk_mul_f32 v[230:231],v[230:231],v[62:63] op_sel:[0,1] op_sel_hi:[1,1]\nv_pk_mul_f32 v[232:233],v[232:233],v[62:63] op_sel:[0,1] op_sel_hi:[1,1]\nv_pk_mul_f32 v[234:235],v[234:235],v[62:63] op_sel:[0,1] op_sel_hi:[1,1]\nv_pk_mul_f32 v[236:237],v[236:237],v[62:63] op_sel:[0,1] op_sel_hi:[1,1]\nv_pk_mul_f32 v[238:239],v[238:239],v[62:63] op_sel:[0,1] op_sel_hi:[1,1]\nv_pk_mul_f32 v[240:241],v[240:241],v[62:63] op_sel:[0,1] op_sel_hi:[1,1]\n" \
"v_pk_mul_f32 v[242:243],v[242:243],v[62:63] op_sel:[0,1] op_sel_hi:[1,1]\nv_pk_mul_f32 v[244:245],v[244:245],v[62:63] op_sel:[0,1] op_sel_hi:[1,1]\nv_pk_mul_f32 v[246:247],v[246:247],v[62:63] op_sel:[0,1] op_sel_hi:[1,1]\nv_pk_mul_f32 v[248:249],v[248:249],v[62:63] op_sel:[0,1] op_sel_hi:[1,1]\nv_pk_mul_f32 v[250:251],v[250:251],v[62:63] op_sel:[0,1] op_sel_hi:[1,1]\nv_pk_mul_f32 v[252:253],v[252:253],v[62:63] op_sel:[0,1] op_sel_hi:[1,1]\nv_pk_mul_f32 v[254:255],v[254:255],v[62:63] op_sel:[0,1] op_sel_hi:[1,1]\nC v128,v128,v129\n" \
"C v129,v130,v131\nC v130,v132,v133\nC v131,v134,v135\nbuffer_store_dwordx4 v[128:131],v4,s[36:39],s77 offen offset:0 sc1\nC v136,v136,v137\nC v137,v138,v139\nC v138,v140,v141\nC v139,v142,v143\n" \
"buffer_store_dwordx4 v[136:139],v4,s[36:39],s77 offen offset:1024 sc1\nC v144,v144,v145\nC v145,v146,v147\nC v146,v148,v149\nC v147,v150,v151\nv_mfma_f32_32x32x16_bf16 v[128:143],v[24:27],v[24:27],0\nbuffer_store_dwordx4 v[144:147],v4,s[36:39],s77 offen offset:2048 sc1\nC v152,v152,v153\n" \
"C v153,v154,v155\nC v154,v156,v157\nC v155,v158,v159\nbuffer_store_dwordx4 v[152:155],v4,s[36:39],s77 offen offset:3072 sc1\ns_add_u32 s77,s77,262144\nC v160,v160,v161\nC v161,v162,v163\nC v162,v164,v165\n" \
"C v163,v166,v167\nv_mfma_f32_32x32x16_bf16 v[144:159],v[24:27],v[24:27],0\nbuffer_store_dwordx4 v[160:163],v4,s[36:39],s77 offen offset:0 sc1\nC v168,v168,v169\nC v169,v170,v171\nC v170,v172,v173\nC v171,v174,v175\nbuffer_store_dwordx4 v[168:171],v4,s[36:39],s77 offen offset:1024 sc1\n" \
"C v176,v176,v177\nC v177,v178,v179\nC v178,v180,v181\nC v179,v182,v183\nv_mfma_f32_32x32x16_bf16 v[160:175],v[24:27],v[24:27],0\nbuffer_store_dwordx4 v[176:179],v4,s[36:39],s77 offen offset:2048 sc1\nC v184,v184,v185\nC v185,v186,v187\n" \
"C v186,v188,v189\nC v187,v190,v191\nbuffer_store_dwordx4 v[184:187],v4,s[36:39],s77 offen offset:3072 sc1\ns_add_u32 s77,s77,262144\nC v192,v192,v193\nC v193,v194,v195\nC v194,v196,v197\nC v195,v198,v199\n" \
"v_mfma_f32_32x32x16_bf16 v[176:191],v[24:27],v[24:27],0\nbuffer_store_dwordx4 v[192:195],v4,s[36:39],s77 offen offset:0 sc1\nC v200,v200,v201\nC v201,v202,v203\nC v202,v204,v205\nC v203,v206,v207\nbuffer_store_dwordx4 v[200:203],v4,s[36:39],s77 offen offset:1024 sc1\nC v208,v208,v209\n" \
"C v209,v210,v211\nC v210,v212,v213\nC v211,v214,v215\nv_mfma_f32_32x32x16_bf16 v[192:207],v[24:27],v[24:27],0\nbuffer_store_dwordx4 v[208:211],v4,s[36:39],s77 offen offset:2048 sc1\nC v216,v216,v217\nC v217,v218,v219\nC v218,v220,v221\n" \
"C v219,v222,v223\nbuffer_store_dwordx4 v[216:219],v4,s[36:39],s77 offen offset:3072 sc1\ns_add_u32 s77,s77,262144\nC v224,v224,v225\nC v225,v226,v227\nC v226,v228,v229\nC v227,v230,v231\nv_mfma_f32_32x32x16_bf16 v[208:223],v[24:27],v[24:27],0\n" \
"buffer_store_dwordx4 v[224:227],v4,s[36:39],s77 offen offset:0 sc1\nC v232,v232,v233\nC v233,v234,v235\nC v234,v236,v237\nC v235,v238,v239\nbuffer_store_dwordx4 v[232:235],v4,s[36:39],s77 offen offset:1024 sc1\nC v240,v240,v241\nC v241,v242,v243\n" \
"C v242,v244,v245\nC v243,v246,v247\nv_mfma_f32_32x32x16_bf16 v[224:239],v[24:27],v[24:27],0\nbuffer_store_dwordx4 v[240:243],v4,s[36:39],s77 offen offset:2048 sc1\nC v248,v248,v249\nC v249,v250,v251\nC v250,v252,v253\nC v251,v254,v255\n" \
"buffer_store_dwordx4 v[248:251],v4,s[36:39],s77 offen offset:3072 sc1\ns_add_u32 s77,s77,262144\ns_nop 0\nv_mfma_f32_32x32x16_bf16 v[240:255],v[24:27],v[24:27],0\ns_add_u32 s56,s4,1\ns_branch LEV4_%=\nLEV3_%=:\ns_mov_b32 s56,s4\n" \
"s_cmp_lt_u32 s52,s4\ns_cbranch_scc1 LEV4_%=\ns_mov_b32 s56,50\ns_add_u32 s77,s4,1\ns_cmp_eq_u32 s52,s77\ns_cbranch_scc0 LEV4_%=\nv_lshrrev_b32 v62,5,v4\nv_mov_b32 v60,s10\n" \
"v_lshlrev_b32 v62,27,v62\nv_mov_b32 v61,s11\nv_lshl_add_u32 v62,v4,5,v62\ns_waitcnt vmcnt(16)\nbuffer_store_dwordx2 v[60:61],v62,s[36:39],s69 offen offset:0 sc1\nLEV4_%=:\ns_cmp_lt_u32 s52,8\ns_cbranch_scc0 LEV5_%=\n" \
"s_min_u32 s56,s56,8\nLEV5_%=:\ns_cmp_eq_u32 s52,8\ns_cbranch_scc0 LEV6_%=\ns_mov_b64 exec,0\ns_cmp_eq_u32 %2,0\ns_cbranch_scc1 LG16_%=\ns_cmp_eq_u32 %2,1\n" \
"s_cbranch_scc1 LG17_%=\ns_cmp_eq_u32 %2,2\ns_cbranch_scc1 LG18_%=\ns_cmp_eq_u32 %2,3\ns_cbranch_scc1 LG19_%=\nLHR2_%=:\ns_mov_b64 exec,-1\nLEV6_%=:\n" \
"s_cmp_eq_u32 s52,0\ns_cbranch_scc1 LEVR0_%=\ns_cmp_eq_u32 s52,8\ns_cbranch_scc1 LEVR8_%=\ns_cmp_eq_u32 s52,9\ns_cbranch_scc1 LEVR9_%=\ns_cmp_eq_u32 s52,10\ns_cbranch_scc1 LEVR10_%=\n" \
"s_cmp_eq_u32 s52,11\ns_cbranch_scc1 LEVR11_%=\ns_cmp_eq_u32 s52,12\ns_cbranch_scc1 LEVR12_%=\ns_cmp_eq_u32 s52,13\ns_cbranch_scc1 LEVR13_%=\ns_cmp_eq_u32 s52,14\ns_cbranch_scc1 LEVR14_%=\n" \
"s_cmp_eq_u32 s52,15\ns_cbranch_scc1 LEVR15_%=\ns_branch LEVR1_%=\nLcs1_%=:\ns_add_u32 s9,s5,2\ns_mov_b32 s8,3\ns_mov_b32 s64,0\ns_branch LSLOW1_%=\n" \
"Lcs3_%=:\ns_add_u32 s9,s5,1\ns_mov_b32 s8,2\ns_mov_b32 s64,0\ns_branch LSLOW0_%=\nLcs5_%=:\ns_add_u32 s9,s5,0\ns_mov_b32 s8,1\n" \
"s_mov_b32 s64,1\ns_branch LSLOW1_%=\nLcs7_%=:\ns_lshl_b32 s9,s52,1\ns_add_u32 s9,s9,0\ns_sub_u32 s8,59,s9\ns_cmp_ge_u32 s9,s70\ns_cselect_b32 s9,s8,s9\n" \
"s_mov_b32 s8,0\ns_mov_b32 s64,1\ns_branch LSLOW0_%=\nLcs9_%=:\ns_lshl_b32 s9,s52,1\ns_add_u32 s9,s9,1\ns_sub_u32 s8,59,s9\ns_cmp_ge_u32 s9,s70\n" \
"s_cselect_b32 s9,s8,s9\ns_mov_b32 s8,0\ns_mov_b32 s64,2\ns_branch LSLOW1_%=\nLcs11_%=:\ns_lshl_b32 s9,s52,1\ns_add_u32 s9,s9,0\ns_sub_u32 s8,59,s9\n" \
"s_cmp_ge_u32 s9,s70\ns_cselect_b32 s9,s8,s9\ns_mov_b32 s8,0\ns_mov_b32 s64,2\ns_branch LSLOW0_%=\nLcs13_%=:\ns_lshl_b32 s9,s52,1\ns_add_u32 s9,s9,1\n" \
"s_sub_u32 s8,59,s9\ns_cmp_ge_u32 s9,s70\ns_cselect_b32 s9,s8,s9\ns_mov_b32 s8,0\ns_mov_b32 s64,3\ns_branch LSLOW1_%=\nLcs39_%=:\ns_lshl_b32 s9,s52,1\n" \
"s_add_u32 s9,s9,0\ns_sub_u32 s8,59,s9\ns_cmp_ge_u32 s9,s70\ns_cselect_b32 s9,s8,s9\ns_mov_b32 s8,0\ns_mov_b32 s64,3\ns_branch LSLOW0_%=\nLcs41_%=:\n" \
"s_lshl_b32 s9,s52,1\ns_add_u32 s9,s9,1\ns_sub_u32 s8,59,s9\ns_cmp_ge_u32 s9,s70\ns_cselect_b32 s9,s8,s9\ns_mov_b32 s8,0\ns_mov_b32 s64,4\ns_branch LSLOW1_%=\n" \
"Lcs43_%=:\ns_lshl_b32 s9,s52,1\ns_add_u32 s9,s9,0\ns_sub_u32 s8,59,s9\ns_cmp_ge_u32 s9,s70\ns_cselect_b32 s9,s8,s9\ns_mov_b32 s8,0\ns_mov_b32 s64,4\n" \
"s_branch LSLOW0_%=\nLcs45_%=:\ns_lshl_b32 s9,s52,1\ns_add_u32 s9,s9,1\ns_sub_u32 s8,59,s9\ns_cmp_ge_u32 s9,s70\ns_cselect_b32 s9,s8,s9\ns_mov_b32 s8,0\n" \
"s_mov_b32 s64,5\ns_branch LSLOW1_%=\nLcs47_%=:\ns_lshl_b32 s9,s52,1\ns_add_u32 s9,s9,0\ns_sub_u32 s8,59,s9\ns_cmp_ge_u32 s9,s70\ns_cselect_b32 s9,s8,s9\n" \
"s_mov_b32 s8,0\ns_mov_b32 s64,5\ns_branch LSLOW0_%=\nLcs49_%=:\ns_lshl_b32 s9,s52,1\ns_add_u32 s9,s9,1\ns_sub_u32 s8,59,s9\ns_cmp_ge_u32 s9,s70\n" \
"s_cselect_b32 s9,s8,s9\ns_mov_b32 s8,0\ns_mov_b32 s64,6\ns_branch LSLOW1_%=\nLcs51_%=:\ns_lshl_b32 s9,s52,1\ns_add_u32 s9,s9,0\ns_sub_u32 s8,59,s9\n" \
"s_cmp_ge_u32 s9,s70\ns_cselect_b32 s9,s8,s9\ns_mov_b32 s8,0\ns_mov_b32 s64,6\ns_branch LSLOW0_%=\nLcs53_%=:\ns_lshl_b32 s9,s52,1\ns_add_u32 s9,s9,1\n" \
"s_sub_u32 s8,59,s9\ns_cmp_ge_u32 s9,s70\ns_cselect_b32 s9,s8,s9\ns_mov_b32 s8,0\ns_mov_b32 s64,7\ns_branch LSLOW1_%=\nLcs55_%=:\ns_lshl_b32 s9,s52,1\n" \
"s_add_u32 s9,s9,0\ns_sub_u32 s8,59,s9\ns_cmp_ge_u32 s9,s70\ns_cselect_b32 s9,s8,s9\ns_mov_b32 s8,0\ns_mov_b32 s64,7\ns_branch LSLOW0_%=\nLcs57_%=:\n" \
"s_lshl_b32 s9,s52,1\ns_add_u32 s9,s9,1\ns_sub_u32 s8,59,s9\ns_cmp_ge_u32 s9,s70\ns_cselect_b32 s9,s8,s9\ns_mov_b32 s8,0\ns_mov_b32 s64,8\ns_branch LSLOW1_%=\n" \
"Lcs59_%=:\ns_lshl_b32 s9,s52,1\ns_add_u32 s9,s9,0\ns_sub_u32 s8,59,s9\ns_cmp_ge_u32 s9,s70\ns_cselect_b32 s9,s8,s9\ns_mov_b32 s8,0\ns_mov_b32 s64,8\n" \
"s_branch LSLOW0_%=\nLcs61_%=:\ns_lshl_b32 s9,s52,1\ns_add_u32 s9,s9,1\ns_sub_u32 s8,59,s9\ns_cmp_ge_u32 s9,s70\ns_cselect_b32 s9,s8,s9\ns_mov_b32 s8,0\n" \
"s_mov_b32 s64,9\ns_branch LSLOW1_%=\nLcs63_%=:\ns_lshl_b32 s9,s52,1\ns_add_u32 s9,s9,0\ns_sub_u32 s8,59,s9\ns_cmp_ge_u32 s9,s70\ns_cselect_b32 s9,s8,s9\n" \
"s_mov_b32 s8,0\ns_mov_b32 s64,9\ns_branch LSLOW0_%=\nLSLOW0_%=:\ns_lshl_b32 s9,s9,14\ns_add_u32 s9,s9,s71\ns_nop 7\ns_waitcnt lgkmcnt(0)\n" \
"buffer_load_dwordx4 a[240:243],v4,s[36:39],s9 offen offset:0 sc1\nbuffer_load_dwordx4 a[244:247],v4,s[36:39],s9 offen offset:2048 sc1\ns_add_u32 s9,s9,4096\nbuffer_load_dwordx4 a[248:251],v4,s[36:39],s9 offen offset:0 sc1\nbuffer_load_dwordx4 a[252:255],v4,s[36:39],s9 offen offset:2048 sc1\ns_sub_u32 s9,s9,3072\ns_waitcnt vmcnt(0)\ns_cmp_gt_u32 s8,0\n" \
"s_cbranch_scc1 Lk65_%=\nM v[64:67],a[240:243],a[128:131],v[32:35]\nM v[64:67],a[244:247],a[132:135],v[64:67]\nM v[64:67],a[248:251],a[136:139],v[64:67]\nM v[64:67],a[252:255],a[140:143],v[64:67]\nLk65_%=:\ns_cmp_gt_u32 s8,1\ns_cbranch_scc1 Lk66_%=\n" \
"M v[72:75],a[240:243],a[144:147],v[36:39]\nM v[72:75],a[244:247],a[148:151],v[72:75]\nM v[72:75],a[248:251],a[152:155],v[72:75]\nM v[72:75],a[252:255],a[156:159],v[72:75]\nLk66_%=:\ns_cmp_gt_u32 s8,2\ns_cbranch_scc1 Lk67_%=\nM v[80:83],a[240:243],a[160:163],v[40:43]\n" \
"M v[80:83],a[244:247],a[164:167],v[80:83]\nM v[80:83],a[248:251],a[168:171],v[80:83]\nM v[80:83],a[252:255],a[172:175],v[80:83]\nLk67_%=:\ns_cmp_gt_u32 s8,3\ns_cbranch_scc1 Lk68_%=\nM v[88:91],a[240:243],a[176:179],v[44:47]\nM v[88:91],a[244:247],a[180:183],v[88:91]\n" \
"M v[88:91],a[248:251],a[184:187],v[88:91]\nM v[88:91],a[252:255],a[188:191],v[88:91]\nLk68_%=:\ns_nop 7\nbuffer_load_dwordx4 a[240:243],v4,s[36:39],s9 offen offset:0 sc1\nbuffer_load_dwordx4 a[244:247],v4,s[36:39],s9 offen offset:2048 sc1\ns_add_u32 s9,s9,4096\nbuffer_load_dwordx4 a[248:251],v4,s[36:39],s9 offen offset:0 sc1\n" \
"buffer_load_dwordx4 a[252:255],v4,s[36:39],s9 offen offset:2048 sc1\ns_sub_u32 s9,s9,3072\ns_waitcnt vmcnt(0)\ns_cmp_gt_u32 s8,0\ns_cbranch_scc1 Lk69_%=\nM v[68:71],a[240:243],a[128:131],v[32:35]\nM v[68:71],a[244:247],a[132:135],v[68:71]\nM v[68:71],a[248:251],a[136:139],v[68:71]\n" \
"M v[68:71],a[252:255],a[140:143],v[68:71]\nLk69_%=:\ns_cmp_gt_u32 s8,1\ns_cbranch_scc1 Lk70_%=\nM v[76:79],a[240:243],a[144:147],v[36:39]\nM v[76:79],a[244:247],a[148:151],v[76:79]\nM v[76:79],a[248:251],a[152:155],v[76:79]\nM v[76:79],a[252:255],a[156:159],v[76:79]\n" \
"Lk70_%=:\ns_cmp_gt_u32 s8,2\ns_cbranch_scc1 Lk71_%=\nM v[84:87],a[240:243],a[160:163],v[40:43]\nM v[84:87],a[244:247],a[164:167],v[84:87]\nM v[84:87],a[248:251],a[168:171],v[84:87]\nM v[84:87],a[252:255],a[172:175],v[84:87]\nLk71_%=:\n" \
"s_cmp_gt_u32 s8,3\ns_cbranch_scc1 Lk72_%=\nM v[92:95],a[240:243],a[176:179],v[44:47]\nM v[92:95],a[244:247],a[180:183],v[92:95]\nM v[92:95],a[248:251],a[184:187],v[92:95]\nM v[92:95],a[252:255],a[188:191],v[92:95]\nLk72_%=:\ns_nop 7\n" \
"s_cmp_gt_u32 s8,0\ns_cbranch_scc0 Lk74_%=\nv_mov_b32 v16,v24\ns_cmp_eq_u32 s8,1\ns_cbranch_scc0 Lk73_%=\nM v[16:19],a[224:227],v[64:67],v[16:19]\ns_nop 7\ns_branch Lk73_%=\n" \
"Lk74_%=:\nv_max3_f32 v62,v64,v65,v66\nv_max3_f32 v63,v67,v68,v69\nv_max3_f32 v62,v62,v70,v71\nv_max3_f32 v62,v62,v63,0\nv_mov_b32 v63,v62\ns_nop 1\nv_permlane32_swap_b32 v62,v63\n" \
"v_max_f32 v62,v62,v63\nv_mov_b32 v63,v62\ns_nop 1\nv_permlane16_swap_b32 v62,v63\nv_max_f32 v62,v62,v63\nv_exp_f32 v63,-v62\nv_sub_f32 v64,v64,v62\nv_sub_f32 v65,v65,v62\n" \
"v_sub_f32 v66,v66,v62\nv_sub_f32 v67,v67,v62\nv_sub_f32 v68,v68,v62\nv_sub_f32 v69,v69,v62\nv_sub_f32 v70,v70,v62\nv_sub_f32 v71,v71,v62\nv_exp_f32 v64,v64\nv_exp_f32 v65,v65\n" \
"v_exp_f32 v66,v66\nv_exp_f32 v67,v67\nv_exp_f32 v68,v68\nv_exp_f32 v69,v69\nv_exp_f32 v70,v70\nv_exp_f32 v71,v71\nv_mul_f32 v16,v24,v63\nC v64,v64,v65\n" \
"C v65,v66,v67\nC v66,v68,v69\nC v67,v70,v71\nv_sub_f32 v32,v32,v62\nv_sub_f32 v33,v33,v62\nv_sub_f32 v34,v34,v62\nv_sub_f32 v35,v35,v62\nM v[16:19],a[224:227],v[64:67],v[16:19]\n" \
"v_mul_f32 v128,v128,v63\nv_mul_f32 v129,v129,v63\nv_mul_f32 v130,v130,v63\nv_mul_f32 v131,v131,v63\nv_mul_f32 v132,v132,v63\nv_mul_f32 v133,v133,v63\nv_mul_f32 v134,v134,v63\nv_mul_f32 v135,v135,v63\n" \
"v_mul_f32 v136,v136,v63\nv_mul_f32 v137,v137,v63\nv_mul_f32 v138,v138,v63\nv_mul_f32 v139,v139,v63\nv_mul_f32 v140,v140,v63\nv_mul_f32 v141,v141,v63\nv_mul_f32 v142,v142,v63\nv_mul_f32 v143,v143,v63\n" \
"v_mul_f32 v144,v144,v63\nv_mul_f32 v145,v145,v63\nv_mul_f32 v146,v146,v63\nv_mul_f32 v147,v147,v63\nv_mul_f32 v148,v148,v63\nv_mul_f32 v149,v149,v63\nv_mul_f32 v150,v150,v63\nv_mul_f32 v151,v151,v63\n" \
"v_mul_f32 v152,v152,v63\nv_mul_f32 v153,v153,v63\nv_mul_f32 v154,v154,v63\nv_mul_f32 v155,v155,v63\nv_mul_f32 v156,v156,v63\nv_mul_f32 v157,v157,v63\nv_mul_f32 v158,v158,v63\nv_mul_f32 v159,v159,v63\n" \
"s_bitcmp1_b32 s64,6\ns_cbranch_scc1 Lk73_%=\nv_sub_f32 v96,v96,v62\nv_sub_f32 v97,v97,v62\nv_sub_f32 v98,v98,v62\nv_sub_f32 v99,v99,v62\nv_sub_f32 v100,v100,v62\nv_sub_f32 v101,v101,v62\n" \
"v_sub_f32 v102,v102,v62\nv_sub_f32 v103,v103,v62\nLk73_%=:\ns_cmp_gt_u32 s8,1\ns_cbranch_scc0 Lk76_%=\nv_mov_b32 v17,v25\ns_cmp_eq_u32 s8,2\ns_cbranch_scc0 Lk75_%=\n" \
"M v[16:19],a[228:231],v[72:75],v[16:19]\ns_nop 7\ns_branch Lk75_%=\nLk76_%=:\nv_max3_f32 v62,v72,v73,v74\nv_max3_f32 v63,v75,v76,v77\nv_max3_f32 v62,v62,v78,v79\nv_max3_f32 v62,v62,v63,0\n" \
"v_mov_b32 v63,v62\ns_nop 1\nv_permlane32_swap_b32 v62,v63\nv_max_f32 v62,v62,v63\nv_mov_b32 v63,v62\ns_nop 1\nv_permlane16_swap_b32 v62,v63\nv_max_f32 v62,v62,v63\n" \
"v_exp_f32 v63,-v62\nv_sub_f32 v72,v72,v62\nv_sub_f32 v73,v73,v62\nv_sub_f32 v74,v74,v62\nv_sub_f32 v75,v75,v62\nv_sub_f32 v76,v76,v62\nv_sub_f32 v77,v77,v62\nv_sub_f32 v78,v78,v62\n" \
"v_sub_f32 v79,v79,v62\nv_exp_f32 v72,v72\nv_exp_f32 v73,v73\nv_exp_f32 v74,v74\nv_exp_f32 v75,v75\nv_exp_f32 v76,v76\nv_exp_f32 v77,v77\nv_exp_f32 v78,v78\n" \
"v_exp_f32 v79,v79\nv_mul_f32 v17,v25,v63\nC v72,v72,v73\nC v73,v74,v75\nC v74,v76,v77\nC v75,v78,v79\nv_sub_f32 v36,v36,v62\nv_sub_f32 v37,v37,v62\n" \
"v_sub_f32 v38,v38,v62\nv_sub_f32 v39,v39,v62\nM v[16:19],a[228:231],v[72:75],v[16:19]\nv_mul_f32 v160,v160,v63\nv_mul_f32 v161,v161,v63\nv_mul_f32 v162,v162,v63\nv_mul_f32 v163,v163,v63\nv_mul_f32 v164,v164,v63\n" \
"v_mul_f32 v165,v165,v63\nv_mul_f32 v166,v166,v63\nv_mul_f32 v167,v167,v63\nv_mul_f32 v168,v168,v63\nv_mul_f32 v169,v169,v63\nv_mul_f32 v170,v170,v63\nv_mul_f32 v171,v171,v63\nv_mul_f32 v172,v172,v63\n" \
"v_mul_f32 v173,v173,v63\nv_mul_f32 v174,v174,v63\nv_mul_f32 v175,v175,v63\nv_mul_f32 v176,v176,v63\nv_mul_f32 v177,v177,v63\nv_mul_f32 v178,v178,v63\nv_mul_f32 v179,v179,v63\nv_mul_f32 v180,v180,v63\n" \
"v_mul_f32 v181,v181,v63\nv_mul_f32 v182,v182,v63\nv_mul_f32 v183,v183,v63\nv_mul_f32 v184,v184,v63\nv_mul_f32 v185,v185,v63\nv_mul_f32 v186,v186,v63\nv_mul_f32 v187,v187,v63\nv_mul_f32 v188,v188,v63\n" \
"v_mul_f32 v189,v189,v63\nv_mul_f32 v190,v190,v63\nv_mul_f32 v191,v191,v63\ns_bitcmp1_b32 s64,6\ns_cbranch_scc1 Lk75_%=\nv_sub_f32 v104,v104,v62\nv_sub_f32 v105,v105,v62\nv_sub_f32 v106,v106,v62\n" \
"v_sub_f32 v107,v107,v62\nv_sub_f32 v108,v108,v62\nv_sub_f32 v109,v109,v62\nv_sub_f32 v110,v110,v62\nv_sub_f32 v111,v111,v62\nLk75_%=:\ns_cmp_gt_u32 s8,2\ns_cbranch_scc0 Lk78_%=\n" \
"v_mov_b32 v18,v26\ns_cmp_eq_u32 s8,3\ns_cbranch_scc0 Lk77_%=\nM v[16:19],a[232:235],v[80:83],v[16:19]\ns_nop 7\ns_branch Lk77_%=\nLk78_%=:\nv_max3_f32 v62,v80,v81,v82\n" \
"v_max3_f32 v63,v83,v84,v85\nv_max3_f32 v62,v62,v86,v87\nv_max3_f32 v62,v62,v63,0\nv_mov_b32 v63,v62\ns_nop 1\nv_permlane32_swap_b32 v62,v63\nv_max_f32 v62,v62,v63\nv_mov_b32 v63,v62\n" \
"s_nop 1\nv_permlane16_swap_b32 v62,v63\nv_max_f32 v62,v62,v63\nv_exp_f32 v63,-v62\nv_sub_f32 v80,v80,v62\nv_sub_f32 v81,v81,v62\nv_sub_f32 v82,v82,v62\nv_sub_f32 v83,v83,v62\n" \
"v_sub_f32 v84,v84,v62\nv_sub_f32 v85,v85,v62\nv_sub_f32 v86,v86,v62\nv_sub_f32 v87,v87,v62\nv_exp_f32 v80,v80\nv_exp_f32 v81,v81\nv_exp_f32 v82,v82\nv_exp_f32 v83,v83\n" \
"v_exp_f32 v84,v84\nv_exp_f32 v85,v85\nv_exp_f32 v86,v86\nv_exp_f32 v87,v87\nv_mul_f32 v18,v26,v63\nC v80,v80,v81\nC v81,v82,v83\nC v82,v84,v85\n" \
"C v83,v86,v87\nv_sub_f32 v40,v40,v62\nv_sub_f32 v41,v41,v62\nv_sub_f32 v42,v42,v62\nv_sub_f32 v43,v43,v62\nM v[16:19],a[232:235],v[80:83],v[16:19]\nv_mul_f32 v192,v192,v63\nv_mul_f32 v193,v193,v63\n" \
"v_mul_f32 v194,v194,v63\nv_mul_f32 v195,v195,v63\nv_mul_f32 v196,v196,v63\nv_mul_f32 v197,v197,v63\nv_mul_f32 v198,v198,v63\nv_mul_f32 v199,v199,v63\nv_mul_f32 v200,v200,v63\nv_mul_f32 v201,v201,v63\n" \
"v_mul_f32 v202,v202,v63\nv_mul_f32 v203,v203,v63\nv_mul_f32 v204,v204,v63\nv_mul_f32 v205,v205,v63\nv_mul_f32 v206,v206,v63\nv_mul_f32 v207,v207,v63\nv_mul_f32 v208,v208,v63\nv_mul_f32 v209,v209,v63\n" \
"v_mul_f32 v210,v210,v63\nv_mul_f32 v211,v211,v63\nv_mul_f32 v212,v212,v63\nv_mul_f32 v213,v213,v63\nv_mul_f32 v214,v214,v63\nv_mul_f32 v215,v215,v63\nv_mul_f32 v216,v216,v63\nv_mul_f32 v217,v217,v63\n" \
"v_mul_f32 v218,v218,v63\nv_mul_f32 v219,v219,v63\nv_mul_f32 v220,v220,v63\nv_mul_f32 v221,v221,v63\nv_mul_f32 v222,v222,v63\nv_mul_f32 v223,v223,v63\ns_bitcmp1_b32 s64,6\ns_cbranch_scc1 Lk77_%=\n" \
"v_sub_f32 v112,v112,v62\nv_sub_f32 v113,v113,v62\nv_sub_f32 v114,v114,v62\nv_sub_f32 v115,v115,v62\nv_sub_f32 v116,v116,v62\nv_sub_f32 v117,v117,v62\nv_sub_f32 v118,v118,v62\nv_sub_f32 v119,v119,v62\n" \
"Lk77_%=:\ns_cmp_gt_u32 s8,3\ns_cbranch_scc0 Lk80_%=\nv_mov_b32 v19,v27\ns_cmp_eq_u32 s8,4\ns_cbranch_scc0 Lk79_%=\nM v[16:19],a[236:239],v[88:91],v[16:19]\ns_nop 7\n" \
"s_branch Lk79_%=\nLk80_%=:\nv_max3_f32 v62,v88,v89,v90\nv_max3_f32 v63,v91,v92,v93\nv_max3_f32 v62,v62,v94,v95\nv_max3_f32 v62,v62,v63,0\nv_mov_b32 v63,v62\ns_nop 1\n" \
"v_permlane32_swap_b32 v62,v63\nv_max_f32 v62,v62,v63\nv_mov_b32 v63,v62\ns_nop 1\nv_permlane16_swap_b32 v62,v63\nv_max_f32 v62,v62,v63\nv_exp_f32 v63,-v62\nv_sub_f32 v88,v88,v62\n" \
"v_sub_f32 v89,v89,v62\nv_sub_f32 v90,v90,v62\nv_sub_f32 v91,v91,v62\nv_sub_f32 v92,v92,v62\nv_sub_f32 v93,v93,v62\nv_sub_f32 v94,v94,v62\nv_sub_f32 v95,v95,v62\nv_exp_f32 v88,v88\n" \
"v_exp_f32 v89,v89\nv_exp_f32 v90,v90\nv_exp_f32 v91,v91\nv_exp_f32 v92,v92\nv_exp_f32 v93,v93\nv_exp_f32 v94,v94\nv_exp_f32 v95,v95\nv_mul_f32 v19,v27,v63\n" \
"C v88,v88,v89\nC v89,v90,v91\nC v90,v92,v93\nC v91,v94,v95\nv_sub_f32 v44,v44,v62\nv_sub_f32 v45,v45,v62\nv_sub_f32 v46,v46,v62\nv_sub_f32 v47,v47,v62\n" \
"M v[16:19],a[236:239],v[88:91],v[16:19]\nv_mul_f32 v224,v224,v63\nv_mul_f32 v225,v225,v63\nv_mul_f32 v226,v226,v63\nv_mul_f32 v227,v227,v63\nv_mul_f32 v228,v228,v63\nv_mul_f32 v229,v229,v63\nv_mul_f32 v230,v230,v63\n" \
"v_mul_f32 v231,v231,v63\nv_mul_f32 v232,v232,v63\nv_mul_f32 v233,v233,v63\nv_mul_f32 v234,v234,v63\nv_mul_f32 v235,v235,v63\nv_mul_f32 v236,v236,v63\nv_mul_f32 v237,v237,v63\nv_mul_f32 v238,v238,v63\n" \
"v_mul_f32 v239,v239,v63\nv_mul_f32 v240,v240,v63\nv_mul_f32 v241,v241,v63\nv_mul_f32 v242,v242,v63\nv_mul_f32 v243,v243,v63\nv_mul_f32 v244,v244,v63\nv_mul_f32 v245,v245,v63\nv_mul_f32 v246,v246,v63\n" \
"v_mul_f32 v247,v247,v63\nv_mul_f32 v248,v248,v63\nv_mul_f32 v249,v249,v63\nv_mul_f32 v250,v250,v63\nv_mul_f32 v251,v251,v63\nv_mul_f32 v252,v252,v63\nv_mul_f32 v253,v253,v63\nv_mul_f32 v254,v254,v63\n" \
"v_mul_f32 v255,v255,v63\ns_bitcmp1_b32 s64,6\ns_cbranch_scc1 Lk79_%=\nv_sub_f32 v120,v120,v62\nv_sub_f32 v121,v121,v62\nv_sub_f32 v122,v122,v62\nv_sub_f32 v123,v123,v62\nv_sub_f32 v124,v124,v62\n" \
"v_sub_f32 v125,v125,v62\nv_sub_f32 v126,v126,v62\nv_sub_f32 v127,v127,v62\nLk79_%=:\ns_nop 7\nLSR0_%=:\ns_cmp_eq_u32 s64,0\ns_cbranch_scc1 Lcr4_%=\n" \
"s_cmp_eq_u32 s64,1\ns_cbranch_scc1 Lcr8_%=\ns_cmp_eq_u32 s64,2\ns_cbranch_scc1 Lcr12_%=\ns_cmp_eq_u32 s64,3\ns_cbranch_scc1 Lcr40_%=\ns_cmp_eq_u32 s64,4\ns_cbranch_scc1 Lcr44_%=\n" \
"s_cmp_eq_u32 s64,5\ns_cbranch_scc1 Lcr48_%=\ns_cmp_eq_u32 s64,6\ns_cbranch_scc1 Lcr52_%=\ns_cmp_eq_u32 s64,7\ns_cbranch_scc1 Lcr56_%=\ns_cmp_eq_u32 s64,8\ns_cbranch_scc1 Lcr60_%=\n" \
"s_branch Lcr64_%=\nLSLOW1_%=:\ns_cmp_eq_u32 s75,1\ns_cbranch_scc1 LSR1_%=\ns_lshl_b32 s9,s9,14\ns_add_u32 s9,s9,s71\ns_nop 7\ns_waitcnt lgkmcnt(0)\n" \
"buffer_load_dwordx4 a[240:243],v4,s[36:39],s9 offen offset:0 sc1\nbuffer_load_dwordx4 a[244:247],v4,s[36:39],s9 offen offset:2048 sc1\ns_add_u32 s9,s9,4096\nbuffer_load_dwordx4 a[248:251],v4,s[36:39],s9 offen offset:0 sc1\nbuffer_load_dwordx4 a[252:255],v4,s[36:39],s9 offen offset:2048 sc1\ns_sub_u32 s9,s9,3072\ns_waitcnt vmcnt(0)\ns_cmp_gt_u32 s8,0\n" \
"s_cbranch_scc1 Lk81_%=\nM v[96:99],a[240:243],a[128:131],v[32:35]\nM v[96:99],a[244:247],a[132:135],v[96:99]\nM v[96:99],a[248:251],a[136:139],v[96:99]\nM v[96:99],a[252:255],a[140:143],v[96:99]\nLk81_%=:\ns_cmp_gt_u32 s8,1\ns_cbranch_scc1 Lk82_%=\n" \
"M v[104:107],a[240:243],a[144:147],v[36:39]\nM v[104:107],a[244:247],a[148:151],v[104:107]\nM v[104:107],a[248:251],a[152:155],v[104:107]\nM v[104:107],a[252:255],a[156:159],v[104:107]\nLk82_%=:\ns_cmp_gt_u32 s8,2\ns_cbranch_scc1 Lk83_%=\nM v[112:115],a[240:243],a[160:163],v[40:43]\n" \
"M v[112:115],a[244:247],a[164:167],v[112:115]\nM v[112:115],a[248:251],a[168:171],v[112:115]\nM v[112:115],a[252:255],a[172:175],v[112:115]\nLk83_%=:\ns_cmp_gt_u32 s8,3\ns_cbranch_scc1 Lk84_%=\nM v[120:123],a[240:243],a[176:179],v[44:47]\nM v[120:123],a[244:247],a[180:183],v[120:123]\n" \
"M v[120:123],a[248:251],a[184:187],v[120:123]\nM v[120:123],a[252:255],a[188:191],v[120:123]\nLk84_%=:\ns_nop 7\nbuffer_load_dwordx4 a[240:243],v4,s[36:39],s9 offen offset:0 sc1\nbuffer_load_dwordx4 a[244:247],v4,s[36:39],s9 offen offset:2048 sc1\ns_add_u32 s9,s9,4096\nbuffer_load_dwordx4 a[248:251],v4,s[36:39],s9 offen offset:0 sc1\n" \
"buffer_load_dwordx4 a[252:255],v4,s[36:39],s9 offen offset:2048 sc1\ns_sub_u32 s9,s9,3072\ns_waitcnt vmcnt(0)\ns_cmp_gt_u32 s8,0\ns_cbranch_scc1 Lk85_%=\nM v[100:103],a[240:243],a[128:131],v[32:35]\nM v[100:103],a[244:247],a[132:135],v[100:103]\nM v[100:103],a[248:251],a[136:139],v[100:103]\n" \
"M v[100:103],a[252:255],a[140:143],v[100:103]\nLk85_%=:\ns_cmp_gt_u32 s8,1\ns_cbranch_scc1 Lk86_%=\nM v[108:111],a[240:243],a[144:147],v[36:39]\nM v[108:111],a[244:247],a[148:151],v[108:111]\nM v[108:111],a[248:251],a[152:155],v[108:111]\nM v[108:111],a[252:255],a[156:159],v[108:111]\n" \
"Lk86_%=:\ns_cmp_gt_u32 s8,2\ns_cbranch_scc1 Lk87_%=\nM v[116:119],a[240:243],a[160:163],v[40:43]\nM v[116:119],a[244:247],a[164:167],v[116:119]\nM v[116:119],a[248:251],a[168:171],v[116:119]\nM v[116:119],a[252:255],a[172:175],v[116:119]\nLk87_%=:\n" \
"s_cmp_gt_u32 s8,3\ns_cbranch_scc1 Lk88_%=\nM v[124:127],a[240:243],a[176:179],v[44:47]\nM v[124:127],a[244:247],a[180:183],v[124:127]\nM v[124:127],a[248:251],a[184:187],v[124:127]\nM v[124:127],a[252:255],a[188:191],v[124:127]\nLk88_%=:\ns_nop 7\n" \
"s_cmp_gt_u32 s8,0\ns_cbranch_scc0 Lk90_%=\nv_mov_b32 v24,v16\ns_cmp_eq_u32 s8,1\ns_cbranch_scc0 Lk89_%=\nM v[24:27],a[224:227],v[96:99],v[24:27]\ns_nop 7\ns_branch Lk89_%=\n" \
"Lk90_%=:\nv_max3_f32 v62,v96,v97,v98\nv_max3_f32 v63,v99,v100,v101\nv_max3_f32 v62,v62,v102,v103\nv_max3_f32 v62,v62,v63,0\nv_mov_b32 v63,v62\ns_nop 1\nv_permlane32_swap_b32 v62,v63\n" \
"v_max_f32 v62,v62,v63\nv_mov_b32 v63,v62\ns_nop 1\nv_permlane16_swap_b32 v62,v63\nv_max_f32 v62,v62,v63\nv_exp_f32 v63,-v62\nv_sub_f32 v96,v96,v62\nv_sub_f32 v97,v97,v62\n" \
"v_sub_f32 v98,v98,v62\nv_sub_f32 v99,v99,v62\nv_sub_f32 v100,v100,v62\nv_sub_f32 v101,v101,v62\nv_sub_f32 v102,v102,v62\nv_sub_f32 v103,v103,v62\nv_exp_f32 v96,v96\nv_exp_f32 v97,v97\n" \
"v_exp_f32 v98,v98\nv_exp_f32 v99,v99\nv_exp_f32 v100,v100\nv_exp_f32 v101,v101\nv_exp_f32 v102,v102\nv_exp_f32 v103,v103\nv_mul_f32 v24,v16,v63\nC v96,v96,v97\n" \
"C v97,v98,v99\nC v98,v100,v101\nC v99,v102,v103\nv_sub_f32 v32,v32,v62\nv_sub_f32 v33,v33,v62\nv_sub_f32 v34,v34,v62\nv_sub_f32 v35,v35,v62\nM v[24:27],a[224:227],v[96:99],v[24:27]\n" \
"v_mul_f32 v128,v128,v63\nv_mul_f32 v129,v129,v63\nv_mul_f32 v130,v130,v63\nv_mul_f32 v131,v131,v63\nv_mul_f32 v132,v132,v63\nv_mul_f32 v133,v133,v63\nv_mul_f32 v134,v134,v63\nv_mul_f32 v135,v135,v63\n" \
"v_mul_f32 v136,v136,v63\nv_mul_f32 v137,v137,v63\nv_mul_f32 v138,v138,v63\nv_mul_f32 v139,v139,v63\nv_mul_f32 v140,v140,v63\nv_mul_f32 v141,v141,v63\nv_mul_f32 v142,v142,v63\nv_mul_f32 v143,v143,v63\n" \
"v_mul_f32 v144,v144,v63\nv_mul_f32 v145,v145,v63\nv_mul_f32 v146,v146,v63\nv_mul_f32 v147,v147,v63\nv_mul_f32 v148,v148,v63\nv_mul_f32 v149,v149,v63\nv_mul_f32 v150,v150,v63\nv_mul_f32 v151,v151,v63\n" \
"v_mul_f32 v152,v152,v63\nv_mul_f32 v153,v153,v63\nv_mul_f32 v154,v154,v63\nv_mul_f32 v155,v155,v63\nv_mul_f32 v156,v156,v63\nv_mul_f32 v157,v157,v63\nv_mul_f32 v158,v158,v63\nv_mul_f32 v159,v159,v63\n" \
"s_bitcmp1_b32 s64,6\ns_cbranch_scc1 Lk89_%=\nv_sub_f32 v64,v64,v62\nv_sub_f32 v65,v65,v62\nv_sub_f32 v66,v66,v62\nv_sub_f32 v67,v67,v62\nv_sub_f32 v68,v68,v62\nv_sub_f32 v69,v69,v62\n" \
"v_sub_f32 v70,v70,v62\nv_sub_f32 v71,v71,v62\nLk89_%=:\ns_cmp_gt_u32 s8,1\ns_cbranch_scc0 Lk92_%=\nv_mov_b32 v25,v17\ns_cmp_eq_u32 s8,2\ns_cbranch_scc0 Lk91_%=\n" \
"M v[24:27],a[228:231],v[104:107],v[24:27]\ns_nop 7\ns_branch Lk91_%=\nLk92_%=:\nv_max3_f32 v62,v104,v105,v106\nv_max3_f32 v63,v107,v108,v109\nv_max3_f32 v62,v62,v110,v111\nv_max3_f32 v62,v62,v63,0\n" \
"v_mov_b32 v63,v62\ns_nop 1\nv_permlane32_swap_b32 v62,v63\nv_max_f32 v62,v62,v63\nv_mov_b32 v63,v62\ns_nop 1\nv_permlane16_swap_b32 v62,v63\nv_max_f32 v62,v62,v63\n" \
"v_exp_f32 v63,-v62\nv_sub_f32 v104,v104,v62\nv_sub_f32 v105,v105,v62\nv_sub_f32 v106,v106,v62\nv_sub_f32 v107,v107,v62\nv_sub_f32 v108,v108,v62\nv_sub_f32 v109,v109,v62\nv_sub_f32 v110,v110,v62\n" \
"v_sub_f32 v111,v111,v62\nv_exp_f32 v104,v104\nv_exp_f32 v105,v105\nv_exp_f32 v106,v106\nv_exp_f32 v107,v107\nv_exp_f32 v108,v108\nv_exp_f32 v109,v109\nv_exp_f32 v110,v110\n" \
"v_exp_f32 v111,v111\nv_mul_f32 v25,v17,v63\nC v104,v104,v105\nC v105,v106,v107\nC v106,v108,v109\nC v107,v110,v111\nv_sub_f32 v36,v36,v62\nv_sub_f32 v37,v37,v62\n" \
"v_sub_f32 v38,v38,v62\nv_sub_f32 v39,v39,v62\nM v[24:27],a[228:231],v[104:107],v[24:27]\nv_mul_f32 v160,v160,v63\nv_mul_f32 v161,v161,v63\nv_mul_f32 v162,v162,v63\nv_mul_f32 v163,v163,v63\nv_mul_f32 v164,v164,v63\n" \
"v_mul_f32 v165,v165,v63\nv_mul_f32 v166,v166,v63\nv_mul_f32 v167,v167,v63\nv_mul_f32 v168,v168,v63\nv_mul_f32 v169,v169,v63\nv_mul_f32 v170,v170,v63\nv_mul_f32 v171,v171,v63\nv_mul_f32 v172,v172,v63\n" \
"v_mul_f32 v173,v173,v63\nv_mul_f32 v174,v174,v63\nv_mul_f32 v175,v175,v63\nv_mul_f32 v176,v176,v63\nv_mul_f32 v177,v177,v63\nv_mul_f32 v178,v178,v63\nv_mul_f32 v179,v179,v63\nv_mul_f32 v180,v180,v63\n" \
"v_mul_f32 v181,v181,v63\nv_mul_f32 v182,v182,v63\nv_mul_f32 v183,v183,v63\nv_mul_f32 v184,v184,v63\nv_mul_f32 v185,v185,v63\nv_mul_f32 v186,v186,v63\nv_mul_f32 v187,v187,v63\nv_mul_f32 v188,v188,v63\n" \
"v_mul_f32 v189,v189,v63\nv_mul_f32 v190,v190,v63\nv_mul_f32 v191,v191,v63\ns_bitcmp1_b32 s64,6\ns_cbranch_scc1 Lk91_%=\nv_sub_f32 v72,v72,v62\nv_sub_f32 v73,v73,v62\nv_sub_f32 v74,v74,v62\n" \
"v_sub_f32 v75,v75,v62\nv_sub_f32 v76,v76,v62\nv_sub_f32 v77,v77,v62\nv_sub_f32 v78,v78,v62\nv_sub_f32 v79,v79,v62\nLk91_%=:\ns_cmp_gt_u32 s8,2\ns_cbranch_scc0 Lk94_%=\n" \
"v_mov_b32 v26,v18\ns_cmp_eq_u32 s8,3\ns_cbranch_scc0 Lk93_%=\nM v[24:27],a[232:235],v[112:115],v[24:27]\ns_nop 7\ns_branch Lk93_%=\nLk94_%=:\nv_max3_f32 v62,v112,v113,v114\n" \
"v_max3_f32 v63,v115,v116,v117\nv_max3_f32 v62,v62,v118,v119\nv_max3_f32 v62,v62,v63,0\nv_mov_b32 v63,v62\ns_nop 1\nv_permlane32_swap_b32 v62,v63\nv_max_f32 v62,v62,v63\nv_mov_b32 v63,v62\n" \
"s_nop 1\nv_permlane16_swap_b32 v62,v63\nv_max_f32 v62,v62,v63\nv_exp_f32 v63,-v62\nv_sub_f32 v112,v112,v62\nv_sub_f32 v113,v113,v62\nv_sub_f32 v114,v114,v62\nv_sub_f32 v115,v115,v62\n" \
"v_sub_f32 v116,v116,v62\nv_sub_f32 v117,v117,v62\nv_sub_f32 v118,v118,v62\nv_sub_f32 v119,v119,v62\nv_exp_f32 v112,v112\nv_exp_f32 v113,v113\nv_exp_f32 v114,v114\nv_exp_f32 v115,v115\n" \
"v_exp_f32 v116,v116\nv_exp_f32 v117,v117\nv_exp_f32 v118,v118\nv_exp_f32 v119,v119\nv_mul_f32 v26,v18,v63\nC v112,v112,v113\nC v113,v114,v115\nC v114,v116,v117\n" \
"C v115,v118,v119\nv_sub_f32 v40,v40,v62\nv_sub_f32 v41,v41,v62\nv_sub_f32 v42,v42,v62\nv_sub_f32 v43,v43,v62\nM v[24:27],a[232:235],v[112:115],v[24:27]\nv_mul_f32 v192,v192,v63\nv_mul_f32 v193,v193,v63\n" \
"v_mul_f32 v194,v194,v63\nv_mul_f32 v195,v195,v63\nv_mul_f32 v196,v196,v63\nv_mul_f32 v197,v197,v63\nv_mul_f32 v198,v198,v63\nv_mul_f32 v199,v199,v63\nv_mul_f32 v200,v200,v63\nv_mul_f32 v201,v201,v63\n" \
"v_mul_f32 v202,v202,v63\nv_mul_f32 v203,v203,v63\nv_mul_f32 v204,v204,v63\nv_mul_f32 v205,v205,v63\nv_mul_f32 v206,v206,v63\nv_mul_f32 v207,v207,v63\nv_mul_f32 v208,v208,v63\nv_mul_f32 v209,v209,v63\n" \
"v_mul_f32 v210,v210,v63\nv_mul_f32 v211,v211,v63\nv_mul_f32 v212,v212,v63\nv_mul_f32 v213,v213,v63\nv_mul_f32 v214,v214,v63\nv_mul_f32 v215,v215,v63\nv_mul_f32 v216,v216,v63\nv_mul_f32 v217,v217,v63\n" \
"v_mul_f32 v218,v218,v63\nv_mul_f32 v219,v219,v63\nv_mul_f32 v220,v220,v63\nv_mul_f32 v221,v221,v63\nv_mul_f32 v222,v222,v63\nv_mul_f32 v223,v223,v63\ns_bitcmp1_b32 s64,6\ns_cbranch_scc1 Lk93_%=\n" \
"v_sub_f32 v80,v80,v62\nv_sub_f32 v81,v81,v62\nv_sub_f32 v82,v82,v62\nv_sub_f32 v83,v83,v62\nv_sub_f32 v84,v84,v62\nv_sub_f32 v85,v85,v62\nv_sub_f32 v86,v86,v62\nv_sub_f32 v87,v87,v62\n" \
"Lk93_%=:\ns_cmp_gt_u32 s8,3\ns_cbranch_scc0 Lk96_%=\nv_mov_b32 v27,v19\ns_cmp_eq_u32 s8,4\ns_cbranch_scc0 Lk95_%=\nM v[24:27],a[236:239],v[120:123],v[24:27]\ns_nop 7\n" \
"s_branch Lk95_%=\nLk96_%=:\nv_max3_f32 v62,v120,v121,v122\nv_max3_f32 v63,v123,v124,v125\nv_max3_f32 v62,v62,v126,v127\nv_max3_f32 v62,v62,v63,0\nv_mov_b32 v63,v62\ns_nop 1\n" \
"v_permlane32_swap_b32 v62,v63\nv_max_f32 v62,v62,v63\nv_mov_b32 v63,v62\ns_nop 1\nv_permlane16_swap_b32 v62,v63\nv_max_f32 v62,v62,v63\nv_exp_f32 v63,-v62\nv_sub_f32 v120,v120,v62\n" \
"v_sub_f32 v121,v121,v62\nv_sub_f32 v122,v122,v62\nv_sub_f32 v123,v123,v62\nv_sub_f32 v124,v124,v62\nv_sub_f32 v125,v125,v62\nv_sub_f32 v126,v126,v62\nv_sub_f32 v127,v127,v62\nv_exp_f32 v120,v120\n" \
"v_exp_f32 v121,v121\nv_exp_f32 v122,v122\nv_exp_f32 v123,v123\nv_exp_f32 v124,v124\nv_exp_f32 v125,v125\nv_exp_f32 v126,v126\nv_exp_f32 v127,v127\nv_mul_f32 v27,v19,v63\n" \
"C v120,v120,v121\nC v121,v122,v123\nC v122,v124,v125\nC v123,v126,v127\nv_sub_f32 v44,v44,v62\nv_sub_f32 v45,v45,v62\nv_sub_f32 v46,v46,v62\nv_sub_f32 v47,v47,v62\n" \
"M v[24:27],a[236:239],v[120:123],v[24:27]\nv_mul_f32 v224,v224,v63\nv_mul_f32 v225,v225,v63\nv_mul_f32 v226,v226,v63\nv_mul_f32 v227,v227,v63\nv_mul_f32 v228,v228,v63\nv_mul_f32 v229,v229,v63\nv_mul_f32 v230,v230,v63\n" \
"v_mul_f32 v231,v231,v63\nv_mul_f32 v232,v232,v63\nv_mul_f32 v233,v233,v63\nv_mul_f32 v234,v234,v63\nv_mul_f32 v235,v235,v63\nv_mul_f32 v236,v236,v63\nv_mul_f32 v237,v237,v63\nv_mul_f32 v238,v238,v63\n" \
"v_mul_f32 v239,v239,v63\nv_mul_f32 v240,v240,v63\nv_mul_f32 v241,v241,v63\nv_mul_f32 v242,v242,v63\nv_mul_f32 v243,v243,v63\nv_mul_f32 v244,v244,v63\nv_mul_f32 v245,v245,v63\nv_mul_f32 v246,v246,v63\n" \
"v_mul_f32 v247,v247,v63\nv_mul_f32 v248,v248,v63\nv_mul_f32 v249,v249,v63\nv_mul_f32 v250,v250,v63\nv_mul_f32 v251,v251,v63\nv_mul_f32 v252,v252,v63\nv_mul_f32 v253,v253,v63\nv_mul_f32 v254,v254,v63\n" \
"v_mul_f32 v255,v255,v63\ns_bitcmp1_b32 s64,6\ns_cbranch_scc1 Lk95_%=\nv_sub_f32 v88,v88,v62\nv_sub_f32 v89,v89,v62\nv_sub_f32 v90,v90,v62\nv_sub_f32 v91,v91,v62\nv_sub_f32 v92,v92,v62\n" \
"v_sub_f32 v93,v93,v62\nv_sub_f32 v94,v94,v62\nv_sub_f32 v95,v95,v62\nLk95_%=:\ns_nop 7\nLSR1_%=:\ns_cmp_eq_u32 s64,0\ns_cbranch_scc1 Lcr2_%=\n" \
"s_cmp_eq_u32 s64,1\ns_cbranch_scc1 Lcr6_%=\ns_cmp_eq_u32 s64,2\ns_cbranch_scc1 Lcr10_%=\ns_cmp_eq_u32 s64,3\ns_cbranch_scc1 Lcr14_%=\ns_cmp_eq_u32 s64,4\ns_cbranch_scc1 Lcr42_%=\n" \
"s_cmp_eq_u32 s64,5\ns_cbranch_scc1 Lcr46_%=\ns_cmp_eq_u32 s64,6\ns_cbranch_scc1 Lcr50_%=\ns_cmp_eq_u32 s64,7\ns_cbranch_scc1 Lcr54_%=\ns_cmp_eq_u32 s64,8\ns_cbranch_scc1 Lcr58_%=\n" \
"s_branch Lcr62_%=\nLPU_%=:\ns_mov_b32 s25,0\nLPUL_%=:\ns_sleep 2\nbuffer_load_dwordx2 v[58:59],v57,s[36:39],0 offen sc1\ns_add_u32 s25,s25,1\ns_waitcnt vmcnt(0)\n" \
"v_cmp_gt_u64 s[98:99],s[14:15],v[58:59]\nv_cmp_le_u64 s[18:19],s[16:17],v[58:59]\ns_cmp_gt_u32 s25,262144\ns_cbranch_scc1 LPUR_%=\ns_or_b64 s[98:99],s[98:99],s[18:19]\ns_cbranch_scc1 LPUL_%=\ns_branch LPUR_%=\nLPK_%=:\n" \
"s_mov_b32 s25,0\nLPKL_%=:\ns_sleep 2\nbuffer_load_dwordx2 v[58:59],v57,s[36:39],0 offen sc1\ns_add_u32 s25,s25,1\ns_waitcnt vmcnt(0)\nv_cmp_gt_u64 s[98:99],s[14:15],v[58:59]\nv_cmp_le_u64 s[18:19],s[16:17],v[58:59]\n" \
"s_cmp_gt_u32 s25,262144\ns_cbranch_scc1 LPKR_%=\ns_or_b64 s[98:99],s[98:99],s[18:19]\ns_cbranch_scc1 LPKL_%=\ns_branch LPKR_%=\nLPZ_%=:\ns_mov_b32 s25,0\nLPZL_%=:\n" \
"s_sleep 2\nbuffer_load_dwordx2 v[10:11],v5,s[36:39],0 offen sc1\ns_add_u32 s25,s25,1\ns_waitcnt vmcnt(0)\nv_cmp_gt_u64 s[98:99],s[14:15],v[10:11]\nv_cmp_le_u64 s[18:19],s[16:17],v[10:11]\ns_cmp_gt_u32 s25,262144\ns_cbranch_scc1 LPZR_%=\n" \
"s_or_b64 s[98:99],s[98:99],s[18:19]\ns_cbranch_scc1 LPZL_%=\ns_branch LPZR_%=\nLPE_%=:\ns_mov_b32 s25,0\nLPEL_%=:\ns_sleep 2\nbuffer_load_dwordx2 v[10:11],v5,s[36:39],0 offen sc1\n" \
"s_add_u32 s25,s25,1\ns_waitcnt vmcnt(0)\nv_cmp_gt_u64 s[98:99],s[14:15],v[10:11]\nv_cmp_le_u64 s[18:19],s[16:17],v[10:11]\ns_cmp_gt_u32 s25,262144\ns_cbranch_scc1 LPER_%=\ns_or_b64 s[98:99],s[98:99],s[18:19]\ns_and_b32 s98,s98,0xffff\n" \
"s_cbranch_scc1 LPEL_%=\ns_branch LPER_%=\nLPF_%=:\ns_mov_b32 s25,0\nLPFL_%=:\ns_sleep 2\nbuffer_load_dwordx2 v[10:11],v5,s[36:39],0 offen sc1\ns_add_u32 s25,s25,1\n" \
"s_waitcnt vmcnt(0)\nv_cmp_gt_u64 s[98:99],s[14:15],v[10:11]\nv_cmp_le_u64 s[18:19],s[16:17],v[10:11]\ns_cmp_gt_u32 s25,262144\ns_cbranch_scc1 LPFR_%=\ns_or_b64 s[98:99],s[98:99],s[18:19]\ns_and_b32 s98,s98,0xff0000\ns_cbranch_scc1 LPFL_%=\n" \
"s_branch LPFR_%=\nLPM_%=:\ns_mov_b32 s25,0\nLPML_%=:\ns_sleep 2\nbuffer_load_dwordx2 v[10:11],v5,s[36:39],0 offen sc1\ns_add_u32 s25,s25,1\ns_waitcnt vmcnt(0)\n" \
"v_cmp_gt_u64 s[98:99],s[14:15],v[10:11]\nv_cmp_le_u64 s[18:19],s[16:17],v[10:11]\ns_cmp_gt_u32 s25,262144\ns_cbranch_scc1 LPMR_%=\ns_or_b64 s[98:99],s[98:99],s[18:19]\ns_and_b32 s98,s98,0xff000000\ns_cbranch_scc1 LPML_%=\ns_branch LPMR_%=\n" \
"LPL_%=:\ns_mov_b32 s25,0\nLPLL_%=:\ns_sleep 2\nbuffer_load_dwordx2 v[10:11],v58,s[36:39],0 offen sc1\ns_add_u32 s25,s25,1\ns_waitcnt vmcnt(0)\nv_cmp_gt_u64 s[98:99],s[14:15],v[10:11]\n" \
"v_cmp_le_u64 s[18:19],s[16:17],v[10:11]\ns_cmp_gt_u32 s25,262144\ns_cbranch_scc1 LPLR_%=\ns_or_b64 s[98:99],s[98:99],s[18:19]\ns_cbranch_scc1 LPLL_%=\ns_branch LPLR_%=\nLPP_%=:\ns_mov_b32 s25,0\n" \
"LPPL_%=:\ns_sleep 2\nbuffer_load_dwordx2 v[10:11],v190,s[36:39],0 offen sc1\ns_add_u32 s25,s25,1\ns_waitcnt vmcnt(0)\nv_cmp_gt_u64 s[98:99],s[14:15],v[10:11]\nv_cmp_le_u64 s[18:19],s[16:17],v[10:11]\ns_cmp_gt_u32 s25,262144\n" \
"s_cbranch_scc1 LPPR_%=\ns_or_b64 s[98:99],s[98:99],s[18:19]\ns_and_b32 s98,s98,0x7\ns_cbranch_scc1 LPPL_%=\ns_branch LPPR_%=\nLIN_%=:\ns_mov_b32 s25,0\nLINL_%=:\n" \
"s_sleep 1\ns_add_u32 s57,s49,4096\nbuffer_load_dwordx4 v[176:179],v4,s[36:39],s57 offen offset:2048 sc1\nbuffer_load_dwordx4 v[180:183],v4,s[36:39],s57 offen offset:3072 sc1\ns_add_u32 s57,s49,8192\nbuffer_load_dwordx4 v[32:35],v4,s[36:39],s57 offen offset:0 sc1\nbuffer_load_dwordx4 v[36:39],v4,s[36:39],s57 offen offset:1024 sc1\ns_add_u32 s25,s25,1\n" \
"s_waitcnt vmcnt(0)\nv_max_u32 v10,v176,v177\nv_max3_u32 v10,v10,v178,v179\nv_max3_u32 v10,v10,v180,v181\nv_max3_u32 v10,v10,v182,v183\nv_max3_u32 v10,v10,v32,v33\nv_max3_u32 v10,v10,v34,v35\nv_max3_u32 v10,v10,v36,v37\n" \
"v_max3_u32 v10,v10,v38,v39\nv_cmp_eq_u32 vcc,-1,v10\ns_cmp_gt_u32 s25,262144\ns_cbranch_scc1 LINR_%=\ns_cbranch_vccnz LINL_%=\ns_branch LINR_%=\nLIO_%=:\ns_mov_b32 s25,0\n" \
"LIOL_%=:\ns_sleep 1\ns_add_u32 s57,s49,8192\nbuffer_load_dwordx4 v[40:43],v4,s[36:39],s57 offen offset:2048 sc1\nbuffer_load_dwordx4 v[44:47],v4,s[36:39],s57 offen offset:3072 sc1\ns_add_u32 s25,s25,1\ns_waitcnt vmcnt(0)\nv_max_u32 v10,v40,v41\n" \
"v_max3_u32 v10,v10,v42,v43\nv_max3_u32 v10,v10,v44,v45\nv_max3_u32 v10,v10,v46,v47\nv_cmp_eq_u32 vcc,-1,v10\ns_cmp_gt_u32 s25,262144\ns_cbranch_scc1 LIOR_%=\ns_cbranch_vccnz LIOL_%=\ns_branch LIOR_%=\n" \
"LEND_%=:\ns_waitcnt vmcnt(0) lgkmcnt(0)\n.purgem M\n.purgem C\n" \
""
constexpr int Q_LDS = 163840;
__global__ __launch_bounds__(256, 1) void fused_kernel(const void* x, const void* k, const void* v, const void* shk, const void* ssmk, const void* ssmD, const void* ckv,
                                                       const void* wq, const void* wuk, const void* wuv, const void* wo, const void* gw, void* out, void* ws) {
    const int w = __builtin_amdgcn_readfirstlane(threadIdx.x >> 6);
    const unsigned long long ka = (unsigned long long)__builtin_amdgcn_kernarg_segment_ptr();
    asm volatile(QKV_ASM_TEXT :: "s"(ka), "s"(blockIdx.x), "s"(w) : QKV_ASM_CLOBBERS);
}

extern "C" void kernel_launch(void* const* d_in, const int* in_sizes, int n_in, void* d_out, int out_size, void* d_ws, size_t ws_size,
                              hipStream_t stream) {
    (void)hipFuncSetAttribute((const void*)fused_kernel, hipFuncAttributeMaxDynamicSharedMemorySize, Q_LDS);
    fused_kernel<<<256, 256, Q_LDS, stream>>>(d_in[0], d_in[1], d_in[2], d_in[3], d_in[4], d_in[5], d_in[7], d_in[8], d_in[9], d_in[10], d_in[11], d_in[12], d_out, d_ws);
}
```

```cpp
#include <hip/hip_runtime.h>
#include <stdint.h>
#define QKV_ASM_CLOBBERS "v0", "v1", "v2", "v3", "v4", "v5", "v6", "v7", "v8", "v9", "v10", "v11", "v12", "v13", "v14", "v15", "v16", "v17", "v18", "v19", "v20", "v21", "v22", "v23", "v24", "v25", "v26", "v27", "v28", "v29", "v30", "v31", "v32", "v33", "v34", "v35", "v36", "v37", "v38", "v39", "v40", "v41", "v42", "v43", "v44", "v45", "v46", "v47", "v48", "v49", "v50", "v51", "v52", "v53", "v54", "v55", "v56", "v57", "v58", "v59", "v60", "v61", "v62", "v63", "v64", "v65", "v66", "v67", "v68", "v69", "v70", "v71", "v72", "v73", "v74", "v75", "v76", "v77", "v78", "v79", "v80", "v81", "v82", "v83", "v84", "v85", "v86", "v87", "v88", "v89", "v90", "v91", "v92", "v93", "v94", "v95", "v96", "v97", "v98", "v99", "v100", "v101", "v102", "v103", "v104", "v105", "v106", "v107", "v108", "v109", "v110", "v111", "v112", "v113", "v114", "v115", "v116", "v117", "v118", "v119", "v120", "v121", "v122", "v123", "v124", "v125", "v126", "v127", "v128", "v129", "v130", "v131", "v132", "v133", "v134", "v135", "v136", "v137", "v138", "v139", "v140", "v141", "v142", "v143", "v144", "v145", "v146", "v147", "v148", "v149", "v150", "v151", "v152", "v153", "v154", "v155", "v156", "v157", "v158", "v159", "v160", "v161", "v162", "v163", "v164", "v165", "v166", "v167", "v168", "v169", "v170", "v171", "v172", "v173", "v174", "v175", "v176", "v177", "v178", "v179", "v180", "v181", "v182", "v183", "v184", "v185", "v186", "v187", "v188", "v189", "v190", "v191", "v192", "v193", "v194", "v195", "v196", "v197", "v198", "v199", "v200", "v201", "v202", "v203", "v204", "v205", "v206", "v207", "v208", "v209", "v210", "v211", "v212", "v213", "v214", "v215", "v216", "v217", "v218", "v219", "v220", "v221", "v222", "v223", "v224", "v225", "v226", "v227", "v228", "v229", "v230", "v231", "v232", "v233", "v234", "v235", "v236", "v237", "v238", "v239", "v240", "v241", "v242", "v243", "v244", "v245", "v246", "v247", "v248", "v249", "v250", "v251", "v252", "v253", "v254", "v255", "a0", "a1", "a2", "a3", "a4", "a5", "a6", "a7", "a8", "a9", "a10", "a11", "a12", "a13", "a14", "a15", "a16", "a17", "a18", "a19", "a20", "a21", "a22", "a23", "a24", "a25", "a26", "a27", "a28", "a29", "a30", "a31", "a32", "a33", "a34", "a35", "a36", "a37", "a38", "a39", "a40", "a41", "a42", "a43", "a44", "a45", "a46", "a47", "a48", "a49", "a50", "a51", "a52", "a53", "a54", "a55", "a56", "a57", "a58", "a59", "a60", "a61", "a62", "a63", "a64", "a65", "a66", "a67", "a68", "a69", "a70", "a71", "a72", "a73", "a74", "a75", "a76", "a77", "a78", "a79", "a80", "a81", "a82", "a83", "a84", "a85", "a86", "a87", "a88", "a89", "a90", "a91", "a92", "a93", "a94", "a95", "a96", "a97", "a98", "a99", "a100", "a101", "a102", "a103", "a104", "a105", "a106", "a107", "a108", "a109", "a110", "a111", "a112", "a113", "a114", "a115", "a116", "a117", "a118", "a119", "a120", "a121", "a122", "a123", "a124", "a125", "a126", "a127", "a128", "a129", "a130", "a131", "a132", "a133", "a134", "a135", "a136", "a137", "a138", "a139", "a140", "a141", "a142", "a143", "a144", "a145", "a146", "a147", "a148", "a149", "a150", "a151", "a152", "a153", "a154", "a155", "a156", "a157", "a158", "a159", "a160", "a161", "a162", "a163", "a164", "a165", "a166", "a167", "a168", "a169", "a170", "a171", "a172", "a173", "a174", "a175", "a176", "a177", "a178", "a179", "a180", "a181", "a182", "a183", "a184", "a185", "a186", "a187", "a188", "a189", "a190", "a191", "a192", "a193", "a194", "a195", "a196", "a197", "a198", "a199", "a200", "a201", "a202", "a203", "a204", "a205", "a206", "a207", "a208", "a209", "a210", "a211", "a212", "a213", "a214", "a215", "a216", "a217", "a218", "a219", "a220", "a221", "a222", "a223", "a224", "a225", "a226", "a227", "a228", "a229", "a230", "a231", "a232", "a233", "a234", "a235", "a236", "a237", "a238", "a239", "a240", "a241", "a242", "a243", "a244", "a245", "a246", "a247", "a248", "a249", "a250", "a251", "a252", "a253", "a254", "a255", "s4", "s5", "s6", "s7", "s8", "s9", "s10", "s11", "s12", "s13", "s14", "s15", "s16", "s17", "s18", "s19", "s20", "s21", "s22", "s23", "s24", "s25", "s26", "s27", "s28", "s29", "s30", "s31", "s32", "s33", "s34", "s35", "s36", "s37", "s38", "s39", "s40", "s41", "s42", "s43", "s44", "s45", "s46", "s47", "s48", "s49", "s50", "s51", "s52", "s53", "s54", "s55", "s56", "s57", "s58", "s59", "s60", "s61", "s62", "s63", "s64", "s65", "s66", "s67", "s68", "s69", "s70", "s71", "s72", "s73", "s74", "s75", "s76", "s77", "s78", "s79", "s80", "s81", "s82", "s83", "s84", "s85", "s86", "s87", "s88", "s89", "s90", "s91", "s92", "s93", "s94", "s95", "s96", "s97", "s98", "s99", "s100", "s101", "vcc", "scc", "memory"
#define QKV_ASM_TEXT \
".macro M d,a,b,c\nv_mfma_f32_16x16x32_bf16 \\d,\\a,\\b,\\c\n.endm\n.macro C d,a,b\nv_cvt_pk_bf16_f32 \\d,\\a,\\b\n.endm\ns_memrealtime s[18:19]\ns_load_dwordx16 s[64:79],%0,0x0\n" \
"s_load_dwordx8 s[80:87],%0,0x40\ns_load_dwordx4 s[88:91],%0,0x60\nv_mbcnt_lo_u32_b32 v23,-1,0\nv_mbcnt_hi_u32_b32 v23,-1,v23\ns_bfe_u32 s7,%1,0x30003\ns_and_b32 s6,%1,7\ns_lshl_b32 s6,s6,2\ns_lshr_b32 s60,%1,6\n" \
"s_add_u32 s6,s6,s60\nv_lshlrev_b32 v4,4,v23\ns_getpc_b64 s[40:41]\nLCTBEG_%=:\ns_add_u32 s40,s40,LBEG_%=-LCTBEG_%=\ns_addc_u32 s41,s41,0\ns_mov_b64 s[100:101],s[40:41]\ns_and_b32 s41,s41,0xffff\n" \
"s_mov_b32 s42,LEND_%=-LBEG_%=\ns_mov_b32 s43,0x00020000\ns_lshr_b32 s9,%1,3\ns_lshl_b32 s9,s9,2\ns_add_u32 s9,s9,%2\ns_and_b32 s9,s9,31\ns_lshl_b32 s9,s9,13\nv_lshlrev_b32 v64,3,v4\n" \
"buffer_load_dword v64,v64,s[40:43],s9 offen\ns_lshl_b32 s60,%2,2\nv_lshrrev_b32 v24,4,v23\nv_add_u32 v25,s60,v24\nv_xor_b32 v25,v25,v23\nv_and_b32 v25,7,v25\ns_lshl_b32 s61,%2,3\nv_lshrrev_b32 v26,3,v23\n" \
"v_add_u32 v26,s61,v26\nv_lshlrev_b32 v26,10,v26\nv_lshl_add_u32 v13,v25,4,v26\nv_add_u32 v63,v13,v26\ns_lshl_b32 s58,s7,17\ns_sub_u32 s59,1966080,s58\nv_add_u32 v17,s59,v13\nv_add_u32 v13,s58,v13\n" \
"v_add_u32 v14,0x8000,v13\nv_add_u32 v18,0x8000,v17\nv_add_u32 v15,0x8000,v14\nv_add_u32 v19,0x8000,v18\nv_add_u32 v16,0x8000,v15\nv_add_u32 v20,0x8000,v19\nv_bfe_u32 v25,v23,2,3\nv_add_u32 v25,s61,v25\n" \
"v_lshlrev_b32 v25,13,v25\ns_lshl_b32 s61,%2,1\nv_and_b32 v26,1,v24\nv_add_u32 v26,s61,v26\nv_xor_b32 v26,v26,v23\nv_and_b32 v26,3,v26\nv_lshrrev_b32 v27,5,v23\nv_lshl_add_u32 v26,v27,2,v26\n" \
"v_lshl_add_u32 v21,v26,4,v25\ns_lshl_b32 s50,%2,10\ns_add_u32 s50,s50,65536\ns_lshl_b32 s22,%2,12\ns_lshl_b32 s51,%2,11\ns_mov_b32 s31,0x00020000\ns_mov_b32 s35,0x00020000\ns_mov_b32 s95,0x00020000\n" \
"s_mov_b32 s30,2097152\ns_lshl_b32 s98,s6,8\ns_sub_u32 s34,4194304,s98\ns_mov_b32 s94,s34\ns_sub_u32 s49,8388608,s98\ns_cmp_lt_u32 %2,2\ns_cselect_b32 s57,s58,s59\ns_lshr_b32 s57,s57,1\n" \
"s_lshl_b32 s60,s6,20\ns_add_u32 s57,s57,s60\ns_and_b32 s54,%2,1\ns_lshl_b32 s60,s54,13\ns_add_u32 s57,s57,s60\ns_add_u32 s57,s57,16777216\ns_bitcmp1_b32 %2,0\ns_cselect_b32 s20,1.0,0x3e0293ee\n" \
"s_lshl_b32 s54,s54,11\ns_lshl_b32 s24,s6,3\ns_add_u32 s24,s24,s7\ns_lshl_b32 s24,s24,3\ns_lshl_b32 s60,%2,27\ns_add_u32 s24,s24,s60\ns_add_u32 s24,s24,94375936\ns_and_b32 s8,%1,7\n" \
"s_lshl_b32 s23,s8,8\ns_add_u32 s23,s23,s60\ns_and_b32 s60,%1,0xf8\ns_add_u32 s23,s23,s60\ns_add_u32 s23,s23,94380032\ns_lshl_b32 s8,s8,5\ns_lshr_b32 s58,%1,3\ns_add_u32 s8,s8,s58\n" \
"v_lshrrev_b32 v23,4,v4\nv_and_b32 v24,15,v23\nv_lshrrev_b32 v25,4,v23\ns_lshr_b32 s58,%2,1\ns_lshl_b32 s58,s58,14\ns_add_u32 s58,s58,65536\nv_lshrrev_b32 v26,1,v24\nv_xor_b32 v26,v26,v25\n" \
"v_lshlrev_b32 v26,4,v26\nv_lshl_add_u32 v26,v24,7,v26\ns_waitcnt lgkmcnt(0)\ns_mov_b64 s[28:29],s[76:77]\ns_and_b32 s29,s29,0xffff\ns_add_u32 s32,s80,s98\ns_addc_u32 s33,s81,0\ns_and_b32 s33,s33,0xffff\n" \
"s_add_u32 s92,s82,s98\ns_addc_u32 s93,s83,0\ns_and_b32 s93,s93,0xffff\ns_mov_b32 s47,0\ns_add_u32 m0,s22,0\nv_add_u32 v5,s58,v26\nbuffer_load_dwordx4 v21,s[32:35],s47 offen offset:0 lds\nv_xor_b32 v6,64,v5\n" \
"v_add_u32 v7,0x10000,v5\nv_add_u32 v8,0x10000,v6\nv_lshrrev_b32 v26,2,v24\ns_mov_b32 s46,0\ns_add_u32 m0,s50,65536\nv_lshlrev_b32 v26,6,v26\nbuffer_load_dwordx4 v13,s[28:31],s46 offen lds\nv_and_b32 v27,1,v23\n" \
"v_lshl_add_u32 v26,v27,3,v26\nv_bfe_u32 v27,v23,1,1\nv_lshlrev_b32 v28,1,v25\ns_add_u32 m0,s22,896\nv_xor_b32 v27,v27,v28\nbuffer_load_dwordx4 v21,s[32:35],s47 offen offset:128 lds\nv_and_b32 v27,3,v27\nv_lshl_add_u32 v26,v27,4,v26\n" \
"v_lshl_add_u32 v9,v25,12,v26\nv_add_u32 v9,s54,v9\ns_add_u32 m0,s50,69632\nv_lshl_add_u32 v62,v25,11,s54\nbuffer_load_dwordx4 v14,s[28:31],s46 offen lds\nv_xor_b32 v10,0x110,v9\nv_xor_b32 v11,32,v9\nv_xor_b32 v12,0x130,v9\n" \
"s_and_b32 s62,%1,7\ns_add_u32 m0,s22,2048\ns_lshl_b32 s62,s62,2\nbuffer_load_dwordx4 v21,s[92:95],s47 offen offset:0 lds\ns_add_u32 s62,s62,%2\ns_lshl_b32 s62,s62,5\ns_and_b32 s63,%1,0xf8\nv_bfe_u32 v24,v4,4,3\n" \
"s_add_u32 m0,s50,73728\nv_lshrrev_b32 v23,7,v4\nbuffer_load_dwordx4 v15,s[28:31],s46 offen lds\nv_lshl_add_u32 v24,v24,2,s62\nv_add_u32 v25,s63,v23\nv_lshlrev_b32 v26,14,v25\nv_lshl_add_u32 v59,v24,1,v26\ns_add_u32 m0,s22,2944\n" \
"v_add_u32 v54,0xffffe800,v59\nbuffer_load_dwordx4 v21,s[92:95],s47 offen offset:128 lds\nv_add_u32 v55,0xfffff800,v59\nv_lshlrev_b32 v60,12,v24\nv_lshl_add_u32 v60,v25,4,v60\nv_lshlrev_b32 v27,3,v24\ns_add_u32 m0,s50,77824\ns_lshl_b32 s62,%1,2\n" \
"buffer_load_dwordx4 v16,s[28:31],s46 offen lds\ns_add_u32 s62,s62,%2\ns_lshl_b32 s63,s62,12\nv_add_u32 v26,s63,v4\ns_lshl_b32 s62,s62,3\ns_mov_b32 s46,0\ns_add_u32 m0,s50,81920\ns_add_u32 s62,s62,71303168\n" \
"buffer_load_dwordx4 v17,s[28:31],s46 offen lds\nv_and_b32 v61,4,v23\nv_add_u32 v61,s62,v61\nv_lshlrev_b32 v2,3,v4\nv_and_b32 v1,0x380,v2\ns_add_u32 m0,s50,86016\nv_lshrrev_b32 v23,7,v4\nbuffer_load_dwordx4 v18,s[28:31],s46 offen lds\n" \
"v_lshl_add_u32 v1,v23,13,v1\ns_lshr_b32 s58,%1,3\ns_lshl_b32 s58,s58,2\ns_add_u32 s58,s58,%2\ns_add_u32 m0,s50,90112\ns_and_b32 s60,%1,7\nbuffer_load_dwordx4 v19,s[28:31],s46 offen lds\ns_lshl_b32 s60,s60,10\n" \
"s_lshl_b32 s59,s58,14\ns_add_u32 s59,s59,s60\ns_mov_b64 s[4:5],s[90:91]\ns_add_u32 m0,s50,94208\ns_lshl_b32 s99,s58,11\nbuffer_load_dwordx4 v20,s[28:31],s46 offen lds\ns_lshl_b32 s96,s60,8\ns_add_u32 s99,s99,s96\n" \
"s_add_u32 s96,s76,s99\ns_addc_u32 s97,s77,0\ns_add_u32 s76,s78,s59\ns_addc_u32 s77,s79,0\ns_mov_b32 s47,262144\ns_add_u32 m0,s22,16384\ns_and_b32 s59,s58,63\nbuffer_load_dwordx4 v21,s[32:35],s47 offen offset:0 lds\n" \
"s_lshl_b32 s59,s59,13\ns_add_u32 s59,s59,s60\ns_cmp_lt_u32 s58,64\ns_cselect_b64 s[84:85],s[80:81],s[82:83]\ns_add_u32 s84,s84,s59\ns_addc_u32 s85,s85,0\ns_add_u32 m0,s22,17280\ns_lshl_b32 s59,s8,2\n" \
"buffer_load_dwordx4 v21,s[32:35],s47 offen offset:128 lds\ns_add_u32 s59,s59,%2\ns_lshl_b32 s59,s59,12\ns_add_u32 s88,s72,s59\ns_addc_u32 s89,s73,0\ns_add_u32 m0,s22,18432\ns_nop 0\nbuffer_load_dwordx4 v21,s[92:95],s47 offen offset:0 lds\n" \
"s_add_u32 m0,s22,19328\ns_nop 0\nbuffer_load_dwordx4 v21,s[92:95],s47 offen offset:128 lds\ns_mov_b64 exec,0xff\nglobal_load_dword v0,v1,s[84:85]\ns_mov_b64 exec,-1\ns_add_u32 s84,s84,524288\ns_addc_u32 s85,s85,0\n" \
"s_mov_b64 exec,0xff\nglobal_load_dword v0,v1,s[84:85]\ns_mov_b64 exec,-1\ns_add_u32 s84,s84,524288\ns_addc_u32 s85,s85,0\ns_mov_b64 exec,0xff\nglobal_load_dword v0,v1,s[84:85]\ns_mov_b64 exec,-1\n" \
"s_mov_b64 exec,0xff\nglobal_load_dword v0,v2,s[96:97]\ns_mov_b64 exec,-1\ns_mov_b64 exec,0xff00\nglobal_load_dword v0,v2,s[96:97]\ns_mov_b64 exec,-1\ns_mov_b64 exec,0\ns_cmp_eq_u32 %2,0\n" \
"s_cbranch_scc1 LG0_%=\ns_cmp_eq_u32 %2,1\ns_cbranch_scc1 LG1_%=\ns_cmp_eq_u32 %2,2\ns_cbranch_scc1 LG4_%=\ns_cmp_eq_u32 %2,3\ns_cbranch_scc1 LG5_%=\nLHR0_%=:\n" \
"s_mov_b64 exec,-1\nLBEG_%=:\ns_bitcmp1_b32 %2,0\ns_cbranch_scc1 LAV_%=\ns_waitcnt vmcnt(9)\ns_barrier\nds_read_b64_tr_b16 a[32:33],v9 offset:0\nds_read_b64_tr_b16 a[34:35],v10 offset:0\n" \
"ds_read_b64_tr_b16 a[36:37],v11 offset:0\nds_read_b64_tr_b16 a[38:39],v12 offset:0\nds_read_b128 a[24:27],v7 offset:12288\nds_read_b128 a[28:31],v7 offset:14336\ns_waitcnt lgkmcnt(0)\nM v[128:131],a[32:35],a[24:27],0\ns_add_u32 m0,s22,32768\ns_mov_b32 s47,524288\n" \
"s_mov_b32 s46,128\nds_read_b64_tr_b16 a[40:41],v9 offset:512\nds_read_b64_tr_b16 a[42:43],v10 offset:512\nds_read_b64_tr_b16 a[44:45],v11 offset:512\nds_read_b64_tr_b16 a[46:47],v12 offset:512\nds_read_b64_tr_b16 a[48:49],v9 offset:1024\nds_read_b64_tr_b16 a[50:51],v10 offset:1024\nds_read_b64_tr_b16 a[52:53],v11 offset:1024\n" \
"ds_read_b64_tr_b16 a[54:55],v12 offset:1024\nM v[132:135],a[36:39],a[24:27],0\nbuffer_load_dwordx4 v21,s[32:35],s47 offen offset:0 lds\nds_read_b64_tr_b16 a[56:57],v9 offset:1536\nds_read_b64_tr_b16 a[58:59],v10 offset:1536\nds_read_b64_tr_b16 a[60:61],v11 offset:1536\nds_read_b64_tr_b16 a[62:63],v12 offset:1536\nM v[140:143],a[36:39],a[28:31],0\n" \
"s_add_u32 m0,s22,33664\nM v[136:139],a[32:35],a[28:31],0\ns_waitcnt lgkmcnt(8)\ns_mov_b64 s[40:41],s[66:67]\nds_read_b128 a[16:19],v7 offset:8192\nM v[152:155],a[40:43],a[28:31],0\ns_and_b32 s41,s41,0xffff\nds_read_b128 a[20:23],v7 offset:10240\n" \
"M v[156:159],a[44:47],a[28:31],0\nbuffer_load_dwordx4 v21,s[32:35],s47 offen offset:128 lds\nM v[148:151],a[44:47],a[24:27],0\ns_add_u32 m0,s22,34816\nds_read_b128 a[0:3],v7 offset:0\nM v[144:147],a[40:43],a[24:27],0\ns_waitcnt lgkmcnt(3)\ns_mov_b32 s42,4194304\n" \
"ds_read_b128 a[4:7],v7 offset:2048\nM v[160:163],a[48:51],a[24:27],0\ns_mov_b32 s43,0x00020000\nds_read_b128 a[8:11],v7 offset:4096\nM v[164:167],a[52:55],a[24:27],0\nbuffer_load_dwordx4 v21,s[92:95],s47 offen offset:0 lds\nM v[172:175],a[52:55],a[28:31],0\ns_add_u32 m0,s22,35712\n" \
"M v[168:171],a[48:51],a[28:31],0\ns_mov_b64 s[80:81],s[68:69]\nds_read_b128 a[12:15],v7 offset:6144\nM v[184:187],a[56:59],a[28:31],0\ns_mov_b32 s52,0\nds_read_b128 a[88:91],v8 offset:12288\nM v[188:191],a[60:63],a[28:31],0\nbuffer_load_dwordx4 v21,s[92:95],s47 offen offset:128 lds\n" \
"M v[180:183],a[60:63],a[24:27],0\ns_add_u32 m0,s50,0\nds_read_b128 a[92:95],v8 offset:14336\nM v[176:179],a[56:59],a[24:27],0\ns_waitcnt lgkmcnt(3)\ns_mov_b32 s47,786432\nds_read_b128 a[80:83],v8 offset:8192\nM v[64:67],a[32:35],a[16:19],0\n" \
"s_waitcnt vmcnt(9) lgkmcnt(9)\nds_read_b128 a[84:87],v8 offset:10240\nM v[68:71],a[36:39],a[16:19],0\nbuffer_load_dwordx4 v13,s[28:31],s46 offen lds\ns_barrier\nM v[76:79],a[36:39],a[20:23],0\ns_add_u32 m0,s50,4096\nM v[72:75],a[32:35],a[20:23],0\n" \
"s_and_b32 s81,s81,0xffff\nds_read_b64_tr_b16 a[96:97],v9 offset:16384\nds_read_b64_tr_b16 a[98:99],v10 offset:16384\nM v[88:91],a[40:43],a[20:23],0\ns_mov_b32 s82,4194304\nds_read_b64_tr_b16 a[100:101],v11 offset:16384\nM v[92:95],a[44:47],a[20:23],0\nbuffer_load_dwordx4 v14,s[28:31],s46 offen lds\n" \
"M v[84:87],a[44:47],a[16:19],0\ns_add_u32 m0,s50,8192\nds_read_b64_tr_b16 a[102:103],v12 offset:16384\nM v[80:83],a[40:43],a[16:19],0\ns_mov_b32 s83,0x00020000\nds_read_b64_tr_b16 a[104:105],v9 offset:16896\nM v[96:99],a[48:51],a[16:19],0\ns_lshl_b32 s60,s7,18\n" \
"ds_read_b64_tr_b16 a[106:107],v10 offset:16896\nM v[100:103],a[52:55],a[16:19],0\nbuffer_load_dwordx4 v15,s[28:31],s46 offen lds\nM v[108:111],a[52:55],a[20:23],0\ns_add_u32 m0,s50,12288\nds_read_b64_tr_b16 a[108:109],v11 offset:16896\nM v[104:107],a[48:51],a[20:23],0\ns_sub_u32 s61,3932160,s60\n" \
"ds_read_b64_tr_b16 a[110:111],v12 offset:16896\nM v[120:123],a[56:59],a[20:23],0\ns_add_u32 s44,s78,s98\ns_addc_u32 s45,s79,0\nds_read_b64_tr_b16 a[112:113],v9 offset:17408\nds_read_b64_tr_b16 a[114:115],v10 offset:17408\nM v[124:127],a[60:63],a[20:23],0\nbuffer_load_dwordx4 v16,s[28:31],s46 offen lds\n" \
"M v[116:119],a[60:63],a[16:19],0\ns_add_u32 m0,s50,16384\ns_mov_b32 s46,128\nM v[112:115],a[56:59],a[16:19],0\ns_and_b32 s45,s45,0xffff\nds_read_b64_tr_b16 a[116:117],v11 offset:17408\nds_read_b64_tr_b16 a[118:119],v12 offset:17408\nM v[192:195],a[32:35],a[0:3],0\n" \
"s_mov_b64 s[36:37],s[90:91]\nds_read_b64_tr_b16 a[120:121],v9 offset:17920\nM v[196:199],a[36:39],a[0:3],0\nbuffer_load_dwordx4 v17,s[28:31],s46 offen lds\nM v[204:207],a[36:39],a[4:7],0\ns_add_u32 m0,s50,20480\nds_read_b64_tr_b16 a[122:123],v10 offset:17920\nM v[200:203],a[32:35],a[4:7],0\n" \
"s_and_b32 s37,s37,0xffff\nds_read_b64_tr_b16 a[124:125],v11 offset:17920\nM v[216:219],a[40:43],a[4:7],0\ns_mov_b32 s38,134217728\nds_read_b64_tr_b16 a[126:127],v12 offset:17920\nM v[220:223],a[44:47],a[4:7],0\nbuffer_load_dwordx4 v18,s[28:31],s46 offen lds\nM v[212:215],a[44:47],a[0:3],0\n" \
"s_add_u32 m0,s50,24576\nds_read_b128 a[64:67],v8 offset:0\nM v[208:211],a[40:43],a[0:3],0\ns_mov_b32 s39,0x00020000\nds_read_b128 a[68:71],v8 offset:2048\nM v[224:227],a[48:51],a[0:3],0\ns_add_u32 s96,s57,32768\nds_read_b128 a[72:75],v8 offset:4096\n" \
"M v[228:231],a[52:55],a[0:3],0\nbuffer_load_dwordx4 v19,s[28:31],s46 offen lds\nM v[236:239],a[52:55],a[4:7],0\ns_add_u32 m0,s50,28672\nM v[232:235],a[48:51],a[4:7],0\ns_lshr_b32 s62,%1,3\nds_read_b128 a[76:79],v8 offset:6144\nM v[248:251],a[56:59],a[4:7],0\n" \
"s_lshl_b32 s62,s62,2\nM v[252:255],a[60:63],a[4:7],0\nbuffer_load_dwordx4 v20,s[28:31],s46 offen lds\nM v[244:247],a[60:63],a[0:3],0\ns_add_u32 m0,s22,49152\nM v[240:243],a[56:59],a[0:3],0\ns_waitcnt lgkmcnt(1)\ns_mov_b32 s46,256\n" \
"M a[128:131],a[32:35],a[8:11],0\ns_add_u32 s62,s62,%2\nM a[132:135],a[36:39],a[8:11],0\nbuffer_load_dwordx4 v21,s[32:35],s47 offen offset:0 lds\nM a[140:143],a[36:39],a[12:15],0\ns_add_u32 m0,s22,50048\nM a[136:139],a[32:35],a[12:15],0\ns_lshl_b32 s62,s62,10\n" \
"M a[152:155],a[40:43],a[12:15],0\ns_min_u32 s62,s62,LEND_%=-LBEG_%=-1024\nM a[156:159],a[44:47],a[12:15],0\nbuffer_load_dwordx4 v21,s[32:35],s47 offen offset:128 lds\nM a[148:151],a[44:47],a[8:11],0\ns_add_u32 m0,s22,51200\nM a[144:147],a[40:43],a[8:11],0\ns_add_u32 s100,s100,s62\n" \
"s_addc_u32 s101,s101,0\nM a[160:163],a[48:51],a[8:11],0\ns_lshl_b32 s62,s8,2\nM a[164:167],a[52:55],a[8:11],0\nbuffer_load_dwordx4 v21,s[92:95],s47 offen offset:0 lds\nM a[172:175],a[52:55],a[12:15],0\ns_add_u32 m0,s22,52096\nM a[168:171],a[48:51],a[12:15],0\n" \
"s_add_u32 s62,s62,%2\nM a[184:187],a[56:59],a[12:15],0\ns_mov_b64 s[10:11],s[72:73]\nM a[188:191],a[60:63],a[12:15],0\nbuffer_load_dwordx4 v21,s[92:95],s47 offen offset:128 lds\nM a[180:183],a[60:63],a[8:11],0\ns_add_u32 m0,s50,32768\nM a[176:179],a[56:59],a[8:11],0\n" \
"s_mov_b32 s47,1048576\nM v[128:131],a[96:99],a[88:91],v[128:131]\ns_lshl_b32 s63,s62,12\nM v[132:135],a[100:103],a[88:91],v[132:135]\nbuffer_load_dwordx4 v13,s[28:31],s46 offen lds\nM v[140:143],a[100:103],a[92:95],v[140:143]\ns_add_u32 m0,s50,36864\nM v[136:139],a[96:99],a[92:95],v[136:139]\n" \
"s_add_u32 s68,s10,s63\ns_addc_u32 s69,s11,0\nM v[152:155],a[104:107],a[92:95],v[152:155]\ns_and_b32 s69,s69,0xffff\nM v[156:159],a[108:111],a[92:95],v[156:159]\nbuffer_load_dwordx4 v14,s[28:31],s46 offen lds\nM v[148:151],a[108:111],a[88:91],v[148:151]\ns_add_u32 m0,s50,40960\n" \
"M v[144:147],a[104:107],a[88:91],v[144:147]\ns_lshl_b32 s62,s62,1\nM v[160:163],a[112:115],a[88:91],v[160:163]\ns_mov_b64 s[12:13],s[74:75]\nM v[164:167],a[116:119],a[88:91],v[164:167]\nbuffer_load_dwordx4 v15,s[28:31],s46 offen lds\nM v[172:175],a[116:119],a[92:95],v[172:175]\ns_add_u32 m0,s50,45056\n" \
"M v[168:171],a[112:115],a[92:95],v[168:171]\ns_sub_u32 s14,s18,2000\ns_subb_u32 s15,s19,0\nM v[184:187],a[120:123],a[92:95],v[184:187]\ns_add_u32 s16,s14,16777216\ns_addc_u32 s17,s15,0\nM v[188:191],a[124:127],a[92:95],v[188:191]\nbuffer_load_dwordx4 v16,s[28:31],s46 offen lds\n" \
"M v[180:183],a[124:127],a[88:91],v[180:183]\ns_add_u32 m0,s50,49152\nM v[176:179],a[120:123],a[88:91],v[176:179]\ns_mov_b32 s46,256\nM v[64:67],a[96:99],a[80:83],v[64:67]\ns_lshr_b32 s21,s63,5\nM v[68:71],a[100:103],a[80:83],v[68:71]\nbuffer_load_dwordx4 v17,s[28:31],s46 offen lds\n" \
"M v[76:79],a[100:103],a[84:87],v[76:79]\ns_add_u32 m0,s50,53248\nM v[72:75],a[96:99],a[84:87],v[72:75]\ns_mov_b64 s[4:5],s[90:91]\nM v[88:91],a[104:107],a[84:87],v[88:91]\ns_waitcnt vmcnt(9) lgkmcnt(0)\nM v[92:95],a[108:111],a[84:87],v[92:95]\ns_add_u32 s63,s63,79691776\n" \
"s_barrier\nds_read_b64_tr_b16 a[32:33],v9 offset:32768\nM v[84:87],a[108:111],a[80:83],v[84:87]\nbuffer_load_dwordx4 v18,s[28:31],s46 offen lds\nM v[80:83],a[104:107],a[80:83],v[80:83]\ns_add_u32 m0,s50,57344\nds_read_b64_tr_b16 a[34:35],v10 offset:32768\nM v[96:99],a[112:115],a[80:83],v[96:99]\n" \
"s_add_u32 s72,s4,s63\ns_addc_u32 s73,s5,0\nds_read_b64_tr_b16 a[36:37],v11 offset:32768\nds_read_b64_tr_b16 a[38:39],v12 offset:32768\nM v[100:103],a[116:119],a[80:83],v[100:103]\ns_mov_b32 s63,65536\nds_read_b128 a[24:27],v5 offset:12288\nM v[108:111],a[116:119],a[84:87],v[108:111]\n" \
"buffer_load_dwordx4 v19,s[28:31],s46 offen lds\nM v[104:107],a[112:115],a[84:87],v[104:107]\ns_add_u32 m0,s50,61440\nM v[120:123],a[120:123],a[84:87],v[120:123]\ns_cmp_lt_u32 %2,2\ns_cselect_b32 s63,32768,s63\nds_read_b128 a[28:31],v5 offset:14336\nM v[124:127],a[124:127],a[84:87],v[124:127]\n" \
"s_lshl_b32 s56,%2,14\nds_read_b64_tr_b16 a[40:41],v9 offset:33280\nds_read_b64_tr_b16 a[42:43],v10 offset:33280\nM v[116:119],a[124:127],a[80:83],v[116:119]\nbuffer_load_dwordx4 v20,s[28:31],s46 offen lds\nM v[112:115],a[120:123],a[80:83],v[112:115]\ns_add_u32 m0,s22,0\nM v[192:195],a[96:99],a[64:67],v[192:195]\n" \
"s_mov_b32 s46,384\nds_read_b64_tr_b16 a[44:45],v11 offset:33280\nds_read_b64_tr_b16 a[46:47],v12 offset:33280\nM v[196:199],a[100:103],a[64:67],v[196:199]\ns_add_u32 s56,s56,s63\nds_read_b64_tr_b16 a[48:49],v9 offset:33792\nM v[204:207],a[100:103],a[68:71],v[204:207]\nbuffer_load_dwordx4 v21,s[32:35],s47 offen offset:0 lds\n" \
"M v[200:203],a[96:99],a[68:71],v[200:203]\ns_add_u32 m0,s22,896\nds_read_b64_tr_b16 a[50:51],v10 offset:33792\nM v[216:219],a[104:107],a[68:71],v[216:219]\ns_and_b32 s73,s73,0xffff\nds_read_b64_tr_b16 a[52:53],v11 offset:33792\nM v[220:223],a[108:111],a[68:71],v[220:223]\ns_mov_b32 s74,4096\n" \
"ds_read_b64_tr_b16 a[54:55],v12 offset:33792\nM v[212:215],a[108:111],a[64:67],v[212:215]\nbuffer_load_dwordx4 v21,s[32:35],s47 offen offset:128 lds\nM v[208:211],a[104:107],a[64:67],v[208:211]\ns_add_u32 m0,s22,2048\nds_read_b64_tr_b16 a[56:57],v9 offset:34304\nM v[224:227],a[112:115],a[64:67],v[224:227]\ns_mov_b32 s75,0x00020000\n" \
"ds_read_b64_tr_b16 a[58:59],v10 offset:34304\nM v[228:231],a[116:119],a[64:67],v[228:231]\ns_add_u32 s84,s84,524288\ns_addc_u32 s85,s85,0\nds_read_b64_tr_b16 a[60:61],v11 offset:34304\nds_read_b64_tr_b16 a[62:63],v12 offset:34304\nM v[236:239],a[116:119],a[68:71],v[236:239]\nbuffer_load_dwordx4 v21,s[92:95],s47 offen offset:0 lds\n" \
"M v[232:235],a[112:115],a[68:71],v[232:235]\ns_add_u32 m0,s22,2944\nM v[248:251],a[120:123],a[68:71],v[248:251]\ns_add_u32 s21,s21,67108864\nds_read_b128 a[16:19],v5 offset:8192\nM v[252:255],a[124:127],a[68:71],v[252:255]\ns_mov_b32 s53,s22\nds_read_b128 a[20:23],v5 offset:10240\n" \
"M v[244:247],a[124:127],a[64:67],v[244:247]\ns_mov_b64 exec,0xff\nglobal_load_dword v0,v1,s[84:85]\ns_mov_b64 exec,-1\nM v[240:243],a[120:123],a[64:67],v[240:243]\ns_add_u32 s84,s84,524288\ns_addc_u32 s85,s85,0\nds_read_b128 a[0:3],v5 offset:0\n" \
"ds_read_b128 a[4:7],v5 offset:2048\nM a[128:131],a[96:99],a[72:75],a[128:131]\ns_mov_b64 exec,0xff\nglobal_load_dword v0,v1,s[84:85]\ns_mov_b64 exec,-1\nM a[132:135],a[100:103],a[72:75],a[132:135]\ns_add_u32 s84,s84,524288\ns_addc_u32 s85,s85,0\n" \
"ds_read_b128 a[8:11],v5 offset:4096\nds_read_b128 a[12:15],v5 offset:6144\nM a[140:143],a[100:103],a[76:79],a[140:143]\nglobal_load_dwordx4 v[46:49],v4,s[86:87] offset:0\nM a[136:139],a[96:99],a[76:79],a[136:139]\nds_read_b128 a[88:91],v6 offset:12288\nds_read_b128 a[92:95],v6 offset:14336\nM a[152:155],a[104:107],a[76:79],a[152:155]\n" \
"ds_read_b128 a[64:67],v6 offset:0\nM a[156:159],a[108:111],a[76:79],a[156:159]\nds_read_b128 a[68:71],v6 offset:2048\nM a[148:151],a[108:111],a[72:75],a[148:151]\nglobal_load_dwordx4 v[50:53],v4,s[86:87] offset:1024\nM a[144:147],a[104:107],a[72:75],a[144:147]\nds_read_b128 a[80:83],v6 offset:8192\nds_read_b128 a[84:87],v6 offset:10240\n" \
"M a[160:163],a[112:115],a[72:75],a[160:163]\nM a[164:167],a[116:119],a[72:75],a[164:167]\nM a[172:175],a[116:119],a[76:79],a[172:175]\nglobal_load_dwordx4 v[30:33],v26,s[64:65] offset:0\nM a[168:171],a[112:115],a[76:79],a[168:171]\nM a[184:187],a[120:123],a[76:79],a[184:187]\nM a[188:191],a[124:127],a[76:79],a[188:191]\nM a[180:183],a[124:127],a[72:75],a[180:183]\n" \
"global_load_dwordx4 v[34:37],v26,s[64:65] offset:1024\nM a[176:179],a[120:123],a[72:75],a[176:179]\ns_waitcnt lgkmcnt(0)\nds_read_b128 a[72:75],v6 offset:4096\nds_read_b128 a[76:79],v6 offset:6144\nM v[128:131],a[32:35],a[24:27],v[128:131]\nM v[132:135],a[36:39],a[24:27],v[132:135]\nLG0_%=:\n" \
"s_cbranch_execz LG2_%=\nM v[140:143],a[36:39],a[28:31],v[140:143]\nbuffer_load_dwordx4 v21,s[92:95],s47 offen offset:128 lds\nM v[136:139],a[32:35],a[28:31],v[136:139]\ns_add_u32 m0,s22,16384\nM v[152:155],a[40:43],a[28:31],v[152:155]\ns_mov_b32 s47,1310720\nM v[156:159],a[44:47],a[28:31],v[156:159]\n" \
"M v[148:151],a[44:47],a[24:27],v[148:151]\nbuffer_load_dwordx4 v21,s[32:35],s47 offen offset:0 lds\nM v[144:147],a[40:43],a[24:27],v[144:147]\ns_add_u32 m0,s22,17280\nM v[160:163],a[48:51],a[24:27],v[160:163]\nM v[164:167],a[52:55],a[24:27],v[164:167]\nM v[172:175],a[52:55],a[28:31],v[172:175]\nbuffer_load_dwordx4 v21,s[32:35],s47 offen offset:128 lds\n" \
"M v[168:171],a[48:51],a[28:31],v[168:171]\ns_add_u32 m0,s22,18432\nM v[184:187],a[56:59],a[28:31],v[184:187]\ns_waitcnt vmcnt(20) lgkmcnt(14)\nM v[188:191],a[60:63],a[28:31],v[188:191]\ns_barrier\nds_read_b64_tr_b16 a[96:97],v9 offset:49152\nM v[180:183],a[60:63],a[24:27],v[180:183]\n" \
"buffer_load_dwordx4 v21,s[92:95],s47 offen offset:0 lds\nM v[176:179],a[56:59],a[24:27],v[176:179]\ns_add_u32 m0,s22,19328\nds_read_b64_tr_b16 a[98:99],v10 offset:49152\nM v[64:67],a[32:35],a[16:19],v[64:67]\nds_read_b64_tr_b16 a[100:101],v11 offset:49152\nds_read_b64_tr_b16 a[102:103],v12 offset:49152\nM v[68:71],a[36:39],a[16:19],v[68:71]\n" \
"ds_read_b64_tr_b16 a[104:105],v9 offset:49664\nM v[76:79],a[36:39],a[20:23],v[76:79]\nbuffer_load_dwordx4 v21,s[92:95],s47 offen offset:128 lds\nM v[72:75],a[32:35],a[20:23],v[72:75]\ns_add_u32 m0,s50,65536\nds_read_b64_tr_b16 a[106:107],v10 offset:49664\nM v[88:91],a[40:43],a[20:23],v[88:91]\ns_mov_b32 s47,1572864\n" \
"ds_read_b64_tr_b16 a[108:109],v11 offset:49664\nM v[92:95],a[44:47],a[20:23],v[92:95]\nds_read_b64_tr_b16 a[110:111],v12 offset:49664\nM v[84:87],a[44:47],a[16:19],v[84:87]\nds_read_b64_tr_b16 a[112:113],v9 offset:50176\nds_read_b64_tr_b16 a[114:115],v10 offset:50176\nM v[80:83],a[40:43],a[16:19],v[80:83]\nds_read_b64_tr_b16 a[116:117],v11 offset:50176\n" \
"M v[96:99],a[48:51],a[16:19],v[96:99]\nds_read_b64_tr_b16 a[118:119],v12 offset:50176\nM v[100:103],a[52:55],a[16:19],v[100:103]\nds_read_b64_tr_b16 a[120:121],v9 offset:50688\nds_read_b64_tr_b16 a[122:123],v10 offset:50688\nM v[108:111],a[52:55],a[20:23],v[108:111]\nds_read_b64_tr_b16 a[124:125],v11 offset:50688\nM v[104:107],a[48:51],a[20:23],v[104:107]\n" \
"s_mov_b64 exec,0\ns_cmp_lt_u32 %2,2\ns_cbranch_scc1 LG3_%=\ns_branch LG7_%=\nLHA155_%=:\ns_mov_b64 exec,-1\nds_read_b64_tr_b16 a[126:127],v12 offset:50688\nM v[120:123],a[56:59],a[20:23],v[120:123]\n" \
"M v[124:127],a[60:63],a[20:23],v[124:127]\nM v[116:119],a[60:63],a[16:19],v[116:119]\nM v[112:115],a[56:59],a[16:19],v[112:115]\nM v[192:195],a[32:35],a[0:3],v[192:195]\nM v[196:199],a[36:39],a[0:3],v[196:199]\nM v[204:207],a[36:39],a[4:7],v[204:207]\nM v[200:203],a[32:35],a[4:7],v[200:203]\nM v[216:219],a[40:43],a[4:7],v[216:219]\n" \
"buffer_load_dwordx4 v13,s[28:31],s46 offen lds\nM v[220:223],a[44:47],a[4:7],v[220:223]\ns_add_u32 m0,s50,69632\nM v[212:215],a[44:47],a[0:3],v[212:215]\nM v[208:211],a[40:43],a[0:3],v[208:211]\nM v[224:227],a[48:51],a[0:3],v[224:227]\nbuffer_load_dwordx4 v14,s[28:31],s46 offen lds\nM v[228:231],a[52:55],a[0:3],v[228:231]\n" \
"s_add_u32 m0,s50,73728\nM v[236:239],a[52:55],a[4:7],v[236:239]\nM v[232:235],a[48:51],a[4:7],v[232:235]\nM v[248:251],a[56:59],a[4:7],v[248:251]\nbuffer_load_dwordx4 v15,s[28:31],s46 offen lds\nM v[252:255],a[60:63],a[4:7],v[252:255]\ns_add_u32 m0,s50,77824\nM v[244:247],a[60:63],a[0:3],v[244:247]\n" \
"M v[240:243],a[56:59],a[0:3],v[240:243]\nM a[128:131],a[32:35],a[8:11],a[128:131]\nbuffer_load_dwordx4 v16,s[28:31],s46 offen lds\nM a[132:135],a[36:39],a[8:11],a[132:135]\ns_add_u32 m0,s50,81920\nM a[140:143],a[36:39],a[12:15],a[140:143]\ns_mov_b32 s46,384\nM a[136:139],a[32:35],a[12:15],a[136:139]\n" \
"M a[152:155],a[40:43],a[12:15],a[152:155]\nbuffer_load_dwordx4 v17,s[28:31],s46 offen lds\nM a[156:159],a[44:47],a[12:15],a[156:159]\ns_add_u32 m0,s50,86016\nM a[148:151],a[44:47],a[8:11],a[148:151]\nM a[144:147],a[40:43],a[8:11],a[144:147]\nM a[160:163],a[48:51],a[8:11],a[160:163]\nbuffer_load_dwordx4 v18,s[28:31],s46 offen lds\n" \
"M a[164:167],a[52:55],a[8:11],a[164:167]\ns_add_u32 m0,s50,90112\nM a[172:175],a[52:55],a[12:15],a[172:175]\nM a[168:171],a[48:51],a[12:15],a[168:171]\nM a[184:187],a[56:59],a[12:15],a[184:187]\nbuffer_load_dwordx4 v19,s[28:31],s46 offen lds\nM a[188:191],a[60:63],a[12:15],a[188:191]\ns_add_u32 m0,s50,94208\n" \
"M a[180:183],a[60:63],a[8:11],a[180:183]\nM a[176:179],a[56:59],a[8:11],a[176:179]\ns_waitcnt lgkmcnt(0)\nM v[128:131],a[96:99],a[88:91],v[128:131]\nbuffer_load_dwordx4 v20,s[28:31],s46 offen lds\nM v[132:135],a[100:103],a[88:91],v[132:135]\ns_add_u32 m0,s22,32768\nM v[140:143],a[100:103],a[92:95],v[140:143]\n" \
"s_waitcnt vmcnt(12) lgkmcnt(0)\nM v[136:139],a[96:99],a[92:95],v[136:139]\ns_mov_b32 s46,512\ns_barrier\nds_read_b64_tr_b16 a[32:33],v9 offset:0\nM v[152:155],a[104:107],a[92:95],v[152:155]\nbuffer_load_dwordx4 v21,s[32:35],s47 offen offset:0 lds\nM v[156:159],a[108:111],a[92:95],v[156:159]\n" \
"s_add_u32 m0,s22,33664\nds_read_b64_tr_b16 a[34:35],v10 offset:0\nM v[148:151],a[108:111],a[88:91],v[148:151]\nds_read_b64_tr_b16 a[36:37],v11 offset:0\nds_read_b64_tr_b16 a[38:39],v12 offset:0\nM v[144:147],a[104:107],a[88:91],v[144:147]\nds_read_b64_tr_b16 a[40:41],v9 offset:512\nM v[160:163],a[112:115],a[88:91],v[160:163]\n" \
"buffer_load_dwordx4 v21,s[32:35],s47 offen offset:128 lds\nM v[164:167],a[116:119],a[88:91],v[164:167]\ns_add_u32 m0,s22,34816\nds_read_b64_tr_b16 a[42:43],v10 offset:512\nM v[172:175],a[116:119],a[92:95],v[172:175]\nds_read_b64_tr_b16 a[44:45],v11 offset:512\nM v[168:171],a[112:115],a[92:95],v[168:171]\nds_read_b64_tr_b16 a[46:47],v12 offset:512\n" \
"M v[184:187],a[120:123],a[92:95],v[184:187]\nbuffer_load_dwordx4 v21,s[92:95],s47 offen offset:0 lds\nM v[188:191],a[124:127],a[92:95],v[188:191]\ns_add_u32 m0,s22,35712\nds_read_b64_tr_b16 a[48:49],v9 offset:1024\nM v[180:183],a[124:127],a[88:91],v[180:183]\nds_read_b64_tr_b16 a[50:51],v10 offset:1024\nM v[176:179],a[120:123],a[88:91],v[176:179]\n" \
"ds_read_b64_tr_b16 a[52:53],v11 offset:1024\nds_read_b64_tr_b16 a[54:55],v12 offset:1024\nM v[64:67],a[96:99],a[80:83],v[64:67]\nbuffer_load_dwordx4 v21,s[92:95],s47 offen offset:128 lds\nM v[68:71],a[100:103],a[80:83],v[68:71]\ns_add_u32 m0,s22,49152\nM v[76:79],a[100:103],a[84:87],v[76:79]\ns_mov_b32 s47,1835008\n" \
"ds_read_b64_tr_b16 a[56:57],v9 offset:1536\nds_read_b64_tr_b16 a[58:59],v10 offset:1536\nM v[72:75],a[96:99],a[84:87],v[72:75]\nds_read_b64_tr_b16 a[60:61],v11 offset:1536\nM v[88:91],a[104:107],a[84:87],v[88:91]\nbuffer_load_dwordx4 v21,s[32:35],s47 offen offset:0 lds\nM v[92:95],a[108:111],a[84:87],v[92:95]\ns_add_u32 m0,s22,50048\n" \
"ds_read_b64_tr_b16 a[62:63],v12 offset:1536\nM v[84:87],a[108:111],a[80:83],v[84:87]\nds_read_b128 a[24:27],v5 offset:45056\nM v[80:83],a[104:107],a[80:83],v[80:83]\nds_read_b128 a[28:31],v5 offset:47104\nM v[96:99],a[112:115],a[80:83],v[96:99]\nbuffer_load_dwordx4 v21,s[32:35],s47 offen offset:128 lds\nM v[100:103],a[116:119],a[80:83],v[100:103]\n" \
"s_add_u32 m0,s22,51200\nM v[108:111],a[116:119],a[84:87],v[108:111]\nds_read_b128 a[16:19],v5 offset:40960\nM v[104:107],a[112:115],a[84:87],v[104:107]\nds_read_b128 a[20:23],v5 offset:43008\nM v[120:123],a[120:123],a[84:87],v[120:123]\nbuffer_load_dwordx4 v21,s[92:95],s47 offen offset:0 lds\nM v[124:127],a[124:127],a[84:87],v[124:127]\n" \
"s_add_u32 m0,s22,52096\nds_read_b128 a[0:3],v5 offset:32768\nM v[116:119],a[124:127],a[80:83],v[116:119]\nds_read_b128 a[4:7],v5 offset:34816\nM v[112:115],a[120:123],a[80:83],v[112:115]\nds_read_b128 a[8:11],v5 offset:36864\nM v[192:195],a[96:99],a[64:67],v[192:195]\nbuffer_load_dwordx4 v21,s[92:95],s47 offen offset:128 lds\n" \
"M v[196:199],a[100:103],a[64:67],v[196:199]\ns_add_u32 m0,s50,0\nM v[204:207],a[100:103],a[68:71],v[204:207]\ns_mov_b32 s47,2097152\nds_read_b128 a[12:15],v5 offset:38912\nM v[200:203],a[96:99],a[68:71],v[200:203]\nds_read_b128 a[80:83],v6 offset:40960\nM v[216:219],a[104:107],a[68:71],v[216:219]\n" \
"buffer_load_dwordx4 v13,s[28:31],s46 offen lds\nM v[220:223],a[108:111],a[68:71],v[220:223]\ns_add_u32 m0,s50,4096\nds_read_b128 a[84:87],v6 offset:43008\nM v[212:215],a[108:111],a[64:67],v[212:215]\nds_read_b128 a[88:91],v6 offset:45056\nM v[208:211],a[104:107],a[64:67],v[208:211]\nds_read_b128 a[92:95],v6 offset:47104\n" \
"M v[224:227],a[112:115],a[64:67],v[224:227]\nbuffer_load_dwordx4 v14,s[28:31],s46 offen lds\nM v[228:231],a[116:119],a[64:67],v[228:231]\ns_add_u32 m0,s50,8192\nM v[236:239],a[116:119],a[68:71],v[236:239]\nM v[232:235],a[112:115],a[68:71],v[232:235]\nM v[248:251],a[120:123],a[68:71],v[248:251]\nM v[252:255],a[124:127],a[68:71],v[252:255]\n" \
"buffer_load_dwordx4 v15,s[28:31],s46 offen lds\nM v[244:247],a[124:127],a[64:67],v[244:247]\ns_add_u32 m0,s50,12288\nM v[240:243],a[120:123],a[64:67],v[240:243]\nds_read_b128 a[64:67],v6 offset:32768\nds_read_b128 a[68:71],v6 offset:34816\nM a[128:131],a[96:99],a[72:75],a[128:131]\nM a[132:135],a[100:103],a[72:75],a[132:135]\n" \
"buffer_load_dwordx4 v16,s[28:31],s46 offen lds\nM a[140:143],a[100:103],a[76:79],a[140:143]\ns_add_u32 m0,s50,16384\nM a[136:139],a[96:99],a[76:79],a[136:139]\ns_mov_b32 s46,512\nM a[152:155],a[104:107],a[76:79],a[152:155]\nM a[156:159],a[108:111],a[76:79],a[156:159]\nM a[148:151],a[108:111],a[72:75],a[148:151]\n" \
"s_waitcnt vmcnt(20) lgkmcnt(14)\nM a[144:147],a[104:107],a[72:75],a[144:147]\ns_barrier\nds_read_b64_tr_b16 a[96:97],v9 offset:16384\nM a[160:163],a[112:115],a[72:75],a[160:163]\nbuffer_load_dwordx4 v17,s[28:31],s46 offen lds\nM a[164:167],a[116:119],a[72:75],a[164:167]\ns_add_u32 m0,s50,20480\n" \
"ds_read_b64_tr_b16 a[98:99],v10 offset:16384\nM a[172:175],a[116:119],a[76:79],a[172:175]\nds_read_b64_tr_b16 a[100:101],v11 offset:16384\nds_read_b64_tr_b16 a[102:103],v12 offset:16384\nM a[168:171],a[112:115],a[76:79],a[168:171]\nds_read_b64_tr_b16 a[104:105],v9 offset:16896\nM a[184:187],a[120:123],a[76:79],a[184:187]\nbuffer_load_dwordx4 v18,s[28:31],s46 offen lds\n" \
"M a[188:191],a[124:127],a[76:79],a[188:191]\ns_add_u32 m0,s50,24576\nds_read_b64_tr_b16 a[106:107],v10 offset:16896\nM a[180:183],a[124:127],a[72:75],a[180:183]\nds_read_b64_tr_b16 a[108:109],v11 offset:16896\nM a[176:179],a[120:123],a[72:75],a[176:179]\ns_waitcnt lgkmcnt(5)\nds_read_b128 a[72:75],v6 offset:36864\n" \
"M v[128:131],a[32:35],a[24:27],v[128:131]\nbuffer_load_dwordx4 v19,s[28:31],s46 offen lds\nM v[132:135],a[36:39],a[24:27],v[132:135]\ns_add_u32 m0,s50,28672\nds_read_b128 a[76:79],v6 offset:38912\nM v[140:143],a[36:39],a[28:31],v[140:143]\nds_read_b64_tr_b16 a[110:111],v12 offset:16896\nM v[136:139],a[32:35],a[28:31],v[136:139]\n" \
"ds_read_b64_tr_b16 a[112:113],v9 offset:17408\nM v[152:155],a[40:43],a[28:31],v[152:155]\nbuffer_load_dwordx4 v20,s[28:31],s46 offen lds\nM v[156:159],a[44:47],a[28:31],v[156:159]\ns_add_u32 m0,s22,0\nds_read_b64_tr_b16 a[114:115],v10 offset:17408\nM v[148:151],a[44:47],a[24:27],v[148:151]\ns_mov_b32 s46,640\n" \
"ds_read_b64_tr_b16 a[116:117],v11 offset:17408\nM v[144:147],a[40:43],a[24:27],v[144:147]\nds_read_b64_tr_b16 a[118:119],v12 offset:17408\nM v[160:163],a[48:51],a[24:27],v[160:163]\nbuffer_load_dwordx4 v21,s[32:35],s47 offen offset:0 lds\nM v[164:167],a[52:55],a[24:27],v[164:167]\ns_add_u32 m0,s22,896\nds_read_b64_tr_b16 a[120:121],v9 offset:17920\n" \
"M v[172:175],a[52:55],a[28:31],v[172:175]\nds_read_b64_tr_b16 a[122:123],v10 offset:17920\nM v[168:171],a[48:51],a[28:31],v[168:171]\nds_read_b64_tr_b16 a[124:125],v11 offset:17920\nds_read_b64_tr_b16 a[126:127],v12 offset:17920\nM v[184:187],a[56:59],a[28:31],v[184:187]\nbuffer_load_dwordx4 v21,s[32:35],s47 offen offset:128 lds\nM v[188:191],a[60:63],a[28:31],v[188:191]\n" \
"s_add_u32 m0,s22,2048\nM v[180:183],a[60:63],a[24:27],v[180:183]\nM v[176:179],a[56:59],a[24:27],v[176:179]\nM v[64:67],a[32:35],a[16:19],v[64:67]\nM v[68:71],a[36:39],a[16:19],v[68:71]\nbuffer_load_dwordx4 v21,s[92:95],s47 offen offset:0 lds\nM v[76:79],a[36:39],a[20:23],v[76:79]\ns_add_u32 m0,s22,2944\n" \
"M v[72:75],a[32:35],a[20:23],v[72:75]\nM v[88:91],a[40:43],a[20:23],v[88:91]\nM v[92:95],a[44:47],a[20:23],v[92:95]\nbuffer_load_dwordx4 v21,s[92:95],s47 offen offset:128 lds\nM v[84:87],a[44:47],a[16:19],v[84:87]\ns_add_u32 m0,s22,16384\nM v[80:83],a[40:43],a[16:19],v[80:83]\ns_mov_b32 s47,2359296\n" \
"M v[96:99],a[48:51],a[16:19],v[96:99]\nM v[100:103],a[52:55],a[16:19],v[100:103]\nglobal_load_dwordx4 v[38:41],v26,s[64:65] offset:2048\nM v[108:111],a[52:55],a[20:23],v[108:111]\nM v[104:107],a[48:51],a[20:23],v[104:107]\nM v[120:123],a[56:59],a[20:23],v[120:123]\nM v[124:127],a[60:63],a[20:23],v[124:127]\nglobal_load_dwordx4 v[42:45],v26,s[64:65] offset:3072\n" \
"M v[116:119],a[60:63],a[16:19],v[116:119]\nM v[112:115],a[56:59],a[16:19],v[112:115]\nM v[192:195],a[32:35],a[0:3],v[192:195]\nM v[196:199],a[36:39],a[0:3],v[196:199]\ns_mov_b64 exec,0xff\nglobal_load_dword v0,v1,s[84:85]\ns_mov_b64 exec,-1\nM v[204:207],a[36:39],a[4:7],v[204:207]\n" \
"s_add_u32 s84,s84,524288\ns_addc_u32 s85,s85,0\nM v[200:203],a[32:35],a[4:7],v[200:203]\nglobal_load_dwordx4 a[230:233],v27,s[70:71] offset:0\nM v[216:219],a[40:43],a[4:7],v[216:219]\nM v[220:223],a[44:47],a[4:7],v[220:223]\nM v[212:215],a[44:47],a[0:3],v[212:215]\nM v[208:211],a[40:43],a[0:3],v[208:211]\n" \
"global_load_dwordx4 a[234:237],v27,s[70:71] offset:16\nM v[224:227],a[48:51],a[0:3],v[224:227]\ns_mov_b32 s70,4096\nM v[228:231],a[52:55],a[0:3],v[228:231]\ns_mov_b32 s71,0x00020000\nM v[236:239],a[52:55],a[4:7],v[236:239]\nM v[232:235],a[48:51],a[4:7],v[232:235]\nbuffer_load_dwordx2 a[192:193],v54,s[40:43],0 offen offset:0 nt\n" \
"M v[248:251],a[56:59],a[4:7],v[248:251]\nM v[252:255],a[60:63],a[4:7],v[252:255]\nM v[244:247],a[60:63],a[0:3],v[244:247]\nM v[240:243],a[56:59],a[0:3],v[240:243]\nbuffer_load_dwordx2 a[194:195],v54,s[40:43],0 offen offset:2048 nt\nM a[128:131],a[32:35],a[8:11],a[128:131]\nM a[132:135],a[36:39],a[8:11],a[132:135]\nM a[140:143],a[36:39],a[12:15],a[140:143]\n" \
"M a[136:139],a[32:35],a[12:15],a[136:139]\nbuffer_load_dwordx2 a[196:197],v55,s[40:43],0 offen offset:0 nt\nM a[152:155],a[40:43],a[12:15],a[152:155]\nM a[156:159],a[44:47],a[12:15],a[156:159]\nM a[148:151],a[44:47],a[8:11],a[148:151]\ns_waitcnt vmcnt(24) lgkmcnt(0)\nM a[144:147],a[40:43],a[8:11],a[144:147]\ns_barrier\n" \
"ds_read_b64_tr_b16 a[32:33],v9 offset:32768\nM a[160:163],a[48:51],a[8:11],a[160:163]\nbuffer_load_dwordx2 a[198:199],v59,s[40:43],s52 offen offset:0 nt\nM a[164:167],a[52:55],a[8:11],a[164:167]\nds_read_b64_tr_b16 a[34:35],v10 offset:32768\nds_read_b64_tr_b16 a[36:37],v11 offset:32768\nM a[172:175],a[52:55],a[12:15],a[172:175]\nds_read_b64_tr_b16 a[38:39],v12 offset:32768\n" \
"M a[168:171],a[48:51],a[12:15],a[168:171]\nds_read_b64_tr_b16 a[40:41],v9 offset:33280\nds_read_b64_tr_b16 a[42:43],v10 offset:33280\nM a[184:187],a[56:59],a[12:15],a[184:187]\nbuffer_load_dwordx2 a[214:215],v59,s[80:83],s52 offen offset:0 nt\nM a[188:191],a[60:63],a[12:15],a[188:191]\nds_read_b64_tr_b16 a[44:45],v11 offset:33280\nds_read_b64_tr_b16 a[46:47],v12 offset:33280\n" \
"M a[180:183],a[60:63],a[8:11],a[180:183]\nds_read_b64_tr_b16 a[48:49],v9 offset:33792\nM a[176:179],a[56:59],a[8:11],a[176:179]\nds_read_b64_tr_b16 a[50:51],v10 offset:33792\nds_read_b64_tr_b16 a[52:53],v11 offset:33792\nM v[128:131],a[96:99],a[88:91],v[128:131]\nbuffer_load_dwordx2 a[200:201],v59,s[40:43],s52 offen offset:2048 nt\nM v[132:135],a[100:103],a[88:91],v[132:135]\n" \
"ds_read_b64_tr_b16 a[54:55],v12 offset:33792\nds_read_b64_tr_b16 a[56:57],v9 offset:34304\nM v[140:143],a[100:103],a[92:95],v[140:143]\nds_read_b64_tr_b16 a[58:59],v10 offset:34304\nM v[136:139],a[96:99],a[92:95],v[136:139]\nds_read_b64_tr_b16 a[60:61],v11 offset:34304\nds_read_b64_tr_b16 a[62:63],v12 offset:34304\nM v[152:155],a[104:107],a[92:95],v[152:155]\n" \
"buffer_load_dwordx2 a[216:217],v59,s[80:83],s52 offen offset:2048 nt\nM v[156:159],a[108:111],a[92:95],v[156:159]\ns_mov_b32 s52,4096\nds_read_b128 a[24:27],v7 offset:12288\nds_read_b128 a[28:31],v7 offset:14336\nM v[148:151],a[108:111],a[88:91],v[148:151]\nds_read_b128 a[16:19],v7 offset:8192\nM v[144:147],a[104:107],a[88:91],v[144:147]\n" \
"ds_read_b128 a[20:23],v7 offset:10240\nM v[160:163],a[112:115],a[88:91],v[160:163]\nbuffer_load_dwordx2 a[202:203],v59,s[40:43],s52 offen offset:0 nt\nM v[164:167],a[116:119],a[88:91],v[164:167]\nds_read_b128 a[0:3],v7 offset:0\nds_read_b128 a[4:7],v7 offset:2048\nM v[172:175],a[116:119],a[92:95],v[172:175]\nds_read_b128 a[8:11],v7 offset:4096\n" \
"M v[168:171],a[112:115],a[92:95],v[168:171]\nds_read_b128 a[12:15],v7 offset:6144\nM v[184:187],a[120:123],a[92:95],v[184:187]\nbuffer_load_dwordx2 a[218:219],v59,s[80:83],s52 offen offset:0 nt\nM v[188:191],a[124:127],a[92:95],v[188:191]\nM v[180:183],a[124:127],a[88:91],v[180:183]\nM v[176:179],a[120:123],a[88:91],v[176:179]\nds_read_b128 a[88:91],v8 offset:12288\n" \
"ds_read_b128 a[92:95],v8 offset:14336\nM v[64:67],a[96:99],a[80:83],v[64:67]\nbuffer_load_dwordx2 a[204:205],v59,s[40:43],s52 offen offset:2048 nt\nM v[68:71],a[100:103],a[80:83],v[68:71]\nM v[76:79],a[100:103],a[84:87],v[76:79]\nM v[72:75],a[96:99],a[84:87],v[72:75]\nM v[88:91],a[104:107],a[84:87],v[88:91]\nbuffer_load_dwordx2 a[220:221],v59,s[80:83],s52 offen offset:2048 nt\n" \
"M v[92:95],a[108:111],a[84:87],v[92:95]\ns_mov_b32 s52,8192\nM v[84:87],a[108:111],a[80:83],v[84:87]\nM v[80:83],a[104:107],a[80:83],v[80:83]\nM v[96:99],a[112:115],a[80:83],v[96:99]\nM v[100:103],a[116:119],a[80:83],v[100:103]\nM v[108:111],a[116:119],a[84:87],v[108:111]\nM v[104:107],a[112:115],a[84:87],v[104:107]\n" \
"M v[120:123],a[120:123],a[84:87],v[120:123]\nM v[124:127],a[124:127],a[84:87],v[124:127]\nM v[116:119],a[124:127],a[80:83],v[116:119]\nM v[112:115],a[120:123],a[80:83],v[112:115]\nds_read_b128 a[80:83],v8 offset:8192\nds_read_b128 a[84:87],v8 offset:10240\nM v[192:195],a[96:99],a[64:67],v[192:195]\nM v[196:199],a[100:103],a[64:67],v[196:199]\n" \
"M v[204:207],a[100:103],a[68:71],v[204:207]\nM v[200:203],a[96:99],a[68:71],v[200:203]\nM v[216:219],a[104:107],a[68:71],v[216:219]\nM v[220:223],a[108:111],a[68:71],v[220:223]\nM v[212:215],a[108:111],a[64:67],v[212:215]\nM v[208:211],a[104:107],a[64:67],v[208:211]\nM v[224:227],a[112:115],a[64:67],v[224:227]\nM v[228:231],a[116:119],a[64:67],v[228:231]\n" \
"M v[236:239],a[116:119],a[68:71],v[236:239]\nM v[232:235],a[112:115],a[68:71],v[232:235]\nM v[248:251],a[120:123],a[68:71],v[248:251]\nM v[252:255],a[124:127],a[68:71],v[252:255]\nM v[244:247],a[124:127],a[64:67],v[244:247]\nM v[240:243],a[120:123],a[64:67],v[240:243]\nds_read_b128 a[64:67],v8 offset:0\nds_read_b128 a[68:71],v8 offset:2048\n" \
"M a[128:131],a[96:99],a[72:75],a[128:131]\nM a[132:135],a[100:103],a[72:75],a[132:135]\nM a[140:143],a[100:103],a[76:79],a[140:143]\nM a[136:139],a[96:99],a[76:79],a[136:139]\nM a[152:155],a[104:107],a[76:79],a[152:155]\nM a[156:159],a[108:111],a[76:79],a[156:159]\nM a[148:151],a[108:111],a[72:75],a[148:151]\ns_waitcnt vmcnt(28) lgkmcnt(14)\n" \
"M a[144:147],a[104:107],a[72:75],a[144:147]\ns_barrier\nds_read_b64_tr_b16 a[96:97],v9 offset:49152\nM a[160:163],a[112:115],a[72:75],a[160:163]\nds_read_b64_tr_b16 a[98:99],v10 offset:49152\nds_read_b64_tr_b16 a[100:101],v11 offset:49152\nM a[164:167],a[116:119],a[72:75],a[164:167]\nds_read_b64_tr_b16 a[102:103],v12 offset:49152\n" \
"M a[172:175],a[116:119],a[76:79],a[172:175]\nds_read_b64_tr_b16 a[104:105],v9 offset:49664\nds_read_b64_tr_b16 a[106:107],v10 offset:49664\nM a[168:171],a[112:115],a[76:79],a[168:171]\nds_read_b64_tr_b16 a[108:109],v11 offset:49664\nM a[184:187],a[120:123],a[76:79],a[184:187]\nds_read_b64_tr_b16 a[110:111],v12 offset:49664\nM a[188:191],a[124:127],a[76:79],a[188:191]\n" \
"ds_read_b64_tr_b16 a[112:113],v9 offset:50176\nds_read_b64_tr_b16 a[114:115],v10 offset:50176\nM a[180:183],a[124:127],a[72:75],a[180:183]\nds_read_b64_tr_b16 a[116:117],v11 offset:50176\nM a[176:179],a[120:123],a[72:75],a[176:179]\ns_waitcnt lgkmcnt(7)\nds_read_b128 a[72:75],v8 offset:4096\nM v[128:131],a[32:35],a[24:27],v[128:131]\n" \
"ds_read_b128 a[76:79],v8 offset:6144\nM v[132:135],a[36:39],a[24:27],v[132:135]\nbuffer_load_dwordx4 v21,s[32:35],s47 offen offset:0 lds\nM v[140:143],a[36:39],a[28:31],v[140:143]\ns_add_u32 m0,s22,17280\nds_read_b64_tr_b16 a[118:119],v12 offset:50176\nM v[136:139],a[32:35],a[28:31],v[136:139]\nds_read_b64_tr_b16 a[120:121],v9 offset:50688\n" \
"M v[152:155],a[40:43],a[28:31],v[152:155]\nds_read_b64_tr_b16 a[122:123],v10 offset:50688\nM v[156:159],a[44:47],a[28:31],v[156:159]\nbuffer_load_dwordx4 v21,s[32:35],s47 offen offset:128 lds\nM v[148:151],a[44:47],a[24:27],v[148:151]\ns_add_u32 m0,s22,18432\nds_read_b64_tr_b16 a[124:125],v11 offset:50688\nM v[144:147],a[40:43],a[24:27],v[144:147]\n" \
"ds_read_b64_tr_b16 a[126:127],v12 offset:50688\nM v[160:163],a[48:51],a[24:27],v[160:163]\nM v[164:167],a[52:55],a[24:27],v[164:167]\nbuffer_load_dwordx4 v21,s[92:95],s47 offen offset:0 lds\nM v[172:175],a[52:55],a[28:31],v[172:175]\ns_add_u32 m0,s22,19328\nM v[168:171],a[48:51],a[28:31],v[168:171]\nM v[184:187],a[56:59],a[28:31],v[184:187]\n" \
"M v[188:191],a[60:63],a[28:31],v[188:191]\nbuffer_load_dwordx4 v21,s[92:95],s47 offen offset:128 lds\nM v[180:183],a[60:63],a[24:27],v[180:183]\ns_add_u32 m0,s50,32768\nM v[176:179],a[56:59],a[24:27],v[176:179]\ns_mov_b32 s47,2621440\nM v[64:67],a[32:35],a[16:19],v[64:67]\nM v[68:71],a[36:39],a[16:19],v[68:71]\n" \
"buffer_load_dwordx4 v13,s[28:31],s46 offen lds\nM v[76:79],a[36:39],a[20:23],v[76:79]\ns_add_u32 m0,s50,36864\nM v[72:75],a[32:35],a[20:23],v[72:75]\nM v[88:91],a[40:43],a[20:23],v[88:91]\nM v[92:95],a[44:47],a[20:23],v[92:95]\nbuffer_load_dwordx4 v14,s[28:31],s46 offen lds\nM v[84:87],a[44:47],a[16:19],v[84:87]\n" \
"s_add_u32 m0,s50,40960\nM v[80:83],a[40:43],a[16:19],v[80:83]\nM v[96:99],a[48:51],a[16:19],v[96:99]\nM v[100:103],a[52:55],a[16:19],v[100:103]\nbuffer_load_dwordx4 v15,s[28:31],s46 offen lds\nM v[108:111],a[52:55],a[20:23],v[108:111]\nLG1_%=:\ns_cbranch_execz LAV_%=\n" \
"s_add_u32 m0,s50,45056\nM v[104:107],a[48:51],a[20:23],v[104:107]\nM v[120:123],a[56:59],a[20:23],v[120:123]\nM v[124:127],a[60:63],a[20:23],v[124:127]\nbuffer_load_dwordx4 v16,s[28:31],s46 offen lds\nM v[116:119],a[60:63],a[16:19],v[116:119]\ns_add_u32 m0,s50,49152\nM v[112:115],a[56:59],a[16:19],v[112:115]\n" \
"s_mov_b32 s46,640\nM v[192:195],a[32:35],a[0:3],v[192:195]\nM v[196:199],a[36:39],a[0:3],v[196:199]\nbuffer_load_dwordx4 v17,s[28:31],s46 offen lds\nM v[204:207],a[36:39],a[4:7],v[204:207]\ns_add_u32 m0,s50,53248\nM v[200:203],a[32:35],a[4:7],v[200:203]\nM v[216:219],a[40:43],a[4:7],v[216:219]\n" \
"M v[220:223],a[44:47],a[4:7],v[220:223]\nbuffer_load_dwordx4 v18,s[28:31],s46 offen lds\nM v[212:215],a[44:47],a[0:3],v[212:215]\ns_add_u32 m0,s50,57344\nM v[208:211],a[40:43],a[0:3],v[208:211]\nM v[224:227],a[48:51],a[0:3],v[224:227]\nM v[228:231],a[52:55],a[0:3],v[228:231]\nbuffer_load_dwordx4 v19,s[28:31],s46 offen lds\n" \
"M v[236:239],a[52:55],a[4:7],v[236:239]\ns_add_u32 m0,s50,61440\nM v[232:235],a[48:51],a[4:7],v[232:235]\nM v[248:251],a[56:59],a[4:7],v[248:251]\nM v[252:255],a[60:63],a[4:7],v[252:255]\nbuffer_load_dwordx4 v20,s[28:31],s46 offen lds\nM v[244:247],a[60:63],a[0:3],v[244:247]\ns_add_u32 m0,s22,32768\n" \
"M v[240:243],a[56:59],a[0:3],v[240:243]\ns_mov_b32 s46,768\nM a[128:131],a[32:35],a[8:11],a[128:131]\nM a[132:135],a[36:39],a[8:11],a[132:135]\nbuffer_load_dwordx4 v21,s[32:35],s47 offen offset:0 lds\nM a[140:143],a[36:39],a[12:15],a[140:143]\ns_add_u32 m0,s22,33664\nM a[136:139],a[32:35],a[12:15],a[136:139]\n" \
"M a[152:155],a[40:43],a[12:15],a[152:155]\nM a[156:159],a[44:47],a[12:15],a[156:159]\nbuffer_load_dwordx4 v21,s[32:35],s47 offen offset:128 lds\nM a[148:151],a[44:47],a[8:11],a[148:151]\ns_waitcnt vmcnt(30) lgkmcnt(0)\nM a[144:147],a[40:43],a[8:11],a[144:147]\ns_add_u32 m0,s22,34816\ns_barrier\n" \
"ds_read_b64_tr_b16 a[32:33],v9 offset:0\nM a[160:163],a[48:51],a[8:11],a[160:163]\nds_read_b64_tr_b16 a[34:35],v10 offset:0\nds_read_b64_tr_b16 a[36:37],v11 offset:0\nM a[164:167],a[52:55],a[8:11],a[164:167]\nbuffer_load_dwordx4 v21,s[92:95],s47 offen offset:0 lds\nM a[172:175],a[52:55],a[12:15],a[172:175]\ns_add_u32 m0,s22,35712\n" \
"ds_read_b64_tr_b16 a[38:39],v12 offset:0\nM a[168:171],a[48:51],a[12:15],a[168:171]\nds_read_b64_tr_b16 a[40:41],v9 offset:512\nM a[184:187],a[56:59],a[12:15],a[184:187]\nds_read_b64_tr_b16 a[42:43],v10 offset:512\nM a[188:191],a[60:63],a[12:15],a[188:191]\nbuffer_load_dwordx4 v21,s[92:95],s47 offen offset:128 lds\nM a[180:183],a[60:63],a[8:11],a[180:183]\n" \
"s_add_u32 m0,s22,49152\nds_read_b64_tr_b16 a[44:45],v11 offset:512\nM a[176:179],a[56:59],a[8:11],a[176:179]\ns_mov_b32 s47,2883584\nds_read_b64_tr_b16 a[46:47],v12 offset:512\nM v[128:131],a[96:99],a[88:91],v[128:131]\nds_read_b64_tr_b16 a[48:49],v9 offset:1024\nds_read_b64_tr_b16 a[50:51],v10 offset:1024\n" \
"M v[132:135],a[100:103],a[88:91],v[132:135]\nbuffer_load_dwordx4 v21,s[32:35],s47 offen offset:0 lds\nM v[140:143],a[100:103],a[92:95],v[140:143]\ns_add_u32 m0,s22,50048\nM v[136:139],a[96:99],a[92:95],v[136:139]\nds_read_b64_tr_b16 a[52:53],v11 offset:1024\nds_read_b64_tr_b16 a[54:55],v12 offset:1024\nM v[152:155],a[104:107],a[92:95],v[152:155]\n" \
"ds_read_b64_tr_b16 a[56:57],v9 offset:1536\nM v[156:159],a[108:111],a[92:95],v[156:159]\nbuffer_load_dwordx4 v21,s[32:35],s47 offen offset:128 lds\nM v[148:151],a[108:111],a[88:91],v[148:151]\ns_add_u32 m0,s22,51200\nds_read_b64_tr_b16 a[58:59],v10 offset:1536\nM v[144:147],a[104:107],a[88:91],v[144:147]\ns_waitcnt vmcnt(32)\n" \
"ds_read_b64_tr_b16 a[60:61],v11 offset:1536\nv_lshlrev_b32 v25,16,v46\nM v[160:163],a[112:115],a[88:91],v[160:163]\nds_read_b64_tr_b16 a[62:63],v12 offset:1536\nv_lshlrev_b32 v26,16,v30\nM v[164:167],a[116:119],a[88:91],v[164:167]\nbuffer_load_dwordx4 v21,s[92:95],s47 offen offset:0 lds\nM v[172:175],a[116:119],a[92:95],v[172:175]\n" \
"s_add_u32 m0,s22,52096\nds_read_b128 a[24:27],v5 offset:12288\nv_and_b32 v46,0xffff0000,v46\nM v[168:171],a[112:115],a[92:95],v[168:171]\nds_read_b128 a[28:31],v5 offset:14336\nv_mul_f32 v23,v26,v25\nM v[184:187],a[120:123],a[92:95],v[184:187]\nds_read_b128 a[16:19],v5 offset:8192\n" \
"v_and_b32 v30,0xffff0000,v30\nM v[188:191],a[124:127],a[92:95],v[188:191]\nds_read_b128 a[20:23],v5 offset:10240\nv_lshlrev_b32 v26,16,v38\nM v[180:183],a[124:127],a[88:91],v[180:183]\nbuffer_load_dwordx4 v21,s[92:95],s47 offen offset:128 lds\nM v[176:179],a[120:123],a[88:91],v[176:179]\ns_add_u32 m0,s50,65536\n" \
"v_fmac_f32 v23,v30,v46\nv_mul_f32 v24,v26,v25\nM v[64:67],a[96:99],a[80:83],v[64:67]\ns_mov_b32 s47,3145728\nds_read_b128 a[0:3],v5 offset:0\nv_lshlrev_b32 v25,16,v47\nM v[68:71],a[100:103],a[80:83],v[68:71]\nds_read_b128 a[4:7],v5 offset:2048\n" \
"v_lshlrev_b32 v26,16,v31\nM v[76:79],a[100:103],a[84:87],v[76:79]\nbuffer_load_dwordx4 v13,s[28:31],s46 offen lds\nM v[72:75],a[96:99],a[84:87],v[72:75]\ns_add_u32 m0,s50,69632\nds_read_b128 a[8:11],v5 offset:4096\nv_and_b32 v38,0xffff0000,v38\nM v[88:91],a[104:107],a[84:87],v[88:91]\n" \
"ds_read_b128 a[12:15],v5 offset:6144\nv_fmac_f32 v23,v26,v25\nM v[92:95],a[108:111],a[84:87],v[92:95]\nds_read_b128 a[88:91],v6 offset:12288\nv_fmac_f32 v24,v38,v46\nM v[84:87],a[108:111],a[80:83],v[84:87]\nds_read_b128 a[92:95],v6 offset:14336\nv_and_b32 v47,0xffff0000,v47\n" \
"M v[80:83],a[104:107],a[80:83],v[80:83]\nbuffer_load_dwordx4 v14,s[28:31],s46 offen lds\nM v[96:99],a[112:115],a[80:83],v[96:99]\ns_add_u32 m0,s50,73728\nv_and_b32 v31,0xffff0000,v31\nv_lshlrev_b32 v26,16,v39\nM v[100:103],a[116:119],a[80:83],v[100:103]\nv_fmac_f32 v23,v31,v47\n" \
"v_fmac_f32 v24,v26,v25\nM v[108:111],a[116:119],a[84:87],v[108:111]\nv_lshlrev_b32 v25,16,v48\nv_lshlrev_b32 v26,16,v32\nM v[104:107],a[112:115],a[84:87],v[104:107]\nbuffer_load_dwordx4 v15,s[28:31],s46 offen lds\nM v[120:123],a[120:123],a[84:87],v[120:123]\ns_add_u32 m0,s50,77824\n" \
"v_and_b32 v39,0xffff0000,v39\nv_fmac_f32 v23,v26,v25\nM v[124:127],a[124:127],a[84:87],v[124:127]\nv_fmac_f32 v24,v39,v47\nv_and_b32 v48,0xffff0000,v48\nM v[116:119],a[124:127],a[80:83],v[116:119]\nv_and_b32 v32,0xffff0000,v32\nv_lshlrev_b32 v26,16,v40\n" \
"M v[112:115],a[120:123],a[80:83],v[112:115]\nds_read_b128 a[80:83],v6 offset:8192\nds_read_b128 a[84:87],v6 offset:10240\nM v[192:195],a[96:99],a[64:67],v[192:195]\nbuffer_load_dwordx4 v16,s[28:31],s46 offen lds\nM v[196:199],a[100:103],a[64:67],v[196:199]\ns_add_u32 m0,s50,81920\nv_fmac_f32 v23,v32,v48\n" \
"v_fmac_f32 v24,v26,v25\nM v[204:207],a[100:103],a[68:71],v[204:207]\ns_mov_b32 s46,768\nv_lshlrev_b32 v25,16,v49\nv_lshlrev_b32 v26,16,v33\nM v[200:203],a[96:99],a[68:71],v[200:203]\nv_and_b32 v40,0xffff0000,v40\nv_fmac_f32 v23,v26,v25\n" \
"M v[216:219],a[104:107],a[68:71],v[216:219]\nbuffer_load_dwordx4 v17,s[28:31],s46 offen lds\nM v[220:223],a[108:111],a[68:71],v[220:223]\ns_add_u32 m0,s50,86016\nv_fmac_f32 v24,v40,v48\nv_and_b32 v49,0xffff0000,v49\nM v[212:215],a[108:111],a[64:67],v[212:215]\nv_and_b32 v33,0xffff0000,v33\n" \
"v_lshlrev_b32 v26,16,v41\nM v[208:211],a[104:107],a[64:67],v[208:211]\nv_fmac_f32 v23,v33,v49\nv_fmac_f32 v24,v26,v25\nM v[224:227],a[112:115],a[64:67],v[224:227]\nbuffer_load_dwordx2 a[206:207],v59,s[40:43],s52 offen offset:0 nt\nM v[228:231],a[116:119],a[64:67],v[228:231]\nv_lshlrev_b32 v25,16,v50\n" \
"v_lshlrev_b32 v26,16,v34\nM v[236:239],a[116:119],a[68:71],v[236:239]\nv_and_b32 v41,0xffff0000,v41\nv_fmac_f32 v23,v26,v25\nM v[232:235],a[112:115],a[68:71],v[232:235]\nv_fmac_f32 v24,v41,v49\nv_and_b32 v50,0xffff0000,v50\nM v[248:251],a[120:123],a[68:71],v[248:251]\n" \
"buffer_load_dwordx2 a[222:223],v59,s[80:83],s52 offen offset:0 nt\nM v[252:255],a[124:127],a[68:71],v[252:255]\nv_and_b32 v34,0xffff0000,v34\nv_lshlrev_b32 v26,16,v42\nM v[244:247],a[124:127],a[64:67],v[244:247]\nv_fmac_f32 v23,v34,v50\nv_fmac_f32 v24,v26,v25\nM v[240:243],a[120:123],a[64:67],v[240:243]\n" \
"ds_read_b128 a[64:67],v6 offset:0\nds_read_b128 a[68:71],v6 offset:2048\nM a[128:131],a[96:99],a[72:75],a[128:131]\nbuffer_load_dwordx2 a[208:209],v59,s[40:43],s52 offen offset:2048 nt\nM a[132:135],a[100:103],a[72:75],a[132:135]\nv_lshlrev_b32 v25,16,v51\nv_lshlrev_b32 v26,16,v35\nM a[140:143],a[100:103],a[76:79],a[140:143]\n" \
"v_and_b32 v42,0xffff0000,v42\nv_fmac_f32 v23,v26,v25\nM a[136:139],a[96:99],a[76:79],a[136:139]\nv_fmac_f32 v24,v42,v50\nv_and_b32 v51,0xffff0000,v51\nM a[152:155],a[104:107],a[76:79],a[152:155]\nbuffer_load_dwordx2 a[224:225],v59,s[80:83],s52 offen offset:2048 nt\nM a[156:159],a[108:111],a[76:79],a[156:159]\n" \
"s_mov_b32 s52,12288\nv_and_b32 v35,0xffff0000,v35\nv_lshlrev_b32 v26,16,v43\nM a[148:151],a[108:111],a[72:75],a[148:151]\ns_waitcnt vmcnt(25) lgkmcnt(14)\nv_fmac_f32 v23,v35,v51\nv_fmac_f32 v24,v26,v25\nM a[144:147],a[104:107],a[72:75],a[144:147]\n" \
"s_barrier\nds_read_b64_tr_b16 a[96:97],v9 offset:16384\nM a[160:163],a[112:115],a[72:75],a[160:163]\nbuffer_load_dwordx2 a[210:211],v59,s[40:43],s52 offen offset:0 nt\nM a[164:167],a[116:119],a[72:75],a[164:167]\nds_read_b64_tr_b16 a[98:99],v10 offset:16384\nds_read_b64_tr_b16 a[100:101],v11 offset:16384\nM a[172:175],a[116:119],a[76:79],a[172:175]\n" \
"ds_read_b64_tr_b16 a[102:103],v12 offset:16384\nv_lshlrev_b32 v25,16,v52\nM a[168:171],a[112:115],a[76:79],a[168:171]\nds_read_b64_tr_b16 a[104:105],v9 offset:16896\nds_read_b64_tr_b16 a[106:107],v10 offset:16896\nM a[184:187],a[120:123],a[76:79],a[184:187]\nbuffer_load_dwordx2 a[226:227],v59,s[80:83],s52 offen offset:0 nt\nM a[188:191],a[124:127],a[76:79],a[188:191]\n" \
"ds_read_b64_tr_b16 a[108:109],v11 offset:16896\nds_read_b64_tr_b16 a[110:111],v12 offset:16896\nM a[180:183],a[124:127],a[72:75],a[180:183]\ns_waitcnt vmcnt(31)\nds_read_b64_tr_b16 a[112:113],v9 offset:17408\nv_lshlrev_b32 v26,16,v36\nM a[176:179],a[120:123],a[72:75],a[176:179]\ns_waitcnt lgkmcnt(6)\n" \
"ds_read_b128 a[72:75],v6 offset:4096\nv_and_b32 v43,0xffff0000,v43\nM v[128:131],a[32:35],a[24:27],v[128:131]\nbuffer_load_dwordx2 a[212:213],v59,s[40:43],s52 offen offset:2048 nt\nM v[132:135],a[36:39],a[24:27],v[132:135]\nds_read_b128 a[76:79],v6 offset:6144\nds_read_b64_tr_b16 a[114:115],v10 offset:17408\nM v[140:143],a[36:39],a[28:31],v[140:143]\n" \
"ds_read_b64_tr_b16 a[116:117],v11 offset:17408\nv_fmac_f32 v23,v26,v25\nM v[136:139],a[32:35],a[28:31],v[136:139]\nds_read_b64_tr_b16 a[118:119],v12 offset:17408\nv_fmac_f32 v24,v43,v51\nM v[152:155],a[40:43],a[28:31],v[152:155]\nbuffer_load_dwordx2 a[228:229],v59,s[80:83],s52 offen offset:2048 nt\nM v[156:159],a[44:47],a[28:31],v[156:159]\n" \
"ds_read_b64_tr_b16 a[120:121],v9 offset:17920\nds_read_b64_tr_b16 a[122:123],v10 offset:17920\nM v[148:151],a[44:47],a[24:27],v[148:151]\nds_read_b64_tr_b16 a[124:125],v11 offset:17920\nv_and_b32 v52,0xffff0000,v52\nM v[144:147],a[40:43],a[24:27],v[144:147]\nds_read_b64_tr_b16 a[126:127],v12 offset:17920\nv_and_b32 v36,0xffff0000,v36\n" \
"M v[160:163],a[48:51],a[24:27],v[160:163]\ns_mov_b64 exec,0xff\nglobal_load_dword v0,v1,s[84:85]\ns_mov_b64 exec,-1\nM v[164:167],a[52:55],a[24:27],v[164:167]\ns_add_u32 s84,s84,524288\ns_addc_u32 s85,s85,0\nv_lshlrev_b32 v26,16,v44\n" \
"v_fmac_f32 v23,v36,v52\nM v[172:175],a[52:55],a[28:31],v[172:175]\ns_mov_b64 exec,0xff\nglobal_load_dword v0,v1,s[84:85]\ns_mov_b64 exec,-1\nM v[168:171],a[48:51],a[28:31],v[168:171]\nv_fmac_f32 v24,v26,v25\nv_lshlrev_b32 v25,16,v53\n" \
"M v[184:187],a[56:59],a[28:31],v[184:187]\ns_mov_b64 exec,0xff\nglobal_load_dword v0,v1,s[76:77]\ns_mov_b64 exec,-1\nM v[188:191],a[60:63],a[28:31],v[188:191]\nv_lshlrev_b32 v26,16,v37\nv_and_b32 v44,0xffff0000,v44\nM v[180:183],a[60:63],a[24:27],v[180:183]\n" \
"s_mov_b64 exec,0xff00\nglobal_load_dword v0,v1,s[76:77]\ns_mov_b64 exec,-1\nM v[176:179],a[56:59],a[24:27],v[176:179]\ns_add_u32 s76,s76,2097152\ns_addc_u32 s77,s77,0\nv_fmac_f32 v23,v26,v25\nv_fmac_f32 v24,v44,v52\n" \
"M v[64:67],a[32:35],a[16:19],v[64:67]\nv_lshlrev_b32 v26,16,v45\nv_and_b32 v53,0xffff0000,v53\nM v[68:71],a[36:39],a[16:19],v[68:71]\nv_and_b32 v45,0xffff0000,v45\nv_fmac_f32 v24,v26,v25\nM v[76:79],a[36:39],a[20:23],v[76:79]\nv_fmac_f32 v24,v45,v53\n" \
"v_and_b32 v37,0xffff0000,v37\nM v[72:75],a[32:35],a[20:23],v[72:75]\nv_accvgpr_read_b32 v45,a231\nv_fmac_f32 v23,v37,v53\nM v[88:91],a[40:43],a[20:23],v[88:91]\nv_accvgpr_read_b32 v49,a233\nv_lshlrev_b32 v44,16,v45\nM v[92:95],a[44:47],a[20:23],v[92:95]\n" \
"v_accvgpr_read_b32 v36,a192\nv_accvgpr_read_b32 v43,a230\nM v[84:87],a[44:47],a[16:19],v[84:87]\nv_accvgpr_read_b32 v37,a194\nv_lshlrev_b32 v48,16,v49\nM v[80:83],a[40:43],a[16:19],v[80:83]\nv_lshlrev_b32 v30,16,v36\nv_and_b32 v45,0xffff0000,v45\n" \
"M v[96:99],a[48:51],a[16:19],v[96:99]\nv_accvgpr_read_b32 v47,a232\nv_lshlrev_b32 v31,16,v37\nM v[100:103],a[52:55],a[16:19],v[100:103]\nv_accvgpr_read_b32 v38,a196\nv_lshlrev_b32 v42,16,v43\nM v[108:111],a[52:55],a[20:23],v[108:111]\nv_and_b32 v49,0xffff0000,v49\n" \
"v_and_b32 v36,0xffff0000,v36\nM v[104:107],a[48:51],a[20:23],v[104:107]\nv_accvgpr_read_b32 v39,a198\nv_mul_f32 v25,v30,v45\nM v[120:123],a[56:59],a[20:23],v[120:123]\nv_lshlrev_b32 v46,16,v47\nv_and_b32 v37,0xffff0000,v37\nM v[124:127],a[60:63],a[20:23],v[124:127]\n" \
"v_lshlrev_b32 v32,16,v38\nv_and_b32 v43,0xffff0000,v43\nM v[116:119],a[60:63],a[16:19],v[116:119]\nv_mul_f32 v26,v36,v49\nv_lshlrev_b32 v33,16,v39\nM v[112:115],a[56:59],a[16:19],v[112:115]\nv_fmac_f32 v25,v31,v44\nv_and_b32 v47,0xffff0000,v47\n" \
"M v[192:195],a[32:35],a[0:3],v[192:195]\nv_and_b32 v38,0xffff0000,v38\nv_fmac_f32 v26,v37,v48\nM v[196:199],a[36:39],a[0:3],v[196:199]\nv_and_b32 v39,0xffff0000,v39\nv_fmac_f32 v25,v32,v43\nM v[204:207],a[36:39],a[4:7],v[204:207]\nv_fmac_f32 v26,v38,v47\n" \
"v_fmac_f32 v25,v33,v42\nM v[200:203],a[32:35],a[4:7],v[200:203]\nv_fmac_f32 v26,v39,v46\nv_accvgpr_read_b32 v28,a214\nM v[216:219],a[40:43],a[4:7],v[216:219]\nC v26,v25,v26\nv_lshlrev_b32 v27,16,v28\nM v[220:223],a[44:47],a[4:7],v[220:223]\n" \
"v_lshlrev_b32 v25,16,v26\nv_and_b32 v28,0xffff0000,v28\nM v[212:215],a[44:47],a[0:3],v[212:215]\nv_and_b32 v26,0xffff0000,v26\nv_mul_f32 v29,v25,v27\nM v[208:211],a[40:43],a[0:3],v[208:211]\nv_mul_f32 v58,v26,v28\nv_accvgpr_read_b32 v40,a200\n" \
"M v[224:227],a[48:51],a[0:3],v[224:227]\nv_mul_f32 v25,v31,v45\nv_mul_f32 v26,v37,v49\nM v[228:231],a[52:55],a[0:3],v[228:231]\nv_lshlrev_b32 v34,16,v40\nv_fmac_f32 v25,v32,v44\nM v[236:239],a[52:55],a[4:7],v[236:239]\nv_fmac_f32 v26,v38,v48\n" \
"v_fmac_f32 v25,v33,v43\nM v[232:235],a[48:51],a[4:7],v[232:235]\nv_and_b32 v40,0xffff0000,v40\nv_fmac_f32 v26,v39,v47\nM v[248:251],a[56:59],a[4:7],v[248:251]\nv_fmac_f32 v25,v34,v42\nv_fmac_f32 v26,v40,v46\nM v[252:255],a[60:63],a[4:7],v[252:255]\n" \
"v_accvgpr_read_b32 v28,a216\nC v26,v25,v26\nM v[244:247],a[60:63],a[0:3],v[244:247]\nv_lshlrev_b32 v27,16,v28\nv_lshlrev_b32 v25,16,v26\nM v[240:243],a[56:59],a[0:3],v[240:243]\nv_and_b32 v28,0xffff0000,v28\nv_and_b32 v26,0xffff0000,v26\n" \
"M a[128:131],a[32:35],a[8:11],a[128:131]\nv_mul_f32 v25,v25,v27\nv_mul_f32 v26,v26,v28\nM a[132:135],a[36:39],a[8:11],a[132:135]\nC v53,v25,v29\nC v57,v26,v58\nM a[140:143],a[36:39],a[12:15],a[140:143]\nv_accvgpr_read_b32 v41,a202\n" \
"v_lshlrev_b32 v35,16,v41\nM a[136:139],a[32:35],a[12:15],a[136:139]\nv_mul_f32 v25,v32,v45\nv_mul_f32 v26,v38,v49\nM a[152:155],a[40:43],a[12:15],a[152:155]\nv_fmac_f32 v25,v33,v44\nv_and_b32 v41,0xffff0000,v41\nM a[156:159],a[44:47],a[12:15],a[156:159]\n" \
"v_fmac_f32 v26,v39,v48\nv_fmac_f32 v25,v34,v43\nM a[148:151],a[44:47],a[8:11],a[148:151]\ns_waitcnt vmcnt(21) lgkmcnt(0)\nv_fmac_f32 v26,v40,v47\nv_fmac_f32 v25,v35,v42\nM a[144:147],a[40:43],a[8:11],a[144:147]\ns_barrier\n" \
"ds_read_b64_tr_b16 a[32:33],v9 offset:32768\nM a[160:163],a[48:51],a[8:11],a[160:163]\nbuffer_load_dwordx4 v18,s[28:31],s46 offen lds\nM a[164:167],a[52:55],a[8:11],a[164:167]\ns_add_u32 m0,s50,90112\nds_read_b64_tr_b16 a[34:35],v10 offset:32768\nv_fmac_f32 v26,v41,v46\nM a[172:175],a[52:55],a[12:15],a[172:175]\n" \
"ds_read_b64_tr_b16 a[36:37],v11 offset:32768\nds_read_b64_tr_b16 a[38:39],v12 offset:32768\nM a[168:171],a[48:51],a[12:15],a[168:171]\nds_read_b64_tr_b16 a[40:41],v9 offset:33280\nv_accvgpr_read_b32 v28,a218\nM a[184:187],a[56:59],a[12:15],a[184:187]\nbuffer_load_dwordx4 v19,s[28:31],s46 offen lds\nM a[188:191],a[60:63],a[12:15],a[188:191]\n" \
"s_add_u32 m0,s50,94208\nds_read_b64_tr_b16 a[42:43],v10 offset:33280\nC v26,v25,v26\nM a[180:183],a[60:63],a[8:11],a[180:183]\nds_read_b64_tr_b16 a[44:45],v11 offset:33280\nv_lshlrev_b32 v27,16,v28\nM a[176:179],a[56:59],a[8:11],a[176:179]\nds_read_b64_tr_b16 a[46:47],v12 offset:33280\n" \
"v_lshlrev_b32 v25,16,v26\nM v[128:131],a[96:99],a[88:91],v[128:131]\nbuffer_load_dwordx4 v20,s[28:31],s46 offen lds\nM v[132:135],a[100:103],a[88:91],v[132:135]\ns_add_u32 m0,s22,0\nds_read_b64_tr_b16 a[48:49],v9 offset:33792\nv_and_b32 v28,0xffff0000,v28\nM v[140:143],a[100:103],a[92:95],v[140:143]\n" \
"s_mov_b32 s46,896\nds_read_b64_tr_b16 a[50:51],v10 offset:33792\nv_and_b32 v26,0xffff0000,v26\nM v[136:139],a[96:99],a[92:95],v[136:139]\nds_read_b64_tr_b16 a[52:53],v11 offset:33792\nds_read_b64_tr_b16 a[54:55],v12 offset:33792\nM v[152:155],a[104:107],a[92:95],v[152:155]\nbuffer_load_dwordx4 v21,s[32:35],s47 offen offset:0 lds\n" \
"M v[156:159],a[108:111],a[92:95],v[156:159]\ns_add_u32 m0,s22,896\nv_mul_f32 v29,v25,v27\nv_mul_f32 v58,v26,v28\nM v[148:151],a[108:111],a[88:91],v[148:151]\nds_read_b64_tr_b16 a[56:57],v9 offset:34304\nds_read_b64_tr_b16 a[58:59],v10 offset:34304\nM v[144:147],a[104:107],a[88:91],v[144:147]\n" \
"ds_read_b64_tr_b16 a[60:61],v11 offset:34304\nv_accvgpr_read_b32 v36,a204\nM v[160:163],a[112:115],a[88:91],v[160:163]\nbuffer_load_dwordx4 v21,s[32:35],s47 offen offset:128 lds\nM v[164:167],a[116:119],a[88:91],v[164:167]\ns_add_u32 m0,s22,2048\nds_read_b64_tr_b16 a[62:63],v12 offset:34304\nv_mul_f32 v25,v33,v45\n" \
"M v[172:175],a[116:119],a[92:95],v[172:175]\nds_read_b128 a[24:27],v5 offset:45056\nv_lshlrev_b32 v30,16,v36\nM v[168:171],a[112:115],a[92:95],v[168:171]\nds_read_b128 a[28:31],v5 offset:47104\nv_mul_f32 v26,v39,v49\nM v[184:187],a[120:123],a[92:95],v[184:187]\nbuffer_load_dwordx4 v21,s[92:95],s47 offen offset:0 lds\n" \
"M v[188:191],a[124:127],a[92:95],v[188:191]\ns_add_u32 m0,s22,2944\nv_fmac_f32 v25,v34,v44\nv_fmac_f32 v26,v40,v48\nM v[180:183],a[124:127],a[88:91],v[180:183]\nds_read_b128 a[16:19],v5 offset:40960\nv_and_b32 v36,0xffff0000,v36\nM v[176:179],a[120:123],a[88:91],v[176:179]\n" \
"ds_read_b128 a[20:23],v5 offset:43008\nv_fmac_f32 v25,v35,v43\nM v[64:67],a[96:99],a[80:83],v[64:67]\nbuffer_load_dwordx4 v21,s[92:95],s47 offen offset:128 lds\nM v[68:71],a[100:103],a[80:83],v[68:71]\ns_add_u32 m0,s22,16384\nds_read_b128 a[0:3],v5 offset:32768\nv_fmac_f32 v26,v41,v47\n" \
"M v[76:79],a[100:103],a[84:87],v[76:79]\ns_mov_b32 s47,3407872\nds_read_b128 a[4:7],v5 offset:34816\nv_fmac_f32 v25,v30,v42\nM v[72:75],a[96:99],a[84:87],v[72:75]\nds_read_b128 a[8:11],v5 offset:36864\nv_fmac_f32 v26,v36,v46\nM v[88:91],a[104:107],a[84:87],v[88:91]\n" \
"buffer_load_dwordx4 v21,s[32:35],s47 offen offset:0 lds\nM v[92:95],a[108:111],a[84:87],v[92:95]\ns_add_u32 m0,s22,17280\nv_accvgpr_read_b32 v28,a220\nC v26,v25,v26\nM v[84:87],a[108:111],a[80:83],v[84:87]\nds_read_b128 a[12:15],v5 offset:38912\nv_lshlrev_b32 v27,16,v28\n" \
"M v[80:83],a[104:107],a[80:83],v[80:83]\nds_read_b128 a[88:91],v6 offset:45056\nv_lshlrev_b32 v25,16,v26\nM v[96:99],a[112:115],a[80:83],v[96:99]\nbuffer_load_dwordx4 v21,s[32:35],s47 offen offset:128 lds\nM v[100:103],a[116:119],a[80:83],v[100:103]\ns_add_u32 m0,s22,18432\nds_read_b128 a[92:95],v6 offset:47104\n" \
"v_and_b32 v28,0xffff0000,v28\nM v[108:111],a[116:119],a[84:87],v[108:111]\nv_and_b32 v26,0xffff0000,v26\nv_mul_f32 v25,v25,v27\nM v[104:107],a[112:115],a[84:87],v[104:107]\nv_mul_f32 v26,v26,v28\nC v52,v25,v29\nM v[120:123],a[120:123],a[84:87],v[120:123]\n" \
"buffer_load_dwordx4 v21,s[92:95],s47 offen offset:0 lds\nM v[124:127],a[124:127],a[84:87],v[124:127]\ns_add_u32 m0,s22,19328\nC v56,v26,v58\nv_mov_b32 v59,s19\nM v[116:119],a[124:127],a[80:83],v[116:119]\nv_permlane32_swap_b32 v23,v24\nM v[112:115],a[120:123],a[80:83],v[112:115]\n" \
"ds_read_b128 a[80:83],v6 offset:40960\nds_read_b128 a[84:87],v6 offset:43008\nM v[192:195],a[96:99],a[64:67],v[192:195]\nbuffer_load_dwordx4 v21,s[92:95],s47 offen offset:128 lds\nM v[196:199],a[100:103],a[64:67],v[196:199]\ns_add_u32 m0,s50,0\nv_add_f32 v23,v23,v24\nv_mov_b32 v24,v23\n" \
"M v[204:207],a[100:103],a[68:71],v[204:207]\ns_mov_b32 s47,3670016\nv_permlane16_swap_b32 v23,v24\nM v[200:203],a[96:99],a[68:71],v[200:203]\nv_add_f32 v23,v23,v24\ns_nop 1\nv_add_f32 v23,v23,v23 row_ror:8 row_mask:0xf bank_mask:0xf\nM v[216:219],a[104:107],a[68:71],v[216:219]\n" \
"buffer_load_dwordx4 v13,s[28:31],s46 offen lds\nM v[220:223],a[108:111],a[68:71],v[220:223]\ns_add_u32 m0,s50,4096\nv_add_u32 v13,s60,v63\nv_add_f32 v23,v23,v23 row_ror:4 row_mask:0xf bank_mask:0xf\nM v[212:215],a[108:111],a[64:67],v[212:215]\ns_nop 0\nv_add_f32 v23,v23,v23 row_ror:2 row_mask:0xf bank_mask:0xf\n" \
"s_nop 1\nv_add_f32 v23,v23,v23 row_ror:1 row_mask:0xf bank_mask:0xf\nM v[208:211],a[104:107],a[64:67],v[208:211]\nC v23,v23,v23\nv_and_b32 v23,0xffff0000,v23\nM v[224:227],a[112:115],a[64:67],v[224:227]\nLG2_%=:\ns_cbranch_execz LHR0_%=\n" \
"buffer_load_dwordx4 v14,s[28:31],s46 offen lds\nM v[228:231],a[116:119],a[64:67],v[228:231]\ns_add_u32 m0,s50,8192\nv_add_u32 v14,0x10000,v13\nv_mul_f32 v23,0xbfb8aa3b,v23\nM v[236:239],a[116:119],a[68:71],v[236:239]\nv_exp_f32 v23,v23\nM v[232:235],a[112:115],a[68:71],v[232:235]\n" \
"v_add_f32 v23,1.0,v23\nv_add_u32 v24,992,v4\nM v[248:251],a[120:123],a[68:71],v[248:251]\nbuffer_load_dwordx4 v15,s[28:31],s46 offen lds\nM v[252:255],a[124:127],a[68:71],v[252:255]\ns_add_u32 m0,s50,12288\nv_add_u32 v15,0x10000,v14\nM v[244:247],a[124:127],a[64:67],v[244:247]\n" \
"v_rcp_f32 v23,v23\nM v[240:243],a[120:123],a[64:67],v[240:243]\nds_read_b128 a[64:67],v6 offset:32768\nds_read_b128 a[68:71],v6 offset:34816\nM a[128:131],a[96:99],a[72:75],a[128:131]\nbuffer_load_dwordx4 v16,s[28:31],s46 offen lds\nM a[132:135],a[100:103],a[72:75],a[132:135]\ns_add_u32 m0,s50,16384\n" \
"v_add_u32 v16,0x10000,v15\nC v23,v23,v23\nM a[140:143],a[100:103],a[76:79],a[140:143]\ns_mov_b32 s46,896\nv_and_b32 v23,0xffff0000,v23\nM a[136:139],a[96:99],a[76:79],a[136:139]\nM a[152:155],a[104:107],a[76:79],a[152:155]\nbuffer_load_dwordx4 v17,s[28:31],s46 offen lds\n" \
"M a[156:159],a[108:111],a[76:79],a[156:159]\ns_add_u32 m0,s50,20480\nv_add_u32 v17,s61,v63\nv_mov_b32 v63,0x3f803f80\nM a[148:151],a[108:111],a[72:75],a[148:151]\ns_waitcnt vmcnt(33) lgkmcnt(14)\nM a[144:147],a[104:107],a[72:75],a[144:147]\ns_barrier\n" \
"ds_read_b64_tr_b16 a[96:97],v9 offset:49152\nM a[160:163],a[112:115],a[72:75],a[160:163]\nbuffer_store_dword v23,v61,s[36:39],0 offen sc1\nM a[164:167],a[116:119],a[72:75],a[164:167]\nds_read_b64_tr_b16 a[98:99],v10 offset:49152\nds_read_b64_tr_b16 a[100:101],v11 offset:49152\nM a[172:175],a[116:119],a[76:79],a[172:175]\nbuffer_load_dwordx4 v18,s[28:31],s46 offen lds\n" \
"M a[168:171],a[112:115],a[76:79],a[168:171]\ns_add_u32 m0,s50,24576\nds_read_b64_tr_b16 a[102:103],v12 offset:49152\nv_add_u32 v18,0x10000,v17\nM a[184:187],a[120:123],a[76:79],a[184:187]\nds_read_b64_tr_b16 a[104:105],v9 offset:49664\nv_add_u32 v23,-32,v4\nM a[188:191],a[124:127],a[76:79],a[188:191]\n" \
"ds_read_b64_tr_b16 a[106:107],v10 offset:49664\nM a[180:183],a[124:127],a[72:75],a[180:183]\ns_waitcnt lgkmcnt(5)\nds_read_b64_tr_b16 a[108:109],v11 offset:49664\nds_read_b64_tr_b16 a[110:111],v12 offset:49664\nM a[176:179],a[120:123],a[72:75],a[176:179]\nbuffer_load_dwordx4 v19,s[28:31],s46 offen lds\nM v[128:131],a[32:35],a[24:27],v[128:131]\n" \
"s_add_u32 m0,s50,28672\nv_add_u32 v19,0x10000,v18\nM v[132:135],a[36:39],a[24:27],v[132:135]\nds_read_b128 a[72:75],v6 offset:36864\nM v[140:143],a[36:39],a[28:31],v[140:143]\nds_read_b128 a[76:79],v6 offset:38912\nM v[136:139],a[32:35],a[28:31],v[136:139]\nbuffer_load_dwordx4 v20,s[28:31],s46 offen lds\n" \
"M v[152:155],a[40:43],a[28:31],v[152:155]\ns_add_u32 m0,s22,32768\nds_read_b64_tr_b16 a[112:113],v9 offset:50176\nv_add_u32 v20,0x10000,v19\nM v[156:159],a[44:47],a[28:31],v[156:159]\ns_mov_b64 s[28:29],s[64:65]\nds_read_b64_tr_b16 a[114:115],v10 offset:50176\nM v[148:151],a[44:47],a[24:27],v[148:151]\n" \
"s_and_b32 s29,s29,0xffff\nds_read_b64_tr_b16 a[116:117],v11 offset:50176\nds_read_b64_tr_b16 a[118:119],v12 offset:50176\nM v[144:147],a[40:43],a[24:27],v[144:147]\ns_mov_b32 s30,4194304\nds_read_b64_tr_b16 a[120:121],v9 offset:50688\nM v[160:163],a[48:51],a[24:27],v[160:163]\nbuffer_load_dwordx4 v21,s[32:35],s47 offen offset:0 lds\n" \
"M v[164:167],a[52:55],a[24:27],v[164:167]\ns_add_u32 m0,s22,33664\nds_read_b64_tr_b16 a[122:123],v10 offset:50688\nM v[172:175],a[52:55],a[28:31],v[172:175]\ns_mov_b32 s46,0\nds_read_b64_tr_b16 a[124:125],v11 offset:50688\nM v[168:171],a[48:51],a[28:31],v[168:171]\nds_read_b64_tr_b16 a[126:127],v12 offset:50688\n" \
"M v[184:187],a[56:59],a[28:31],v[184:187]\nbuffer_load_dwordx4 v21,s[32:35],s47 offen offset:128 lds\nM v[188:191],a[60:63],a[28:31],v[188:191]\ns_add_u32 m0,s22,34816\nM v[180:183],a[60:63],a[24:27],v[180:183]\nM v[176:179],a[56:59],a[24:27],v[176:179]\nM v[64:67],a[32:35],a[16:19],v[64:67]\nM v[68:71],a[36:39],a[16:19],v[68:71]\n" \
"buffer_load_dwordx4 v21,s[92:95],s47 offen offset:0 lds\nM v[76:79],a[36:39],a[20:23],v[76:79]\ns_add_u32 m0,s22,35712\nM v[72:75],a[32:35],a[20:23],v[72:75]\nM v[88:91],a[40:43],a[20:23],v[88:91]\nM v[92:95],a[44:47],a[20:23],v[92:95]\nbuffer_load_dwordx4 v21,s[92:95],s47 offen offset:128 lds\nM v[84:87],a[44:47],a[16:19],v[84:87]\n" \
"s_add_u32 m0,s22,49152\nM v[80:83],a[40:43],a[16:19],v[80:83]\ns_mov_b32 s47,3932160\nM v[96:99],a[48:51],a[16:19],v[96:99]\nM v[100:103],a[52:55],a[16:19],v[100:103]\nM v[108:111],a[52:55],a[20:23],v[108:111]\nM v[104:107],a[48:51],a[20:23],v[104:107]\nM v[120:123],a[56:59],a[20:23],v[120:123]\n" \
"M v[124:127],a[60:63],a[20:23],v[124:127]\nM v[116:119],a[60:63],a[16:19],v[116:119]\nM v[112:115],a[56:59],a[16:19],v[112:115]\nM v[192:195],a[32:35],a[0:3],v[192:195]\ns_mov_b64 exec,0xff\nglobal_load_dword v0,v2,s[88:89]\ns_mov_b64 exec,-1\nM v[196:199],a[36:39],a[0:3],v[196:199]\n" \
"M v[204:207],a[36:39],a[4:7],v[204:207]\ns_mov_b64 exec,0xff00\nglobal_load_dword v0,v2,s[88:89]\ns_mov_b64 exec,-1\nM v[200:203],a[32:35],a[4:7],v[200:203]\ns_add_u32 s88,s88,2048\ns_addc_u32 s89,s89,0\nM v[216:219],a[40:43],a[4:7],v[216:219]\n" \
"s_mov_b64 exec,0xff\nglobal_load_dword v0,v2,s[88:89]\ns_mov_b64 exec,-1\nM v[220:223],a[44:47],a[4:7],v[220:223]\nM v[212:215],a[44:47],a[0:3],v[212:215]\ns_mov_b64 exec,0xff00\nglobal_load_dword v0,v2,s[88:89]\ns_mov_b64 exec,-1\n" \
"M v[208:211],a[40:43],a[0:3],v[208:211]\nM v[224:227],a[48:51],a[0:3],v[224:227]\ns_mov_b64 exec,0xff\nglobal_load_dword v0,v1,s[76:77]\ns_mov_b64 exec,-1\nM v[228:231],a[52:55],a[0:3],v[228:231]\nM v[236:239],a[52:55],a[4:7],v[236:239]\ns_mov_b64 exec,0xff00\n" \
"global_load_dword v0,v1,s[76:77]\ns_mov_b64 exec,-1\nM v[232:235],a[48:51],a[4:7],v[232:235]\ns_add_u32 s76,s76,2097152\ns_addc_u32 s77,s77,0\nM v[248:251],a[56:59],a[4:7],v[248:251]\nM v[252:255],a[60:63],a[4:7],v[252:255]\nM v[244:247],a[60:63],a[0:3],v[244:247]\n" \
"s_waitcnt vmcnt(34)\nv_accvgpr_read_b32 v37,a206\nv_mul_f32 v25,v34,v45\nM v[240:243],a[56:59],a[0:3],v[240:243]\ns_mov_b32 s98,79691776\nv_mul_f32 v26,v40,v49\nv_lshlrev_b32 v31,16,v37\nM a[128:131],a[32:35],a[8:11],a[128:131]\n" \
"s_mov_b32 s99,79695872\nv_fmac_f32 v25,v35,v44\nv_fmac_f32 v26,v41,v48\nM a[132:135],a[36:39],a[8:11],a[132:135]\nv_and_b32 v37,0xffff0000,v37\nv_fmac_f32 v25,v30,v43\nM a[140:143],a[36:39],a[12:15],a[140:143]\nv_fmac_f32 v26,v36,v47\n" \
"v_fmac_f32 v25,v31,v42\nM a[136:139],a[32:35],a[12:15],a[136:139]\nv_fmac_f32 v26,v37,v46\nv_accvgpr_read_b32 v28,a222\nM a[152:155],a[40:43],a[12:15],a[152:155]\nC v26,v25,v26\nv_lshlrev_b32 v27,16,v28\nM a[156:159],a[44:47],a[12:15],a[156:159]\n" \
"v_lshlrev_b32 v25,16,v26\nv_and_b32 v28,0xffff0000,v28\nM a[148:151],a[44:47],a[8:11],a[148:151]\ns_waitcnt vmcnt(23) lgkmcnt(0)\nv_and_b32 v26,0xffff0000,v26\nv_mul_f32 v29,v25,v27\nM a[144:147],a[40:43],a[8:11],a[144:147]\ns_barrier\n" \
"v_mul_f32 v58,v26,v28\nM a[160:163],a[48:51],a[8:11],a[160:163]\nv_accvgpr_read_b32 v38,a208\nv_mul_f32 v25,v35,v45\nM a[164:167],a[52:55],a[8:11],a[164:167]\nv_lshlrev_b32 v32,16,v38\nv_mul_f32 v26,v41,v49\nM a[172:175],a[52:55],a[12:15],a[172:175]\n" \
"v_fmac_f32 v25,v30,v44\nv_and_b32 v38,0xffff0000,v38\nM a[168:171],a[48:51],a[12:15],a[168:171]\nv_fmac_f32 v26,v36,v48\nv_fmac_f32 v25,v31,v43\nM a[184:187],a[56:59],a[12:15],a[184:187]\nds_read_b64_tr_b16 a[32:33],v9 offset:0\nds_read_b64_tr_b16 a[34:35],v10 offset:0\n" \
"M a[188:191],a[60:63],a[12:15],a[188:191]\nds_read_b64_tr_b16 a[36:37],v11 offset:0\nv_fmac_f32 v26,v37,v47\nM a[180:183],a[60:63],a[8:11],a[180:183]\nds_read_b64_tr_b16 a[38:39],v12 offset:0\nds_read_b64_tr_b16 a[40:41],v9 offset:512\nM a[176:179],a[56:59],a[8:11],a[176:179]\nds_read_b64_tr_b16 a[42:43],v10 offset:512\n" \
"v_fmac_f32 v25,v32,v42\nM v[128:131],a[96:99],a[88:91],v[128:131]\nv_fmac_f32 v26,v38,v46\nv_accvgpr_read_b32 v28,a224\nM v[132:135],a[100:103],a[88:91],v[132:135]\nds_read_b64_tr_b16 a[44:45],v11 offset:512\nC v26,v25,v26\nM v[140:143],a[100:103],a[92:95],v[140:143]\n" \
"ds_read_b64_tr_b16 a[46:47],v12 offset:512\nv_lshlrev_b32 v27,16,v28\nM v[136:139],a[96:99],a[92:95],v[136:139]\nds_read_b64_tr_b16 a[48:49],v9 offset:1024\nds_read_b64_tr_b16 a[50:51],v10 offset:1024\nM v[152:155],a[104:107],a[92:95],v[152:155]\nds_read_b64_tr_b16 a[52:53],v11 offset:1024\nv_lshlrev_b32 v25,16,v26\n" \
"M v[156:159],a[108:111],a[92:95],v[156:159]\nds_read_b64_tr_b16 a[54:55],v12 offset:1024\nds_read_b64_tr_b16 a[56:57],v9 offset:1536\nM v[148:151],a[108:111],a[88:91],v[148:151]\nds_read_b64_tr_b16 a[58:59],v10 offset:1536\nv_and_b32 v28,0xffff0000,v28\nM v[144:147],a[104:107],a[88:91],v[144:147]\nds_read_b64_tr_b16 a[60:61],v11 offset:1536\n" \
"v_and_b32 v26,0xffff0000,v26\nM v[160:163],a[112:115],a[88:91],v[160:163]\nds_read_b64_tr_b16 a[62:63],v12 offset:1536\nv_mul_f32 v25,v25,v27\nM v[164:167],a[116:119],a[88:91],v[164:167]\nds_read_b128 a[24:27],v7 offset:12288\nv_mul_f32 v26,v26,v28\nM v[172:175],a[116:119],a[92:95],v[172:175]\n" \
"ds_read_b128 a[28:31],v7 offset:14336\nC v51,v25,v29\nM v[168:171],a[112:115],a[92:95],v[168:171]\nC v55,v26,v58\nv_accvgpr_read_b32 v39,a210\nM v[184:187],a[120:123],a[92:95],v[184:187]\nv_mul_f32 v25,v30,v45\nv_mul_f32 v26,v36,v49\n" \
"M v[188:191],a[124:127],a[92:95],v[188:191]\nv_lshlrev_b32 v33,16,v39\nv_fmac_f32 v25,v31,v44\nM v[180:183],a[124:127],a[88:91],v[180:183]\nv_fmac_f32 v26,v37,v48\nv_and_b32 v39,0xffff0000,v39\nM v[176:179],a[120:123],a[88:91],v[176:179]\nv_fmac_f32 v25,v32,v43\n" \
"v_fmac_f32 v26,v38,v47\nM v[64:67],a[96:99],a[80:83],v[64:67]\nv_fmac_f32 v25,v33,v42\nv_fmac_f32 v26,v39,v46\nM v[68:71],a[100:103],a[80:83],v[68:71]\nv_accvgpr_read_b32 v28,a226\nC v26,v25,v26\nM v[76:79],a[100:103],a[84:87],v[76:79]\n" \
"v_lshlrev_b32 v27,16,v28\nv_lshlrev_b32 v25,16,v26\nM v[72:75],a[96:99],a[84:87],v[72:75]\nv_and_b32 v28,0xffff0000,v28\nv_and_b32 v26,0xffff0000,v26\nM v[88:91],a[104:107],a[84:87],v[88:91]\nv_mul_f32 v29,v25,v27\nv_mul_f32 v58,v26,v28\n" \
"M v[92:95],a[108:111],a[84:87],v[92:95]\nv_accvgpr_read_b32 v40,a212\nv_lshlrev_b32 v34,16,v40\nM v[84:87],a[108:111],a[80:83],v[84:87]\nv_mul_f32 v25,v31,v45\nv_mul_f32 v26,v37,v49\nM v[80:83],a[104:107],a[80:83],v[80:83]\nds_read_b128 a[16:19],v7 offset:8192\n" \
"v_fmac_f32 v25,v32,v44\nM v[96:99],a[112:115],a[80:83],v[96:99]\nds_read_b128 a[20:23],v7 offset:10240\nv_and_b32 v40,0xffff0000,v40\nM v[100:103],a[116:119],a[80:83],v[100:103]\nv_fmac_f32 v26,v38,v48\nv_fmac_f32 v25,v33,v43\nM v[108:111],a[116:119],a[84:87],v[108:111]\n" \
"v_fmac_f32 v26,v39,v47\nv_fmac_f32 v25,v34,v42\nM v[104:107],a[112:115],a[84:87],v[104:107]\nv_fmac_f32 v26,v40,v46\nv_accvgpr_read_b32 v28,a228\nM v[120:123],a[120:123],a[84:87],v[120:123]\nC v26,v25,v26\nv_lshlrev_b32 v27,16,v28\n" \
"M v[124:127],a[124:127],a[84:87],v[124:127]\nv_lshlrev_b32 v25,16,v26\nv_mul_f32 v25,v25,v27\nM v[116:119],a[124:127],a[80:83],v[116:119]\nC v50,v25,v29\nv_accvgpr_read_b32 v45,a235\nM v[112:115],a[120:123],a[80:83],v[112:115]\nv_and_b32 v28,0xffff0000,v28\n" \
"v_and_b32 v26,0xffff0000,v26\nM v[192:195],a[96:99],a[64:67],v[192:195]\nv_lshlrev_b32 v44,16,v45\nv_accvgpr_read_b32 v49,a237\nM v[196:199],a[100:103],a[64:67],v[196:199]\nds_read_b128 a[0:3],v7 offset:0\nv_accvgpr_read_b32 v36,a193\nM v[204:207],a[100:103],a[68:71],v[204:207]\n" \
"ds_read_b128 a[4:7],v7 offset:2048\nv_accvgpr_read_b32 v37,a195\nM v[200:203],a[96:99],a[68:71],v[200:203]\nv_mul_f32 v26,v26,v28\nv_accvgpr_read_b32 v43,a234\nM v[216:219],a[104:107],a[68:71],v[216:219]\nv_accvgpr_read_b32 v47,a236\nv_lshlrev_b32 v48,16,v49\n" \
"M v[220:223],a[108:111],a[68:71],v[220:223]\nv_and_b32 v45,0xffff0000,v45\nv_lshlrev_b32 v30,16,v36\nM v[212:215],a[108:111],a[64:67],v[212:215]\nbuffer_store_dwordx4 v[50:53],v60,s[36:39],s98 offen offset:0 sc1\nM v[208:211],a[104:107],a[64:67],v[208:211]\ns_mov_b32 s98,79699968\nv_lshlrev_b32 v31,16,v37\n" \
"v_accvgpr_read_b32 v38,a197\nM v[224:227],a[112:115],a[64:67],v[224:227]\nC v54,v26,v58\nv_accvgpr_read_b32 v39,a199\nM v[228:231],a[116:119],a[64:67],v[228:231]\nv_lshlrev_b32 v42,16,v43\nv_and_b32 v49,0xffff0000,v49\nM v[236:239],a[116:119],a[68:71],v[236:239]\n" \
"v_lshlrev_b32 v46,16,v47\nv_and_b32 v36,0xffff0000,v36\nM v[232:235],a[112:115],a[68:71],v[232:235]\nv_lshlrev_b32 v32,16,v38\nv_mul_f32 v25,v30,v45\nM v[248:251],a[120:123],a[68:71],v[248:251]\nds_read_b128 a[8:11],v7 offset:4096\nds_read_b128 a[12:15],v7 offset:6144\n" \
"M v[252:255],a[124:127],a[68:71],v[252:255]\nv_and_b32 v37,0xffff0000,v37\nv_lshlrev_b32 v33,16,v39\nM v[244:247],a[124:127],a[64:67],v[244:247]\nv_and_b32 v43,0xffff0000,v43\nv_mul_f32 v26,v36,v49\nM v[240:243],a[120:123],a[64:67],v[240:243]\nds_read_b128 a[64:67],v8 offset:0\n" \
"ds_read_b128 a[68:71],v8 offset:2048\nM a[128:131],a[96:99],a[72:75],a[128:131]\nv_and_b32 v47,0xffff0000,v47\nv_and_b32 v38,0xffff0000,v38\nM a[132:135],a[100:103],a[72:75],a[132:135]\nds_read_b128 a[80:83],v8 offset:8192\nv_fmac_f32 v25,v31,v44\nM a[140:143],a[100:103],a[76:79],a[140:143]\n" \
"v_and_b32 v39,0xffff0000,v39\nv_fmac_f32 v26,v37,v48\nM a[136:139],a[96:99],a[76:79],a[136:139]\nv_fmac_f32 v25,v32,v43\nv_fmac_f32 v26,v38,v47\nM a[152:155],a[104:107],a[76:79],a[152:155]\nv_fmac_f32 v25,v33,v42\nv_fmac_f32 v26,v39,v46\n" \
"M a[156:159],a[108:111],a[76:79],a[156:159]\nds_read_b128 a[84:87],v8 offset:10240\nv_accvgpr_read_b32 v28,a215\nM a[148:151],a[108:111],a[72:75],a[148:151]\ns_waitcnt vmcnt(20) lgkmcnt(12)\nds_read_b128 a[88:91],v8 offset:12288\nds_read_b128 a[92:95],v8 offset:14336\nM a[144:147],a[104:107],a[72:75],a[144:147]\n" \
"s_barrier\nds_read_b64_tr_b16 a[96:97],v9 offset:16384\nM a[160:163],a[112:115],a[72:75],a[160:163]\nds_read_b64_tr_b16 a[98:99],v10 offset:16384\nC v26,v25,v26\nM a[164:167],a[116:119],a[72:75],a[164:167]\nds_read_b64_tr_b16 a[100:101],v11 offset:16384\nv_lshlrev_b32 v27,16,v28\n" \
"M a[172:175],a[116:119],a[76:79],a[172:175]\nds_read_b64_tr_b16 a[102:103],v12 offset:16384\nds_read_b64_tr_b16 a[104:105],v9 offset:16896\nM a[168:171],a[112:115],a[76:79],a[168:171]\nds_read_b64_tr_b16 a[106:107],v10 offset:16896\nv_lshlrev_b32 v25,16,v26\nM a[184:187],a[120:123],a[76:79],a[184:187]\nds_read_b64_tr_b16 a[108:109],v11 offset:16896\n" \
"v_and_b32 v28,0xffff0000,v28\nM a[188:191],a[124:127],a[76:79],a[188:191]\ns_waitcnt lgkmcnt(6)\nds_read_b64_tr_b16 a[110:111],v12 offset:16896\nds_read_b64_tr_b16 a[112:113],v9 offset:17408\nM a[180:183],a[124:127],a[72:75],a[180:183]\nbuffer_store_dwordx4 v[54:57],v60,s[36:39],s99 offen offset:0 sc1\nM a[176:179],a[120:123],a[72:75],a[176:179]\n" \
"s_mov_b32 s99,79704064\nds_read_b128 a[72:75],v8 offset:4096\nds_read_b128 a[76:79],v8 offset:6144\nM v[128:131],a[32:35],a[24:27],v[128:131]\nds_read_b64_tr_b16 a[114:115],v10 offset:17408\nv_and_b32 v26,0xffff0000,v26\nM v[132:135],a[36:39],a[24:27],v[132:135]\nds_read_b64_tr_b16 a[116:117],v11 offset:17408\n" \
"v_mul_f32 v29,v25,v27\nM v[140:143],a[36:39],a[28:31],v[140:143]\nds_read_b64_tr_b16 a[118:119],v12 offset:17408\nv_mul_f32 v58,v26,v28\nM v[136:139],a[32:35],a[28:31],v[136:139]\nds_read_b64_tr_b16 a[120:121],v9 offset:17920\nds_read_b64_tr_b16 a[122:123],v10 offset:17920\nM v[152:155],a[40:43],a[28:31],v[152:155]\n" \
"ds_read_b64_tr_b16 a[124:125],v11 offset:17920\nv_accvgpr_read_b32 v40,a201\nM v[156:159],a[44:47],a[28:31],v[156:159]\nds_read_b64_tr_b16 a[126:127],v12 offset:17920\nv_lshlrev_b32 v34,16,v40\nM v[148:151],a[44:47],a[24:27],v[148:151]\nv_mul_f32 v25,v31,v45\nv_mul_f32 v26,v37,v49\n" \
"M v[144:147],a[40:43],a[24:27],v[144:147]\nv_fmac_f32 v25,v32,v44\nv_and_b32 v40,0xffff0000,v40\nM v[160:163],a[48:51],a[24:27],v[160:163]\nv_fmac_f32 v26,v38,v48\nv_fmac_f32 v25,v33,v43\nM v[164:167],a[52:55],a[24:27],v[164:167]\nv_fmac_f32 v26,v39,v47\n" \
"v_fmac_f32 v25,v34,v42\nM v[172:175],a[52:55],a[28:31],v[172:175]\nv_fmac_f32 v26,v40,v46\nv_accvgpr_read_b32 v28,a217\nM v[168:171],a[48:51],a[28:31],v[168:171]\nC v26,v25,v26\nv_lshlrev_b32 v27,16,v28\nM v[184:187],a[56:59],a[28:31],v[184:187]\n" \
"v_lshlrev_b32 v25,16,v26\nv_and_b32 v28,0xffff0000,v28\nM v[188:191],a[60:63],a[28:31],v[188:191]\nv_and_b32 v26,0xffff0000,v26\nv_mul_f32 v25,v25,v27\nM v[180:183],a[60:63],a[24:27],v[180:183]\nv_mul_f32 v26,v26,v28\nC v53,v25,v29\n" \
"M v[176:179],a[56:59],a[24:27],v[176:179]\nC v57,v26,v58\nv_accvgpr_read_b32 v41,a203\nM v[64:67],a[32:35],a[16:19],v[64:67]\nv_mul_f32 v25,v32,v45\nv_lshlrev_b32 v35,16,v41\nM v[68:71],a[36:39],a[16:19],v[68:71]\nv_mul_f32 v26,v38,v49\n" \
"v_fmac_f32 v25,v33,v44\nM v[76:79],a[36:39],a[20:23],v[76:79]\nv_fmac_f32 v26,v39,v48\nv_and_b32 v41,0xffff0000,v41\nM v[72:75],a[32:35],a[20:23],v[72:75]\nv_fmac_f32 v25,v34,v43\nv_fmac_f32 v26,v40,v47\nM v[88:91],a[40:43],a[20:23],v[88:91]\n" \
"v_fmac_f32 v25,v35,v42\nv_fmac_f32 v26,v41,v46\nM v[92:95],a[44:47],a[20:23],v[92:95]\nv_accvgpr_read_b32 v28,a219\nC v26,v25,v26\nM v[84:87],a[44:47],a[16:19],v[84:87]\ns_mov_b64 exec,0xff\nglobal_load_dword v0,v2,s[100:101]\n" \
"s_mov_b64 exec,-1\nM v[80:83],a[40:43],a[16:19],v[80:83]\nv_lshlrev_b32 v27,16,v28\nv_lshlrev_b32 v25,16,v26\nM v[96:99],a[48:51],a[16:19],v[96:99]\nbuffer_load_dwordx4 v21,s[32:35],s47 offen offset:0 lds\nM v[100:103],a[52:55],a[16:19],v[100:103]\ns_add_u32 m0,s22,50048\n" \
"v_and_b32 v28,0xffff0000,v28\nv_and_b32 v26,0xffff0000,v26\nM v[108:111],a[52:55],a[20:23],v[108:111]\nv_mul_f32 v29,v25,v27\nv_mul_f32 v58,v26,v28\nM v[104:107],a[48:51],a[20:23],v[104:107]\nv_accvgpr_read_b32 v36,a205\nv_lshlrev_b32 v30,16,v36\n" \
"M v[120:123],a[56:59],a[20:23],v[120:123]\nbuffer_load_dwordx4 v21,s[32:35],s47 offen offset:128 lds\nM v[124:127],a[60:63],a[20:23],v[124:127]\ns_add_u32 m0,s22,51200\nv_mul_f32 v25,v33,v45\nv_mul_f32 v26,v39,v49\nM v[116:119],a[60:63],a[16:19],v[116:119]\ns_mov_b64 s[32:33],s[44:45]\n" \
"v_fmac_f32 v25,v34,v44\nv_and_b32 v36,0xffff0000,v36\nM v[112:115],a[56:59],a[16:19],v[112:115]\ns_mov_b32 s34,s49\nv_fmac_f32 v26,v40,v48\nv_fmac_f32 v25,v35,v43\nM v[192:195],a[32:35],a[0:3],v[192:195]\nbuffer_load_dwordx4 v21,s[92:95],s47 offen offset:0 lds\n" \
"M v[196:199],a[36:39],a[0:3],v[196:199]\ns_add_u32 m0,s22,52096\nv_fmac_f32 v26,v41,v47\nv_fmac_f32 v25,v30,v42\nM v[204:207],a[36:39],a[4:7],v[204:207]\nv_fmac_f32 v26,v36,v46\nv_accvgpr_read_b32 v28,a221\nM v[200:203],a[32:35],a[4:7],v[200:203]\n" \
"C v26,v25,v26\nv_lshlrev_b32 v27,16,v28\nM v[216:219],a[40:43],a[4:7],v[216:219]\nbuffer_load_dwordx4 v21,s[92:95],s47 offen offset:128 lds\nM v[220:223],a[44:47],a[4:7],v[220:223]\ns_add_u32 m0,s50,32768\nv_lshlrev_b32 v25,16,v26\nv_and_b32 v28,0xffff0000,v28\n" \
"M v[212:215],a[44:47],a[0:3],v[212:215]\ns_mov_b32 s47,0\nv_and_b32 v26,0xffff0000,v26\nv_mul_f32 v25,v25,v27\nM v[208:211],a[40:43],a[0:3],v[208:211]\nv_mul_f32 v26,v26,v28\nC v52,v25,v29\nM v[224:227],a[48:51],a[0:3],v[224:227]\n" \
"buffer_load_dwordx4 v13,s[28:31],s46 offen lds\nM v[228:231],a[52:55],a[0:3],v[228:231]\ns_add_u32 m0,s50,36864\nC v56,v26,v58\nv_accvgpr_read_b32 v37,a207\nM v[236:239],a[52:55],a[4:7],v[236:239]\nv_mul_f32 v25,v34,v45\nv_lshlrev_b32 v31,16,v37\n" \
"M v[232:235],a[48:51],a[4:7],v[232:235]\nv_mul_f32 v26,v40,v49\nv_fmac_f32 v25,v35,v44\nM v[248:251],a[56:59],a[4:7],v[248:251]\nbuffer_load_dwordx4 v14,s[28:31],s46 offen lds\nM v[252:255],a[60:63],a[4:7],v[252:255]\ns_add_u32 m0,s50,40960\nv_fmac_f32 v26,v41,v48\n" \
"v_and_b32 v37,0xffff0000,v37\nM v[244:247],a[60:63],a[0:3],v[244:247]\nv_fmac_f32 v25,v30,v43\nv_fmac_f32 v26,v36,v47\nM v[240:243],a[56:59],a[0:3],v[240:243]\nv_fmac_f32 v25,v31,v42\nv_fmac_f32 v26,v37,v46\nM a[128:131],a[32:35],a[8:11],a[128:131]\n" \
"buffer_load_dwordx4 v15,s[28:31],s46 offen lds\nM a[132:135],a[36:39],a[8:11],a[132:135]\ns_add_u32 m0,s50,45056\nv_accvgpr_read_b32 v28,a223\nC v26,v25,v26\nM a[140:143],a[36:39],a[12:15],a[140:143]\nv_lshlrev_b32 v27,16,v28\nv_lshlrev_b32 v25,16,v26\n" \
"M a[136:139],a[32:35],a[12:15],a[136:139]\nv_and_b32 v28,0xffff0000,v28\nv_and_b32 v26,0xffff0000,v26\nM a[152:155],a[40:43],a[12:15],a[152:155]\nbuffer_load_dwordx4 v16,s[28:31],s46 offen lds\nM a[156:159],a[44:47],a[12:15],a[156:159]\ns_add_u32 m0,s50,49152\nv_mul_f32 v29,v25,v27\n" \
"v_mul_f32 v58,v26,v28\nM a[148:151],a[44:47],a[8:11],a[148:151]\ns_waitcnt vmcnt(17) lgkmcnt(0)\nv_accvgpr_read_b32 v38,a209\nv_mul_f32 v25,v35,v45\nM a[144:147],a[40:43],a[8:11],a[144:147]\ns_mov_b32 s46,0\ns_barrier\n" \
"ds_read_b64_tr_b16 a[32:33],v9 offset:32768\nM a[160:163],a[48:51],a[8:11],a[160:163]\nbuffer_load_dwordx4 v17,s[28:31],s46 offen lds\nM a[164:167],a[52:55],a[8:11],a[164:167]\nLG3_%=:\ns_cbranch_execz LG10_%=\ns_add_u32 m0,s50,53248\nds_read_b64_tr_b16 a[34:35],v10 offset:32768\n" \
"v_mul_f32 v26,v41,v49\nM a[172:175],a[52:55],a[12:15],a[172:175]\nds_read_b64_tr_b16 a[36:37],v11 offset:32768\nds_read_b64_tr_b16 a[38:39],v12 offset:32768\nM a[168:171],a[48:51],a[12:15],a[168:171]\nds_read_b64_tr_b16 a[40:41],v9 offset:33280\nv_lshlrev_b32 v32,16,v38\nM a[184:187],a[56:59],a[12:15],a[184:187]\n" \
"buffer_load_dwordx4 v18,s[28:31],s46 offen lds\nM a[188:191],a[60:63],a[12:15],a[188:191]\ns_add_u32 m0,s50,57344\nds_read_b64_tr_b16 a[42:43],v10 offset:33280\nv_fmac_f32 v25,v30,v44\nM a[180:183],a[60:63],a[8:11],a[180:183]\nds_read_b64_tr_b16 a[44:45],v11 offset:33280\nv_fmac_f32 v26,v36,v48\n" \
"M a[176:179],a[56:59],a[8:11],a[176:179]\nds_read_b64_tr_b16 a[46:47],v12 offset:33280\nv_and_b32 v38,0xffff0000,v38\nM v[128:131],a[96:99],a[88:91],v[128:131]\nbuffer_load_dwordx4 v19,s[28:31],s46 offen lds\nM v[132:135],a[100:103],a[88:91],v[132:135]\ns_add_u32 m0,s50,61440\nds_read_b64_tr_b16 a[48:49],v9 offset:33792\n" \
"v_fmac_f32 v25,v31,v43\nM v[140:143],a[100:103],a[92:95],v[140:143]\nds_read_b64_tr_b16 a[50:51],v10 offset:33792\nv_fmac_f32 v26,v37,v47\nM v[136:139],a[96:99],a[92:95],v[136:139]\nds_read_b64_tr_b16 a[52:53],v11 offset:33792\nds_read_b64_tr_b16 a[54:55],v12 offset:33792\nM v[152:155],a[104:107],a[92:95],v[152:155]\n" \
"buffer_load_dwordx4 v20,s[28:31],s46 offen lds\nM v[156:159],a[108:111],a[92:95],v[156:159]\ns_add_u32 m0,s51,0\nv_fmac_f32 v25,v32,v42\nv_fmac_f32 v26,v38,v46\nM v[148:151],a[108:111],a[88:91],v[148:151]\ns_mov_b32 s46,128\nds_read_b64_tr_b16 a[56:57],v9 offset:34304\n" \
"ds_read_b64_tr_b16 a[58:59],v10 offset:34304\nM v[144:147],a[104:107],a[88:91],v[144:147]\nds_read_b64_tr_b16 a[60:61],v11 offset:34304\nv_accvgpr_read_b32 v28,a225\nM v[160:163],a[112:115],a[88:91],v[160:163]\nbuffer_load_dwordx4 v21,s[32:35],s47 offen offset:0 lds\nM v[164:167],a[116:119],a[88:91],v[164:167]\ns_add_u32 m0,s51,896\n" \
"ds_read_b64_tr_b16 a[62:63],v12 offset:34304\nC v26,v25,v26\nM v[172:175],a[116:119],a[92:95],v[172:175]\nds_read_b128 a[24:27],v5 offset:12288\nv_lshlrev_b32 v27,16,v28\nM v[168:171],a[112:115],a[92:95],v[168:171]\nds_read_b128 a[28:31],v5 offset:14336\nv_lshlrev_b32 v25,16,v26\n" \
"M v[184:187],a[120:123],a[92:95],v[184:187]\nbuffer_load_dwordx4 v21,s[32:35],s47 offen offset:128 lds\nM v[188:191],a[124:127],a[92:95],v[188:191]\ns_add_u32 m0,s51,8192\nv_and_b32 v28,0xffff0000,v28\nv_and_b32 v26,0xffff0000,v26\nM v[180:183],a[124:127],a[88:91],v[180:183]\ns_mov_b32 s47,262144\n" \
"v_mul_f32 v25,v25,v27\nv_mul_f32 v26,v26,v28\nM v[176:179],a[120:123],a[88:91],v[176:179]\nC v51,v25,v29\nC v55,v26,v58\nM v[64:67],a[96:99],a[80:83],v[64:67]\nbuffer_load_dwordx4 v21,s[32:35],s47 offen offset:0 lds\nM v[68:71],a[100:103],a[80:83],v[68:71]\n" \
"s_add_u32 m0,s51,9088\nv_accvgpr_read_b32 v39,a211\nv_mul_f32 v25,v30,v45\nM v[76:79],a[100:103],a[84:87],v[76:79]\nv_lshlrev_b32 v33,16,v39\nv_mul_f32 v26,v36,v49\nM v[72:75],a[96:99],a[84:87],v[72:75]\nv_fmac_f32 v25,v31,v44\n" \
"v_fmac_f32 v26,v37,v48\nM v[88:91],a[104:107],a[84:87],v[88:91]\nbuffer_load_dwordx4 v21,s[32:35],s47 offen offset:128 lds\nM v[92:95],a[108:111],a[84:87],v[92:95]\ns_add_u32 m0,s51,16384\nv_and_b32 v39,0xffff0000,v39\nv_fmac_f32 v25,v32,v43\nM v[84:87],a[108:111],a[80:83],v[84:87]\n" \
"s_mov_b32 s47,524288\nv_fmac_f32 v26,v38,v47\nv_fmac_f32 v25,v33,v42\nM v[80:83],a[104:107],a[80:83],v[80:83]\nv_fmac_f32 v26,v39,v46\nv_accvgpr_read_b32 v28,a227\nM v[96:99],a[112:115],a[80:83],v[96:99]\nbuffer_load_dwordx4 v21,s[32:35],s47 offen offset:0 lds\n" \
"M v[100:103],a[116:119],a[80:83],v[100:103]\ns_add_u32 m0,s51,17280\nC v26,v25,v26\nv_lshlrev_b32 v27,16,v28\nM v[108:111],a[116:119],a[84:87],v[108:111]\nds_read_b128 a[16:19],v5 offset:8192\nv_lshlrev_b32 v25,16,v26\nM v[104:107],a[112:115],a[84:87],v[104:107]\n" \
"ds_read_b128 a[20:23],v5 offset:10240\nv_and_b32 v28,0xffff0000,v28\nM v[120:123],a[120:123],a[84:87],v[120:123]\nbuffer_load_dwordx4 v21,s[32:35],s47 offen offset:128 lds\nM v[124:127],a[124:127],a[84:87],v[124:127]\ns_add_u32 m0,s51,24576\nv_and_b32 v26,0xffff0000,v26\nv_mul_f32 v29,v25,v27\n" \
"M v[116:119],a[124:127],a[80:83],v[116:119]\ns_mov_b32 s47,786432\nv_mul_f32 v58,v26,v28\nv_accvgpr_read_b32 v40,a213\nM v[112:115],a[120:123],a[80:83],v[112:115]\nv_lshlrev_b32 v34,16,v40\nv_mul_f32 v25,v31,v45\nM v[192:195],a[96:99],a[64:67],v[192:195]\n" \
"s_mov_b64 exec,0xff\nglobal_load_dword v0,v1,s[76:77]\ns_mov_b64 exec,-1\nM v[196:199],a[100:103],a[64:67],v[196:199]\nv_mul_f32 v26,v37,v49\nv_fmac_f32 v25,v32,v44\nM v[204:207],a[100:103],a[68:71],v[204:207]\ns_mov_b64 exec,0xff00\n" \
"global_load_dword v0,v1,s[76:77]\ns_mov_b64 exec,-1\nM v[200:203],a[96:99],a[68:71],v[200:203]\ns_add_u32 s76,s76,2097152\ns_addc_u32 s77,s77,0\nv_and_b32 v40,0xffff0000,v40\nv_fmac_f32 v26,v38,v48\nM v[216:219],a[104:107],a[68:71],v[216:219]\n" \
"s_mov_b64 exec,0xff\nglobal_load_dword v0,v1,s[76:77]\ns_mov_b64 exec,-1\nM v[220:223],a[108:111],a[68:71],v[220:223]\nv_fmac_f32 v25,v33,v43\nv_fmac_f32 v26,v39,v47\nM v[212:215],a[108:111],a[64:67],v[212:215]\ns_mov_b64 exec,0xff00\n" \
"global_load_dword v0,v1,s[76:77]\ns_mov_b64 exec,-1\nM v[208:211],a[104:107],a[64:67],v[208:211]\ns_mov_b32 s76,0xf149f2ca\nv_fmac_f32 v25,v34,v42\nv_fmac_f32 v26,v40,v46\nM v[224:227],a[112:115],a[64:67],v[224:227]\nv_accvgpr_read_b32 v28,a229\n" \
"C v26,v25,v26\nM v[228:231],a[116:119],a[64:67],v[228:231]\nv_lshlrev_b32 v27,16,v28\nv_lshlrev_b32 v25,16,v26\nM v[236:239],a[116:119],a[68:71],v[236:239]\nv_mul_f32 v25,v25,v27\nC v50,v25,v29\nM v[232:235],a[112:115],a[68:71],v[232:235]\n" \
"ds_read_b128 a[0:3],v5 offset:0\nds_read_b128 a[4:7],v5 offset:2048\nM v[248:251],a[120:123],a[68:71],v[248:251]\nds_read_b128 a[8:11],v5 offset:4096\nv_and_b32 v28,0xffff0000,v28\nM v[252:255],a[124:127],a[68:71],v[252:255]\nds_read_b128 a[12:15],v5 offset:6144\nv_and_b32 v26,0xffff0000,v26\n" \
"M v[244:247],a[124:127],a[64:67],v[244:247]\nv_mul_f32 v26,v26,v28\nC v54,v26,v58\nM v[240:243],a[120:123],a[64:67],v[240:243]\nds_read_b128 a[64:67],v6 offset:0\nds_read_b128 a[68:71],v6 offset:2048\nM a[128:131],a[96:99],a[72:75],a[128:131]\nds_read_b128 a[80:83],v6 offset:8192\n" \
"v_add_u32 v5,s54,v5\nM a[132:135],a[100:103],a[72:75],a[132:135]\nds_read_b128 a[84:87],v6 offset:10240\nv_add_u32 v7,s54,v7\nM a[140:143],a[100:103],a[76:79],a[140:143]\nds_read_b128 a[88:91],v6 offset:12288\nv_add_u32 v8,s54,v8\nM a[136:139],a[96:99],a[76:79],a[136:139]\n" \
"ds_read_b128 a[92:95],v6 offset:14336\nv_mov_b32 v58,s18\nM a[152:155],a[104:107],a[76:79],a[152:155]\nv_add_u32 v25,-16,v4\nv_mov_b32 v29,s62\nM a[156:159],a[108:111],a[76:79],a[156:159]\ns_and_b32 s62,%1,7\nv_mov_b32 v28,0\n" \
"v_add_u32 v26,-512,v4\nM a[148:151],a[108:111],a[72:75],a[148:151]\ns_waitcnt vmcnt(18) lgkmcnt(14)\nv_add_u32 v27,512,v4\nM a[144:147],a[104:107],a[72:75],a[144:147]\ns_lshl_b32 s62,s62,8\ns_barrier\nds_read_b64_tr_b16 a[96:97],v9 offset:49152\n" \
"M a[160:163],a[112:115],a[72:75],a[160:163]\ns_add_u32 s62,s62,94380032\nds_read_b64_tr_b16 a[98:99],v10 offset:49152\nds_read_b64_tr_b16 a[100:101],v11 offset:49152\nM a[164:167],a[116:119],a[72:75],a[164:167]\nds_read_b64_tr_b16 a[102:103],v12 offset:49152\nM a[172:175],a[116:119],a[76:79],a[172:175]\nds_read_b64_tr_b16 a[104:105],v9 offset:49664\n" \
"ds_read_b64_tr_b16 a[106:107],v10 offset:49664\nM a[168:171],a[112:115],a[76:79],a[168:171]\nds_read_b64_tr_b16 a[108:109],v11 offset:49664\nM a[184:187],a[120:123],a[76:79],a[184:187]\nds_read_b64_tr_b16 a[110:111],v12 offset:49664\nM a[188:191],a[124:127],a[76:79],a[188:191]\nds_read_b64_tr_b16 a[112:113],v9 offset:50176\nds_read_b64_tr_b16 a[114:115],v10 offset:50176\n" \
"M a[180:183],a[124:127],a[72:75],a[180:183]\nds_read_b64_tr_b16 a[116:117],v11 offset:50176\nM a[176:179],a[120:123],a[72:75],a[176:179]\ns_waitcnt lgkmcnt(7)\nds_read_b128 a[72:75],v6 offset:4096\nM v[128:131],a[32:35],a[24:27],v[128:131]\nds_read_b128 a[76:79],v6 offset:6144\nv_add_u32 v6,s54,v6\n" \
"M v[132:135],a[36:39],a[24:27],v[132:135]\nds_read_b64_tr_b16 a[118:119],v12 offset:50176\nM v[140:143],a[36:39],a[28:31],v[140:143]\nds_read_b64_tr_b16 a[120:121],v9 offset:50688\nds_read_b64_tr_b16 a[122:123],v10 offset:50688\nM v[136:139],a[32:35],a[28:31],v[136:139]\nds_read_b64_tr_b16 a[124:125],v11 offset:50688\nv_sub_u32 v9,v9,v62\n" \
"M v[152:155],a[40:43],a[28:31],v[152:155]\nds_read_b64_tr_b16 a[126:127],v12 offset:50688\nv_sub_u32 v10,v10,v62\nM v[156:159],a[44:47],a[28:31],v[156:159]\nv_sub_u32 v11,v11,v62\nv_sub_u32 v12,v12,v62\nM v[148:151],a[44:47],a[24:27],v[148:151]\nM v[144:147],a[40:43],a[24:27],v[144:147]\n" \
"M v[160:163],a[48:51],a[24:27],v[160:163]\nM v[164:167],a[52:55],a[24:27],v[164:167]\nM v[172:175],a[52:55],a[28:31],v[172:175]\nM v[168:171],a[48:51],a[28:31],v[168:171]\nM v[184:187],a[56:59],a[28:31],v[184:187]\nM v[188:191],a[60:63],a[28:31],v[188:191]\nM v[180:183],a[60:63],a[24:27],v[180:183]\nbuffer_load_dwordx4 v21,s[32:35],s47 offen offset:0 lds\n" \
"M v[176:179],a[56:59],a[24:27],v[176:179]\ns_add_u32 m0,s51,25472\nM v[64:67],a[32:35],a[16:19],v[64:67]\nM v[68:71],a[36:39],a[16:19],v[68:71]\nM v[76:79],a[36:39],a[20:23],v[76:79]\nbuffer_store_dwordx4 v[50:53],v60,s[36:39],s98 offen offset:0 sc1\nM v[72:75],a[32:35],a[20:23],v[72:75]\nM v[88:91],a[40:43],a[20:23],v[88:91]\n" \
"M v[92:95],a[44:47],a[20:23],v[92:95]\nM v[84:87],a[44:47],a[16:19],v[84:87]\nM v[80:83],a[40:43],a[16:19],v[80:83]\nM v[96:99],a[48:51],a[16:19],v[96:99]\nM v[100:103],a[52:55],a[16:19],v[100:103]\nM v[108:111],a[52:55],a[20:23],v[108:111]\nM v[104:107],a[48:51],a[20:23],v[104:107]\nM v[120:123],a[56:59],a[20:23],v[120:123]\n" \
"M v[124:127],a[60:63],a[20:23],v[124:127]\nM v[116:119],a[60:63],a[16:19],v[116:119]\nM v[112:115],a[56:59],a[16:19],v[112:115]\nM v[192:195],a[32:35],a[0:3],v[192:195]\nM v[196:199],a[36:39],a[0:3],v[196:199]\nM v[204:207],a[36:39],a[4:7],v[204:207]\nM v[200:203],a[32:35],a[4:7],v[200:203]\nM v[216:219],a[40:43],a[4:7],v[216:219]\n" \
"M v[220:223],a[44:47],a[4:7],v[220:223]\nM v[212:215],a[44:47],a[0:3],v[212:215]\nM v[208:211],a[40:43],a[0:3],v[208:211]\nM v[224:227],a[48:51],a[0:3],v[224:227]\nM v[228:231],a[52:55],a[0:3],v[228:231]\nM v[236:239],a[52:55],a[4:7],v[236:239]\nbuffer_store_dwordx4 v[54:57],v60,s[36:39],s99 offen offset:0 sc1\nM v[232:235],a[48:51],a[4:7],v[232:235]\n" \
"s_nop 0\nv_lshlrev_b32 v56,23,v4\nv_bfe_u32 v57,v4,4,5\nM v[248:251],a[56:59],a[4:7],v[248:251]\nv_lshl_add_u32 v57,v57,3,s62\nv_add_u32 v55,s56,v4\nM v[252:255],a[60:63],a[4:7],v[252:255]\nM v[244:247],a[60:63],a[0:3],v[244:247]\n" \
"M v[240:243],a[56:59],a[0:3],v[240:243]\nM a[128:131],a[32:35],a[8:11],a[128:131]\nM a[132:135],a[36:39],a[8:11],a[132:135]\nM a[140:143],a[36:39],a[12:15],a[140:143]\nM a[136:139],a[32:35],a[12:15],a[136:139]\nM a[152:155],a[40:43],a[12:15],a[152:155]\nM a[156:159],a[44:47],a[12:15],a[156:159]\nM a[148:151],a[44:47],a[8:11],a[148:151]\n" \
"s_waitcnt vmcnt(9) lgkmcnt(0)\nM a[144:147],a[40:43],a[8:11],a[144:147]\ns_barrier\nds_read_b64_tr_b16 a[0:1],v9 offset:0\nM a[160:163],a[48:51],a[8:11],a[160:163]\nds_read_b64_tr_b16 a[2:3],v10 offset:0\nds_read_b64_tr_b16 a[4:5],v11 offset:0\nM a[164:167],a[52:55],a[8:11],a[164:167]\n" \
"ds_read_b128 a[44:47],v5 offset:45056\nM a[172:175],a[52:55],a[12:15],a[172:175]\nds_read_b64_tr_b16 a[6:7],v12 offset:0\nM a[168:171],a[48:51],a[12:15],a[168:171]\nds_read_b64_tr_b16 a[16:17],v9 offset:1024\nds_read_b64_tr_b16 a[18:19],v10 offset:1024\nM a[184:187],a[56:59],a[12:15],a[184:187]\nds_read_b64_tr_b16 a[20:21],v11 offset:1024\n" \
"M a[188:191],a[60:63],a[12:15],a[188:191]\nds_read_b64_tr_b16 a[22:23],v12 offset:1024\nM a[180:183],a[60:63],a[8:11],a[180:183]\nds_read_b64_tr_b16 a[24:25],v9 offset:1536\nds_read_b64_tr_b16 a[26:27],v10 offset:1536\nM a[176:179],a[56:59],a[8:11],a[176:179]\nds_read_b64_tr_b16 a[8:9],v9 offset:512\nM v[128:131],a[96:99],a[88:91],v[128:131]\n" \
"ds_read_b64_tr_b16 a[10:11],v10 offset:512\nM v[132:135],a[100:103],a[88:91],v[132:135]\nds_read_b64_tr_b16 a[12:13],v11 offset:512\nds_read_b64_tr_b16 a[14:15],v12 offset:512\nM v[140:143],a[100:103],a[92:95],v[140:143]\nbuffer_load_dwordx4 v21,s[32:35],s47 offen offset:128 lds\nM v[136:139],a[96:99],a[92:95],v[136:139]\ns_add_u32 m0,s50,65536\n" \
"v_mul_f32 v128,s20,v128\nv_mul_f32 v129,s20,v129\nM v[152:155],a[104:107],a[92:95],v[152:155]\ns_mov_b32 s47,1048576\nv_mul_f32 v130,s20,v130\nv_mul_f32 v131,s20,v131\nM v[156:159],a[108:111],a[92:95],v[156:159]\nv_mul_f32 v132,s20,v132\n" \
"v_mul_f32 v133,s20,v133\nM v[148:151],a[108:111],a[88:91],v[148:151]\nbuffer_load_dwordx4 v13,s[28:31],s46 offen lds\nM v[144:147],a[104:107],a[88:91],v[144:147]\ns_add_u32 m0,s50,69632\nv_mul_f32 v134,s20,v134\nv_mul_f32 v135,s20,v135\nM v[160:163],a[112:115],a[88:91],v[160:163]\n" \
"v_mul_f32 v136,s20,v136\nv_mul_f32 v137,s20,v137\nM v[164:167],a[116:119],a[88:91],v[164:167]\nv_mul_f32 v138,s20,v138\nv_mul_f32 v139,s20,v139\nM v[172:175],a[116:119],a[92:95],v[172:175]\nbuffer_load_dwordx4 v14,s[28:31],s46 offen lds\nM v[168:171],a[112:115],a[92:95],v[168:171]\n" \
"s_add_u32 m0,s50,73728\nds_read_b64_tr_b16 a[28:29],v11 offset:1536\nv_mul_f32 v140,s20,v140\nM v[184:187],a[120:123],a[92:95],v[184:187]\nds_read_b64_tr_b16 a[30:31],v12 offset:1536\nds_read_b128 a[40:43],v5 offset:40960\nM v[188:191],a[124:127],a[92:95],v[188:191]\nv_mul_f32 v141,s20,v141\n" \
"v_mul_f32 v142,s20,v142\nM v[180:183],a[124:127],a[88:91],v[180:183]\nbuffer_load_dwordx4 v15,s[28:31],s46 offen lds\nM v[176:179],a[120:123],a[88:91],v[176:179]\ns_add_u32 m0,s50,77824\nv_mul_f32 v143,s20,v143\nv_mul_f32 v144,s20,v144\nM v[64:67],a[96:99],a[80:83],v[64:67]\n" \
"v_mul_f32 v145,s20,v145\nv_mul_f32 v146,s20,v146\nM v[68:71],a[100:103],a[80:83],v[68:71]\nds_read_b128 a[32:35],v5 offset:32768\nv_mul_f32 v147,s20,v147\nM v[76:79],a[100:103],a[84:87],v[76:79]\nbuffer_load_dwordx4 v16,s[28:31],s46 offen lds\nM v[72:75],a[96:99],a[84:87],v[72:75]\n" \
"s_add_u32 m0,s50,81920\ns_mov_b32 s46,128\nv_mul_f32 v64,s20,v64\nv_mul_f32 v65,s20,v65\nM v[88:91],a[104:107],a[84:87],v[88:91]\nv_mul_f32 v66,s20,v66\nv_mul_f32 v67,s20,v67\nM v[92:95],a[108:111],a[84:87],v[92:95]\n" \
"C v64,v64,v65\nC v65,v66,v67\nM v[84:87],a[108:111],a[80:83],v[84:87]\nbuffer_load_dwordx4 v17,s[28:31],s46 offen lds\nM v[80:83],a[104:107],a[80:83],v[80:83]\ns_add_u32 m0,s50,86016\nv_mul_f32 v68,s20,v68\nv_mul_f32 v69,s20,v69\n" \
"M v[96:99],a[112:115],a[80:83],v[96:99]\nv_mul_f32 v148,s20,v148\nv_mul_f32 v149,s20,v149\nM v[100:103],a[116:119],a[80:83],v[100:103]\nv_mul_f32 v150,s20,v150\nv_mul_f32 v151,s20,v151\nM v[108:111],a[116:119],a[84:87],v[108:111]\nbuffer_load_dwordx4 v18,s[28:31],s46 offen lds\n" \
"M v[104:107],a[112:115],a[84:87],v[104:107]\ns_add_u32 m0,s50,90112\nv_mul_f32 v70,s20,v70\nv_mul_f32 v152,s20,v152\nM v[120:123],a[120:123],a[84:87],v[120:123]\nv_mul_f32 v71,s20,v71\nv_mul_f32 v153,s20,v153\nM v[124:127],a[124:127],a[84:87],v[124:127]\n" \
"v_mul_f32 v154,s20,v154\nv_mul_f32 v155,s20,v155\nM v[116:119],a[124:127],a[80:83],v[116:119]\nbuffer_load_dwordx4 v19,s[28:31],s46 offen lds\nM v[112:115],a[120:123],a[80:83],v[112:115]\ns_add_u32 m0,s50,94208\nv_mul_f32 v156,s20,v156\nv_mul_f32 v157,s20,v157\n" \
"M v[192:195],a[96:99],a[64:67],v[192:195]\nv_mul_f32 v158,s20,v158\nv_mul_f32 v159,s20,v159\nM v[196:199],a[100:103],a[64:67],v[196:199]\nv_mul_f32 v160,s20,v160\nv_mul_f32 v161,s20,v161\nM v[204:207],a[100:103],a[68:71],v[204:207]\nbuffer_load_dwordx4 v20,s[28:31],s46 offen lds\n" \
"M v[200:203],a[96:99],a[68:71],v[200:203]\ns_add_u32 m0,s51,32768\nds_read_b128 a[36:39],v5 offset:36864\nv_mul_f32 v162,s20,v162\nM v[216:219],a[104:107],a[68:71],v[216:219]\ns_mov_b32 s46,256\nv_mul_f32 v163,s20,v163\nC v66,v68,v69\n" \
"M v[220:223],a[108:111],a[68:71],v[220:223]\nv_mul_f32 v72,s20,v72\nv_mul_f32 v164,s20,v164\nM v[212:215],a[108:111],a[64:67],v[212:215]\nbuffer_load_dwordx4 v21,s[32:35],s47 offen offset:0 lds\nM v[208:211],a[104:107],a[64:67],v[208:211]\ns_add_u32 m0,s51,33664\nv_mul_f32 v73,s20,v73\n" \
"C v30,v128,v129\nM v[224:227],a[112:115],a[64:67],v[224:227]\nv_mul_f32 v165,s20,v165\nC v31,v130,v131\nM v[228:231],a[116:119],a[64:67],v[228:231]\nv_mul_f32 v166,s20,v166\nC v32,v132,v133\nM v[236:239],a[116:119],a[68:71],v[236:239]\n" \
"buffer_load_dwordx4 v21,s[32:35],s47 offen offset:128 lds\nM v[232:235],a[112:115],a[68:71],v[232:235]\ns_add_u32 m0,s51,40960\nv_mul_f32 v167,s20,v167\nC v33,v134,v135\nM v[248:251],a[120:123],a[68:71],v[248:251]\ns_mov_b32 s47,1310720\nv_mul_f32 v168,s20,v168\n" \
"C v34,v136,v137\nM v[252:255],a[124:127],a[68:71],v[252:255]\nv_mul_f32 v169,s20,v169\nC v35,v138,v139\nM v[244:247],a[124:127],a[64:67],v[244:247]\nbuffer_load_dwordx4 v21,s[32:35],s47 offen offset:0 lds\nM v[240:243],a[120:123],a[64:67],v[240:243]\ns_add_u32 m0,s51,41856\n" \
"ds_read_b64_tr_b16 a[48:49],v9 offset:8192\nv_mul_f32 v170,s20,v170\nM a[128:131],a[96:99],a[72:75],a[128:131]\nds_read_b64_tr_b16 a[50:51],v10 offset:8192\nds_read_b128 a[80:83],v6 offset:32768\nM a[132:135],a[100:103],a[72:75],a[132:135]\nds_read_b128 a[84:87],v6 offset:36864\nv_mul_f32 v171,s20,v171\n" \
"M a[140:143],a[100:103],a[76:79],a[140:143]\nbuffer_load_dwordx4 v21,s[32:35],s47 offen offset:128 lds\nM a[136:139],a[96:99],a[76:79],a[136:139]\ns_add_u32 m0,s50,0\nv_mul_f32 v74,s20,v74\nC v36,v140,v141\nM a[152:155],a[104:107],a[76:79],a[152:155]\ns_mov_b32 s47,1572864\n" \
"ds_read_b64_tr_b16 a[52:53],v11 offset:8192\nv_mul_f32 v75,s20,v75\nM a[156:159],a[108:111],a[76:79],a[156:159]\nds_read_b64_tr_b16 a[54:55],v12 offset:8192\nds_read_b64_tr_b16 a[56:57],v9 offset:8704\nM a[148:151],a[108:111],a[72:75],a[148:151]\nbuffer_load_dwordx4 v13,s[28:31],s46 offen lds\nM a[144:147],a[104:107],a[72:75],a[144:147]\n" \
"s_add_u32 m0,s50,4096\nC v37,v142,v143\nC v38,v144,v145\nM a[160:163],a[112:115],a[72:75],a[160:163]\nds_read_b64_tr_b16 a[58:59],v10 offset:8704\nds_read_b64_tr_b16 a[60:61],v11 offset:8704\nM a[164:167],a[116:119],a[72:75],a[164:167]\nds_read_b64_tr_b16 a[62:63],v12 offset:8704\n" \
"C v39,v146,v147\nM a[172:175],a[116:119],a[76:79],a[172:175]\nbuffer_load_dwordx4 v14,s[28:31],s46 offen lds\nM a[168:171],a[112:115],a[76:79],a[168:171]\ns_add_u32 m0,s50,8192\nds_read_b64_tr_b16 a[64:65],v9 offset:9216\nv_mul_f32 v172,s20,v172\nM a[184:187],a[120:123],a[76:79],a[184:187]\n" \
"ds_read_b64_tr_b16 a[66:67],v10 offset:9216\nv_mul_f32 v173,s20,v173\nM a[188:191],a[124:127],a[76:79],a[188:191]\ns_waitcnt lgkmcnt(5)\nds_read_b64_tr_b16 a[68:69],v11 offset:9216\nv_mul_f32 v174,s20,v174\nM a[180:183],a[124:127],a[72:75],a[180:183]\nbuffer_load_dwordx4 v15,s[28:31],s46 offen lds\n" \
"M a[176:179],a[120:123],a[72:75],a[176:179]\ns_branch LC_%=\n.p2align 12\nLAV_%=:\ns_mov_b64 exec,-1\ns_waitcnt vmcnt(9)\ns_barrier\nds_read_b64_tr_b16 a[32:33],v9 offset:0\n" \
"ds_read_b64_tr_b16 a[34:35],v10 offset:0\nds_read_b64_tr_b16 a[36:37],v11 offset:0\nds_read_b64_tr_b16 a[38:39],v12 offset:0\nds_read_b128 a[24:27],v7 offset:12288\nds_read_b128 a[28:31],v7 offset:14336\ns_waitcnt lgkmcnt(0)\nM v[128:131],a[24:27],a[32:35],0\ns_add_u32 m0,s22,32768\n" \
"s_mov_b32 s47,524288\ns_mov_b32 s46,128\nds_read_b64_tr_b16 a[40:41],v9 offset:512\nds_read_b64_tr_b16 a[42:43],v10 offset:512\nds_read_b64_tr_b16 a[44:45],v11 offset:512\nds_read_b64_tr_b16 a[46:47],v12 offset:512\nds_read_b64_tr_b16 a[48:49],v9 offset:1024\nds_read_b64_tr_b16 a[50:51],v10 offset:1024\n" \
"ds_read_b64_tr_b16 a[52:53],v11 offset:1024\nds_read_b64_tr_b16 a[54:55],v12 offset:1024\nM v[132:135],a[28:31],a[32:35],0\nbuffer_load_dwordx4 v21,s[32:35],s47 offen offset:0 lds\nds_read_b64_tr_b16 a[56:57],v9 offset:1536\nds_read_b64_tr_b16 a[58:59],v10 offset:1536\nds_read_b64_tr_b16 a[60:61],v11 offset:1536\nds_read_b64_tr_b16 a[62:63],v12 offset:1536\n" \
"M v[140:143],a[28:31],a[36:39],0\ns_add_u32 m0,s22,33664\nM v[136:139],a[24:27],a[36:39],0\ns_waitcnt lgkmcnt(8)\ns_mov_b64 s[40:41],s[66:67]\nds_read_b128 a[16:19],v7 offset:8192\nM v[152:155],a[24:27],a[44:47],0\ns_and_b32 s41,s41,0xffff\n" \
"ds_read_b128 a[20:23],v7 offset:10240\nM v[156:159],a[28:31],a[44:47],0\nbuffer_load_dwordx4 v21,s[32:35],s47 offen offset:128 lds\nM v[148:151],a[28:31],a[40:43],0\ns_add_u32 m0,s22,34816\nds_read_b128 a[0:3],v7 offset:0\nM v[144:147],a[24:27],a[40:43],0\ns_waitcnt lgkmcnt(3)\n" \
"s_mov_b32 s42,4194304\nds_read_b128 a[4:7],v7 offset:2048\nM v[160:163],a[24:27],a[48:51],0\ns_mov_b32 s43,0x00020000\nds_read_b128 a[8:11],v7 offset:4096\nM v[164:167],a[28:31],a[48:51],0\nbuffer_load_dwordx4 v21,s[92:95],s47 offen offset:0 lds\nM v[172:175],a[28:31],a[52:55],0\n" \
"s_add_u32 m0,s22,35712\nM v[168:171],a[24:27],a[52:55],0\ns_mov_b64 s[80:81],s[68:69]\nds_read_b128 a[12:15],v7 offset:6144\nM v[184:187],a[24:27],a[60:63],0\ns_mov_b32 s52,0\nds_read_b128 a[88:91],v8 offset:12288\nM v[188:191],a[28:31],a[60:63],0\n" \
"buffer_load_dwordx4 v21,s[92:95],s47 offen offset:128 lds\nM v[180:183],a[28:31],a[56:59],0\ns_add_u32 m0,s50,0\nds_read_b128 a[92:95],v8 offset:14336\nM v[176:179],a[24:27],a[56:59],0\ns_waitcnt lgkmcnt(3)\ns_mov_b32 s47,786432\nds_read_b128 a[80:83],v8 offset:8192\n" \
"M v[64:67],a[16:19],a[32:35],0\ns_waitcnt vmcnt(9) lgkmcnt(9)\nds_read_b128 a[84:87],v8 offset:10240\nM v[68:71],a[20:23],a[32:35],0\nbuffer_load_dwordx4 v13,s[28:31],s46 offen lds\ns_barrier\nM v[76:79],a[20:23],a[36:39],0\ns_add_u32 m0,s50,4096\n" \
"M v[72:75],a[16:19],a[36:39],0\ns_and_b32 s81,s81,0xffff\nds_read_b64_tr_b16 a[96:97],v9 offset:16384\nds_read_b64_tr_b16 a[98:99],v10 offset:16384\nM v[88:91],a[16:19],a[44:47],0\ns_mov_b32 s82,4194304\nds_read_b64_tr_b16 a[100:101],v11 offset:16384\nM v[92:95],a[20:23],a[44:47],0\n" \
"buffer_load_dwordx4 v14,s[28:31],s46 offen lds\nM v[84:87],a[20:23],a[40:43],0\ns_add_u32 m0,s50,8192\nds_read_b64_tr_b16 a[102:103],v12 offset:16384\nM v[80:83],a[16:19],a[40:43],0\ns_mov_b32 s83,0x00020000\nds_read_b64_tr_b16 a[104:105],v9 offset:16896\nM v[96:99],a[16:19],a[48:51],0\n" \
"s_lshl_b32 s60,s7,18\nds_read_b64_tr_b16 a[106:107],v10 offset:16896\nM v[100:103],a[20:23],a[48:51],0\nbuffer_load_dwordx4 v15,s[28:31],s46 offen lds\nM v[108:111],a[20:23],a[52:55],0\ns_add_u32 m0,s50,12288\nds_read_b64_tr_b16 a[108:109],v11 offset:16896\nM v[104:107],a[16:19],a[52:55],0\n" \
"s_sub_u32 s61,3932160,s60\nds_read_b64_tr_b16 a[110:111],v12 offset:16896\nM v[120:123],a[16:19],a[60:63],0\ns_add_u32 s44,s78,s98\ns_addc_u32 s45,s79,0\nds_read_b64_tr_b16 a[112:113],v9 offset:17408\nds_read_b64_tr_b16 a[114:115],v10 offset:17408\nM v[124:127],a[20:23],a[60:63],0\n" \
"buffer_load_dwordx4 v16,s[28:31],s46 offen lds\nM v[116:119],a[20:23],a[56:59],0\ns_add_u32 m0,s50,16384\ns_mov_b32 s46,128\nM v[112:115],a[16:19],a[56:59],0\ns_and_b32 s45,s45,0xffff\nds_read_b64_tr_b16 a[116:117],v11 offset:17408\nds_read_b64_tr_b16 a[118:119],v12 offset:17408\n" \
"M v[192:195],a[0:3],a[32:35],0\ns_mov_b64 s[36:37],s[90:91]\nds_read_b64_tr_b16 a[120:121],v9 offset:17920\nM v[196:199],a[4:7],a[32:35],0\nbuffer_load_dwordx4 v17,s[28:31],s46 offen lds\nM v[204:207],a[4:7],a[36:39],0\ns_add_u32 m0,s50,20480\nds_read_b64_tr_b16 a[122:123],v10 offset:17920\n" \
"M v[200:203],a[0:3],a[36:39],0\ns_and_b32 s37,s37,0xffff\nds_read_b64_tr_b16 a[124:125],v11 offset:17920\nM v[216:219],a[0:3],a[44:47],0\ns_mov_b32 s38,134217728\nds_read_b64_tr_b16 a[126:127],v12 offset:17920\nM v[220:223],a[4:7],a[44:47],0\nbuffer_load_dwordx4 v18,s[28:31],s46 offen lds\n" \
"M v[212:215],a[4:7],a[40:43],0\ns_add_u32 m0,s50,24576\nds_read_b128 a[64:67],v8 offset:0\nM v[208:211],a[0:3],a[40:43],0\ns_mov_b32 s39,0x00020000\nds_read_b128 a[68:71],v8 offset:2048\nM v[224:227],a[0:3],a[48:51],0\ns_add_u32 s96,s57,32768\n" \
"ds_read_b128 a[72:75],v8 offset:4096\nM v[228:231],a[4:7],a[48:51],0\nbuffer_load_dwordx4 v19,s[28:31],s46 offen lds\nM v[236:239],a[4:7],a[52:55],0\ns_add_u32 m0,s50,28672\nM v[232:235],a[0:3],a[52:55],0\ns_lshr_b32 s62,%1,3\nds_read_b128 a[76:79],v8 offset:6144\n" \
"M v[248:251],a[0:3],a[60:63],0\ns_lshl_b32 s62,s62,2\nM v[252:255],a[4:7],a[60:63],0\nbuffer_load_dwordx4 v20,s[28:31],s46 offen lds\nM v[244:247],a[4:7],a[56:59],0\ns_add_u32 m0,s22,49152\nM v[240:243],a[0:3],a[56:59],0\ns_waitcnt lgkmcnt(1)\n" \
"s_mov_b32 s46,256\nM a[128:131],a[8:11],a[32:35],0\ns_add_u32 s62,s62,%2\nM a[132:135],a[12:15],a[32:35],0\nbuffer_load_dwordx4 v21,s[32:35],s47 offen offset:0 lds\nM a[140:143],a[12:15],a[36:39],0\ns_add_u32 m0,s22,50048\nM a[136:139],a[8:11],a[36:39],0\n" \
"s_lshl_b32 s62,s62,10\nM a[152:155],a[8:11],a[44:47],0\ns_min_u32 s62,s62,LEND_%=-LBEG_%=-1024\nM a[156:159],a[12:15],a[44:47],0\nbuffer_load_dwordx4 v21,s[32:35],s47 offen offset:128 lds\nM a[148:151],a[12:15],a[40:43],0\ns_add_u32 m0,s22,51200\nM a[144:147],a[8:11],a[40:43],0\n" \
"s_add_u32 s100,s100,s62\ns_addc_u32 s101,s101,0\nM a[160:163],a[8:11],a[48:51],0\ns_lshl_b32 s62,s8,2\nM a[164:167],a[12:15],a[48:51],0\nbuffer_load_dwordx4 v21,s[92:95],s47 offen offset:0 lds\nM a[172:175],a[12:15],a[52:55],0\ns_add_u32 m0,s22,52096\n" \
"M a[168:171],a[8:11],a[52:55],0\ns_add_u32 s62,s62,%2\nM a[184:187],a[8:11],a[60:63],0\ns_mov_b64 s[10:11],s[72:73]\nM a[188:191],a[12:15],a[60:63],0\nbuffer_load_dwordx4 v21,s[92:95],s47 offen offset:128 lds\nM a[180:183],a[12:15],a[56:59],0\ns_add_u32 m0,s50,32768\n" \
"M a[176:179],a[8:11],a[56:59],0\ns_mov_b32 s47,1048576\nM v[128:131],a[88:91],a[96:99],v[128:131]\ns_lshl_b32 s63,s62,12\nM v[132:135],a[92:95],a[96:99],v[132:135]\nbuffer_load_dwordx4 v13,s[28:31],s46 offen lds\nM v[140:143],a[92:95],a[100:103],v[140:143]\ns_add_u32 m0,s50,36864\n" \
"M v[136:139],a[88:91],a[100:103],v[136:139]\ns_add_u32 s68,s10,s63\ns_addc_u32 s69,s11,0\nM v[152:155],a[88:91],a[108:111],v[152:155]\ns_and_b32 s69,s69,0xffff\nM v[156:159],a[92:95],a[108:111],v[156:159]\nbuffer_load_dwordx4 v14,s[28:31],s46 offen lds\nM v[148:151],a[92:95],a[104:107],v[148:151]\n" \
"s_add_u32 m0,s50,40960\nM v[144:147],a[88:91],a[104:107],v[144:147]\ns_lshl_b32 s62,s62,1\nM v[160:163],a[88:91],a[112:115],v[160:163]\ns_mov_b64 s[12:13],s[74:75]\nM v[164:167],a[92:95],a[112:115],v[164:167]\nbuffer_load_dwordx4 v15,s[28:31],s46 offen lds\nM v[172:175],a[92:95],a[116:119],v[172:175]\n" \
"s_add_u32 m0,s50,45056\nM v[168:171],a[88:91],a[116:119],v[168:171]\ns_sub_u32 s14,s18,2000\ns_subb_u32 s15,s19,0\nM v[184:187],a[88:91],a[124:127],v[184:187]\ns_add_u32 s16,s14,16777216\ns_addc_u32 s17,s15,0\nM v[188:191],a[92:95],a[124:127],v[188:191]\n" \
"buffer_load_dwordx4 v16,s[28:31],s46 offen lds\nM v[180:183],a[92:95],a[120:123],v[180:183]\ns_add_u32 m0,s50,49152\nM v[176:179],a[88:91],a[120:123],v[176:179]\ns_mov_b32 s46,256\nM v[64:67],a[80:83],a[96:99],v[64:67]\ns_lshr_b32 s21,s63,5\nM v[68:71],a[84:87],a[96:99],v[68:71]\n" \
"buffer_load_dwordx4 v17,s[28:31],s46 offen lds\nM v[76:79],a[84:87],a[100:103],v[76:79]\ns_add_u32 m0,s50,53248\nM v[72:75],a[80:83],a[100:103],v[72:75]\ns_mov_b64 s[4:5],s[90:91]\nM v[88:91],a[80:83],a[108:111],v[88:91]\ns_waitcnt vmcnt(9) lgkmcnt(0)\nM v[92:95],a[84:87],a[108:111],v[92:95]\n" \
"s_add_u32 s63,s63,79691776\ns_barrier\nds_read_b64_tr_b16 a[32:33],v9 offset:32768\nM v[84:87],a[84:87],a[104:107],v[84:87]\nbuffer_load_dwordx4 v18,s[28:31],s46 offen lds\nM v[80:83],a[80:83],a[104:107],v[80:83]\ns_add_u32 m0,s50,57344\nds_read_b64_tr_b16 a[34:35],v10 offset:32768\n" \
"M v[96:99],a[80:83],a[112:115],v[96:99]\ns_add_u32 s72,s4,s63\ns_addc_u32 s73,s5,0\nds_read_b64_tr_b16 a[36:37],v11 offset:32768\nds_read_b64_tr_b16 a[38:39],v12 offset:32768\nM v[100:103],a[84:87],a[112:115],v[100:103]\ns_mov_b32 s63,65536\nds_read_b128 a[24:27],v5 offset:12288\n" \
"M v[108:111],a[84:87],a[116:119],v[108:111]\nbuffer_load_dwordx4 v19,s[28:31],s46 offen lds\nM v[104:107],a[80:83],a[116:119],v[104:107]\ns_add_u32 m0,s50,61440\nM v[120:123],a[80:83],a[124:127],v[120:123]\ns_cmp_lt_u32 %2,2\ns_cselect_b32 s63,32768,s63\nds_read_b128 a[28:31],v5 offset:14336\n" \
"M v[124:127],a[84:87],a[124:127],v[124:127]\ns_lshl_b32 s56,%2,14\nds_read_b64_tr_b16 a[40:41],v9 offset:33280\nds_read_b64_tr_b16 a[42:43],v10 offset:33280\nM v[116:119],a[84:87],a[120:123],v[116:119]\nbuffer_load_dwordx4 v20,s[28:31],s46 offen lds\nM v[112:115],a[80:83],a[120:123],v[112:115]\ns_add_u32 m0,s22,0\n" \
"M v[192:195],a[64:67],a[96:99],v[192:195]\ns_mov_b32 s46,384\nds_read_b64_tr_b16 a[44:45],v11 offset:33280\nds_read_b64_tr_b16 a[46:47],v12 offset:33280\nM v[196:199],a[68:71],a[96:99],v[196:199]\ns_add_u32 s56,s56,s63\nds_read_b64_tr_b16 a[48:49],v9 offset:33792\nM v[204:207],a[68:71],a[100:103],v[204:207]\n" \
"buffer_load_dwordx4 v21,s[32:35],s47 offen offset:0 lds\nM v[200:203],a[64:67],a[100:103],v[200:203]\ns_add_u32 m0,s22,896\nds_read_b64_tr_b16 a[50:51],v10 offset:33792\nM v[216:219],a[64:67],a[108:111],v[216:219]\ns_and_b32 s73,s73,0xffff\nds_read_b64_tr_b16 a[52:53],v11 offset:33792\nM v[220:223],a[68:71],a[108:111],v[220:223]\n" \
"s_mov_b32 s74,4096\nds_read_b64_tr_b16 a[54:55],v12 offset:33792\nM v[212:215],a[68:71],a[104:107],v[212:215]\nbuffer_load_dwordx4 v21,s[32:35],s47 offen offset:128 lds\nM v[208:211],a[64:67],a[104:107],v[208:211]\ns_add_u32 m0,s22,2048\nds_read_b64_tr_b16 a[56:57],v9 offset:34304\nM v[224:227],a[64:67],a[112:115],v[224:227]\n" \
"s_mov_b32 s75,0x00020000\nds_read_b64_tr_b16 a[58:59],v10 offset:34304\nM v[228:231],a[68:71],a[112:115],v[228:231]\ns_add_u32 s84,s84,524288\ns_addc_u32 s85,s85,0\nds_read_b64_tr_b16 a[60:61],v11 offset:34304\nds_read_b64_tr_b16 a[62:63],v12 offset:34304\nM v[236:239],a[68:71],a[116:119],v[236:239]\n" \
"buffer_load_dwordx4 v21,s[92:95],s47 offen offset:0 lds\nM v[232:235],a[64:67],a[116:119],v[232:235]\ns_add_u32 m0,s22,2944\nM v[248:251],a[64:67],a[124:127],v[248:251]\ns_add_u32 s21,s21,67108864\nds_read_b128 a[16:19],v5 offset:8192\nM v[252:255],a[68:71],a[124:127],v[252:255]\ns_mov_b32 s53,s22\n" \
"ds_read_b128 a[20:23],v5 offset:10240\nM v[244:247],a[68:71],a[120:123],v[244:247]\ns_mov_b64 exec,0xff\nglobal_load_dword v0,v1,s[84:85]\ns_mov_b64 exec,-1\nM v[240:243],a[64:67],a[120:123],v[240:243]\ns_add_u32 s84,s84,524288\ns_addc_u32 s85,s85,0\n" \
"ds_read_b128 a[0:3],v5 offset:0\nds_read_b128 a[4:7],v5 offset:2048\nM a[128:131],a[72:75],a[96:99],a[128:131]\ns_mov_b64 exec,0xff\nglobal_load_dword v0,v1,s[84:85]\ns_mov_b64 exec,-1\nM a[132:135],a[76:79],a[96:99],a[132:135]\ns_add_u32 s84,s84,524288\n" \
"s_addc_u32 s85,s85,0\nds_read_b128 a[8:11],v5 offset:4096\nds_read_b128 a[12:15],v5 offset:6144\nM a[140:143],a[76:79],a[100:103],a[140:143]\nglobal_load_dwordx4 v[46:49],v4,s[86:87] offset:0\nM a[136:139],a[72:75],a[100:103],a[136:139]\nds_read_b128 a[88:91],v6 offset:12288\nds_read_b128 a[92:95],v6 offset:14336\n" \
"M a[152:155],a[72:75],a[108:111],a[152:155]\nds_read_b128 a[64:67],v6 offset:0\nM a[156:159],a[76:79],a[108:111],a[156:159]\nds_read_b128 a[68:71],v6 offset:2048\nM a[148:151],a[76:79],a[104:107],a[148:151]\nglobal_load_dwordx4 v[50:53],v4,s[86:87] offset:1024\nM a[144:147],a[72:75],a[104:107],a[144:147]\nds_read_b128 a[80:83],v6 offset:8192\n" \
"ds_read_b128 a[84:87],v6 offset:10240\nM a[160:163],a[72:75],a[112:115],a[160:163]\nM a[164:167],a[76:79],a[112:115],a[164:167]\nM a[172:175],a[76:79],a[116:119],a[172:175]\nglobal_load_dwordx4 v[30:33],v26,s[64:65] offset:0\nM a[168:171],a[72:75],a[116:119],a[168:171]\nM a[184:187],a[72:75],a[124:127],a[184:187]\nM a[188:191],a[76:79],a[124:127],a[188:191]\n" \
"M a[180:183],a[76:79],a[120:123],a[180:183]\nglobal_load_dwordx4 v[34:37],v26,s[64:65] offset:1024\nM a[176:179],a[72:75],a[120:123],a[176:179]\ns_waitcnt lgkmcnt(0)\nds_read_b128 a[72:75],v6 offset:4096\nds_read_b128 a[76:79],v6 offset:6144\nM v[128:131],a[24:27],a[32:35],v[128:131]\nM v[132:135],a[28:31],a[32:35],v[132:135]\n" \
"M v[140:143],a[28:31],a[36:39],v[140:143]\nbuffer_load_dwordx4 v21,s[92:95],s47 offen offset:128 lds\nM v[136:139],a[24:27],a[36:39],v[136:139]\ns_add_u32 m0,s22,16384\nM v[152:155],a[24:27],a[44:47],v[152:155]\ns_mov_b32 s47,1310720\nM v[156:159],a[28:31],a[44:47],v[156:159]\nM v[148:151],a[28:31],a[40:43],v[148:151]\n" \
"buffer_load_dwordx4 v21,s[32:35],s47 offen offset:0 lds\nM v[144:147],a[24:27],a[40:43],v[144:147]\ns_add_u32 m0,s22,17280\nM v[160:163],a[24:27],a[48:51],v[160:163]\nM v[164:167],a[28:31],a[48:51],v[164:167]\nM v[172:175],a[28:31],a[52:55],v[172:175]\nbuffer_load_dwordx4 v21,s[32:35],s47 offen offset:128 lds\nM v[168:171],a[24:27],a[52:55],v[168:171]\n" \
"s_add_u32 m0,s22,18432\nM v[184:187],a[24:27],a[60:63],v[184:187]\ns_waitcnt vmcnt(20) lgkmcnt(14)\nM v[188:191],a[28:31],a[60:63],v[188:191]\ns_barrier\nds_read_b64_tr_b16 a[96:97],v9 offset:49152\nM v[180:183],a[28:31],a[56:59],v[180:183]\nbuffer_load_dwordx4 v21,s[92:95],s47 offen offset:0 lds\n" \
"M v[176:179],a[24:27],a[56:59],v[176:179]\ns_add_u32 m0,s22,19328\nds_read_b64_tr_b16 a[98:99],v10 offset:49152\nM v[64:67],a[16:19],a[32:35],v[64:67]\nds_read_b64_tr_b16 a[100:101],v11 offset:49152\nds_read_b64_tr_b16 a[102:103],v12 offset:49152\nM v[68:71],a[20:23],a[32:35],v[68:71]\nds_read_b64_tr_b16 a[104:105],v9 offset:49664\n" \
"M v[76:79],a[20:23],a[36:39],v[76:79]\nbuffer_load_dwordx4 v21,s[92:95],s47 offen offset:128 lds\nM v[72:75],a[16:19],a[36:39],v[72:75]\ns_add_u32 m0,s50,65536\nds_read_b64_tr_b16 a[106:107],v10 offset:49664\nM v[88:91],a[16:19],a[44:47],v[88:91]\ns_mov_b32 s47,1572864\nds_read_b64_tr_b16 a[108:109],v11 offset:49664\n" \
"M v[92:95],a[20:23],a[44:47],v[92:95]\nds_read_b64_tr_b16 a[110:111],v12 offset:49664\nM v[84:87],a[20:23],a[40:43],v[84:87]\nds_read_b64_tr_b16 a[112:113],v9 offset:50176\nds_read_b64_tr_b16 a[114:115],v10 offset:50176\nM v[80:83],a[16:19],a[40:43],v[80:83]\nds_read_b64_tr_b16 a[116:117],v11 offset:50176\nM v[96:99],a[16:19],a[48:51],v[96:99]\n" \
"ds_read_b64_tr_b16 a[118:119],v12 offset:50176\nM v[100:103],a[20:23],a[48:51],v[100:103]\nds_read_b64_tr_b16 a[120:121],v9 offset:50688\nds_read_b64_tr_b16 a[122:123],v10 offset:50688\nM v[108:111],a[20:23],a[52:55],v[108:111]\nds_read_b64_tr_b16 a[124:125],v11 offset:50688\nM v[104:107],a[16:19],a[52:55],v[104:107]\ns_mov_b64 exec,0\n" \
"s_cmp_lt_u32 %2,2\ns_cbranch_scc1 LG8_%=\ns_branch LG9_%=\nLHA155V_%=:\ns_mov_b64 exec,-1\nds_read_b64_tr_b16 a[126:127],v12 offset:50688\nM v[120:123],a[16:19],a[60:63],v[120:123]\nM v[124:127],a[20:23],a[60:63],v[124:127]\n" \
"M v[116:119],a[20:23],a[56:59],v[116:119]\nM v[112:115],a[16:19],a[56:59],v[112:115]\nM v[192:195],a[0:3],a[32:35],v[192:195]\nM v[196:199],a[4:7],a[32:35],v[196:199]\nM v[204:207],a[4:7],a[36:39],v[204:207]\nM v[200:203],a[0:3],a[36:39],v[200:203]\nM v[216:219],a[0:3],a[44:47],v[216:219]\nbuffer_load_dwordx4 v13,s[28:31],s46 offen lds\n" \
"M v[220:223],a[4:7],a[44:47],v[220:223]\ns_add_u32 m0,s50,69632\nM v[212:215],a[4:7],a[40:43],v[212:215]\nM v[208:211],a[0:3],a[40:43],v[208:211]\nM v[224:227],a[0:3],a[48:51],v[224:227]\nbuffer_load_dwordx4 v14,s[28:31],s46 offen lds\nM v[228:231],a[4:7],a[48:51],v[228:231]\ns_add_u32 m0,s50,73728\n" \
"M v[236:239],a[4:7],a[52:55],v[236:239]\nM v[232:235],a[0:3],a[52:55],v[232:235]\nM v[248:251],a[0:3],a[60:63],v[248:251]\nbuffer_load_dwordx4 v15,s[28:31],s46 offen lds\nM v[252:255],a[4:7],a[60:63],v[252:255]\ns_add_u32 m0,s50,77824\nM v[244:247],a[4:7],a[56:59],v[244:247]\nM v[240:243],a[0:3],a[56:59],v[240:243]\n" \
"M a[128:131],a[8:11],a[32:35],a[128:131]\nbuffer_load_dwordx4 v16,s[28:31],s46 offen lds\nM a[132:135],a[12:15],a[32:35],a[132:135]\ns_add_u32 m0,s50,81920\nM a[140:143],a[12:15],a[36:39],a[140:143]\ns_mov_b32 s46,384\nM a[136:139],a[8:11],a[36:39],a[136:139]\nM a[152:155],a[8:11],a[44:47],a[152:155]\n" \
"buffer_load_dwordx4 v17,s[28:31],s46 offen lds\nM a[156:159],a[12:15],a[44:47],a[156:159]\ns_add_u32 m0,s50,86016\nM a[148:151],a[12:15],a[40:43],a[148:151]\nM a[144:147],a[8:11],a[40:43],a[144:147]\nM a[160:163],a[8:11],a[48:51],a[160:163]\nbuffer_load_dwordx4 v18,s[28:31],s46 offen lds\nM a[164:167],a[12:15],a[48:51],a[164:167]\n" \
"s_add_u32 m0,s50,90112\nM a[172:175],a[12:15],a[52:55],a[172:175]\nM a[168:171],a[8:11],a[52:55],a[168:171]\nM a[184:187],a[8:11],a[60:63],a[184:187]\nbuffer_load_dwordx4 v19,s[28:31],s46 offen lds\nM a[188:191],a[12:15],a[60:63],a[188:191]\ns_add_u32 m0,s50,94208\nM a[180:183],a[12:15],a[56:59],a[180:183]\n" \
"M a[176:179],a[8:11],a[56:59],a[176:179]\ns_waitcnt lgkmcnt(0)\nM v[128:131],a[88:91],a[96:99],v[128:131]\nbuffer_load_dwordx4 v20,s[28:31],s46 offen lds\nM v[132:135],a[92:95],a[96:99],v[132:135]\ns_add_u32 m0,s22,32768\nM v[140:143],a[92:95],a[100:103],v[140:143]\ns_waitcnt vmcnt(12) lgkmcnt(0)\n" \
"M v[136:139],a[88:91],a[100:103],v[136:139]\ns_mov_b32 s46,512\ns_barrier\nds_read_b64_tr_b16 a[32:33],v9 offset:0\nM v[152:155],a[88:91],a[108:111],v[152:155]\nbuffer_load_dwordx4 v21,s[32:35],s47 offen offset:0 lds\nM v[156:159],a[92:95],a[108:111],v[156:159]\ns_add_u32 m0,s22,33664\n" \
"ds_read_b64_tr_b16 a[34:35],v10 offset:0\nM v[148:151],a[92:95],a[104:107],v[148:151]\nds_read_b64_tr_b16 a[36:37],v11 offset:0\nds_read_b64_tr_b16 a[38:39],v12 offset:0\nM v[144:147],a[88:91],a[104:107],v[144:147]\nds_read_b64_tr_b16 a[40:41],v9 offset:512\nM v[160:163],a[88:91],a[112:115],v[160:163]\nbuffer_load_dwordx4 v21,s[32:35],s47 offen offset:128 lds\n" \
"M v[164:167],a[92:95],a[112:115],v[164:167]\ns_add_u32 m0,s22,34816\nds_read_b64_tr_b16 a[42:43],v10 offset:512\nM v[172:175],a[92:95],a[116:119],v[172:175]\nds_read_b64_tr_b16 a[44:45],v11 offset:512\nM v[168:171],a[88:91],a[116:119],v[168:171]\nds_read_b64_tr_b16 a[46:47],v12 offset:512\nM v[184:187],a[88:91],a[124:127],v[184:187]\n" \
"buffer_load_dwordx4 v21,s[92:95],s47 offen offset:0 lds\nM v[188:191],a[92:95],a[124:127],v[188:191]\ns_add_u32 m0,s22,35712\nds_read_b64_tr_b16 a[48:49],v9 offset:1024\nM v[180:183],a[92:95],a[120:123],v[180:183]\nds_read_b64_tr_b16 a[50:51],v10 offset:1024\nM v[176:179],a[88:91],a[120:123],v[176:179]\nds_read_b64_tr_b16 a[52:53],v11 offset:1024\n" \
"ds_read_b64_tr_b16 a[54:55],v12 offset:1024\nM v[64:67],a[80:83],a[96:99],v[64:67]\nbuffer_load_dwordx4 v21,s[92:95],s47 offen offset:128 lds\nM v[68:71],a[84:87],a[96:99],v[68:71]\ns_add_u32 m0,s22,49152\nM v[76:79],a[84:87],a[100:103],v[76:79]\ns_mov_b32 s47,1835008\nds_read_b64_tr_b16 a[56:57],v9 offset:1536\n" \
"ds_read_b64_tr_b16 a[58:59],v10 offset:1536\nM v[72:75],a[80:83],a[100:103],v[72:75]\nds_read_b64_tr_b16 a[60:61],v11 offset:1536\nM v[88:91],a[80:83],a[108:111],v[88:91]\nbuffer_load_dwordx4 v21,s[32:35],s47 offen offset:0 lds\nM v[92:95],a[84:87],a[108:111],v[92:95]\ns_add_u32 m0,s22,50048\nds_read_b64_tr_b16 a[62:63],v12 offset:1536\n" \
"M v[84:87],a[84:87],a[104:107],v[84:87]\nds_read_b128 a[24:27],v5 offset:45056\nM v[80:83],a[80:83],a[104:107],v[80:83]\nds_read_b128 a[28:31],v5 offset:47104\nM v[96:99],a[80:83],a[112:115],v[96:99]\nbuffer_load_dwordx4 v21,s[32:35],s47 offen offset:128 lds\nM v[100:103],a[84:87],a[112:115],v[100:103]\ns_add_u32 m0,s22,51200\n" \
"M v[108:111],a[84:87],a[116:119],v[108:111]\nds_read_b128 a[16:19],v5 offset:40960\nM v[104:107],a[80:83],a[116:119],v[104:107]\nds_read_b128 a[20:23],v5 offset:43008\nM v[120:123],a[80:83],a[124:127],v[120:123]\nbuffer_load_dwordx4 v21,s[92:95],s47 offen offset:0 lds\nM v[124:127],a[84:87],a[124:127],v[124:127]\ns_add_u32 m0,s22,52096\n" \
"ds_read_b128 a[0:3],v5 offset:32768\nM v[116:119],a[84:87],a[120:123],v[116:119]\nds_read_b128 a[4:7],v5 offset:34816\nM v[112:115],a[80:83],a[120:123],v[112:115]\nLG4_%=:\ns_cbranch_execz LG6_%=\nds_read_b128 a[8:11],v5 offset:36864\nM v[192:195],a[64:67],a[96:99],v[192:195]\n" \
"buffer_load_dwordx4 v21,s[92:95],s47 offen offset:128 lds\nM v[196:199],a[68:71],a[96:99],v[196:199]\ns_add_u32 m0,s50,0\nM v[204:207],a[68:71],a[100:103],v[204:207]\ns_mov_b32 s47,2097152\nds_read_b128 a[12:15],v5 offset:38912\nM v[200:203],a[64:67],a[100:103],v[200:203]\nds_read_b128 a[80:83],v6 offset:40960\n" \
"M v[216:219],a[64:67],a[108:111],v[216:219]\nbuffer_load_dwordx4 v13,s[28:31],s46 offen lds\nM v[220:223],a[68:71],a[108:111],v[220:223]\ns_add_u32 m0,s50,4096\nds_read_b128 a[84:87],v6 offset:43008\nM v[212:215],a[68:71],a[104:107],v[212:215]\nds_read_b128 a[88:91],v6 offset:45056\nM v[208:211],a[64:67],a[104:107],v[208:211]\n" \
"ds_read_b128 a[92:95],v6 offset:47104\nM v[224:227],a[64:67],a[112:115],v[224:227]\nbuffer_load_dwordx4 v14,s[28:31],s46 offen lds\nM v[228:231],a[68:71],a[112:115],v[228:231]\ns_add_u32 m0,s50,8192\nM v[236:239],a[68:71],a[116:119],v[236:239]\nM v[232:235],a[64:67],a[116:119],v[232:235]\nM v[248:251],a[64:67],a[124:127],v[248:251]\n" \
"M v[252:255],a[68:71],a[124:127],v[252:255]\nbuffer_load_dwordx4 v15,s[28:31],s46 offen lds\nM v[244:247],a[68:71],a[120:123],v[244:247]\ns_add_u32 m0,s50,12288\nM v[240:243],a[64:67],a[120:123],v[240:243]\nds_read_b128 a[64:67],v6 offset:32768\nds_read_b128 a[68:71],v6 offset:34816\nM a[128:131],a[72:75],a[96:99],a[128:131]\n" \
"M a[132:135],a[76:79],a[96:99],a[132:135]\nbuffer_load_dwordx4 v16,s[28:31],s46 offen lds\nM a[140:143],a[76:79],a[100:103],a[140:143]\ns_add_u32 m0,s50,16384\nM a[136:139],a[72:75],a[100:103],a[136:139]\ns_mov_b32 s46,512\nM a[152:155],a[72:75],a[108:111],a[152:155]\nM a[156:159],a[76:79],a[108:111],a[156:159]\n" \
"M a[148:151],a[76:79],a[104:107],a[148:151]\ns_waitcnt vmcnt(20) lgkmcnt(14)\nM a[144:147],a[72:75],a[104:107],a[144:147]\ns_barrier\nds_read_b64_tr_b16 a[96:97],v9 offset:16384\nM a[160:163],a[72:75],a[112:115],a[160:163]\nbuffer_load_dwordx4 v17,s[28:31],s46 offen lds\nM a[164:167],a[76:79],a[112:115],a[164:167]\n" \
"s_add_u32 m0,s50,20480\nds_read_b64_tr_b16 a[98:99],v10 offset:16384\nM a[172:175],a[76:79],a[116:119],a[172:175]\nds_read_b64_tr_b16 a[100:101],v11 offset:16384\nds_read_b64_tr_b16 a[102:103],v12 offset:16384\nM a[168:171],a[72:75],a[116:119],a[168:171]\nds_read_b64_tr_b16 a[104:105],v9 offset:16896\nM a[184:187],a[72:75],a[124:127],a[184:187]\n" \
"buffer_load_dwordx4 v18,s[28:31],s46 offen lds\nM a[188:191],a[76:79],a[124:127],a[188:191]\ns_add_u32 m0,s50,24576\nds_read_b64_tr_b16 a[106:107],v10 offset:16896\nM a[180:183],a[76:79],a[120:123],a[180:183]\nds_read_b64_tr_b16 a[108:109],v11 offset:16896\nM a[176:179],a[72:75],a[120:123],a[176:179]\ns_waitcnt lgkmcnt(5)\n" \
"ds_read_b128 a[72:75],v6 offset:36864\nM v[128:131],a[24:27],a[32:35],v[128:131]\nbuffer_load_dwordx4 v19,s[28:31],s46 offen lds\nM v[132:135],a[28:31],a[32:35],v[132:135]\ns_add_u32 m0,s50,28672\nds_read_b128 a[76:79],v6 offset:38912\nM v[140:143],a[28:31],a[36:39],v[140:143]\nds_read_b64_tr_b16 a[110:111],v12 offset:16896\n" \
"M v[136:139],a[24:27],a[36:39],v[136:139]\nds_read_b64_tr_b16 a[112:113],v9 offset:17408\nM v[152:155],a[24:27],a[44:47],v[152:155]\nbuffer_load_dwordx4 v20,s[28:31],s46 offen lds\nM v[156:159],a[28:31],a[44:47],v[156:159]\ns_add_u32 m0,s22,0\nds_read_b64_tr_b16 a[114:115],v10 offset:17408\nM v[148:151],a[28:31],a[40:43],v[148:151]\n" \
"s_mov_b32 s46,640\nds_read_b64_tr_b16 a[116:117],v11 offset:17408\nM v[144:147],a[24:27],a[40:43],v[144:147]\nds_read_b64_tr_b16 a[118:119],v12 offset:17408\nM v[160:163],a[24:27],a[48:51],v[160:163]\nbuffer_load_dwordx4 v21,s[32:35],s47 offen offset:0 lds\nM v[164:167],a[28:31],a[48:51],v[164:167]\ns_add_u32 m0,s22,896\n" \
"ds_read_b64_tr_b16 a[120:121],v9 offset:17920\nM v[172:175],a[28:31],a[52:55],v[172:175]\nds_read_b64_tr_b16 a[122:123],v10 offset:17920\nM v[168:171],a[24:27],a[52:55],v[168:171]\nds_read_b64_tr_b16 a[124:125],v11 offset:17920\nds_read_b64_tr_b16 a[126:127],v12 offset:17920\nM v[184:187],a[24:27],a[60:63],v[184:187]\nbuffer_load_dwordx4 v21,s[32:35],s47 offen offset:128 lds\n" \
"M v[188:191],a[28:31],a[60:63],v[188:191]\ns_add_u32 m0,s22,2048\nM v[180:183],a[28:31],a[56:59],v[180:183]\nM v[176:179],a[24:27],a[56:59],v[176:179]\nM v[64:67],a[16:19],a[32:35],v[64:67]\nM v[68:71],a[20:23],a[32:35],v[68:71]\nbuffer_load_dwordx4 v21,s[92:95],s47 offen offset:0 lds\nM v[76:79],a[20:23],a[36:39],v[76:79]\n" \
"s_add_u32 m0,s22,2944\nM v[72:75],a[16:19],a[36:39],v[72:75]\nM v[88:91],a[16:19],a[44:47],v[88:91]\nM v[92:95],a[20:23],a[44:47],v[92:95]\nbuffer_load_dwordx4 v21,s[92:95],s47 offen offset:128 lds\nM v[84:87],a[20:23],a[40:43],v[84:87]\ns_add_u32 m0,s22,16384\nM v[80:83],a[16:19],a[40:43],v[80:83]\n" \
"s_mov_b32 s47,2359296\nM v[96:99],a[16:19],a[48:51],v[96:99]\nM v[100:103],a[20:23],a[48:51],v[100:103]\nglobal_load_dwordx4 v[38:41],v26,s[64:65] offset:2048\nM v[108:111],a[20:23],a[52:55],v[108:111]\nM v[104:107],a[16:19],a[52:55],v[104:107]\nM v[120:123],a[16:19],a[60:63],v[120:123]\nM v[124:127],a[20:23],a[60:63],v[124:127]\n" \
"global_load_dwordx4 v[42:45],v26,s[64:65] offset:3072\nM v[116:119],a[20:23],a[56:59],v[116:119]\nM v[112:115],a[16:19],a[56:59],v[112:115]\nM v[192:195],a[0:3],a[32:35],v[192:195]\nM v[196:199],a[4:7],a[32:35],v[196:199]\ns_mov_b64 exec,0xff\nglobal_load_dword v0,v1,s[84:85]\ns_mov_b64 exec,-1\n" \
"M v[204:207],a[4:7],a[36:39],v[204:207]\ns_add_u32 s84,s84,524288\ns_addc_u32 s85,s85,0\nM v[200:203],a[0:3],a[36:39],v[200:203]\nglobal_load_dwordx4 a[230:233],v27,s[70:71] offset:0\nM v[216:219],a[0:3],a[44:47],v[216:219]\nM v[220:223],a[4:7],a[44:47],v[220:223]\nM v[212:215],a[4:7],a[40:43],v[212:215]\n" \
"M v[208:211],a[0:3],a[40:43],v[208:211]\nglobal_load_dwordx4 a[234:237],v27,s[70:71] offset:16\nM v[224:227],a[0:3],a[48:51],v[224:227]\ns_mov_b32 s70,4096\nM v[228:231],a[4:7],a[48:51],v[228:231]\ns_mov_b32 s71,0x00020000\nM v[236:239],a[4:7],a[52:55],v[236:239]\nM v[232:235],a[0:3],a[52:55],v[232:235]\n" \
"buffer_load_dwordx2 a[192:193],v54,s[40:43],0 offen offset:0 nt\nM v[248:251],a[0:3],a[60:63],v[248:251]\nM v[252:255],a[4:7],a[60:63],v[252:255]\nM v[244:247],a[4:7],a[56:59],v[244:247]\nM v[240:243],a[0:3],a[56:59],v[240:243]\nbuffer_load_dwordx2 a[194:195],v54,s[40:43],0 offen offset:2048 nt\nM a[128:131],a[8:11],a[32:35],a[128:131]\nM a[132:135],a[12:15],a[32:35],a[132:135]\n" \
"M a[140:143],a[12:15],a[36:39],a[140:143]\nM a[136:139],a[8:11],a[36:39],a[136:139]\nbuffer_load_dwordx2 a[196:197],v55,s[40:43],0 offen offset:0 nt\nM a[152:155],a[8:11],a[44:47],a[152:155]\nM a[156:159],a[12:15],a[44:47],a[156:159]\nM a[148:151],a[12:15],a[40:43],a[148:151]\ns_waitcnt vmcnt(24) lgkmcnt(0)\nM a[144:147],a[8:11],a[40:43],a[144:147]\n" \
"s_barrier\nds_read_b64_tr_b16 a[32:33],v9 offset:32768\nM a[160:163],a[8:11],a[48:51],a[160:163]\nbuffer_load_dwordx2 a[198:199],v59,s[40:43],s52 offen offset:0 nt\nM a[164:167],a[12:15],a[48:51],a[164:167]\nds_read_b64_tr_b16 a[34:35],v10 offset:32768\nds_read_b64_tr_b16 a[36:37],v11 offset:32768\nM a[172:175],a[12:15],a[52:55],a[172:175]\n" \
"ds_read_b64_tr_b16 a[38:39],v12 offset:32768\nM a[168:171],a[8:11],a[52:55],a[168:171]\nds_read_b64_tr_b16 a[40:41],v9 offset:33280\nds_read_b64_tr_b16 a[42:43],v10 offset:33280\nM a[184:187],a[8:11],a[60:63],a[184:187]\nbuffer_load_dwordx2 a[214:215],v59,s[80:83],s52 offen offset:0 nt\nM a[188:191],a[12:15],a[60:63],a[188:191]\nds_read_b64_tr_b16 a[44:45],v11 offset:33280\n" \
"ds_read_b64_tr_b16 a[46:47],v12 offset:33280\nM a[180:183],a[12:15],a[56:59],a[180:183]\nds_read_b64_tr_b16 a[48:49],v9 offset:33792\nM a[176:179],a[8:11],a[56:59],a[176:179]\nds_read_b64_tr_b16 a[50:51],v10 offset:33792\nds_read_b64_tr_b16 a[52:53],v11 offset:33792\nM v[128:131],a[88:91],a[96:99],v[128:131]\nbuffer_load_dwordx2 a[200:201],v59,s[40:43],s52 offen offset:2048 nt\n" \
"M v[132:135],a[92:95],a[96:99],v[132:135]\nds_read_b64_tr_b16 a[54:55],v12 offset:33792\nds_read_b64_tr_b16 a[56:57],v9 offset:34304\nM v[140:143],a[92:95],a[100:103],v[140:143]\nds_read_b64_tr_b16 a[58:59],v10 offset:34304\nM v[136:139],a[88:91],a[100:103],v[136:139]\nds_read_b64_tr_b16 a[60:61],v11 offset:34304\nds_read_b64_tr_b16 a[62:63],v12 offset:34304\n" \
"M v[152:155],a[88:91],a[108:111],v[152:155]\nbuffer_load_dwordx2 a[216:217],v59,s[80:83],s52 offen offset:2048 nt\nM v[156:159],a[92:95],a[108:111],v[156:159]\ns_mov_b32 s52,4096\nds_read_b128 a[24:27],v7 offset:12288\nds_read_b128 a[28:31],v7 offset:14336\nM v[148:151],a[92:95],a[104:107],v[148:151]\nds_read_b128 a[16:19],v7 offset:8192\n" \
"M v[144:147],a[88:91],a[104:107],v[144:147]\nds_read_b128 a[20:23],v7 offset:10240\nM v[160:163],a[88:91],a[112:115],v[160:163]\nbuffer_load_dwordx2 a[202:203],v59,s[40:43],s52 offen offset:0 nt\nM v[164:167],a[92:95],a[112:115],v[164:167]\nds_read_b128 a[0:3],v7 offset:0\nds_read_b128 a[4:7],v7 offset:2048\nM v[172:175],a[92:95],a[116:119],v[172:175]\n" \
"ds_read_b128 a[8:11],v7 offset:4096\nM v[168:171],a[88:91],a[116:119],v[168:171]\nds_read_b128 a[12:15],v7 offset:6144\nM v[184:187],a[88:91],a[124:127],v[184:187]\nbuffer_load_dwordx2 a[218:219],v59,s[80:83],s52 offen offset:0 nt\nM v[188:191],a[92:95],a[124:127],v[188:191]\nM v[180:183],a[92:95],a[120:123],v[180:183]\nM v[176:179],a[88:91],a[120:123],v[176:179]\n" \
"ds_read_b128 a[88:91],v8 offset:12288\nds_read_b128 a[92:95],v8 offset:14336\nM v[64:67],a[80:83],a[96:99],v[64:67]\nbuffer_load_dwordx2 a[204:205],v59,s[40:43],s52 offen offset:2048 nt\nM v[68:71],a[84:87],a[96:99],v[68:71]\nM v[76:79],a[84:87],a[100:103],v[76:79]\nM v[72:75],a[80:83],a[100:103],v[72:75]\nM v[88:91],a[80:83],a[108:111],v[88:91]\n" \
"buffer_load_dwordx2 a[220:221],v59,s[80:83],s52 offen offset:2048 nt\nM v[92:95],a[84:87],a[108:111],v[92:95]\ns_mov_b32 s52,8192\nM v[84:87],a[84:87],a[104:107],v[84:87]\nM v[80:83],a[80:83],a[104:107],v[80:83]\nM v[96:99],a[80:83],a[112:115],v[96:99]\nM v[100:103],a[84:87],a[112:115],v[100:103]\nM v[108:111],a[84:87],a[116:119],v[108:111]\n" \
"M v[104:107],a[80:83],a[116:119],v[104:107]\nM v[120:123],a[80:83],a[124:127],v[120:123]\nM v[124:127],a[84:87],a[124:127],v[124:127]\nM v[116:119],a[84:87],a[120:123],v[116:119]\nM v[112:115],a[80:83],a[120:123],v[112:115]\nds_read_b128 a[80:83],v8 offset:8192\nds_read_b128 a[84:87],v8 offset:10240\nM v[192:195],a[64:67],a[96:99],v[192:195]\n" \
"M v[196:199],a[68:71],a[96:99],v[196:199]\nM v[204:207],a[68:71],a[100:103],v[204:207]\nM v[200:203],a[64:67],a[100:103],v[200:203]\nM v[216:219],a[64:67],a[108:111],v[216:219]\nM v[220:223],a[68:71],a[108:111],v[220:223]\nM v[212:215],a[68:71],a[104:107],v[212:215]\nM v[208:211],a[64:67],a[104:107],v[208:211]\nM v[224:227],a[64:67],a[112:115],v[224:227]\n" \
"M v[228:231],a[68:71],a[112:115],v[228:231]\nM v[236:239],a[68:71],a[116:119],v[236:239]\nM v[232:235],a[64:67],a[116:119],v[232:235]\nM v[248:251],a[64:67],a[124:127],v[248:251]\nM v[252:255],a[68:71],a[124:127],v[252:255]\nM v[244:247],a[68:71],a[120:123],v[244:247]\nM v[240:243],a[64:67],a[120:123],v[240:243]\nds_read_b128 a[64:67],v8 offset:0\n" \
"ds_read_b128 a[68:71],v8 offset:2048\nM a[128:131],a[72:75],a[96:99],a[128:131]\nM a[132:135],a[76:79],a[96:99],a[132:135]\nM a[140:143],a[76:79],a[100:103],a[140:143]\nM a[136:139],a[72:75],a[100:103],a[136:139]\nM a[152:155],a[72:75],a[108:111],a[152:155]\nM a[156:159],a[76:79],a[108:111],a[156:159]\nM a[148:151],a[76:79],a[104:107],a[148:151]\n" \
"s_waitcnt vmcnt(28) lgkmcnt(14)\nM a[144:147],a[72:75],a[104:107],a[144:147]\ns_barrier\nds_read_b64_tr_b16 a[96:97],v9 offset:49152\nM a[160:163],a[72:75],a[112:115],a[160:163]\nds_read_b64_tr_b16 a[98:99],v10 offset:49152\nds_read_b64_tr_b16 a[100:101],v11 offset:49152\nM a[164:167],a[76:79],a[112:115],a[164:167]\n" \
"ds_read_b64_tr_b16 a[102:103],v12 offset:49152\nM a[172:175],a[76:79],a[116:119],a[172:175]\nds_read_b64_tr_b16 a[104:105],v9 offset:49664\nds_read_b64_tr_b16 a[106:107],v10 offset:49664\nM a[168:171],a[72:75],a[116:119],a[168:171]\nds_read_b64_tr_b16 a[108:109],v11 offset:49664\nM a[184:187],a[72:75],a[124:127],a[184:187]\nds_read_b64_tr_b16 a[110:111],v12 offset:49664\n" \
"M a[188:191],a[76:79],a[124:127],a[188:191]\nds_read_b64_tr_b16 a[112:113],v9 offset:50176\nds_read_b64_tr_b16 a[114:115],v10 offset:50176\nM a[180:183],a[76:79],a[120:123],a[180:183]\nds_read_b64_tr_b16 a[116:117],v11 offset:50176\nM a[176:179],a[72:75],a[120:123],a[176:179]\ns_waitcnt lgkmcnt(7)\nds_read_b128 a[72:75],v8 offset:4096\n" \
"M v[128:131],a[24:27],a[32:35],v[128:131]\nds_read_b128 a[76:79],v8 offset:6144\nM v[132:135],a[28:31],a[32:35],v[132:135]\nbuffer_load_dwordx4 v21,s[32:35],s47 offen offset:0 lds\nM v[140:143],a[28:31],a[36:39],v[140:143]\ns_add_u32 m0,s22,17280\nds_read_b64_tr_b16 a[118:119],v12 offset:50176\nM v[136:139],a[24:27],a[36:39],v[136:139]\n" \
"ds_read_b64_tr_b16 a[120:121],v9 offset:50688\nM v[152:155],a[24:27],a[44:47],v[152:155]\nds_read_b64_tr_b16 a[122:123],v10 offset:50688\nM v[156:159],a[28:31],a[44:47],v[156:159]\nbuffer_load_dwordx4 v21,s[32:35],s47 offen offset:128 lds\nM v[148:151],a[28:31],a[40:43],v[148:151]\ns_add_u32 m0,s22,18432\nds_read_b64_tr_b16 a[124:125],v11 offset:50688\n" \
"M v[144:147],a[24:27],a[40:43],v[144:147]\nds_read_b64_tr_b16 a[126:127],v12 offset:50688\nM v[160:163],a[24:27],a[48:51],v[160:163]\nM v[164:167],a[28:31],a[48:51],v[164:167]\nbuffer_load_dwordx4 v21,s[92:95],s47 offen offset:0 lds\nM v[172:175],a[28:31],a[52:55],v[172:175]\ns_add_u32 m0,s22,19328\nM v[168:171],a[24:27],a[52:55],v[168:171]\n" \
"M v[184:187],a[24:27],a[60:63],v[184:187]\nM v[188:191],a[28:31],a[60:63],v[188:191]\nbuffer_load_dwordx4 v21,s[92:95],s47 offen offset:128 lds\nM v[180:183],a[28:31],a[56:59],v[180:183]\ns_add_u32 m0,s50,32768\nM v[176:179],a[24:27],a[56:59],v[176:179]\ns_mov_b32 s47,2621440\nM v[64:67],a[16:19],a[32:35],v[64:67]\n" \
"M v[68:71],a[20:23],a[32:35],v[68:71]\nbuffer_load_dwordx4 v13,s[28:31],s46 offen lds\nM v[76:79],a[20:23],a[36:39],v[76:79]\ns_add_u32 m0,s50,36864\nM v[72:75],a[16:19],a[36:39],v[72:75]\nM v[88:91],a[16:19],a[44:47],v[88:91]\nM v[92:95],a[20:23],a[44:47],v[92:95]\nbuffer_load_dwordx4 v14,s[28:31],s46 offen lds\n" \
"M v[84:87],a[20:23],a[40:43],v[84:87]\ns_add_u32 m0,s50,40960\nM v[80:83],a[16:19],a[40:43],v[80:83]\nM v[96:99],a[16:19],a[48:51],v[96:99]\nM v[100:103],a[20:23],a[48:51],v[100:103]\nbuffer_load_dwordx4 v15,s[28:31],s46 offen lds\nM v[108:111],a[20:23],a[52:55],v[108:111]\ns_add_u32 m0,s50,45056\n" \
"M v[104:107],a[16:19],a[52:55],v[104:107]\nM v[120:123],a[16:19],a[60:63],v[120:123]\nM v[124:127],a[20:23],a[60:63],v[124:127]\nbuffer_load_dwordx4 v16,s[28:31],s46 offen lds\nM v[116:119],a[20:23],a[56:59],v[116:119]\ns_add_u32 m0,s50,49152\nM v[112:115],a[16:19],a[56:59],v[112:115]\ns_mov_b32 s46,640\n" \
"M v[192:195],a[0:3],a[32:35],v[192:195]\nM v[196:199],a[4:7],a[32:35],v[196:199]\nbuffer_load_dwordx4 v17,s[28:31],s46 offen lds\nM v[204:207],a[4:7],a[36:39],v[204:207]\ns_add_u32 m0,s50,53248\nM v[200:203],a[0:3],a[36:39],v[200:203]\nM v[216:219],a[0:3],a[44:47],v[216:219]\nM v[220:223],a[4:7],a[44:47],v[220:223]\n" \
"buffer_load_dwordx4 v18,s[28:31],s46 offen lds\nM v[212:215],a[4:7],a[40:43],v[212:215]\ns_add_u32 m0,s50,57344\nM v[208:211],a[0:3],a[40:43],v[208:211]\nM v[224:227],a[0:3],a[48:51],v[224:227]\nM v[228:231],a[4:7],a[48:51],v[228:231]\nbuffer_load_dwordx4 v19,s[28:31],s46 offen lds\nM v[236:239],a[4:7],a[52:55],v[236:239]\n" \
"s_add_u32 m0,s50,61440\nM v[232:235],a[0:3],a[52:55],v[232:235]\nM v[248:251],a[0:3],a[60:63],v[248:251]\nM v[252:255],a[4:7],a[60:63],v[252:255]\nbuffer_load_dwordx4 v20,s[28:31],s46 offen lds\nM v[244:247],a[4:7],a[56:59],v[244:247]\ns_add_u32 m0,s22,32768\nM v[240:243],a[0:3],a[56:59],v[240:243]\n" \
"s_mov_b32 s46,768\nM a[128:131],a[8:11],a[32:35],a[128:131]\nM a[132:135],a[12:15],a[32:35],a[132:135]\nbuffer_load_dwordx4 v21,s[32:35],s47 offen offset:0 lds\nM a[140:143],a[12:15],a[36:39],a[140:143]\ns_add_u32 m0,s22,33664\nM a[136:139],a[8:11],a[36:39],a[136:139]\nM a[152:155],a[8:11],a[44:47],a[152:155]\n" \
"M a[156:159],a[12:15],a[44:47],a[156:159]\nbuffer_load_dwordx4 v21,s[32:35],s47 offen offset:128 lds\nM a[148:151],a[12:15],a[40:43],a[148:151]\ns_waitcnt vmcnt(30) lgkmcnt(0)\nM a[144:147],a[8:11],a[40:43],a[144:147]\ns_add_u32 m0,s22,34816\ns_barrier\nds_read_b64_tr_b16 a[32:33],v9 offset:0\n" \
"M a[160:163],a[8:11],a[48:51],a[160:163]\nds_read_b64_tr_b16 a[34:35],v10 offset:0\nds_read_b64_tr_b16 a[36:37],v11 offset:0\nM a[164:167],a[12:15],a[48:51],a[164:167]\nbuffer_load_dwordx4 v21,s[92:95],s47 offen offset:0 lds\nM a[172:175],a[12:15],a[52:55],a[172:175]\ns_add_u32 m0,s22,35712\nds_read_b64_tr_b16 a[38:39],v12 offset:0\n" \
"M a[168:171],a[8:11],a[52:55],a[168:171]\nds_read_b64_tr_b16 a[40:41],v9 offset:512\nM a[184:187],a[8:11],a[60:63],a[184:187]\nds_read_b64_tr_b16 a[42:43],v10 offset:512\nM a[188:191],a[12:15],a[60:63],a[188:191]\nbuffer_load_dwordx4 v21,s[92:95],s47 offen offset:128 lds\nM a[180:183],a[12:15],a[56:59],a[180:183]\ns_add_u32 m0,s22,49152\n" \
"ds_read_b64_tr_b16 a[44:45],v11 offset:512\nM a[176:179],a[8:11],a[56:59],a[176:179]\ns_mov_b32 s47,2883584\nds_read_b64_tr_b16 a[46:47],v12 offset:512\nM v[128:131],a[88:91],a[96:99],v[128:131]\nds_read_b64_tr_b16 a[48:49],v9 offset:1024\nds_read_b64_tr_b16 a[50:51],v10 offset:1024\nM v[132:135],a[92:95],a[96:99],v[132:135]\n" \
"buffer_load_dwordx4 v21,s[32:35],s47 offen offset:0 lds\nM v[140:143],a[92:95],a[100:103],v[140:143]\ns_add_u32 m0,s22,50048\nM v[136:139],a[88:91],a[100:103],v[136:139]\nds_read_b64_tr_b16 a[52:53],v11 offset:1024\nds_read_b64_tr_b16 a[54:55],v12 offset:1024\nM v[152:155],a[88:91],a[108:111],v[152:155]\nds_read_b64_tr_b16 a[56:57],v9 offset:1536\n" \
"M v[156:159],a[92:95],a[108:111],v[156:159]\nbuffer_load_dwordx4 v21,s[32:35],s47 offen offset:128 lds\nM v[148:151],a[92:95],a[104:107],v[148:151]\ns_add_u32 m0,s22,51200\nds_read_b64_tr_b16 a[58:59],v10 offset:1536\nM v[144:147],a[88:91],a[104:107],v[144:147]\ns_waitcnt vmcnt(32)\nds_read_b64_tr_b16 a[60:61],v11 offset:1536\n" \
"v_lshlrev_b32 v25,16,v46\nM v[160:163],a[88:91],a[112:115],v[160:163]\nds_read_b64_tr_b16 a[62:63],v12 offset:1536\nv_lshlrev_b32 v26,16,v30\nM v[164:167],a[92:95],a[112:115],v[164:167]\nbuffer_load_dwordx4 v21,s[92:95],s47 offen offset:0 lds\nM v[172:175],a[92:95],a[116:119],v[172:175]\ns_add_u32 m0,s22,52096\n" \
"ds_read_b128 a[24:27],v5 offset:12288\nv_and_b32 v46,0xffff0000,v46\nM v[168:171],a[88:91],a[116:119],v[168:171]\nds_read_b128 a[28:31],v5 offset:14336\nv_mul_f32 v23,v26,v25\nM v[184:187],a[88:91],a[124:127],v[184:187]\nds_read_b128 a[16:19],v5 offset:8192\nv_and_b32 v30,0xffff0000,v30\n" \
"M v[188:191],a[92:95],a[124:127],v[188:191]\nds_read_b128 a[20:23],v5 offset:10240\nv_lshlrev_b32 v26,16,v38\nM v[180:183],a[92:95],a[120:123],v[180:183]\nbuffer_load_dwordx4 v21,s[92:95],s47 offen offset:128 lds\nM v[176:179],a[88:91],a[120:123],v[176:179]\ns_add_u32 m0,s50,65536\nv_fmac_f32 v23,v30,v46\n" \
"v_mul_f32 v24,v26,v25\nM v[64:67],a[80:83],a[96:99],v[64:67]\ns_mov_b32 s47,3145728\nds_read_b128 a[0:3],v5 offset:0\nv_lshlrev_b32 v25,16,v47\nM v[68:71],a[84:87],a[96:99],v[68:71]\nds_read_b128 a[4:7],v5 offset:2048\nv_lshlrev_b32 v26,16,v31\n" \
"M v[76:79],a[84:87],a[100:103],v[76:79]\nbuffer_load_dwordx4 v13,s[28:31],s46 offen lds\nM v[72:75],a[80:83],a[100:103],v[72:75]\ns_add_u32 m0,s50,69632\nds_read_b128 a[8:11],v5 offset:4096\nv_and_b32 v38,0xffff0000,v38\nM v[88:91],a[80:83],a[108:111],v[88:91]\nds_read_b128 a[12:15],v5 offset:6144\n" \
"v_fmac_f32 v23,v26,v25\nM v[92:95],a[84:87],a[108:111],v[92:95]\nds_read_b128 a[88:91],v6 offset:12288\nv_fmac_f32 v24,v38,v46\nM v[84:87],a[84:87],a[104:107],v[84:87]\nds_read_b128 a[92:95],v6 offset:14336\nv_and_b32 v47,0xffff0000,v47\nM v[80:83],a[80:83],a[104:107],v[80:83]\n" \
"buffer_load_dwordx4 v14,s[28:31],s46 offen lds\nM v[96:99],a[80:83],a[112:115],v[96:99]\ns_add_u32 m0,s50,73728\nv_and_b32 v31,0xffff0000,v31\nv_lshlrev_b32 v26,16,v39\nM v[100:103],a[84:87],a[112:115],v[100:103]\nv_fmac_f32 v23,v31,v47\nv_fmac_f32 v24,v26,v25\n" \
"M v[108:111],a[84:87],a[116:119],v[108:111]\nv_lshlrev_b32 v25,16,v48\nv_lshlrev_b32 v26,16,v32\nM v[104:107],a[80:83],a[116:119],v[104:107]\nbuffer_load_dwordx4 v15,s[28:31],s46 offen lds\nM v[120:123],a[80:83],a[124:127],v[120:123]\ns_add_u32 m0,s50,77824\nv_and_b32 v39,0xffff0000,v39\n" \
"v_fmac_f32 v23,v26,v25\nM v[124:127],a[84:87],a[124:127],v[124:127]\nv_fmac_f32 v24,v39,v47\nv_and_b32 v48,0xffff0000,v48\nM v[116:119],a[84:87],a[120:123],v[116:119]\nv_and_b32 v32,0xffff0000,v32\nv_lshlrev_b32 v26,16,v40\nM v[112:115],a[80:83],a[120:123],v[112:115]\n" \
"ds_read_b128 a[80:83],v6 offset:8192\nds_read_b128 a[84:87],v6 offset:10240\nM v[192:195],a[64:67],a[96:99],v[192:195]\nbuffer_load_dwordx4 v16,s[28:31],s46 offen lds\nM v[196:199],a[68:71],a[96:99],v[196:199]\ns_add_u32 m0,s50,81920\nv_fmac_f32 v23,v32,v48\nv_fmac_f32 v24,v26,v25\n" \
"M v[204:207],a[68:71],a[100:103],v[204:207]\ns_mov_b32 s46,768\nv_lshlrev_b32 v25,16,v49\nv_lshlrev_b32 v26,16,v33\nM v[200:203],a[64:67],a[100:103],v[200:203]\nv_and_b32 v40,0xffff0000,v40\nv_fmac_f32 v23,v26,v25\nM v[216:219],a[64:67],a[108:111],v[216:219]\n" \
"buffer_load_dwordx4 v17,s[28:31],s46 offen lds\nM v[220:223],a[68:71],a[108:111],v[220:223]\ns_add_u32 m0,s50,86016\nv_fmac_f32 v24,v40,v48\nv_and_b32 v49,0xffff0000,v49\nM v[212:215],a[68:71],a[104:107],v[212:215]\nv_and_b32 v33,0xffff0000,v33\nv_lshlrev_b32 v26,16,v41\n" \
"M v[208:211],a[64:67],a[104:107],v[208:211]\nv_fmac_f32 v23,v33,v49\nv_fmac_f32 v24,v26,v25\nM v[224:227],a[64:67],a[112:115],v[224:227]\nbuffer_load_dwordx2 a[206:207],v59,s[40:43],s52 offen offset:0 nt\nM v[228:231],a[68:71],a[112:115],v[228:231]\nv_lshlrev_b32 v25,16,v50\nv_lshlrev_b32 v26,16,v34\n" \
"M v[236:239],a[68:71],a[116:119],v[236:239]\nv_and_b32 v41,0xffff0000,v41\nv_fmac_f32 v23,v26,v25\nM v[232:235],a[64:67],a[116:119],v[232:235]\nLG5_%=:\ns_cbranch_execz LAV_%=\nv_fmac_f32 v24,v41,v49\nv_and_b32 v50,0xffff0000,v50\n" \
"M v[248:251],a[64:67],a[124:127],v[248:251]\nbuffer_load_dwordx2 a[222:223],v59,s[80:83],s52 offen offset:0 nt\nM v[252:255],a[68:71],a[124:127],v[252:255]\nv_and_b32 v34,0xffff0000,v34\nv_lshlrev_b32 v26,16,v42\nM v[244:247],a[68:71],a[120:123],v[244:247]\nv_fmac_f32 v23,v34,v50\nv_fmac_f32 v24,v26,v25\n" \
"M v[240:243],a[64:67],a[120:123],v[240:243]\nds_read_b128 a[64:67],v6 offset:0\nds_read_b128 a[68:71],v6 offset:2048\nM a[128:131],a[72:75],a[96:99],a[128:131]\nbuffer_load_dwordx2 a[208:209],v59,s[40:43],s52 offen offset:2048 nt\nM a[132:135],a[76:79],a[96:99],a[132:135]\nv_lshlrev_b32 v25,16,v51\nv_lshlrev_b32 v26,16,v35\n" \
"M a[140:143],a[76:79],a[100:103],a[140:143]\nv_and_b32 v42,0xffff0000,v42\nv_fmac_f32 v23,v26,v25\nM a[136:139],a[72:75],a[100:103],a[136:139]\nv_fmac_f32 v24,v42,v50\nv_and_b32 v51,0xffff0000,v51\nM a[152:155],a[72:75],a[108:111],a[152:155]\nbuffer_load_dwordx2 a[224:225],v59,s[80:83],s52 offen offset:2048 nt\n" \
"M a[156:159],a[76:79],a[108:111],a[156:159]\ns_mov_b32 s52,12288\nv_and_b32 v35,0xffff0000,v35\nv_lshlrev_b32 v26,16,v43\nM a[148:151],a[76:79],a[104:107],a[148:151]\ns_waitcnt vmcnt(25) lgkmcnt(14)\nv_fmac_f32 v23,v35,v51\nv_fmac_f32 v24,v26,v25\n" \
"M a[144:147],a[72:75],a[104:107],a[144:147]\ns_barrier\nds_read_b64_tr_b16 a[96:97],v9 offset:16384\nM a[160:163],a[72:75],a[112:115],a[160:163]\nbuffer_load_dwordx2 a[210:211],v59,s[40:43],s52 offen offset:0 nt\nM a[164:167],a[76:79],a[112:115],a[164:167]\nds_read_b64_tr_b16 a[98:99],v10 offset:16384\nds_read_b64_tr_b16 a[100:101],v11 offset:16384\n" \
"M a[172:175],a[76:79],a[116:119],a[172:175]\nds_read_b64_tr_b16 a[102:103],v12 offset:16384\nv_lshlrev_b32 v25,16,v52\nM a[168:171],a[72:75],a[116:119],a[168:171]\nds_read_b64_tr_b16 a[104:105],v9 offset:16896\nds_read_b64_tr_b16 a[106:107],v10 offset:16896\nM a[184:187],a[72:75],a[124:127],a[184:187]\nbuffer_load_dwordx2 a[226:227],v59,s[80:83],s52 offen offset:0 nt\n" \
"M a[188:191],a[76:79],a[124:127],a[188:191]\nds_read_b64_tr_b16 a[108:109],v11 offset:16896\nds_read_b64_tr_b16 a[110:111],v12 offset:16896\nM a[180:183],a[76:79],a[120:123],a[180:183]\ns_waitcnt vmcnt(31)\nds_read_b64_tr_b16 a[112:113],v9 offset:17408\nv_lshlrev_b32 v26,16,v36\nM a[176:179],a[72:75],a[120:123],a[176:179]\n" \
"s_waitcnt lgkmcnt(6)\nds_read_b128 a[72:75],v6 offset:4096\nv_and_b32 v43,0xffff0000,v43\nM v[128:131],a[24:27],a[32:35],v[128:131]\nbuffer_load_dwordx2 a[212:213],v59,s[40:43],s52 offen offset:2048 nt\nM v[132:135],a[28:31],a[32:35],v[132:135]\nds_read_b128 a[76:79],v6 offset:6144\nds_read_b64_tr_b16 a[114:115],v10 offset:17408\n" \
"M v[140:143],a[28:31],a[36:39],v[140:143]\nds_read_b64_tr_b16 a[116:117],v11 offset:17408\nv_fmac_f32 v23,v26,v25\nM v[136:139],a[24:27],a[36:39],v[136:139]\nds_read_b64_tr_b16 a[118:119],v12 offset:17408\nv_fmac_f32 v24,v43,v51\nM v[152:155],a[24:27],a[44:47],v[152:155]\nbuffer_load_dwordx2 a[228:229],v59,s[80:83],s52 offen offset:2048 nt\n" \
"M v[156:159],a[28:31],a[44:47],v[156:159]\nds_read_b64_tr_b16 a[120:121],v9 offset:17920\nds_read_b64_tr_b16 a[122:123],v10 offset:17920\nM v[148:151],a[28:31],a[40:43],v[148:151]\nds_read_b64_tr_b16 a[124:125],v11 offset:17920\nv_and_b32 v52,0xffff0000,v52\nM v[144:147],a[24:27],a[40:43],v[144:147]\nds_read_b64_tr_b16 a[126:127],v12 offset:17920\n" \
"v_and_b32 v36,0xffff0000,v36\nM v[160:163],a[24:27],a[48:51],v[160:163]\ns_mov_b64 exec,0xff\nglobal_load_dword v0,v1,s[84:85]\ns_mov_b64 exec,-1\nM v[164:167],a[28:31],a[48:51],v[164:167]\ns_add_u32 s84,s84,524288\ns_addc_u32 s85,s85,0\n" \
"v_lshlrev_b32 v26,16,v44\nv_fmac_f32 v23,v36,v52\nM v[172:175],a[28:31],a[52:55],v[172:175]\ns_mov_b64 exec,0xff\nglobal_load_dword v0,v1,s[84:85]\ns_mov_b64 exec,-1\nM v[168:171],a[24:27],a[52:55],v[168:171]\nv_fmac_f32 v24,v26,v25\n" \
"v_lshlrev_b32 v25,16,v53\nM v[184:187],a[24:27],a[60:63],v[184:187]\ns_mov_b64 exec,0xff\nglobal_load_dword v0,v1,s[76:77]\ns_mov_b64 exec,-1\nM v[188:191],a[28:31],a[60:63],v[188:191]\nv_lshlrev_b32 v26,16,v37\nv_and_b32 v44,0xffff0000,v44\n" \
"M v[180:183],a[28:31],a[56:59],v[180:183]\ns_mov_b64 exec,0xff00\nglobal_load_dword v0,v1,s[76:77]\ns_mov_b64 exec,-1\nM v[176:179],a[24:27],a[56:59],v[176:179]\ns_add_u32 s76,s76,2097152\ns_addc_u32 s77,s77,0\nv_fmac_f32 v23,v26,v25\n" \
"v_fmac_f32 v24,v44,v52\nM v[64:67],a[16:19],a[32:35],v[64:67]\nv_lshlrev_b32 v26,16,v45\nv_and_b32 v53,0xffff0000,v53\nM v[68:71],a[20:23],a[32:35],v[68:71]\nv_and_b32 v45,0xffff0000,v45\nv_fmac_f32 v24,v26,v25\nM v[76:79],a[20:23],a[36:39],v[76:79]\n" \
"v_fmac_f32 v24,v45,v53\nv_and_b32 v37,0xffff0000,v37\nM v[72:75],a[16:19],a[36:39],v[72:75]\nv_accvgpr_read_b32 v45,a231\nv_fmac_f32 v23,v37,v53\nM v[88:91],a[16:19],a[44:47],v[88:91]\nv_accvgpr_read_b32 v49,a233\nv_lshlrev_b32 v44,16,v45\n" \
"M v[92:95],a[20:23],a[44:47],v[92:95]\nv_accvgpr_read_b32 v36,a192\nv_accvgpr_read_b32 v43,a230\nM v[84:87],a[20:23],a[40:43],v[84:87]\nv_accvgpr_read_b32 v37,a194\nv_lshlrev_b32 v48,16,v49\nM v[80:83],a[16:19],a[40:43],v[80:83]\nv_lshlrev_b32 v30,16,v36\n" \
"v_and_b32 v45,0xffff0000,v45\nM v[96:99],a[16:19],a[48:51],v[96:99]\nv_accvgpr_read_b32 v47,a232\nv_lshlrev_b32 v31,16,v37\nM v[100:103],a[20:23],a[48:51],v[100:103]\nv_accvgpr_read_b32 v38,a196\nv_lshlrev_b32 v42,16,v43\nM v[108:111],a[20:23],a[52:55],v[108:111]\n" \
"v_and_b32 v49,0xffff0000,v49\nv_and_b32 v36,0xffff0000,v36\nM v[104:107],a[16:19],a[52:55],v[104:107]\nv_accvgpr_read_b32 v39,a198\nv_mul_f32 v25,v30,v45\nM v[120:123],a[16:19],a[60:63],v[120:123]\nv_lshlrev_b32 v46,16,v47\nv_and_b32 v37,0xffff0000,v37\n" \
"M v[124:127],a[20:23],a[60:63],v[124:127]\nv_lshlrev_b32 v32,16,v38\nv_and_b32 v43,0xffff0000,v43\nM v[116:119],a[20:23],a[56:59],v[116:119]\nv_mul_f32 v26,v36,v49\nv_lshlrev_b32 v33,16,v39\nM v[112:115],a[16:19],a[56:59],v[112:115]\nv_fmac_f32 v25,v31,v44\n" \
"v_and_b32 v47,0xffff0000,v47\nM v[192:195],a[0:3],a[32:35],v[192:195]\nv_and_b32 v38,0xffff0000,v38\nv_fmac_f32 v26,v37,v48\nM v[196:199],a[4:7],a[32:35],v[196:199]\nv_and_b32 v39,0xffff0000,v39\nv_fmac_f32 v25,v32,v43\nM v[204:207],a[4:7],a[36:39],v[204:207]\n" \
"v_fmac_f32 v26,v38,v47\nv_fmac_f32 v25,v33,v42\nM v[200:203],a[0:3],a[36:39],v[200:203]\nv_fmac_f32 v26,v39,v46\nv_accvgpr_read_b32 v28,a214\nM v[216:219],a[0:3],a[44:47],v[216:219]\nC v26,v25,v26\nv_lshlrev_b32 v27,16,v28\n" \
"M v[220:223],a[4:7],a[44:47],v[220:223]\nv_lshlrev_b32 v25,16,v26\nv_and_b32 v28,0xffff0000,v28\nM v[212:215],a[4:7],a[40:43],v[212:215]\nv_and_b32 v26,0xffff0000,v26\nv_mul_f32 v29,v25,v27\nM v[208:211],a[0:3],a[40:43],v[208:211]\nv_mul_f32 v58,v26,v28\n" \
"v_accvgpr_read_b32 v40,a200\nM v[224:227],a[0:3],a[48:51],v[224:227]\nv_mul_f32 v25,v31,v45\nv_mul_f32 v26,v37,v49\nM v[228:231],a[4:7],a[48:51],v[228:231]\nv_lshlrev_b32 v34,16,v40\nv_fmac_f32 v25,v32,v44\nM v[236:239],a[4:7],a[52:55],v[236:239]\n" \
"v_fmac_f32 v26,v38,v48\nv_fmac_f32 v25,v33,v43\nM v[232:235],a[0:3],a[52:55],v[232:235]\nv_and_b32 v40,0xffff0000,v40\nv_fmac_f32 v26,v39,v47\nM v[248:251],a[0:3],a[60:63],v[248:251]\nv_fmac_f32 v25,v34,v42\nv_fmac_f32 v26,v40,v46\n" \
"M v[252:255],a[4:7],a[60:63],v[252:255]\nv_accvgpr_read_b32 v28,a216\nC v26,v25,v26\nM v[244:247],a[4:7],a[56:59],v[244:247]\nv_lshlrev_b32 v27,16,v28\nv_lshlrev_b32 v25,16,v26\nM v[240:243],a[0:3],a[56:59],v[240:243]\nv_and_b32 v28,0xffff0000,v28\n" \
"v_and_b32 v26,0xffff0000,v26\nM a[128:131],a[8:11],a[32:35],a[128:131]\nv_mul_f32 v25,v25,v27\nv_mul_f32 v26,v26,v28\nM a[132:135],a[12:15],a[32:35],a[132:135]\nC v53,v25,v29\nC v57,v26,v58\nM a[140:143],a[12:15],a[36:39],a[140:143]\n" \
"v_accvgpr_read_b32 v41,a202\nv_lshlrev_b32 v35,16,v41\nM a[136:139],a[8:11],a[36:39],a[136:139]\nv_mul_f32 v25,v32,v45\nv_mul_f32 v26,v38,v49\nM a[152:155],a[8:11],a[44:47],a[152:155]\nv_fmac_f32 v25,v33,v44\nv_and_b32 v41,0xffff0000,v41\n" \
"M a[156:159],a[12:15],a[44:47],a[156:159]\nv_fmac_f32 v26,v39,v48\nv_fmac_f32 v25,v34,v43\nM a[148:151],a[12:15],a[40:43],a[148:151]\ns_waitcnt vmcnt(21) lgkmcnt(0)\nv_fmac_f32 v26,v40,v47\nv_fmac_f32 v25,v35,v42\nM a[144:147],a[8:11],a[40:43],a[144:147]\n" \
"s_barrier\nds_read_b64_tr_b16 a[32:33],v9 offset:32768\nM a[160:163],a[8:11],a[48:51],a[160:163]\nbuffer_load_dwordx4 v18,s[28:31],s46 offen lds\nM a[164:167],a[12:15],a[48:51],a[164:167]\ns_add_u32 m0,s50,90112\nds_read_b64_tr_b16 a[34:35],v10 offset:32768\nv_fmac_f32 v26,v41,v46\n" \
"M a[172:175],a[12:15],a[52:55],a[172:175]\nds_read_b64_tr_b16 a[36:37],v11 offset:32768\nds_read_b64_tr_b16 a[38:39],v12 offset:32768\nM a[168:171],a[8:11],a[52:55],a[168:171]\nds_read_b64_tr_b16 a[40:41],v9 offset:33280\nv_accvgpr_read_b32 v28,a218\nM a[184:187],a[8:11],a[60:63],a[184:187]\nbuffer_load_dwordx4 v19,s[28:31],s46 offen lds\n" \
"M a[188:191],a[12:15],a[60:63],a[188:191]\ns_add_u32 m0,s50,94208\nds_read_b64_tr_b16 a[42:43],v10 offset:33280\nC v26,v25,v26\nM a[180:183],a[12:15],a[56:59],a[180:183]\nds_read_b64_tr_b16 a[44:45],v11 offset:33280\nv_lshlrev_b32 v27,16,v28\nM a[176:179],a[8:11],a[56:59],a[176:179]\n" \
"ds_read_b64_tr_b16 a[46:47],v12 offset:33280\nv_lshlrev_b32 v25,16,v26\nM v[128:131],a[88:91],a[96:99],v[128:131]\nbuffer_load_dwordx4 v20,s[28:31],s46 offen lds\nM v[132:135],a[92:95],a[96:99],v[132:135]\ns_add_u32 m0,s22,0\nds_read_b64_tr_b16 a[48:49],v9 offset:33792\nv_and_b32 v28,0xffff0000,v28\n" \
"M v[140:143],a[92:95],a[100:103],v[140:143]\ns_mov_b32 s46,896\nds_read_b64_tr_b16 a[50:51],v10 offset:33792\nv_and_b32 v26,0xffff0000,v26\nM v[136:139],a[88:91],a[100:103],v[136:139]\nds_read_b64_tr_b16 a[52:53],v11 offset:33792\nds_read_b64_tr_b16 a[54:55],v12 offset:33792\nM v[152:155],a[88:91],a[108:111],v[152:155]\n" \
"buffer_load_dwordx4 v21,s[32:35],s47 offen offset:0 lds\nM v[156:159],a[92:95],a[108:111],v[156:159]\ns_add_u32 m0,s22,896\nv_mul_f32 v29,v25,v27\nv_mul_f32 v58,v26,v28\nM v[148:151],a[92:95],a[104:107],v[148:151]\nds_read_b64_tr_b16 a[56:57],v9 offset:34304\nds_read_b64_tr_b16 a[58:59],v10 offset:34304\n" \
"M v[144:147],a[88:91],a[104:107],v[144:147]\nds_read_b64_tr_b16 a[60:61],v11 offset:34304\nv_accvgpr_read_b32 v36,a204\nM v[160:163],a[88:91],a[112:115],v[160:163]\nbuffer_load_dwordx4 v21,s[32:35],s47 offen offset:128 lds\nM v[164:167],a[92:95],a[112:115],v[164:167]\ns_add_u32 m0,s22,2048\nds_read_b64_tr_b16 a[62:63],v12 offset:34304\n" \
"v_mul_f32 v25,v33,v45\nM v[172:175],a[92:95],a[116:119],v[172:175]\nds_read_b128 a[24:27],v5 offset:45056\nv_lshlrev_b32 v30,16,v36\nM v[168:171],a[88:91],a[116:119],v[168:171]\nds_read_b128 a[28:31],v5 offset:47104\nv_mul_f32 v26,v39,v49\nM v[184:187],a[88:91],a[124:127],v[184:187]\n" \
"buffer_load_dwordx4 v21,s[92:95],s47 offen offset:0 lds\nM v[188:191],a[92:95],a[124:127],v[188:191]\ns_add_u32 m0,s22,2944\nv_fmac_f32 v25,v34,v44\nv_fmac_f32 v26,v40,v48\nM v[180:183],a[92:95],a[120:123],v[180:183]\nds_read_b128 a[16:19],v5 offset:40960\nv_and_b32 v36,0xffff0000,v36\n" \
"M v[176:179],a[88:91],a[120:123],v[176:179]\nds_read_b128 a[20:23],v5 offset:43008\nv_fmac_f32 v25,v35,v43\nM v[64:67],a[80:83],a[96:99],v[64:67]\nbuffer_load_dwordx4 v21,s[92:95],s47 offen offset:128 lds\nM v[68:71],a[84:87],a[96:99],v[68:71]\ns_add_u32 m0,s22,16384\nds_read_b128 a[0:3],v5 offset:32768\n" \
"v_fmac_f32 v26,v41,v47\nM v[76:79],a[84:87],a[100:103],v[76:79]\ns_mov_b32 s47,3407872\nds_read_b128 a[4:7],v5 offset:34816\nv_fmac_f32 v25,v30,v42\nM v[72:75],a[80:83],a[100:103],v[72:75]\nds_read_b128 a[8:11],v5 offset:36864\nv_fmac_f32 v26,v36,v46\n" \
"M v[88:91],a[80:83],a[108:111],v[88:91]\nbuffer_load_dwordx4 v21,s[32:35],s47 offen offset:0 lds\nM v[92:95],a[84:87],a[108:111],v[92:95]\ns_add_u32 m0,s22,17280\nv_accvgpr_read_b32 v28,a220\nC v26,v25,v26\nM v[84:87],a[84:87],a[104:107],v[84:87]\nds_read_b128 a[12:15],v5 offset:38912\n" \
"v_lshlrev_b32 v27,16,v28\nM v[80:83],a[80:83],a[104:107],v[80:83]\nds_read_b128 a[88:91],v6 offset:45056\nv_lshlrev_b32 v25,16,v26\nM v[96:99],a[80:83],a[112:115],v[96:99]\nbuffer_load_dwordx4 v21,s[32:35],s47 offen offset:128 lds\nM v[100:103],a[84:87],a[112:115],v[100:103]\ns_add_u32 m0,s22,18432\n" \
"ds_read_b128 a[92:95],v6 offset:47104\nv_and_b32 v28,0xffff0000,v28\nM v[108:111],a[84:87],a[116:119],v[108:111]\nv_and_b32 v26,0xffff0000,v26\nv_mul_f32 v25,v25,v27\nM v[104:107],a[80:83],a[116:119],v[104:107]\nv_mul_f32 v26,v26,v28\nC v52,v25,v29\n" \
"M v[120:123],a[80:83],a[124:127],v[120:123]\nbuffer_load_dwordx4 v21,s[92:95],s47 offen offset:0 lds\nM v[124:127],a[84:87],a[124:127],v[124:127]\ns_add_u32 m0,s22,19328\nC v56,v26,v58\nv_mov_b32 v59,s19\nM v[116:119],a[84:87],a[120:123],v[116:119]\nv_permlane32_swap_b32 v23,v24\n" \
"M v[112:115],a[80:83],a[120:123],v[112:115]\nds_read_b128 a[80:83],v6 offset:40960\nds_read_b128 a[84:87],v6 offset:43008\nM v[192:195],a[64:67],a[96:99],v[192:195]\nbuffer_load_dwordx4 v21,s[92:95],s47 offen offset:128 lds\nM v[196:199],a[68:71],a[96:99],v[196:199]\ns_add_u32 m0,s50,0\nv_add_f32 v23,v23,v24\n" \
"v_mov_b32 v24,v23\nM v[204:207],a[68:71],a[100:103],v[204:207]\ns_mov_b32 s47,3670016\nv_permlane16_swap_b32 v23,v24\nM v[200:203],a[64:67],a[100:103],v[200:203]\nv_add_f32 v23,v23,v24\ns_nop 1\nv_add_f32 v23,v23,v23 row_ror:8 row_mask:0xf bank_mask:0xf\n" \
"M v[216:219],a[64:67],a[108:111],v[216:219]\nbuffer_load_dwordx4 v13,s[28:31],s46 offen lds\nM v[220:223],a[68:71],a[108:111],v[220:223]\ns_add_u32 m0,s50,4096\nv_add_u32 v13,s60,v63\nv_add_f32 v23,v23,v23 row_ror:4 row_mask:0xf bank_mask:0xf\nM v[212:215],a[68:71],a[104:107],v[212:215]\ns_nop 0\n" \
"v_add_f32 v23,v23,v23 row_ror:2 row_mask:0xf bank_mask:0xf\ns_nop 1\nv_add_f32 v23,v23,v23 row_ror:1 row_mask:0xf bank_mask:0xf\nM v[208:211],a[64:67],a[104:107],v[208:211]\nC v23,v23,v23\nv_and_b32 v23,0xffff0000,v23\nM v[224:227],a[64:67],a[112:115],v[224:227]\nbuffer_load_dwordx4 v14,s[28:31],s46 offen lds\n" \
"M v[228:231],a[68:71],a[112:115],v[228:231]\ns_add_u32 m0,s50,8192\nv_add_u32 v14,0x10000,v13\nv_mul_f32 v23,0xbfb8aa3b,v23\nM v[236:239],a[68:71],a[116:119],v[236:239]\nv_exp_f32 v23,v23\nM v[232:235],a[64:67],a[116:119],v[232:235]\nv_add_f32 v23,1.0,v23\n" \
"v_add_u32 v24,992,v4\nM v[248:251],a[64:67],a[124:127],v[248:251]\nbuffer_load_dwordx4 v15,s[28:31],s46 offen lds\nM v[252:255],a[68:71],a[124:127],v[252:255]\ns_add_u32 m0,s50,12288\nv_add_u32 v15,0x10000,v14\nM v[244:247],a[68:71],a[120:123],v[244:247]\nv_rcp_f32 v23,v23\n" \
"M v[240:243],a[64:67],a[120:123],v[240:243]\nds_read_b128 a[64:67],v6 offset:32768\nds_read_b128 a[68:71],v6 offset:34816\nM a[128:131],a[72:75],a[96:99],a[128:131]\nbuffer_load_dwordx4 v16,s[28:31],s46 offen lds\nM a[132:135],a[76:79],a[96:99],a[132:135]\ns_add_u32 m0,s50,16384\nv_add_u32 v16,0x10000,v15\n" \
"C v23,v23,v23\nM a[140:143],a[76:79],a[100:103],a[140:143]\ns_mov_b32 s46,896\nv_and_b32 v23,0xffff0000,v23\nM a[136:139],a[72:75],a[100:103],a[136:139]\nM a[152:155],a[72:75],a[108:111],a[152:155]\nbuffer_load_dwordx4 v17,s[28:31],s46 offen lds\nM a[156:159],a[76:79],a[108:111],a[156:159]\n" \
"s_add_u32 m0,s50,20480\nv_add_u32 v17,s61,v63\nv_mov_b32 v63,0x3f803f80\nM a[148:151],a[76:79],a[104:107],a[148:151]\ns_waitcnt vmcnt(33) lgkmcnt(14)\nM a[144:147],a[72:75],a[104:107],a[144:147]\ns_barrier\nds_read_b64_tr_b16 a[96:97],v9 offset:49152\n" \
"M a[160:163],a[72:75],a[112:115],a[160:163]\nbuffer_store_dword v23,v61,s[36:39],0 offen sc1\nM a[164:167],a[76:79],a[112:115],a[164:167]\nds_read_b64_tr_b16 a[98:99],v10 offset:49152\nds_read_b64_tr_b16 a[100:101],v11 offset:49152\nM a[172:175],a[76:79],a[116:119],a[172:175]\nbuffer_load_dwordx4 v18,s[28:31],s46 offen lds\nM a[168:171],a[72:75],a[116:119],a[168:171]\n" \
"s_add_u32 m0,s50,24576\nds_read_b64_tr_b16 a[102:103],v12 offset:49152\nv_add_u32 v18,0x10000,v17\nM a[184:187],a[72:75],a[124:127],a[184:187]\nds_read_b64_tr_b16 a[104:105],v9 offset:49664\nv_add_u32 v23,-32,v4\nM a[188:191],a[76:79],a[124:127],a[188:191]\nds_read_b64_tr_b16 a[106:107],v10 offset:49664\n" \
"M a[180:183],a[76:79],a[120:123],a[180:183]\ns_waitcnt lgkmcnt(5)\nds_read_b64_tr_b16 a[108:109],v11 offset:49664\nds_read_b64_tr_b16 a[110:111],v12 offset:49664\nM a[176:179],a[72:75],a[120:123],a[176:179]\nbuffer_load_dwordx4 v19,s[28:31],s46 offen lds\nM v[128:131],a[24:27],a[32:35],v[128:131]\ns_add_u32 m0,s50,28672\n" \
"v_add_u32 v19,0x10000,v18\nM v[132:135],a[28:31],a[32:35],v[132:135]\nds_read_b128 a[72:75],v6 offset:36864\nM v[140:143],a[28:31],a[36:39],v[140:143]\nds_read_b128 a[76:79],v6 offset:38912\nM v[136:139],a[24:27],a[36:39],v[136:139]\nbuffer_load_dwordx4 v20,s[28:31],s46 offen lds\nM v[152:155],a[24:27],a[44:47],v[152:155]\n" \
"s_add_u32 m0,s22,32768\nds_read_b64_tr_b16 a[112:113],v9 offset:50176\nv_add_u32 v20,0x10000,v19\nM v[156:159],a[28:31],a[44:47],v[156:159]\ns_mov_b64 s[28:29],s[64:65]\nds_read_b64_tr_b16 a[114:115],v10 offset:50176\nM v[148:151],a[28:31],a[40:43],v[148:151]\ns_and_b32 s29,s29,0xffff\n" \
"ds_read_b64_tr_b16 a[116:117],v11 offset:50176\nds_read_b64_tr_b16 a[118:119],v12 offset:50176\nM v[144:147],a[24:27],a[40:43],v[144:147]\ns_mov_b32 s30,4194304\nds_read_b64_tr_b16 a[120:121],v9 offset:50688\nM v[160:163],a[24:27],a[48:51],v[160:163]\nbuffer_load_dwordx4 v21,s[32:35],s47 offen offset:0 lds\nM v[164:167],a[28:31],a[48:51],v[164:167]\n" \
"s_add_u32 m0,s22,33664\nds_read_b64_tr_b16 a[122:123],v10 offset:50688\nM v[172:175],a[28:31],a[52:55],v[172:175]\ns_mov_b32 s46,0\nds_read_b64_tr_b16 a[124:125],v11 offset:50688\nM v[168:171],a[24:27],a[52:55],v[168:171]\nds_read_b64_tr_b16 a[126:127],v12 offset:50688\nM v[184:187],a[24:27],a[60:63],v[184:187]\n" \
"buffer_load_dwordx4 v21,s[32:35],s47 offen offset:128 lds\nM v[188:191],a[28:31],a[60:63],v[188:191]\ns_add_u32 m0,s22,34816\nM v[180:183],a[28:31],a[56:59],v[180:183]\nM v[176:179],a[24:27],a[56:59],v[176:179]\nM v[64:67],a[16:19],a[32:35],v[64:67]\nM v[68:71],a[20:23],a[32:35],v[68:71]\nbuffer_load_dwordx4 v21,s[92:95],s47 offen offset:0 lds\n" \
"M v[76:79],a[20:23],a[36:39],v[76:79]\ns_add_u32 m0,s22,35712\nM v[72:75],a[16:19],a[36:39],v[72:75]\nM v[88:91],a[16:19],a[44:47],v[88:91]\nM v[92:95],a[20:23],a[44:47],v[92:95]\nbuffer_load_dwordx4 v21,s[92:95],s47 offen offset:128 lds\nM v[84:87],a[20:23],a[40:43],v[84:87]\ns_add_u32 m0,s22,49152\n" \
"M v[80:83],a[16:19],a[40:43],v[80:83]\ns_mov_b32 s47,3932160\nM v[96:99],a[16:19],a[48:51],v[96:99]\nM v[100:103],a[20:23],a[48:51],v[100:103]\nM v[108:111],a[20:23],a[52:55],v[108:111]\nM v[104:107],a[16:19],a[52:55],v[104:107]\nM v[120:123],a[16:19],a[60:63],v[120:123]\nM v[124:127],a[20:23],a[60:63],v[124:127]\n" \
"M v[116:119],a[20:23],a[56:59],v[116:119]\nM v[112:115],a[16:19],a[56:59],v[112:115]\nM v[192:195],a[0:3],a[32:35],v[192:195]\ns_mov_b64 exec,0xff\nglobal_load_dword v0,v2,s[88:89]\ns_mov_b64 exec,-1\nM v[196:199],a[4:7],a[32:35],v[196:199]\nM v[204:207],a[4:7],a[36:39],v[204:207]\n" \
"s_mov_b64 exec,0xff00\nglobal_load_dword v0,v2,s[88:89]\ns_mov_b64 exec,-1\nM v[200:203],a[0:3],a[36:39],v[200:203]\ns_add_u32 s88,s88,2048\ns_addc_u32 s89,s89,0\nM v[216:219],a[0:3],a[44:47],v[216:219]\ns_mov_b64 exec,0xff\n" \
"global_load_dword v0,v2,s[88:89]\ns_mov_b64 exec,-1\nM v[220:223],a[4:7],a[44:47],v[220:223]\nM v[212:215],a[4:7],a[40:43],v[212:215]\ns_mov_b64 exec,0xff00\nglobal_load_dword v0,v2,s[88:89]\ns_mov_b64 exec,-1\nM v[208:211],a[0:3],a[40:43],v[208:211]\n" \
"M v[224:227],a[0:3],a[48:51],v[224:227]\ns_mov_b64 exec,0xff\nglobal_load_dword v0,v1,s[76:77]\ns_mov_b64 exec,-1\nM v[228:231],a[4:7],a[48:51],v[228:231]\nM v[236:239],a[4:7],a[52:55],v[236:239]\ns_mov_b64 exec,0xff00\nglobal_load_dword v0,v1,s[76:77]\n" \
"s_mov_b64 exec,-1\nM v[232:235],a[0:3],a[52:55],v[232:235]\ns_add_u32 s76,s76,2097152\ns_addc_u32 s77,s77,0\nM v[248:251],a[0:3],a[60:63],v[248:251]\nM v[252:255],a[4:7],a[60:63],v[252:255]\nM v[244:247],a[4:7],a[56:59],v[244:247]\ns_waitcnt vmcnt(34)\n" \
"v_accvgpr_read_b32 v37,a206\nv_mul_f32 v25,v34,v45\nM v[240:243],a[0:3],a[56:59],v[240:243]\ns_mov_b32 s98,79691776\nv_mul_f32 v26,v40,v49\nv_lshlrev_b32 v31,16,v37\nM a[128:131],a[8:11],a[32:35],a[128:131]\ns_mov_b32 s99,79695872\n" \
"v_fmac_f32 v25,v35,v44\nv_fmac_f32 v26,v41,v48\nM a[132:135],a[12:15],a[32:35],a[132:135]\nv_and_b32 v37,0xffff0000,v37\nv_fmac_f32 v25,v30,v43\nM a[140:143],a[12:15],a[36:39],a[140:143]\nv_fmac_f32 v26,v36,v47\nv_fmac_f32 v25,v31,v42\n" \
"M a[136:139],a[8:11],a[36:39],a[136:139]\nv_fmac_f32 v26,v37,v46\nv_accvgpr_read_b32 v28,a222\nM a[152:155],a[8:11],a[44:47],a[152:155]\nC v26,v25,v26\nv_lshlrev_b32 v27,16,v28\nM a[156:159],a[12:15],a[44:47],a[156:159]\nv_lshlrev_b32 v25,16,v26\n" \
"v_and_b32 v28,0xffff0000,v28\nM a[148:151],a[12:15],a[40:43],a[148:151]\ns_waitcnt vmcnt(23) lgkmcnt(0)\nv_and_b32 v26,0xffff0000,v26\nv_mul_f32 v29,v25,v27\nM a[144:147],a[8:11],a[40:43],a[144:147]\ns_barrier\nv_mul_f32 v58,v26,v28\n" \
"M a[160:163],a[8:11],a[48:51],a[160:163]\nv_accvgpr_read_b32 v38,a208\nv_mul_f32 v25,v35,v45\nM a[164:167],a[12:15],a[48:51],a[164:167]\nv_lshlrev_b32 v32,16,v38\nv_mul_f32 v26,v41,v49\nM a[172:175],a[12:15],a[52:55],a[172:175]\nLG6_%=:\n" \
"s_cbranch_execz LHR0_%=\nv_fmac_f32 v25,v30,v44\nv_and_b32 v38,0xffff0000,v38\nM a[168:171],a[8:11],a[52:55],a[168:171]\nv_fmac_f32 v26,v36,v48\nv_fmac_f32 v25,v31,v43\nM a[184:187],a[8:11],a[60:63],a[184:187]\nds_read_b64_tr_b16 a[32:33],v9 offset:0\n" \
"ds_read_b64_tr_b16 a[34:35],v10 offset:0\nM a[188:191],a[12:15],a[60:63],a[188:191]\nds_read_b64_tr_b16 a[36:37],v11 offset:0\nv_fmac_f32 v26,v37,v47\nM a[180:183],a[12:15],a[56:59],a[180:183]\nds_read_b64_tr_b16 a[38:39],v12 offset:0\nds_read_b64_tr_b16 a[40:41],v9 offset:512\nM a[176:179],a[8:11],a[56:59],a[176:179]\n" \
"ds_read_b64_tr_b16 a[42:43],v10 offset:512\nv_fmac_f32 v25,v32,v42\nM v[128:131],a[88:91],a[96:99],v[128:131]\nv_fmac_f32 v26,v38,v46\nv_accvgpr_read_b32 v28,a224\nM v[132:135],a[92:95],a[96:99],v[132:135]\nds_read_b64_tr_b16 a[44:45],v11 offset:512\nC v26,v25,v26\n" \
"M v[140:143],a[92:95],a[100:103],v[140:143]\nds_read_b64_tr_b16 a[46:47],v12 offset:512\nv_lshlrev_b32 v27,16,v28\nM v[136:139],a[88:91],a[100:103],v[136:139]\nds_read_b64_tr_b16 a[48:49],v9 offset:1024\nds_read_b64_tr_b16 a[50:51],v10 offset:1024\nM v[152:155],a[88:91],a[108:111],v[152:155]\nds_read_b64_tr_b16 a[52:53],v11 offset:1024\n" \
"v_lshlrev_b32 v25,16,v26\nM v[156:159],a[92:95],a[108:111],v[156:159]\nds_read_b64_tr_b16 a[54:55],v12 offset:1024\nds_read_b64_tr_b16 a[56:57],v9 offset:1536\nM v[148:151],a[92:95],a[104:107],v[148:151]\nds_read_b64_tr_b16 a[58:59],v10 offset:1536\nv_and_b32 v28,0xffff0000,v28\nM v[144:147],a[88:91],a[104:107],v[144:147]\n" \
"ds_read_b64_tr_b16 a[60:61],v11 offset:1536\nv_and_b32 v26,0xffff0000,v26\nM v[160:163],a[88:91],a[112:115],v[160:163]\nds_read_b64_tr_b16 a[62:63],v12 offset:1536\nv_mul_f32 v25,v25,v27\nM v[164:167],a[92:95],a[112:115],v[164:167]\nds_read_b128 a[24:27],v7 offset:12288\nv_mul_f32 v26,v26,v28\n" \
"M v[172:175],a[92:95],a[116:119],v[172:175]\nds_read_b128 a[28:31],v7 offset:14336\nC v51,v25,v29\nM v[168:171],a[88:91],a[116:119],v[168:171]\nC v55,v26,v58\nv_accvgpr_read_b32 v39,a210\nM v[184:187],a[88:91],a[124:127],v[184:187]\nv_mul_f32 v25,v30,v45\n" \
"v_mul_f32 v26,v36,v49\nM v[188:191],a[92:95],a[124:127],v[188:191]\nv_lshlrev_b32 v33,16,v39\nv_fmac_f32 v25,v31,v44\nM v[180:183],a[92:95],a[120:123],v[180:183]\nv_fmac_f32 v26,v37,v48\nv_and_b32 v39,0xffff0000,v39\nM v[176:179],a[88:91],a[120:123],v[176:179]\n" \
"v_fmac_f32 v25,v32,v43\nv_fmac_f32 v26,v38,v47\nM v[64:67],a[80:83],a[96:99],v[64:67]\nv_fmac_f32 v25,v33,v42\nv_fmac_f32 v26,v39,v46\nM v[68:71],a[84:87],a[96:99],v[68:71]\nv_accvgpr_read_b32 v28,a226\nC v26,v25,v26\n" \
"M v[76:79],a[84:87],a[100:103],v[76:79]\nv_lshlrev_b32 v27,16,v28\nv_lshlrev_b32 v25,16,v26\nM v[72:75],a[80:83],a[100:103],v[72:75]\nv_and_b32 v28,0xffff0000,v28\nv_and_b32 v26,0xffff0000,v26\nM v[88:91],a[80:83],a[108:111],v[88:91]\nv_mul_f32 v29,v25,v27\n" \
"v_mul_f32 v58,v26,v28\nM v[92:95],a[84:87],a[108:111],v[92:95]\nv_accvgpr_read_b32 v40,a212\nv_lshlrev_b32 v34,16,v40\nM v[84:87],a[84:87],a[104:107],v[84:87]\nv_mul_f32 v25,v31,v45\nv_mul_f32 v26,v37,v49\nM v[80:83],a[80:83],a[104:107],v[80:83]\n" \
"ds_read_b128 a[16:19],v7 offset:8192\nv_fmac_f32 v25,v32,v44\nM v[96:99],a[80:83],a[112:115],v[96:99]\nds_read_b128 a[20:23],v7 offset:10240\nv_and_b32 v40,0xffff0000,v40\nM v[100:103],a[84:87],a[112:115],v[100:103]\nv_fmac_f32 v26,v38,v48\nv_fmac_f32 v25,v33,v43\n" \
"M v[108:111],a[84:87],a[116:119],v[108:111]\nv_fmac_f32 v26,v39,v47\nv_fmac_f32 v25,v34,v42\nM v[104:107],a[80:83],a[116:119],v[104:107]\nv_fmac_f32 v26,v40,v46\nv_accvgpr_read_b32 v28,a228\nM v[120:123],a[80:83],a[124:127],v[120:123]\nC v26,v25,v26\n" \
"v_lshlrev_b32 v27,16,v28\nM v[124:127],a[84:87],a[124:127],v[124:127]\nv_lshlrev_b32 v25,16,v26\nv_mul_f32 v25,v25,v27\nM v[116:119],a[84:87],a[120:123],v[116:119]\nC v50,v25,v29\nv_accvgpr_read_b32 v45,a235\nM v[112:115],a[80:83],a[120:123],v[112:115]\n" \
"v_and_b32 v28,0xffff0000,v28\nv_and_b32 v26,0xffff0000,v26\nM v[192:195],a[64:67],a[96:99],v[192:195]\nv_lshlrev_b32 v44,16,v45\nv_accvgpr_read_b32 v49,a237\nM v[196:199],a[68:71],a[96:99],v[196:199]\nds_read_b128 a[0:3],v7 offset:0\nv_accvgpr_read_b32 v36,a193\n" \
"M v[204:207],a[68:71],a[100:103],v[204:207]\nds_read_b128 a[4:7],v7 offset:2048\nv_accvgpr_read_b32 v37,a195\nM v[200:203],a[64:67],a[100:103],v[200:203]\nv_mul_f32 v26,v26,v28\nv_accvgpr_read_b32 v43,a234\nM v[216:219],a[64:67],a[108:111],v[216:219]\nv_accvgpr_read_b32 v47,a236\n" \
"v_lshlrev_b32 v48,16,v49\nM v[220:223],a[68:71],a[108:111],v[220:223]\nv_and_b32 v45,0xffff0000,v45\nv_lshlrev_b32 v30,16,v36\nM v[212:215],a[68:71],a[104:107],v[212:215]\nbuffer_store_dwordx4 v[50:53],v60,s[36:39],s98 offen offset:0 sc1\nM v[208:211],a[64:67],a[104:107],v[208:211]\ns_mov_b32 s98,79699968\n" \
"v_lshlrev_b32 v31,16,v37\nv_accvgpr_read_b32 v38,a197\nM v[224:227],a[64:67],a[112:115],v[224:227]\nC v54,v26,v58\nv_accvgpr_read_b32 v39,a199\nM v[228:231],a[68:71],a[112:115],v[228:231]\nv_lshlrev_b32 v42,16,v43\nv_and_b32 v49,0xffff0000,v49\n" \
"M v[236:239],a[68:71],a[116:119],v[236:239]\nv_lshlrev_b32 v46,16,v47\nv_and_b32 v36,0xffff0000,v36\nM v[232:235],a[64:67],a[116:119],v[232:235]\nv_lshlrev_b32 v32,16,v38\nv_mul_f32 v25,v30,v45\nM v[248:251],a[64:67],a[124:127],v[248:251]\nds_read_b128 a[8:11],v7 offset:4096\n" \
"ds_read_b128 a[12:15],v7 offset:6144\nM v[252:255],a[68:71],a[124:127],v[252:255]\nv_and_b32 v37,0xffff0000,v37\nv_lshlrev_b32 v33,16,v39\nM v[244:247],a[68:71],a[120:123],v[244:247]\nv_and_b32 v43,0xffff0000,v43\nv_mul_f32 v26,v36,v49\nM v[240:243],a[64:67],a[120:123],v[240:243]\n" \
"ds_read_b128 a[64:67],v8 offset:0\nds_read_b128 a[68:71],v8 offset:2048\nM a[128:131],a[72:75],a[96:99],a[128:131]\nv_and_b32 v47,0xffff0000,v47\nv_and_b32 v38,0xffff0000,v38\nM a[132:135],a[76:79],a[96:99],a[132:135]\nds_read_b128 a[80:83],v8 offset:8192\nv_fmac_f32 v25,v31,v44\n" \
"M a[140:143],a[76:79],a[100:103],a[140:143]\nv_and_b32 v39,0xffff0000,v39\nv_fmac_f32 v26,v37,v48\nM a[136:139],a[72:75],a[100:103],a[136:139]\nv_fmac_f32 v25,v32,v43\nv_fmac_f32 v26,v38,v47\nM a[152:155],a[72:75],a[108:111],a[152:155]\nv_fmac_f32 v25,v33,v42\n" \
"v_fmac_f32 v26,v39,v46\nM a[156:159],a[76:79],a[108:111],a[156:159]\nds_read_b128 a[84:87],v8 offset:10240\nv_accvgpr_read_b32 v28,a215\nM a[148:151],a[76:79],a[104:107],a[148:151]\ns_waitcnt vmcnt(20) lgkmcnt(12)\nds_read_b128 a[88:91],v8 offset:12288\nds_read_b128 a[92:95],v8 offset:14336\n" \
"M a[144:147],a[72:75],a[104:107],a[144:147]\ns_barrier\nds_read_b64_tr_b16 a[96:97],v9 offset:16384\nM a[160:163],a[72:75],a[112:115],a[160:163]\nds_read_b64_tr_b16 a[98:99],v10 offset:16384\nC v26,v25,v26\nM a[164:167],a[76:79],a[112:115],a[164:167]\nds_read_b64_tr_b16 a[100:101],v11 offset:16384\n" \
"v_lshlrev_b32 v27,16,v28\nM a[172:175],a[76:79],a[116:119],a[172:175]\nds_read_b64_tr_b16 a[102:103],v12 offset:16384\nds_read_b64_tr_b16 a[104:105],v9 offset:16896\nM a[168:171],a[72:75],a[116:119],a[168:171]\nds_read_b64_tr_b16 a[106:107],v10 offset:16896\nv_lshlrev_b32 v25,16,v26\nM a[184:187],a[72:75],a[124:127],a[184:187]\n" \
"ds_read_b64_tr_b16 a[108:109],v11 offset:16896\nv_and_b32 v28,0xffff0000,v28\nM a[188:191],a[76:79],a[124:127],a[188:191]\ns_waitcnt lgkmcnt(6)\nds_read_b64_tr_b16 a[110:111],v12 offset:16896\nds_read_b64_tr_b16 a[112:113],v9 offset:17408\nM a[180:183],a[76:79],a[120:123],a[180:183]\nbuffer_store_dwordx4 v[54:57],v60,s[36:39],s99 offen offset:0 sc1\n" \
"M a[176:179],a[72:75],a[120:123],a[176:179]\ns_mov_b32 s99,79704064\nds_read_b128 a[72:75],v8 offset:4096\nds_read_b128 a[76:79],v8 offset:6144\nM v[128:131],a[24:27],a[32:35],v[128:131]\nds_read_b64_tr_b16 a[114:115],v10 offset:17408\nv_and_b32 v26,0xffff0000,v26\nM v[132:135],a[28:31],a[32:35],v[132:135]\n" \
"ds_read_b64_tr_b16 a[116:117],v11 offset:17408\nv_mul_f32 v29,v25,v27\nM v[140:143],a[28:31],a[36:39],v[140:143]\nds_read_b64_tr_b16 a[118:119],v12 offset:17408\nv_mul_f32 v58,v26,v28\nM v[136:139],a[24:27],a[36:39],v[136:139]\nds_read_b64_tr_b16 a[120:121],v9 offset:17920\nds_read_b64_tr_b16 a[122:123],v10 offset:17920\n" \
"M v[152:155],a[24:27],a[44:47],v[152:155]\nds_read_b64_tr_b16 a[124:125],v11 offset:17920\nv_accvgpr_read_b32 v40,a201\nM v[156:159],a[28:31],a[44:47],v[156:159]\nds_read_b64_tr_b16 a[126:127],v12 offset:17920\nv_lshlrev_b32 v34,16,v40\nM v[148:151],a[28:31],a[40:43],v[148:151]\nv_mul_f32 v25,v31,v45\n" \
"v_mul_f32 v26,v37,v49\nM v[144:147],a[24:27],a[40:43],v[144:147]\nv_fmac_f32 v25,v32,v44\nv_and_b32 v40,0xffff0000,v40\nM v[160:163],a[24:27],a[48:51],v[160:163]\nv_fmac_f32 v26,v38,v48\nv_fmac_f32 v25,v33,v43\nM v[164:167],a[28:31],a[48:51],v[164:167]\n" \
"v_fmac_f32 v26,v39,v47\nv_fmac_f32 v25,v34,v42\nM v[172:175],a[28:31],a[52:55],v[172:175]\nv_fmac_f32 v26,v40,v46\nv_accvgpr_read_b32 v28,a217\nM v[168:171],a[24:27],a[52:55],v[168:171]\nC v26,v25,v26\nv_lshlrev_b32 v27,16,v28\n" \
"M v[184:187],a[24:27],a[60:63],v[184:187]\nv_lshlrev_b32 v25,16,v26\nv_and_b32 v28,0xffff0000,v28\nM v[188:191],a[28:31],a[60:63],v[188:191]\nv_and_b32 v26,0xffff0000,v26\nv_mul_f32 v25,v25,v27\nM v[180:183],a[28:31],a[56:59],v[180:183]\nv_mul_f32 v26,v26,v28\n" \
"C v53,v25,v29\nM v[176:179],a[24:27],a[56:59],v[176:179]\nC v57,v26,v58\nv_accvgpr_read_b32 v41,a203\nM v[64:67],a[16:19],a[32:35],v[64:67]\nv_mul_f32 v25,v32,v45\nv_lshlrev_b32 v35,16,v41\nM v[68:71],a[20:23],a[32:35],v[68:71]\n" \
"v_mul_f32 v26,v38,v49\nv_fmac_f32 v25,v33,v44\nM v[76:79],a[20:23],a[36:39],v[76:79]\nv_fmac_f32 v26,v39,v48\nv_and_b32 v41,0xffff0000,v41\nM v[72:75],a[16:19],a[36:39],v[72:75]\nv_fmac_f32 v25,v34,v43\nv_fmac_f32 v26,v40,v47\n" \
"M v[88:91],a[16:19],a[44:47],v[88:91]\nv_fmac_f32 v25,v35,v42\nv_fmac_f32 v26,v41,v46\nM v[92:95],a[20:23],a[44:47],v[92:95]\nv_accvgpr_read_b32 v28,a219\nC v26,v25,v26\nM v[84:87],a[20:23],a[40:43],v[84:87]\ns_mov_b64 exec,0xff\n" \
"global_load_dword v0,v2,s[100:101]\ns_mov_b64 exec,-1\nM v[80:83],a[16:19],a[40:43],v[80:83]\nv_lshlrev_b32 v27,16,v28\nv_lshlrev_b32 v25,16,v26\nM v[96:99],a[16:19],a[48:51],v[96:99]\nbuffer_load_dwordx4 v21,s[32:35],s47 offen offset:0 lds\nM v[100:103],a[20:23],a[48:51],v[100:103]\n" \
"s_add_u32 m0,s22,50048\nv_and_b32 v28,0xffff0000,v28\nv_and_b32 v26,0xffff0000,v26\nM v[108:111],a[20:23],a[52:55],v[108:111]\nv_mul_f32 v29,v25,v27\nv_mul_f32 v58,v26,v28\nM v[104:107],a[16:19],a[52:55],v[104:107]\nv_accvgpr_read_b32 v36,a205\n" \
"v_lshlrev_b32 v30,16,v36\nM v[120:123],a[16:19],a[60:63],v[120:123]\nbuffer_load_dwordx4 v21,s[32:35],s47 offen offset:128 lds\nM v[124:127],a[20:23],a[60:63],v[124:127]\ns_add_u32 m0,s22,51200\nv_mul_f32 v25,v33,v45\nv_mul_f32 v26,v39,v49\nM v[116:119],a[20:23],a[56:59],v[116:119]\n" \
"s_mov_b64 s[32:33],s[44:45]\nv_fmac_f32 v25,v34,v44\nv_and_b32 v36,0xffff0000,v36\nM v[112:115],a[16:19],a[56:59],v[112:115]\ns_mov_b32 s34,s49\nv_fmac_f32 v26,v40,v48\nv_fmac_f32 v25,v35,v43\nM v[192:195],a[0:3],a[32:35],v[192:195]\n" \
"buffer_load_dwordx4 v21,s[92:95],s47 offen offset:0 lds\nM v[196:199],a[4:7],a[32:35],v[196:199]\ns_add_u32 m0,s22,52096\nv_fmac_f32 v26,v41,v47\nv_fmac_f32 v25,v30,v42\nM v[204:207],a[4:7],a[36:39],v[204:207]\nv_fmac_f32 v26,v36,v46\nv_accvgpr_read_b32 v28,a221\n" \
"M v[200:203],a[0:3],a[36:39],v[200:203]\nC v26,v25,v26\nv_lshlrev_b32 v27,16,v28\nM v[216:219],a[0:3],a[44:47],v[216:219]\nbuffer_load_dwordx4 v21,s[92:95],s47 offen offset:128 lds\nM v[220:223],a[4:7],a[44:47],v[220:223]\ns_add_u32 m0,s50,32768\nv_lshlrev_b32 v25,16,v26\n" \
"v_and_b32 v28,0xffff0000,v28\nM v[212:215],a[4:7],a[40:43],v[212:215]\ns_mov_b32 s47,0\nv_and_b32 v26,0xffff0000,v26\nv_mul_f32 v25,v25,v27\nM v[208:211],a[0:3],a[40:43],v[208:211]\nv_mul_f32 v26,v26,v28\nC v52,v25,v29\n" \
"M v[224:227],a[0:3],a[48:51],v[224:227]\nbuffer_load_dwordx4 v13,s[28:31],s46 offen lds\nM v[228:231],a[4:7],a[48:51],v[228:231]\ns_add_u32 m0,s50,36864\nC v56,v26,v58\nv_accvgpr_read_b32 v37,a207\nM v[236:239],a[4:7],a[52:55],v[236:239]\nv_mul_f32 v25,v34,v45\n" \
"v_lshlrev_b32 v31,16,v37\nM v[232:235],a[0:3],a[52:55],v[232:235]\nv_mul_f32 v26,v40,v49\nv_fmac_f32 v25,v35,v44\nM v[248:251],a[0:3],a[60:63],v[248:251]\nbuffer_load_dwordx4 v14,s[28:31],s46 offen lds\nM v[252:255],a[4:7],a[60:63],v[252:255]\ns_add_u32 m0,s50,40960\n" \
"v_fmac_f32 v26,v41,v48\nv_and_b32 v37,0xffff0000,v37\nM v[244:247],a[4:7],a[56:59],v[244:247]\nv_fmac_f32 v25,v30,v43\nv_fmac_f32 v26,v36,v47\nM v[240:243],a[0:3],a[56:59],v[240:243]\nv_fmac_f32 v25,v31,v42\nv_fmac_f32 v26,v37,v46\n" \
"M a[128:131],a[8:11],a[32:35],a[128:131]\nbuffer_load_dwordx4 v15,s[28:31],s46 offen lds\nM a[132:135],a[12:15],a[32:35],a[132:135]\ns_add_u32 m0,s50,45056\nv_accvgpr_read_b32 v28,a223\nC v26,v25,v26\nM a[140:143],a[12:15],a[36:39],a[140:143]\nv_lshlrev_b32 v27,16,v28\n" \
"v_lshlrev_b32 v25,16,v26\nM a[136:139],a[8:11],a[36:39],a[136:139]\nv_and_b32 v28,0xffff0000,v28\nv_and_b32 v26,0xffff0000,v26\nM a[152:155],a[8:11],a[44:47],a[152:155]\nbuffer_load_dwordx4 v16,s[28:31],s46 offen lds\nM a[156:159],a[12:15],a[44:47],a[156:159]\ns_add_u32 m0,s50,49152\n" \
"v_mul_f32 v29,v25,v27\nv_mul_f32 v58,v26,v28\nM a[148:151],a[12:15],a[40:43],a[148:151]\ns_waitcnt vmcnt(17) lgkmcnt(0)\nv_accvgpr_read_b32 v38,a209\nv_mul_f32 v25,v35,v45\nM a[144:147],a[8:11],a[40:43],a[144:147]\ns_mov_b32 s46,0\n" \
"s_barrier\nds_read_b64_tr_b16 a[32:33],v9 offset:32768\nM a[160:163],a[8:11],a[48:51],a[160:163]\nbuffer_load_dwordx4 v17,s[28:31],s46 offen lds\nM a[164:167],a[12:15],a[48:51],a[164:167]\ns_add_u32 m0,s50,53248\nds_read_b64_tr_b16 a[34:35],v10 offset:32768\nv_mul_f32 v26,v41,v49\n" \
"M a[172:175],a[12:15],a[52:55],a[172:175]\nds_read_b64_tr_b16 a[36:37],v11 offset:32768\nds_read_b64_tr_b16 a[38:39],v12 offset:32768\nM a[168:171],a[8:11],a[52:55],a[168:171]\nds_read_b64_tr_b16 a[40:41],v9 offset:33280\nv_lshlrev_b32 v32,16,v38\nM a[184:187],a[8:11],a[60:63],a[184:187]\nbuffer_load_dwordx4 v18,s[28:31],s46 offen lds\n" \
"M a[188:191],a[12:15],a[60:63],a[188:191]\ns_add_u32 m0,s50,57344\nds_read_b64_tr_b16 a[42:43],v10 offset:33280\nv_fmac_f32 v25,v30,v44\nM a[180:183],a[12:15],a[56:59],a[180:183]\nds_read_b64_tr_b16 a[44:45],v11 offset:33280\nv_fmac_f32 v26,v36,v48\nM a[176:179],a[8:11],a[56:59],a[176:179]\n" \
"ds_read_b64_tr_b16 a[46:47],v12 offset:33280\nv_and_b32 v38,0xffff0000,v38\nM v[128:131],a[88:91],a[96:99],v[128:131]\nbuffer_load_dwordx4 v19,s[28:31],s46 offen lds\nM v[132:135],a[92:95],a[96:99],v[132:135]\ns_add_u32 m0,s50,61440\nds_read_b64_tr_b16 a[48:49],v9 offset:33792\nv_fmac_f32 v25,v31,v43\n" \
"M v[140:143],a[92:95],a[100:103],v[140:143]\nds_read_b64_tr_b16 a[50:51],v10 offset:33792\nv_fmac_f32 v26,v37,v47\nM v[136:139],a[88:91],a[100:103],v[136:139]\nds_read_b64_tr_b16 a[52:53],v11 offset:33792\nds_read_b64_tr_b16 a[54:55],v12 offset:33792\nM v[152:155],a[88:91],a[108:111],v[152:155]\nbuffer_load_dwordx4 v20,s[28:31],s46 offen lds\n" \
"M v[156:159],a[92:95],a[108:111],v[156:159]\ns_add_u32 m0,s51,0\nv_fmac_f32 v25,v32,v42\nv_fmac_f32 v26,v38,v46\nM v[148:151],a[92:95],a[104:107],v[148:151]\ns_mov_b32 s46,128\nds_read_b64_tr_b16 a[56:57],v9 offset:34304\nds_read_b64_tr_b16 a[58:59],v10 offset:34304\n" \
"M v[144:147],a[88:91],a[104:107],v[144:147]\nds_read_b64_tr_b16 a[60:61],v11 offset:34304\nv_accvgpr_read_b32 v28,a225\nM v[160:163],a[88:91],a[112:115],v[160:163]\nbuffer_load_dwordx4 v21,s[32:35],s47 offen offset:0 lds\nM v[164:167],a[92:95],a[112:115],v[164:167]\ns_add_u32 m0,s51,896\nds_read_b64_tr_b16 a[62:63],v12 offset:34304\n" \
"C v26,v25,v26\nM v[172:175],a[92:95],a[116:119],v[172:175]\nds_read_b128 a[24:27],v5 offset:12288\nv_lshlrev_b32 v27,16,v28\nM v[168:171],a[88:91],a[116:119],v[168:171]\nds_read_b128 a[28:31],v5 offset:14336\nv_lshlrev_b32 v25,16,v26\nM v[184:187],a[88:91],a[124:127],v[184:187]\n" \
"buffer_load_dwordx4 v21,s[32:35],s47 offen offset:128 lds\nM v[188:191],a[92:95],a[124:127],v[188:191]\ns_add_u32 m0,s51,8192\nv_and_b32 v28,0xffff0000,v28\nv_and_b32 v26,0xffff0000,v26\nM v[180:183],a[92:95],a[120:123],v[180:183]\ns_mov_b32 s47,262144\nv_mul_f32 v25,v25,v27\n" \
"v_mul_f32 v26,v26,v28\nM v[176:179],a[88:91],a[120:123],v[176:179]\nC v51,v25,v29\nC v55,v26,v58\nM v[64:67],a[80:83],a[96:99],v[64:67]\nbuffer_load_dwordx4 v21,s[32:35],s47 offen offset:0 lds\nM v[68:71],a[84:87],a[96:99],v[68:71]\ns_add_u32 m0,s51,9088\n" \
"v_accvgpr_read_b32 v39,a211\nv_mul_f32 v25,v30,v45\nM v[76:79],a[84:87],a[100:103],v[76:79]\nv_lshlrev_b32 v33,16,v39\nv_mul_f32 v26,v36,v49\nM v[72:75],a[80:83],a[100:103],v[72:75]\nv_fmac_f32 v25,v31,v44\nv_fmac_f32 v26,v37,v48\n" \
"M v[88:91],a[80:83],a[108:111],v[88:91]\nbuffer_load_dwordx4 v21,s[32:35],s47 offen offset:128 lds\nM v[92:95],a[84:87],a[108:111],v[92:95]\ns_add_u32 m0,s51,16384\nv_and_b32 v39,0xffff0000,v39\nv_fmac_f32 v25,v32,v43\nM v[84:87],a[84:87],a[104:107],v[84:87]\ns_mov_b32 s47,524288\n" \
"v_fmac_f32 v26,v38,v47\nv_fmac_f32 v25,v33,v42\nM v[80:83],a[80:83],a[104:107],v[80:83]\nv_fmac_f32 v26,v39,v46\nv_accvgpr_read_b32 v28,a227\nM v[96:99],a[80:83],a[112:115],v[96:99]\nbuffer_load_dwordx4 v21,s[32:35],s47 offen offset:0 lds\nM v[100:103],a[84:87],a[112:115],v[100:103]\n" \
"s_add_u32 m0,s51,17280\nC v26,v25,v26\nv_lshlrev_b32 v27,16,v28\nM v[108:111],a[84:87],a[116:119],v[108:111]\nds_read_b128 a[16:19],v5 offset:8192\nv_lshlrev_b32 v25,16,v26\nM v[104:107],a[80:83],a[116:119],v[104:107]\nds_read_b128 a[20:23],v5 offset:10240\n" \
"v_and_b32 v28,0xffff0000,v28\nM v[120:123],a[80:83],a[124:127],v[120:123]\nbuffer_load_dwordx4 v21,s[32:35],s47 offen offset:128 lds\nM v[124:127],a[84:87],a[124:127],v[124:127]\ns_add_u32 m0,s51,24576\nv_and_b32 v26,0xffff0000,v26\nv_mul_f32 v29,v25,v27\nM v[116:119],a[84:87],a[120:123],v[116:119]\n" \
"s_mov_b32 s47,786432\nv_mul_f32 v58,v26,v28\nv_accvgpr_read_b32 v40,a213\nM v[112:115],a[80:83],a[120:123],v[112:115]\nv_lshlrev_b32 v34,16,v40\nv_mul_f32 v25,v31,v45\nM v[192:195],a[64:67],a[96:99],v[192:195]\ns_mov_b64 exec,0xff\n" \
"global_load_dword v0,v1,s[76:77]\ns_mov_b64 exec,-1\nM v[196:199],a[68:71],a[96:99],v[196:199]\nv_mul_f32 v26,v37,v49\nv_fmac_f32 v25,v32,v44\nM v[204:207],a[68:71],a[100:103],v[204:207]\ns_mov_b64 exec,0xff00\nglobal_load_dword v0,v1,s[76:77]\n" \
"s_mov_b64 exec,-1\nM v[200:203],a[64:67],a[100:103],v[200:203]\ns_add_u32 s76,s76,2097152\ns_addc_u32 s77,s77,0\nv_and_b32 v40,0xffff0000,v40\nv_fmac_f32 v26,v38,v48\nM v[216:219],a[64:67],a[108:111],v[216:219]\ns_mov_b64 exec,0xff\n" \
"global_load_dword v0,v1,s[76:77]\ns_mov_b64 exec,-1\nM v[220:223],a[68:71],a[108:111],v[220:223]\nv_fmac_f32 v25,v33,v43\nv_fmac_f32 v26,v39,v47\nM v[212:215],a[68:71],a[104:107],v[212:215]\ns_mov_b64 exec,0xff00\nglobal_load_dword v0,v1,s[76:77]\n" \
"s_mov_b64 exec,-1\nM v[208:211],a[64:67],a[104:107],v[208:211]\ns_mov_b32 s76,0xf149f2ca\nv_fmac_f32 v25,v34,v42\nv_fmac_f32 v26,v40,v46\nM v[224:227],a[64:67],a[112:115],v[224:227]\nv_accvgpr_read_b32 v28,a229\nC v26,v25,v26\n" \
"M v[228:231],a[68:71],a[112:115],v[228:231]\nv_lshlrev_b32 v27,16,v28\nv_lshlrev_b32 v25,16,v26\nM v[236:239],a[68:71],a[116:119],v[236:239]\nv_mul_f32 v25,v25,v27\nC v50,v25,v29\nM v[232:235],a[64:67],a[116:119],v[232:235]\nds_read_b128 a[0:3],v5 offset:0\n" \
"ds_read_b128 a[4:7],v5 offset:2048\nM v[248:251],a[64:67],a[124:127],v[248:251]\nds_read_b128 a[8:11],v5 offset:4096\nv_and_b32 v28,0xffff0000,v28\nM v[252:255],a[68:71],a[124:127],v[252:255]\nds_read_b128 a[12:15],v5 offset:6144\nv_and_b32 v26,0xffff0000,v26\nM v[244:247],a[68:71],a[120:123],v[244:247]\n" \
"v_mul_f32 v26,v26,v28\nC v54,v26,v58\nM v[240:243],a[64:67],a[120:123],v[240:243]\nds_read_b128 a[64:67],v6 offset:0\nds_read_b128 a[68:71],v6 offset:2048\nM a[128:131],a[72:75],a[96:99],a[128:131]\nds_read_b128 a[80:83],v6 offset:8192\nv_add_u32 v5,s54,v5\n" \
"M a[132:135],a[76:79],a[96:99],a[132:135]\nds_read_b128 a[84:87],v6 offset:10240\nv_add_u32 v7,s54,v7\nM a[140:143],a[76:79],a[100:103],a[140:143]\nds_read_b128 a[88:91],v6 offset:12288\nv_add_u32 v8,s54,v8\nM a[136:139],a[72:75],a[100:103],a[136:139]\nds_read_b128 a[92:95],v6 offset:14336\n" \
"v_mov_b32 v58,s18\nM a[152:155],a[72:75],a[108:111],a[152:155]\nv_add_u32 v25,-16,v4\nv_mov_b32 v29,s62\nM a[156:159],a[76:79],a[108:111],a[156:159]\ns_and_b32 s62,%1,7\nv_mov_b32 v28,0\nv_add_u32 v26,-512,v4\n" \
"M a[148:151],a[76:79],a[104:107],a[148:151]\ns_waitcnt vmcnt(18) lgkmcnt(14)\nv_add_u32 v27,512,v4\nM a[144:147],a[72:75],a[104:107],a[144:147]\ns_lshl_b32 s62,s62,8\ns_barrier\nds_read_b64_tr_b16 a[96:97],v9 offset:49152\nM a[160:163],a[72:75],a[112:115],a[160:163]\n" \
"s_add_u32 s62,s62,94380032\nds_read_b64_tr_b16 a[98:99],v10 offset:49152\nds_read_b64_tr_b16 a[100:101],v11 offset:49152\nM a[164:167],a[76:79],a[112:115],a[164:167]\nds_read_b64_tr_b16 a[102:103],v12 offset:49152\nM a[172:175],a[76:79],a[116:119],a[172:175]\nLG7_%=:\ns_cbranch_execz LG11_%=\n" \
"ds_read_b64_tr_b16 a[104:105],v9 offset:49664\nds_read_b64_tr_b16 a[106:107],v10 offset:49664\nM a[168:171],a[72:75],a[116:119],a[168:171]\nds_read_b64_tr_b16 a[108:109],v11 offset:49664\nM a[184:187],a[72:75],a[124:127],a[184:187]\nds_read_b64_tr_b16 a[110:111],v12 offset:49664\nM a[188:191],a[76:79],a[124:127],a[188:191]\nds_read_b64_tr_b16 a[112:113],v9 offset:50176\n" \
"ds_read_b64_tr_b16 a[114:115],v10 offset:50176\nM a[180:183],a[76:79],a[120:123],a[180:183]\nds_read_b64_tr_b16 a[116:117],v11 offset:50176\nM a[176:179],a[72:75],a[120:123],a[176:179]\ns_waitcnt lgkmcnt(7)\nds_read_b128 a[72:75],v6 offset:4096\nM v[128:131],a[24:27],a[32:35],v[128:131]\nds_read_b128 a[76:79],v6 offset:6144\n" \
"v_add_u32 v6,s54,v6\nM v[132:135],a[28:31],a[32:35],v[132:135]\nds_read_b64_tr_b16 a[118:119],v12 offset:50176\nM v[140:143],a[28:31],a[36:39],v[140:143]\nds_read_b64_tr_b16 a[120:121],v9 offset:50688\nds_read_b64_tr_b16 a[122:123],v10 offset:50688\nM v[136:139],a[24:27],a[36:39],v[136:139]\nds_read_b64_tr_b16 a[124:125],v11 offset:50688\n" \
"v_sub_u32 v9,v9,v62\nM v[152:155],a[24:27],a[44:47],v[152:155]\nds_read_b64_tr_b16 a[126:127],v12 offset:50688\nv_sub_u32 v10,v10,v62\nM v[156:159],a[28:31],a[44:47],v[156:159]\nv_sub_u32 v11,v11,v62\nv_sub_u32 v12,v12,v62\nM v[148:151],a[28:31],a[40:43],v[148:151]\n" \
"M v[144:147],a[24:27],a[40:43],v[144:147]\nM v[160:163],a[24:27],a[48:51],v[160:163]\nM v[164:167],a[28:31],a[48:51],v[164:167]\nM v[172:175],a[28:31],a[52:55],v[172:175]\nM v[168:171],a[24:27],a[52:55],v[168:171]\nM v[184:187],a[24:27],a[60:63],v[184:187]\nM v[188:191],a[28:31],a[60:63],v[188:191]\nM v[180:183],a[28:31],a[56:59],v[180:183]\n" \
"buffer_load_dwordx4 v21,s[32:35],s47 offen offset:0 lds\nM v[176:179],a[24:27],a[56:59],v[176:179]\ns_add_u32 m0,s51,25472\nM v[64:67],a[16:19],a[32:35],v[64:67]\nM v[68:71],a[20:23],a[32:35],v[68:71]\nM v[76:79],a[20:23],a[36:39],v[76:79]\nbuffer_store_dwordx4 v[50:53],v60,s[36:39],s98 offen offset:0 sc1\nM v[72:75],a[16:19],a[36:39],v[72:75]\n" \
"M v[88:91],a[16:19],a[44:47],v[88:91]\nM v[92:95],a[20:23],a[44:47],v[92:95]\nM v[84:87],a[20:23],a[40:43],v[84:87]\nM v[80:83],a[16:19],a[40:43],v[80:83]\nM v[96:99],a[16:19],a[48:51],v[96:99]\nM v[100:103],a[20:23],a[48:51],v[100:103]\nM v[108:111],a[20:23],a[52:55],v[108:111]\nM v[104:107],a[16:19],a[52:55],v[104:107]\n" \
"M v[120:123],a[16:19],a[60:63],v[120:123]\nM v[124:127],a[20:23],a[60:63],v[124:127]\nM v[116:119],a[20:23],a[56:59],v[116:119]\nM v[112:115],a[16:19],a[56:59],v[112:115]\nM v[192:195],a[0:3],a[32:35],v[192:195]\nM v[196:199],a[4:7],a[32:35],v[196:199]\nM v[204:207],a[4:7],a[36:39],v[204:207]\nM v[200:203],a[0:3],a[36:39],v[200:203]\n" \
"M v[216:219],a[0:3],a[44:47],v[216:219]\nM v[220:223],a[4:7],a[44:47],v[220:223]\nM v[212:215],a[4:7],a[40:43],v[212:215]\nM v[208:211],a[0:3],a[40:43],v[208:211]\nM v[224:227],a[0:3],a[48:51],v[224:227]\nM v[228:231],a[4:7],a[48:51],v[228:231]\nM v[236:239],a[4:7],a[52:55],v[236:239]\nbuffer_store_dwordx4 v[54:57],v60,s[36:39],s99 offen offset:0 sc1\n" \
"M v[232:235],a[0:3],a[52:55],v[232:235]\ns_nop 0\nv_lshlrev_b32 v56,23,v4\nv_bfe_u32 v57,v4,4,5\nM v[248:251],a[0:3],a[60:63],v[248:251]\nv_lshl_add_u32 v57,v57,3,s62\nv_add_u32 v55,s56,v4\nM v[252:255],a[4:7],a[60:63],v[252:255]\n" \
"M v[244:247],a[4:7],a[56:59],v[244:247]\nM v[240:243],a[0:3],a[56:59],v[240:243]\nM a[128:131],a[8:11],a[32:35],a[128:131]\nM a[132:135],a[12:15],a[32:35],a[132:135]\nM a[140:143],a[12:15],a[36:39],a[140:143]\nM a[136:139],a[8:11],a[36:39],a[136:139]\nM a[152:155],a[8:11],a[44:47],a[152:155]\nM a[156:159],a[12:15],a[44:47],a[156:159]\n" \
"M a[148:151],a[12:15],a[40:43],a[148:151]\ns_waitcnt vmcnt(9) lgkmcnt(0)\nM a[144:147],a[8:11],a[40:43],a[144:147]\ns_barrier\nds_read_b64_tr_b16 a[0:1],v9 offset:0\nM a[160:163],a[8:11],a[48:51],a[160:163]\nds_read_b64_tr_b16 a[2:3],v10 offset:0\nds_read_b64_tr_b16 a[4:5],v11 offset:0\n" \
"M a[164:167],a[12:15],a[48:51],a[164:167]\nds_read_b128 a[44:47],v5 offset:45056\nM a[172:175],a[12:15],a[52:55],a[172:175]\nds_read_b64_tr_b16 a[6:7],v12 offset:0\nM a[168:171],a[8:11],a[52:55],a[168:171]\nds_read_b64_tr_b16 a[16:17],v9 offset:1024\nds_read_b64_tr_b16 a[18:19],v10 offset:1024\nM a[184:187],a[8:11],a[60:63],a[184:187]\n" \
"ds_read_b64_tr_b16 a[20:21],v11 offset:1024\nM a[188:191],a[12:15],a[60:63],a[188:191]\nds_read_b64_tr_b16 a[22:23],v12 offset:1024\nM a[180:183],a[12:15],a[56:59],a[180:183]\nds_read_b64_tr_b16 a[24:25],v9 offset:1536\nds_read_b64_tr_b16 a[26:27],v10 offset:1536\nM a[176:179],a[8:11],a[56:59],a[176:179]\nds_read_b64_tr_b16 a[8:9],v9 offset:512\n" \
"M v[128:131],a[88:91],a[96:99],v[128:131]\nds_read_b64_tr_b16 a[10:11],v10 offset:512\nM v[132:135],a[92:95],a[96:99],v[132:135]\nds_read_b64_tr_b16 a[12:13],v11 offset:512\nds_read_b64_tr_b16 a[14:15],v12 offset:512\nM v[140:143],a[92:95],a[100:103],v[140:143]\nbuffer_load_dwordx4 v21,s[32:35],s47 offen offset:128 lds\nM v[136:139],a[88:91],a[100:103],v[136:139]\n" \
"s_add_u32 m0,s50,65536\nv_mul_f32 v128,s20,v128\nv_mul_f32 v129,s20,v129\nM v[152:155],a[88:91],a[108:111],v[152:155]\ns_mov_b32 s47,1048576\nv_mul_f32 v130,s20,v130\nv_mul_f32 v131,s20,v131\nM v[156:159],a[92:95],a[108:111],v[156:159]\n" \
"v_mul_f32 v132,s20,v132\nv_mul_f32 v133,s20,v133\nM v[148:151],a[92:95],a[104:107],v[148:151]\nbuffer_load_dwordx4 v13,s[28:31],s46 offen lds\nM v[144:147],a[88:91],a[104:107],v[144:147]\ns_add_u32 m0,s50,69632\nv_mul_f32 v134,s20,v134\nv_mul_f32 v135,s20,v135\n" \
"M v[160:163],a[88:91],a[112:115],v[160:163]\nv_mul_f32 v136,s20,v136\nv_mul_f32 v137,s20,v137\nM v[164:167],a[92:95],a[112:115],v[164:167]\nv_mul_f32 v138,s20,v138\nv_mul_f32 v139,s20,v139\nM v[172:175],a[92:95],a[116:119],v[172:175]\nbuffer_load_dwordx4 v14,s[28:31],s46 offen lds\n" \
"M v[168:171],a[88:91],a[116:119],v[168:171]\ns_add_u32 m0,s50,73728\nds_read_b64_tr_b16 a[28:29],v11 offset:1536\nv_mul_f32 v140,s20,v140\nM v[184:187],a[88:91],a[124:127],v[184:187]\nds_read_b64_tr_b16 a[30:31],v12 offset:1536\nds_read_b128 a[40:43],v5 offset:40960\nM v[188:191],a[92:95],a[124:127],v[188:191]\n" \
"v_mul_f32 v141,s20,v141\nv_mul_f32 v142,s20,v142\nM v[180:183],a[92:95],a[120:123],v[180:183]\nbuffer_load_dwordx4 v15,s[28:31],s46 offen lds\nM v[176:179],a[88:91],a[120:123],v[176:179]\ns_add_u32 m0,s50,77824\nv_mul_f32 v143,s20,v143\nv_mul_f32 v144,s20,v144\n" \
"M v[64:67],a[80:83],a[96:99],v[64:67]\nv_mul_f32 v145,s20,v145\nv_mul_f32 v146,s20,v146\nM v[68:71],a[84:87],a[96:99],v[68:71]\nds_read_b128 a[32:35],v5 offset:32768\nv_mul_f32 v147,s20,v147\nM v[76:79],a[84:87],a[100:103],v[76:79]\nbuffer_load_dwordx4 v16,s[28:31],s46 offen lds\n" \
"M v[72:75],a[80:83],a[100:103],v[72:75]\ns_add_u32 m0,s50,81920\ns_mov_b32 s46,128\nv_mul_f32 v64,s20,v64\nv_mul_f32 v65,s20,v65\nM v[88:91],a[80:83],a[108:111],v[88:91]\nv_mul_f32 v66,s20,v66\nv_mul_f32 v67,s20,v67\n" \
"M v[92:95],a[84:87],a[108:111],v[92:95]\nC v64,v64,v65\nC v65,v66,v67\nM v[84:87],a[84:87],a[104:107],v[84:87]\nbuffer_load_dwordx4 v17,s[28:31],s46 offen lds\nM v[80:83],a[80:83],a[104:107],v[80:83]\ns_add_u32 m0,s50,86016\nv_mul_f32 v68,s20,v68\n" \
"v_mul_f32 v69,s20,v69\nM v[96:99],a[80:83],a[112:115],v[96:99]\nv_mul_f32 v148,s20,v148\nv_mul_f32 v149,s20,v149\nM v[100:103],a[84:87],a[112:115],v[100:103]\nv_mul_f32 v150,s20,v150\nv_mul_f32 v151,s20,v151\nM v[108:111],a[84:87],a[116:119],v[108:111]\n" \
"buffer_load_dwordx4 v18,s[28:31],s46 offen lds\nM v[104:107],a[80:83],a[116:119],v[104:107]\ns_add_u32 m0,s50,90112\nv_mul_f32 v70,s20,v70\nv_mul_f32 v152,s20,v152\nM v[120:123],a[80:83],a[124:127],v[120:123]\nv_mul_f32 v71,s20,v71\nv_mul_f32 v153,s20,v153\n" \
"M v[124:127],a[84:87],a[124:127],v[124:127]\nv_mul_f32 v154,s20,v154\nv_mul_f32 v155,s20,v155\nM v[116:119],a[84:87],a[120:123],v[116:119]\nbuffer_load_dwordx4 v19,s[28:31],s46 offen lds\nM v[112:115],a[80:83],a[120:123],v[112:115]\ns_add_u32 m0,s50,94208\nv_mul_f32 v156,s20,v156\n" \
"v_mul_f32 v157,s20,v157\nM v[192:195],a[64:67],a[96:99],v[192:195]\nv_mul_f32 v158,s20,v158\nv_mul_f32 v159,s20,v159\nM v[196:199],a[68:71],a[96:99],v[196:199]\nv_mul_f32 v160,s20,v160\nv_mul_f32 v161,s20,v161\nM v[204:207],a[68:71],a[100:103],v[204:207]\n" \
"buffer_load_dwordx4 v20,s[28:31],s46 offen lds\nM v[200:203],a[64:67],a[100:103],v[200:203]\ns_add_u32 m0,s51,32768\nds_read_b128 a[36:39],v5 offset:36864\nv_mul_f32 v162,s20,v162\nM v[216:219],a[64:67],a[108:111],v[216:219]\ns_mov_b32 s46,256\nv_mul_f32 v163,s20,v163\n" \
"C v66,v68,v69\nM v[220:223],a[68:71],a[108:111],v[220:223]\nv_mul_f32 v72,s20,v72\nv_mul_f32 v164,s20,v164\nM v[212:215],a[68:71],a[104:107],v[212:215]\nbuffer_load_dwordx4 v21,s[32:35],s47 offen offset:0 lds\nM v[208:211],a[64:67],a[104:107],v[208:211]\ns_add_u32 m0,s51,33664\n" \
"v_mul_f32 v73,s20,v73\nC v30,v128,v129\nM v[224:227],a[64:67],a[112:115],v[224:227]\nv_mul_f32 v165,s20,v165\nC v31,v130,v131\nM v[228:231],a[68:71],a[112:115],v[228:231]\nv_mul_f32 v166,s20,v166\nC v32,v132,v133\n" \
"M v[236:239],a[68:71],a[116:119],v[236:239]\nbuffer_load_dwordx4 v21,s[32:35],s47 offen offset:128 lds\nM v[232:235],a[64:67],a[116:119],v[232:235]\ns_add_u32 m0,s51,40960\nv_mul_f32 v167,s20,v167\nC v33,v134,v135\nM v[248:251],a[64:67],a[124:127],v[248:251]\ns_mov_b32 s47,1310720\n" \
"v_mul_f32 v168,s20,v168\nC v34,v136,v137\nM v[252:255],a[68:71],a[124:127],v[252:255]\nv_mul_f32 v169,s20,v169\nC v35,v138,v139\nM v[244:247],a[68:71],a[120:123],v[244:247]\nbuffer_load_dwordx4 v21,s[32:35],s47 offen offset:0 lds\nM v[240:243],a[64:67],a[120:123],v[240:243]\n" \
"s_add_u32 m0,s51,41856\nds_read_b64_tr_b16 a[48:49],v9 offset:8192\nv_mul_f32 v170,s20,v170\nM a[128:131],a[72:75],a[96:99],a[128:131]\nds_read_b64_tr_b16 a[50:51],v10 offset:8192\nds_read_b128 a[80:83],v6 offset:32768\nM a[132:135],a[76:79],a[96:99],a[132:135]\nds_read_b128 a[84:87],v6 offset:36864\n" \
"v_mul_f32 v171,s20,v171\nM a[140:143],a[76:79],a[100:103],a[140:143]\nbuffer_load_dwordx4 v21,s[32:35],s47 offen offset:128 lds\nM a[136:139],a[72:75],a[100:103],a[136:139]\ns_add_u32 m0,s50,0\nv_mul_f32 v74,s20,v74\nC v36,v140,v141\nM a[152:155],a[72:75],a[108:111],a[152:155]\n" \
"s_mov_b32 s47,1572864\nds_read_b64_tr_b16 a[52:53],v11 offset:8192\nv_mul_f32 v75,s20,v75\nM a[156:159],a[76:79],a[108:111],a[156:159]\nds_read_b64_tr_b16 a[54:55],v12 offset:8192\nds_read_b64_tr_b16 a[56:57],v9 offset:8704\nM a[148:151],a[76:79],a[104:107],a[148:151]\nbuffer_load_dwordx4 v13,s[28:31],s46 offen lds\n" \
"M a[144:147],a[72:75],a[104:107],a[144:147]\ns_add_u32 m0,s50,4096\nC v37,v142,v143\nC v38,v144,v145\nM a[160:163],a[72:75],a[112:115],a[160:163]\nds_read_b64_tr_b16 a[58:59],v10 offset:8704\nds_read_b64_tr_b16 a[60:61],v11 offset:8704\nM a[164:167],a[76:79],a[112:115],a[164:167]\n" \
"ds_read_b64_tr_b16 a[62:63],v12 offset:8704\nC v39,v146,v147\nM a[172:175],a[76:79],a[116:119],a[172:175]\nbuffer_load_dwordx4 v14,s[28:31],s46 offen lds\nM a[168:171],a[72:75],a[116:119],a[168:171]\ns_add_u32 m0,s50,8192\nds_read_b64_tr_b16 a[64:65],v9 offset:9216\nv_mul_f32 v172,s20,v172\n" \
"M a[184:187],a[72:75],a[124:127],a[184:187]\nds_read_b64_tr_b16 a[66:67],v10 offset:9216\nv_mul_f32 v173,s20,v173\nM a[188:191],a[76:79],a[124:127],a[188:191]\ns_waitcnt lgkmcnt(5)\nds_read_b64_tr_b16 a[68:69],v11 offset:9216\nv_mul_f32 v174,s20,v174\nM a[180:183],a[76:79],a[120:123],a[180:183]\n" \
"buffer_load_dwordx4 v15,s[28:31],s46 offen lds\nM a[176:179],a[72:75],a[120:123],a[176:179]\nLC_%=:\ns_add_u32 m0,s50,12288\nds_read_b64_tr_b16 a[70:71],v12 offset:9216\nC v40,v148,v149\nM a[192:195],a[0:3],a[44:47],0\nds_read_b64_tr_b16 a[72:73],v9 offset:9728\n" \
"v_mul_f32 v175,s20,v175\nM a[196:199],a[4:7],a[44:47],0\nds_read_b64_tr_b16 a[74:75],v10 offset:9728\nds_read_b64_tr_b16 a[76:77],v11 offset:9728\nM a[200:203],a[8:11],a[44:47],0\nbuffer_load_dwordx4 v16,s[28:31],s46 offen lds\nM a[204:207],a[12:15],a[44:47],0\ns_mov_b32 s46,256\n" \
"C v41,v150,v151\nC v67,v70,v71\nM a[208:211],a[16:19],a[44:47],0\ns_add_u32 m0,s50,16384\nds_read_b64_tr_b16 a[78:79],v12 offset:9728\nds_read_b128 a[88:91],v6 offset:40960\nM a[212:215],a[20:23],a[44:47],0\nds_read_b128 a[92:95],v6 offset:45056\n" \
"v_mul_f32 v76,s20,v76\nM a[216:219],a[24:27],a[44:47],0\nbuffer_load_dwordx4 v17,s[28:31],s46 offen lds\nM a[220:223],a[28:31],a[44:47],0\ns_add_u32 m0,s50,20480\nv_mul_f32 v77,s20,v77\nC v42,v152,v153\nM a[224:227],a[0:3],a[40:43],0\n" \
"C v43,v154,v155\nC v44,v156,v157\nM a[228:231],a[4:7],a[40:43],0\nC v45,v158,v159\nC v46,v160,v161\nM a[232:235],a[8:11],a[40:43],0\nbuffer_load_dwordx4 v18,s[28:31],s46 offen lds\nM a[236:239],a[12:15],a[40:43],0\n" \
"s_add_u32 m0,s50,24576\nC v47,v162,v163\nC v68,v72,v73\nM a[240:243],a[16:19],a[40:43],0\nv_mul_f32 v176,s20,v176\nC v48,v164,v165\nM a[244:247],a[20:23],a[40:43],0\nv_mul_f32 v177,s20,v177\n" \
"C v49,v166,v167\nM a[248:251],a[24:27],a[40:43],0\nbuffer_load_dwordx4 v19,s[28:31],s46 offen lds\nM a[252:255],a[28:31],a[40:43],0\ns_add_u32 m0,s50,28672\nv_mul_f32 v178,s20,v178\nC v50,v168,v169\nM v[128:131],a[0:3],a[32:35],0\n" \
"v_mul_f32 v179,s20,v179\nC v51,v170,v171\nM v[132:135],a[4:7],a[32:35],0\nC v69,v74,v75\nv_mul_f32 v180,s20,v180\nM v[136:139],a[8:11],a[32:35],0\nbuffer_load_dwordx4 v20,s[28:31],s46 offen lds\nM v[140:143],a[12:15],a[32:35],0\n" \
"s_add_u32 m0,s51,49152\nv_mul_f32 v181,s20,v181\nC v52,v172,v173\nM v[144:147],a[16:19],a[32:35],0\ns_mov_b32 s46,384\nC v53,v174,v175\nC v70,v76,v77\nM v[148:151],a[20:23],a[32:35],0\n" \
"v_mul_f32 v182,s20,v182\nv_mul_f32 v183,s20,v183\nM v[152:155],a[24:27],a[32:35],0\nbuffer_load_dwordx4 v21,s[32:35],s47 offen offset:0 lds\nM v[156:159],a[28:31],a[32:35],0\ns_add_u32 m0,s51,50048\nv_mul_f32 v184,s20,v184\nv_mul_f32 v78,s20,v78\n" \
"M v[160:163],a[0:3],a[36:39],0\ns_waitcnt vmcnt(13) lgkmcnt(0)\nv_mul_f32 v185,s20,v185\nv_mul_f32 v79,s20,v79\nM v[164:167],a[4:7],a[36:39],0\ns_barrier\nC v72,v176,v177\nM v[168:171],a[8:11],a[36:39],0\n" \
"buffer_store_dwordx2 v[58:59],v56,s[36:39],s23 offen sc1\nM v[172:175],a[12:15],a[36:39],0\nC v73,v178,v179\nv_mul_f32 v186,s20,v186\nM v[176:179],a[16:19],a[36:39],0\nC v74,v180,v181\nC v75,v182,v183\nv_mul_f32 v187,s20,v187\n" \
"M v[180:183],a[20:23],a[36:39],0\nC v76,v184,v185\nC v77,v186,v187\nM v[184:187],a[24:27],a[36:39],0\nC v71,v78,v79\nv_mul_f32 v188,s20,v188\nv_mul_f32 v189,s20,v189\nv_mul_f32 v190,s20,v190\n" \
"v_mul_f32 v191,s20,v191\nC v78,v188,v189\nC v79,v190,v191\nM v[188:191],a[28:31],a[36:39],0\nbuffer_load_dwordx4 v21,s[32:35],s47 offen offset:128 lds\nM v[128:131],a[48:51],a[80:83],v[128:131]\ns_add_u32 m0,s51,57344\nds_read_b64_tr_b16 a[0:1],v9 offset:16384\n" \
"v_mul_f32 v80,s20,v80\nM v[160:163],a[48:51],a[84:87],v[160:163]\ns_mov_b32 s47,1835008\nds_read_b64_tr_b16 a[2:3],v10 offset:16384\nds_read_b128 a[32:35],v7 offset:0\nM a[224:227],a[48:51],a[88:91],a[224:227]\nds_read_b128 a[36:39],v7 offset:4096\nv_mul_f32 v81,s20,v81\n" \
"M a[192:195],a[48:51],a[92:95],a[192:195]\nbuffer_load_dwordx4 v21,s[32:35],s47 offen offset:0 lds\nM a[196:199],a[52:55],a[92:95],a[196:199]\ns_add_u32 m0,s51,58240\nv_mul_f32 v112,s20,v112\nv_mul_f32 v82,s20,v82\nM a[228:231],a[52:55],a[88:91],a[228:231]\nds_read_b128 a[40:43],v7 offset:8192\n" \
"v_mul_f32 v113,s20,v113\nM v[164:167],a[52:55],a[84:87],v[164:167]\nds_read_b128 a[44:47],v7 offset:12288\nv_mul_f32 v83,s20,v83\nM v[132:135],a[52:55],a[80:83],v[132:135]\nbuffer_load_dwordx4 v21,s[32:35],s47 offen offset:128 lds\nM v[136:139],a[56:59],a[80:83],v[136:139]\ns_add_u32 m0,s50,32768\n" \
"ds_read_b64_tr_b16 a[4:5],v11 offset:16384\nC v80,v80,v81\nM v[168:171],a[56:59],a[84:87],v[168:171]\ns_mov_b32 s47,2097152\nds_read_b64_tr_b16 a[6:7],v12 offset:16384\nv_mul_f32 v114,s20,v114\nM a[232:235],a[56:59],a[88:91],a[232:235]\nds_read_b64_tr_b16 a[8:9],v9 offset:16896\n" \
"v_mul_f32 v115,s20,v115\nM a[200:203],a[56:59],a[92:95],a[200:203]\nbuffer_load_dwordx4 v13,s[28:31],s46 offen lds\nM a[204:207],a[60:63],a[92:95],a[204:207]\ns_add_u32 m0,s50,36864\nds_read_b64_tr_b16 a[10:11],v10 offset:16896\nC v112,v112,v113\nM a[236:239],a[60:63],a[88:91],a[236:239]\n" \
"ds_read_b64_tr_b16 a[12:13],v11 offset:16896\nC v81,v82,v83\nM v[172:175],a[60:63],a[84:87],v[172:175]\nds_read_b64_tr_b16 a[14:15],v12 offset:16896\nds_read_b64_tr_b16 a[16:17],v9 offset:17408\nM v[140:143],a[60:63],a[80:83],v[140:143]\nbuffer_load_dwordx4 v14,s[28:31],s46 offen lds\nM v[144:147],a[64:67],a[80:83],v[144:147]\n" \
"s_add_u32 m0,s50,40960\nv_mul_f32 v84,s20,v84\nv_mul_f32 v85,s20,v85\nM v[176:179],a[64:67],a[84:87],v[176:179]\nds_read_b64_tr_b16 a[18:19],v10 offset:17408\nds_read_b64_tr_b16 a[20:21],v11 offset:17408\nM a[240:243],a[64:67],a[88:91],a[240:243]\nds_read_b64_tr_b16 a[22:23],v12 offset:17408\n" \
"C v113,v114,v115\nM a[208:211],a[64:67],a[92:95],a[208:211]\nbuffer_load_dwordx4 v15,s[28:31],s46 offen lds\nM a[212:215],a[68:71],a[92:95],a[212:215]\ns_add_u32 m0,s50,45056\nds_read_b64_tr_b16 a[24:25],v9 offset:17920\nv_mul_f32 v118,s20,v118\nM a[244:247],a[68:71],a[88:91],a[244:247]\n" \
"ds_read_b64_tr_b16 a[26:27],v10 offset:17920\nv_mul_f32 v119,s20,v119\nM v[180:183],a[68:71],a[84:87],v[180:183]\nds_read_b64_tr_b16 a[28:29],v11 offset:17920\nv_mul_f32 v86,s20,v86\nM v[148:151],a[68:71],a[80:83],v[148:151]\nbuffer_load_dwordx4 v16,s[28:31],s46 offen lds\nM v[152:155],a[72:75],a[80:83],v[152:155]\n" \
"s_add_u32 m0,s50,49152\nds_read_b64_tr_b16 a[30:31],v12 offset:17920\nv_mul_f32 v87,s20,v87\nM v[184:187],a[72:75],a[84:87],v[184:187]\ns_mov_b32 s46,384\nds_read_b64_tr_b16 a[48:49],v9 offset:24576\nC v82,v84,v85\nM a[248:251],a[72:75],a[88:91],a[248:251]\n" \
"ds_read_b64_tr_b16 a[50:51],v10 offset:24576\nds_read_b64_tr_b16 a[52:53],v11 offset:24576\nM a[216:219],a[72:75],a[92:95],a[216:219]\nbuffer_load_dwordx4 v17,s[28:31],s46 offen lds\nM a[220:223],a[76:79],a[92:95],a[220:223]\ns_add_u32 m0,s50,53248\nv_mul_f32 v88,s20,v88\nv_mul_f32 v89,s20,v89\n" \
"M a[252:255],a[76:79],a[88:91],a[252:255]\nds_read_b64_tr_b16 a[54:55],v12 offset:24576\nds_read_b64_tr_b16 a[56:57],v9 offset:25088\nM v[188:191],a[76:79],a[84:87],v[188:191]\ns_waitcnt lgkmcnt(5)\nds_read_b128 a[84:87],v8 offset:4096\nv_mul_f32 v90,s20,v90\nM v[156:159],a[76:79],a[80:83],v[156:159]\n" \
"buffer_load_dwordx4 v18,s[28:31],s46 offen lds\nM v[128:131],a[0:3],a[32:35],v[128:131]\ns_add_u32 m0,s50,57344\nv_mul_f32 v91,s20,v91\nC v115,v118,v119\nM v[160:163],a[0:3],a[36:39],v[160:163]\nds_read_b128 a[80:83],v8 offset:0\nv_mul_f32 v124,s20,v124\n" \
"M a[224:227],a[0:3],a[40:43],a[224:227]\nds_read_b64_tr_b16 a[58:59],v10 offset:25088\nds_read_b64_tr_b16 a[60:61],v11 offset:25088\nM a[192:195],a[0:3],a[44:47],a[192:195]\nbuffer_load_dwordx4 v19,s[28:31],s46 offen lds\nM a[196:199],a[4:7],a[44:47],a[196:199]\ns_add_u32 m0,s50,61440\nC v83,v86,v87\n" \
"v_mul_f32 v125,s20,v125\nM a[228:231],a[4:7],a[40:43],a[228:231]\nds_read_b64_tr_b16 a[62:63],v12 offset:25088\nds_read_b64_tr_b16 a[64:65],v9 offset:25600\nM v[164:167],a[4:7],a[36:39],v[164:167]\nds_read_b64_tr_b16 a[66:67],v10 offset:25600\nv_mul_f32 v92,s20,v92\nM v[132:135],a[4:7],a[32:35],v[132:135]\n" \
"buffer_load_dwordx4 v20,s[28:31],s46 offen lds\nM v[136:139],a[8:11],a[32:35],v[136:139]\ns_add_u32 m0,s51,0\nds_read_b64_tr_b16 a[68:69],v11 offset:25600\nv_mul_f32 v93,s20,v93\nM v[168:171],a[8:11],a[36:39],v[168:171]\ns_mov_b32 s46,512\nds_read_b64_tr_b16 a[70:71],v12 offset:25600\n" \
"C v84,v88,v89\nM a[232:235],a[8:11],a[40:43],a[232:235]\nds_read_b64_tr_b16 a[72:73],v9 offset:26112\nC v85,v90,v91\nM a[200:203],a[8:11],a[44:47],a[200:203]\nbuffer_load_dwordx4 v21,s[32:35],s47 offen offset:0 lds\nM a[204:207],a[12:15],a[44:47],a[204:207]\ns_add_u32 m0,s51,896\n" \
"ds_read_b64_tr_b16 a[74:75],v10 offset:26112\nv_mul_f32 v94,s20,v94\nM a[236:239],a[12:15],a[40:43],a[236:239]\nds_read_b64_tr_b16 a[76:77],v11 offset:26112\nv_mul_f32 v95,s20,v95\nM v[172:175],a[12:15],a[36:39],v[172:175]\nds_read_b64_tr_b16 a[78:79],v12 offset:26112\nds_read_b128 a[88:91],v8 offset:8192\n" \
"M v[140:143],a[12:15],a[32:35],v[140:143]\nbuffer_load_dwordx4 v21,s[32:35],s47 offen offset:128 lds\nM v[144:147],a[16:19],a[32:35],v[144:147]\ns_add_u32 m0,s51,8192\nC v118,v124,v125\nv_mul_f32 v96,s20,v96\nM v[176:179],a[16:19],a[36:39],v[176:179]\ns_mov_b32 s47,2359296\n" \
"ds_read_b128 a[92:95],v8 offset:12288\nv_mul_f32 v97,s20,v97\nM a[240:243],a[16:19],a[40:43],a[240:243]\nC v86,v92,v93\nv_mov_b32 v88,v30\nM a[208:211],a[16:19],a[44:47],a[208:211]\nbuffer_load_dwordx4 v21,s[32:35],s47 offen offset:0 lds\nM a[212:215],a[20:23],a[44:47],a[212:215]\n" \
"LG8_%=:\ns_cbranch_execz LG12_%=\ns_add_u32 m0,s51,9088\nv_mov_b32 v89,v31\nv_mov_b32 v90,v32\nM a[244:247],a[20:23],a[40:43],a[244:247]\nv_mov_b32 v91,v33\nC v87,v94,v95\n" \
"M v[180:183],a[20:23],a[36:39],v[180:183]\nv_mov_b32 v124,v50\nv_mul_f32 v98,s20,v98\nM v[148:151],a[20:23],a[32:35],v[148:151]\nbuffer_store_dwordx4 v[64:67],v4,s[36:39],s96 offen offset:0 sc1\nM v[152:155],a[24:27],a[32:35],v[152:155]\ns_waitcnt vmcnt(17) lgkmcnt(0)\nv_mul_f32 v99,s20,v99\n" \
"C v96,v96,v97\nM v[184:187],a[24:27],a[36:39],v[184:187]\ns_barrier\nds_read_b64_tr_b16 a[0:1],v9 offset:32768\nM a[248:251],a[24:27],a[40:43],a[248:251]\nds_read_b64_tr_b16 a[2:3],v10 offset:32768\nds_read_b64_tr_b16 a[4:5],v11 offset:32768\nM a[216:219],a[24:27],a[44:47],a[216:219]\n" \
"buffer_load_dwordx4 v21,s[32:35],s47 offen offset:128 lds\nM a[220:223],a[28:31],a[44:47],a[220:223]\ns_add_u32 m0,s50,65536\nds_read_b128 a[44:47],v5 offset:12288\nv_mov_b32 v92,v34\nM a[252:255],a[28:31],a[40:43],a[252:255]\ns_mov_b32 s47,2621440\nds_read_b128 a[40:43],v5 offset:8192\n" \
"v_mov_b32 v93,v35\nM v[188:191],a[28:31],a[36:39],v[188:191]\nds_read_b128 a[36:39],v5 offset:4096\nv_mov_b32 v94,v36\nM v[156:159],a[28:31],a[32:35],v[156:159]\nbuffer_load_dwordx4 v13,s[28:31],s46 offen lds\nM v[128:131],a[48:51],a[80:83],v[128:131]\ns_add_u32 m0,s50,69632\n" \
"v_mov_b32 v95,v37\nC v97,v98,v99\nM v[160:163],a[48:51],a[84:87],v[160:163]\nds_read_b128 a[32:35],v5 offset:0\nv_mul_f32 v100,s20,v100\nM a[224:227],a[48:51],a[88:91],a[224:227]\nds_read_b64_tr_b16 a[6:7],v12 offset:32768\nv_mul_f32 v101,s20,v101\n" \
"M a[192:195],a[48:51],a[92:95],a[192:195]\nbuffer_load_dwordx4 v14,s[28:31],s46 offen lds\nM a[196:199],a[52:55],a[92:95],a[196:199]\ns_add_u32 m0,s50,73728\nds_read_b64_tr_b16 a[8:9],v9 offset:33280\nv_mov_b32 v125,v51\nM a[228:231],a[52:55],a[88:91],a[228:231]\nds_read_b64_tr_b16 a[10:11],v10 offset:33280\n" \
"C v98,v100,v101\nM v[164:167],a[52:55],a[84:87],v[164:167]\nds_read_b64_tr_b16 a[12:13],v11 offset:33280\nds_read_b64_tr_b16 a[14:15],v12 offset:33280\nM v[132:135],a[52:55],a[80:83],v[132:135]\nbuffer_load_dwordx4 v15,s[28:31],s46 offen lds\nM v[136:139],a[56:59],a[80:83],v[136:139]\ns_add_u32 m0,s50,77824\n" \
"v_mul_f32 v104,s20,v104\nv_mul_f32 v105,s20,v105\nM v[168:171],a[56:59],a[84:87],v[168:171]\nds_read_b64_tr_b16 a[16:17],v9 offset:33792\nds_read_b64_tr_b16 a[18:19],v10 offset:33792\nM a[232:235],a[56:59],a[88:91],a[232:235]\nds_read_b64_tr_b16 a[20:21],v11 offset:33792\nv_mul_f32 v106,s20,v106\n" \
"M a[200:203],a[56:59],a[92:95],a[200:203]\nbuffer_load_dwordx4 v16,s[28:31],s46 offen lds\nM a[204:207],a[60:63],a[92:95],a[204:207]\ns_mov_b32 s46,512\nds_read_b64_tr_b16 a[22:23],v12 offset:33792\nv_mul_f32 v107,s20,v107\nM a[236:239],a[60:63],a[88:91],a[236:239]\ns_add_u32 m0,s50,81920\n" \
"ds_read_b64_tr_b16 a[24:25],v9 offset:34304\nC v100,v104,v105\nM v[172:175],a[60:63],a[84:87],v[172:175]\nds_read_b64_tr_b16 a[26:27],v10 offset:34304\nC v101,v106,v107\nM v[140:143],a[60:63],a[80:83],v[140:143]\nbuffer_load_dwordx4 v17,s[28:31],s46 offen lds\nM v[144:147],a[64:67],a[80:83],v[144:147]\n" \
"s_add_u32 m0,s50,86016\nds_read_b64_tr_b16 a[28:29],v11 offset:34304\nv_mov_b32 v104,v38\nM v[176:179],a[64:67],a[84:87],v[176:179]\nds_read_b64_tr_b16 a[30:31],v12 offset:34304\nv_mov_b32 v105,v39\nM a[240:243],a[64:67],a[88:91],a[240:243]\nds_read_b64_tr_b16 a[48:49],v9 offset:40960\n" \
"ds_read_b64_tr_b16 a[50:51],v10 offset:40960\nM a[208:211],a[64:67],a[92:95],a[208:211]\nbuffer_load_dwordx4 v18,s[28:31],s46 offen lds\nM a[212:215],a[68:71],a[92:95],a[212:215]\ns_add_u32 m0,s50,90112\nv_mov_b32 v106,v40\nv_mov_b32 v107,v41\nM a[244:247],a[68:71],a[88:91],a[244:247]\n" \
"ds_read_b64_tr_b16 a[52:53],v11 offset:40960\nds_read_b64_tr_b16 a[54:55],v12 offset:40960\nM v[180:183],a[68:71],a[84:87],v[180:183]\nds_read_b64_tr_b16 a[56:57],v9 offset:41472\nv_mul_f32 v126,s20,v126\nM v[148:151],a[68:71],a[80:83],v[148:151]\nbuffer_load_dwordx4 v19,s[28:31],s46 offen lds\nM v[152:155],a[72:75],a[80:83],v[152:155]\n" \
"s_add_u32 m0,s50,94208\nds_read_b64_tr_b16 a[58:59],v10 offset:41472\nv_mul_f32 v127,s20,v127\nM v[184:187],a[72:75],a[84:87],v[184:187]\nds_read_b64_tr_b16 a[60:61],v11 offset:41472\nC v119,v126,v127\nM a[248:251],a[72:75],a[88:91],a[248:251]\nds_read_b64_tr_b16 a[62:63],v12 offset:41472\n" \
"v_mul_f32 v102,s20,v102\nM a[216:219],a[72:75],a[92:95],a[216:219]\nbuffer_load_dwordx4 v20,s[28:31],s46 offen lds\nM a[220:223],a[76:79],a[92:95],a[220:223]\ns_add_u32 m0,s51,16384\nds_read_b64_tr_b16 a[64:65],v9 offset:41984\nv_mul_f32 v103,s20,v103\nM a[252:255],a[76:79],a[88:91],a[252:255]\n" \
"s_mov_b32 s46,640\nds_read_b64_tr_b16 a[66:67],v10 offset:41984\nv_mov_b32 v126,v52\nM v[188:191],a[76:79],a[84:87],v[188:191]\ns_waitcnt lgkmcnt(4)\nds_read_b128 a[84:87],v6 offset:4096\nC v99,v102,v103\nM v[156:159],a[76:79],a[80:83],v[156:159]\n" \
"buffer_load_dwordx4 v21,s[32:35],s47 offen offset:0 lds\nM v[128:131],a[0:3],a[32:35],v[128:131]\ns_add_u32 m0,s51,17280\nds_read_b128 a[80:83],v6 offset:0\nv_mul_f32 v108,s20,v108\nM v[160:163],a[0:3],a[36:39],v[160:163]\nds_read_b64_tr_b16 a[68:69],v11 offset:41984\nv_mul_f32 v109,s20,v109\n" \
"M a[224:227],a[0:3],a[40:43],a[224:227]\nds_read_b64_tr_b16 a[70:71],v12 offset:41984\nv_mul_f32 v110,s20,v110\nM a[192:195],a[0:3],a[44:47],a[192:195]\nbuffer_load_dwordx4 v21,s[32:35],s47 offen offset:128 lds\nM a[196:199],a[4:7],a[44:47],a[196:199]\ns_add_u32 m0,s51,24576\nds_read_b64_tr_b16 a[72:73],v9 offset:42496\n" \
"v_mul_f32 v111,s20,v111\nM a[228:231],a[4:7],a[40:43],a[228:231]\ns_mov_b32 s47,2883584\nds_read_b64_tr_b16 a[74:75],v10 offset:42496\nC v102,v108,v109\nM v[164:167],a[4:7],a[36:39],v[164:167]\nds_read_b64_tr_b16 a[76:77],v11 offset:42496\nds_read_b64_tr_b16 a[78:79],v12 offset:42496\n" \
"M v[132:135],a[4:7],a[32:35],v[132:135]\nbuffer_load_dwordx4 v21,s[32:35],s47 offen offset:0 lds\nM v[136:139],a[8:11],a[32:35],v[136:139]\ns_add_u32 m0,s51,25472\nC v103,v110,v111\nv_mov_b32 v108,v42\nM v[168:171],a[8:11],a[36:39],v[168:171]\nds_read_b128 a[88:91],v6 offset:8192\n" \
"v_mov_b32 v109,v43\nM a[232:235],a[8:11],a[40:43],a[232:235]\nds_read_b128 a[92:95],v6 offset:12288\nv_mov_b32 v110,v44\nM a[200:203],a[8:11],a[44:47],a[200:203]\nbuffer_load_dwordx4 v21,s[32:35],s47 offen offset:128 lds\nM a[204:207],a[12:15],a[44:47],a[204:207]\ns_add_u32 m0,s50,0\n" \
"v_mov_b32 v111,v45\nv_mul_f32 v116,s20,v116\nM a[236:239],a[12:15],a[40:43],a[236:239]\ns_mov_b32 s47,3145728\nv_mul_f32 v117,s20,v117\nC v114,v116,v117\nM v[172:175],a[12:15],a[36:39],v[172:175]\nv_mov_b32 v127,v53\n" \
"v_mul_f32 v120,s20,v120\nM v[140:143],a[12:15],a[32:35],v[140:143]\nbuffer_load_dwordx4 v[30:33],v23,s[68:71],0 offen offset:0\nM v[144:147],a[16:19],a[32:35],v[144:147]\nv_mul_f32 v121,s20,v121\nv_mul_f32 v122,s20,v122\nM v[176:179],a[16:19],a[36:39],v[176:179]\nv_mul_f32 v123,s20,v123\n" \
"C v116,v120,v121\nM a[240:243],a[16:19],a[40:43],a[240:243]\nC v117,v122,v123\nv_mov_b32 v120,v46\nM a[208:211],a[16:19],a[44:47],a[208:211]\nv_mov_b32 v121,v47\nv_mov_b32 v122,v48\nM a[212:215],a[20:23],a[44:47],a[212:215]\n" \
"v_mov_b32 v123,v49\nv_mul_f32 v192,s20,v192\nM a[244:247],a[20:23],a[40:43],a[244:247]\nbuffer_load_dword v50,v25,s[68:71],0 offen offset:0\nM v[180:183],a[20:23],a[36:39],v[180:183]\nv_mul_f32 v193,s20,v193\nv_mul_f32 v194,s20,v194\nM v[148:151],a[20:23],a[32:35],v[148:151]\n" \
"v_mul_f32 v195,s20,v195\nC v192,v192,v193\nM v[152:155],a[24:27],a[32:35],v[152:155]\ns_waitcnt vmcnt(19) lgkmcnt(0)\nC v193,v194,v195\nv_mul_f32 v196,s20,v196\nM v[184:187],a[24:27],a[36:39],v[184:187]\ns_barrier\n" \
"ds_read_b64_tr_b16 a[0:1],v9 offset:49152\nM a[248:251],a[24:27],a[40:43],a[248:251]\nds_read_b64_tr_b16 a[2:3],v10 offset:49152\nds_read_b64_tr_b16 a[4:5],v11 offset:49152\nM a[216:219],a[24:27],a[44:47],a[216:219]\nbuffer_load_dwordx4 v[34:37],v24,s[68:71],0 offen offset:0\nM a[220:223],a[28:31],a[44:47],a[220:223]\nds_read_b128 a[44:47],v5 offset:45056\n" \
"ds_read_b64_tr_b16 a[6:7],v12 offset:49152\nM a[252:255],a[28:31],a[40:43],a[252:255]\nds_read_b128 a[40:43],v5 offset:40960\nv_mul_f32 v197,s20,v197\nM v[188:191],a[28:31],a[36:39],v[188:191]\nds_read_b128 a[36:39],v5 offset:36864\nv_mul_f32 v198,s20,v198\nM v[156:159],a[28:31],a[32:35],v[156:159]\n" \
"ds_read_b128 a[32:35],v5 offset:32768\nv_mul_f32 v199,s20,v199\nM v[128:131],a[48:51],a[80:83],v[128:131]\nds_read_b64_tr_b16 a[8:9],v9 offset:49664\nC v194,v196,v197\nM v[160:163],a[48:51],a[84:87],v[160:163]\nbuffer_load_dwordx2 v[58:59],v57,s[36:39],0 offen sc1\nM a[224:227],a[48:51],a[88:91],a[224:227]\n" \
"ds_read_b64_tr_b16 a[10:11],v10 offset:49664\nds_read_b64_tr_b16 a[12:13],v11 offset:49664\nM a[192:195],a[48:51],a[92:95],a[192:195]\nbuffer_store_dwordx4 v[68:71],v4,s[36:39],s96 offen offset:1024 sc1\nM a[196:199],a[52:55],a[92:95],a[196:199]\nds_read_b64_tr_b16 a[14:15],v12 offset:49664\nds_read_b64_tr_b16 a[16:17],v9 offset:50176\nM a[228:231],a[52:55],a[88:91],a[228:231]\n" \
"ds_read_b64_tr_b16 a[18:19],v10 offset:50176\nv_mul_f32 v200,s20,v200\nM v[164:167],a[52:55],a[84:87],v[164:167]\nds_read_b64_tr_b16 a[20:21],v11 offset:50176\nds_read_b64_tr_b16 a[22:23],v12 offset:50176\nM v[132:135],a[52:55],a[80:83],v[132:135]\nbuffer_load_dword v51,v24,s[68:71],0 offen offset:16\nM v[136:139],a[56:59],a[80:83],v[136:139]\n" \
"ds_read_b64_tr_b16 a[24:25],v9 offset:50688\nds_read_b64_tr_b16 a[26:27],v10 offset:50688\nM v[168:171],a[56:59],a[84:87],v[168:171]\nds_read_b64_tr_b16 a[28:29],v11 offset:50688\nv_mul_f32 v201,s20,v201\nM a[232:235],a[56:59],a[88:91],a[232:235]\nds_read_b64_tr_b16 a[30:31],v12 offset:50688\nds_read_b64_tr_b16 a[48:49],v9 offset:57344\n" \
"M a[200:203],a[56:59],a[92:95],a[200:203]\nds_read_b64_tr_b16 a[50:51],v10 offset:57344\nv_mul_f32 v202,s20,v202\nM a[204:207],a[60:63],a[92:95],a[204:207]\nds_read_b64_tr_b16 a[52:53],v11 offset:57344\nC v195,v198,v199\nM a[236:239],a[60:63],a[88:91],a[236:239]\nbuffer_load_dwordx4 v[38:41],v24,s[68:71],0 offen offset:1024\n" \
"M v[172:175],a[60:63],a[84:87],v[172:175]\nds_read_b64_tr_b16 a[54:55],v12 offset:57344\nds_read_b64_tr_b16 a[56:57],v9 offset:57856\nM v[140:143],a[60:63],a[80:83],v[140:143]\nds_read_b64_tr_b16 a[58:59],v10 offset:57856\nv_mul_f32 v203,s20,v203\nM v[144:147],a[64:67],a[80:83],v[144:147]\nds_read_b64_tr_b16 a[60:61],v11 offset:57856\n" \
"ds_read_b64_tr_b16 a[62:63],v12 offset:57856\nM v[176:179],a[64:67],a[84:87],v[176:179]\nv_mul_f32 v204,s20,v204\nv_mul_f32 v205,s20,v205\nM a[240:243],a[64:67],a[88:91],a[240:243]\nC v196,v200,v201\nv_mul_f32 v208,s20,v208\nM a[208:211],a[64:67],a[92:95],a[208:211]\n" \
"buffer_load_dwordx4 v13,s[28:31],s46 offen lds\nM a[212:215],a[68:71],a[92:95],a[212:215]\ns_add_u32 m0,s50,4096\nds_read_b64_tr_b16 a[64:65],v9 offset:58368\nv_mul_f32 v209,s20,v209\nM a[244:247],a[68:71],a[88:91],a[244:247]\nds_read_b64_tr_b16 a[66:67],v10 offset:58368\nv_mul_f32 v210,s20,v210\n" \
"M v[180:183],a[68:71],a[84:87],v[180:183]\nv_mul_f32 v211,s20,v211\nv_mul_f32 v206,s20,v206\nM v[148:151],a[68:71],a[80:83],v[148:151]\nbuffer_load_dwordx4 v14,s[28:31],s46 offen lds\nM v[152:155],a[72:75],a[80:83],v[152:155]\ns_add_u32 m0,s50,8192\nds_read_b64_tr_b16 a[68:69],v11 offset:58368\n" \
"v_mul_f32 v207,s20,v207\nM v[184:187],a[72:75],a[84:87],v[184:187]\nds_read_b64_tr_b16 a[70:71],v12 offset:58368\nC v197,v202,v203\nM a[248:251],a[72:75],a[88:91],a[248:251]\nv_mul_f32 v212,s20,v212\nv_mul_f32 v213,s20,v213\nM a[216:219],a[72:75],a[92:95],a[216:219]\n" \
"buffer_load_dwordx4 v15,s[28:31],s46 offen lds\nM a[220:223],a[76:79],a[92:95],a[220:223]\ns_add_u32 m0,s50,12288\nds_read_b64_tr_b16 a[72:73],v9 offset:58880\nv_mul_f32 v214,s20,v214\nM a[252:255],a[76:79],a[88:91],a[252:255]\nds_read_b64_tr_b16 a[74:75],v10 offset:58880\nds_read_b128 a[88:91],v6 offset:40960\n" \
"M v[188:191],a[76:79],a[84:87],v[188:191]\ns_waitcnt lgkmcnt(4)\nds_read_b128 a[84:87],v6 offset:36864\nC v198,v204,v205\nM v[156:159],a[76:79],a[80:83],v[156:159]\nbuffer_load_dwordx4 v16,s[28:31],s46 offen lds\nM v[128:131],a[0:3],a[32:35],v[128:131]\ns_add_u32 m0,s50,16384\n" \
"v_mul_f32 v215,s20,v215\nv_mul_f32 v216,s20,v216\nM v[160:163],a[0:3],a[36:39],v[160:163]\ns_mov_b32 s46,640\nds_read_b128 a[80:83],v6 offset:32768\nv_mul_f32 v217,s20,v217\nM a[224:227],a[0:3],a[40:43],a[224:227]\nds_read_b64_tr_b16 a[76:77],v11 offset:58880\n" \
"v_mul_f32 v218,s20,v218\nM a[192:195],a[0:3],a[44:47],a[192:195]\nbuffer_load_dword v52,v24,s[68:71],0 offen offset:1040\nM a[196:199],a[4:7],a[44:47],a[196:199]\nds_read_b64_tr_b16 a[78:79],v12 offset:58880\nds_read_b128 a[92:95],v6 offset:45056\nM a[228:231],a[4:7],a[40:43],a[228:231]\nC v200,v208,v209\n" \
"v_mul_f32 v219,s20,v219\nM v[164:167],a[4:7],a[36:39],v[164:167]\nv_mul_f32 v224,s20,v224\nv_mul_f32 v225,s20,v225\nM v[132:135],a[4:7],a[32:35],v[132:135]\nv_mul_f32 v226,s20,v226\nv_mul_f32 v227,s20,v227\nM v[136:139],a[8:11],a[32:35],v[136:139]\n" \
"C v201,v210,v211\nv_mul_f32 v228,s20,v228\nM v[168:171],a[8:11],a[36:39],v[168:171]\nbuffer_load_dwordx4 v17,s[28:31],s46 offen lds\nM a[232:235],a[8:11],a[40:43],a[232:235]\ns_add_u32 m0,s50,20480\nv_mul_f32 v229,s20,v229\nv_mul_f32 v230,s20,v230\n" \
"M a[200:203],a[8:11],a[44:47],a[200:203]\nv_mul_f32 v231,s20,v231\nC v199,v206,v207\nM a[204:207],a[12:15],a[44:47],a[204:207]\nC v202,v212,v213\nv_mul_f32 v220,s20,v220\nM a[236:239],a[12:15],a[40:43],a[236:239]\nbuffer_load_dwordx4 v[42:45],v24,s[68:71],0 offen offset:2048\n" \
"M v[172:175],a[12:15],a[36:39],v[172:175]\nv_mul_f32 v221,s20,v221\nv_mul_f32 v232,s20,v232\nM v[140:143],a[12:15],a[32:35],v[140:143]\nv_mul_f32 v233,s20,v233\nv_mul_f32 v234,s20,v234\nM v[144:147],a[16:19],a[32:35],v[144:147]\nv_mul_f32 v235,s20,v235\n" \
"C v203,v214,v215\nM v[176:179],a[16:19],a[36:39],v[176:179]\nv_mul_f32 v236,s20,v236\nv_mul_f32 v237,s20,v237\nM a[240:243],a[16:19],a[40:43],a[240:243]\nv_mul_f32 v238,s20,v238\nv_mul_f32 v239,s20,v239\nM a[208:211],a[16:19],a[44:47],a[208:211]\n" \
"s_waitcnt vmcnt(10)\nbuffer_store_dwordx4 v[80:83],v4,s[36:39],s96 offen offset:2048 sc1\nM a[212:215],a[20:23],a[44:47],a[212:215]\nv_cmp_gt_u64 s[98:99],s[14:15],v[58:59]\nv_cmp_le_u64 s[18:19],s[16:17],v[58:59]\nM a[244:247],a[20:23],a[40:43],a[244:247]\ns_or_b64 s[98:99],s[98:99],s[18:19]\ns_cbranch_scc1 LPU_%=\n" \
"LPUR_%=:\nC v204,v216,v217\nv_mul_f32 v240,s20,v240\nM v[180:183],a[20:23],a[36:39],v[180:183]\nv_mul_f32 v241,s20,v241\nC v205,v218,v219\nM v[148:151],a[20:23],a[32:35],v[148:151]\nbuffer_load_dword v53,v24,s[68:71],0 offen offset:2064\n" \
"M v[152:155],a[24:27],a[32:35],v[152:155]\ns_waitcnt vmcnt(20) lgkmcnt(0)\nC v208,v224,v225\nC v209,v226,v227\nM v[184:187],a[24:27],a[36:39],v[184:187]\ns_barrier\nds_read_b64_tr_b16 a[0:1],v9 offset:0\nM a[248:251],a[24:27],a[40:43],a[248:251]\n" \
"ds_read_b64_tr_b16 a[2:3],v10 offset:0\nds_read_b64_tr_b16 a[4:5],v11 offset:0\nM a[216:219],a[24:27],a[44:47],a[216:219]\nds_read_b64_tr_b16 a[6:7],v12 offset:0\nv_mul_f32 v242,s20,v242\nM a[220:223],a[28:31],a[44:47],a[220:223]\nds_read_b128 a[44:47],v7 offset:12288\nv_mul_f32 v243,s20,v243\n" \
"M a[252:255],a[28:31],a[40:43],a[252:255]\nbuffer_load_dwordx4 v[46:49],v24,s[68:71],0 offen offset:3072\nM v[188:191],a[28:31],a[36:39],v[188:191]\nds_read_b128 a[36:39],v7 offset:4096\nds_read_b128 a[40:43],v7 offset:8192\nM v[156:159],a[28:31],a[32:35],v[156:159]\nds_read_b128 a[32:35],v7 offset:0\nv_mul_f32 v244,s20,v244\n" \
"M v[128:131],a[48:51],a[80:83],v[128:131]\nds_read_b64_tr_b16 a[8:9],v9 offset:512\nv_mul_f32 v245,s20,v245\nM v[160:163],a[48:51],a[84:87],v[160:163]\nds_read_b64_tr_b16 a[10:11],v10 offset:512\nv_mul_f32 v222,s20,v222\nM a[224:227],a[48:51],a[88:91],a[224:227]\nds_read_b64_tr_b16 a[12:13],v11 offset:512\n" \
"v_mul_f32 v223,s20,v223\nM a[192:195],a[48:51],a[92:95],a[192:195]\nbuffer_load_dword v54,v24,s[68:71],0 offen offset:3088\nM a[196:199],a[52:55],a[92:95],a[196:199]\nds_read_b64_tr_b16 a[14:15],v12 offset:512\nds_read_b64_tr_b16 a[16:17],v9 offset:1024\nM a[228:231],a[52:55],a[88:91],a[228:231]\nds_read_b64_tr_b16 a[18:19],v10 offset:1024\n" \
"C v210,v228,v229\nM v[164:167],a[52:55],a[84:87],v[164:167]\nds_read_b64_tr_b16 a[20:21],v11 offset:1024\nds_read_b64_tr_b16 a[22:23],v12 offset:1024\nM v[132:135],a[52:55],a[80:83],v[132:135]\nds_read_b64_tr_b16 a[24:25],v9 offset:1536\nC v211,v230,v231\nM v[136:139],a[56:59],a[80:83],v[136:139]\n" \
"ds_read_b64_tr_b16 a[26:27],v10 offset:1536\nv_mul_f32 v246,s20,v246\nM v[168:171],a[56:59],a[84:87],v[168:171]\nglobal_load_short_d16_hi v28,v29,s[12:13]\nM a[232:235],a[56:59],a[88:91],a[232:235]\nds_read_b64_tr_b16 a[28:29],v11 offset:1536\nds_read_b64_tr_b16 a[30:31],v12 offset:1536\nM a[200:203],a[56:59],a[92:95],a[200:203]\n" \
"ds_read_b64_tr_b16 a[48:49],v9 offset:8192\nv_mul_f32 v247,s20,v247\nM a[204:207],a[60:63],a[92:95],a[204:207]\nds_read_b64_tr_b16 a[50:51],v10 offset:8192\nds_read_b64_tr_b16 a[52:53],v11 offset:8192\nM a[236:239],a[60:63],a[88:91],a[236:239]\nds_read_b64_tr_b16 a[54:55],v12 offset:8192\nC v206,v220,v221\n" \
"M v[172:175],a[60:63],a[84:87],v[172:175]\nds_read_b64_tr_b16 a[56:57],v9 offset:8704\nC v212,v232,v233\nM v[140:143],a[60:63],a[80:83],v[140:143]\nbuffer_load_dwordx4 v18,s[28:31],s46 offen lds\nM v[144:147],a[64:67],a[80:83],v[144:147]\ns_add_u32 m0,s50,24576\nds_read_b64_tr_b16 a[58:59],v10 offset:8704\n" \
"C v213,v234,v235\nM v[176:179],a[64:67],a[84:87],v[176:179]\nds_read_b64_tr_b16 a[60:61],v11 offset:8704\nv_mul_f32 v248,s20,v248\nM a[240:243],a[64:67],a[88:91],a[240:243]\nds_read_b64_tr_b16 a[62:63],v12 offset:8704\nv_mul_f32 v249,s20,v249\nM a[208:211],a[64:67],a[92:95],a[208:211]\n" \
"buffer_load_dwordx4 v19,s[28:31],s46 offen lds\nM a[212:215],a[68:71],a[92:95],a[212:215]\ns_add_u32 m0,s50,28672\nds_read_b64_tr_b16 a[64:65],v9 offset:9216\nv_mul_f32 v250,s20,v250\nM a[244:247],a[68:71],a[88:91],a[244:247]\nds_read_b64_tr_b16 a[66:67],v10 offset:9216\nC v214,v236,v237\n" \
"M v[180:183],a[68:71],a[84:87],v[180:183]\nv_mul_f32 v251,s20,v251\nC v215,v238,v239\nM v[148:151],a[68:71],a[80:83],v[148:151]\nbuffer_load_dwordx4 v20,s[28:31],s46 offen lds\nM v[152:155],a[72:75],a[80:83],v[152:155]\ns_add_u32 m0,s56,0\nds_read_b64_tr_b16 a[68:69],v11 offset:9216\n" \
"C v216,v240,v241\nM v[184:187],a[72:75],a[84:87],v[184:187]\ns_mov_b32 s46,768\nds_read_b64_tr_b16 a[70:71],v12 offset:9216\nC v217,v242,v243\nM a[248:251],a[72:75],a[88:91],a[248:251]\nC v218,v244,v245\nC v207,v222,v223\n" \
"M a[216:219],a[72:75],a[92:95],a[216:219]\nbuffer_load_dwordx4 v23,s[68:71],0 offen offset:0 lds\nM a[220:223],a[76:79],a[92:95],a[220:223]\ns_add_u32 m0,s56,1024\nds_read_b64_tr_b16 a[72:73],v9 offset:9728\nC v219,v246,v247\nM a[252:255],a[76:79],a[88:91],a[252:255]\nds_read_b64_tr_b16 a[74:75],v10 offset:9728\n" \
"ds_read_b128 a[88:91],v8 offset:8192\nM v[188:191],a[76:79],a[84:87],v[188:191]\ns_waitcnt lgkmcnt(4)\nds_read_b128 a[84:87],v8 offset:4096\nC v220,v248,v249\nM v[156:159],a[76:79],a[80:83],v[156:159]\nbuffer_load_dwordx4 v24,s[68:71],0 offen offset:0 lds\nM v[128:131],a[0:3],a[32:35],v[128:131]\n" \
"s_add_u32 m0,s56,1024\nC v221,v250,v251\nv_mul_f32 v252,s20,v252\nM v[160:163],a[0:3],a[36:39],v[160:163]\nds_read_b128 a[80:83],v8 offset:0\nv_mul_f32 v253,s20,v253\nM a[224:227],a[0:3],a[40:43],a[224:227]\nds_read_b64_tr_b16 a[76:77],v11 offset:9728\n" \
"v_mul_f32 v254,s20,v254\nM a[192:195],a[0:3],a[44:47],a[192:195]\nbuffer_load_dwordx4 v24,s[68:71],0 offen offset:1024 lds\nM a[196:199],a[4:7],a[44:47],a[196:199]\ns_add_u32 m0,s56,1024\nds_read_b64_tr_b16 a[78:79],v12 offset:9728\nv_mul_f32 v255,s20,v255\nM a[228:231],a[4:7],a[40:43],a[228:231]\n" \
"ds_read_b128 a[92:95],v8 offset:12288\nC v222,v252,v253\nM v[164:167],a[4:7],a[36:39],v[164:167]\nC v223,v254,v255\nM v[132:135],a[4:7],a[32:35],v[132:135]\nbuffer_load_dwordx4 v24,s[68:71],0 offen offset:2048 lds\nM v[136:139],a[8:11],a[32:35],v[136:139]\ns_add_u32 m0,s56,1024\n" \
"M v[168:171],a[8:11],a[36:39],v[168:171]\nM a[232:235],a[8:11],a[40:43],a[232:235]\nM a[200:203],a[8:11],a[44:47],a[200:203]\nbuffer_load_dwordx4 v24,s[68:71],0 offen offset:3072 lds\nM a[204:207],a[12:15],a[44:47],a[204:207]\ns_add_u32 m0,s56,10304\nM a[236:239],a[12:15],a[40:43],a[236:239]\nM v[172:175],a[12:15],a[36:39],v[172:175]\n" \
"M v[140:143],a[12:15],a[32:35],v[140:143]\nM v[144:147],a[16:19],a[32:35],v[144:147]\nbuffer_load_dwordx4 v26,s[72:75],0 offen offset:0 sc1 lds\nM v[176:179],a[16:19],a[36:39],v[176:179]\ns_add_u32 m0,s56,11328\nM a[240:243],a[16:19],a[40:43],a[240:243]\nM a[208:211],a[16:19],a[44:47],a[208:211]\nM a[212:215],a[20:23],a[44:47],a[212:215]\n" \
"s_mov_b64 exec,0xff\nglobal_load_dword v0,v2,s[100:101]\ns_mov_b64 exec,-1\nM a[244:247],a[20:23],a[40:43],a[244:247]\nM v[180:183],a[20:23],a[36:39],v[180:183]\nbuffer_store_dwordx4 v[84:87],v4,s[36:39],s96 offen offset:3072 sc1\nM v[148:151],a[20:23],a[32:35],v[148:151]\ns_add_u32 s96,s57,36864\n" \
"M v[152:155],a[24:27],a[32:35],v[152:155]\ns_waitcnt vmcnt(8) lgkmcnt(0)\nM v[184:187],a[24:27],a[36:39],v[184:187]\ns_barrier\nds_read_b64_tr_b16 a[0:1],v9 offset:16384\nM a[248:251],a[24:27],a[40:43],a[248:251]\nbuffer_load_dwordx4 v27,s[72:75],0 offen offset:0 sc1 lds\nM a[216:219],a[24:27],a[44:47],a[216:219]\n" \
"LG9_%=:\ns_cbranch_execz LG13_%=\ns_add_u32 m0,s56,11328\nds_read_b64_tr_b16 a[2:3],v10 offset:16384\nM a[220:223],a[28:31],a[44:47],a[220:223]\nds_read_b128 a[44:47],v5 offset:12288\nM a[252:255],a[28:31],a[40:43],a[252:255]\nds_read_b128 a[40:43],v5 offset:8192\n" \
"M v[188:191],a[28:31],a[36:39],v[188:191]\nbuffer_load_dwordx4 v27,s[72:75],0 offen offset:1024 sc1 lds\nM v[156:159],a[28:31],a[32:35],v[156:159]\ns_add_u32 m0,s56,11328\nds_read_b128 a[32:35],v5 offset:0\nM v[128:131],a[48:51],a[80:83],v[128:131]\nds_read_b128 a[36:39],v5 offset:4096\nM v[160:163],a[48:51],a[84:87],v[160:163]\n" \
"ds_read_b64_tr_b16 a[4:5],v11 offset:16384\nM a[224:227],a[48:51],a[88:91],a[224:227]\nds_read_b64_tr_b16 a[6:7],v12 offset:16384\nM a[192:195],a[48:51],a[92:95],a[192:195]\nbuffer_load_dwordx4 v27,s[72:75],0 offen offset:2048 sc1 lds\nM a[196:199],a[52:55],a[92:95],a[196:199]\ns_add_u32 m0,s56,11328\nds_read_b64_tr_b16 a[8:9],v9 offset:16896\n" \
"M a[228:231],a[52:55],a[88:91],a[228:231]\nds_read_b64_tr_b16 a[10:11],v10 offset:16896\nM v[164:167],a[52:55],a[84:87],v[164:167]\nds_read_b64_tr_b16 a[12:13],v11 offset:16896\nds_read_b64_tr_b16 a[14:15],v12 offset:16896\nM v[132:135],a[52:55],a[80:83],v[132:135]\nbuffer_load_dwordx4 v27,s[72:75],0 offen offset:3072 sc1 lds\nM v[136:139],a[56:59],a[80:83],v[136:139]\n" \
"s_add_u32 m0,s51,0\nM v[168:171],a[56:59],a[84:87],v[168:171]\ns_mov_b32 s74,0\nds_read_b64_tr_b16 a[16:17],v9 offset:17408\nds_read_b64_tr_b16 a[18:19],v10 offset:17408\nM a[232:235],a[56:59],a[88:91],a[232:235]\nds_read_b64_tr_b16 a[20:21],v11 offset:17408\nM a[200:203],a[56:59],a[92:95],a[200:203]\n" \
"ds_read_b64_tr_b16 a[22:23],v12 offset:17408\nM a[204:207],a[60:63],a[92:95],a[204:207]\nbuffer_load_dwordx4 v21,s[32:35],s47 offen offset:0 lds\nM a[236:239],a[60:63],a[88:91],a[236:239]\ns_add_u32 m0,s51,896\nds_read_b64_tr_b16 a[24:25],v9 offset:17920\nM v[172:175],a[60:63],a[84:87],v[172:175]\nds_read_b64_tr_b16 a[26:27],v10 offset:17920\n" \
"M v[140:143],a[60:63],a[80:83],v[140:143]\nds_read_b64_tr_b16 a[28:29],v11 offset:17920\nds_read_b64_tr_b16 a[30:31],v12 offset:17920\nM v[144:147],a[64:67],a[80:83],v[144:147]\nbuffer_load_dwordx4 v21,s[32:35],s47 offen offset:128 lds\nM v[176:179],a[64:67],a[84:87],v[176:179]\ns_add_u32 m0,s51,8192\nM a[240:243],a[64:67],a[88:91],a[240:243]\n" \
"s_mov_b32 s47,3407872\nds_read_b64_tr_b16 a[48:49],v9 offset:24576\nds_read_b64_tr_b16 a[50:51],v10 offset:24576\nM a[208:211],a[64:67],a[92:95],a[208:211]\nds_read_b64_tr_b16 a[52:53],v11 offset:24576\nM a[212:215],a[68:71],a[92:95],a[212:215]\nds_read_b64_tr_b16 a[54:55],v12 offset:24576\nM a[244:247],a[68:71],a[88:91],a[244:247]\n" \
"s_waitcnt vmcnt(17)\nv_alignbyte_b32 v30,v31,v30,2\nv_alignbyte_b32 v31,v32,v31,2\nM v[180:183],a[68:71],a[84:87],v[180:183]\nv_alignbyte_b32 v32,v33,v32,2\nv_alignbyte_b32 v33,v50,v33,2\nM v[148:151],a[68:71],a[80:83],v[148:151]\nds_write_b128 v55,v[30:33] offset:5184\n" \
"M v[152:155],a[72:75],a[80:83],v[152:155]\nv_alignbyte_b32 v34,v35,v34,2\nv_alignbyte_b32 v35,v36,v35,2\nM v[184:187],a[72:75],a[84:87],v[184:187]\nv_alignbyte_b32 v36,v37,v36,2\nv_alignbyte_b32 v37,v51,v37,2\nM a[248:251],a[72:75],a[88:91],a[248:251]\nv_alignbyte_b32 v46,v47,v46,2\n" \
"v_alignbyte_b32 v47,v48,v47,2\nM a[216:219],a[72:75],a[92:95],a[216:219]\nds_write_b128 v55,v[34:37] offset:6208\nM a[220:223],a[76:79],a[92:95],a[220:223]\nv_lshrrev_b32 v30,4,v4\nv_alignbyte_b32 v48,v49,v48,2\nM a[252:255],a[76:79],a[88:91],a[252:255]\nv_and_b32 v31,15,v30\n" \
"v_alignbyte_b32 v49,v54,v49,2\nM v[188:191],a[76:79],a[84:87],v[188:191]\nv_add_u32 v33,1,v31\nv_lshrrev_b32 v32,4,v30\nM v[156:159],a[76:79],a[80:83],v[156:159]\ns_waitcnt lgkmcnt(1)\nv_and_b32 v34,1,v33\nv_and_b32 v33,30,v33\n" \
"M v[128:131],a[0:3],a[32:35],v[128:131]\nv_mul_u32_u24 v34,5184,v34\nv_lshl_add_u32 v33,v33,1,v34\nM v[160:163],a[0:3],a[36:39],v[160:163]\nds_write_b128 v55,v[46:49] offset:9280\nM a[224:227],a[0:3],a[40:43],a[224:227]\nv_lshl_add_u32 v33,v32,4,v33\nv_add_u32 v46,s56,v33\n" \
"M a[192:195],a[0:3],a[44:47],a[192:195]\nv_lshlrev_b32 v33,1,v31\nv_sub_u32 v33,v33,v32\nM a[196:199],a[4:7],a[44:47],a[196:199]\nds_read_b128 a[84:87],v6 offset:4096\nv_lshlrev_b32 v33,4,v33\nv_add_u32 v33,10320,v33\nM a[228:231],a[4:7],a[40:43],a[228:231]\n" \
"ds_read_b128 a[80:83],v6 offset:0\nv_add_u32 v50,s56,v33\nv_bfe_u32 v33,v4,6,3\nM v[164:167],a[4:7],a[36:39],v[164:167]\nds_read_b64_tr_b16 a[56:57],v9 offset:25088\nv_lshlrev_b32 v60,17,v33\nv_and_b32 v33,16,v4\nM v[132:135],a[4:7],a[32:35],v[132:135]\n" \
"v_lshl_add_u32 v60,v33,2,v60\nv_and_b32 v33,32,v4\nv_alignbyte_b32 v38,v39,v38,2\nM v[136:139],a[8:11],a[32:35],v[136:139]\nv_add_u32 v60,v60,v33\nv_lshrrev_b32 v33,9,v4\nv_alignbyte_b32 v39,v40,v39,2\nM v[168:171],a[8:11],a[36:39],v[168:171]\n" \
"v_lshl_add_u32 v60,v33,4,v60\nv_lshl_add_u32 v33,v32,3,v31\nv_alignbyte_b32 v40,v41,v40,2\nM a[232:235],a[8:11],a[40:43],a[232:235]\nv_sub_u32 v33,15,v33\nv_lshrrev_b32 v33,1,v33\nv_alignbyte_b32 v42,v43,v42,2\nM a[200:203],a[8:11],a[44:47],a[200:203]\n" \
"v_and_b32 v34,16,v4\nv_cmp_eq_u32 vcc,0,v33\nv_alignbyte_b32 v41,v52,v41,2\nM a[204:207],a[12:15],a[44:47],a[204:207]\nv_alignbyte_b32 v43,v44,v43,2\nv_lshrrev_b32 v34,v34,v28\nv_alignbyte_b32 v44,v45,v44,2\nM a[236:239],a[12:15],a[40:43],a[236:239]\n" \
"ds_read_b64_tr_b16 a[58:59],v10 offset:25088\nds_read_b64_tr_b16 a[60:61],v11 offset:25088\nv_cndmask_b32 v52,0,v34,vcc\nM v[172:175],a[12:15],a[36:39],v[172:175]\nds_read_b64_tr_b16 a[62:63],v12 offset:25088\nv_cmp_eq_u32 vcc,1,v33\nv_alignbyte_b32 v45,v53,v45,2\nM v[140:143],a[12:15],a[32:35],v[140:143]\n" \
"ds_read_b64_tr_b16 a[64:65],v9 offset:25600\nds_read_b64_tr_b16 a[66:67],v10 offset:25600\nM v[144:147],a[16:19],a[32:35],v[144:147]\nbuffer_store_dwordx4 v[96:99],v4,s[36:39],s96 offen offset:0 sc1\nds_read_b64_tr_b16 a[68:69],v11 offset:25600\nM v[176:179],a[16:19],a[36:39],v[176:179]\nds_read_b64_tr_b16 a[70:71],v12 offset:25600\nv_cndmask_b32 v53,0,v34,vcc\n" \
"M a[240:243],a[16:19],a[40:43],a[240:243]\nds_read_b64_tr_b16 a[72:73],v9 offset:26112\nds_read_b64_tr_b16 a[74:75],v10 offset:26112\nds_read_b64_tr_b16 a[76:77],v11 offset:26112\nds_read_b64_tr_b16 a[78:79],v12 offset:26112\nds_read_b128 a[88:91],v6 offset:8192\nds_read_b128 a[92:95],v6 offset:12288\nM a[208:211],a[16:19],a[44:47],a[208:211]\n" \
"v_cmp_eq_u32 vcc,2,v33\nv_add_u32 v47,1024,v46\nM a[212:215],a[20:23],a[44:47],a[212:215]\nv_cndmask_b32 v54,0,v34,vcc\nv_cmp_eq_u32 vcc,3,v33\nM a[244:247],a[20:23],a[40:43],a[244:247]\nds_write_b128 v55,v[38:41] offset:7232\nM v[180:183],a[20:23],a[36:39],v[180:183]\n" \
"v_add_u32 v48,2048,v46\nv_add_u32 v49,3072,v46\nM v[148:151],a[20:23],a[32:35],v[148:151]\nbuffer_load_dwordx4 v21,s[32:35],s47 offen offset:0 lds\nM v[152:155],a[24:27],a[32:35],v[152:155]\ns_waitcnt lgkmcnt(1)\nv_accvgpr_read_b32 v30,a128\nv_accvgpr_read_b32 v31,a129\n" \
"M v[184:187],a[24:27],a[36:39],v[184:187]\ns_add_u32 m0,s51,9088\ns_barrier\nv_accvgpr_read_b32 v32,a130\nM a[248:251],a[24:27],a[40:43],a[248:251]\nds_write_b128 v55,v[42:45] offset:8256\nM a[216:219],a[24:27],a[44:47],a[216:219]\nv_cndmask_b32 v55,0,v34,vcc\n" \
"v_accvgpr_read_b32 v33,a131\nM a[220:223],a[28:31],a[44:47],a[220:223]\nbuffer_load_dwordx4 v21,s[32:35],s47 offen offset:128 lds\nM a[252:255],a[28:31],a[40:43],a[252:255]\ns_add_u32 m0,s50,65536\nv_mul_f32 v30,s20,v30\nv_mul_f32 v31,s20,v31\nM v[188:191],a[28:31],a[36:39],v[188:191]\n" \
"s_mov_b32 s47,3670016\nv_mul_f32 v32,s20,v32\nv_mul_f32 v33,s20,v33\nM v[156:159],a[28:31],a[32:35],v[156:159]\nC v30,v30,v31\nv_accvgpr_read_b32 v34,a132\nM v[128:131],a[48:51],a[80:83],v[128:131]\nbuffer_load_dwordx4 v13,s[28:31],s46 offen lds\n" \
"M v[160:163],a[48:51],a[84:87],v[160:163]\ns_waitcnt vmcnt(6) lgkmcnt(0)\nds_read_b128 a[32:35],v50 offset:512\nv_accvgpr_read_b32 v35,a133\nM a[224:227],a[48:51],a[88:91],a[224:227]\ns_add_u32 m0,s50,69632\nds_read_b128 a[36:39],v50 offset:1024\nv_accvgpr_read_b32 v36,a134\n" \
"M a[192:195],a[48:51],a[92:95],a[192:195]\nds_read_b128 a[40:43],v50 offset:1536\nv_accvgpr_read_b32 v37,a135\nM a[196:199],a[52:55],a[92:95],a[196:199]\nds_read_b128 a[44:47],v50 offset:2048\nv_accvgpr_read_b32 v38,a136\nM a[228:231],a[52:55],a[88:91],a[228:231]\nbuffer_store_dwordx4 v[100:103],v4,s[36:39],s96 offen offset:1024 sc1\n" \
"M v[164:167],a[52:55],a[84:87],v[164:167]\nds_read_b128 a[48:51],v50 offset:2560\nds_read2_b32 a[0:1],v46 offset0:0 offset1:1\nM v[132:135],a[52:55],a[80:83],v[132:135]\nds_read_b128 a[52:55],v50 offset:3072\nC v31,v32,v33\nM v[136:139],a[56:59],a[80:83],v[136:139]\nds_read2_b32 a[2:3],v46 offset0:2 offset1:3\n" \
"v_accvgpr_read_b32 v39,a137\nM v[168:171],a[56:59],a[84:87],v[168:171]\nds_read2_b32 a[4:5],v46 offset0:128 offset1:129\nv_mul_f32 v34,s20,v34\nM a[232:235],a[56:59],a[88:91],a[232:235]\nds_read2_b32 a[6:7],v46 offset0:130 offset1:131\nds_read2_b32 a[8:9],v47 offset0:0 offset1:1\nM a[200:203],a[56:59],a[92:95],a[200:203]\n" \
"ds_read_b128 a[56:59],v50 offset:3584\nv_mul_f32 v35,s20,v35\nM a[204:207],a[60:63],a[92:95],a[204:207]\nds_read2_b32 a[10:11],v47 offset0:2 offset1:3\nv_mul_f32 v36,s20,v36\nM a[236:239],a[60:63],a[88:91],a[236:239]\nds_read2_b32 a[12:13],v47 offset0:128 offset1:129\nv_mul_f32 v37,s20,v37\n" \
"M v[172:175],a[60:63],a[84:87],v[172:175]\nds_read2_b32 a[14:15],v47 offset0:130 offset1:131\nds_read2_b32 a[16:17],v48 offset0:0 offset1:1\nM v[140:143],a[60:63],a[80:83],v[140:143]\nds_read_b128 a[60:63],v50 offset:4096\nv_accvgpr_read_b32 v40,a138\nM v[144:147],a[64:67],a[80:83],v[144:147]\nds_read2_b32 a[18:19],v48 offset0:2 offset1:3\n" \
"v_accvgpr_read_b32 v41,a139\nM v[176:179],a[64:67],a[84:87],v[176:179]\nds_read2_b32 a[20:21],v48 offset0:128 offset1:129\nv_mul_f32 v38,s20,v38\nM a[240:243],a[64:67],a[88:91],a[240:243]\nbuffer_store_dwordx4 v[112:115],v4,s[36:39],s96 offen offset:2048 sc1\nM a[208:211],a[64:67],a[92:95],a[208:211]\nds_read2_b32 a[22:23],v48 offset0:130 offset1:131\n" \
"ds_read2_b32 a[24:25],v49 offset0:0 offset1:1\nM a[212:215],a[68:71],a[92:95],a[212:215]\nds_read2_b32 a[26:27],v49 offset0:2 offset1:3\nv_mul_f32 v39,s20,v39\nM a[244:247],a[68:71],a[88:91],a[244:247]\nds_read2_b32 a[28:29],v49 offset0:128 offset1:129\nds_read2_b32 a[30:31],v49 offset0:130 offset1:131\nM v[180:183],a[68:71],a[84:87],v[180:183]\n" \
"ds_read_b128 a[96:99],v50 offset:448\nC v32,v34,v35\nM v[148:151],a[68:71],a[80:83],v[148:151]\nds_read2_b32 a[64:65],v46 offset0:16 offset1:17\nC v33,v36,v37\nM v[152:155],a[72:75],a[80:83],v[152:155]\nds_read2_b32 a[66:67],v46 offset0:18 offset1:19\nv_accvgpr_write_b32 a128,v30\n" \
"M v[184:187],a[72:75],a[84:87],v[184:187]\nds_read_b128 a[100:103],v50 offset:960\nv_accvgpr_write_b32 a129,v31\nM a[248:251],a[72:75],a[88:91],a[248:251]\nds_read2_b32 a[68:69],v46 offset0:144 offset1:145\nds_read2_b32 a[70:71],v46 offset0:146 offset1:147\nM a[216:219],a[72:75],a[92:95],a[216:219]\nds_read_b128 a[104:107],v50 offset:1472\n" \
"v_mul_f32 v40,s20,v40\nM a[220:223],a[76:79],a[92:95],a[220:223]\nds_read2_b32 a[72:73],v47 offset0:16 offset1:17\nv_mul_f32 v41,s20,v41\nM a[252:255],a[76:79],a[88:91],a[252:255]\nds_read2_b32 a[74:75],v47 offset0:18 offset1:19\nC v38,v38,v39\nM v[188:191],a[76:79],a[84:87],v[188:191]\n" \
"s_waitcnt lgkmcnt(6)\nds_read_b128 a[108:111],v50 offset:1984\nv_accvgpr_read_b32 v42,a140\nM v[156:159],a[76:79],a[80:83],v[156:159]\nbuffer_store_dwordx4 v[116:119],v4,s[36:39],s96 offen offset:3072 sc1\nM v[224:227],v[52:55],a[32:35],0\ns_add_u32 s96,s57,0\nds_read2_b32 a[76:77],v47 offset0:144 offset1:145\n" \
"ds_read2_b32 a[78:79],v47 offset0:146 offset1:147\nM v[228:231],v[52:55],a[36:39],0\nds_read_b128 a[112:115],v50 offset:2496\nv_accvgpr_read_b32 v43,a141\nM v[232:235],v[52:55],a[40:43],0\nds_read2_b32 a[80:81],v48 offset0:16 offset1:17\nv_accvgpr_read_b32 v44,a142\nM v[236:239],v[52:55],a[44:47],0\n" \
"ds_read2_b32 a[82:83],v48 offset0:18 offset1:19\nds_read_b128 a[116:119],v50 offset:3008\nM v[240:243],v[52:55],a[48:51],0\nds_read2_b32 a[84:85],v48 offset0:144 offset1:145\nv_accvgpr_read_b32 v45,a143\nM v[244:247],v[52:55],a[52:55],0\nds_read2_b32 a[86:87],v48 offset0:146 offset1:147\nv_accvgpr_write_b32 a130,v32\n" \
"M v[248:251],v[52:55],a[56:59],0\nds_read_b128 a[120:123],v50 offset:3520\nv_accvgpr_write_b32 a131,v33\nM v[252:255],v[52:55],a[60:63],0\nds_read2_b32 a[88:89],v49 offset0:16 offset1:17\nv_accvgpr_read_b32 v30,a144\nM v[224:227],a[0:3],a[32:35],v[224:227]\nds_read2_b32 a[90:91],v49 offset0:18 offset1:19\n" \
"ds_read_b128 a[124:127],v50 offset:4032\nM v[228:231],a[0:3],a[36:39],v[228:231]\nds_read2_b32 a[92:93],v49 offset0:144 offset1:145\nv_accvgpr_read_b32 v31,a145\nM v[228:231],a[4:7],a[32:35],v[228:231]\nds_read2_b32 a[94:95],v49 offset0:146 offset1:147\nC v39,v40,v41\nM v[232:235],a[0:3],a[40:43],v[232:235]\n" \
"v_mul_f32 v42,s20,v42\nv_mul_f32 v43,s20,v43\nM v[232:235],a[4:7],a[36:39],v[232:235]\nbuffer_store_dwordx4 v[192:195],v4,s[36:39],s96 offen offset:0 sc1\nM v[232:235],a[8:11],a[32:35],v[232:235]\nv_mul_f32 v44,s20,v44\nv_mul_f32 v45,s20,v45\nM v[236:239],a[0:3],a[44:47],v[236:239]\n" \
"v_accvgpr_read_b32 v32,a146\nv_accvgpr_read_b32 v33,a147\nM v[236:239],a[4:7],a[40:43],v[236:239]\nv_mul_f32 v30,s20,v30\nv_mul_f32 v31,s20,v31\nM v[236:239],a[8:11],a[36:39],v[236:239]\nbuffer_load_dwordx4 v14,s[28:31],s46 offen lds\nM v[236:239],a[12:15],a[32:35],v[236:239]\n" \
"s_add_u32 m0,s50,73728\nC v40,v42,v43\nC v41,v44,v45\nM v[240:243],a[0:3],a[48:51],v[240:243]\nv_accvgpr_write_b32 a132,v38\nv_accvgpr_write_b32 a133,v39\nM v[240:243],a[4:7],a[44:47],v[240:243]\nv_mul_f32 v32,s20,v32\n" \
"v_mul_f32 v33,s20,v33\nM v[240:243],a[8:11],a[40:43],v[240:243]\nbuffer_load_dwordx4 v15,s[28:31],s46 offen lds\nM v[240:243],a[12:15],a[36:39],v[240:243]\ns_add_u32 m0,s50,77824\nC v30,v30,v31\nv_accvgpr_write_b32 a134,v40\nM v[240:243],a[16:19],a[32:35],v[240:243]\n" \
"v_accvgpr_write_b32 a135,v41\nv_accvgpr_read_b32 v34,a148\nM v[244:247],a[0:3],a[52:55],v[244:247]\nv_accvgpr_read_b32 v35,a149\nv_accvgpr_read_b32 v36,a150\nM v[244:247],a[4:7],a[48:51],v[244:247]\nbuffer_load_dwordx4 v16,s[28:31],s46 offen lds\nM v[244:247],a[8:11],a[44:47],v[244:247]\n" \
"s_add_u32 m0,s50,81920\nv_accvgpr_read_b32 v37,a151\nv_accvgpr_read_b32 v38,a152\nM v[244:247],a[12:15],a[40:43],v[244:247]\ns_mov_b32 s46,768\nC v31,v32,v33\nv_accvgpr_read_b32 v39,a153\nM v[244:247],a[16:19],a[36:39],v[244:247]\n" \
"v_mul_f32 v34,s20,v34\nv_mul_f32 v35,s20,v35\nM v[244:247],a[20:23],a[32:35],v[244:247]\nv_mul_f32 v36,s20,v36\nv_mul_f32 v37,s20,v37\nM v[248:251],a[0:3],a[56:59],v[248:251]\nbuffer_store_dwordx4 v[196:199],v4,s[36:39],s96 offen offset:1024 sc1\nM v[248:251],a[4:7],a[52:55],v[248:251]\n" \
"v_accvgpr_read_b32 v40,a154\nv_accvgpr_read_b32 v41,a155\nM v[248:251],a[8:11],a[48:51],v[248:251]\nv_mul_f32 v38,s20,v38\nv_mul_f32 v39,s20,v39\nM v[248:251],a[12:15],a[44:47],v[248:251]\nC v32,v34,v35\nC v33,v36,v37\n" \
"M v[248:251],a[16:19],a[40:43],v[248:251]\nbuffer_load_dwordx4 v17,s[28:31],s46 offen lds\nM v[248:251],a[20:23],a[36:39],v[248:251]\ns_add_u32 m0,s50,86016\nv_mul_f32 v40,s20,v40\nv_mul_f32 v41,s20,v41\nM v[248:251],a[24:27],a[32:35],v[248:251]\nC v38,v38,v39\n" \
"v_accvgpr_read_b32 v42,a156\nM v[252:255],a[0:3],a[60:63],v[252:255]\nds_read2_b32 a[0:1],v46 offset0:32 offset1:33\nds_read2_b32 a[2:3],v46 offset0:34 offset1:35\nM v[252:255],a[4:7],a[56:59],v[252:255]\nbuffer_load_dwordx4 v18,s[28:31],s46 offen lds\nM v[252:255],a[8:11],a[52:55],v[252:255]\ns_add_u32 m0,s50,90112\n" \
"ds_read2_b32 a[4:5],v46 offset0:160 offset1:161\nv_accvgpr_write_b32 a136,v30\nM v[252:255],a[12:15],a[48:51],v[252:255]\nds_read2_b32 a[6:7],v46 offset0:162 offset1:163\nv_accvgpr_read_b32 v43,a157\nM v[252:255],a[16:19],a[44:47],v[252:255]\nds_read2_b32 a[8:9],v47 offset0:32 offset1:33\nv_accvgpr_write_b32 a137,v31\n" \
"M v[252:255],a[20:23],a[40:43],v[252:255]\nds_read_b128 a[40:43],v50 offset:1408\nv_accvgpr_write_b32 a138,v32\nM v[252:255],a[24:27],a[36:39],v[252:255]\nbuffer_store_dwordx4 v[200:203],v4,s[36:39],s96 offen offset:2048 sc1\nM v[252:255],a[28:31],a[32:35],v[252:255]\nds_read_b128 a[32:35],v50 offset:384\nds_read_b128 a[36:39],v50 offset:896\n" \
"M v[224:227],a[64:67],a[96:99],v[224:227]\ns_waitcnt lgkmcnt(5)\nds_read2_b32 a[10:11],v47 offset0:34 offset1:35\nv_accvgpr_write_b32 a139,v33\nM v[228:231],a[64:67],a[100:103],v[228:231]\nds_read_b128 a[44:47],v50 offset:1920\nv_accvgpr_read_b32 v44,a158\nM v[228:231],a[68:71],a[96:99],v[228:231]\n" \
"buffer_load_dwordx4 v19,s[28:31],s46 offen lds\nM v[232:235],a[64:67],a[104:107],v[232:235]\ns_add_u32 m0,s50,94208\nC v39,v40,v41\nv_accvgpr_read_b32 v45,a159\nM v[232:235],a[68:71],a[100:103],v[232:235]\nds_read2_b32 a[12:13],v47 offset0:160 offset1:161\nds_read2_b32 a[14:15],v47 offset0:162 offset1:163\n" \
"M v[232:235],a[72:75],a[96:99],v[232:235]\nds_read_b128 a[48:51],v50 offset:2432\nv_mul_f32 v42,s20,v42\nM v[236:239],a[64:67],a[108:111],v[236:239]\nbuffer_load_dwordx4 v20,s[28:31],s46 offen lds\nM v[236:239],a[68:71],a[104:107],v[236:239]\ns_add_u32 m0,s51,16384\nv_mul_f32 v43,s20,v43\n" \
"v_mul_f32 v44,s20,v44\nM v[236:239],a[72:75],a[100:103],v[236:239]\ns_mov_b32 s46,896\nds_read2_b32 a[16:17],v48 offset0:32 offset1:33\nds_read2_b32 a[18:19],v48 offset0:34 offset1:35\nM v[236:239],a[76:79],a[96:99],v[236:239]\nds_read_b128 a[52:55],v50 offset:2944\nv_mul_f32 v45,s20,v45\n" \
"M v[240:243],a[64:67],a[112:115],v[240:243]\nbuffer_load_dwordx4 v21,s[32:35],s47 offen offset:0 lds\nM v[240:243],a[68:71],a[108:111],v[240:243]\ns_add_u32 m0,s51,17280\nv_accvgpr_read_b32 v30,a160\nv_accvgpr_read_b32 v31,a161\nM v[240:243],a[72:75],a[104:107],v[240:243]\nds_read2_b32 a[20:21],v48 offset0:160 offset1:161\n" \
"ds_read2_b32 a[22:23],v48 offset0:162 offset1:163\nM v[240:243],a[76:79],a[100:103],v[240:243]\nds_read_b128 a[56:59],v50 offset:3456\nC v40,v42,v43\nM v[240:243],a[80:83],a[96:99],v[240:243]\nbuffer_load_dwordx4 v21,s[32:35],s47 offen offset:128 lds\nM v[244:247],a[64:67],a[116:119],v[244:247]\ns_add_u32 m0,s51,24576\n" \
"C v41,v44,v45\nv_accvgpr_read_b32 v32,a162\nM v[244:247],a[68:71],a[112:115],v[244:247]\ns_mov_b32 s47,3932160\nds_read2_b32 a[24:25],v49 offset0:32 offset1:33\nds_read2_b32 a[26:27],v49 offset0:34 offset1:35\nM v[244:247],a[72:75],a[108:111],v[244:247]\nds_read_b128 a[60:63],v50 offset:3968\n" \
"v_accvgpr_read_b32 v33,a163\nM v[244:247],a[76:79],a[104:107],v[244:247]\nbuffer_load_dwordx4 v21,s[32:35],s47 offen offset:0 lds\nM v[244:247],a[80:83],a[100:103],v[244:247]\ns_add_u32 m0,s51,25472\nv_mul_f32 v30,s20,v30\nv_accvgpr_write_b32 a140,v38\nM v[244:247],a[84:87],a[96:99],v[244:247]\n" \
"ds_read2_b32 a[28:29],v49 offset0:160 offset1:161\nds_read2_b32 a[30:31],v49 offset0:162 offset1:163\nM v[248:251],a[64:67],a[120:123],v[248:251]\nv_mul_f32 v31,s20,v31\nv_accvgpr_write_b32 a141,v39\nM v[248:251],a[68:71],a[116:119],v[248:251]\nv_accvgpr_write_b32 a142,v40\nv_accvgpr_write_b32 a143,v41\n" \
"M v[248:251],a[72:75],a[112:115],v[248:251]\nbuffer_store_dwordx4 v[204:207],v4,s[36:39],s96 offen offset:3072 sc1\nM v[248:251],a[76:79],a[108:111],v[248:251]\ns_add_u32 s96,s57,4096\nv_mul_f32 v32,s20,v32\nv_mul_f32 v33,s20,v33\nM v[248:251],a[80:83],a[104:107],v[248:251]\nC v30,v30,v31\n" \
"v_accvgpr_read_b32 v34,a164\nM v[248:251],a[84:87],a[100:103],v[248:251]\nv_accvgpr_read_b32 v35,a165\nv_accvgpr_read_b32 v36,a166\nM v[248:251],a[88:91],a[96:99],v[248:251]\nbuffer_load_dwordx4 v21,s[32:35],s47 offen offset:128 lds\nM v[252:255],a[64:67],a[124:127],v[252:255]\ns_add_u32 m0,s50,0\n" \
"ds_read2_b32 a[64:65],v46 offset0:48 offset1:49\nv_accvgpr_read_b32 v37,a167\nM v[252:255],a[68:71],a[120:123],v[252:255]\ns_mov_b32 s47,4194304\nds_read2_b32 a[66:67],v46 offset0:50 offset1:51\nds_read2_b32 a[68:69],v46 offset0:176 offset1:177\nM v[252:255],a[72:75],a[116:119],v[252:255]\nds_read2_b32 a[70:71],v46 offset0:178 offset1:179\n" \
"C v31,v32,v33\nM v[252:255],a[76:79],a[112:115],v[252:255]\nbuffer_load_dwordx4 v13,s[28:31],s46 offen lds\nM v[252:255],a[80:83],a[108:111],v[252:255]\ns_add_u32 m0,s50,4096\nds_read2_b32 a[72:73],v47 offset0:48 offset1:49\nv_mul_f32 v34,s20,v34\nM v[252:255],a[84:87],a[104:107],v[252:255]\n" \
"ds_read_b128 a[104:107],v50 offset:1344\nv_mul_f32 v35,s20,v35\nM v[252:255],a[88:91],a[100:103],v[252:255]\ns_waitcnt lgkmcnt(5)\nds_read_b128 a[100:103],v50 offset:832\nv_mul_f32 v36,s20,v36\nM v[252:255],a[92:95],a[96:99],v[252:255]\nbuffer_load_dwordx4 v14,s[28:31],s46 offen lds\n" \
"M v[224:227],a[0:3],a[32:35],v[224:227]\ns_add_u32 m0,s50,8192\nv_mul_f32 v37,s20,v37\nv_accvgpr_read_b32 v38,a168\nM v[228:231],a[0:3],a[36:39],v[228:231]\nds_read_b128 a[96:99],v50 offset:320\nv_accvgpr_read_b32 v39,a169\nM v[228:231],a[4:7],a[32:35],v[228:231]\n" \
"ds_read2_b32 a[74:75],v47 offset0:50 offset1:51\nds_read_b128 a[108:111],v50 offset:1856\nM v[232:235],a[0:3],a[40:43],v[232:235]\nLG10_%=:\ns_cbranch_execz LHA155_%=\nds_read2_b32 a[76:77],v47 offset0:176 offset1:177\nC v32,v34,v35\nM v[232:235],a[4:7],a[36:39],v[232:235]\n" \
"buffer_store_dwordx4 v[208:211],v4,s[36:39],s96 offen offset:0 sc1\nM v[232:235],a[8:11],a[32:35],v[232:235]\nds_read2_b32 a[78:79],v47 offset0:178 offset1:179\nds_read_b128 a[112:115],v50 offset:2368\nM v[236:239],a[0:3],a[44:47],v[236:239]\nds_read2_b32 a[80:81],v48 offset0:48 offset1:49\nC v33,v36,v37\nM v[236:239],a[4:7],a[40:43],v[236:239]\n" \
"ds_read2_b32 a[82:83],v48 offset0:50 offset1:51\nv_accvgpr_read_b32 v40,a170\nM v[236:239],a[8:11],a[36:39],v[236:239]\nbuffer_load_dwordx4 v15,s[28:31],s46 offen lds\nM v[236:239],a[12:15],a[32:35],v[236:239]\ns_add_u32 m0,s50,12288\nds_read_b128 a[116:119],v50 offset:2880\nv_accvgpr_read_b32 v41,a171\n" \
"M v[240:243],a[0:3],a[48:51],v[240:243]\nds_read2_b32 a[84:85],v48 offset0:176 offset1:177\nv_mul_f32 v38,s20,v38\nM v[240:243],a[4:7],a[44:47],v[240:243]\nds_read2_b32 a[86:87],v48 offset0:178 offset1:179\nv_mul_f32 v39,s20,v39\nM v[240:243],a[8:11],a[40:43],v[240:243]\nds_read_b128 a[120:123],v50 offset:3392\n" \
"v_accvgpr_write_b32 a144,v30\nM v[240:243],a[12:15],a[36:39],v[240:243]\nbuffer_store_dwordx4 v[212:215],v4,s[36:39],s96 offen offset:1024 sc1\nM v[240:243],a[16:19],a[32:35],v[240:243]\nds_read2_b32 a[88:89],v49 offset0:48 offset1:49\nds_read2_b32 a[90:91],v49 offset0:50 offset1:51\nM v[244:247],a[0:3],a[52:55],v[244:247]\nds_read_b128 a[124:127],v50 offset:3904\n" \
"v_accvgpr_write_b32 a145,v31\nM v[244:247],a[4:7],a[48:51],v[244:247]\nds_read2_b32 a[92:93],v49 offset0:176 offset1:177\nv_accvgpr_write_b32 a146,v32\nM v[244:247],a[8:11],a[44:47],v[244:247]\nbuffer_load_dwordx4 v16,s[28:31],s46 offen lds\nM v[244:247],a[12:15],a[40:43],v[244:247]\ns_add_u32 m0,s50,16384\n" \
"ds_read2_b32 a[94:95],v49 offset0:178 offset1:179\nv_accvgpr_write_b32 a147,v33\nM v[244:247],a[16:19],a[36:39],v[244:247]\ns_mov_b32 s46,896\nv_mul_f32 v40,s20,v40\nv_mul_f32 v41,s20,v41\nM v[244:247],a[20:23],a[32:35],v[244:247]\nC v38,v38,v39\n" \
"v_accvgpr_read_b32 v42,a172\nM v[248:251],a[0:3],a[56:59],v[248:251]\nv_accvgpr_read_b32 v43,a173\nv_accvgpr_read_b32 v44,a174\nM v[248:251],a[4:7],a[52:55],v[248:251]\nbuffer_store_dwordx4 v[216:219],v4,s[36:39],s96 offen offset:2048 sc1\nM v[248:251],a[8:11],a[48:51],v[248:251]\nC v39,v40,v41\n" \
"v_accvgpr_read_b32 v45,a175\nM v[248:251],a[12:15],a[44:47],v[248:251]\nv_mul_f32 v42,s20,v42\nv_mul_f32 v43,s20,v43\nM v[248:251],a[16:19],a[40:43],v[248:251]\nv_mul_f32 v44,s20,v44\nv_mul_f32 v45,s20,v45\nM v[248:251],a[20:23],a[36:39],v[248:251]\n" \
"buffer_load_dwordx4 v17,s[28:31],s46 offen lds\nM v[248:251],a[24:27],a[32:35],v[248:251]\ns_add_u32 m0,s50,20480\nC v40,v42,v43\nC v41,v44,v45\nM v[252:255],a[0:3],a[60:63],v[252:255]\nds_read2_b32 a[0:1],v46 offset0:64 offset1:65\nds_read2_b32 a[2:3],v46 offset0:66 offset1:67\n" \
"M v[252:255],a[4:7],a[56:59],v[252:255]\nds_read2_b32 a[4:5],v46 offset0:192 offset1:193\nv_accvgpr_write_b32 a148,v38\nM v[252:255],a[8:11],a[52:55],v[252:255]\nds_read2_b32 a[6:7],v46 offset0:194 offset1:195\nds_read2_b32 a[8:9],v47 offset0:64 offset1:65\nM v[252:255],a[12:15],a[48:51],v[252:255]\nbuffer_store_dwordx4 v[220:223],v4,s[36:39],s96 offen offset:3072 sc1\n" \
"M v[252:255],a[16:19],a[44:47],v[252:255]\ns_add_u32 s96,s57,16384\nds_read2_b32 a[10:11],v47 offset0:66 offset1:67\nds_read_b128 a[44:47],v50 offset:1792\nM v[252:255],a[20:23],a[40:43],v[252:255]\nds_read_b128 a[40:43],v50 offset:1280\nv_accvgpr_write_b32 a149,v39\nM v[252:255],a[24:27],a[36:39],v[252:255]\n" \
"s_waitcnt lgkmcnt(6)\nds_read_b128 a[36:39],v50 offset:768\nv_accvgpr_write_b32 a150,v40\nM v[252:255],a[28:31],a[32:35],v[252:255]\nbuffer_load_dwordx4 v18,s[28:31],s46 offen lds\nM v[224:227],a[64:67],a[96:99],v[224:227]\ns_add_u32 m0,s50,24576\nv_accvgpr_write_b32 a151,v41\n" \
"v_accvgpr_read_b32 v30,a176\nM v[228:231],a[64:67],a[100:103],v[228:231]\nds_read_b128 a[32:35],v50 offset:256\nv_accvgpr_read_b32 v31,a177\nM v[228:231],a[68:71],a[96:99],v[228:231]\nds_read2_b32 a[12:13],v47 offset0:192 offset1:193\nds_read2_b32 a[14:15],v47 offset0:194 offset1:195\nM v[232:235],a[64:67],a[104:107],v[232:235]\n" \
"ds_read_b128 a[48:51],v50 offset:2304\nv_accvgpr_read_b32 v32,a178\nM v[232:235],a[68:71],a[100:103],v[232:235]\nbuffer_store_dwordx4 a[128:131],v4,s[36:39],s96 offen offset:0 sc1\nM v[232:235],a[72:75],a[96:99],v[232:235]\nds_read2_b32 a[16:17],v48 offset0:64 offset1:65\nds_read2_b32 a[18:19],v48 offset0:66 offset1:67\nM v[236:239],a[64:67],a[108:111],v[236:239]\n" \
"ds_read_b128 a[52:55],v50 offset:2816\nv_accvgpr_read_b32 v33,a179\nM v[236:239],a[68:71],a[104:107],v[236:239]\nds_read2_b32 a[20:21],v48 offset0:192 offset1:193\nv_mul_f32 v30,s20,v30\nM v[236:239],a[72:75],a[100:103],v[236:239]\nds_read2_b32 a[22:23],v48 offset0:194 offset1:195\nds_read_b128 a[56:59],v50 offset:3328\n" \
"M v[236:239],a[76:79],a[96:99],v[236:239]\nds_read2_b32 a[24:25],v49 offset0:64 offset1:65\nv_mul_f32 v31,s20,v31\nM v[240:243],a[64:67],a[112:115],v[240:243]\nds_read2_b32 a[26:27],v49 offset0:66 offset1:67\nv_mul_f32 v32,s20,v32\nM v[240:243],a[68:71],a[108:111],v[240:243]\nds_read_b128 a[60:63],v50 offset:3840\n" \
"v_mul_f32 v33,s20,v33\nM v[240:243],a[72:75],a[104:107],v[240:243]\nds_read2_b32 a[28:29],v49 offset0:192 offset1:193\nC v30,v30,v31\nM v[240:243],a[76:79],a[100:103],v[240:243]\nds_read2_b32 a[30:31],v49 offset0:194 offset1:195\nv_accvgpr_read_b32 v34,a180\nM v[240:243],a[80:83],a[96:99],v[240:243]\n" \
"v_accvgpr_read_b32 v35,a181\nv_accvgpr_read_b32 v36,a182\nM v[244:247],a[64:67],a[116:119],v[244:247]\nC v31,v32,v33\nv_accvgpr_read_b32 v37,a183\nM v[244:247],a[68:71],a[112:115],v[244:247]\nv_mul_f32 v34,s20,v34\nv_mul_f32 v35,s20,v35\n" \
"M v[244:247],a[72:75],a[108:111],v[244:247]\nbuffer_store_dwordx4 a[132:135],v4,s[36:39],s96 offen offset:1024 sc1\nM v[244:247],a[76:79],a[104:107],v[244:247]\nv_mul_f32 v36,s20,v36\nv_mul_f32 v37,s20,v37\nM v[244:247],a[80:83],a[100:103],v[244:247]\nC v32,v34,v35\nC v33,v36,v37\n" \
"M v[244:247],a[84:87],a[96:99],v[244:247]\nv_accvgpr_write_b32 a152,v30\nv_accvgpr_write_b32 a153,v31\nM v[248:251],a[64:67],a[120:123],v[248:251]\nv_accvgpr_write_b32 a154,v32\nv_accvgpr_write_b32 a155,v33\nM v[248:251],a[68:71],a[116:119],v[248:251]\nv_accvgpr_read_b32 v38,a184\n" \
"v_accvgpr_read_b32 v39,a185\nM v[248:251],a[72:75],a[112:115],v[248:251]\nv_accvgpr_read_b32 v40,a186\nv_accvgpr_read_b32 v41,a187\nM v[248:251],a[76:79],a[108:111],v[248:251]\nv_mul_f32 v38,s20,v38\nv_mul_f32 v39,s20,v39\nM v[248:251],a[80:83],a[104:107],v[248:251]\n" \
"v_mul_f32 v40,s20,v40\nv_mul_f32 v41,s20,v41\nM v[248:251],a[84:87],a[100:103],v[248:251]\nC v38,v38,v39\nv_accvgpr_read_b32 v42,a188\nM v[248:251],a[88:91],a[96:99],v[248:251]\nv_accvgpr_read_b32 v43,a189\nv_accvgpr_read_b32 v44,a190\n" \
"M v[252:255],a[64:67],a[124:127],v[252:255]\nds_read2_b32 a[64:65],v46 offset0:80 offset1:81\nds_read2_b32 a[66:67],v46 offset0:82 offset1:83\nM v[252:255],a[68:71],a[120:123],v[252:255]\nds_read2_b32 a[68:69],v46 offset0:208 offset1:209\nC v39,v40,v41\nM v[252:255],a[72:75],a[116:119],v[252:255]\nbuffer_store_dwordx4 a[136:139],v4,s[36:39],s96 offen offset:2048 sc1\n" \
"M v[252:255],a[76:79],a[112:115],v[252:255]\nds_read2_b32 a[70:71],v46 offset0:210 offset1:211\nds_read2_b32 a[72:73],v47 offset0:80 offset1:81\nM v[252:255],a[80:83],a[108:111],v[252:255]\nds_read2_b32 a[74:75],v47 offset0:82 offset1:83\nv_accvgpr_read_b32 v45,a191\nM v[252:255],a[84:87],a[104:107],v[252:255]\nds_read_b128 a[104:107],v50 offset:1216\n" \
"v_mul_f32 v42,s20,v42\nM v[252:255],a[88:91],a[100:103],v[252:255]\nds_read_b128 a[100:103],v50 offset:704\nv_mul_f32 v43,s20,v43\nM v[252:255],a[92:95],a[96:99],v[252:255]\ns_waitcnt lgkmcnt(5)\nds_read_b128 a[96:99],v50 offset:192\nv_mul_f32 v44,s20,v44\n" \
"M v[224:227],a[0:3],a[32:35],v[224:227]\nds_read_b128 a[108:111],v50 offset:1728\nv_mul_f32 v45,s20,v45\nM v[228:231],a[0:3],a[36:39],v[228:231]\nds_read2_b32 a[76:77],v47 offset0:208 offset1:209\nds_read2_b32 a[78:79],v47 offset0:210 offset1:211\nM v[228:231],a[4:7],a[32:35],v[228:231]\nds_read_b128 a[112:115],v50 offset:2240\n" \
"C v40,v42,v43\nM v[232:235],a[0:3],a[40:43],v[232:235]\nds_read2_b32 a[80:81],v48 offset0:80 offset1:81\nC v41,v44,v45\nM v[232:235],a[4:7],a[36:39],v[232:235]\nds_read2_b32 a[82:83],v48 offset0:82 offset1:83\nv_accvgpr_write_b32 a156,v38\nM v[232:235],a[8:11],a[32:35],v[232:235]\n" \
"ds_read_b128 a[116:119],v50 offset:2752\nv_accvgpr_write_b32 a157,v39\nM v[236:239],a[0:3],a[44:47],v[236:239]\nds_read2_b32 a[84:85],v48 offset0:208 offset1:209\nds_read2_b32 a[86:87],v48 offset0:210 offset1:211\nM v[236:239],a[4:7],a[40:43],v[236:239]\nbuffer_store_dwordx4 a[140:143],v4,s[36:39],s96 offen offset:3072 sc1\nM v[236:239],a[8:11],a[36:39],v[236:239]\n" \
"s_add_u32 s96,s57,20480\nds_read_b128 a[120:123],v50 offset:3264\nds_read2_b32 a[88:89],v49 offset0:80 offset1:81\nM v[236:239],a[12:15],a[32:35],v[236:239]\nds_read2_b32 a[90:91],v49 offset0:82 offset1:83\nv_accvgpr_write_b32 a158,v40\nM v[240:243],a[0:3],a[48:51],v[240:243]\nds_read_b128 a[124:127],v50 offset:3776\n" \
"v_accvgpr_write_b32 a159,v41\nM v[240:243],a[4:7],a[44:47],v[240:243]\nbuffer_load_dwordx4 v19,s[28:31],s46 offen lds\nM v[240:243],a[8:11],a[40:43],v[240:243]\ns_add_u32 m0,s50,28672\nds_read2_b32 a[92:93],v49 offset0:208 offset1:209\nM v[240:243],a[12:15],a[36:39],v[240:243]\nds_read2_b32 a[94:95],v49 offset0:210 offset1:211\n" \
"M v[240:243],a[16:19],a[32:35],v[240:243]\nM v[244:247],a[0:3],a[52:55],v[244:247]\nM v[244:247],a[4:7],a[48:51],v[244:247]\nbuffer_store_dwordx4 a[144:147],v4,s[36:39],s96 offen offset:0 sc1\nM v[244:247],a[8:11],a[44:47],v[244:247]\nM v[244:247],a[12:15],a[40:43],v[244:247]\nM v[244:247],a[16:19],a[36:39],v[244:247]\nM v[244:247],a[20:23],a[32:35],v[244:247]\n" \
"M v[248:251],a[0:3],a[56:59],v[248:251]\nM v[248:251],a[4:7],a[52:55],v[248:251]\nM v[248:251],a[8:11],a[48:51],v[248:251]\nM v[248:251],a[12:15],a[44:47],v[248:251]\nM v[248:251],a[16:19],a[40:43],v[248:251]\nM v[248:251],a[20:23],a[36:39],v[248:251]\nM v[248:251],a[24:27],a[32:35],v[248:251]\nM v[252:255],a[0:3],a[60:63],v[252:255]\n" \
"ds_read2_b32 a[0:1],v46 offset0:96 offset1:97\nds_read2_b32 a[2:3],v46 offset0:98 offset1:99\nM v[252:255],a[4:7],a[56:59],v[252:255]\nbuffer_store_dwordx4 a[148:151],v4,s[36:39],s96 offen offset:1024 sc1\nM v[252:255],a[8:11],a[52:55],v[252:255]\nds_read2_b32 a[4:5],v46 offset0:224 offset1:225\nds_read2_b32 a[6:7],v46 offset0:226 offset1:227\nM v[252:255],a[12:15],a[48:51],v[252:255]\n" \
"ds_read2_b32 a[8:9],v47 offset0:96 offset1:97\nM v[252:255],a[16:19],a[44:47],v[252:255]\nds_read_b128 a[44:47],v50 offset:1664\nM v[252:255],a[20:23],a[40:43],v[252:255]\nds_read_b128 a[40:43],v50 offset:1152\nM v[252:255],a[24:27],a[36:39],v[252:255]\nds_read_b128 a[36:39],v50 offset:640\nM v[252:255],a[28:31],a[32:35],v[252:255]\n" \
"s_waitcnt lgkmcnt(6)\nds_read_b128 a[32:35],v50 offset:128\nM v[224:227],a[64:67],a[96:99],v[224:227]\nds_read2_b32 a[10:11],v47 offset0:98 offset1:99\nds_read2_b32 a[12:13],v47 offset0:224 offset1:225\nM v[228:231],a[64:67],a[100:103],v[228:231]\nds_read2_b32 a[14:15],v47 offset0:226 offset1:227\nM v[228:231],a[68:71],a[96:99],v[228:231]\n" \
"ds_read_b128 a[48:51],v50 offset:2176\nM v[232:235],a[64:67],a[104:107],v[232:235]\nds_read2_b32 a[16:17],v48 offset0:96 offset1:97\nM v[232:235],a[68:71],a[100:103],v[232:235]\nds_read2_b32 a[18:19],v48 offset0:98 offset1:99\nds_read_b128 a[52:55],v50 offset:2688\nM v[232:235],a[72:75],a[96:99],v[232:235]\nds_read2_b32 a[20:21],v48 offset0:224 offset1:225\n" \
"M v[236:239],a[64:67],a[108:111],v[236:239]\nbuffer_store_dwordx4 a[152:155],v4,s[36:39],s96 offen offset:2048 sc1\nM v[236:239],a[68:71],a[104:107],v[236:239]\nds_read2_b32 a[22:23],v48 offset0:226 offset1:227\nds_read_b128 a[56:59],v50 offset:3200\nM v[236:239],a[72:75],a[100:103],v[236:239]\nds_read2_b32 a[24:25],v49 offset0:96 offset1:97\nM v[236:239],a[76:79],a[96:99],v[236:239]\n" \
"ds_read2_b32 a[26:27],v49 offset0:98 offset1:99\nM v[240:243],a[64:67],a[112:115],v[240:243]\nds_read_b128 a[60:63],v50 offset:3712\nM v[240:243],a[68:71],a[108:111],v[240:243]\nds_read2_b32 a[28:29],v49 offset0:224 offset1:225\nds_read2_b32 a[30:31],v49 offset0:226 offset1:227\nM v[240:243],a[72:75],a[104:107],v[240:243]\nM v[240:243],a[76:79],a[100:103],v[240:243]\n" \
"M v[240:243],a[80:83],a[96:99],v[240:243]\nM v[244:247],a[64:67],a[116:119],v[244:247]\nM v[244:247],a[68:71],a[112:115],v[244:247]\nM v[244:247],a[72:75],a[108:111],v[244:247]\nM v[244:247],a[76:79],a[104:107],v[244:247]\nM v[244:247],a[80:83],a[100:103],v[244:247]\nbuffer_store_dwordx4 a[156:159],v4,s[36:39],s96 offen offset:3072 sc1\nM v[244:247],a[84:87],a[96:99],v[244:247]\n" \
"s_add_u32 s96,s57,49152\nM v[248:251],a[64:67],a[120:123],v[248:251]\nM v[248:251],a[68:71],a[116:119],v[248:251]\nM v[248:251],a[72:75],a[112:115],v[248:251]\nM v[248:251],a[76:79],a[108:111],v[248:251]\nM v[248:251],a[80:83],a[104:107],v[248:251]\nM v[248:251],a[84:87],a[100:103],v[248:251]\nM v[248:251],a[88:91],a[96:99],v[248:251]\n" \
"M v[252:255],a[64:67],a[124:127],v[252:255]\nds_read2_b32 a[64:65],v46 offset0:112 offset1:113\nds_read2_b32 a[66:67],v46 offset0:114 offset1:115\nM v[252:255],a[68:71],a[120:123],v[252:255]\nds_read2_b32 a[68:69],v46 offset0:240 offset1:241\nM v[252:255],a[72:75],a[116:119],v[252:255]\nbuffer_load_dwordx4 v20,s[28:31],s46 offen lds\nM v[252:255],a[76:79],a[112:115],v[252:255]\n" \
"s_add_u32 m0,s51,32768\nds_read2_b32 a[70:71],v46 offset0:242 offset1:243\nM v[252:255],a[80:83],a[108:111],v[252:255]\ns_mov_b32 s46,1024\nds_read2_b32 a[72:73],v47 offset0:112 offset1:113\nM v[252:255],a[84:87],a[104:107],v[252:255]\nds_read_b128 a[104:107],v50 offset:1088\nM v[252:255],a[88:91],a[100:103],v[252:255]\n" \
"s_waitcnt lgkmcnt(4)\nds_read_b128 a[100:103],v50 offset:576\nM v[252:255],a[92:95],a[96:99],v[252:255]\nbuffer_store_dwordx4 v[88:91],v4,s[36:39],s96 offen offset:0 sc1\nM v[224:227],a[0:3],a[32:35],v[224:227]\nds_read_b128 a[96:99],v50 offset:64\nds_read2_b32 a[74:75],v47 offset0:114 offset1:115\nM v[228:231],a[0:3],a[36:39],v[228:231]\n" \
"ds_read_b128 a[108:111],v50 offset:1600\nM v[228:231],a[4:7],a[32:35],v[228:231]\nds_read2_b32 a[76:77],v47 offset0:240 offset1:241\nM v[232:235],a[0:3],a[40:43],v[232:235]\nds_read2_b32 a[78:79],v47 offset0:242 offset1:243\nM v[232:235],a[4:7],a[36:39],v[232:235]\nds_read_b128 a[112:115],v50 offset:2112\nM v[232:235],a[8:11],a[32:35],v[232:235]\n" \
"ds_read2_b32 a[80:81],v48 offset0:112 offset1:113\nds_read2_b32 a[82:83],v48 offset0:114 offset1:115\nM v[236:239],a[0:3],a[44:47],v[236:239]\nds_read_b128 a[116:119],v50 offset:2624\nM v[236:239],a[4:7],a[40:43],v[236:239]\nds_read2_b32 a[84:85],v48 offset0:240 offset1:241\nM v[236:239],a[8:11],a[36:39],v[236:239]\nds_read2_b32 a[86:87],v48 offset0:242 offset1:243\n" \
"M v[236:239],a[12:15],a[32:35],v[236:239]\nds_read_b128 a[120:123],v50 offset:3136\nM v[240:243],a[0:3],a[48:51],v[240:243]\nds_read2_b32 a[88:89],v49 offset0:112 offset1:113\nds_read2_b32 a[90:91],v49 offset0:114 offset1:115\nM v[240:243],a[4:7],a[44:47],v[240:243]\nds_read_b128 a[124:127],v50 offset:3648\nM v[240:243],a[8:11],a[40:43],v[240:243]\n" \
"ds_read2_b32 a[92:93],v49 offset0:240 offset1:241\nM v[240:243],a[12:15],a[36:39],v[240:243]\nds_read2_b32 a[94:95],v49 offset0:242 offset1:243\nM v[240:243],a[16:19],a[32:35],v[240:243]\nM v[244:247],a[0:3],a[52:55],v[244:247]\nM v[244:247],a[4:7],a[48:51],v[244:247]\nM v[244:247],a[8:11],a[44:47],v[244:247]\nM v[244:247],a[12:15],a[40:43],v[244:247]\n" \
"M v[244:247],a[16:19],a[36:39],v[244:247]\nM v[244:247],a[20:23],a[32:35],v[244:247]\nM v[248:251],a[0:3],a[56:59],v[248:251]\nM v[248:251],a[4:7],a[52:55],v[248:251]\nM v[248:251],a[8:11],a[48:51],v[248:251]\nM v[248:251],a[12:15],a[44:47],v[248:251]\nM v[248:251],a[16:19],a[40:43],v[248:251]\nM v[248:251],a[20:23],a[36:39],v[248:251]\n" \
"M v[248:251],a[24:27],a[32:35],v[248:251]\nM v[252:255],a[0:3],a[60:63],v[252:255]\nM v[252:255],a[4:7],a[56:59],v[252:255]\nM v[252:255],a[8:11],a[52:55],v[252:255]\nM v[252:255],a[12:15],a[48:51],v[252:255]\nM v[252:255],a[16:19],a[44:47],v[252:255]\nM v[252:255],a[20:23],a[40:43],v[252:255]\nM v[252:255],a[24:27],a[36:39],v[252:255]\n" \
"M v[252:255],a[28:31],a[32:35],v[252:255]\ns_waitcnt lgkmcnt(0)\nM v[224:227],a[64:67],a[96:99],v[224:227]\ns_waitcnt vmcnt(26) lgkmcnt(0)\nM v[228:231],a[64:67],a[100:103],v[228:231]\ns_barrier\nds_read_b64_tr_b16 a[0:1],v9 offset:0\nM v[228:231],a[68:71],a[96:99],v[228:231]\n" \
"buffer_load_dwordx4 v21,s[32:35],s47 offen offset:0 lds\nM v[232:235],a[64:67],a[104:107],v[232:235]\ns_add_u32 m0,s51,33664\nC v224,v224,v225\nC v225,v226,v227\nM v[232:235],a[68:71],a[100:103],v[232:235]\nC v226,v228,v229\nC v227,v230,v231\n" \
"M v[232:235],a[72:75],a[96:99],v[232:235]\nv_permlane16_swap_b32 v224,v226\nM v[236:239],a[64:67],a[108:111],v[236:239]\nbuffer_load_dwordx4 v21,s[32:35],s47 offen offset:128 lds\nM v[236:239],a[68:71],a[104:107],v[236:239]\ns_mov_b32 s47,4456448\nds_read_b64_tr_b16 a[2:3],v10 offset:0\nC v232,v232,v233\n" \
"M v[236:239],a[72:75],a[100:103],v[236:239]\ns_add_u32 m0,s51,40960\nv_permlane16_swap_b32 v225,v227\nM v[236:239],a[76:79],a[96:99],v[236:239]\nds_read_b128 a[32:35],v7 offset:0\nds_read_b128 a[36:39],v7 offset:4096\nM v[240:243],a[64:67],a[112:115],v[240:243]\nbuffer_load_dwordx4 v21,s[32:35],s47 offen offset:0 lds\n" \
"M v[240:243],a[68:71],a[108:111],v[240:243]\ns_add_u32 m0,s51,41856\nC v233,v234,v235\nC v234,v236,v237\nM v[240:243],a[72:75],a[104:107],v[240:243]\nds_read_b128 a[40:43],v7 offset:8192\nC v235,v238,v239\nM v[240:243],a[76:79],a[100:103],v[240:243]\n" \
"ds_read_b64_tr_b16 a[4:5],v11 offset:0\nM v[240:243],a[80:83],a[96:99],v[240:243]\nbuffer_load_dwordx4 v21,s[32:35],s47 offen offset:128 lds\nM v[244:247],a[64:67],a[116:119],v[244:247]\ns_add_u32 m0,s50,32768\nv_permlane16_swap_b32 v232,v234\nM v[244:247],a[68:71],a[112:115],v[244:247]\ns_mov_b32 s47,4718592\n" \
"v_permlane16_swap_b32 v233,v235\nM v[244:247],a[72:75],a[108:111],v[244:247]\nds_read_b128 a[44:47],v7 offset:12288\nds_read_b64_tr_b16 a[6:7],v12 offset:0\nM v[244:247],a[76:79],a[104:107],v[244:247]\nbuffer_load_dwordx4 v13,s[28:31],s46 offen lds\nM v[244:247],a[80:83],a[100:103],v[244:247]\ns_add_u32 m0,s50,36864\n" \
"C v240,v240,v241\nC v241,v242,v243\nM v[244:247],a[84:87],a[96:99],v[244:247]\nds_read_b64_tr_b16 a[8:9],v9 offset:512\nds_read_b64_tr_b16 a[10:11],v10 offset:512\nM v[248:251],a[64:67],a[120:123],v[248:251]\nds_read_b64_tr_b16 a[12:13],v11 offset:512\nM v[248:251],a[68:71],a[116:119],v[248:251]\n" \
"buffer_load_dwordx4 v14,s[28:31],s46 offen lds\nM v[248:251],a[72:75],a[112:115],v[248:251]\ns_add_u32 m0,s50,40960\nds_read_b64_tr_b16 a[14:15],v12 offset:512\nC v242,v244,v245\nM v[248:251],a[76:79],a[108:111],v[248:251]\nds_read_b64_tr_b16 a[16:17],v9 offset:1024\nC v243,v246,v247\n" \
"M v[248:251],a[80:83],a[104:107],v[248:251]\nds_read_b64_tr_b16 a[18:19],v10 offset:1024\nM v[248:251],a[84:87],a[100:103],v[248:251]\nbuffer_load_dwordx4 v15,s[28:31],s46 offen lds\nM v[248:251],a[88:91],a[96:99],v[248:251]\ns_add_u32 m0,s50,45056\nds_read_b64_tr_b16 a[20:21],v11 offset:1024\nM v[252:255],a[64:67],a[124:127],v[252:255]\n" \
"ds_read_b64_tr_b16 a[22:23],v12 offset:1024\nM v[252:255],a[68:71],a[120:123],v[252:255]\nds_read_b64_tr_b16 a[24:25],v9 offset:1536\nds_read_b64_tr_b16 a[26:27],v10 offset:1536\nM v[252:255],a[72:75],a[116:119],v[252:255]\nbuffer_load_dwordx4 v16,s[28:31],s46 offen lds\nM v[252:255],a[76:79],a[112:115],v[252:255]\ns_mov_b32 s46,1024\n" \
"v_permlane16_swap_b32 v240,v242\nM v[252:255],a[80:83],a[108:111],v[252:255]\ns_add_u32 m0,s50,49152\nds_read_b64_tr_b16 a[28:29],v11 offset:1536\nds_read_b64_tr_b16 a[30:31],v12 offset:1536\nM v[252:255],a[84:87],a[104:107],v[252:255]\ns_waitcnt lgkmcnt(5)\nds_read_b64_tr_b16 a[48:49],v9 offset:8192\n" \
"C v248,v248,v249\nM v[252:255],a[88:91],a[100:103],v[252:255]\nbuffer_load_dwordx4 v17,s[28:31],s46 offen lds\nM v[252:255],a[92:95],a[96:99],v[252:255]\ns_add_u32 m0,s50,53248\nds_read_b64_tr_b16 a[50:51],v10 offset:8192\nC v249,v250,v251\nM v[128:131],a[0:3],a[32:35],v[128:131]\n" \
"s_load_dwordx2 s[84:85],%0,0x50\nds_read_b128 a[80:83],v8 offset:0\nM v[160:163],a[0:3],a[36:39],v[160:163]\ns_load_dwordx2 s[88:89],%0,0x60\nds_read_b128 a[84:87],v8 offset:4096\nC v250,v252,v253\nM a[224:227],a[0:3],a[40:43],a[224:227]\nbuffer_load_dwordx4 v18,s[28:31],s46 offen lds\n" \
"M a[192:195],a[0:3],a[44:47],a[192:195]\ns_add_u32 m0,s50,57344\nv_permlane16_swap_b32 v241,v243\nM a[196:199],a[4:7],a[44:47],a[196:199]\ns_load_dwordx2 s[12:13],%0,0x0\nds_read_b64_tr_b16 a[52:53],v11 offset:8192\nds_read_b64_tr_b16 a[54:55],v12 offset:8192\nM a[228:231],a[4:7],a[40:43],a[228:231]\n" \
"s_memrealtime s[10:11]\nds_read_b64_tr_b16 a[56:57],v9 offset:8704\nC v251,v254,v255\nM v[164:167],a[4:7],a[36:39],v[164:167]\nbuffer_load_dwordx4 v19,s[28:31],s46 offen lds\nM v[132:135],a[4:7],a[32:35],v[132:135]\ns_add_u32 m0,s50,61440\nds_read_b64_tr_b16 a[58:59],v10 offset:8704\n" \
"M v[136:139],a[8:11],a[32:35],v[136:139]\nds_read_b64_tr_b16 a[60:61],v11 offset:8704\nM v[168:171],a[8:11],a[36:39],v[168:171]\nds_read_b64_tr_b16 a[62:63],v12 offset:8704\nM a[232:235],a[8:11],a[40:43],a[232:235]\nbuffer_load_dwordx4 v20,s[28:31],s46 offen lds\nM a[200:203],a[8:11],a[44:47],a[200:203]\nLG11_%=:\n" \
"s_cbranch_execz LHA155_%=\ns_add_u32 m0,s51,49152\nds_read_b64_tr_b16 a[64:65],v9 offset:9216\nM a[204:207],a[12:15],a[44:47],a[204:207]\ns_mov_b32 s46,1152\nds_read_b64_tr_b16 a[66:67],v10 offset:9216\nM a[236:239],a[12:15],a[40:43],a[236:239]\nds_read_b64_tr_b16 a[68:69],v11 offset:9216\n" \
"ds_read_b64_tr_b16 a[70:71],v12 offset:9216\nM v[172:175],a[12:15],a[36:39],v[172:175]\nbuffer_load_dwordx4 v21,s[32:35],s47 offen offset:0 lds\nM v[140:143],a[12:15],a[32:35],v[140:143]\ns_add_u32 m0,s51,50048\nv_permlane16_swap_b32 v248,v250\nM v[144:147],a[16:19],a[32:35],v[144:147]\nds_read_b64_tr_b16 a[72:73],v9 offset:9728\n" \
"ds_read_b64_tr_b16 a[74:75],v10 offset:9728\nM v[176:179],a[16:19],a[36:39],v[176:179]\nds_read_b64_tr_b16 a[76:77],v11 offset:9728\nv_mov_b32 v58,s10\nM a[240:243],a[16:19],a[40:43],a[240:243]\ns_waitcnt lgkmcnt(5)\nds_read_b64_tr_b16 a[78:79],v12 offset:9728\nds_read_b128 a[88:91],v8 offset:8192\n" \
"ds_read_b128 a[92:95],v8 offset:12288\nM a[208:211],a[16:19],a[44:47],a[208:211]\nbuffer_load_dwordx4 v21,s[32:35],s47 offen offset:128 lds\nM a[212:215],a[20:23],a[44:47],a[212:215]\ns_add_u32 m0,s51,57344\nv_permlane16_swap_b32 v249,v251\nM a[244:247],a[20:23],a[40:43],a[244:247]\ns_mov_b32 s47,4980736\n" \
"v_mov_b32 v59,s11\nM v[180:183],a[20:23],a[36:39],v[180:183]\nM v[148:151],a[20:23],a[32:35],v[148:151]\nbuffer_load_dwordx4 v21,s[32:35],s47 offen offset:0 lds\nM v[152:155],a[24:27],a[32:35],v[152:155]\ns_waitcnt vmcnt(16) lgkmcnt(0)\nM v[184:187],a[24:27],a[36:39],v[184:187]\ns_add_u32 m0,s51,58240\n" \
"s_barrier\nds_read_b64_tr_b16 a[0:1],v9 offset:16384\nM a[248:251],a[24:27],a[40:43],a[248:251]\ns_lshl_b32 s62,s6,20\nds_read_b64_tr_b16 a[2:3],v10 offset:16384\nds_read_b64_tr_b16 a[4:5],v11 offset:16384\nM a[216:219],a[24:27],a[44:47],a[216:219]\nbuffer_store_dwordx4 v[224:227],v60,s[36:39],s21 offen offset:0 sc1\n" \
"M a[220:223],a[28:31],a[44:47],a[220:223]\ns_add_u32 s21,s21,1048576\nds_read_b128 a[44:47],v5 offset:12288\nds_read_b64_tr_b16 a[6:7],v12 offset:16384\nM a[252:255],a[28:31],a[40:43],a[252:255]\ns_add_u32 s62,s62,16777216\nds_read_b128 a[40:43],v5 offset:8192\nv_bfe_u32 v57,v4,4,3\n" \
"M v[188:191],a[28:31],a[36:39],v[188:191]\ns_add_u32 s40,s4,s62\ns_addc_u32 s41,s5,0\nds_read_b128 a[36:39],v5 offset:4096\nv_bfe_u32 v61,v4,4,4\nM v[156:159],a[28:31],a[32:35],v[156:159]\ns_lshl_b32 s62,s7,16\nds_read_b128 a[32:35],v5 offset:0\n" \
"v_mov_b32 v24,0\nM v[128:131],a[48:51],a[80:83],v[128:131]\ns_sub_u32 s63,983040,s62\nds_read_b64_tr_b16 a[8:9],v9 offset:16896\nv_mov_b32 v25,0\nM v[160:163],a[48:51],a[84:87],v[160:163]\ns_cmp_lt_u32 %2,2\ns_cselect_b32 s44,s63,s62\n" \
"s_cselect_b32 s42,1048576,s63\nds_read_b64_tr_b16 a[10:11],v10 offset:16896\nv_mov_b32 v26,0\nM a[224:227],a[48:51],a[88:91],a[224:227]\ns_and_b32 s63,%2,1\nds_read_b64_tr_b16 a[12:13],v11 offset:16896\nds_read_b64_tr_b16 a[14:15],v12 offset:16896\nM a[192:195],a[48:51],a[92:95],a[192:195]\n" \
"s_lshl_b32 s63,s63,12\nds_read_b64_tr_b16 a[16:17],v9 offset:17408\nv_add_u32 v22,s63,v4\nM a[196:199],a[52:55],a[92:95],a[196:199]\ns_and_b32 s41,s41,0xffff\nds_read_b64_tr_b16 a[18:19],v10 offset:17408\nv_mov_b32 v27,0\nM a[228:231],a[52:55],a[88:91],a[228:231]\n" \
"s_add_u32 s61,s44,32768\nds_read_b64_tr_b16 a[20:21],v11 offset:17408\nds_read_b64_tr_b16 a[22:23],v12 offset:17408\nM v[164:167],a[52:55],a[84:87],v[164:167]\ns_lshl_b32 s62,s6,6\nds_read_b64_tr_b16 a[24:25],v9 offset:17920\nM v[132:135],a[52:55],a[80:83],v[132:135]\ns_add_u32 s62,s62,94375936\n" \
"ds_read_b64_tr_b16 a[26:27],v10 offset:17920\nv_lshl_add_u32 v57,v57,3,s62\nM v[136:139],a[56:59],a[80:83],v[136:139]\ns_lshl_b32 s62,s7,1\nds_read_b64_tr_b16 a[28:29],v11 offset:17920\nds_read_b64_tr_b16 a[30:31],v12 offset:17920\nM v[168:171],a[56:59],a[84:87],v[168:171]\ns_mov_b32 s4,s62\n" \
"ds_read_b64_tr_b16 a[48:49],v9 offset:24576\nM a[232:235],a[56:59],a[88:91],a[232:235]\ns_cmp_lt_u32 %2,2\ns_cselect_b32 s62,s62,50\ns_cselect_b32 s54,8192,0\nds_read_b64_tr_b16 a[50:51],v10 offset:24576\nM a[200:203],a[56:59],a[92:95],a[200:203]\nbuffer_load_dwordx4 v21,s[32:35],s47 offen offset:128 lds\n" \
"M a[204:207],a[60:63],a[92:95],a[204:207]\ns_add_u32 m0,s50,65536\nds_read_b64_tr_b16 a[52:53],v11 offset:24576\nM a[236:239],a[60:63],a[88:91],a[236:239]\ns_mov_b32 s47,5242880\nds_read_b64_tr_b16 a[54:55],v12 offset:24576\nM v[172:175],a[60:63],a[84:87],v[172:175]\ns_sub_u32 s55,s62,1\n" \
"ds_read_b64_tr_b16 a[56:57],v9 offset:25088\nds_read_b64_tr_b16 a[58:59],v10 offset:25088\nM v[140:143],a[60:63],a[80:83],v[140:143]\nbuffer_load_dwordx4 v13,s[28:31],s46 offen lds\nM v[144:147],a[64:67],a[80:83],v[144:147]\ns_add_u32 m0,s50,69632\nM v[176:179],a[64:67],a[84:87],v[176:179]\ns_lshl_b32 s70,s62,1\n" \
"ds_read_b64_tr_b16 a[60:61],v11 offset:25088\nds_read_b64_tr_b16 a[62:63],v12 offset:25088\nM a[240:243],a[64:67],a[88:91],a[240:243]\ns_cmp_eq_u32 s62,0\ns_cselect_b64 s[66:67],-1,0\nM a[208:211],a[64:67],a[92:95],a[208:211]\nbuffer_load_dwordx4 v14,s[28:31],s46 offen lds\nM a[212:215],a[68:71],a[92:95],a[212:215]\n" \
"s_add_u32 m0,s50,73728\nds_read_b64_tr_b16 a[64:65],v9 offset:25600\nM a[244:247],a[68:71],a[88:91],a[244:247]\ns_lshl_b32 s62,s7,2\nds_read_b64_tr_b16 a[66:67],v10 offset:25600\nM v[180:183],a[68:71],a[84:87],v[180:183]\ns_sub_u32 s63,60,s62\nM v[148:151],a[68:71],a[80:83],v[148:151]\n" \
"buffer_load_dwordx4 v15,s[28:31],s46 offen lds\nM v[152:155],a[72:75],a[80:83],v[152:155]\ns_add_u32 m0,s50,77824\nds_read_b64_tr_b16 a[68:69],v11 offset:25600\nM v[184:187],a[72:75],a[84:87],v[184:187]\ns_cmp_lt_u32 %2,2\ns_cselect_b32 s5,s62,s63\nds_read_b64_tr_b16 a[70:71],v12 offset:25600\n" \
"M a[248:251],a[72:75],a[88:91],a[248:251]\ns_and_b32 s63,%2,1\nM a[216:219],a[72:75],a[92:95],a[216:219]\nbuffer_load_dwordx4 v16,s[28:31],s46 offen lds\nM a[220:223],a[76:79],a[92:95],a[220:223]\ns_mov_b32 s46,1152\nds_read_b64_tr_b16 a[72:73],v9 offset:26112\nM a[252:255],a[76:79],a[88:91],a[252:255]\n" \
"s_add_u32 m0,s50,81920\nds_read_b64_tr_b16 a[74:75],v10 offset:26112\nds_read_b128 a[88:91],v6 offset:8192\nM v[188:191],a[76:79],a[84:87],v[188:191]\ns_add_u32 s69,s62,s63\nds_read_b128 a[84:87],v6 offset:4096\nM v[156:159],a[76:79],a[80:83],v[156:159]\ns_waitcnt lgkmcnt(4)\n" \
"buffer_load_dwordx4 v17,s[28:31],s46 offen lds\nM v[128:131],a[0:3],a[32:35],v[128:131]\ns_add_u32 m0,s50,86016\nM v[160:163],a[0:3],a[36:39],v[160:163]\ns_add_u32 s68,s69,s62\nds_read_b128 a[80:83],v6 offset:0\nM a[224:227],a[0:3],a[40:43],a[224:227]\ns_bfe_u32 s62,%1,0x20001\n" \
"ds_read_b64_tr_b16 a[76:77],v11 offset:26112\nM a[192:195],a[0:3],a[44:47],a[192:195]\nbuffer_store_dwordx4 v[232:235],v60,s[36:39],s21 offen offset:0 sc1\nM a[196:199],a[4:7],a[44:47],a[196:199]\ns_add_u32 s21,s21,1048576\nds_read_b64_tr_b16 a[78:79],v12 offset:26112\nds_read_b128 a[92:95],v6 offset:12288\nM a[228:231],a[4:7],a[40:43],a[228:231]\n" \
"s_lshr_b32 s63,%1,5\nM v[164:167],a[4:7],a[36:39],v[164:167]\ns_lshl_b32 s62,s62,21\nM v[132:135],a[4:7],a[32:35],v[132:135]\ns_lshl_b32 s63,s63,8\nM v[136:139],a[8:11],a[32:35],v[136:139]\ns_add_u32 s62,s62,s63\nM v[168:171],a[8:11],a[36:39],v[168:171]\n" \
"s_sub_u32 s86,8388608,s62\nM a[232:235],a[8:11],a[40:43],a[232:235]\ns_add_u32 s84,s84,s62\ns_addc_u32 s85,s85,0\nM a[200:203],a[8:11],a[44:47],a[200:203]\ns_and_b32 s63,%2,1\nM a[204:207],a[12:15],a[44:47],a[204:207]\ns_lshl_b32 s62,s63,4\n" \
"M a[236:239],a[12:15],a[40:43],a[236:239]\ns_and_b32 s63,%2,1\nM v[172:175],a[12:15],a[36:39],v[172:175]\ns_lshl_b32 s63,s63,14\nM v[140:143],a[12:15],a[32:35],v[140:143]\ns_add_u32 s63,s63,0x20000\nM v[144:147],a[16:19],a[32:35],v[144:147]\ns_cmp_lt_u32 %2,2\n" \
"s_cselect_b32 s65,0,-4096\nM v[176:179],a[16:19],a[36:39],v[176:179]\ns_cmp_lt_u32 %2,2\ns_cselect_b32 s49,1,-1\ns_cselect_b32 s45,-1,60\nM a[240:243],a[16:19],a[40:43],a[240:243]\ns_lshl_b32 s45,s45,14\nM a[208:211],a[16:19],a[44:47],a[208:211]\n" \
"buffer_load_dwordx4 v18,s[28:31],s46 offen lds\nM a[212:215],a[20:23],a[44:47],a[212:215]\ns_add_u32 m0,s50,90112\nM a[244:247],a[20:23],a[40:43],a[244:247]\ns_lshl_b32 s49,s49,14\nM v[180:183],a[20:23],a[36:39],v[180:183]\ns_add_u32 s48,s45,8192\nM v[148:151],a[20:23],a[32:35],v[148:151]\n" \
"buffer_load_dwordx4 v19,s[28:31],s46 offen lds\nM v[152:155],a[24:27],a[32:35],v[152:155]\ns_waitcnt vmcnt(13) lgkmcnt(0)\nM v[184:187],a[24:27],a[36:39],v[184:187]\ns_add_u32 m0,s50,94208\ns_barrier\nds_read_b64_tr_b16 a[0:1],v9 offset:32768\nM a[248:251],a[24:27],a[40:43],a[248:251]\n" \
"s_add_u32 s45,s45,s49\nds_read_b64_tr_b16 a[2:3],v10 offset:32768\nds_read_b64_tr_b16 a[4:5],v11 offset:32768\nM a[216:219],a[24:27],a[44:47],a[216:219]\nbuffer_load_dwordx4 v20,s[28:31],s46 offen lds\nM a[220:223],a[28:31],a[44:47],a[220:223]\ns_add_u32 m0,s51,0\nds_read_b128 a[44:47],v5 offset:45056\n" \
"M a[252:255],a[28:31],a[40:43],a[252:255]\ns_mov_b32 s46,1280\nds_read_b128 a[40:43],v5 offset:40960\nM v[188:191],a[28:31],a[36:39],v[188:191]\ns_add_u32 s48,s48,s49\nds_read_b128 a[36:39],v5 offset:36864\nM v[156:159],a[28:31],a[32:35],v[156:159]\nbuffer_load_dwordx4 v21,s[32:35],s47 offen offset:0 lds\n" \
"M v[128:131],a[48:51],a[80:83],v[128:131]\ns_add_u32 m0,s51,896\nM v[160:163],a[48:51],a[84:87],v[160:163]\ns_mov_b32 s52,-1\nds_read_b128 a[32:35],v5 offset:32768\nM a[224:227],a[48:51],a[88:91],a[224:227]\ns_min_u32 s56,s4,8\nds_read_b64_tr_b16 a[6:7],v12 offset:32768\n" \
"M a[192:195],a[48:51],a[92:95],a[192:195]\nbuffer_load_dwordx4 v21,s[32:35],s47 offen offset:128 lds\nM a[196:199],a[52:55],a[92:95],a[196:199]\ns_add_u32 m0,s51,8192\nds_read_b64_tr_b16 a[8:9],v9 offset:33280\nM a[228:231],a[52:55],a[88:91],a[228:231]\ns_mov_b32 s47,5505024\nds_read_b64_tr_b16 a[10:11],v10 offset:33280\n" \
"M v[164:167],a[52:55],a[84:87],v[164:167]\ns_mov_b32 s87,0x00020000\nds_read_b64_tr_b16 a[12:13],v11 offset:33280\nds_read_b64_tr_b16 a[14:15],v12 offset:33280\nM v[132:135],a[52:55],a[80:83],v[132:135]\nbuffer_load_dwordx4 v21,s[32:35],s47 offen offset:0 lds\nM v[136:139],a[56:59],a[80:83],v[136:139]\ns_add_u32 m0,s51,9088\n" \
"M v[168:171],a[56:59],a[84:87],v[168:171]\ns_and_b32 s85,s85,0xffff\nds_read_b64_tr_b16 a[16:17],v9 offset:33792\nds_read_b64_tr_b16 a[18:19],v10 offset:33792\nM a[232:235],a[56:59],a[88:91],a[232:235]\ns_lshl_b32 s72,%1,2\nds_read_b64_tr_b16 a[20:21],v11 offset:33792\nM a[200:203],a[56:59],a[92:95],a[200:203]\n" \
"buffer_load_dwordx4 v21,s[32:35],s47 offen offset:128 lds\nM a[204:207],a[60:63],a[92:95],a[204:207]\ns_add_u32 m0,s50,0\nds_read_b64_tr_b16 a[22:23],v12 offset:33792\nM a[236:239],a[60:63],a[88:91],a[236:239]\ns_mov_b32 s47,5767168\nds_read_b64_tr_b16 a[24:25],v9 offset:34304\nM v[172:175],a[60:63],a[84:87],v[172:175]\n" \
"s_add_u32 s72,s72,%2\nds_read_b64_tr_b16 a[26:27],v10 offset:34304\nM v[140:143],a[60:63],a[80:83],v[140:143]\nbuffer_load_dwordx4 v13,s[28:31],s46 offen lds\nM v[144:147],a[64:67],a[80:83],v[144:147]\ns_add_u32 m0,s50,4096\nds_read_b64_tr_b16 a[28:29],v11 offset:34304\nM v[176:179],a[64:67],a[84:87],v[176:179]\n" \
"s_mul_i32 s72,s72,12288\nds_read_b64_tr_b16 a[30:31],v12 offset:34304\nM a[240:243],a[64:67],a[88:91],a[240:243]\ns_add_u32 s72,s72,109060096\nds_read_b64_tr_b16 a[48:49],v9 offset:40960\nds_read_b64_tr_b16 a[50:51],v10 offset:40960\nM a[208:211],a[64:67],a[92:95],a[208:211]\nbuffer_load_dwordx4 v14,s[28:31],s46 offen lds\n" \
"M a[212:215],a[68:71],a[92:95],a[212:215]\ns_add_u32 m0,s50,8192\nM a[244:247],a[68:71],a[88:91],a[244:247]\ns_lshl_b32 s68,s68,5\nds_read_b64_tr_b16 a[52:53],v11 offset:40960\nds_read_b64_tr_b16 a[54:55],v12 offset:40960\nM v[180:183],a[68:71],a[84:87],v[180:183]\ns_add_u32 s68,s68,s6\n" \
"ds_read_b64_tr_b16 a[56:57],v9 offset:41472\nM v[148:151],a[68:71],a[80:83],v[148:151]\nbuffer_load_dwordx4 v15,s[28:31],s46 offen lds\nM v[152:155],a[72:75],a[80:83],v[152:155]\ns_add_u32 m0,s50,12288\nds_read_b64_tr_b16 a[58:59],v10 offset:41472\nM v[184:187],a[72:75],a[84:87],v[184:187]\ns_lshl_b32 s68,s68,12\n" \
"ds_read_b64_tr_b16 a[60:61],v11 offset:41472\nM a[248:251],a[72:75],a[88:91],a[248:251]\ns_add_u32 s68,s68,50331648\nds_read_b64_tr_b16 a[62:63],v12 offset:41472\nM a[216:219],a[72:75],a[92:95],a[216:219]\nbuffer_load_dwordx4 v16,s[28:31],s46 offen lds\nM a[220:223],a[76:79],a[92:95],a[220:223]\ns_mov_b32 s46,1280\n" \
"ds_read_b64_tr_b16 a[64:65],v9 offset:41984\nM a[252:255],a[76:79],a[88:91],a[252:255]\ns_add_u32 m0,s50,16384\nds_read_b64_tr_b16 a[66:67],v10 offset:41984\nM v[188:191],a[76:79],a[84:87],v[188:191]\ns_lshl_b32 s69,s69,5\nds_read_b128 a[84:87],v6 offset:36864\nM v[156:159],a[76:79],a[80:83],v[156:159]\n" \
"s_waitcnt lgkmcnt(4)\nbuffer_store_dwordx4 v[240:243],v60,s[36:39],s21 offen offset:0 sc1\nM v[128:131],a[0:3],a[32:35],v[128:131]\ns_add_u32 s21,s21,1048576\nds_read_b128 a[80:83],v6 offset:32768\nds_read_b64_tr_b16 a[68:69],v11 offset:41984\nM v[160:163],a[0:3],a[36:39],v[160:163]\ns_add_u32 s69,s69,s6\n" \
"ds_read_b64_tr_b16 a[70:71],v12 offset:41984\nM a[224:227],a[0:3],a[40:43],a[224:227]\ns_lshl_b32 s69,s69,3\nds_read_b64_tr_b16 a[72:73],v9 offset:42496\nM a[192:195],a[0:3],a[44:47],a[192:195]\ns_add_u32 s69,s69,94388224\nds_read_b64_tr_b16 a[74:75],v10 offset:42496\nds_read_b64_tr_b16 a[76:77],v11 offset:42496\n" \
"M a[196:199],a[4:7],a[44:47],a[196:199]\ns_and_b32 s89,s89,0xffff\nds_read_b64_tr_b16 a[78:79],v12 offset:42496\nM a[228:231],a[4:7],a[40:43],a[228:231]\ns_mov_b32 s90,4194304\nds_read_b128 a[88:91],v6 offset:40960\nM v[164:167],a[4:7],a[36:39],v[164:167]\ns_mov_b32 s91,0x00020000\n" \
"ds_read_b128 a[92:95],v6 offset:45056\nM v[132:135],a[4:7],a[32:35],v[132:135]\ns_lshl_b32 s71,s6,20\nM v[136:139],a[8:11],a[32:35],v[136:139]\ns_mov_b32 s75,0\nM v[168:171],a[8:11],a[36:39],v[168:171]\ns_add_u32 s71,s71,16777216\nM a[232:235],a[8:11],a[40:43],a[232:235]\n" \
"M a[200:203],a[8:11],a[44:47],a[200:203]\nM a[204:207],a[12:15],a[44:47],a[204:207]\nM a[236:239],a[12:15],a[40:43],a[236:239]\nM v[172:175],a[12:15],a[36:39],v[172:175]\nM v[140:143],a[12:15],a[32:35],v[140:143]\nbuffer_load_dwordx4 v17,s[28:31],s46 offen lds\nM v[144:147],a[16:19],a[32:35],v[144:147]\ns_add_u32 m0,s50,20480\n" \
"M v[176:179],a[16:19],a[36:39],v[176:179]\nM a[240:243],a[16:19],a[40:43],a[240:243]\nM a[208:211],a[16:19],a[44:47],a[208:211]\nbuffer_load_dwordx4 v18,s[28:31],s46 offen lds\nM a[212:215],a[20:23],a[44:47],a[212:215]\ns_add_u32 m0,s50,24576\nM a[244:247],a[20:23],a[40:43],a[244:247]\nM v[180:183],a[20:23],a[36:39],v[180:183]\n" \
"M v[148:151],a[20:23],a[32:35],v[148:151]\nbuffer_load_dwordx4 v19,s[28:31],s46 offen lds\nM v[152:155],a[24:27],a[32:35],v[152:155]\ns_waitcnt vmcnt(12) lgkmcnt(0)\nM v[184:187],a[24:27],a[36:39],v[184:187]\ns_add_u32 m0,s50,28672\ns_barrier\nds_read_b64_tr_b16 a[0:1],v9 offset:49152\n" \
"M a[248:251],a[24:27],a[40:43],a[248:251]\nds_read_b64_tr_b16 a[2:3],v10 offset:49152\nds_read_b64_tr_b16 a[4:5],v11 offset:49152\nM a[216:219],a[24:27],a[44:47],a[216:219]\nbuffer_load_dwordx4 v20,s[28:31],s46 offen lds\nM a[220:223],a[28:31],a[44:47],a[220:223]\ns_add_u32 m0,s51,16384\nds_read_b128 a[44:47],v7 offset:12288\n" \
"M a[252:255],a[28:31],a[40:43],a[252:255]\ns_mov_b32 s46,1408\nds_read_b128 a[40:43],v7 offset:8192\nM v[188:191],a[28:31],a[36:39],v[188:191]\nds_read_b128 a[36:39],v7 offset:4096\nM v[156:159],a[28:31],a[32:35],v[156:159]\nbuffer_load_dwordx4 v21,s[32:35],s47 offen offset:0 lds\nM v[128:131],a[48:51],a[80:83],v[128:131]\n" \
"s_add_u32 m0,s51,17280\nM v[160:163],a[48:51],a[84:87],v[160:163]\nds_read_b128 a[32:35],v7 offset:0\nM a[224:227],a[48:51],a[88:91],a[224:227]\nds_read_b64_tr_b16 a[6:7],v12 offset:49152\nM a[192:195],a[48:51],a[92:95],a[192:195]\nbuffer_load_dwordx4 v21,s[32:35],s47 offen offset:128 lds\nM a[196:199],a[52:55],a[92:95],a[196:199]\n" \
"s_add_u32 m0,s51,24576\nds_read_b64_tr_b16 a[8:9],v9 offset:49664\nM a[228:231],a[52:55],a[88:91],a[228:231]\ns_mov_b32 s47,6029312\nds_read_b64_tr_b16 a[10:11],v10 offset:49664\nM v[164:167],a[52:55],a[84:87],v[164:167]\nds_read_b64_tr_b16 a[12:13],v11 offset:49664\nds_read_b64_tr_b16 a[14:15],v12 offset:49664\n" \
"M v[132:135],a[52:55],a[80:83],v[132:135]\nbuffer_load_dwordx4 v21,s[32:35],s47 offen offset:0 lds\nM v[136:139],a[56:59],a[80:83],v[136:139]\ns_add_u32 m0,s51,25472\nM v[168:171],a[56:59],a[84:87],v[168:171]\nds_read_b64_tr_b16 a[16:17],v9 offset:50176\nds_read_b64_tr_b16 a[18:19],v10 offset:50176\nM a[232:235],a[56:59],a[88:91],a[232:235]\n" \
"ds_read_b64_tr_b16 a[20:21],v11 offset:50176\nM a[200:203],a[56:59],a[92:95],a[200:203]\nbuffer_store_dwordx4 v[92:95],v4,s[36:39],s96 offen offset:1024 sc1\nM a[204:207],a[60:63],a[92:95],a[204:207]\nds_read_b64_tr_b16 a[22:23],v12 offset:50176\nds_read_b64_tr_b16 a[24:25],v9 offset:50688\nM a[236:239],a[60:63],a[88:91],a[236:239]\nds_read_b64_tr_b16 a[26:27],v10 offset:50688\n" \
"M v[172:175],a[60:63],a[84:87],v[172:175]\nds_read_b64_tr_b16 a[28:29],v11 offset:50688\nds_read_b64_tr_b16 a[30:31],v12 offset:50688\nM v[140:143],a[60:63],a[80:83],v[140:143]\nbuffer_load_dwordx4 v21,s[32:35],s47 offen offset:128 lds\nM v[144:147],a[64:67],a[80:83],v[144:147]\ns_add_u32 m0,s50,32768\nM v[176:179],a[64:67],a[84:87],v[176:179]\n" \
"s_mov_b32 s47,6291456\nds_read_b64_tr_b16 a[48:49],v9 offset:57344\nds_read_b64_tr_b16 a[50:51],v10 offset:57344\nM a[240:243],a[64:67],a[88:91],a[240:243]\nds_read_b64_tr_b16 a[52:53],v11 offset:57344\nM a[208:211],a[64:67],a[92:95],a[208:211]\nbuffer_load_dwordx4 v13,s[28:31],s46 offen lds\nM a[212:215],a[68:71],a[92:95],a[212:215]\n" \
"s_add_u32 m0,s50,36864\nds_read_b64_tr_b16 a[54:55],v12 offset:57344\nM a[244:247],a[68:71],a[88:91],a[244:247]\nds_read_b64_tr_b16 a[56:57],v9 offset:57856\nM v[180:183],a[68:71],a[84:87],v[180:183]\nds_read_b64_tr_b16 a[58:59],v10 offset:57856\nM v[148:151],a[68:71],a[80:83],v[148:151]\nbuffer_load_dwordx4 v14,s[28:31],s46 offen lds\n" \
"M v[152:155],a[72:75],a[80:83],v[152:155]\ns_add_u32 m0,s50,40960\nds_read_b64_tr_b16 a[60:61],v11 offset:57856\nM v[184:187],a[72:75],a[84:87],v[184:187]\nds_read_b64_tr_b16 a[62:63],v12 offset:57856\nM a[248:251],a[72:75],a[88:91],a[248:251]\nds_read_b64_tr_b16 a[64:65],v9 offset:58368\nds_read_b64_tr_b16 a[66:67],v10 offset:58368\n" \
"M a[216:219],a[72:75],a[92:95],a[216:219]\nbuffer_load_dwordx4 v15,s[28:31],s46 offen lds\nM a[220:223],a[76:79],a[92:95],a[220:223]\ns_add_u32 m0,s50,45056\nM a[252:255],a[76:79],a[88:91],a[252:255]\nds_read_b64_tr_b16 a[68:69],v11 offset:58368\nds_read_b64_tr_b16 a[70:71],v12 offset:58368\nM v[188:191],a[76:79],a[84:87],v[188:191]\n" \
"s_waitcnt lgkmcnt(5)\nds_read_b128 a[84:87],v8 offset:4096\nM v[156:159],a[76:79],a[80:83],v[156:159]\nbuffer_load_dwordx4 v16,s[28:31],s46 offen lds\nM v[128:131],a[0:3],a[32:35],v[128:131]\ns_mov_b32 s46,1408\nM v[160:163],a[0:3],a[36:39],v[160:163]\ns_add_u32 m0,s50,49152\n" \
"ds_read_b128 a[80:83],v8 offset:0\nM a[224:227],a[0:3],a[40:43],a[224:227]\nds_read_b64_tr_b16 a[72:73],v9 offset:58880\nds_read_b64_tr_b16 a[74:75],v10 offset:58880\nM a[192:195],a[0:3],a[44:47],a[192:195]\nbuffer_load_dwordx4 v17,s[28:31],s46 offen lds\nM a[196:199],a[4:7],a[44:47],a[196:199]\ns_add_u32 m0,s50,53248\n" \
"M a[228:231],a[4:7],a[40:43],a[228:231]\nds_read_b64_tr_b16 a[76:77],v11 offset:58880\nds_read_b64_tr_b16 a[78:79],v12 offset:58880\nM v[164:167],a[4:7],a[36:39],v[164:167]\nds_read_b128 a[88:91],v8 offset:8192\nM v[132:135],a[4:7],a[32:35],v[132:135]\nbuffer_load_dwordx4 v18,s[28:31],s46 offen lds\nM v[136:139],a[8:11],a[32:35],v[136:139]\n" \
"s_add_u32 m0,s50,57344\nM v[168:171],a[8:11],a[36:39],v[168:171]\nds_read_b128 a[92:95],v8 offset:12288\nM a[232:235],a[8:11],a[40:43],a[232:235]\nM a[200:203],a[8:11],a[44:47],a[200:203]\nbuffer_load_dwordx4 v19,s[28:31],s46 offen lds\nM a[204:207],a[12:15],a[44:47],a[204:207]\ns_add_u32 m0,s50,61440\n" \
"M a[236:239],a[12:15],a[40:43],a[236:239]\nM v[172:175],a[12:15],a[36:39],v[172:175]\nM v[140:143],a[12:15],a[32:35],v[140:143]\nbuffer_load_dwordx4 v20,s[28:31],s46 offen lds\nM v[144:147],a[16:19],a[32:35],v[144:147]\ns_add_u32 m0,s51,32768\nM v[176:179],a[16:19],a[36:39],v[176:179]\ns_mov_b32 s46,1536\n" \
"M a[240:243],a[16:19],a[40:43],a[240:243]\nM a[208:211],a[16:19],a[44:47],a[208:211]\nbuffer_load_dwordx4 v21,s[32:35],s47 offen offset:0 lds\nM a[212:215],a[20:23],a[44:47],a[212:215]\ns_add_u32 m0,s51,33664\nM a[244:247],a[20:23],a[40:43],a[244:247]\nM v[180:183],a[20:23],a[36:39],v[180:183]\nM v[148:151],a[20:23],a[32:35],v[148:151]\n" \
"buffer_store_dwordx4 v[248:251],v60,s[36:39],s21 offen offset:0 sc1\nM v[152:155],a[24:27],a[32:35],v[152:155]\ns_waitcnt vmcnt(15) lgkmcnt(0)\nv_lshrrev_b32 v60,8,v4\nv_lshlrev_b32 v60,2,v60\nM v[184:187],a[24:27],a[36:39],v[184:187]\ns_barrier\nds_read_b64_tr_b16 a[0:1],v9 offset:0\n" \
"M a[248:251],a[24:27],a[40:43],a[248:251]\nds_read_b64_tr_b16 a[2:3],v10 offset:0\nds_read_b64_tr_b16 a[4:5],v11 offset:0\nM a[216:219],a[24:27],a[44:47],a[216:219]\nds_read_b64_tr_b16 a[6:7],v12 offset:0\nv_sub_u32 v60,v60,v61\nM a[220:223],a[28:31],a[44:47],a[220:223]\nds_read_b128 a[44:47],v5 offset:12288\n" \
"v_subrev_u32 v60,s62,v60\nM a[252:255],a[28:31],a[40:43],a[252:255]\ns_xor_b32 s62,s22,0x2000\nds_read_b128 a[40:43],v5 offset:8192\nv_mov_b32 v61,0xff800000\nM v[188:191],a[28:31],a[36:39],v[188:191]\ns_add_u32 s62,s62,84992\nds_read_b128 a[36:39],v5 offset:4096\n" \
"v_add_u32 v23,s62,v4\nM v[156:159],a[28:31],a[32:35],v[156:159]\nLG12_%=:\ns_cbranch_execz LHA155V_%=\ns_and_b32 s62,%2,1\nds_read_b128 a[32:35],v5 offset:0\nv_cmp_lt_i32 vcc,0,v60\nM v[128:131],a[48:51],a[80:83],v[128:131]\n" \
"s_lshl_b32 s62,s62,15\nds_read_b64_tr_b16 a[8:9],v9 offset:512\nds_read_b64_tr_b16 a[10:11],v10 offset:512\nM v[160:163],a[48:51],a[84:87],v[160:163]\ns_add_u32 s62,s62,s54\nds_read_b64_tr_b16 a[12:13],v11 offset:512\nv_cndmask_b32 v48,0,v61,vcc\nM a[224:227],a[48:51],a[88:91],a[224:227]\n" \
"s_add_u32 s62,s62,32768\nds_read_b64_tr_b16 a[14:15],v12 offset:512\nv_add_u32 v1,s62,v4\nM a[192:195],a[48:51],a[92:95],a[192:195]\nds_read_b64_tr_b16 a[16:17],v9 offset:1024\nds_read_b64_tr_b16 a[18:19],v10 offset:1024\nM a[196:199],a[52:55],a[92:95],a[196:199]\nds_read_b64_tr_b16 a[20:21],v11 offset:1024\n" \
"v_cmp_lt_i32 vcc,-1,v60\nM a[228:231],a[52:55],a[88:91],a[228:231]\nds_read_b64_tr_b16 a[22:23],v12 offset:1024\nv_cndmask_b32 v49,0,v61,vcc\nM v[164:167],a[52:55],a[84:87],v[164:167]\nds_read_b64_tr_b16 a[24:25],v9 offset:1536\nds_read_b64_tr_b16 a[26:27],v10 offset:1536\nM v[132:135],a[52:55],a[80:83],v[132:135]\n" \
"buffer_load_dwordx4 v21,s[32:35],s47 offen offset:128 lds\nM v[136:139],a[56:59],a[80:83],v[136:139]\ns_add_u32 m0,s51,40960\nv_cmp_lt_i32 vcc,-2,v60\nM v[168:171],a[56:59],a[84:87],v[168:171]\ns_mov_b32 s47,6553600\nds_read_b64_tr_b16 a[28:29],v11 offset:1536\nds_read_b64_tr_b16 a[30:31],v12 offset:1536\n" \
"M a[232:235],a[56:59],a[88:91],a[232:235]\nds_read_b64_tr_b16 a[48:49],v9 offset:8192\nv_cndmask_b32 v50,0,v61,vcc\nM a[200:203],a[56:59],a[92:95],a[200:203]\ns_mov_b64 exec,0xff\nglobal_load_dword v0,v2,s[100:101]\ns_mov_b64 exec,-1\nM a[204:207],a[60:63],a[92:95],a[204:207]\n" \
"ds_read_b64_tr_b16 a[50:51],v10 offset:8192\nds_read_b64_tr_b16 a[52:53],v11 offset:8192\nM a[236:239],a[60:63],a[88:91],a[236:239]\nbuffer_load_dwordx4 v21,s[32:35],s47 offen offset:0 lds\nM v[172:175],a[60:63],a[84:87],v[172:175]\ns_add_u32 m0,s51,41856\nds_read_b64_tr_b16 a[54:55],v12 offset:8192\nv_cmp_lt_i32 vcc,-3,v60\n" \
"M v[140:143],a[60:63],a[80:83],v[140:143]\nds_read_b64_tr_b16 a[56:57],v9 offset:8704\nv_cndmask_b32 v51,0,v61,vcc\nM v[144:147],a[64:67],a[80:83],v[144:147]\nds_read_b64_tr_b16 a[58:59],v10 offset:8704\nv_cmp_lt_i32 vcc,-16,v60\nM v[176:179],a[64:67],a[84:87],v[176:179]\nbuffer_store_dwordx4 v[104:107],v4,s[36:39],s96 offen offset:2048 sc1\n" \
"M a[240:243],a[64:67],a[88:91],a[240:243]\nds_read_b64_tr_b16 a[60:61],v11 offset:8704\nds_read_b64_tr_b16 a[62:63],v12 offset:8704\nM a[208:211],a[64:67],a[92:95],a[208:211]\nds_read_b64_tr_b16 a[64:65],v9 offset:9216\nv_cndmask_b32 v52,0,v61,vcc\nM a[212:215],a[68:71],a[92:95],a[212:215]\nds_read_b64_tr_b16 a[66:67],v10 offset:9216\n" \
"v_cmp_lt_i32 vcc,-17,v60\nM a[244:247],a[68:71],a[88:91],a[244:247]\nbuffer_load_dwordx4 v21,s[32:35],s47 offen offset:128 lds\nM v[180:183],a[68:71],a[84:87],v[180:183]\ns_add_u32 m0,s50,65536\nv_cndmask_b32 v53,0,v61,vcc\nv_cmp_lt_i32 vcc,-18,v60\nM v[148:151],a[68:71],a[80:83],v[148:151]\n" \
"s_mov_b32 s47,6815744\nds_read_b64_tr_b16 a[68:69],v11 offset:9216\nds_read_b64_tr_b16 a[70:71],v12 offset:9216\nM v[152:155],a[72:75],a[80:83],v[152:155]\nv_cndmask_b32 v54,0,v61,vcc\nv_cmp_lt_i32 vcc,-19,v60\nM v[184:187],a[72:75],a[84:87],v[184:187]\nbuffer_load_dwordx4 v13,s[28:31],s46 offen lds\n" \
"M a[248:251],a[72:75],a[88:91],a[248:251]\ns_add_u32 m0,s50,69632\nv_cndmask_b32 v55,0,v61,vcc\nM a[216:219],a[72:75],a[92:95],a[216:219]\nds_read_b64_tr_b16 a[72:73],v9 offset:9728\nds_read_b64_tr_b16 a[74:75],v10 offset:9728\nM a[220:223],a[76:79],a[92:95],a[220:223]\nds_read_b128 a[92:95],v6 offset:12288\n" \
"M a[252:255],a[76:79],a[88:91],a[252:255]\nbuffer_load_dwordx4 v14,s[28:31],s46 offen lds\nM v[188:191],a[76:79],a[84:87],v[188:191]\ns_add_u32 m0,s50,73728\nds_read_b128 a[84:87],v6 offset:4096\nM v[156:159],a[76:79],a[80:83],v[156:159]\ns_waitcnt lgkmcnt(4)\nds_read_b128 a[80:83],v6 offset:0\n" \
"M v[128:131],a[0:3],a[32:35],v[128:131]\nds_read_b64_tr_b16 a[76:77],v11 offset:9728\nM v[160:163],a[0:3],a[36:39],v[160:163]\nbuffer_load_dwordx4 v15,s[28:31],s46 offen lds\nM a[224:227],a[0:3],a[40:43],a[224:227]\ns_add_u32 m0,s50,77824\nM a[192:195],a[0:3],a[44:47],a[192:195]\nds_read_b64_tr_b16 a[78:79],v12 offset:9728\n" \
"ds_read_b128 a[88:91],v6 offset:8192\nM a[196:199],a[4:7],a[44:47],a[196:199]\nM a[228:231],a[4:7],a[40:43],a[228:231]\nbuffer_load_dwordx4 v16,s[28:31],s46 offen lds\nM v[164:167],a[4:7],a[36:39],v[164:167]\ns_add_u32 m0,s50,81920\nM v[132:135],a[4:7],a[32:35],v[132:135]\ns_mov_b32 s46,1536\n" \
"M v[136:139],a[8:11],a[32:35],v[136:139]\nM v[168:171],a[8:11],a[36:39],v[168:171]\nbuffer_load_dwordx4 v17,s[28:31],s46 offen lds\nM a[232:235],a[8:11],a[40:43],a[232:235]\ns_add_u32 m0,s50,86016\nM a[200:203],a[8:11],a[44:47],a[200:203]\nM a[204:207],a[12:15],a[44:47],a[204:207]\nM a[236:239],a[12:15],a[40:43],a[236:239]\n" \
"buffer_load_dwordx4 v18,s[28:31],s46 offen lds\nM v[172:175],a[12:15],a[36:39],v[172:175]\ns_add_u32 m0,s50,90112\nM v[140:143],a[12:15],a[32:35],v[140:143]\nM v[144:147],a[16:19],a[32:35],v[144:147]\nM v[176:179],a[16:19],a[36:39],v[176:179]\nbuffer_load_dwordx4 v19,s[28:31],s46 offen lds\nM a[240:243],a[16:19],a[40:43],a[240:243]\n" \
"s_add_u32 m0,s50,94208\nM a[208:211],a[16:19],a[44:47],a[208:211]\nM a[212:215],a[20:23],a[44:47],a[212:215]\nM a[244:247],a[20:23],a[40:43],a[244:247]\nbuffer_load_dwordx4 v20,s[28:31],s46 offen lds\nM v[180:183],a[20:23],a[36:39],v[180:183]\ns_add_u32 m0,s51,49152\nM v[148:151],a[20:23],a[32:35],v[148:151]\n" \
"s_mov_b32 s46,1664\nM v[152:155],a[24:27],a[32:35],v[152:155]\ns_waitcnt vmcnt(15) lgkmcnt(0)\nM v[184:187],a[24:27],a[36:39],v[184:187]\ns_barrier\nbuffer_store_dwordx4 v[108:111],v4,s[36:39],s96 offen offset:3072 sc1\nM a[248:251],a[24:27],a[40:43],a[248:251]\ns_add_u32 s96,s57,53248\n" \
"ds_read_b64_tr_b16 a[0:1],v9 offset:16384\nds_read_b64_tr_b16 a[2:3],v10 offset:16384\nM a[216:219],a[24:27],a[44:47],a[216:219]\nds_read_b64_tr_b16 a[4:5],v11 offset:16384\nM a[220:223],a[28:31],a[44:47],a[220:223]\nds_read_b128 a[44:47],v5 offset:45056\nM a[252:255],a[28:31],a[40:43],a[252:255]\nbuffer_load_dwordx4 v21,s[32:35],s47 offen offset:0 lds\n" \
"M v[188:191],a[28:31],a[36:39],v[188:191]\ns_add_u32 m0,s51,50048\nds_read_b128 a[36:39],v5 offset:36864\nM v[156:159],a[28:31],a[32:35],v[156:159]\nds_read_b128 a[32:35],v5 offset:32768\nM v[128:131],a[48:51],a[80:83],v[128:131]\nds_read_b128 a[40:43],v5 offset:40960\nM v[160:163],a[48:51],a[84:87],v[160:163]\n" \
"buffer_load_dwordx4 v21,s[32:35],s47 offen offset:128 lds\nM a[224:227],a[48:51],a[88:91],a[224:227]\ns_add_u32 m0,s51,57344\nM a[192:195],a[48:51],a[92:95],a[192:195]\ns_mov_b32 s47,7077888\nds_read_b64_tr_b16 a[6:7],v12 offset:16384\nds_read_b64_tr_b16 a[8:9],v9 offset:16896\nM a[196:199],a[52:55],a[92:95],a[196:199]\n" \
"ds_read_b64_tr_b16 a[10:11],v10 offset:16896\nM a[228:231],a[52:55],a[88:91],a[228:231]\nbuffer_load_dwordx4 v21,s[32:35],s47 offen offset:0 lds\nM v[164:167],a[52:55],a[84:87],v[164:167]\ns_add_u32 m0,s51,58240\nds_read_b64_tr_b16 a[12:13],v11 offset:16896\nM v[132:135],a[52:55],a[80:83],v[132:135]\nds_read_b64_tr_b16 a[14:15],v12 offset:16896\n" \
"M v[136:139],a[56:59],a[80:83],v[136:139]\nds_read_b64_tr_b16 a[16:17],v9 offset:17408\nM v[168:171],a[56:59],a[84:87],v[168:171]\nbuffer_load_dwordx4 v21,s[32:35],s47 offen offset:128 lds\nM a[232:235],a[56:59],a[88:91],a[232:235]\ns_add_u32 m0,s50,0\nds_read_b64_tr_b16 a[18:19],v10 offset:17408\nM a[200:203],a[56:59],a[92:95],a[200:203]\n" \
"s_mov_b32 s47,7340032\nds_read_b64_tr_b16 a[20:21],v11 offset:17408\nM a[204:207],a[60:63],a[92:95],a[204:207]\nds_read_b64_tr_b16 a[22:23],v12 offset:17408\nds_read_b64_tr_b16 a[24:25],v9 offset:17920\nM a[236:239],a[60:63],a[88:91],a[236:239]\nbuffer_load_dwordx4 v13,s[28:31],s46 offen lds\nM v[172:175],a[60:63],a[84:87],v[172:175]\n" \
"s_add_u32 m0,s50,4096\nM v[140:143],a[60:63],a[80:83],v[140:143]\nds_read_b64_tr_b16 a[26:27],v10 offset:17920\nds_read_b64_tr_b16 a[28:29],v11 offset:17920\nM v[144:147],a[64:67],a[80:83],v[144:147]\nds_read_b64_tr_b16 a[30:31],v12 offset:17920\nM v[176:179],a[64:67],a[84:87],v[176:179]\nbuffer_load_dwordx4 v14,s[28:31],s46 offen lds\n" \
"M a[240:243],a[64:67],a[88:91],a[240:243]\ns_add_u32 m0,s50,8192\nds_read_b64_tr_b16 a[48:49],v9 offset:24576\nM a[208:211],a[64:67],a[92:95],a[208:211]\nds_read_b64_tr_b16 a[50:51],v10 offset:24576\nM a[212:215],a[68:71],a[92:95],a[212:215]\nds_read_b64_tr_b16 a[52:53],v11 offset:24576\nM a[244:247],a[68:71],a[88:91],a[244:247]\n" \
"buffer_load_dwordx4 v15,s[28:31],s46 offen lds\nM v[180:183],a[68:71],a[84:87],v[180:183]\ns_add_u32 m0,s50,12288\nds_read_b64_tr_b16 a[54:55],v12 offset:24576\nM v[148:151],a[68:71],a[80:83],v[148:151]\nds_read_b64_tr_b16 a[56:57],v9 offset:25088\nM v[152:155],a[72:75],a[80:83],v[152:155]\nds_read_b64_tr_b16 a[58:59],v10 offset:25088\n" \
"ds_read_b64_tr_b16 a[60:61],v11 offset:25088\nM v[184:187],a[72:75],a[84:87],v[184:187]\nbuffer_load_dwordx4 v16,s[28:31],s46 offen lds\nM a[248:251],a[72:75],a[88:91],a[248:251]\ns_mov_b32 s46,1664\nM a[216:219],a[72:75],a[92:95],a[216:219]\ns_add_u32 m0,s50,16384\nds_read_b64_tr_b16 a[62:63],v12 offset:25088\n" \
"ds_read_b64_tr_b16 a[64:65],v9 offset:25600\nM a[220:223],a[76:79],a[92:95],a[220:223]\nds_read_b64_tr_b16 a[66:67],v10 offset:25600\nM a[252:255],a[76:79],a[88:91],a[252:255]\nbuffer_load_dwordx4 v17,s[28:31],s46 offen lds\nM v[188:191],a[76:79],a[84:87],v[188:191]\ns_add_u32 m0,s50,20480\nds_read_b128 a[84:87],v6 offset:36864\n" \
"M v[156:159],a[76:79],a[80:83],v[156:159]\ns_waitcnt lgkmcnt(4)\nds_read_b128 a[80:83],v6 offset:32768\nM v[128:131],a[0:3],a[32:35],v[128:131]\nds_read_b64_tr_b16 a[68:69],v11 offset:25600\nM v[160:163],a[0:3],a[36:39],v[160:163]\nbuffer_load_dwordx4 v18,s[28:31],s46 offen lds\nM a[224:227],a[0:3],a[40:43],a[224:227]\n" \
"s_add_u32 m0,s50,24576\nM a[192:195],a[0:3],a[44:47],a[192:195]\nds_read_b64_tr_b16 a[70:71],v12 offset:25600\nds_read_b64_tr_b16 a[72:73],v9 offset:26112\nM a[196:199],a[4:7],a[44:47],a[196:199]\nds_read_b64_tr_b16 a[74:75],v10 offset:26112\nM a[228:231],a[4:7],a[40:43],a[228:231]\nbuffer_load_dwordx4 v19,s[28:31],s46 offen lds\n" \
"M v[164:167],a[4:7],a[36:39],v[164:167]\ns_add_u32 m0,s50,28672\nds_read_b64_tr_b16 a[76:77],v11 offset:26112\nM v[132:135],a[4:7],a[32:35],v[132:135]\nds_read_b64_tr_b16 a[78:79],v12 offset:26112\nM v[136:139],a[8:11],a[32:35],v[136:139]\nds_read_b128 a[88:91],v6 offset:40960\nM v[168:171],a[8:11],a[36:39],v[168:171]\n" \
"buffer_load_dwordx4 v20,s[28:31],s46 offen lds\nM a[232:235],a[8:11],a[40:43],a[232:235]\ns_add_u32 m0,s51,0\nds_read_b128 a[92:95],v6 offset:45056\nM a[200:203],a[8:11],a[44:47],a[200:203]\ns_mov_b32 s46,1792\nM a[204:207],a[12:15],a[44:47],a[204:207]\nM a[236:239],a[12:15],a[40:43],a[236:239]\n" \
"buffer_load_dwordx4 v21,s[32:35],s47 offen offset:0 lds\nM v[172:175],a[12:15],a[36:39],v[172:175]\ns_add_u32 m0,s51,896\nM v[140:143],a[12:15],a[32:35],v[140:143]\nM v[144:147],a[16:19],a[32:35],v[144:147]\nM v[176:179],a[16:19],a[36:39],v[176:179]\nbuffer_load_dwordx4 v21,s[32:35],s47 offen offset:128 lds\nM a[240:243],a[16:19],a[40:43],a[240:243]\n" \
"s_add_u32 m0,s51,8192\nM a[208:211],a[16:19],a[44:47],a[208:211]\ns_mov_b32 s47,7602176\nM a[212:215],a[20:23],a[44:47],a[212:215]\nM a[244:247],a[20:23],a[40:43],a[244:247]\nbuffer_load_dwordx4 v21,s[32:35],s47 offen offset:0 lds\nM v[180:183],a[20:23],a[36:39],v[180:183]\ns_add_u32 m0,s51,9088\n" \
"M v[148:151],a[20:23],a[32:35],v[148:151]\nM v[152:155],a[24:27],a[32:35],v[152:155]\ns_waitcnt vmcnt(16) lgkmcnt(0)\nM v[184:187],a[24:27],a[36:39],v[184:187]\nbuffer_store_dwordx4 v[120:123],v4,s[36:39],s96 offen offset:0 sc1\ns_barrier\nM a[248:251],a[24:27],a[40:43],a[248:251]\nds_read_b64_tr_b16 a[0:1],v9 offset:32768\n" \
"ds_read_b64_tr_b16 a[2:3],v10 offset:32768\nM a[216:219],a[24:27],a[44:47],a[216:219]\nds_read_b64_tr_b16 a[4:5],v11 offset:32768\nM a[220:223],a[28:31],a[44:47],a[220:223]\nds_read_b128 a[44:47],v7 offset:12288\nM a[252:255],a[28:31],a[40:43],a[252:255]\nbuffer_load_dwordx4 v21,s[32:35],s47 offen offset:128 lds\nM v[188:191],a[28:31],a[36:39],v[188:191]\n" \
"s_add_u32 m0,s50,32768\nds_read_b128 a[36:39],v7 offset:4096\nM v[156:159],a[28:31],a[32:35],v[156:159]\ns_mov_b32 s47,7864320\nds_read_b128 a[32:35],v7 offset:0\nM v[128:131],a[48:51],a[80:83],v[128:131]\nds_read_b128 a[40:43],v7 offset:8192\nM v[160:163],a[48:51],a[84:87],v[160:163]\n" \
"buffer_load_dwordx4 v13,s[28:31],s46 offen lds\nM a[224:227],a[48:51],a[88:91],a[224:227]\ns_add_u32 m0,s50,36864\nM a[192:195],a[48:51],a[92:95],a[192:195]\nds_read_b64_tr_b16 a[6:7],v12 offset:32768\nds_read_b64_tr_b16 a[8:9],v9 offset:33280\nM a[196:199],a[52:55],a[92:95],a[196:199]\nds_read_b64_tr_b16 a[10:11],v10 offset:33280\n" \
"M a[228:231],a[52:55],a[88:91],a[228:231]\nbuffer_load_dwordx4 v14,s[28:31],s46 offen lds\nM v[164:167],a[52:55],a[84:87],v[164:167]\ns_add_u32 m0,s50,40960\nds_read_b64_tr_b16 a[12:13],v11 offset:33280\nM v[132:135],a[52:55],a[80:83],v[132:135]\nds_read_b64_tr_b16 a[14:15],v12 offset:33280\nM v[136:139],a[56:59],a[80:83],v[136:139]\n" \
"ds_read_b64_tr_b16 a[16:17],v9 offset:33792\nM v[168:171],a[56:59],a[84:87],v[168:171]\nbuffer_load_dwordx4 v15,s[28:31],s46 offen lds\nM a[232:235],a[56:59],a[88:91],a[232:235]\ns_add_u32 m0,s50,45056\nds_read_b64_tr_b16 a[18:19],v10 offset:33792\nM a[200:203],a[56:59],a[92:95],a[200:203]\nds_read_b64_tr_b16 a[20:21],v11 offset:33792\n" \
"M a[204:207],a[60:63],a[92:95],a[204:207]\nds_read_b64_tr_b16 a[22:23],v12 offset:33792\nds_read_b64_tr_b16 a[24:25],v9 offset:34304\nM a[236:239],a[60:63],a[88:91],a[236:239]\nbuffer_load_dwordx4 v16,s[28:31],s46 offen lds\nM v[172:175],a[60:63],a[84:87],v[172:175]\ns_add_u32 m0,s50,49152\nM v[140:143],a[60:63],a[80:83],v[140:143]\n" \
"s_mov_b32 s46,1792\nds_read_b64_tr_b16 a[26:27],v10 offset:34304\nds_read_b64_tr_b16 a[28:29],v11 offset:34304\nM v[144:147],a[64:67],a[80:83],v[144:147]\nds_read_b64_tr_b16 a[30:31],v12 offset:34304\nM v[176:179],a[64:67],a[84:87],v[176:179]\nbuffer_load_dwordx4 v17,s[28:31],s46 offen lds\nM a[240:243],a[64:67],a[88:91],a[240:243]\n" \
"s_add_u32 m0,s50,53248\nds_read_b64_tr_b16 a[48:49],v9 offset:40960\nM a[208:211],a[64:67],a[92:95],a[208:211]\nds_read_b64_tr_b16 a[50:51],v10 offset:40960\nM a[212:215],a[68:71],a[92:95],a[212:215]\nds_read_b64_tr_b16 a[52:53],v11 offset:40960\nM a[244:247],a[68:71],a[88:91],a[244:247]\nbuffer_load_dwordx4 v18,s[28:31],s46 offen lds\n" \
"M v[180:183],a[68:71],a[84:87],v[180:183]\ns_add_u32 m0,s50,57344\nds_read_b64_tr_b16 a[54:55],v12 offset:40960\nM v[148:151],a[68:71],a[80:83],v[148:151]\nds_read_b64_tr_b16 a[56:57],v9 offset:41472\nM v[152:155],a[72:75],a[80:83],v[152:155]\nds_read_b64_tr_b16 a[58:59],v10 offset:41472\nds_read_b64_tr_b16 a[60:61],v11 offset:41472\n" \
"M v[184:187],a[72:75],a[84:87],v[184:187]\nbuffer_load_dwordx4 v19,s[28:31],s46 offen lds\nM a[248:251],a[72:75],a[88:91],a[248:251]\ns_add_u32 m0,s50,61440\nM a[216:219],a[72:75],a[92:95],a[216:219]\nds_read_b64_tr_b16 a[62:63],v12 offset:41472\nds_read_b64_tr_b16 a[64:65],v9 offset:41984\nM a[220:223],a[76:79],a[92:95],a[220:223]\n" \
"s_waitcnt lgkmcnt(5)\nds_read_b64_tr_b16 a[66:67],v10 offset:41984\nM a[252:255],a[76:79],a[88:91],a[252:255]\nbuffer_load_dwordx4 v20,s[28:31],s46 offen lds\nM v[188:191],a[76:79],a[84:87],v[188:191]\ns_add_u32 m0,s51,16384\nds_read_b128 a[84:87],v8 offset:4096\nM v[156:159],a[76:79],a[80:83],v[156:159]\n" \
"s_mov_b32 s46,1920\nds_read_b128 a[80:83],v8 offset:0\nM v[128:131],a[0:3],a[32:35],v[128:131]\nds_read_b64_tr_b16 a[68:69],v11 offset:41984\nM v[160:163],a[0:3],a[36:39],v[160:163]\nbuffer_load_dwordx4 v21,s[32:35],s47 offen offset:0 lds\nM a[224:227],a[0:3],a[40:43],a[224:227]\ns_add_u32 m0,s51,17280\n" \
"M a[192:195],a[0:3],a[44:47],a[192:195]\nds_read_b64_tr_b16 a[70:71],v12 offset:41984\nds_read_b64_tr_b16 a[72:73],v9 offset:42496\nM a[196:199],a[4:7],a[44:47],a[196:199]\nds_read_b64_tr_b16 a[74:75],v10 offset:42496\nM a[228:231],a[4:7],a[40:43],a[228:231]\nbuffer_load_dwordx4 v21,s[32:35],s47 offen offset:128 lds\nM v[164:167],a[4:7],a[36:39],v[164:167]\n" \
"s_add_u32 m0,s51,24576\nds_read_b64_tr_b16 a[76:77],v11 offset:42496\nM v[132:135],a[4:7],a[32:35],v[132:135]\ns_mov_b32 s47,8126464\nds_read_b64_tr_b16 a[78:79],v12 offset:42496\nM v[136:139],a[8:11],a[32:35],v[136:139]\nds_read_b128 a[88:91],v8 offset:8192\nM v[168:171],a[8:11],a[36:39],v[168:171]\n" \
"buffer_load_dwordx4 v21,s[32:35],s47 offen offset:0 lds\nM a[232:235],a[8:11],a[40:43],a[232:235]\ns_add_u32 m0,s51,25472\nds_read_b128 a[92:95],v8 offset:12288\nM a[200:203],a[8:11],a[44:47],a[200:203]\nM a[204:207],a[12:15],a[44:47],a[204:207]\nM a[236:239],a[12:15],a[40:43],a[236:239]\nM v[172:175],a[12:15],a[36:39],v[172:175]\n" \
"buffer_load_dwordx4 v21,s[32:35],s47 offen offset:128 lds\nM v[140:143],a[12:15],a[32:35],v[140:143]\ns_add_u32 m0,s50,65536\nv_add_u32 v21,s63,v4\nM v[144:147],a[16:19],a[32:35],v[144:147]\ns_add_u32 s63,s22,16384\nv_add_u32 v62,s63,v4\nM v[176:179],a[16:19],a[36:39],v[176:179]\n" \
"s_cmp_lt_u32 %2,2\ns_cselect_b64 s[62:63],-1,0\nM a[240:243],a[16:19],a[40:43],a[240:243]\nbuffer_store_dwordx4 v[124:127],v4,s[36:39],s96 offen offset:1024 sc1\nM a[208:211],a[16:19],a[44:47],a[208:211]\nM a[212:215],a[20:23],a[44:47],a[212:215]\nM a[244:247],a[20:23],a[40:43],a[244:247]\nM v[180:183],a[20:23],a[36:39],v[180:183]\n" \
"M v[148:151],a[20:23],a[32:35],v[148:151]\nM v[152:155],a[24:27],a[32:35],v[152:155]\ns_waitcnt vmcnt(18) lgkmcnt(0)\nM v[184:187],a[24:27],a[36:39],v[184:187]\ns_barrier\nds_read_b64_tr_b16 a[0:1],v9 offset:49152\nM a[248:251],a[24:27],a[40:43],a[248:251]\nbuffer_load_dwordx4 v13,s[28:31],s46 offen lds\n" \
"M a[216:219],a[24:27],a[44:47],a[216:219]\ns_add_u32 m0,s50,69632\nds_read_b64_tr_b16 a[2:3],v10 offset:49152\nv_add_u32 v13,s54,v4\nM a[220:223],a[28:31],a[44:47],a[220:223]\nds_read_b128 a[44:47],v5 offset:12288\nM a[252:255],a[28:31],a[40:43],a[252:255]\nds_read_b128 a[40:43],v5 offset:8192\n" \
"M v[188:191],a[28:31],a[36:39],v[188:191]\nbuffer_load_dwordx4 v14,s[28:31],s46 offen lds\nM v[156:159],a[28:31],a[32:35],v[156:159]\ns_add_u32 m0,s50,73728\nds_read_b128 a[32:35],v5 offset:0\nv_add_u32 v14,0x10000,v13\nM v[128:131],a[48:51],a[80:83],v[128:131]\nds_read_b128 a[36:39],v5 offset:4096\n" \
"M v[160:163],a[48:51],a[84:87],v[160:163]\nds_read_b64_tr_b16 a[4:5],v11 offset:49152\nM a[224:227],a[48:51],a[88:91],a[224:227]\nbuffer_load_dwordx4 v15,s[28:31],s46 offen lds\nM a[192:195],a[48:51],a[92:95],a[192:195]\ns_add_u32 m0,s50,77824\nds_read_b64_tr_b16 a[6:7],v12 offset:49152\nM a[196:199],a[52:55],a[92:95],a[196:199]\n" \
"ds_read_b64_tr_b16 a[8:9],v9 offset:49664\nM a[228:231],a[52:55],a[88:91],a[228:231]\nds_read_b64_tr_b16 a[10:11],v10 offset:49664\nM v[164:167],a[52:55],a[84:87],v[164:167]\nbuffer_load_dwordx4 v16,s[28:31],s46 offen lds\nM v[132:135],a[52:55],a[80:83],v[132:135]\ns_add_u32 m0,s50,81920\nds_read_b64_tr_b16 a[12:13],v11 offset:49664\n" \
"M v[136:139],a[56:59],a[80:83],v[136:139]\ns_mov_b32 s46,1920\nds_read_b64_tr_b16 a[14:15],v12 offset:49664\nM v[168:171],a[56:59],a[84:87],v[168:171]\nds_read_b64_tr_b16 a[16:17],v9 offset:50176\nds_read_b64_tr_b16 a[18:19],v10 offset:50176\nM a[232:235],a[56:59],a[88:91],a[232:235]\nbuffer_store_dwordx4 v[72:75],v4,s[36:39],s96 offen offset:2048 sc1\n" \
"M a[200:203],a[56:59],a[92:95],a[200:203]\nds_read_b64_tr_b16 a[20:21],v11 offset:50176\nds_read_b64_tr_b16 a[22:23],v12 offset:50176\nM a[204:207],a[60:63],a[92:95],a[204:207]\nds_read_b64_tr_b16 a[24:25],v9 offset:50688\nM a[236:239],a[60:63],a[88:91],a[236:239]\nds_read_b64_tr_b16 a[26:27],v10 offset:50688\nds_read_b64_tr_b16 a[28:29],v11 offset:50688\n" \
"M v[172:175],a[60:63],a[84:87],v[172:175]\nbuffer_load_dwordx4 v17,s[28:31],s46 offen lds\nM v[140:143],a[60:63],a[80:83],v[140:143]\ns_add_u32 m0,s50,86016\nM v[144:147],a[64:67],a[80:83],v[144:147]\nds_read_b64_tr_b16 a[30:31],v12 offset:50688\nds_read_b64_tr_b16 a[48:49],v9 offset:57344\nM v[176:179],a[64:67],a[84:87],v[176:179]\n" \
"ds_read_b64_tr_b16 a[50:51],v10 offset:57344\nM a[240:243],a[64:67],a[88:91],a[240:243]\nbuffer_load_dwordx4 v18,s[28:31],s46 offen lds\nM a[208:211],a[64:67],a[92:95],a[208:211]\ns_add_u32 m0,s50,90112\nds_read_b64_tr_b16 a[52:53],v11 offset:57344\nM a[212:215],a[68:71],a[92:95],a[212:215]\nds_read_b64_tr_b16 a[54:55],v12 offset:57344\n" \
"M a[244:247],a[68:71],a[88:91],a[244:247]\nds_read_b64_tr_b16 a[56:57],v9 offset:57856\nM v[180:183],a[68:71],a[84:87],v[180:183]\nbuffer_load_dwordx4 v19,s[28:31],s46 offen lds\nM v[148:151],a[68:71],a[80:83],v[148:151]\ns_add_u32 m0,s50,94208\nds_read_b64_tr_b16 a[58:59],v10 offset:57856\nM v[152:155],a[72:75],a[80:83],v[152:155]\n" \
"ds_read_b64_tr_b16 a[60:61],v11 offset:57856\nM v[184:187],a[72:75],a[84:87],v[184:187]\nds_read_b64_tr_b16 a[62:63],v12 offset:57856\nds_read_b64_tr_b16 a[64:65],v9 offset:58368\nM a[248:251],a[72:75],a[88:91],a[248:251]\nbuffer_load_dwordx4 v20,s[28:31],s46 offen lds\nM a[216:219],a[72:75],a[92:95],a[216:219]\ns_add_u32 m0,s22,49152\n" \
"v_add_u32 v20,12288,v21\nv_cndmask_b32 v20,v20,v62,s[62:63]\nM a[220:223],a[76:79],a[92:95],a[220:223]\ns_lshr_b32 s62,%1,3\nds_read_b64_tr_b16 a[66:67],v10 offset:58368\nds_read_b64_tr_b16 a[68:69],v11 offset:58368\nM a[252:255],a[76:79],a[88:91],a[252:255]\ns_lshl_b32 s62,s62,2\n" \
"ds_read_b64_tr_b16 a[70:71],v12 offset:58368\nM v[188:191],a[76:79],a[84:87],v[188:191]\nbuffer_store_dwordx4 v[76:79],v4,s[36:39],s96 offen offset:3072 sc1\nM v[156:159],a[76:79],a[80:83],v[156:159]\nLG13_%=:\ns_cbranch_execz LHA155V_%=\ns_waitcnt lgkmcnt(3)\ns_add_u32 s62,s62,%2\n" \
"ds_read_b128 a[80:83],v6 offset:0\nds_read_b128 a[84:87],v6 offset:4096\nM v[128:131],a[0:3],a[32:35],v[128:131]\ns_lshr_b32 s63,%1,5\nds_read_b64_tr_b16 a[72:73],v9 offset:58880\nM v[160:163],a[0:3],a[36:39],v[160:163]\ns_lshl_b32 s62,s62,14\nds_read_b64_tr_b16 a[74:75],v10 offset:58880\n" \
"M a[224:227],a[0:3],a[40:43],a[224:227]\ns_lshl_b32 s63,s63,8\nds_read_b64_tr_b16 a[76:77],v11 offset:58880\nM a[192:195],a[0:3],a[44:47],a[192:195]\ns_sub_u32 s62,s62,s63\nds_read_b64_tr_b16 a[78:79],v12 offset:58880\nds_read_b128 a[88:91],v6 offset:8192\nM a[196:199],a[4:7],a[44:47],a[196:199]\n" \
"s_mov_b32 s46,0\nds_read_b128 a[92:95],v6 offset:12288\nM a[228:231],a[4:7],a[40:43],a[228:231]\nM v[164:167],a[4:7],a[36:39],v[164:167]\nM v[132:135],a[4:7],a[32:35],v[132:135]\nM v[136:139],a[8:11],a[32:35],v[136:139]\nM v[168:171],a[8:11],a[36:39],v[168:171]\nM a[232:235],a[8:11],a[40:43],a[232:235]\n" \
"M a[200:203],a[8:11],a[44:47],a[200:203]\nM a[204:207],a[12:15],a[44:47],a[204:207]\nM a[236:239],a[12:15],a[40:43],a[236:239]\nM v[172:175],a[12:15],a[36:39],v[172:175]\nM v[140:143],a[12:15],a[32:35],v[140:143]\nM v[144:147],a[16:19],a[32:35],v[144:147]\nM v[176:179],a[16:19],a[36:39],v[176:179]\nM a[240:243],a[16:19],a[40:43],a[240:243]\n" \
"M a[208:211],a[16:19],a[44:47],a[208:211]\nM a[212:215],a[20:23],a[44:47],a[212:215]\nM a[244:247],a[20:23],a[40:43],a[244:247]\nM v[180:183],a[20:23],a[36:39],v[180:183]\nM v[148:151],a[20:23],a[32:35],v[148:151]\nM v[152:155],a[24:27],a[32:35],v[152:155]\ns_waitcnt vmcnt(15) lgkmcnt(0)\nM v[184:187],a[24:27],a[36:39],v[184:187]\n" \
"s_barrier\nds_read_b64_tr_b16 a[0:1],v9 offset:0\nM a[248:251],a[24:27],a[40:43],a[248:251]\nbuffer_load_dwordx4 v22,s[40:43],s61 offen offset:0 sc1 lds\nM a[216:219],a[24:27],a[44:47],a[216:219]\nds_read_b64_tr_b16 a[2:3],v10 offset:0\nM a[220:223],a[28:31],a[44:47],a[220:223]\nds_read_b64_tr_b16 a[4:5],v11 offset:0\n" \
"ds_read_b64_tr_b16 a[6:7],v12 offset:0\nM a[252:255],a[28:31],a[40:43],a[252:255]\nds_read_b64_tr_b16 a[8:9],v9 offset:512\nM v[188:191],a[28:31],a[36:39],v[188:191]\nbuffer_load_dwordx4 v22,s[40:43],s61 offen offset:1024 sc1 lds\nM v[156:159],a[28:31],a[32:35],v[156:159]\nds_read_b128 a[32:35],v5 offset:45056\nM v[128:131],a[48:51],a[80:83],v[128:131]\n" \
"ds_read_b64_tr_b16 a[10:11],v10 offset:512\nM v[160:163],a[48:51],a[84:87],v[160:163]\nds_read_b64_tr_b16 a[12:13],v11 offset:512\nM a[224:227],a[48:51],a[88:91],a[224:227]\nbuffer_load_dwordx4 v22,s[40:43],s61 offen offset:2048 sc1 lds\nM a[192:195],a[48:51],a[92:95],a[192:195]\nds_read_b64_tr_b16 a[14:15],v12 offset:512\nM a[196:199],a[52:55],a[92:95],a[196:199]\n" \
"ds_read_b64_tr_b16 a[16:17],v9 offset:1024\nM a[228:231],a[52:55],a[88:91],a[228:231]\nds_read_b64_tr_b16 a[18:19],v10 offset:1024\nM v[164:167],a[52:55],a[84:87],v[164:167]\nbuffer_load_dwordx4 v22,s[40:43],s61 offen offset:3072 sc1 lds\nM v[132:135],a[52:55],a[80:83],v[132:135]\ns_add_u32 m0,s22,81920\nds_read_b64_tr_b16 a[20:21],v11 offset:1024\n" \
"M v[136:139],a[56:59],a[80:83],v[136:139]\ns_add_u32 s61,s44,40960\nds_read_b64_tr_b16 a[22:23],v12 offset:1024\nM v[168:171],a[56:59],a[84:87],v[168:171]\nds_read_b64_tr_b16 a[24:25],v9 offset:1536\nds_read_b64_tr_b16 a[26:27],v10 offset:1536\nM a[232:235],a[56:59],a[88:91],a[232:235]\nbuffer_load_dwordx4 v22,s[40:43],s61 offen offset:0 sc1 lds\n" \
"M a[200:203],a[56:59],a[92:95],a[200:203]\nM a[204:207],a[60:63],a[92:95],a[204:207]\nds_read_b64_tr_b16 a[28:29],v11 offset:1536\nds_read_b64_tr_b16 a[30:31],v12 offset:1536\nM a[236:239],a[60:63],a[88:91],a[236:239]\nds_read_b128 a[44:47],v6 offset:45056\nM v[172:175],a[60:63],a[84:87],v[172:175]\nbuffer_load_dwordx4 v22,s[40:43],s61 offen offset:1024 sc1 lds\n" \
"M v[140:143],a[60:63],a[80:83],v[140:143]\nM v[144:147],a[64:67],a[80:83],v[144:147]\nds_read_b64_tr_b16 a[48:49],v9 offset:8192\nds_read_b64_tr_b16 a[50:51],v10 offset:8192\nM v[176:179],a[64:67],a[84:87],v[176:179]\nds_read_b64_tr_b16 a[52:53],v11 offset:8192\nM a[240:243],a[64:67],a[88:91],a[240:243]\ns_waitcnt vmcnt(7)\n" \
"ds_read_b64_tr_b16 a[54:55],v12 offset:8192\nM a[208:211],a[64:67],a[92:95],a[208:211]\ns_barrier\nds_read_b128 a[36:39],v7 offset:12288\nM a[212:215],a[68:71],a[92:95],a[212:215]\nbuffer_load_dwordx4 v22,s[40:43],s61 offen offset:2048 sc1 lds\nM a[244:247],a[68:71],a[88:91],a[244:247]\nds_read_b64_tr_b16 a[96:97],v9 offset:16384\n" \
"M v[180:183],a[68:71],a[84:87],v[180:183]\nds_read_b64_tr_b16 a[98:99],v10 offset:16384\nM v[148:151],a[68:71],a[80:83],v[148:151]\nds_read_b64_tr_b16 a[100:101],v11 offset:16384\nds_read_b64_tr_b16 a[102:103],v12 offset:16384\nM v[152:155],a[72:75],a[80:83],v[152:155]\nds_read_b64_tr_b16 a[104:105],v9 offset:16896\nM v[184:187],a[72:75],a[84:87],v[184:187]\n" \
"ds_read_b64_tr_b16 a[106:107],v10 offset:16896\nM a[248:251],a[72:75],a[88:91],a[248:251]\nds_read_b64_tr_b16 a[108:109],v11 offset:16896\nds_read_b64_tr_b16 a[110:111],v12 offset:16896\nM a[216:219],a[72:75],a[92:95],a[216:219]\nds_read_b64_tr_b16 a[112:113],v9 offset:17408\nM a[220:223],a[76:79],a[92:95],a[220:223]\nds_read_b64_tr_b16 a[114:115],v10 offset:17408\n" \
"M a[252:255],a[76:79],a[88:91],a[252:255]\nds_read_b64_tr_b16 a[116:117],v11 offset:17408\nds_read_b64_tr_b16 a[118:119],v12 offset:17408\nM v[188:191],a[76:79],a[84:87],v[188:191]\nds_read_b64_tr_b16 a[120:121],v9 offset:17920\nM v[156:159],a[76:79],a[80:83],v[156:159]\ns_waitcnt lgkmcnt(7)\nds_read_b64_tr_b16 a[122:123],v10 offset:17920\n" \
"M a[192:195],a[0:3],a[32:35],a[192:195]\nds_read_b64_tr_b16 a[124:125],v11 offset:17920\nds_read_b64_tr_b16 a[126:127],v12 offset:17920\nM a[196:199],a[4:7],a[32:35],a[196:199]\nds_read_b128 a[40:43],v8 offset:12288\nM a[200:203],a[8:11],a[32:35],a[200:203]\nds_read_b64_tr_b16 a[128:129],v9 offset:24576\nM a[204:207],a[12:15],a[32:35],a[204:207]\n" \
"ds_read_b64_tr_b16 a[130:131],v10 offset:24576\nM a[208:211],a[16:19],a[32:35],a[208:211]\nds_read_b64_tr_b16 a[132:133],v11 offset:24576\nds_read_b64_tr_b16 a[134:135],v12 offset:24576\nM a[212:215],a[20:23],a[32:35],a[212:215]\nds_read_b64_tr_b16 a[136:137],v9 offset:25088\nM a[216:219],a[24:27],a[32:35],a[216:219]\nds_read_b64_tr_b16 a[138:139],v10 offset:25088\n" \
"M a[220:223],a[28:31],a[32:35],a[220:223]\nds_read_b64_tr_b16 a[140:141],v11 offset:25088\nds_read_b64_tr_b16 a[142:143],v12 offset:25088\nM a[192:195],a[96:99],a[36:39],a[192:195]\nds_read_b64_tr_b16 a[144:145],v9 offset:25600\nM a[196:199],a[100:103],a[36:39],a[196:199]\nds_read_b64_tr_b16 a[146:147],v10 offset:25600\nM a[200:203],a[104:107],a[36:39],a[200:203]\n" \
"s_waitcnt lgkmcnt(6)\nds_read_b64_tr_b16 a[148:149],v11 offset:25600\nds_read_b64_tr_b16 a[150:151],v12 offset:25600\nM a[204:207],a[108:111],a[36:39],a[204:207]\nds_read_b64_tr_b16 a[152:153],v9 offset:26112\nds_read_b64_tr_b16 a[154:155],v10 offset:26112\nM a[208:211],a[112:115],a[36:39],a[208:211]\nds_read_b64_tr_b16 a[156:157],v11 offset:26112\n" \
"ds_read_b64_tr_b16 a[158:159],v12 offset:26112\nM a[212:215],a[116:119],a[36:39],a[212:215]\nbuffer_load_dwordx4 v22,s[40:43],s61 offen offset:3072 sc1 lds\nM a[216:219],a[120:123],a[36:39],a[216:219]\ns_add_u32 m0,s22,98304\nM a[220:223],a[124:127],a[36:39],a[220:223]\ns_add_u32 s61,s44,16384\nds_read_b64_tr_b16 a[56:57],v9 offset:8704\n" \
"ds_read_b64_tr_b16 a[58:59],v10 offset:8704\nM a[192:195],a[128:131],a[40:43],a[192:195]\nds_read_b64_tr_b16 a[60:61],v11 offset:8704\nds_read_b64_tr_b16 a[62:63],v12 offset:8704\nM a[196:199],a[132:135],a[40:43],a[196:199]\ns_waitcnt lgkmcnt(6)\nds_read_b64_tr_b16 a[64:65],v9 offset:9216\nds_read_b64_tr_b16 a[66:67],v10 offset:9216\n" \
"M a[200:203],a[136:139],a[40:43],a[200:203]\nds_read_b128 a[80:83],v5 offset:40960\nds_read_b128 v[240:243],v5 offset:36864\nds_read_b128 v[224:227],v5 offset:32768\nds_read_b128 a[84:87],v7 offset:8192\nds_read_b128 v[244:247],v7 offset:4096\nds_read_b128 v[228:231],v7 offset:0\nds_read_b128 a[88:91],v8 offset:8192\n" \
"ds_read_b128 v[248:251],v8 offset:4096\nds_read_b128 v[232:235],v8 offset:0\nds_read_b64_tr_b16 a[68:69],v11 offset:9216\nds_read_b64_tr_b16 a[70:71],v12 offset:9216\nds_read_b64_tr_b16 a[72:73],v9 offset:9728\nds_read_b64_tr_b16 a[74:75],v10 offset:9728\nds_read_b64_tr_b16 a[76:77],v11 offset:9728\nds_read_b64_tr_b16 a[78:79],v12 offset:9728\n" \
"ds_read_b128 a[92:95],v6 offset:40960\nds_read_b128 v[252:255],v6 offset:36864\nds_read_b128 v[236:239],v6 offset:32768\nM a[204:207],a[140:143],a[40:43],a[204:207]\nM a[208:211],a[144:147],a[40:43],a[208:211]\ns_mov_b64 exec,0\ns_cmp_eq_u32 %2,0\ns_cbranch_scc1 LG14_%=\n" \
"s_cmp_eq_u32 %2,1\ns_cbranch_scc1 LG15_%=\ns_cmp_eq_u32 %2,2\ns_cbranch_scc1 LG26_%=\ns_cmp_eq_u32 %2,3\ns_cbranch_scc1 LG27_%=\nLHR1_%=:\ns_mov_b64 exec,-1\n" \
"M a[212:215],a[148:151],a[40:43],a[212:215]\nds_write_b128 v1,v[88:91] offset:0\nM a[216:219],a[152:155],a[40:43],a[216:219]\ns_waitcnt lgkmcnt(15)\nM a[220:223],a[156:159],a[40:43],a[220:223]\nM a[192:195],a[48:51],a[44:47],a[192:195]\ns_waitcnt vmcnt(8) lgkmcnt(1)\nM a[196:199],a[52:55],a[44:47],a[196:199]\n" \
"s_barrier\nM a[200:203],a[56:59],a[44:47],a[200:203]\nbuffer_store_dwordx2 v[58:59],v56,s[36:39],s24 offen sc1\nM a[204:207],a[60:63],a[44:47],a[204:207]\nds_write_b128 v1,v[92:95] offset:1024\nM a[208:211],a[64:67],a[44:47],a[208:211]\nbuffer_load_dwordx4 v22,s[40:43],s61 offen offset:0 sc1 lds\nM a[212:215],a[68:71],a[44:47],a[212:215]\n" \
"v_accvgpr_read_b32 v5,a192\nv_accvgpr_read_b32 v10,a193\nv_accvgpr_read_b32 v11,a194\nM a[216:219],a[72:75],a[44:47],a[216:219]\nv_accvgpr_read_b32 v12,a195\nC v6,v5,v10\nC v7,v11,v12\nM a[220:223],a[76:79],a[44:47],a[220:223]\n" \
"ds_write_b128 v1,v[104:107] offset:2048\nv_accvgpr_read_b32 v5,a196\nM a[224:227],a[0:3],a[80:83],a[224:227]\nbuffer_load_dwordx4 v22,s[40:43],s61 offen offset:1024 sc1 lds\nv_accvgpr_read_b32 v10,a197\nM a[224:227],a[96:99],a[84:87],a[224:227]\nv_accvgpr_read_b32 v11,a198\nv_accvgpr_read_b32 v12,a199\n" \
"C v8,v5,v10\nM a[224:227],a[128:131],a[88:91],a[224:227]\nC v9,v11,v12\nv_accvgpr_read_b32 v32,a200\nv_accvgpr_read_b32 v33,a201\nM a[224:227],a[48:51],a[92:95],a[224:227]\nds_write_b128 v20,v[6:9] offset:0\nv_accvgpr_read_b32 v34,a202\n" \
"M a[228:231],a[52:55],a[92:95],a[228:231]\nbuffer_load_dwordx4 v22,s[40:43],s61 offen offset:2048 sc1 lds\nv_accvgpr_read_b32 v35,a203\nM a[228:231],a[132:135],a[88:91],a[228:231]\nC v36,v32,v33\nC v37,v34,v35\nv_accvgpr_read_b32 v32,a204\nM a[228:231],a[100:103],a[84:87],a[228:231]\n" \
"v_accvgpr_read_b32 v33,a205\nv_accvgpr_read_b32 v34,a206\nv_accvgpr_read_b32 v35,a207\nM a[228:231],a[4:7],a[80:83],a[228:231]\nC v38,v32,v33\nC v39,v34,v35\nv_accvgpr_read_b32 v5,a208\nM a[232:235],a[8:11],a[80:83],a[232:235]\n" \
"buffer_load_dwordx4 v22,s[40:43],s61 offen offset:3072 sc1 lds\nv_accvgpr_read_b32 v10,a209\nM a[232:235],a[104:107],a[84:87],a[232:235]\ns_add_u32 m0,s22,114688\nv_accvgpr_read_b32 v11,a210\nv_accvgpr_read_b32 v12,a211\nC v6,v5,v10\nM a[232:235],a[136:139],a[88:91],a[232:235]\n" \
"s_add_u32 s61,s44,0\nds_write_b128 v1,v[108:111] offset:3072\nC v7,v11,v12\nM a[232:235],a[56:59],a[92:95],a[232:235]\nv_accvgpr_read_b32 v5,a212\nv_accvgpr_read_b32 v10,a213\nv_accvgpr_read_b32 v11,a214\nM a[236:239],a[60:63],a[92:95],a[236:239]\n" \
"buffer_load_dwordx4 v22,s[40:43],s61 offen offset:0 sc1 lds\nv_accvgpr_read_b32 v12,a215\nM a[236:239],a[140:143],a[88:91],a[236:239]\nC v8,v5,v10\nC v9,v11,v12\nv_accvgpr_read_b32 v32,a216\nM a[236:239],a[108:111],a[84:87],a[236:239]\nv_accvgpr_read_b32 v33,a217\n" \
"v_accvgpr_read_b32 v34,a218\nv_accvgpr_read_b32 v35,a219\nM a[236:239],a[12:15],a[80:83],a[236:239]\nds_write_b128 v20,v[36:39] offset:1024\nC v36,v32,v33\nM a[240:243],a[16:19],a[80:83],a[240:243]\nbuffer_load_dwordx4 v22,s[40:43],s61 offen offset:1024 sc1 lds\nC v37,v34,v35\n" \
"M a[240:243],a[112:115],a[84:87],a[240:243]\nv_accvgpr_read_b32 v32,a220\nv_accvgpr_read_b32 v33,a221\nv_accvgpr_read_b32 v34,a222\nM a[240:243],a[144:147],a[88:91],a[240:243]\nv_accvgpr_read_b32 v35,a223\nC v38,v32,v33\nC v39,v34,v35\n" \
"M a[240:243],a[64:67],a[92:95],a[240:243]\nds_write_b128 v20,v[6:9] offset:2048\nM a[244:247],a[68:71],a[92:95],a[244:247]\nv_accvgpr_read_b32 v5,a224\nv_accvgpr_read_b32 v10,a225\nM a[244:247],a[148:151],a[88:91],a[244:247]\nv_accvgpr_read_b32 v11,a226\nv_accvgpr_read_b32 v12,a227\n" \
"M a[244:247],a[116:119],a[84:87],a[244:247]\nds_write_b128 v1,v[120:123] offset:4096\nds_write_b128 v1,v[124:127] offset:5120\nds_write_b128 v1,v[72:75] offset:6144\nds_write_b128 v1,v[76:79] offset:7168\nM a[244:247],a[20:23],a[80:83],a[244:247]\nC v6,v5,v10\nC v7,v11,v12\n" \
"M a[248:251],a[24:27],a[80:83],a[248:251]\nv_accvgpr_read_b32 v32,a232\nv_accvgpr_read_b32 v33,a233\nM a[248:251],a[120:123],a[84:87],a[248:251]\nv_accvgpr_read_b32 v34,a234\nv_accvgpr_read_b32 v35,a235\nM a[248:251],a[152:155],a[88:91],a[248:251]\nbuffer_load_dwordx4 v22,s[40:43],s61 offen offset:2048 sc1 lds\n" \
"M a[248:251],a[72:75],a[92:95],a[248:251]\nv_accvgpr_read_b32 v5,a228\nv_accvgpr_read_b32 v10,a229\nM a[252:255],a[76:79],a[92:95],a[252:255]\ns_waitcnt lgkmcnt(0)\nv_accvgpr_read_b32 v11,a230\nv_accvgpr_read_b32 v12,a231\nM a[252:255],a[156:159],a[88:91],a[252:255]\n" \
"s_barrier\nC v8,v5,v10\nM a[252:255],a[124:127],a[84:87],a[252:255]\nds_write_b128 v20,v[36:39] offset:3072\nM a[252:255],a[28:31],a[80:83],a[252:255]\nC v36,v32,v33\nC v37,v34,v35\nM v[160:163],a[0:3],v[240:243],v[160:163]\n" \
"buffer_load_dwordx4 v22,s[40:43],s61 offen offset:3072 sc1 lds\nM v[160:163],a[96:99],v[244:247],v[160:163]\ns_add_u32 m0,s22,0\nC v9,v11,v12\nv_accvgpr_read_b32 v32,a236\nM v[164:167],a[100:103],v[244:247],v[164:167]\ns_add_u32 s61,s44,24576\nds_read_b128 a[176:179],v20 offset:0\n" \
"v_accvgpr_read_b32 v33,a237\nM v[164:167],a[4:7],v[240:243],v[164:167]\nds_read_b128 a[180:183],v20 offset:1024\nv_accvgpr_read_b32 v34,a238\nM v[168:171],a[8:11],v[240:243],v[168:171]\nds_read_b128 a[184:187],v20 offset:2048\nv_accvgpr_read_b32 v35,a239\nM v[168:171],a[104:107],v[244:247],v[168:171]\n" \
"ds_read_b128 a[192:195],v13 offset:32768\nC v38,v32,v33\nM v[172:175],a[108:111],v[244:247],v[172:175]\nds_read_b128 a[196:199],v13 offset:34816\nC v39,v34,v35\nM v[172:175],a[12:15],v[240:243],v[172:175]\nds_read_b128 a[200:203],v13 offset:36864\nv_accvgpr_read_b32 v5,a240\n" \
"M v[176:179],a[16:19],v[240:243],v[176:179]\nds_read_b128 a[188:191],v20 offset:3072\nds_read_b128 a[204:207],v13 offset:38912\nv_add_u32 v20,s65,v20\nM v[176:179],a[112:115],v[244:247],v[176:179]\nds_read_b128 a[208:211],v13 offset:33792\nv_accvgpr_read_b32 v10,a241\nM v[180:183],a[116:119],v[244:247],v[180:183]\n" \
"ds_read_b128 a[212:215],v13 offset:35840\nv_accvgpr_read_b32 v11,a242\nM v[180:183],a[20:23],v[240:243],v[180:183]\nds_read_b128 a[216:219],v13 offset:37888\nv_accvgpr_read_b32 v12,a243\nM v[184:187],a[24:27],v[240:243],v[184:187]\nds_read_b128 a[220:223],v13 offset:39936\nv_accvgpr_read_b32 v32,a248\n" \
"M v[184:187],a[120:123],v[244:247],v[184:187]\nbuffer_load_dwordx4 v22,s[40:43],s61 offen offset:0 sc1 lds\nM v[188:191],a[124:127],v[244:247],v[188:191]\nv_accvgpr_read_b32 v33,a249\nv_accvgpr_read_b32 v34,a250\nM v[188:191],a[28:31],v[240:243],v[188:191]\ns_waitcnt lgkmcnt(3)\nv_accvgpr_read_b32 v35,a251\n" \
"M v[88:91],a[192:195],a[176:179],v[48:51]\nds_write_b128 v20,v[6:9] offset:0\nC v6,v5,v10\nM v[88:91],a[196:199],a[180:183],v[88:91]\nC v7,v11,v12\nv_accvgpr_read_b32 v5,a244\nM v[88:91],a[200:203],a[184:187],v[88:91]\nbuffer_load_dwordx4 v22,s[40:43],s61 offen offset:1024 sc1 lds\n" \
"M v[88:91],a[204:207],a[188:191],v[88:91]\nv_accvgpr_read_b32 v10,a245\nv_accvgpr_read_b32 v11,a246\nM v[92:95],a[208:211],a[176:179],v[52:55]\ns_waitcnt lgkmcnt(1)\nv_accvgpr_read_b32 v12,a247\nC v8,v5,v10\nM v[92:95],a[212:215],a[180:183],v[92:95]\n" \
"ds_write_b128 v20,v[36:39] offset:1024\nC v36,v32,v33\nM v[92:95],a[216:219],a[184:187],v[92:95]\nC v37,v34,v35\nC v9,v11,v12\nM v[92:95],a[220:223],a[188:191],v[92:95]\nbuffer_load_dwordx4 v22,s[40:43],s61 offen offset:2048 sc1 lds\nM v[160:163],a[128:131],v[248:251],v[160:163]\n" \
"v_accvgpr_read_b32 v32,a252\nv_accvgpr_read_b32 v33,a253\nM v[160:163],a[48:51],v[252:255],v[160:163]\nv_accvgpr_read_b32 v34,a254\nv_accvgpr_read_b32 v35,a255\nM v[164:167],a[52:55],v[252:255],v[164:167]\nv_max3_f32 v60,v88,v89,v90\nC v38,v32,v33\n" \
"M v[164:167],a[132:135],v[248:251],v[164:167]\nC v39,v34,v35\nv_max3_f32 v61,v91,v92,v93\nM v[168:171],a[136:139],v[248:251],v[168:171]\nds_write_b128 v20,v[6:9] offset:2048\nv_max3_f32 v60,v60,v94,v95\nM v[168:171],a[56:59],v[252:255],v[168:171]\nv_max3_f32 v60,v60,v61,s76\n" \
"v_mov_b32 v61,v60\nM v[172:175],a[60:63],v[252:255],v[172:175]\ns_nop 0\nv_permlane32_swap_b32 v60,v61\nv_max_f32 v60,v60,v61\nM v[172:175],a[140:143],v[248:251],v[172:175]\nv_mov_b32 v61,v60\nC v160,v160,v161\n" \
"M v[176:179],a[144:147],v[248:251],v[176:179]\ns_waitcnt vmcnt(12)\nds_write_b128 v20,v[36:39] offset:3072\nM v[176:179],a[64:67],v[252:255],v[176:179]\ns_barrier\nC v161,v162,v163\nM v[180:183],a[68:71],v[252:255],v[180:183]\nv_permlane16_swap_b32 v60,v61\n" \
"v_min_f32 v44,-v60,-v61\nM v[180:183],a[148:151],v[248:251],v[180:183]\nds_read_b128 a[160:163],v20 offset:0\nv_add_f32 v88,v88,v44\nM v[184:187],a[152:155],v[248:251],v[184:187]\nds_read_b128 a[164:167],v20 offset:1024\nv_exp_f32 v88,v88\nM v[184:187],a[72:75],v[252:255],v[184:187]\n" \
"ds_read_b128 a[168:171],v20 offset:2048\nv_add_f32 v89,v89,v44\nM v[188:191],a[76:79],v[252:255],v[188:191]\nds_read_b128 a[172:175],v20 offset:3072\nv_add_f32 v90,v90,v44\nM v[188:191],a[156:159],v[248:251],v[188:191]\nv_exp_f32 v89,v89\nM v[128:131],a[128:131],v[232:235],v[128:131]\n" \
"buffer_load_dwordx4 v22,s[40:43],s61 offen offset:3072 sc1 lds\nM v[128:131],a[0:3],v[224:227],v[128:131]\ns_add_u32 m0,s22,32768\nds_read_b128 v[192:195],v13 offset:49152\nC v168,v168,v169\nM v[132:135],a[4:7],v[224:227],v[132:135]\ns_add_u32 s61,s44,8192\nds_read_b128 v[196:199],v13 offset:51200\n" \
"C v176,v176,v177\nM v[132:135],a[132:135],v[232:235],v[132:135]\nds_read_b128 v[200:203],v13 offset:53248\nv_add_u32 v20,s65,v20\nM v[136:139],a[136:139],v[232:235],v[136:139]\nds_read_b128 v[204:207],v13 offset:55296\nC v162,v164,v165\nM v[136:139],a[8:11],v[224:227],v[136:139]\n" \
"ds_read_b128 v[208:211],v13 offset:50176\nC v163,v166,v167\nM v[140:143],a[12:15],v[224:227],v[140:143]\nds_read_b128 v[212:215],v13 offset:52224\nC v169,v170,v171\nM v[140:143],a[140:143],v[232:235],v[140:143]\nds_read_b128 v[216:219],v13 offset:54272\nv_add_f32 v91,v91,v44\n" \
"M v[144:147],a[144:147],v[232:235],v[144:147]\nv_exp_f32 v90,v90\nM v[144:147],a[16:19],v[224:227],v[144:147]\nds_read_b128 v[220:223],v13 offset:56320\nC v177,v178,v179\nM v[148:151],a[20:23],v[224:227],v[148:151]\nds_write_b128 v20,v[160:163] offset:0\nM v[148:151],a[148:151],v[232:235],v[148:151]\n" \
"C v170,v172,v173\nC v171,v174,v175\nM v[152:155],a[152:155],v[232:235],v[152:155]\nC v178,v180,v181\nC v179,v182,v183\nM v[152:155],a[24:27],v[224:227],v[152:155]\nv_exp_f32 v91,v91\nM v[156:159],a[28:31],v[224:227],v[156:159]\n" \
"v_add_f32 v92,v92,v44\nv_min_f32 v45,-v60,-v61\nM v[156:159],a[156:159],v[232:235],v[156:159]\ns_waitcnt lgkmcnt(1)\nds_write_b128 v20,v[168:171] offset:1024\nM v[112:115],v[192:195],a[160:163],v[48:51]\nC v184,v184,v185\nv_min_f32 v46,-v60,-v61\n" \
"M v[112:115],v[196:199],a[164:167],v[112:115]\nv_min_f32 v47,-v60,-v61\nC v185,v186,v187\nM v[112:115],v[200:203],a[168:171],v[112:115]\nv_exp_f32 v92,v92\nM v[112:115],v[204:207],a[172:175],v[112:115]\nds_write_b128 v20,v[176:179] offset:2048\nM v[116:119],v[208:211],a[160:163],v[52:55]\n" \
"v_min_f32 v29,-v60,-v61\nv_add_f32 v93,v93,v44\nM v[116:119],v[212:215],a[164:167],v[116:119]\nC v186,v188,v189\nC v187,v190,v191\nM v[116:119],v[216:219],a[168:171],v[116:119]\nds_read_b128 a[192:195],v14 offset:0\nv_max3_f32 v60,v112,v113,v114\n" \
"M v[116:119],v[220:223],a[172:175],v[116:119]\nds_read_b128 a[196:199],v14 offset:1024\nv_add_f32 v94,v94,v44\nM v[120:123],v[192:195],a[176:179],v[44:47]\nv_exp_f32 v93,v93\nM v[120:123],v[196:199],a[180:183],v[120:123]\nds_write_b128 v20,v[184:187] offset:3072\nM v[120:123],v[200:203],a[184:187],v[120:123]\n" \
"v_max3_f32 v61,v115,v116,v117\nv_max3_f32 v60,v60,v118,v119\nM v[120:123],v[204:207],a[188:191],v[120:123]\nLG14_%=:\ns_cbranch_execz LG28_%=\nv_max3_f32 v60,v60,v61,s76\nv_mov_b32 v61,v60\nM v[124:127],v[208:211],a[176:179],v[44:47]\n" \
"ds_read_b128 a[200:203],v14 offset:2048\nv_add_f32 v95,v95,v44\nM v[124:127],v[212:215],a[180:183],v[124:127]\nv_permlane32_swap_b32 v60,v61\nv_max_f32 v60,v60,v61\nM v[124:127],v[216:219],a[184:187],v[124:127]\nds_read_b128 a[204:207],v14 offset:3072\nv_mov_b32 v61,v60\n" \
"v_exp_f32 v94,v94\nM v[124:127],v[220:223],a[188:191],v[124:127]\nv_permlane16_swap_b32 v60,v61\nv_min_f32 v40,-v60,-v61\nM v[128:131],a[96:99],v[228:231],v[128:131]\nv_add_f32 v112,v112,v40\nv_add_f32 v113,v113,v40\nv_add_f32 v114,v114,v40\n" \
"M v[128:131],a[48:51],v[236:239],v[128:131]\nv_exp_f32 v112,v112\nv_add_f32 v115,v115,v40\nM v[132:135],a[52:55],v[236:239],v[132:135]\ns_waitcnt vmcnt(8) lgkmcnt(15)\nv_exp_f32 v113,v113\nv_add_f32 v116,v116,v40\nM v[132:135],a[100:103],v[228:231],v[132:135]\n" \
"s_barrier\nv_add_f32 v117,v117,v40\nv_exp_f32 v114,v114\nM v[136:139],a[104:107],v[228:231],v[136:139]\nbuffer_load_dwordx2 v[58:59],v57,s[36:39],0 offen sc1\nv_add_f32 v118,v118,v40\nM v[136:139],a[56:59],v[236:239],v[136:139]\nv_exp_f32 v115,v115\n" \
"v_exp_f32 v95,v95\nM v[140:143],a[60:63],v[236:239],v[140:143]\nds_read_b128 a[208:211],v14 offset:4096\nv_exp_f32 v116,v116\nM v[140:143],a[108:111],v[228:231],v[140:143]\nds_read_b128 a[212:215],v14 offset:5120\nv_exp_f32 v117,v117\nM v[144:147],a[112:115],v[228:231],v[144:147]\n" \
"ds_read_b128 a[216:219],v14 offset:6144\nv_add_f32 v119,v119,v40\nv_exp_f32 v118,v118\nM v[144:147],a[64:67],v[236:239],v[144:147]\nds_read_b128 a[220:223],v14 offset:7168\nC v88,v88,v89\nv_exp_f32 v119,v119\nM v[148:151],a[68:71],v[236:239],v[148:151]\n" \
"ds_read_b128 a[144:147],v20 offset:0\nC v89,v90,v91\nv_exp_f32 v120,v120\nM v[148:151],a[116:119],v[228:231],v[148:151]\nv_exp_f32 v121,v121\nv_xor_b32 v13,0x10000,v13\nM v[152:155],a[120:123],v[228:231],v[152:155]\nds_read_b128 a[148:151],v20 offset:1024\n" \
"ds_read_b128 a[152:155],v20 offset:2048\nv_exp_f32 v122,v122\nM v[152:155],a[72:75],v[236:239],v[152:155]\nC v128,v128,v129\nC v90,v92,v93\nC v91,v94,v95\nM v[156:159],a[76:79],v[236:239],v[156:159]\nds_read_b128 a[0:3],v13 offset:32768\n" \
"ds_read_b128 a[156:159],v20 offset:3072\nC v136,v136,v137\nM v[156:159],a[124:127],v[228:231],v[156:159]\ns_waitcnt lgkmcnt(5)\nds_read_b128 a[4:7],v13 offset:34816\nC v129,v130,v131\nC v144,v144,v145\nM v[224:227],a[192:195],v[88:91],0\n" \
"ds_read_b128 a[8:11],v13 offset:36864\nC v137,v138,v139\nv_exp_f32 v123,v123\nM v[228:231],a[196:199],v[88:91],0\nds_read_b128 a[12:15],v13 offset:38912\nv_add_u32 v20,s65,v20\nM v[232:235],a[200:203],v[88:91],0\nds_read_b128 a[16:19],v13 offset:33792\n" \
"C v130,v132,v133\nC v131,v134,v135\nM v[236:239],a[204:207],v[88:91],0\nds_read_b128 a[20:23],v13 offset:35840\nC v145,v146,v147\nM v[240:243],a[208:211],v[88:91],0\nds_read_b128 a[24:27],v13 offset:37888\nC v138,v140,v141\n" \
"M v[244:247],a[212:215],v[88:91],0\nds_read_b128 a[28:31],v13 offset:39936\nC v152,v152,v153\nM v[248:251],a[216:219],v[88:91],0\ns_waitcnt lgkmcnt(5)\nv_exp_f32 v124,v124\nM v[252:255],a[220:223],v[88:91],0\nds_write_b128 v20,v[128:131] offset:0\n" \
"s_waitcnt vmcnt(0)\nv_cmp_gt_u64 s[98:99],s[14:15],v[58:59]\nv_cmp_le_u64 s[18:19],s[16:17],v[58:59]\ns_or_b64 s[98:99],s[98:99],s[18:19]\ns_cbranch_scc1 LPK_%=\nLPKR_%=:\nv_and_b32 v58,0xf0,v4\nv_lshl_add_u32 v57,v58,3,s62\n" \
"s_mov_b32 s62,0x11111111\ns_mov_b32 s63,s62\nv_cndmask_b32 v62,0,v63,s[62:63]\nv_accvgpr_write_b32 a224,v62\nv_accvgpr_write_b32 a225,v62\nv_accvgpr_write_b32 a226,v62\nv_accvgpr_write_b32 a227,v62\ns_mov_b32 s62,0x22222222\n" \
"s_mov_b32 s63,s62\nv_cndmask_b32 v62,0,v63,s[62:63]\nv_accvgpr_write_b32 a228,v62\nv_accvgpr_write_b32 a229,v62\nv_accvgpr_write_b32 a230,v62\nv_accvgpr_write_b32 a231,v62\ns_mov_b32 s62,0x44444444\ns_mov_b32 s63,s62\n" \
"v_cndmask_b32 v62,0,v63,s[62:63]\nv_accvgpr_write_b32 a232,v62\nv_accvgpr_write_b32 a233,v62\nv_accvgpr_write_b32 a234,v62\nv_accvgpr_write_b32 a235,v62\ns_mov_b32 s62,0x88888888\ns_mov_b32 s63,s62\nv_cndmask_b32 v62,0,v63,s[62:63]\n" \
"v_accvgpr_write_b32 a236,v62\nv_accvgpr_write_b32 a237,v62\nv_accvgpr_write_b32 a238,v62\nv_accvgpr_write_b32 a239,v62\ns_nop 1\nM v[16:19],a[236:239],v[88:91],v[24:27]\ns_waitcnt vmcnt(5) lgkmcnt(13)\nC v112,v112,v113\n" \
"C v139,v142,v143\nM v[72:75],a[0:3],a[144:147],v[48:51]\ns_barrier\nC v146,v148,v149\nM v[72:75],a[4:7],a[148:151],v[72:75]\nC v147,v150,v151\nv_min_f32 v41,-v60,-v61\nM v[72:75],a[8:11],a[152:155],v[72:75]\n" \
"s_waitcnt lgkmcnt(1)\nC v153,v154,v155\nv_min_f32 v42,-v60,-v61\nM v[72:75],a[12:15],a[156:159],v[72:75]\nv_min_f32 v43,-v60,-v61\nv_min_f32 v28,-v60,-v61\nM v[76:79],a[16:19],a[144:147],v[52:55]\nds_write_b128 v20,v[136:139] offset:1024\n" \
"M v[76:79],a[20:23],a[148:151],v[76:79]\nbuffer_load_dwordx4 v22,s[40:43],s45 offen offset:0 sc1 lds\nM v[76:79],a[24:27],a[152:155],v[76:79]\nv_exp_f32 v125,v125\nM v[76:79],a[28:31],a[156:159],v[76:79]\nds_read_b128 a[96:99],v14 offset:16384\nC v113,v114,v115\nC v154,v156,v157\n" \
"M v[80:83],a[0:3],a[160:163],v[40:43]\nC v155,v158,v159\nv_max3_f32 v60,v72,v73,v74\nM v[80:83],a[4:7],a[164:167],v[80:83]\nds_write_b128 v20,v[144:147] offset:2048\nM v[80:83],a[8:11],a[168:171],v[80:83]\nv_exp_f32 v126,v126\nM v[80:83],a[12:15],a[172:175],v[80:83]\n" \
"C v114,v116,v117\nC v120,v120,v121\nM v[88:91],a[0:3],a[176:179],v[44:47]\nds_read_b128 a[100:103],v14 offset:17408\nC v115,v118,v119\nv_max3_f32 v61,v75,v76,v77\nM v[88:91],a[4:7],a[180:183],v[88:91]\nv_max3_f32 v60,v60,v78,v79\n" \
"v_max3_f32 v60,v60,v61,s76\nv_mov_b32 v61,v60\nM v[88:91],a[8:11],a[184:187],v[88:91]\nds_write_b128 v20,v[152:155] offset:3072\nC v121,v122,v123\nM v[24:27],a[232:235],v[112:115],v[16:19]\nv_exp_f32 v127,v127\nM v[88:91],a[12:15],a[188:191],v[88:91]\n" \
"C v122,v124,v125\nC v123,v126,v127\nM v[84:87],a[16:19],a[160:163],v[40:43]\nv_permlane32_swap_b32 v60,v61\nv_max_f32 v60,v60,v61\nM v[24:27],a[236:239],v[120:123],v[24:27]\nds_read_b128 a[104:107],v14 offset:18432\nds_read_b128 a[108:111],v14 offset:19456\n" \
"v_mov_b32 v61,v60\nM v[84:87],a[20:23],a[164:167],v[84:87]\nbuffer_load_dwordx4 v22,s[40:43],s45 offen offset:1024 sc1 lds\nM v[84:87],a[24:27],a[168:171],v[84:87]\nv_permlane16_swap_b32 v60,v61\nv_min_f32 v36,-v60,-v61\nM v[84:87],a[28:31],a[172:175],v[84:87]\nv_max3_f32 v15,v24,v25,v26\n" \
"v_max_f32 v15,v15,v27\nv_cmp_nge_f32 vcc,0x53800000,v15\nM v[92:95],a[16:19],a[176:179],v[44:47]\nv_add_f32 v72,v72,v36\nv_add_f32 v73,v73,v36\nv_add_f32 v74,v74,v36\nM v[92:95],a[20:23],a[180:183],v[92:95]\nbuffer_load_dwordx4 v22,s[40:43],s45 offen offset:2048 sc1 lds\n" \
"ds_read_b128 a[128:131],v20 offset:0\nds_read_b128 a[132:135],v20 offset:1024\nds_read_b128 a[136:139],v20 offset:2048\nds_read_b128 a[140:143],v20 offset:3072\nds_read_b128 a[112:115],v14 offset:20480\ns_cbranch_vccnz Lcs1_%=\nLcr2_%=:\nM v[92:95],a[24:27],a[184:187],v[92:95]\n" \
"v_exp_f32 v72,v72\nv_add_f32 v75,v75,v36\nM v[92:95],a[28:31],a[188:191],v[92:95]\ns_waitcnt lgkmcnt(5)\nds_read_b128 a[116:119],v14 offset:21504\nv_add_f32 v76,v76,v36\nv_exp_f32 v73,v73\nM v[192:195],a[96:99],v[112:115],0\n" \
"ds_read_b128 a[120:123],v14 offset:22528\nds_read_b128 a[124:127],v14 offset:23552\nv_exp_f32 v74,v74\nM v[224:227],a[96:99],v[120:123],v[224:227]\nbuffer_load_dwordx4 v22,s[40:43],s45 offen offset:3072 sc1 lds\nv_exp_f32 v75,v75\nM v[228:231],a[100:103],v[120:123],v[228:231]\ns_add_u32 s45,s45,s49\n" \
"s_add_u32 m0,s22,49152\nv_exp_f32 v76,v76\nv_add_f32 v77,v77,v36\nM v[196:199],a[100:103],v[112:115],0\ns_waitcnt lgkmcnt(4)\nv_exp_f32 v77,v77\nv_add_f32 v78,v78,v36\nM v[200:203],a[104:107],v[112:115],0\n" \
"ds_read_b128 a[48:51],v13 offset:49152\nv_exp_f32 v78,v78\nM v[232:235],a[104:107],v[120:123],v[232:235]\nbuffer_load_dwordx4 v22,s[40:43],s45 offen offset:0 sc1 lds\nv_add_f32 v79,v79,v36\nM v[236:239],a[108:111],v[120:123],v[236:239]\ns_waitcnt vmcnt(6) lgkmcnt(1)\nds_read_b128 a[52:55],v13 offset:51200\n" \
"v_min_f32 v37,-v60,-v61\nv_exp_f32 v79,v79\nM v[204:207],a[108:111],v[112:115],0\ns_barrier\nds_read_b128 a[56:59],v13 offset:53248\nv_exp_f32 v80,v80\nM v[208:211],a[112:115],v[112:115],0\nds_read_b128 a[60:63],v13 offset:55296\n" \
"v_min_f32 v38,-v60,-v61\nv_exp_f32 v81,v81\nM v[240:243],a[112:115],v[120:123],v[240:243]\nbuffer_load_dwordx4 v22,s[40:43],s45 offen offset:1024 sc1 lds\nds_read_b128 a[64:67],v13 offset:50176\nM v[244:247],a[116:119],v[120:123],v[244:247]\nds_read_b128 a[68:71],v13 offset:52224\nv_min_f32 v39,-v60,-v61\n" \
"v_exp_f32 v82,v82\nM v[212:215],a[116:119],v[112:115],0\nds_read_b128 a[72:75],v13 offset:54272\nv_min_f32 v31,-v60,-v61\nv_exp_f32 v83,v83\nM v[216:219],a[120:123],v[112:115],0\nds_read_b128 a[76:79],v13 offset:56320\nC v72,v72,v73\n" \
"v_exp_f32 v84,v84\nM v[248:251],a[120:123],v[120:123],v[248:251]\nbuffer_load_dwordx4 v22,s[40:43],s45 offen offset:2048 sc1 lds\nM v[252:255],a[124:127],v[120:123],v[252:255]\nv_exp_f32 v85,v85\nM v[220:223],a[124:127],v[112:115],0\ns_waitcnt lgkmcnt(3)\nv_exp_f32 v86,v86\n" \
"M v[96:99],a[48:51],a[128:131],v[48:51]\nv_exp_f32 v87,v87\nM v[96:99],a[52:55],a[132:135],v[96:99]\nbuffer_load_dwordx4 v22,s[40:43],s45 offen offset:3072 sc1 lds\nM v[96:99],a[56:59],a[136:139],v[96:99]\ns_add_u32 m0,s22,65536\ns_add_u32 s45,s45,s49\nv_exp_f32 v88,v88\n" \
"M v[96:99],a[60:63],a[140:143],v[96:99]\nv_exp_f32 v89,v89\nM v[100:103],a[64:67],a[128:131],v[52:55]\ns_waitcnt lgkmcnt(0)\nv_exp_f32 v90,v90\nM v[100:103],a[68:71],a[132:135],v[100:103]\nbuffer_load_dwordx4 v22,s[40:43],s48 offen offset:0 sc1 lds\nM v[100:103],a[72:75],a[136:139],v[100:103]\n" \
"v_exp_f32 v91,v91\nM v[100:103],a[76:79],a[140:143],v[100:103]\nv_exp_f32 v92,v92\nM v[104:107],a[48:51],a[144:147],v[36:39]\nv_max3_f32 v60,v96,v97,v98\nC v73,v74,v75\nM v[104:107],a[52:55],a[148:151],v[104:107]\nbuffer_load_dwordx4 v22,s[40:43],s48 offen offset:1024 sc1 lds\n" \
"M v[104:107],a[56:59],a[152:155],v[104:107]\nv_max3_f32 v61,v99,v100,v101\nv_max3_f32 v60,v60,v102,v103\nM v[104:107],a[60:63],a[156:159],v[104:107]\nv_max3_f32 v60,v60,v61,s76\nv_mov_b32 v61,v60\nM v[112:115],a[48:51],a[160:163],v[40:43]\nv_exp_f32 v93,v93\n" \
"M v[112:115],a[52:55],a[164:167],v[112:115]\nbuffer_load_dwordx4 v22,s[40:43],s48 offen offset:2048 sc1 lds\nM v[112:115],a[56:59],a[168:171],v[112:115]\nv_permlane32_swap_b32 v60,v61\nM v[112:115],a[60:63],a[172:175],v[112:115]\nC v80,v80,v81\nC v88,v88,v89\nM v[120:123],a[48:51],a[176:179],v[44:47]\n" \
"v_max_f32 v60,v60,v61\nC v74,v76,v77\nM v[120:123],a[52:55],a[180:183],v[120:123]\nbuffer_load_dwordx4 v22,s[40:43],s48 offen offset:3072 sc1 lds\nv_exp_f32 v94,v94\nM v[120:123],a[56:59],a[184:187],v[120:123]\ns_add_u32 m0,s22,98304\nC v75,v78,v79\n" \
"C v81,v82,v83\nM v[120:123],a[60:63],a[188:191],v[120:123]\ns_add_u32 s48,s48,s49\nv_mov_b32 v61,v60\nC v89,v90,v91\nM v[108:111],a[64:67],a[144:147],v[36:39]\nv_xor_b32 v14,0x10000,v14\nC v82,v84,v85\n" \
"M v[16:19],a[228:231],v[72:75],v[24:27]\nbuffer_load_dwordx4 v22,s[40:43],s45 offen offset:0 sc1 lds\nv_exp_f32 v95,v95\nM v[108:111],a[68:71],a[148:151],v[108:111]\nds_read_b128 a[0:3],v14 offset:0\nC v83,v86,v87\nM v[108:111],a[72:75],a[152:155],v[108:111]\nv_permlane16_swap_b32 v60,v61\n" \
"M v[16:19],a[232:235],v[80:83],v[16:19]\nds_read_b128 a[4:7],v14 offset:1024\nC v90,v92,v93\nM v[108:111],a[76:79],a[156:159],v[108:111]\nbuffer_load_dwordx4 v22,s[40:43],s45 offen offset:1024 sc1 lds\nC v91,v94,v95\nM v[116:119],a[64:67],a[160:163],v[40:43]\nds_read_b128 a[8:11],v14 offset:2048\n" \
"v_min_f32 v32,-v60,-v61\nM v[16:19],a[236:239],v[88:91],v[16:19]\nv_xor_b32 v13,s54,v13\nv_min_f32 v33,-v60,-v61\nM v[116:119],a[68:71],a[164:167],v[116:119]\nds_read_b128 a[12:15],v14 offset:3072\nv_min_f32 v34,-v60,-v61\nM v[116:119],a[72:75],a[168:171],v[116:119]\n" \
"buffer_load_dwordx4 v22,s[40:43],s45 offen offset:2048 sc1 lds\nv_min_f32 v35,-v60,-v61\nM v[116:119],a[76:79],a[172:175],v[116:119]\nv_max3_f32 v15,v16,v17,v18\nv_max_f32 v15,v15,v19\nv_cmp_nge_f32 vcc,0x53800000,v15\nM v[124:127],a[64:67],a[176:179],v[44:47]\ns_waitcnt lgkmcnt(2)\n" \
"v_min_f32 v30,-v60,-v61\nv_add_f32 v96,v96,v32\nv_add_f32 v97,v97,v32\nM v[124:127],a[68:71],a[180:183],v[124:127]\ns_cbranch_vccnz Lcs3_%=\nLcr4_%=:\nv_add_f32 v98,v98,v32\nv_add_f32 v99,v99,v32\n" \
"v_add_f32 v100,v100,v32\nv_add_f32 v101,v101,v32\nv_add_f32 v102,v102,v32\nv_add_f32 v103,v103,v32\nM v[124:127],a[72:75],a[184:187],v[124:127]\ns_cmp_eq_u32 s52,s55\nbuffer_load_dwordx4 v22,s[40:43],s45 offen offset:3072 sc1 lds\nds_write_b128 v23,v[28:31] offset:0\n" \
"ds_read_b128 a[16:19],v14 offset:4096\nds_read_b128 a[20:23],v14 offset:5120\nds_read_b128 a[24:27],v14 offset:6144\nds_read_b128 a[28:31],v14 offset:7168\nM v[124:127],a[76:79],a[188:191],v[124:127]\ns_cbranch_scc1 LH1_%=\nLH1R0_%=:\ns_add_u32 m0,s22,16384\n" \
"v_exp_f32 v96,v96\nM v[160:163],a[0:3],v[72:75],0\ns_add_u32 s45,s45,s49\nv_exp_f32 v97,v97\nM v[192:195],a[0:3],v[80:83],v[192:195]\nv_exp_f32 v98,v98\nM v[224:227],a[0:3],v[88:91],v[224:227]\nbuffer_load_dwordx4 v22,s[40:43],s61 offen offset:0 sc1 lds\n" \
"M v[228:231],a[4:7],v[88:91],v[228:231]\nv_exp_f32 v99,v99\nM v[196:199],a[4:7],v[80:83],v[196:199]\ns_waitcnt vmcnt(13) lgkmcnt(0)\nv_exp_f32 v100,v100\nM v[164:167],a[4:7],v[72:75],0\ns_barrier\nv_xor_b32 v59,0x2000,v23\n" \
"M v[168:171],a[8:11],v[72:75],0\nbuffer_load_dwordx4 v22,s[40:43],s61 offen offset:1024 sc1 lds\nM v[200:203],a[8:11],v[80:83],v[200:203]\nds_read_b128 v[28:31],v59 offset:0\nv_xor_b32 v13,0x10000,v13\nv_exp_f32 v101,v101\nM v[232:235],a[8:11],v[88:91],v[232:235]\nv_exp_f32 v102,v102\n" \
"M v[236:239],a[12:15],v[88:91],v[236:239]\nv_exp_f32 v103,v103\nM v[204:207],a[12:15],v[80:83],v[204:207]\nbuffer_load_dwordx4 v22,s[40:43],s61 offen offset:2048 sc1 lds\nv_exp_f32 v104,v104\nM v[172:175],a[12:15],v[72:75],0\nv_exp_f32 v105,v105\nM v[176:179],a[16:19],v[72:75],0\n" \
"v_exp_f32 v106,v106\nM v[208:211],a[16:19],v[80:83],v[208:211]\ns_waitcnt lgkmcnt(0)\nds_read_b128 a[192:195],v13 offset:32768\nds_read_b128 a[196:199],v13 offset:34816\nv_exp_f32 v107,v107\nM v[240:243],a[16:19],v[88:91],v[240:243]\nbuffer_load_dwordx4 v22,s[40:43],s61 offen offset:3072 sc1 lds\n" \
"v_exp_f32 v108,v108\nM v[244:247],a[20:23],v[88:91],v[244:247]\ns_add_u32 m0,s22,81920\nv_exp_f32 v109,v109\nM v[212:215],a[20:23],v[80:83],v[212:215]\nds_read_b128 a[200:203],v13 offset:36864\nC v96,v96,v97\nv_exp_f32 v110,v110\n" \
"M v[180:183],a[20:23],v[72:75],0\nds_read_b128 a[204:207],v13 offset:38912\nC v97,v98,v99\nv_exp_f32 v111,v111\nM v[184:187],a[24:27],v[72:75],0\nv_exp_f32 v112,v112\nM v[216:219],a[24:27],v[80:83],v[216:219]\nv_exp_f32 v113,v113\n" \
"M v[248:251],a[24:27],v[88:91],v[248:251]\nv_exp_f32 v114,v114\nM v[252:255],a[28:31],v[88:91],v[252:255]\nbuffer_load_dwordx4 v22,s[40:43],s48 offen offset:0 sc1 lds\nM v[220:223],a[28:31],v[80:83],v[220:223]\nv_cndmask_b32 v32,v32,v28,s[66:67]\nv_cndmask_b32 v33,v33,v28,s[66:67]\nv_cndmask_b32 v34,v34,v28,s[66:67]\n" \
"v_cndmask_b32 v35,v35,v28,s[66:67]\nM v[188:191],a[28:31],v[72:75],0\ns_waitcnt lgkmcnt(0)\nds_read_b128 a[208:211],v13 offset:33792\nds_read_b128 a[212:215],v13 offset:35840\nM v[64:67],a[192:195],a[128:131],v[32:35]\nC v98,v100,v101\nC v99,v102,v103\n" \
"v_exp_f32 v115,v115\nM v[64:67],a[196:199],a[132:135],v[64:67]\nds_read_b128 a[216:219],v13 offset:37888\nC v104,v104,v105\nM v[24:27],a[224:227],v[96:99],v[16:19]\nds_read_b128 a[220:223],v13 offset:39936\nC v105,v106,v107\nM v[64:67],a[200:203],a[136:139],v[64:67]\n" \
"v_cndmask_b32 v36,v36,v29,s[66:67]\nv_cndmask_b32 v37,v37,v29,s[66:67]\nv_cndmask_b32 v38,v38,v29,s[66:67]\nv_cndmask_b32 v39,v39,v29,s[66:67]\nM v[64:67],a[204:207],a[140:143],v[64:67]\nv_exp_f32 v116,v116\nM v[72:75],a[192:195],a[144:147],v[36:39]\nC v106,v108,v109\n" \
"C v107,v110,v111\nM v[72:75],a[196:199],a[148:151],v[72:75]\nbuffer_load_dwordx4 v22,s[40:43],s48 offen offset:1024 sc1 lds\nM v[24:27],a[228:231],v[104:107],v[24:27]\nv_exp_f32 v117,v117\nM v[72:75],a[200:203],a[152:155],v[72:75]\nv_cndmask_b32 v40,v40,v30,s[66:67]\nv_cndmask_b32 v41,v41,v30,s[66:67]\n" \
"v_cndmask_b32 v42,v42,v30,s[66:67]\nv_cndmask_b32 v43,v43,v30,s[66:67]\nM v[72:75],a[204:207],a[156:159],v[72:75]\ns_waitcnt vmcnt(2)\nv_exp_f32 v118,v118\nM v[80:83],a[192:195],a[160:163],v[40:43]\ns_barrier\nv_cndmask_b32 v44,v44,v31,s[66:67]\n" \
"M v[80:83],a[196:199],a[164:167],v[80:83]\nbuffer_load_dwordx4 v22,s[40:43],s48 offen offset:2048 sc1 lds\nM v[80:83],a[200:203],a[168:171],v[80:83]\nv_cndmask_b32 v45,v45,v31,s[66:67]\nv_cndmask_b32 v46,v46,v31,s[66:67]\nv_cndmask_b32 v47,v47,v31,s[66:67]\nM v[80:83],a[204:207],a[172:175],v[80:83]\nv_exp_f32 v119,v119\n" \
"M v[88:91],a[192:195],a[176:179],v[44:47]\nC v112,v112,v113\nC v113,v114,v115\nM v[88:91],a[196:199],a[180:183],v[88:91]\nbuffer_load_dwordx4 v22,s[40:43],s48 offen offset:3072 sc1 lds\nM v[88:91],a[200:203],a[184:187],v[88:91]\ns_add_u32 m0,s22,114688\nC v114,v116,v117\n" \
"C v115,v118,v119\nM v[88:91],a[204:207],a[188:191],v[88:91]\ns_add_u32 s48,s48,s49\nv_exp_f32 v120,v120\nM v[24:27],a[232:235],v[112:115],v[24:27]\ns_waitcnt lgkmcnt(0)\nv_exp_f32 v121,v121\nM v[68:71],a[208:211],a[128:131],v[32:35]\n" \
"v_exp_f32 v122,v122\nM v[68:71],a[212:215],a[132:135],v[68:71]\nv_exp_f32 v123,v123\nM v[68:71],a[216:219],a[136:139],v[68:71]\nv_exp_f32 v124,v124\nM v[68:71],a[220:223],a[140:143],v[68:71]\nv_exp_f32 v125,v125\nM v[76:79],a[208:211],a[144:147],v[36:39]\n" \
"v_exp_f32 v126,v126\nM v[76:79],a[212:215],a[148:151],v[76:79]\nv_exp_f32 v127,v127\nM v[76:79],a[216:219],a[152:155],v[76:79]\nds_read_b128 a[192:195],v14 offset:16384\nC v120,v120,v121\nC v121,v122,v123\nC v122,v124,v125\n" \
"C v123,v126,v127\nM v[76:79],a[220:223],a[156:159],v[76:79]\nbuffer_load_dwordx4 v22,s[40:43],s45 offen offset:0 sc1 lds\nM v[24:27],a[236:239],v[120:123],v[24:27]\nds_read_b128 a[196:199],v14 offset:17408\nv_lshrrev_b32 v58,8,v4\nM v[84:87],a[208:211],a[160:163],v[40:43]\nds_read_b128 a[200:203],v14 offset:18432\n" \
"v_lshl_add_u32 v57,v58,27,v57\nM v[84:87],a[212:215],a[164:167],v[84:87]\nds_read_b128 a[204:207],v14 offset:19456\nv_max3_f32 v15,v24,v25,v26\nM v[84:87],a[216:219],a[168:171],v[84:87]\nbuffer_load_dwordx4 v22,s[40:43],s45 offen offset:1024 sc1 lds\nM v[84:87],a[220:223],a[172:175],v[84:87]\nv_max_f32 v15,v15,v27\n" \
"v_cmp_nge_f32 vcc,0x53800000,v15\nM v[92:95],a[208:211],a[176:179],v[44:47]\nds_read_b128 a[208:211],v14 offset:20480\nM v[92:95],a[212:215],a[180:183],v[92:95]\ns_cbranch_vccnz Lcs5_%=\nLcr6_%=:\nds_read_b128 a[212:215],v14 offset:21504\nM v[92:95],a[216:219],a[184:187],v[92:95]\n" \
"buffer_load_dwordx4 v22,s[40:43],s45 offen offset:2048 sc1 lds\nM v[92:95],a[220:223],a[188:191],v[92:95]\ns_waitcnt lgkmcnt(2)\nds_read_b128 a[216:219],v14 offset:22528\nM v[128:131],a[192:195],v[96:99],0\nds_read_b128 a[220:223],v14 offset:23552\nv_xor_b32 v14,s54,v14\nM v[160:163],a[192:195],v[104:107],v[160:163]\n" \
"v_exp_f32 v64,v64\nM v[192:195],a[192:195],v[112:115],v[192:195]\nbuffer_load_dwordx4 v22,s[40:43],s45 offen offset:3072 sc1 lds\nM v[224:227],a[192:195],v[120:123],v[224:227]\ns_add_u32 m0,s22,0\nv_exp_f32 v65,v65\nM v[228:231],a[196:199],v[120:123],v[228:231]\ns_add_u32 s45,s45,s49\n" \
"v_exp_f32 v66,v66\nM v[196:199],a[196:199],v[112:115],v[196:199]\nv_exp_f32 v67,v67\nM v[164:167],a[196:199],v[104:107],v[164:167]\ns_waitcnt vmcnt(16) lgkmcnt(0)\nv_exp_f32 v68,v68\nM v[132:135],a[196:199],v[96:99],0\ns_barrier\n" \
"ds_read_b128 a[192:195],v13 offset:49152\nM v[136:139],a[200:203],v[96:99],0\nbuffer_load_dwordx4 v22,s[40:43],s48 offen offset:0 sc1 lds\nM v[168:171],a[200:203],v[104:107],v[168:171]\nv_exp_f32 v69,v69\nM v[200:203],a[200:203],v[112:115],v[200:203]\nv_exp_f32 v70,v70\nM v[232:235],a[200:203],v[120:123],v[232:235]\n" \
"v_exp_f32 v71,v71\nM v[236:239],a[204:207],v[120:123],v[236:239]\nbuffer_load_dwordx4 v22,s[40:43],s48 offen offset:1024 sc1 lds\nM v[204:207],a[204:207],v[112:115],v[204:207]\nds_read_b128 a[196:199],v13 offset:51200\nC v64,v64,v65\nM v[172:175],a[204:207],v[104:107],v[172:175]\nv_exp_f32 v72,v72\n" \
"M v[140:143],a[204:207],v[96:99],0\nLG15_%=:\ns_cbranch_execz LHR1_%=\nds_read_b128 a[200:203],v13 offset:53248\nds_read_b128 a[204:207],v13 offset:55296\nM v[144:147],a[208:211],v[96:99],0\nbuffer_load_dwordx4 v22,s[40:43],s48 offen offset:2048 sc1 lds\nM v[176:179],a[208:211],v[104:107],v[176:179]\n" \
"v_exp_f32 v73,v73\nM v[208:211],a[208:211],v[112:115],v[208:211]\nv_exp_f32 v74,v74\nM v[240:243],a[208:211],v[120:123],v[240:243]\nv_exp_f32 v75,v75\nM v[244:247],a[212:215],v[120:123],v[244:247]\nbuffer_load_dwordx4 v22,s[40:43],s48 offen offset:3072 sc1 lds\nM v[212:215],a[212:215],v[112:115],v[212:215]\n" \
"s_add_u32 m0,s53,32768\nv_exp_f32 v76,v76\nM v[180:183],a[212:215],v[104:107],v[180:183]\ns_add_u32 s52,s52,1\nv_exp_f32 v77,v77\nM v[148:151],a[212:215],v[96:99],0\ns_add_u32 s48,s48,s49\nv_exp_f32 v78,v78\n" \
"M v[152:155],a[216:219],v[96:99],0\nbuffer_load_dwordx4 v22,s[40:43],s45 offen offset:0 sc1 lds\nM v[184:187],a[216:219],v[104:107],v[184:187]\nv_exp_f32 v79,v79\nM v[216:219],a[216:219],v[112:115],v[216:219]\nv_exp_f32 v80,v80\nM v[248:251],a[216:219],v[120:123],v[248:251]\nC v65,v66,v67\n" \
"C v66,v68,v69\nM v[252:255],a[220:223],v[120:123],v[252:255]\nbuffer_load_dwordx4 v22,s[40:43],s45 offen offset:1024 sc1 lds\nM v[220:223],a[220:223],v[112:115],v[220:223]\ns_waitcnt lgkmcnt(0)\nv_exp_f32 v81,v81\nM v[188:191],a[220:223],v[104:107],v[188:191]\ns_cmp_eq_u32 s52,s56\n" \
"C v67,v70,v71\nC v72,v72,v73\nM v[156:159],a[220:223],v[96:99],0\ns_cbranch_scc1 LEV_%=\nLEVR0_%=:\nds_read_b128 a[208:211],v13 offset:50176\nds_read_b128 a[212:215],v13 offset:52224\nM v[96:99],a[192:195],a[128:131],v[32:35]\n" \
"buffer_load_dwordx4 v22,s[40:43],s45 offen offset:2048 sc1 lds\nM v[96:99],a[196:199],a[132:135],v[96:99]\nv_exp_f32 v82,v82\nM v[16:19],a[224:227],v[64:67],v[24:27]\nds_read_b128 a[216:219],v13 offset:54272\nC v73,v74,v75\nM v[96:99],a[200:203],a[136:139],v[96:99]\nds_read_b128 a[220:223],v13 offset:56320\n" \
"C v74,v76,v77\nM v[96:99],a[204:207],a[140:143],v[96:99]\nv_exp_f32 v83,v83\nM v[104:107],a[192:195],a[144:147],v[36:39]\nC v75,v78,v79\nC v80,v80,v81\nM v[104:107],a[196:199],a[148:151],v[104:107]\nv_exp_f32 v84,v84\n" \
"M v[16:19],a[228:231],v[72:75],v[16:19]\nbuffer_load_dwordx4 v22,s[40:43],s45 offen offset:3072 sc1 lds\nM v[104:107],a[200:203],a[152:155],v[104:107]\ns_add_u32 m0,s53,16384\nv_exp_f32 v85,v85\nM v[104:107],a[204:207],a[156:159],v[104:107]\ns_add_u32 s45,s45,s49\nv_exp_f32 v86,v86\n" \
"M v[112:115],a[192:195],a[160:163],v[40:43]\nv_exp_f32 v87,v87\nM v[112:115],a[196:199],a[164:167],v[112:115]\nbuffer_load_dwordx4 v22,s[40:43],s48 offen offset:0 sc1 lds\nM v[112:115],a[200:203],a[168:171],v[112:115]\nv_exp_f32 v88,v88\nM v[112:115],a[204:207],a[172:175],v[112:115]\nC v81,v82,v83\n" \
"C v82,v84,v85\nM v[120:123],a[192:195],a[176:179],v[44:47]\nv_exp_f32 v89,v89\nM v[120:123],a[196:199],a[180:183],v[120:123]\nbuffer_load_dwordx4 v22,s[40:43],s48 offen offset:1024 sc1 lds\nM v[120:123],a[200:203],a[184:187],v[120:123]\nC v83,v86,v87\nC v88,v88,v89\n" \
"M v[120:123],a[204:207],a[188:191],v[120:123]\nv_exp_f32 v90,v90\nM v[16:19],a[232:235],v[80:83],v[16:19]\ns_waitcnt lgkmcnt(0)\nv_exp_f32 v91,v91\nM v[100:103],a[208:211],a[128:131],v[32:35]\nbuffer_load_dwordx4 v22,s[40:43],s48 offen offset:2048 sc1 lds\nM v[100:103],a[212:215],a[132:135],v[100:103]\n" \
"v_exp_f32 v92,v92\nM v[100:103],a[216:219],a[136:139],v[100:103]\nv_exp_f32 v93,v93\nM v[100:103],a[220:223],a[140:143],v[100:103]\nv_exp_f32 v94,v94\nM v[108:111],a[208:211],a[144:147],v[36:39]\nbuffer_load_dwordx4 v22,s[40:43],s48 offen offset:3072 sc1 lds\nM v[108:111],a[212:215],a[148:151],v[108:111]\n" \
"s_add_u32 m0,s53,49152\nv_exp_f32 v95,v95\nv_xor_b32 v14,0x10000,v14\nM v[108:111],a[216:219],a[152:155],v[108:111]\ns_xor_b32 s53,s53,0x10000\nds_read_b128 a[192:195],v14 offset:0\nC v89,v90,v91\nC v90,v92,v93\n" \
"C v91,v94,v95\nM v[108:111],a[220:223],a[156:159],v[108:111]\ns_add_u32 s48,s48,s49\nds_read_b128 a[196:199],v14 offset:1024\nds_read_b128 a[200:203],v14 offset:2048\nM v[16:19],a[236:239],v[88:91],v[16:19]\nds_read_b128 a[204:207],v14 offset:3072\nM v[116:119],a[208:211],a[160:163],v[40:43]\n" \
"M v[116:119],a[212:215],a[164:167],v[116:119]\nv_max3_f32 v15,v16,v17,v18\nv_max_f32 v15,v15,v19\nM v[116:119],a[216:219],a[168:171],v[116:119]\nv_cmp_nge_f32 vcc,0x53800000,v15\nM v[116:119],a[220:223],a[172:175],v[116:119]\nM v[124:127],a[208:211],a[176:179],v[44:47]\ns_waitcnt lgkmcnt(1)\n" \
"ds_read_b128 a[208:211],v14 offset:4096\nM v[124:127],a[212:215],a[180:183],v[124:127]\ns_cbranch_vccnz Lcs7_%=\nLcr8_%=:\nds_read_b128 a[212:215],v14 offset:5120\nM v[124:127],a[216:219],a[184:187],v[124:127]\ns_cmp_eq_u32 s52,s55\nds_read_b128 a[216:219],v14 offset:6144\n" \
"M v[124:127],a[220:223],a[188:191],v[124:127]\ns_cbranch_scc1 LH1_%=\nLH1R1_%=:\nds_read_b128 a[220:223],v14 offset:7168\nv_xor_b32 v13,0x10000,v13\nM v[128:131],a[192:195],v[64:67],v[128:131]\nv_exp_f32 v96,v96\nM v[160:163],a[192:195],v[72:75],v[160:163]\n" \
"v_exp_f32 v97,v97\nM v[192:195],a[192:195],v[80:83],v[192:195]\nv_exp_f32 v98,v98\nM v[224:227],a[192:195],v[88:91],v[224:227]\nv_exp_f32 v99,v99\nM v[228:231],a[196:199],v[88:91],v[228:231]\nv_exp_f32 v100,v100\nM v[196:199],a[196:199],v[80:83],v[196:199]\n" \
"v_exp_f32 v101,v101\nM v[164:167],a[196:199],v[72:75],v[164:167]\ns_waitcnt vmcnt(16) lgkmcnt(0)\nv_exp_f32 v102,v102\nM v[132:135],a[196:199],v[64:67],v[132:135]\ns_barrier\nds_read_b128 a[192:195],v13 offset:32768\nM v[136:139],a[200:203],v[64:67],v[136:139]\n" \
"buffer_load_dwordx4 v22,s[40:43],s45 offen offset:0 sc1 lds\nM v[168:171],a[200:203],v[72:75],v[168:171]\nv_exp_f32 v103,v103\nM v[200:203],a[200:203],v[80:83],v[200:203]\nv_exp_f32 v104,v104\nM v[232:235],a[200:203],v[88:91],v[232:235]\nv_exp_f32 v105,v105\nM v[236:239],a[204:207],v[88:91],v[236:239]\n" \
"buffer_load_dwordx4 v22,s[40:43],s45 offen offset:1024 sc1 lds\nM v[204:207],a[204:207],v[80:83],v[204:207]\nds_read_b128 a[196:199],v13 offset:34816\nC v96,v96,v97\nM v[172:175],a[204:207],v[72:75],v[172:175]\nv_exp_f32 v106,v106\nM v[140:143],a[204:207],v[64:67],v[140:143]\nds_read_b128 a[200:203],v13 offset:36864\n" \
"ds_read_b128 a[204:207],v13 offset:38912\nM v[144:147],a[208:211],v[64:67],v[144:147]\nbuffer_load_dwordx4 v22,s[40:43],s45 offen offset:2048 sc1 lds\nM v[176:179],a[208:211],v[72:75],v[176:179]\nv_exp_f32 v107,v107\nM v[208:211],a[208:211],v[80:83],v[208:211]\nv_exp_f32 v108,v108\nM v[240:243],a[208:211],v[88:91],v[240:243]\n" \
"v_exp_f32 v109,v109\nM v[244:247],a[212:215],v[88:91],v[244:247]\nbuffer_load_dwordx4 v22,s[40:43],s45 offen offset:3072 sc1 lds\nM v[212:215],a[212:215],v[80:83],v[212:215]\ns_add_u32 m0,s53,0\nv_exp_f32 v110,v110\nM v[180:183],a[212:215],v[72:75],v[180:183]\ns_add_u32 s45,s45,s49\n" \
"v_exp_f32 v111,v111\nM v[148:151],a[212:215],v[64:67],v[148:151]\nv_exp_f32 v112,v112\nM v[152:155],a[216:219],v[64:67],v[152:155]\nbuffer_load_dwordx4 v22,s[40:43],s48 offen offset:0 sc1 lds\nM v[184:187],a[216:219],v[72:75],v[184:187]\nv_exp_f32 v113,v113\nM v[216:219],a[216:219],v[80:83],v[216:219]\n" \
"C v97,v98,v99\nC v98,v100,v101\nM v[248:251],a[216:219],v[88:91],v[248:251]\nv_exp_f32 v114,v114\nM v[252:255],a[220:223],v[88:91],v[252:255]\nbuffer_load_dwordx4 v22,s[40:43],s48 offen offset:1024 sc1 lds\nM v[220:223],a[220:223],v[80:83],v[220:223]\nC v99,v102,v103\n" \
"C v104,v104,v105\nM v[188:191],a[220:223],v[72:75],v[188:191]\nv_exp_f32 v115,v115\nM v[156:159],a[220:223],v[64:67],v[156:159]\ns_waitcnt lgkmcnt(0)\nv_exp_f32 v116,v116\nM v[64:67],a[192:195],a[128:131],v[32:35]\nbuffer_load_dwordx4 v22,s[40:43],s48 offen offset:2048 sc1 lds\n" \
"M v[64:67],a[196:199],a[132:135],v[64:67]\nds_read_b128 a[208:211],v13 offset:33792\nC v105,v106,v107\nM v[24:27],a[224:227],v[96:99],v[16:19]\nds_read_b128 a[212:215],v13 offset:35840\nC v106,v108,v109\nM v[64:67],a[200:203],a[136:139],v[64:67]\nds_read_b128 a[216:219],v13 offset:37888\n" \
"C v107,v110,v111\nM v[64:67],a[204:207],a[140:143],v[64:67]\nbuffer_load_dwordx4 v22,s[40:43],s48 offen offset:3072 sc1 lds\nM v[72:75],a[192:195],a[144:147],v[36:39]\ns_add_u32 m0,s53,32768\nds_read_b128 a[220:223],v13 offset:39936\nC v112,v112,v113\nM v[72:75],a[196:199],a[148:151],v[72:75]\n" \
"s_add_u32 s48,s48,s49\nv_exp_f32 v117,v117\nM v[24:27],a[228:231],v[104:107],v[24:27]\nv_exp_f32 v118,v118\nM v[72:75],a[200:203],a[152:155],v[72:75]\nv_exp_f32 v119,v119\nM v[72:75],a[204:207],a[156:159],v[72:75]\nv_exp_f32 v120,v120\n" \
"M v[80:83],a[192:195],a[160:163],v[40:43]\nv_exp_f32 v121,v121\nM v[80:83],a[196:199],a[164:167],v[80:83]\nC v113,v114,v115\nC v114,v116,v117\nM v[80:83],a[200:203],a[168:171],v[80:83]\nv_exp_f32 v122,v122\nM v[80:83],a[204:207],a[172:175],v[80:83]\n" \
"C v115,v118,v119\nC v120,v120,v121\nM v[88:91],a[192:195],a[176:179],v[44:47]\nv_exp_f32 v123,v123\nM v[88:91],a[196:199],a[180:183],v[88:91]\nv_exp_f32 v124,v124\nM v[88:91],a[200:203],a[184:187],v[88:91]\nv_exp_f32 v125,v125\n" \
"M v[88:91],a[204:207],a[188:191],v[88:91]\nds_read_b128 a[192:195],v14 offset:16384\nC v121,v122,v123\nM v[24:27],a[232:235],v[112:115],v[24:27]\ns_waitcnt lgkmcnt(1)\nv_exp_f32 v126,v126\nM v[68:71],a[208:211],a[128:131],v[32:35]\nds_read_b128 a[196:199],v14 offset:17408\n" \
"C v122,v124,v125\nM v[68:71],a[212:215],a[132:135],v[68:71]\nv_exp_f32 v127,v127\nM v[68:71],a[216:219],a[136:139],v[68:71]\nds_read_b128 a[200:203],v14 offset:18432\nC v123,v126,v127\nM v[68:71],a[220:223],a[140:143],v[68:71]\nds_read_b128 a[204:207],v14 offset:19456\n" \
"M v[76:79],a[208:211],a[144:147],v[36:39]\nM v[76:79],a[212:215],a[148:151],v[76:79]\nM v[76:79],a[216:219],a[152:155],v[76:79]\nM v[76:79],a[220:223],a[156:159],v[76:79]\nM v[24:27],a[236:239],v[120:123],v[24:27]\nM v[84:87],a[208:211],a[160:163],v[40:43]\ns_mov_b64 exec,0xff\nglobal_load_dword v0,v2,s[100:101]\n" \
"s_mov_b64 exec,-1\nM v[84:87],a[212:215],a[164:167],v[84:87]\nv_max3_f32 v15,v24,v25,v26\nv_max_f32 v15,v15,v27\nM v[84:87],a[216:219],a[168:171],v[84:87]\nv_cmp_nge_f32 vcc,0x53800000,v15\nM v[84:87],a[220:223],a[172:175],v[84:87]\nM v[92:95],a[208:211],a[176:179],v[44:47]\n" \
"ds_read_b128 a[208:211],v14 offset:20480\nM v[92:95],a[212:215],a[180:183],v[92:95]\ns_cbranch_vccnz Lcs9_%=\nLcr10_%=:\nds_read_b128 a[212:215],v14 offset:21504\nM v[92:95],a[216:219],a[184:187],v[92:95]\nds_read_b128 a[216:219],v14 offset:22528\nM v[92:95],a[220:223],a[188:191],v[92:95]\n" \
"s_waitcnt lgkmcnt(3)\nds_read_b128 a[220:223],v14 offset:23552\nM v[128:131],a[192:195],v[96:99],v[128:131]\nv_exp_f32 v64,v64\nM v[160:163],a[192:195],v[104:107],v[160:163]\nv_exp_f32 v65,v65\nM v[192:195],a[192:195],v[112:115],v[192:195]\nbuffer_load_dword v58,v57,s[84:87],s74 offen\n" \
"M v[224:227],a[192:195],v[120:123],v[224:227]\ns_add_u32 s74,s74,2048\nv_exp_f32 v66,v66\nM v[228:231],a[196:199],v[120:123],v[228:231]\nv_exp_f32 v67,v67\nM v[196:199],a[196:199],v[112:115],v[196:199]\nv_exp_f32 v68,v68\nM v[164:167],a[196:199],v[104:107],v[164:167]\n" \
"s_waitcnt vmcnt(18) lgkmcnt(0)\nv_exp_f32 v69,v69\nM v[132:135],a[196:199],v[96:99],v[132:135]\ns_barrier\nds_read_b128 a[192:195],v13 offset:49152\nM v[136:139],a[200:203],v[96:99],v[136:139]\nbuffer_load_dwordx4 v22,s[40:43],s45 offen offset:0 sc1 lds\nM v[168:171],a[200:203],v[104:107],v[168:171]\n" \
"v_exp_f32 v70,v70\nM v[200:203],a[200:203],v[112:115],v[200:203]\nv_exp_f32 v71,v71\nM v[232:235],a[200:203],v[120:123],v[232:235]\nv_exp_f32 v72,v72\nM v[236:239],a[204:207],v[120:123],v[236:239]\nbuffer_load_dwordx4 v22,s[40:43],s45 offen offset:1024 sc1 lds\nM v[204:207],a[204:207],v[112:115],v[204:207]\n" \
"ds_read_b128 a[196:199],v13 offset:51200\nC v64,v64,v65\nM v[172:175],a[204:207],v[104:107],v[172:175]\nv_exp_f32 v73,v73\nM v[140:143],a[204:207],v[96:99],v[140:143]\nds_read_b128 a[200:203],v13 offset:53248\nds_read_b128 a[204:207],v13 offset:55296\nM v[144:147],a[208:211],v[96:99],v[144:147]\n" \
"buffer_load_dwordx4 v22,s[40:43],s45 offen offset:2048 sc1 lds\nM v[176:179],a[208:211],v[104:107],v[176:179]\nv_exp_f32 v74,v74\nM v[208:211],a[208:211],v[112:115],v[208:211]\nv_exp_f32 v75,v75\nM v[240:243],a[208:211],v[120:123],v[240:243]\nv_exp_f32 v76,v76\nM v[244:247],a[212:215],v[120:123],v[244:247]\n" \
"buffer_load_dwordx4 v22,s[40:43],s45 offen offset:3072 sc1 lds\nM v[212:215],a[212:215],v[112:115],v[212:215]\ns_add_u32 m0,s53,16384\nv_exp_f32 v77,v77\nM v[180:183],a[212:215],v[104:107],v[180:183]\ns_add_u32 s52,s52,1\nv_exp_f32 v78,v78\nM v[148:151],a[212:215],v[96:99],v[148:151]\n" \
"s_add_u32 s45,s45,s49\nv_exp_f32 v79,v79\nM v[152:155],a[216:219],v[96:99],v[152:155]\nbuffer_load_dwordx4 v22,s[40:43],s48 offen offset:0 sc1 lds\nM v[184:187],a[216:219],v[104:107],v[184:187]\nv_exp_f32 v80,v80\nM v[216:219],a[216:219],v[112:115],v[216:219]\nC v65,v66,v67\n" \
"C v66,v68,v69\nM v[248:251],a[216:219],v[120:123],v[248:251]\nv_exp_f32 v81,v81\nM v[252:255],a[220:223],v[120:123],v[252:255]\nbuffer_load_dwordx4 v22,s[40:43],s48 offen offset:1024 sc1 lds\nM v[220:223],a[220:223],v[112:115],v[220:223]\ns_waitcnt lgkmcnt(0)\nC v67,v70,v71\n" \
"C v72,v72,v73\nM v[188:191],a[220:223],v[104:107],v[188:191]\ns_cmp_eq_u32 s52,s56\nv_exp_f32 v82,v82\nM v[156:159],a[220:223],v[96:99],v[156:159]\ns_cbranch_scc1 LEV_%=\nLEVR1_%=:\nds_read_b128 a[208:211],v13 offset:50176\n" \
"ds_read_b128 a[212:215],v13 offset:52224\nM v[96:99],a[192:195],a[128:131],v[32:35]\nbuffer_load_dwordx4 v22,s[40:43],s48 offen offset:2048 sc1 lds\nM v[96:99],a[196:199],a[132:135],v[96:99]\nv_exp_f32 v83,v83\nM v[16:19],a[224:227],v[64:67],v[24:27]\nds_read_b128 a[216:219],v13 offset:54272\nC v73,v74,v75\n" \
"M v[96:99],a[200:203],a[136:139],v[96:99]\nds_read_b128 a[220:223],v13 offset:56320\nC v74,v76,v77\nM v[96:99],a[204:207],a[140:143],v[96:99]\nbuffer_load_dwordx4 v22,s[40:43],s48 offen offset:3072 sc1 lds\nM v[104:107],a[192:195],a[144:147],v[36:39]\ns_add_u32 m0,s53,49152\nC v75,v78,v79\n" \
"C v80,v80,v81\nM v[104:107],a[196:199],a[148:151],v[104:107]\ns_xor_b32 s53,s53,0x10000\nv_exp_f32 v84,v84\nM v[16:19],a[228:231],v[72:75],v[16:19]\ns_add_u32 s48,s48,s49\nv_exp_f32 v85,v85\nM v[104:107],a[200:203],a[152:155],v[104:107]\n" \
"v_exp_f32 v86,v86\nM v[104:107],a[204:207],a[156:159],v[104:107]\nv_exp_f32 v87,v87\nM v[112:115],a[192:195],a[160:163],v[40:43]\nv_exp_f32 v88,v88\nM v[112:115],a[196:199],a[164:167],v[112:115]\nC v81,v82,v83\nC v82,v84,v85\n" \
"M v[112:115],a[200:203],a[168:171],v[112:115]\nv_exp_f32 v89,v89\nM v[112:115],a[204:207],a[172:175],v[112:115]\nC v83,v86,v87\nC v88,v88,v89\nM v[120:123],a[192:195],a[176:179],v[44:47]\nv_exp_f32 v90,v90\nM v[120:123],a[196:199],a[180:183],v[120:123]\n" \
"v_exp_f32 v91,v91\nM v[120:123],a[200:203],a[184:187],v[120:123]\nv_exp_f32 v92,v92\nM v[120:123],a[204:207],a[188:191],v[120:123]\nv_exp_f32 v93,v93\nM v[16:19],a[232:235],v[80:83],v[16:19]\ns_waitcnt lgkmcnt(0)\nv_exp_f32 v94,v94\n" \
"M v[100:103],a[208:211],a[128:131],v[32:35]\nC v89,v90,v91\nC v90,v92,v93\nM v[100:103],a[212:215],a[132:135],v[100:103]\nv_exp_f32 v95,v95\nM v[100:103],a[216:219],a[136:139],v[100:103]\nv_xor_b32 v14,0x10000,v14\nC v91,v94,v95\n" \
"M v[100:103],a[220:223],a[140:143],v[100:103]\nds_read_b128 a[192:195],v14 offset:0\nds_read_b128 a[196:199],v14 offset:1024\nM v[108:111],a[208:211],a[144:147],v[36:39]\nds_read_b128 a[200:203],v14 offset:2048\nM v[108:111],a[212:215],a[148:151],v[108:111]\nds_read_b128 a[204:207],v14 offset:3072\nM v[108:111],a[216:219],a[152:155],v[108:111]\n" \
"M v[108:111],a[220:223],a[156:159],v[108:111]\nM v[16:19],a[236:239],v[88:91],v[16:19]\nM v[116:119],a[208:211],a[160:163],v[40:43]\nM v[116:119],a[212:215],a[164:167],v[116:119]\nv_max3_f32 v15,v16,v17,v18\nv_max_f32 v15,v15,v19\nM v[116:119],a[216:219],a[168:171],v[116:119]\nv_cmp_nge_f32 vcc,0x53800000,v15\n" \
"M v[116:119],a[220:223],a[172:175],v[116:119]\nM v[124:127],a[208:211],a[176:179],v[44:47]\ns_waitcnt lgkmcnt(0)\nds_read_b128 a[208:211],v14 offset:4096\nM v[124:127],a[212:215],a[180:183],v[124:127]\ns_cbranch_vccnz Lcs11_%=\nLcr12_%=:\nds_read_b128 a[212:215],v14 offset:5120\n" \
"M v[124:127],a[216:219],a[184:187],v[124:127]\ns_cmp_eq_u32 s52,s55\nds_read_b128 a[216:219],v14 offset:6144\nM v[124:127],a[220:223],a[188:191],v[124:127]\ns_cbranch_scc1 LH1_%=\nLH1R2_%=:\nds_read_b128 a[220:223],v14 offset:7168\nv_xor_b32 v13,0x10000,v13\n" \
"M v[128:131],a[192:195],v[64:67],v[128:131]\nv_exp_f32 v96,v96\nM v[160:163],a[192:195],v[72:75],v[160:163]\nv_exp_f32 v97,v97\nM v[192:195],a[192:195],v[80:83],v[192:195]\nv_exp_f32 v98,v98\nM v[224:227],a[192:195],v[88:91],v[224:227]\nv_exp_f32 v99,v99\n" \
"M v[228:231],a[196:199],v[88:91],v[228:231]\nv_exp_f32 v100,v100\nM v[196:199],a[196:199],v[80:83],v[196:199]\nv_exp_f32 v101,v101\nM v[164:167],a[196:199],v[72:75],v[164:167]\ns_waitcnt vmcnt(18) lgkmcnt(0)\nv_exp_f32 v102,v102\nM v[132:135],a[196:199],v[64:67],v[132:135]\n" \
"s_barrier\nds_read_b128 a[192:195],v13 offset:32768\nM v[136:139],a[200:203],v[64:67],v[136:139]\nbuffer_load_dwordx4 v22,s[40:43],s45 offen offset:0 sc1 lds\nM v[168:171],a[200:203],v[72:75],v[168:171]\nv_exp_f32 v103,v103\nM v[200:203],a[200:203],v[80:83],v[200:203]\nv_exp_f32 v104,v104\n" \
"M v[232:235],a[200:203],v[88:91],v[232:235]\nv_exp_f32 v105,v105\nM v[236:239],a[204:207],v[88:91],v[236:239]\nbuffer_load_dwordx4 v22,s[40:43],s45 offen offset:1024 sc1 lds\nM v[204:207],a[204:207],v[80:83],v[204:207]\nds_read_b128 a[196:199],v13 offset:34816\nC v96,v96,v97\nM v[172:175],a[204:207],v[72:75],v[172:175]\n" \
"v_exp_f32 v106,v106\nM v[140:143],a[204:207],v[64:67],v[140:143]\nds_read_b128 a[200:203],v13 offset:36864\nds_read_b128 a[204:207],v13 offset:38912\nM v[144:147],a[208:211],v[64:67],v[144:147]\nbuffer_load_dwordx4 v22,s[40:43],s45 offen offset:2048 sc1 lds\nM v[176:179],a[208:211],v[72:75],v[176:179]\nv_exp_f32 v107,v107\n" \
"M v[208:211],a[208:211],v[80:83],v[208:211]\nv_exp_f32 v108,v108\nM v[240:243],a[208:211],v[88:91],v[240:243]\nv_exp_f32 v109,v109\nM v[244:247],a[212:215],v[88:91],v[244:247]\nbuffer_load_dwordx4 v22,s[40:43],s45 offen offset:3072 sc1 lds\nM v[212:215],a[212:215],v[80:83],v[212:215]\ns_add_u32 m0,s53,0\n" \
"v_exp_f32 v110,v110\nM v[180:183],a[212:215],v[72:75],v[180:183]\ns_add_u32 s45,s45,s49\nv_exp_f32 v111,v111\nM v[148:151],a[212:215],v[64:67],v[148:151]\nv_exp_f32 v112,v112\nM v[152:155],a[216:219],v[64:67],v[152:155]\nbuffer_load_dwordx4 v22,s[40:43],s48 offen offset:0 sc1 lds\n" \
"M v[184:187],a[216:219],v[72:75],v[184:187]\nC v97,v98,v99\nC v98,v100,v101\nM v[216:219],a[216:219],v[80:83],v[216:219]\nv_exp_f32 v113,v113\nM v[248:251],a[216:219],v[88:91],v[248:251]\nC v99,v102,v103\nC v104,v104,v105\n" \
"M v[252:255],a[220:223],v[88:91],v[252:255]\nbuffer_load_dwordx4 v22,s[40:43],s48 offen offset:1024 sc1 lds\nM v[220:223],a[220:223],v[80:83],v[220:223]\nv_exp_f32 v114,v114\nM v[188:191],a[220:223],v[72:75],v[188:191]\nv_exp_f32 v115,v115\nM v[156:159],a[220:223],v[64:67],v[156:159]\ns_waitcnt lgkmcnt(0)\n" \
"ds_read_b128 a[208:211],v13 offset:33792\nds_read_b128 a[212:215],v13 offset:35840\nM v[64:67],a[192:195],a[128:131],v[32:35]\nbuffer_load_dwordx4 v22,s[40:43],s48 offen offset:2048 sc1 lds\nM v[64:67],a[196:199],a[132:135],v[64:67]\nC v105,v106,v107\nC v106,v108,v109\nM v[24:27],a[224:227],v[96:99],v[16:19]\n" \
"ds_read_b128 a[216:219],v13 offset:37888\nC v107,v110,v111\nM v[64:67],a[200:203],a[136:139],v[64:67]\nds_read_b128 a[220:223],v13 offset:39936\nC v112,v112,v113\nM v[64:67],a[204:207],a[140:143],v[64:67]\nbuffer_load_dwordx4 v22,s[40:43],s48 offen offset:3072 sc1 lds\nM v[72:75],a[192:195],a[144:147],v[36:39]\n" \
"s_add_u32 m0,s53,32768\nv_exp_f32 v116,v116\nM v[72:75],a[196:199],a[148:151],v[72:75]\ns_add_u32 s48,s48,s49\nv_exp_f32 v117,v117\nM v[24:27],a[228:231],v[104:107],v[24:27]\nv_exp_f32 v118,v118\nM v[72:75],a[200:203],a[152:155],v[72:75]\n" \
"v_exp_f32 v119,v119\nM v[72:75],a[204:207],a[156:159],v[72:75]\nv_exp_f32 v120,v120\nM v[80:83],a[192:195],a[160:163],v[40:43]\nC v113,v114,v115\nC v114,v116,v117\nM v[80:83],a[196:199],a[164:167],v[80:83]\nv_exp_f32 v121,v121\n" \
"M v[80:83],a[200:203],a[168:171],v[80:83]\nC v115,v118,v119\nC v120,v120,v121\nM v[80:83],a[204:207],a[172:175],v[80:83]\nv_exp_f32 v122,v122\nM v[88:91],a[192:195],a[176:179],v[44:47]\nv_exp_f32 v123,v123\nM v[88:91],a[196:199],a[180:183],v[88:91]\n" \
"v_exp_f32 v124,v124\nM v[88:91],a[200:203],a[184:187],v[88:91]\nv_exp_f32 v125,v125\nM v[88:91],a[204:207],a[188:191],v[88:91]\nds_read_b128 a[192:195],v14 offset:16384\nC v121,v122,v123\nM v[24:27],a[232:235],v[112:115],v[24:27]\ns_waitcnt lgkmcnt(1)\n" \
"v_exp_f32 v126,v126\nM v[68:71],a[208:211],a[128:131],v[32:35]\nds_read_b128 a[196:199],v14 offset:17408\nC v122,v124,v125\nM v[68:71],a[212:215],a[132:135],v[68:71]\nv_exp_f32 v127,v127\nM v[68:71],a[216:219],a[136:139],v[68:71]\nds_read_b128 a[200:203],v14 offset:18432\n" \
"C v123,v126,v127\nM v[68:71],a[220:223],a[140:143],v[68:71]\nds_read_b128 a[204:207],v14 offset:19456\nM v[76:79],a[208:211],a[144:147],v[36:39]\nM v[76:79],a[212:215],a[148:151],v[76:79]\nM v[76:79],a[216:219],a[152:155],v[76:79]\nM v[76:79],a[220:223],a[156:159],v[76:79]\nM v[24:27],a[236:239],v[120:123],v[24:27]\n" \
"M v[84:87],a[208:211],a[160:163],v[40:43]\ns_mov_b64 exec,0xff\nglobal_load_dword v0,v2,s[100:101]\ns_mov_b64 exec,-1\nM v[84:87],a[212:215],a[164:167],v[84:87]\nLG16_%=:\ns_cbranch_execz LHR2_%=\nv_max3_f32 v15,v24,v25,v26\n" \
"v_max_f32 v15,v15,v27\nM v[84:87],a[216:219],a[168:171],v[84:87]\nv_cmp_nge_f32 vcc,0x53800000,v15\nM v[84:87],a[220:223],a[172:175],v[84:87]\nM v[92:95],a[208:211],a[176:179],v[44:47]\nds_read_b128 a[208:211],v14 offset:20480\nM v[92:95],a[212:215],a[180:183],v[92:95]\ns_cbranch_vccnz Lcs13_%=\n" \
"Lcr14_%=:\nds_read_b128 a[212:215],v14 offset:21504\nM v[92:95],a[216:219],a[184:187],v[92:95]\nds_read_b128 a[216:219],v14 offset:22528\nM v[92:95],a[220:223],a[188:191],v[92:95]\ns_waitcnt lgkmcnt(3)\nds_read_b128 a[220:223],v14 offset:23552\nM v[128:131],a[192:195],v[96:99],v[128:131]\n" \
"v_exp_f32 v64,v64\nM v[160:163],a[192:195],v[104:107],v[160:163]\nv_exp_f32 v65,v65\nM v[192:195],a[192:195],v[112:115],v[192:195]\nbuffer_load_dword v58,v57,s[84:87],s74 offen\nM v[224:227],a[192:195],v[120:123],v[224:227]\ns_add_u32 s74,s74,2048\nv_exp_f32 v66,v66\n" \
"M v[228:231],a[196:199],v[120:123],v[228:231]\nv_exp_f32 v67,v67\nM v[196:199],a[196:199],v[112:115],v[196:199]\nv_exp_f32 v68,v68\nM v[164:167],a[196:199],v[104:107],v[164:167]\ns_waitcnt vmcnt(20) lgkmcnt(0)\nv_exp_f32 v69,v69\nM v[132:135],a[196:199],v[96:99],v[132:135]\n" \
"s_barrier\nds_read_b128 a[192:195],v13 offset:49152\nM v[136:139],a[200:203],v[96:99],v[136:139]\nbuffer_load_dwordx4 v22,s[40:43],s45 offen offset:0 sc1 lds\nM v[168:171],a[200:203],v[104:107],v[168:171]\nv_exp_f32 v70,v70\nM v[200:203],a[200:203],v[112:115],v[200:203]\nv_exp_f32 v71,v71\n" \
"M v[232:235],a[200:203],v[120:123],v[232:235]\nv_exp_f32 v72,v72\nM v[236:239],a[204:207],v[120:123],v[236:239]\nbuffer_load_dwordx4 v22,s[40:43],s45 offen offset:1024 sc1 lds\nM v[204:207],a[204:207],v[112:115],v[204:207]\nds_read_b128 a[196:199],v13 offset:51200\nC v64,v64,v65\nM v[172:175],a[204:207],v[104:107],v[172:175]\n" \
"v_exp_f32 v73,v73\nM v[140:143],a[204:207],v[96:99],v[140:143]\nds_read_b128 a[200:203],v13 offset:53248\nds_read_b128 a[204:207],v13 offset:55296\nM v[144:147],a[208:211],v[96:99],v[144:147]\nbuffer_load_dwordx4 v22,s[40:43],s45 offen offset:2048 sc1 lds\nM v[176:179],a[208:211],v[104:107],v[176:179]\nv_exp_f32 v74,v74\n" \
"M v[208:211],a[208:211],v[112:115],v[208:211]\nv_exp_f32 v75,v75\nM v[240:243],a[208:211],v[120:123],v[240:243]\nv_exp_f32 v76,v76\nM v[244:247],a[212:215],v[120:123],v[244:247]\nbuffer_load_dwordx4 v22,s[40:43],s45 offen offset:3072 sc1 lds\nM v[212:215],a[212:215],v[112:115],v[212:215]\ns_add_u32 m0,s53,16384\n" \
"v_exp_f32 v77,v77\nM v[180:183],a[212:215],v[104:107],v[180:183]\ns_add_u32 s52,s52,1\nv_exp_f32 v78,v78\nM v[148:151],a[212:215],v[96:99],v[148:151]\ns_add_u32 s45,s45,s49\nv_exp_f32 v79,v79\nM v[152:155],a[216:219],v[96:99],v[152:155]\n" \
"buffer_load_dwordx4 v22,s[40:43],s48 offen offset:0 sc1 lds\nM v[184:187],a[216:219],v[104:107],v[184:187]\nv_exp_f32 v80,v80\nM v[216:219],a[216:219],v[112:115],v[216:219]\nC v65,v66,v67\nC v66,v68,v69\nM v[248:251],a[216:219],v[120:123],v[248:251]\nv_exp_f32 v81,v81\n" \
"M v[252:255],a[220:223],v[120:123],v[252:255]\nbuffer_load_dwordx4 v22,s[40:43],s48 offen offset:1024 sc1 lds\nM v[220:223],a[220:223],v[112:115],v[220:223]\ns_waitcnt lgkmcnt(0)\nC v67,v70,v71\nC v72,v72,v73\nM v[188:191],a[220:223],v[104:107],v[188:191]\ns_cmp_eq_u32 s52,s56\n" \
"v_exp_f32 v82,v82\nM v[156:159],a[220:223],v[96:99],v[156:159]\ns_cbranch_scc0 LEVR1_%=\ns_branch LEV_%=\nLEVR8_%=:\nds_read_b128 a[208:211],v13 offset:50176\nds_read_b128 a[212:215],v13 offset:52224\nM v[96:99],a[192:195],a[128:131],v[32:35]\n" \
"buffer_load_dwordx4 v22,s[40:43],s48 offen offset:2048 sc1 lds\nM v[96:99],a[196:199],a[132:135],v[96:99]\ns_lshl_b32 s58,%2,2\nv_exp_f32 v83,v83\nM v[16:19],a[224:227],v[64:67],v[24:27]\ns_lshl_b32 s59,%2,1\nds_read_b128 a[216:219],v13 offset:54272\nC v73,v74,v75\n" \
"M v[96:99],a[200:203],a[136:139],v[96:99]\nds_read_b128 a[220:223],v13 offset:56320\nC v74,v76,v77\nM v[96:99],a[204:207],a[140:143],v[96:99]\nbuffer_load_dwordx4 v22,s[40:43],s48 offen offset:3072 sc1 lds\nM v[104:107],a[192:195],a[144:147],v[36:39]\ns_add_u32 m0,s22,49152\nC v75,v78,v79\n" \
"C v80,v80,v81\nM v[104:107],a[196:199],a[148:151],v[104:107]\ns_add_u32 s48,s48,s49\nv_exp_f32 v84,v84\nM v[16:19],a[228:231],v[72:75],v[16:19]\nv_exp_f32 v85,v85\nM v[104:107],a[200:203],a[152:155],v[104:107]\nv_exp_f32 v86,v86\n" \
"M v[104:107],a[204:207],a[156:159],v[104:107]\nv_exp_f32 v87,v87\nM v[112:115],a[192:195],a[160:163],v[40:43]\nv_exp_f32 v88,v88\nM v[112:115],a[196:199],a[164:167],v[112:115]\nC v81,v82,v83\nC v82,v84,v85\nM v[112:115],a[200:203],a[168:171],v[112:115]\n" \
"v_exp_f32 v89,v89\nM v[112:115],a[204:207],a[172:175],v[112:115]\nC v83,v86,v87\nC v88,v88,v89\nM v[120:123],a[192:195],a[176:179],v[44:47]\nv_exp_f32 v90,v90\nM v[120:123],a[196:199],a[180:183],v[120:123]\nv_exp_f32 v91,v91\n" \
"M v[120:123],a[200:203],a[184:187],v[120:123]\nv_exp_f32 v92,v92\nM v[120:123],a[204:207],a[188:191],v[120:123]\nv_exp_f32 v93,v93\nM v[16:19],a[232:235],v[80:83],v[16:19]\ns_waitcnt lgkmcnt(0)\nv_exp_f32 v94,v94\nM v[100:103],a[208:211],a[128:131],v[32:35]\n" \
"C v89,v90,v91\nv_xor_b32 v14,0x10000,v14\nM v[100:103],a[212:215],a[132:135],v[100:103]\nds_read_b128 a[192:195],v14 offset:0\nds_read_b128 a[196:199],v14 offset:1024\nM v[100:103],a[216:219],a[136:139],v[100:103]\nv_exp_f32 v95,v95\nM v[100:103],a[220:223],a[140:143],v[100:103]\n" \
"C v90,v92,v93\nC v91,v94,v95\nM v[108:111],a[208:211],a[144:147],v[36:39]\nds_read_b128 a[200:203],v14 offset:2048\nv_bfe_u32 v52,v4,6,2\nM v[108:111],a[212:215],a[148:151],v[108:111]\nv_add_u32 v52,s58,v52\nv_lshrrev_b32 v5,1,v4\n" \
"M v[108:111],a[216:219],a[152:155],v[108:111]\ns_lshl_b32 s58,%2,9\nds_read_b128 a[204:207],v14 offset:3072\nv_xor_b32 v13,0x10000,v13\nM v[108:111],a[220:223],a[156:159],v[108:111]\ns_add_u32 s58,s58,94375936\nv_add_u32 v5,s58,v5\nv_bfe_u32 v53,v4,8,1\n" \
"M v[16:19],a[236:239],v[88:91],v[16:19]\ns_lshl_b32 s58,%2,9\nv_lshl_add_u32 v52,v53,4,v52\nv_add_u32 v53,s59,v53\nM v[116:119],a[208:211],a[160:163],v[40:43]\nv_lshrrev_b32 v54,4,v4\nv_xor_b32 v53,v53,v54\nM v[116:119],a[212:215],a[164:167],v[116:119]\n" \
"buffer_load_dwordx2 v[10:11],v5,s[36:39],0 offen sc1\nM v[116:119],a[216:219],a[168:171],v[116:119]\nv_max3_f32 v15,v16,v17,v18\nv_max_f32 v15,v15,v19\nM v[116:119],a[220:223],a[172:175],v[116:119]\nv_cmp_nge_f32 vcc,0x53800000,v15\nv_lshlrev_b32 v52,11,v52\nM v[124:127],a[208:211],a[176:179],v[44:47]\n" \
"ds_read_b128 a[208:211],v14 offset:4096\nv_and_b32 v53,3,v53\nM v[124:127],a[212:215],a[180:183],v[124:127]\ns_cbranch_vccnz Lcs39_%=\nLcr40_%=:\nds_read_b128 a[212:215],v14 offset:5120\nv_lshrrev_b32 v54,9,v4\nM v[124:127],a[216:219],a[184:187],v[124:127]\n" \
"ds_read_b128 a[216:219],v14 offset:6144\nv_lshl_add_u32 v53,v54,2,v53\nM v[124:127],a[220:223],a[188:191],v[124:127]\ns_waitcnt lgkmcnt(3)\nds_read_b128 a[220:223],v14 offset:7168\nv_lshl_add_u32 v1,v53,4,v52\nM v[128:131],a[192:195],v[64:67],v[128:131]\nv_exp_f32 v96,v96\n" \
"M v[160:163],a[192:195],v[72:75],v[160:163]\nv_exp_f32 v97,v97\nM v[192:195],a[192:195],v[80:83],v[192:195]\nv_exp_f32 v98,v98\nM v[224:227],a[192:195],v[88:91],v[224:227]\nv_exp_f32 v99,v99\nM v[228:231],a[196:199],v[88:91],v[228:231]\nv_exp_f32 v100,v100\n" \
"M v[196:199],a[196:199],v[80:83],v[196:199]\nv_exp_f32 v101,v101\nM v[164:167],a[196:199],v[72:75],v[164:167]\ns_waitcnt vmcnt(19) lgkmcnt(0)\nv_exp_f32 v102,v102\nM v[132:135],a[196:199],v[64:67],v[132:135]\ns_barrier\nds_read_b128 a[192:195],v13 offset:32768\n" \
"M v[136:139],a[200:203],v[64:67],v[136:139]\nbuffer_load_dwordx4 v1,s[84:87],s46 offen offset:0 lds\nM v[168:171],a[200:203],v[72:75],v[168:171]\ns_add_u32 m0,s22,50048\nv_exp_f32 v103,v103\nM v[200:203],a[200:203],v[80:83],v[200:203]\nv_exp_f32 v104,v104\nM v[232:235],a[200:203],v[88:91],v[232:235]\n" \
"v_exp_f32 v105,v105\nM v[236:239],a[204:207],v[88:91],v[236:239]\nbuffer_load_dwordx4 v1,s[84:87],s46 offen offset:128 lds\nM v[204:207],a[204:207],v[80:83],v[204:207]\ns_add_u32 m0,s22,51200\nv_exp_f32 v106,v106\nM v[172:175],a[204:207],v[72:75],v[172:175]\ns_mov_b32 s46,65536\n" \
"ds_read_b128 a[196:199],v13 offset:34816\nds_read_b128 a[200:203],v13 offset:36864\nM v[140:143],a[204:207],v[64:67],v[140:143]\nv_exp_f32 v107,v107\nM v[144:147],a[208:211],v[64:67],v[144:147]\nbuffer_load_dwordx4 v1,s[84:87],s46 offen offset:0 lds\nM v[176:179],a[208:211],v[72:75],v[176:179]\ns_add_u32 m0,s22,52096\n" \
"ds_read_b128 a[204:207],v13 offset:38912\nC v96,v96,v97\nM v[208:211],a[208:211],v[80:83],v[208:211]\nv_exp_f32 v108,v108\nM v[240:243],a[208:211],v[88:91],v[240:243]\nv_exp_f32 v109,v109\nM v[244:247],a[212:215],v[88:91],v[244:247]\nbuffer_load_dwordx4 v1,s[84:87],s46 offen offset:128 lds\n" \
"M v[212:215],a[212:215],v[80:83],v[212:215]\ns_add_u32 m0,s22,65536\nv_exp_f32 v110,v110\nM v[180:183],a[212:215],v[72:75],v[180:183]\ns_mov_b32 s46,131072\nv_exp_f32 v111,v111\nM v[148:151],a[212:215],v[64:67],v[148:151]\ns_and_b32 s61,%1,1\n" \
"v_exp_f32 v112,v112\nM v[152:155],a[216:219],v[64:67],v[152:155]\nbuffer_load_dwordx4 v1,s[84:87],s46 offen offset:0 lds\nM v[184:187],a[216:219],v[72:75],v[184:187]\ns_add_u32 m0,s22,66432\nv_exp_f32 v113,v113\nM v[216:219],a[216:219],v[80:83],v[216:219]\ns_lshl_b32 s61,s61,2\n" \
"v_exp_f32 v114,v114\nM v[248:251],a[216:219],v[88:91],v[248:251]\ns_bfe_u32 s60,%1,0x20001\nC v97,v98,v99\nC v98,v100,v101\nM v[252:255],a[220:223],v[88:91],v[252:255]\nbuffer_load_dwordx4 v1,s[84:87],s46 offen offset:128 lds\nM v[220:223],a[220:223],v[80:83],v[220:223]\n" \
"s_add_u32 m0,s22,67584\nv_exp_f32 v115,v115\nM v[188:191],a[220:223],v[72:75],v[188:191]\ns_mov_b32 s46,196608\nC v99,v102,v103\nC v104,v104,v105\nM v[156:159],a[220:223],v[64:67],v[156:159]\ns_cmp_eq_u32 s60,0\n" \
"s_cselect_b64 s[96:97],-1,0\ns_waitcnt lgkmcnt(0)\nv_exp_f32 v116,v116\nM v[64:67],a[192:195],a[128:131],v[32:35]\nbuffer_load_dwordx4 v1,s[84:87],s46 offen offset:0 lds\nM v[64:67],a[196:199],a[132:135],v[64:67]\ns_waitcnt vmcnt(7)\nv_cmp_gt_u64 s[98:99],s[14:15],v[10:11]\n" \
"v_cmp_le_u64 s[18:19],s[16:17],v[10:11]\nM v[24:27],a[224:227],v[96:99],v[16:19]\ns_or_b64 s[98:99],s[98:99],s[18:19]\ns_cbranch_scc1 LPZ_%=\nLPZR_%=:\nds_read_b128 a[208:211],v13 offset:33792\nds_read_b128 a[212:215],v13 offset:35840\nM v[64:67],a[200:203],a[136:139],v[64:67]\n" \
"s_add_u32 m0,s22,68480\nds_read_b128 a[216:219],v13 offset:37888\nC v105,v106,v107\nM v[64:67],a[204:207],a[140:143],v[64:67]\nbuffer_load_dwordx4 v1,s[84:87],s46 offen offset:128 lds\nM v[72:75],a[192:195],a[144:147],v[36:39]\ns_add_u32 m0,s22,98304\nC v106,v108,v109\n" \
"C v107,v110,v111\nv_exp_f32 v117,v117\nM v[72:75],a[196:199],a[148:151],v[72:75]\ns_mov_b32 s46,262144\nds_read_b128 a[220:223],v13 offset:39936\nC v112,v112,v113\nM v[24:27],a[228:231],v[104:107],v[24:27]\ns_add_u32 s14,s14,3000\n" \
"s_addc_u32 s15,s15,0\nv_exp_f32 v118,v118\nM v[72:75],a[200:203],a[152:155],v[72:75]\ns_sub_u32 s73,s72,2048\nv_exp_f32 v119,v119\nM v[72:75],a[204:207],a[156:159],v[72:75]\ns_lshl_b32 s95,%1,2\nv_exp_f32 v120,v120\n" \
"M v[80:83],a[192:195],a[160:163],v[40:43]\ns_add_u32 s95,s95,%2\nv_exp_f32 v121,v121\nM v[80:83],a[196:199],a[164:167],v[80:83]\ns_lshl_b32 s95,s95,5\nC v113,v114,v115\nC v114,v116,v117\nM v[80:83],a[200:203],a[168:171],v[80:83]\n" \
"s_add_u32 s95,s95,94437376\nv_exp_f32 v122,v122\nM v[80:83],a[204:207],a[172:175],v[80:83]\nC v115,v118,v119\nC v120,v120,v121\nM v[88:91],a[192:195],a[176:179],v[44:47]\nv_exp_f32 v123,v123\nM v[88:91],a[196:199],a[180:183],v[88:91]\n" \
"v_exp_f32 v124,v124\nM v[88:91],a[200:203],a[184:187],v[88:91]\nv_exp_f32 v125,v125\nM v[88:91],a[204:207],a[188:191],v[88:91]\nds_read_b128 a[192:195],v14 offset:16384\nC v121,v122,v123\nM v[24:27],a[232:235],v[112:115],v[24:27]\ns_waitcnt lgkmcnt(1)\n" \
"v_exp_f32 v126,v126\nM v[68:71],a[208:211],a[128:131],v[32:35]\nds_read_b128 a[196:199],v14 offset:17408\nC v122,v124,v125\nM v[68:71],a[212:215],a[132:135],v[68:71]\nv_exp_f32 v127,v127\nM v[68:71],a[216:219],a[136:139],v[68:71]\nds_read_b128 a[200:203],v14 offset:18432\n" \
"C v123,v126,v127\nM v[68:71],a[220:223],a[140:143],v[68:71]\nds_read_b128 a[204:207],v14 offset:19456\nv_and_b32 v52,0xc0,v4\nM v[76:79],a[208:211],a[144:147],v[36:39]\nv_bfe_u32 v53,v4,4,1\nv_lshl_add_u32 v52,v53,3,v52\nM v[76:79],a[212:215],a[148:151],v[76:79]\n" \
"v_lshrrev_b32 v54,7,v4\nv_bfe_u32 v53,v4,5,1\nM v[76:79],a[216:219],a[152:155],v[76:79]\nv_and_b32 v54,2,v54\nv_xor_b32 v53,v53,v54\nM v[76:79],a[220:223],a[156:159],v[76:79]\nv_lshl_add_u32 v52,v53,4,v52\nv_lshrrev_b32 v53,8,v4\n" \
"M v[24:27],a[236:239],v[120:123],v[24:27]\nv_lshl_add_u32 v52,v53,12,v52\nv_add_u32 v6,s58,v52\nM v[84:87],a[208:211],a[160:163],v[40:43]\ns_bfe_u32 s58,%1,0x20003\nv_xor_b32 v7,0x110,v6\nv_xor_b32 v8,32,v6\nM v[84:87],a[212:215],a[164:167],v[84:87]\n" \
"s_add_u32 s61,s61,s58\nv_max3_f32 v15,v24,v25,v26\nv_max_f32 v15,v15,v27\nM v[84:87],a[216:219],a[168:171],v[84:87]\ns_and_b32 s58,s61,6\nv_cmp_nge_f32 vcc,0x53800000,v15\nv_xor_b32 v9,0x130,v6\nM v[84:87],a[220:223],a[172:175],v[84:87]\n" \
"s_add_u32 s58,s58,s61\nv_bfe_u32 v52,v4,7,2\nv_mov_b32 v48,-1\nM v[92:95],a[208:211],a[176:179],v[44:47]\ns_lshl_b32 s58,s58,2\nds_read_b128 a[208:211],v14 offset:20480\nv_mov_b32 v49,-1\nM v[92:95],a[212:215],a[180:183],v[92:95]\n" \
"s_cbranch_vccnz Lcs41_%=\nLcr42_%=:\nds_read_b128 a[212:215],v14 offset:21504\nv_mov_b32 v50,-1\nM v[92:95],a[216:219],a[184:187],v[92:95]\ns_add_u32 s58,s58,s60\nds_read_b128 a[216:219],v14 offset:22528\nv_mov_b32 v51,-1\n" \
"M v[92:95],a[220:223],a[188:191],v[92:95]\ns_waitcnt lgkmcnt(3)\nbuffer_store_dwordx4 v[48:51],v4,s[36:39],s72 offen offset:0 sc1\nM v[128:131],a[192:195],v[96:99],v[128:131]\ns_lshl_b32 s58,s58,15\nds_read_b128 a[220:223],v14 offset:23552\nv_mov_b32 v53,s11\nM v[160:163],a[192:195],v[104:107],v[160:163]\n" \
"s_add_u32 s28,s58,50331648\nv_exp_f32 v64,v64\nM v[192:195],a[192:195],v[112:115],v[192:195]\ns_add_u32 s29,s58,52428800\nv_exp_f32 v65,v65\nM v[224:227],a[192:195],v[120:123],v[224:227]\ns_add_u32 s30,s58,54525952\nv_exp_f32 v66,v66\n" \
"M v[228:231],a[196:199],v[120:123],v[228:231]\ns_add_u32 s31,s58,56623104\nv_exp_f32 v67,v67\nM v[196:199],a[196:199],v[112:115],v[196:199]\ns_lshr_b32 s58,%2,1\nv_exp_f32 v68,v68\nM v[164:167],a[196:199],v[104:107],v[164:167]\ns_waitcnt vmcnt(20) lgkmcnt(0)\n" \
"v_exp_f32 v69,v69\nM v[132:135],a[196:199],v[96:99],v[132:135]\ns_lshl_b32 s58,s58,18\ns_barrier\nds_read_b128 a[192:195],v13 offset:49152\nM v[136:139],a[200:203],v[96:99],v[136:139]\nbuffer_store_dwordx4 v[48:51],v4,s[36:39],s72 offen offset:1024 sc1\nM v[168:171],a[200:203],v[104:107],v[168:171]\n" \
"s_add_u32 s47,s28,0\nv_exp_f32 v70,v70\nM v[200:203],a[200:203],v[112:115],v[200:203]\nv_exp_f32 v71,v71\nM v[232:235],a[200:203],v[120:123],v[232:235]\nv_exp_f32 v72,v72\nM v[236:239],a[204:207],v[120:123],v[236:239]\nv_exp_f32 v73,v73\n" \
"M v[204:207],a[204:207],v[112:115],v[204:207]\nv_exp_f32 v74,v74\nM v[172:175],a[204:207],v[104:107],v[172:175]\nds_read_b128 a[196:199],v13 offset:51200\nds_read_b128 a[200:203],v13 offset:53248\nM v[140:143],a[204:207],v[96:99],v[140:143]\nv_exp_f32 v75,v75\nM v[144:147],a[208:211],v[96:99],v[144:147]\n" \
"ds_read_b128 a[204:207],v13 offset:55296\nC v64,v64,v65\nM v[176:179],a[208:211],v[104:107],v[176:179]\nv_exp_f32 v76,v76\nM v[208:211],a[208:211],v[112:115],v[208:211]\nv_exp_f32 v77,v77\nM v[240:243],a[208:211],v[120:123],v[240:243]\nbuffer_load_dwordx4 v22,s[40:43],s45 offen offset:0 sc1 lds\n" \
"M v[244:247],a[212:215],v[120:123],v[244:247]\nv_exp_f32 v78,v78\nM v[212:215],a[212:215],v[112:115],v[212:215]\nv_exp_f32 v79,v79\nM v[180:183],a[212:215],v[104:107],v[180:183]\ns_add_u32 s52,s52,1\nv_exp_f32 v80,v80\nM v[148:151],a[212:215],v[96:99],v[148:151]\n" \
"v_exp_f32 v81,v81\nM v[152:155],a[216:219],v[96:99],v[152:155]\nbuffer_load_dwordx4 v22,s[40:43],s45 offen offset:1024 sc1 lds\nM v[184:187],a[216:219],v[104:107],v[184:187]\nC v65,v66,v67\nC v66,v68,v69\nM v[216:219],a[216:219],v[112:115],v[216:219]\nv_exp_f32 v82,v82\n" \
"M v[248:251],a[216:219],v[120:123],v[248:251]\nC v67,v70,v71\nC v72,v72,v73\nM v[252:255],a[220:223],v[120:123],v[252:255]\nbuffer_load_dwordx4 v22,s[40:43],s45 offen offset:2048 sc1 lds\nM v[220:223],a[220:223],v[112:115],v[220:223]\ns_waitcnt lgkmcnt(0)\nv_exp_f32 v83,v83\n" \
"M v[188:191],a[220:223],v[104:107],v[188:191]\ns_cmp_eq_u32 s52,s56\nv_exp_f32 v84,v84\nM v[156:159],a[220:223],v[96:99],v[156:159]\ns_cbranch_scc1 LEV_%=\nLEVR9_%=:\nC v73,v74,v75\nC v74,v76,v77\n" \
"M v[96:99],a[192:195],a[128:131],v[32:35]\nds_read_b128 a[208:211],v13 offset:50176\nds_read_b128 a[212:215],v13 offset:52224\nM v[96:99],a[196:199],a[132:135],v[96:99]\nbuffer_load_dwordx4 v22,s[40:43],s45 offen offset:3072 sc1 lds\nM v[16:19],a[224:227],v[64:67],v[24:27]\ns_add_u32 m0,s22,81920\nv_exp_f32 v85,v85\n" \
"M v[96:99],a[200:203],a[136:139],v[96:99]\ns_add_u32 s45,s45,s49\nds_read_b128 a[216:219],v13 offset:54272\nC v75,v78,v79\nM v[96:99],a[204:207],a[140:143],v[96:99]\nds_read_b128 a[220:223],v13 offset:56320\nC v80,v80,v81\nM v[104:107],a[192:195],a[144:147],v[36:39]\n" \
"buffer_load_dwordx4 v22,s[40:43],s48 offen offset:0 sc1 lds\nM v[104:107],a[196:199],a[148:151],v[104:107]\nv_exp_f32 v86,v86\nM v[16:19],a[228:231],v[72:75],v[16:19]\nv_exp_f32 v87,v87\nM v[104:107],a[200:203],a[152:155],v[104:107]\nv_exp_f32 v88,v88\nM v[104:107],a[204:207],a[156:159],v[104:107]\n" \
"C v81,v82,v83\nC v82,v84,v85\nM v[112:115],a[192:195],a[160:163],v[40:43]\nbuffer_store_dwordx4 v[48:51],v4,s[36:39],s72 offen offset:2048 sc1\nM v[112:115],a[196:199],a[164:167],v[112:115]\nv_exp_f32 v89,v89\nM v[112:115],a[200:203],a[168:171],v[112:115]\nC v83,v86,v87\n" \
"C v88,v88,v89\nM v[112:115],a[204:207],a[172:175],v[112:115]\nv_exp_f32 v90,v90\nM v[120:123],a[192:195],a[176:179],v[44:47]\nv_exp_f32 v91,v91\nM v[120:123],a[196:199],a[180:183],v[120:123]\nv_exp_f32 v92,v92\nM v[120:123],a[200:203],a[184:187],v[120:123]\n" \
"v_exp_f32 v93,v93\nM v[120:123],a[204:207],a[188:191],v[120:123]\nv_exp_f32 v94,v94\nM v[16:19],a[232:235],v[80:83],v[16:19]\ns_waitcnt lgkmcnt(0)\nC v89,v90,v91\nC v90,v92,v93\nM v[100:103],a[208:211],a[128:131],v[32:35]\n" \
"v_exp_f32 v95,v95\nM v[100:103],a[212:215],a[132:135],v[100:103]\nv_xor_b32 v14,0x10000,v14\nC v91,v94,v95\nM v[100:103],a[216:219],a[136:139],v[100:103]\nds_read_b128 a[192:195],v14 offset:0\nds_read_b128 a[196:199],v14 offset:1024\nM v[100:103],a[220:223],a[140:143],v[100:103]\n" \
"ds_read_b128 a[200:203],v14 offset:2048\nv_add_u32 v2,s58,v22\nM v[108:111],a[208:211],a[144:147],v[36:39]\nbuffer_store_dwordx4 v[48:51],v4,s[36:39],s72 offen offset:3072 sc1\nM v[108:111],a[212:215],a[148:151],v[108:111]\ns_lshl_b32 s58,s61,2\nds_read_b128 a[204:207],v14 offset:3072\nM v[108:111],a[216:219],a[152:155],v[108:111]\n" \
"s_add_u32 s58,s58,s60\nM v[108:111],a[220:223],a[156:159],v[108:111]\ns_lshl_b32 s58,s58,6\nM v[16:19],a[236:239],v[88:91],v[16:19]\ns_add_u32 s58,s58,94388224\nv_lshl_add_u32 v5,v52,11,s58\nv_bfe_u32 v52,v4,4,3\nM v[116:119],a[208:211],a[160:163],v[40:43]\n" \
"s_lshl_b32 s58,%2,13\nv_lshl_add_u32 v5,v52,3,v5\nv_mov_b32 v52,s10\nM v[116:119],a[212:215],a[164:167],v[116:119]\ns_add_u32 s58,s58,0x20000\nv_max3_f32 v15,v16,v17,v18\nv_max_f32 v15,v15,v19\nM v[116:119],a[216:219],a[168:171],v[116:119]\n" \
"v_cmp_nge_f32 vcc,0x53800000,v15\nv_add_u32 v12,s58,v4\nM v[116:119],a[220:223],a[172:175],v[116:119]\ns_lshl_b32 s58,s7,1\nv_xor_b32 v57,0x4000,v12\nM v[124:127],a[208:211],a[176:179],v[44:47]\ns_sub_u32 s58,15,s58\nds_read_b128 a[208:211],v14 offset:4096\n" \
"M v[124:127],a[212:215],a[180:183],v[124:127]\ns_cbranch_vccnz Lcs43_%=\nLcr44_%=:\nds_read_b128 a[212:215],v14 offset:5120\nM v[124:127],a[216:219],a[184:187],v[124:127]\ns_cmp_eq_u32 s52,s55\nds_read_b128 a[216:219],v14 offset:6144\nM v[124:127],a[220:223],a[188:191],v[124:127]\n" \
"s_cbranch_scc1 LH1_%=\nLH1R10_%=:\ns_waitcnt lgkmcnt(3)\nds_read_b128 a[220:223],v14 offset:7168\nv_xor_b32 v13,0x10000,v13\nM v[128:131],a[192:195],v[64:67],v[128:131]\nbuffer_store_dwordx4 v[48:51],v4,s[36:39],s73 offen offset:0 sc1\nM v[160:163],a[192:195],v[72:75],v[160:163]\n" \
"s_lshl_b32 s59,s58,20\nv_exp_f32 v96,v96\nM v[192:195],a[192:195],v[80:83],v[192:195]\ns_add_u32 s51,s68,s59\nv_exp_f32 v97,v97\nM v[224:227],a[192:195],v[88:91],v[224:227]\ns_lshl_b32 s59,s54,6\nv_exp_f32 v98,v98\n" \
"M v[228:231],a[196:199],v[88:91],v[228:231]\nbuffer_load_dwordx4 v22,s[40:43],s48 offen offset:1024 sc1 lds\nM v[196:199],a[196:199],v[80:83],v[196:199]\ns_add_u32 s51,s51,s59\nv_exp_f32 v99,v99\nM v[164:167],a[196:199],v[72:75],v[164:167]\ns_waitcnt vmcnt(15) lgkmcnt(0)\nv_exp_f32 v100,v100\n" \
"M v[132:135],a[196:199],v[64:67],v[132:135]\ns_add_u32 s62,s51,0\ns_barrier\nds_read_b128 a[192:195],v13 offset:32768\nv_exp_f32 v101,v101\nM v[136:139],a[200:203],v[64:67],v[136:139]\ns_lshl_b32 s59,s58,10\nv_exp_f32 v102,v102\n" \
"M v[168:171],a[200:203],v[72:75],v[168:171]\nLG17_%=:\ns_cbranch_execz LHR2_%=\ns_add_u32 s50,s69,s59\nv_exp_f32 v103,v103\nM v[200:203],a[200:203],v[80:83],v[200:203]\nbuffer_load_dwordx4 v22,s[40:43],s48 offen offset:2048 sc1 lds\nv_exp_f32 v104,v104\n" \
"M v[232:235],a[200:203],v[88:91],v[232:235]\ns_lshr_b32 s59,s54,4\nv_exp_f32 v105,v105\nM v[236:239],a[204:207],v[88:91],v[236:239]\ns_add_u32 s50,s50,s59\nv_exp_f32 v106,v106\nM v[204:207],a[204:207],v[80:83],v[204:207]\nv_exp_f32 v107,v107\n" \
"M v[172:175],a[204:207],v[72:75],v[172:175]\nbuffer_load_dwordx4 v22,s[40:43],s48 offen offset:3072 sc1 lds\nv_exp_f32 v108,v108\nM v[140:143],a[204:207],v[64:67],v[140:143]\ns_add_u32 m0,s22,114688\nv_exp_f32 v109,v109\nM v[144:147],a[208:211],v[64:67],v[144:147]\ns_add_u32 s48,s48,s49\n" \
"v_exp_f32 v110,v110\nM v[176:179],a[208:211],v[72:75],v[176:179]\nv_exp_f32 v111,v111\nM v[208:211],a[208:211],v[80:83],v[208:211]\nv_exp_f32 v112,v112\nM v[240:243],a[208:211],v[88:91],v[240:243]\nv_exp_f32 v113,v113\nM v[244:247],a[212:215],v[88:91],v[244:247]\n" \
"v_exp_f32 v114,v114\nM v[212:215],a[212:215],v[80:83],v[212:215]\nv_exp_f32 v115,v115\nM v[180:183],a[212:215],v[72:75],v[180:183]\nds_read_b128 a[196:199],v13 offset:34816\nds_read_b128 a[200:203],v13 offset:36864\nv_exp_f32 v116,v116\nM v[148:151],a[212:215],v[64:67],v[148:151]\n" \
"v_exp_f32 v117,v117\nM v[152:155],a[216:219],v[64:67],v[152:155]\nbuffer_load_dwordx4 v1,s[84:87],s46 offen offset:0 lds\nM v[184:187],a[216:219],v[72:75],v[184:187]\ns_add_u32 m0,s22,115584\nds_read_b128 a[204:207],v13 offset:38912\nC v96,v96,v97\nv_exp_f32 v118,v118\n" \
"M v[216:219],a[216:219],v[80:83],v[216:219]\nv_exp_f32 v119,v119\nM v[248:251],a[216:219],v[88:91],v[248:251]\nC v97,v98,v99\nC v104,v104,v105\nM v[252:255],a[220:223],v[88:91],v[252:255]\nbuffer_load_dwordx4 v1,s[84:87],s46 offen offset:128 lds\nM v[220:223],a[220:223],v[80:83],v[220:223]\n" \
"s_add_u32 m0,s22,116736\nds_read_b128 a[208:211],v13 offset:33792\nC v98,v100,v101\nM v[188:191],a[220:223],v[72:75],v[188:191]\ns_mov_b32 s46,327680\nds_read_b128 a[212:215],v13 offset:35840\nC v99,v102,v103\nM v[156:159],a[220:223],v[64:67],v[156:159]\n" \
"s_waitcnt lgkmcnt(2)\nv_exp_f32 v120,v120\nM v[64:67],a[192:195],a[128:131],v[32:35]\nbuffer_load_dwordx4 v1,s[84:87],s46 offen offset:0 lds\nM v[64:67],a[196:199],a[132:135],v[64:67]\ns_add_u32 m0,s22,117632\nds_read_b128 a[216:219],v13 offset:37888\nC v105,v106,v107\n" \
"M v[24:27],a[224:227],v[96:99],v[16:19]\nds_read_b128 a[220:223],v13 offset:39936\nC v106,v108,v109\nM v[64:67],a[200:203],a[136:139],v[64:67]\nv_exp_f32 v121,v121\nM v[64:67],a[204:207],a[140:143],v[64:67]\nbuffer_load_dwordx4 v1,s[84:87],s46 offen offset:128 lds\nM v[72:75],a[192:195],a[144:147],v[36:39]\n" \
"s_add_u32 m0,s22,0\nC v107,v110,v111\nC v112,v112,v113\nM v[72:75],a[196:199],a[148:151],v[72:75]\ns_mov_b32 s46,393216\nv_exp_f32 v122,v122\nM v[24:27],a[228:231],v[104:107],v[24:27]\nv_exp_f32 v123,v123\n" \
"M v[72:75],a[200:203],a[152:155],v[72:75]\nbuffer_load_dwordx4 v22,s[40:43],s45 offen offset:0 sc1 lds\nM v[72:75],a[204:207],a[156:159],v[72:75]\nv_exp_f32 v124,v124\nM v[80:83],a[192:195],a[160:163],v[40:43]\nC v113,v114,v115\nC v114,v116,v117\nM v[80:83],a[196:199],a[164:167],v[80:83]\n" \
"C v115,v118,v119\nC v120,v120,v121\nM v[80:83],a[200:203],a[168:171],v[80:83]\nbuffer_load_dwordx4 v22,s[40:43],s45 offen offset:1024 sc1 lds\nM v[80:83],a[204:207],a[172:175],v[80:83]\nv_exp_f32 v125,v125\nM v[88:91],a[192:195],a[176:179],v[44:47]\nds_read_b128 a[192:195],v14 offset:16384\n" \
"C v121,v122,v123\nM v[88:91],a[196:199],a[180:183],v[88:91]\nv_exp_f32 v126,v126\nM v[88:91],a[200:203],a[184:187],v[88:91]\nbuffer_load_dwordx4 v22,s[40:43],s45 offen offset:2048 sc1 lds\nM v[88:91],a[204:207],a[188:191],v[88:91]\nds_read_b128 a[196:199],v14 offset:17408\nC v122,v124,v125\n" \
"M v[24:27],a[232:235],v[112:115],v[24:27]\ns_waitcnt lgkmcnt(2)\nds_read_b128 a[200:203],v14 offset:18432\nv_xor_b32 v13,0x10000,v13\nM v[68:71],a[208:211],a[128:131],v[32:35]\nds_read_b64_tr_b16 a[0:1],v6 offset:49152\nM v[68:71],a[212:215],a[132:135],v[68:71]\nbuffer_load_dwordx4 v22,s[40:43],s45 offen offset:3072 sc1 lds\n" \
"M v[68:71],a[216:219],a[136:139],v[68:71]\ns_add_u32 m0,s22,32768\nds_read_b64_tr_b16 a[2:3],v7 offset:49152\nM v[68:71],a[220:223],a[140:143],v[68:71]\ns_add_u32 s45,s45,s49\nds_read_b64_tr_b16 a[4:5],v8 offset:49152\nM v[76:79],a[208:211],a[144:147],v[36:39]\nv_exp_f32 v127,v127\n" \
"M v[76:79],a[212:215],a[148:151],v[76:79]\nbuffer_store_dwordx4 v[48:51],v4,s[36:39],s73 offen offset:1024 sc1\nM v[76:79],a[216:219],a[152:155],v[76:79]\nds_read_b64_tr_b16 a[6:7],v9 offset:49152\nC v123,v126,v127\nM v[76:79],a[220:223],a[156:159],v[76:79]\nds_read_b64_tr_b16 a[8:9],v6 offset:51200\nds_read_b64_tr_b16 a[10:11],v7 offset:51200\n" \
"M v[24:27],a[236:239],v[120:123],v[24:27]\nds_read_b64_tr_b16 a[12:13],v8 offset:51200\nv_xor_b32 v6,0x10000,v6\nM v[84:87],a[208:211],a[160:163],v[40:43]\nds_read_b64_tr_b16 a[14:15],v9 offset:51200\nds_read_b128 a[204:207],v14 offset:19456\nM v[84:87],a[212:215],a[164:167],v[84:87]\nv_max3_f32 v15,v24,v25,v26\n" \
"v_max_f32 v15,v15,v27\nM v[84:87],a[216:219],a[168:171],v[84:87]\nv_cmp_nge_f32 vcc,0x53800000,v15\nv_xor_b32 v7,0x10000,v7\nM v[84:87],a[220:223],a[172:175],v[84:87]\nv_xor_b32 v8,0x10000,v8\nv_xor_b32 v9,0x10000,v9\nM v[92:95],a[208:211],a[176:179],v[44:47]\n" \
"ds_read_b128 a[208:211],v14 offset:20480\nM v[92:95],a[212:215],a[180:183],v[92:95]\ns_cbranch_vccnz Lcs45_%=\nLcr46_%=:\nds_read_b128 a[212:215],v14 offset:21504\nM v[92:95],a[216:219],a[184:187],v[92:95]\nds_read_b128 a[216:219],v14 offset:22528\nM v[92:95],a[220:223],a[188:191],v[92:95]\n" \
"s_waitcnt lgkmcnt(3)\nds_read_b128 a[220:223],v14 offset:23552\nM v[128:131],a[192:195],v[96:99],v[128:131]\nv_exp_f32 v64,v64\nM v[160:163],a[192:195],v[104:107],v[160:163]\nv_exp_f32 v65,v65\nM v[192:195],a[192:195],v[112:115],v[192:195]\nv_exp_f32 v66,v66\n" \
"M v[224:227],a[192:195],v[120:123],v[224:227]\nv_exp_f32 v67,v67\nM v[228:231],a[196:199],v[120:123],v[228:231]\nv_exp_f32 v68,v68\nM v[196:199],a[196:199],v[112:115],v[196:199]\nv_exp_f32 v69,v69\nM v[164:167],a[196:199],v[104:107],v[164:167]\ns_waitcnt vmcnt(9) lgkmcnt(0)\n" \
"v_exp_f32 v70,v70\nM v[132:135],a[196:199],v[96:99],v[132:135]\ns_barrier\nbuffer_load_dwordx4 v22,s[40:43],s48 offen offset:0 sc1 lds\nM v[136:139],a[200:203],v[96:99],v[136:139]\nv_exp_f32 v71,v71\nM v[168:171],a[200:203],v[104:107],v[168:171]\nv_exp_f32 v72,v72\n" \
"M v[200:203],a[200:203],v[112:115],v[200:203]\nv_exp_f32 v73,v73\nM v[232:235],a[200:203],v[120:123],v[232:235]\nbuffer_load_dwordx4 v22,s[40:43],s48 offen offset:1024 sc1 lds\nM v[236:239],a[204:207],v[120:123],v[236:239]\nds_read_b128 a[192:195],v13 offset:32768\nC v64,v64,v65\nM v[204:207],a[204:207],v[112:115],v[204:207]\n" \
"v_exp_f32 v74,v74\nM v[172:175],a[204:207],v[104:107],v[172:175]\nv_exp_f32 v75,v75\nM v[140:143],a[204:207],v[96:99],v[140:143]\nbuffer_load_dwordx4 v22,s[40:43],s48 offen offset:2048 sc1 lds\nv_exp_f32 v76,v76\nM v[144:147],a[208:211],v[96:99],v[144:147]\nv_exp_f32 v77,v77\n" \
"M v[176:179],a[208:211],v[104:107],v[176:179]\nds_read_b128 a[196:199],v13 offset:34816\nds_read_b128 a[200:203],v13 offset:36864\nM v[208:211],a[208:211],v[112:115],v[208:211]\nv_exp_f32 v78,v78\nM v[240:243],a[208:211],v[120:123],v[240:243]\nbuffer_load_dwordx4 v22,s[40:43],s48 offen offset:3072 sc1 lds\nv_exp_f32 v79,v79\n" \
"M v[244:247],a[212:215],v[120:123],v[244:247]\ns_add_u32 m0,s22,16384\nv_exp_f32 v80,v80\nM v[212:215],a[212:215],v[112:115],v[212:215]\ns_add_u32 s48,s48,s49\nds_read_b128 a[204:207],v13 offset:38912\nC v65,v66,v67\nM v[180:183],a[212:215],v[104:107],v[180:183]\n" \
"s_add_u32 s52,s52,1\nv_exp_f32 v81,v81\nM v[148:151],a[212:215],v[96:99],v[148:151]\nbuffer_load_dwordx4 v1,s[84:87],s46 offen offset:0 lds\nM v[152:155],a[216:219],v[96:99],v[152:155]\ns_add_u32 m0,s22,17280\nv_exp_f32 v82,v82\nM v[184:187],a[216:219],v[104:107],v[184:187]\n" \
"v_exp_f32 v83,v83\nM v[216:219],a[216:219],v[112:115],v[216:219]\nv_exp_f32 v84,v84\nM v[248:251],a[216:219],v[120:123],v[248:251]\nbuffer_load_dwordx4 v1,s[84:87],s46 offen offset:128 lds\nM v[252:255],a[220:223],v[120:123],v[252:255]\ns_add_u32 m0,s22,18432\nv_exp_f32 v85,v85\n" \
"M v[220:223],a[220:223],v[112:115],v[220:223]\ns_mov_b32 s46,458752\nv_exp_f32 v86,v86\nM v[188:191],a[220:223],v[104:107],v[188:191]\ns_cmp_eq_u32 s52,s56\nC v66,v68,v69\nC v67,v70,v71\nM v[156:159],a[220:223],v[96:99],v[156:159]\n" \
"s_waitcnt lgkmcnt(0)\nbuffer_load_dwordx4 v1,s[84:87],s46 offen offset:0 lds\nM v[96:99],a[192:195],a[128:131],v[32:35]\ns_cbranch_scc1 LEV_%=\nLEVR10_%=:\nds_read_b128 a[208:211],v13 offset:33792\nC v72,v72,v73\nM v[96:99],a[196:199],a[132:135],v[96:99]\n" \
"s_add_u32 m0,s22,19328\nds_read_b128 a[212:215],v13 offset:35840\nC v73,v74,v75\nM v[16:19],a[224:227],v[64:67],v[24:27]\nds_read_b128 a[216:219],v13 offset:37888\nC v74,v76,v77\nM v[96:99],a[200:203],a[136:139],v[96:99]\nv_exp_f32 v87,v87\n" \
"M v[96:99],a[204:207],a[140:143],v[96:99]\nbuffer_load_dwordx4 v1,s[84:87],s46 offen offset:128 lds\nM v[104:107],a[192:195],a[144:147],v[36:39]\ns_add_u32 m0,s22,49152\nds_read_b128 a[220:223],v13 offset:39936\nC v75,v78,v79\nv_exp_f32 v88,v88\nM v[104:107],a[196:199],a[148:151],v[104:107]\n" \
"s_mov_b32 s46,524288\nv_exp_f32 v89,v89\nM v[16:19],a[228:231],v[72:75],v[16:19]\nv_exp_f32 v90,v90\nM v[104:107],a[200:203],a[152:155],v[104:107]\nbuffer_load_dwordx4 v22,s[40:43],s45 offen offset:0 sc1 lds\nM v[104:107],a[204:207],a[156:159],v[104:107]\nv_exp_f32 v91,v91\n" \
"M v[112:115],a[192:195],a[160:163],v[40:43]\nv_exp_f32 v92,v92\nM v[112:115],a[196:199],a[164:167],v[112:115]\nC v80,v80,v81\nC v81,v82,v83\nM v[112:115],a[200:203],a[168:171],v[112:115]\nbuffer_load_dwordx4 v22,s[40:43],s45 offen offset:1024 sc1 lds\nM v[112:115],a[204:207],a[172:175],v[112:115]\n" \
"v_exp_f32 v93,v93\nM v[120:123],a[192:195],a[176:179],v[44:47]\nC v82,v84,v85\nC v83,v86,v87\nM v[120:123],a[196:199],a[180:183],v[120:123]\nv_exp_f32 v94,v94\nM v[120:123],a[200:203],a[184:187],v[120:123]\nbuffer_load_dwordx4 v22,s[40:43],s45 offen offset:2048 sc1 lds\n" \
"M v[120:123],a[204:207],a[188:191],v[120:123]\nC v88,v88,v89\nC v89,v90,v91\nM v[16:19],a[232:235],v[80:83],v[16:19]\ns_waitcnt lgkmcnt(0)\nds_read_b64_tr_b16 a[16:17],v6 offset:0\nds_read_b64_tr_b16 a[18:19],v7 offset:0\nM v[100:103],a[208:211],a[128:131],v[32:35]\n" \
"ds_read_b64_tr_b16 a[20:21],v8 offset:0\nC v90,v92,v93\nM v[100:103],a[212:215],a[132:135],v[100:103]\nbuffer_load_dwordx4 v22,s[40:43],s45 offen offset:3072 sc1 lds\nM v[100:103],a[216:219],a[136:139],v[100:103]\ns_add_u32 m0,s22,65536\nv_exp_f32 v95,v95\nM v[100:103],a[220:223],a[140:143],v[100:103]\n" \
"s_add_u32 s45,s45,s49\nds_read_b64_tr_b16 a[22:23],v9 offset:0\nds_read_b64_tr_b16 a[24:25],v6 offset:2048\nM v[108:111],a[208:211],a[144:147],v[36:39]\nds_read_b64_tr_b16 a[26:27],v7 offset:2048\nv_xor_b32 v14,0x10000,v14\nM v[108:111],a[212:215],a[148:151],v[108:111]\nds_read_b128 a[192:195],v14 offset:16384\n" \
"C v91,v94,v95\nM v[108:111],a[216:219],a[152:155],v[108:111]\nds_read_b128 a[196:199],v14 offset:17408\nv_xor_b32 v13,0x10000,v13\nM v[108:111],a[220:223],a[156:159],v[108:111]\nds_read_b128 a[200:203],v14 offset:18432\nM v[16:19],a[236:239],v[88:91],v[16:19]\nds_read_b64_tr_b16 a[28:29],v8 offset:2048\n" \
"ds_read_b64_tr_b16 a[30:31],v9 offset:2048\nM v[116:119],a[208:211],a[160:163],v[40:43]\nds_read_b128 a[204:207],v14 offset:19456\nM v[116:119],a[212:215],a[164:167],v[116:119]\nv_max3_f32 v15,v16,v17,v18\nv_max_f32 v15,v15,v19\nM v[116:119],a[216:219],a[168:171],v[116:119]\nv_cmp_nge_f32 vcc,0x53800000,v15\n" \
"M v[116:119],a[220:223],a[172:175],v[116:119]\nM v[124:127],a[208:211],a[176:179],v[44:47]\nds_read_b128 a[208:211],v14 offset:20480\nM v[124:127],a[212:215],a[180:183],v[124:127]\ns_cbranch_vccnz Lcs47_%=\nLcr48_%=:\nds_read_b128 a[212:215],v14 offset:21504\nM v[124:127],a[216:219],a[184:187],v[124:127]\n" \
"ds_read_b128 a[216:219],v14 offset:22528\nM v[124:127],a[220:223],a[188:191],v[124:127]\ns_waitcnt lgkmcnt(3)\nds_read_b128 a[220:223],v14 offset:23552\nv_xor_b32 v14,0x10000,v14\nM v[128:131],a[192:195],v[64:67],v[128:131]\nv_exp_f32 v96,v96\nM v[160:163],a[192:195],v[72:75],v[160:163]\n" \
"v_exp_f32 v97,v97\nM v[192:195],a[192:195],v[80:83],v[192:195]\nv_exp_f32 v98,v98\nM v[224:227],a[192:195],v[88:91],v[224:227]\nv_exp_f32 v99,v99\nM v[228:231],a[196:199],v[88:91],v[228:231]\nv_exp_f32 v100,v100\nM v[196:199],a[196:199],v[80:83],v[196:199]\n" \
"v_exp_f32 v101,v101\nM v[164:167],a[196:199],v[72:75],v[164:167]\ns_waitcnt vmcnt(8) lgkmcnt(0)\nv_exp_f32 v102,v102\nM v[132:135],a[196:199],v[64:67],v[132:135]\ns_barrier\nbuffer_load_dwordx4 v22,s[40:43],s48 offen offset:0 sc1 lds\nM v[136:139],a[200:203],v[64:67],v[136:139]\n" \
"v_exp_f32 v103,v103\nM v[168:171],a[200:203],v[72:75],v[168:171]\nv_exp_f32 v104,v104\nM v[200:203],a[200:203],v[80:83],v[200:203]\nv_exp_f32 v105,v105\nM v[232:235],a[200:203],v[88:91],v[232:235]\nbuffer_load_dwordx4 v22,s[40:43],s48 offen offset:1024 sc1 lds\nM v[236:239],a[204:207],v[88:91],v[236:239]\n" \
"ds_read_b128 a[192:195],v13 offset:0\nC v96,v96,v97\nM v[204:207],a[204:207],v[80:83],v[204:207]\nv_exp_f32 v106,v106\nM v[172:175],a[204:207],v[72:75],v[172:175]\nds_read_b128 a[196:199],v13 offset:2048\nds_read_b128 a[200:203],v13 offset:4096\nM v[140:143],a[204:207],v[64:67],v[140:143]\n" \
"buffer_load_dwordx4 v22,s[40:43],s48 offen offset:2048 sc1 lds\nM v[144:147],a[208:211],v[64:67],v[144:147]\nv_exp_f32 v107,v107\nM v[176:179],a[208:211],v[72:75],v[176:179]\nds_read_b128 a[204:207],v13 offset:6144\nC v97,v98,v99\nM v[208:211],a[208:211],v[80:83],v[208:211]\nv_exp_f32 v108,v108\n" \
"M v[240:243],a[208:211],v[88:91],v[240:243]\nbuffer_load_dwordx4 v22,s[40:43],s48 offen offset:3072 sc1 lds\nv_exp_f32 v109,v109\nM v[244:247],a[212:215],v[88:91],v[244:247]\ns_add_u32 m0,s22,98304\nv_exp_f32 v110,v110\nM v[212:215],a[212:215],v[80:83],v[212:215]\ns_add_u32 s48,s48,s49\n" \
"v_exp_f32 v111,v111\nM v[180:183],a[212:215],v[72:75],v[180:183]\nv_exp_f32 v112,v112\nM v[148:151],a[212:215],v[64:67],v[148:151]\nbuffer_load_dwordx4 v1,s[84:87],s46 offen offset:0 lds\nM v[152:155],a[216:219],v[64:67],v[152:155]\ns_add_u32 m0,s22,99200\nv_exp_f32 v113,v113\n" \
"M v[184:187],a[216:219],v[72:75],v[184:187]\nv_exp_f32 v114,v114\nM v[216:219],a[216:219],v[80:83],v[216:219]\nv_exp_f32 v115,v115\nM v[248:251],a[216:219],v[88:91],v[248:251]\nbuffer_load_dwordx4 v1,s[84:87],s46 offen offset:128 lds\nM v[252:255],a[220:223],v[88:91],v[252:255]\ns_add_u32 m0,s22,100352\n" \
"v_exp_f32 v116,v116\nM v[220:223],a[220:223],v[80:83],v[220:223]\ns_mov_b32 s46,589824\nC v98,v100,v101\nC v99,v102,v103\nM v[188:191],a[220:223],v[72:75],v[188:191]\ns_waitcnt lgkmcnt(0)\nv_exp_f32 v117,v117\n" \
"M v[156:159],a[220:223],v[64:67],v[156:159]\nbuffer_load_dwordx4 v1,s[84:87],s46 offen offset:0 lds\nM v[64:67],a[192:195],a[128:131],v[32:35]\ns_add_u32 m0,s22,101248\nds_read_b128 a[208:211],v13 offset:1024\nC v104,v104,v105\nM v[64:67],a[196:199],a[132:135],v[64:67]\nds_read_b128 a[212:215],v13 offset:3072\n" \
"C v105,v106,v107\nM v[24:27],a[224:227],v[96:99],v[16:19]\nds_read_b128 a[216:219],v13 offset:5120\nC v106,v108,v109\nM v[64:67],a[200:203],a[136:139],v[64:67]\nbuffer_load_dwordx4 v1,s[84:87],s46 offen offset:128 lds\nM v[64:67],a[204:207],a[140:143],v[64:67]\ns_add_u32 m0,s22,81920\n" \
"ds_read_b128 a[220:223],v13 offset:7168\nC v107,v110,v111\nM v[72:75],a[192:195],a[144:147],v[36:39]\ns_mov_b32 s46,655360\nv_exp_f32 v118,v118\nM v[72:75],a[196:199],a[148:151],v[72:75]\nv_exp_f32 v119,v119\nM v[24:27],a[228:231],v[104:107],v[24:27]\n" \
"buffer_load_dwordx4 v22,s[40:43],s45 offen offset:0 sc1 lds\nM v[72:75],a[200:203],a[152:155],v[72:75]\ns_waitcnt vmcnt(21)\nv_exp_f32 v120,v120\nM v[72:75],a[204:207],a[156:159],v[72:75]\nv_exp_f32 v121,v121\nM v[80:83],a[192:195],a[160:163],v[40:43]\nv_exp_f32 v122,v122\n" \
"M v[80:83],a[196:199],a[164:167],v[80:83]\nbuffer_store_dwordx2 v[52:53],v56,s[36:39],s95 offen offset:24 sc1\nM v[80:83],a[200:203],a[168:171],v[80:83]\nC v112,v112,v113\nC v113,v114,v115\nM v[80:83],a[204:207],a[172:175],v[80:83]\nbuffer_load_dwordx4 v22,s[40:43],s45 offen offset:1024 sc1 lds\nM v[88:91],a[192:195],a[176:179],v[44:47]\n" \
"ds_read_b128 a[192:195],v14 offset:32768\nC v114,v116,v117\nM v[88:91],a[196:199],a[180:183],v[88:91]\nv_exp_f32 v123,v123\nM v[88:91],a[200:203],a[184:187],v[88:91]\nds_read_b128 a[196:199],v14 offset:33792\nC v115,v118,v119\nM v[88:91],a[204:207],a[188:191],v[88:91]\n" \
"buffer_load_dwordx4 v22,s[40:43],s45 offen offset:2048 sc1 lds\nM v[24:27],a[232:235],v[112:115],v[24:27]\ns_waitcnt lgkmcnt(2)\nds_read_b128 a[200:203],v14 offset:34816\nC v120,v120,v121\nM v[68:71],a[208:211],a[128:131],v[32:35]\nv_exp_f32 v124,v124\nM v[68:71],a[212:215],a[132:135],v[68:71]\n" \
"v_exp_f32 v125,v125\nM v[68:71],a[216:219],a[136:139],v[68:71]\nv_exp_f32 v126,v126\nM v[68:71],a[220:223],a[140:143],v[68:71]\nbuffer_load_dwordx4 v22,s[40:43],s45 offen offset:3072 sc1 lds\nM v[76:79],a[208:211],a[144:147],v[36:39]\ns_add_u32 m0,s22,114688\nds_read_b64_tr_b16 a[32:33],v6 offset:49152\n" \
"C v121,v122,v123\nM v[76:79],a[212:215],a[148:151],v[76:79]\ns_add_u32 s45,s45,s49\nv_exp_f32 v127,v127\nM v[76:79],a[216:219],a[152:155],v[76:79]\nC v122,v124,v125\nC v123,v126,v127\nM v[76:79],a[220:223],a[156:159],v[76:79]\n" \
"ds_read_b64_tr_b16 a[34:35],v7 offset:49152\nds_read_b64_tr_b16 a[36:37],v8 offset:49152\nM v[24:27],a[236:239],v[120:123],v[24:27]\nds_read_b64_tr_b16 a[38:39],v9 offset:49152\nM v[84:87],a[208:211],a[160:163],v[40:43]\nds_read_b64_tr_b16 a[40:41],v6 offset:51200\nds_read_b64_tr_b16 a[42:43],v7 offset:51200\nM v[84:87],a[212:215],a[164:167],v[84:87]\n" \
"ds_read_b64_tr_b16 a[44:45],v8 offset:51200\nv_max3_f32 v15,v24,v25,v26\nM v[84:87],a[216:219],a[168:171],v[84:87]\nds_read_b64_tr_b16 a[46:47],v9 offset:51200\nv_max_f32 v15,v15,v27\nM v[84:87],a[220:223],a[172:175],v[84:87]\nds_read_b128 a[204:207],v14 offset:35840\nv_cmp_nge_f32 vcc,0x53800000,v15\n" \
"M v[92:95],a[208:211],a[176:179],v[44:47]\nds_read_b128 a[208:211],v14 offset:36864\nv_xor_b32 v6,0x10000,v6\nM v[92:95],a[212:215],a[180:183],v[92:95]\ns_cbranch_vccnz Lcs49_%=\nLcr50_%=:\nds_read_b128 a[212:215],v14 offset:37888\nv_xor_b32 v7,0x10000,v7\n" \
"M v[92:95],a[216:219],a[184:187],v[92:95]\nds_read_b128 a[216:219],v14 offset:38912\nv_xor_b32 v8,0x10000,v8\nM v[92:95],a[220:223],a[188:191],v[92:95]\ns_waitcnt lgkmcnt(5)\nds_read_b128 a[220:223],v14 offset:39936\nv_xor_b32 v9,0x10000,v9\nM v[128:131],a[192:195],v[96:99],v[128:131]\n" \
"v_exp_f32 v64,v64\nM v[160:163],a[192:195],v[104:107],v[160:163]\nv_exp_f32 v65,v65\nM v[192:195],a[192:195],v[112:115],v[192:195]\nv_exp_f32 v66,v66\nM v[224:227],a[192:195],v[120:123],v[224:227]\nv_exp_f32 v67,v67\nM v[228:231],a[196:199],v[120:123],v[228:231]\n" \
"v_exp_f32 v68,v68\nM v[196:199],a[196:199],v[112:115],v[196:199]\nv_exp_f32 v69,v69\nM v[164:167],a[196:199],v[104:107],v[164:167]\ns_waitcnt vmcnt(9) lgkmcnt(0)\nv_exp_f32 v70,v70\nM v[132:135],a[196:199],v[96:99],v[132:135]\ns_barrier\n" \
"buffer_load_dwordx4 v22,s[40:43],s48 offen offset:0 sc1 lds\nM v[136:139],a[200:203],v[96:99],v[136:139]\nv_exp_f32 v71,v71\nM v[168:171],a[200:203],v[104:107],v[168:171]\nv_exp_f32 v72,v72\nM v[200:203],a[200:203],v[112:115],v[200:203]\nv_exp_f32 v73,v73\nM v[232:235],a[200:203],v[120:123],v[232:235]\n" \
"buffer_load_dwordx4 v22,s[40:43],s48 offen offset:1024 sc1 lds\nM v[236:239],a[204:207],v[120:123],v[236:239]\nds_read_b128 a[192:195],v13 offset:49152\nC v64,v64,v65\nM v[204:207],a[204:207],v[112:115],v[204:207]\nv_exp_f32 v74,v74\nM v[172:175],a[204:207],v[104:107],v[172:175]\nds_read_b128 a[196:199],v13 offset:51200\n" \
"ds_read_b128 a[200:203],v13 offset:53248\nM v[140:143],a[204:207],v[96:99],v[140:143]\nbuffer_load_dwordx4 v22,s[40:43],s48 offen offset:2048 sc1 lds\nv_exp_f32 v75,v75\nM v[144:147],a[208:211],v[96:99],v[144:147]\nv_exp_f32 v76,v76\nM v[176:179],a[208:211],v[104:107],v[176:179]\nds_read_b128 a[204:207],v13 offset:55296\n" \
"C v65,v66,v67\nM v[208:211],a[208:211],v[112:115],v[208:211]\nv_exp_f32 v77,v77\nM v[240:243],a[208:211],v[120:123],v[240:243]\nbuffer_load_dwordx4 v22,s[40:43],s48 offen offset:3072 sc1 lds\nv_exp_f32 v78,v78\nM v[244:247],a[212:215],v[120:123],v[244:247]\ns_add_u32 m0,s22,0\n" \
"v_exp_f32 v79,v79\nM v[212:215],a[212:215],v[112:115],v[212:215]\ns_add_u32 s48,s48,s49\nv_exp_f32 v80,v80\nM v[180:183],a[212:215],v[104:107],v[180:183]\ns_add_u32 s52,s52,1\nv_exp_f32 v81,v81\nM v[148:151],a[212:215],v[96:99],v[148:151]\n" \
"buffer_load_dwordx4 v1,s[84:87],s46 offen offset:0 lds\nM v[152:155],a[216:219],v[96:99],v[152:155]\ns_add_u32 m0,s22,896\nv_exp_f32 v82,v82\nM v[184:187],a[216:219],v[104:107],v[184:187]\nv_exp_f32 v83,v83\nM v[216:219],a[216:219],v[112:115],v[216:219]\nv_exp_f32 v84,v84\n" \
"M v[248:251],a[216:219],v[120:123],v[248:251]\nbuffer_load_dwordx4 v1,s[84:87],s46 offen offset:128 lds\nM v[252:255],a[220:223],v[120:123],v[252:255]\ns_add_u32 m0,s22,2048\nv_exp_f32 v85,v85\nM v[220:223],a[220:223],v[112:115],v[220:223]\ns_mov_b32 s46,720896\nv_exp_f32 v86,v86\n" \
"M v[188:191],a[220:223],v[104:107],v[188:191]\ns_cmp_eq_u32 s52,s56\nC v66,v68,v69\nC v67,v70,v71\nM v[156:159],a[220:223],v[96:99],v[156:159]\nLG18_%=:\ns_cbranch_execz LHR2_%=\ns_waitcnt lgkmcnt(0)\n" \
"buffer_load_dwordx4 v1,s[84:87],s46 offen offset:0 lds\nM v[96:99],a[192:195],a[128:131],v[32:35]\ns_cbranch_scc1 LEV_%=\nLEVR11_%=:\nds_read_b128 a[208:211],v13 offset:50176\nC v72,v72,v73\nM v[96:99],a[196:199],a[132:135],v[96:99]\ns_add_u32 m0,s22,2944\n" \
"ds_read_b128 a[212:215],v13 offset:52224\nC v73,v74,v75\nM v[16:19],a[224:227],v[64:67],v[24:27]\nds_read_b128 a[216:219],v13 offset:54272\nC v74,v76,v77\nM v[96:99],a[200:203],a[136:139],v[96:99]\nv_exp_f32 v87,v87\nM v[96:99],a[204:207],a[140:143],v[96:99]\n" \
"buffer_load_dwordx4 v1,s[84:87],s46 offen offset:128 lds\nM v[104:107],a[192:195],a[144:147],v[36:39]\ns_add_u32 m0,s22,32768\nds_read_b128 a[220:223],v13 offset:56320\nC v75,v78,v79\nv_exp_f32 v88,v88\nM v[104:107],a[196:199],a[148:151],v[104:107]\ns_mov_b32 s46,786432\n" \
"v_exp_f32 v89,v89\nM v[16:19],a[228:231],v[72:75],v[16:19]\nv_exp_f32 v90,v90\nM v[104:107],a[200:203],a[152:155],v[104:107]\nbuffer_load_dwordx4 v22,s[40:43],s45 offen offset:0 sc1 lds\nM v[104:107],a[204:207],a[156:159],v[104:107]\nv_exp_f32 v91,v91\nM v[112:115],a[192:195],a[160:163],v[40:43]\n" \
"v_exp_f32 v92,v92\nM v[112:115],a[196:199],a[164:167],v[112:115]\nC v80,v80,v81\nC v81,v82,v83\nM v[112:115],a[200:203],a[168:171],v[112:115]\nbuffer_load_dwordx4 v22,s[40:43],s45 offen offset:1024 sc1 lds\nM v[112:115],a[204:207],a[172:175],v[112:115]\nv_exp_f32 v93,v93\n" \
"M v[120:123],a[192:195],a[176:179],v[44:47]\nC v82,v84,v85\nC v83,v86,v87\nM v[120:123],a[196:199],a[180:183],v[120:123]\nv_exp_f32 v94,v94\nM v[120:123],a[200:203],a[184:187],v[120:123]\nbuffer_load_dwordx4 v22,s[40:43],s45 offen offset:2048 sc1 lds\nM v[120:123],a[204:207],a[188:191],v[120:123]\n" \
"C v88,v88,v89\nC v89,v90,v91\nM v[16:19],a[232:235],v[80:83],v[16:19]\ns_waitcnt lgkmcnt(0)\nds_read_b64_tr_b16 a[48:49],v6 offset:16384\nds_read_b64_tr_b16 a[50:51],v7 offset:16384\nM v[100:103],a[208:211],a[128:131],v[32:35]\nds_read_b64_tr_b16 a[52:53],v8 offset:16384\n" \
"C v90,v92,v93\nM v[100:103],a[212:215],a[132:135],v[100:103]\nbuffer_load_dwordx4 v22,s[40:43],s45 offen offset:3072 sc1 lds\nM v[100:103],a[216:219],a[136:139],v[100:103]\ns_add_u32 m0,s22,16384\nv_exp_f32 v95,v95\nM v[100:103],a[220:223],a[140:143],v[100:103]\ns_add_u32 s45,s45,s49\n" \
"ds_read_b64_tr_b16 a[54:55],v9 offset:16384\nds_read_b64_tr_b16 a[56:57],v6 offset:18432\nM v[108:111],a[208:211],a[144:147],v[36:39]\nds_read_b64_tr_b16 a[58:59],v7 offset:18432\nv_xor_b32 v14,0x10000,v14\nM v[108:111],a[212:215],a[148:151],v[108:111]\nds_read_b128 a[192:195],v14 offset:0\nC v91,v94,v95\n" \
"M v[108:111],a[216:219],a[152:155],v[108:111]\nds_read_b128 a[196:199],v14 offset:1024\nv_xor_b32 v6,0x10000,v6\nM v[108:111],a[220:223],a[156:159],v[108:111]\nds_read_b128 a[200:203],v14 offset:2048\nv_xor_b32 v7,0x10000,v7\nM v[16:19],a[236:239],v[88:91],v[16:19]\nds_read_b64_tr_b16 a[60:61],v8 offset:18432\n" \
"ds_read_b64_tr_b16 a[62:63],v9 offset:18432\nM v[116:119],a[208:211],a[160:163],v[40:43]\nds_read_b128 a[204:207],v14 offset:3072\nv_xor_b32 v8,0x10000,v8\nM v[116:119],a[212:215],a[164:167],v[116:119]\nv_max3_f32 v15,v16,v17,v18\nv_max_f32 v15,v15,v19\nM v[116:119],a[216:219],a[168:171],v[116:119]\n" \
"v_cmp_nge_f32 vcc,0x53800000,v15\nv_xor_b32 v9,0x10000,v9\nM v[116:119],a[220:223],a[172:175],v[116:119]\nM v[124:127],a[208:211],a[176:179],v[44:47]\ns_waitcnt lgkmcnt(3)\nds_read_b128 a[208:211],v14 offset:4096\nM v[124:127],a[212:215],a[180:183],v[124:127]\ns_cbranch_vccnz Lcs51_%=\n" \
"Lcr52_%=:\nds_read_b128 a[212:215],v14 offset:5120\nM v[124:127],a[216:219],a[184:187],v[124:127]\ns_cmp_eq_u32 s52,s55\nds_read_b128 a[216:219],v14 offset:6144\nM v[124:127],a[220:223],a[188:191],v[124:127]\ns_cbranch_scc1 LH1_%=\nLH1R12_%=:\n" \
"ds_read_b128 a[220:223],v14 offset:7168\nv_xor_b32 v13,0x10000,v13\nM v[128:131],a[192:195],v[64:67],v[128:131]\nv_exp_f32 v96,v96\nM v[160:163],a[192:195],v[72:75],v[160:163]\nv_exp_f32 v97,v97\nM v[192:195],a[192:195],v[80:83],v[192:195]\nv_exp_f32 v98,v98\n" \
"M v[224:227],a[192:195],v[88:91],v[224:227]\nv_exp_f32 v99,v99\nM v[228:231],a[196:199],v[88:91],v[228:231]\nv_exp_f32 v100,v100\nM v[196:199],a[196:199],v[80:83],v[196:199]\nv_exp_f32 v101,v101\nM v[164:167],a[196:199],v[72:75],v[164:167]\ns_waitcnt vmcnt(8) lgkmcnt(0)\n" \
"v_exp_f32 v102,v102\nM v[132:135],a[196:199],v[64:67],v[132:135]\ns_barrier\nbuffer_load_dwordx4 v22,s[40:43],s48 offen offset:0 sc1 lds\nM v[136:139],a[200:203],v[64:67],v[136:139]\nv_exp_f32 v103,v103\nM v[168:171],a[200:203],v[72:75],v[168:171]\nv_exp_f32 v104,v104\n" \
"M v[200:203],a[200:203],v[80:83],v[200:203]\nv_exp_f32 v105,v105\nM v[232:235],a[200:203],v[88:91],v[232:235]\nbuffer_load_dwordx4 v22,s[40:43],s48 offen offset:1024 sc1 lds\nM v[236:239],a[204:207],v[88:91],v[236:239]\nds_read_b128 a[192:195],v13 offset:16384\nC v96,v96,v97\nM v[204:207],a[204:207],v[80:83],v[204:207]\n" \
"v_exp_f32 v106,v106\nM v[172:175],a[204:207],v[72:75],v[172:175]\nv_exp_f32 v107,v107\nM v[140:143],a[204:207],v[64:67],v[140:143]\nbuffer_load_dwordx4 v22,s[40:43],s48 offen offset:2048 sc1 lds\nv_exp_f32 v108,v108\nM v[144:147],a[208:211],v[64:67],v[144:147]\nv_exp_f32 v109,v109\n" \
"M v[176:179],a[208:211],v[72:75],v[176:179]\nds_read_b128 a[196:199],v13 offset:18432\nds_read_b128 a[200:203],v13 offset:20480\nM v[208:211],a[208:211],v[80:83],v[208:211]\nv_exp_f32 v110,v110\nM v[240:243],a[208:211],v[88:91],v[240:243]\nbuffer_load_dwordx4 v22,s[40:43],s48 offen offset:3072 sc1 lds\nv_exp_f32 v111,v111\n" \
"M v[244:247],a[212:215],v[88:91],v[244:247]\ns_add_u32 m0,s22,49152\nv_exp_f32 v112,v112\nM v[212:215],a[212:215],v[80:83],v[212:215]\ns_add_u32 s48,s48,s49\nds_read_b128 a[204:207],v13 offset:22528\nC v97,v98,v99\nM v[180:183],a[212:215],v[72:75],v[180:183]\n" \
"v_exp_f32 v113,v113\nM v[148:151],a[212:215],v[64:67],v[148:151]\nbuffer_load_dwordx4 v1,s[84:87],s46 offen offset:0 lds\nM v[152:155],a[216:219],v[64:67],v[152:155]\ns_add_u32 m0,s22,50048\nv_exp_f32 v114,v114\nM v[184:187],a[216:219],v[72:75],v[184:187]\nv_exp_f32 v115,v115\n" \
"M v[216:219],a[216:219],v[80:83],v[216:219]\nv_exp_f32 v116,v116\nM v[248:251],a[216:219],v[88:91],v[248:251]\nbuffer_load_dwordx4 v1,s[84:87],s46 offen offset:128 lds\nM v[252:255],a[220:223],v[88:91],v[252:255]\ns_add_u32 m0,s22,51200\nv_exp_f32 v117,v117\nM v[220:223],a[220:223],v[80:83],v[220:223]\n" \
"s_mov_b32 s46,851968\nv_exp_f32 v118,v118\nM v[188:191],a[220:223],v[72:75],v[188:191]\ns_waitcnt lgkmcnt(0)\nC v98,v100,v101\nC v99,v102,v103\nM v[156:159],a[220:223],v[64:67],v[156:159]\nbuffer_load_dwordx4 v1,s[84:87],s46 offen offset:0 lds\n" \
"M v[64:67],a[192:195],a[128:131],v[32:35]\ns_add_u32 m0,s22,52096\nds_read_b128 a[208:211],v13 offset:17408\nC v104,v104,v105\nM v[64:67],a[196:199],a[132:135],v[64:67]\nds_read_b128 a[212:215],v13 offset:19456\nC v105,v106,v107\nM v[24:27],a[224:227],v[96:99],v[16:19]\n" \
"ds_read_b128 a[216:219],v13 offset:21504\nC v106,v108,v109\nM v[64:67],a[200:203],a[136:139],v[64:67]\nv_exp_f32 v119,v119\nM v[64:67],a[204:207],a[140:143],v[64:67]\nbuffer_load_dwordx4 v1,s[84:87],s46 offen offset:128 lds\nM v[72:75],a[192:195],a[144:147],v[36:39]\ns_add_u32 m0,s22,65536\n" \
"ds_read_b128 a[220:223],v13 offset:23552\nC v107,v110,v111\nv_exp_f32 v120,v120\nM v[72:75],a[196:199],a[148:151],v[72:75]\ns_mov_b32 s46,917504\nv_exp_f32 v121,v121\nM v[24:27],a[228:231],v[104:107],v[24:27]\nv_exp_f32 v122,v122\n" \
"M v[72:75],a[200:203],a[152:155],v[72:75]\nbuffer_load_dwordx4 v22,s[40:43],s45 offen offset:0 sc1 lds\nM v[72:75],a[204:207],a[156:159],v[72:75]\nv_exp_f32 v123,v123\nM v[80:83],a[192:195],a[160:163],v[40:43]\nv_exp_f32 v124,v124\nM v[80:83],a[196:199],a[164:167],v[80:83]\nC v112,v112,v113\n" \
"C v113,v114,v115\nM v[80:83],a[200:203],a[168:171],v[80:83]\nbuffer_load_dwordx4 v22,s[40:43],s45 offen offset:1024 sc1 lds\nM v[80:83],a[204:207],a[172:175],v[80:83]\nv_exp_f32 v125,v125\nM v[88:91],a[192:195],a[176:179],v[44:47]\nds_read_b128 a[192:195],v14 offset:49152\nC v114,v116,v117\n" \
"M v[88:91],a[196:199],a[180:183],v[88:91]\nC v115,v118,v119\nC v120,v120,v121\nM v[88:91],a[200:203],a[184:187],v[88:91]\nbuffer_load_dwordx4 v22,s[40:43],s45 offen offset:2048 sc1 lds\nM v[88:91],a[204:207],a[188:191],v[88:91]\nds_read_b128 a[196:199],v14 offset:50176\nC v121,v122,v123\n" \
"M v[24:27],a[232:235],v[112:115],v[24:27]\ns_waitcnt lgkmcnt(2)\nds_read_b128 a[200:203],v14 offset:51200\nC v122,v124,v125\nM v[68:71],a[208:211],a[128:131],v[32:35]\nv_exp_f32 v126,v126\nM v[68:71],a[212:215],a[132:135],v[68:71]\nbuffer_load_dwordx4 v22,s[40:43],s45 offen offset:3072 sc1 lds\n" \
"M v[68:71],a[216:219],a[136:139],v[68:71]\ns_add_u32 m0,s22,98304\nv_exp_f32 v127,v127\nM v[68:71],a[220:223],a[140:143],v[68:71]\ns_add_u32 s45,s45,s49\nds_read_b64_tr_b16 a[64:65],v6 offset:32768\nds_read_b64_tr_b16 a[66:67],v7 offset:32768\nM v[76:79],a[208:211],a[144:147],v[36:39]\n" \
"ds_read_b64_tr_b16 a[68:69],v8 offset:32768\nC v123,v126,v127\nM v[76:79],a[212:215],a[148:151],v[76:79]\nds_read_b64_tr_b16 a[70:71],v9 offset:32768\nds_read_b64_tr_b16 a[72:73],v6 offset:34816\nM v[76:79],a[216:219],a[152:155],v[76:79]\nds_read_b64_tr_b16 a[74:75],v7 offset:34816\nv_xor_b32 v13,0x10000,v13\n" \
"M v[76:79],a[220:223],a[156:159],v[76:79]\nds_read_b64_tr_b16 a[76:77],v8 offset:34816\nv_xor_b32 v6,0x10000,v6\nM v[24:27],a[236:239],v[120:123],v[24:27]\nds_read_b64_tr_b16 a[78:79],v9 offset:34816\nds_read_b128 a[204:207],v14 offset:52224\nM v[84:87],a[208:211],a[160:163],v[40:43]\nv_xor_b32 v7,0x10000,v7\n" \
"v_xor_b32 v8,0x10000,v8\nM v[84:87],a[212:215],a[164:167],v[84:87]\nv_max3_f32 v15,v24,v25,v26\nv_max_f32 v15,v15,v27\nM v[84:87],a[216:219],a[168:171],v[84:87]\nv_cmp_nge_f32 vcc,0x53800000,v15\nv_xor_b32 v9,0x10000,v9\nM v[84:87],a[220:223],a[172:175],v[84:87]\n" \
"M v[92:95],a[208:211],a[176:179],v[44:47]\nds_read_b128 a[208:211],v14 offset:53248\nM v[92:95],a[212:215],a[180:183],v[92:95]\ns_cbranch_vccnz Lcs53_%=\nLcr54_%=:\nds_read_b128 a[212:215],v14 offset:54272\nM v[92:95],a[216:219],a[184:187],v[92:95]\nds_read_b128 a[216:219],v14 offset:55296\n" \
"M v[92:95],a[220:223],a[188:191],v[92:95]\ns_waitcnt lgkmcnt(3)\nds_read_b128 a[220:223],v14 offset:56320\nM v[128:131],a[192:195],v[96:99],v[128:131]\nv_exp_f32 v64,v64\nM v[160:163],a[192:195],v[104:107],v[160:163]\nv_exp_f32 v65,v65\nM v[192:195],a[192:195],v[112:115],v[192:195]\n" \
"v_exp_f32 v66,v66\nM v[224:227],a[192:195],v[120:123],v[224:227]\nv_exp_f32 v67,v67\nM v[228:231],a[196:199],v[120:123],v[228:231]\nv_exp_f32 v68,v68\nM v[196:199],a[196:199],v[112:115],v[196:199]\nv_exp_f32 v69,v69\nM v[164:167],a[196:199],v[104:107],v[164:167]\n" \
"s_waitcnt vmcnt(8) lgkmcnt(0)\nv_exp_f32 v70,v70\nM v[132:135],a[196:199],v[96:99],v[132:135]\ns_barrier\nbuffer_load_dwordx4 v22,s[40:43],s48 offen offset:0 sc1 lds\nM v[136:139],a[200:203],v[96:99],v[136:139]\nv_exp_f32 v71,v71\nM v[168:171],a[200:203],v[104:107],v[168:171]\n" \
"v_exp_f32 v72,v72\nM v[200:203],a[200:203],v[112:115],v[200:203]\nv_exp_f32 v73,v73\nM v[232:235],a[200:203],v[120:123],v[232:235]\nbuffer_load_dwordx4 v22,s[40:43],s48 offen offset:1024 sc1 lds\nv_exp_f32 v74,v74\nM v[236:239],a[204:207],v[120:123],v[236:239]\nds_read_b128 a[192:195],v13 offset:32768\n" \
"C v64,v64,v65\nM v[204:207],a[204:207],v[112:115],v[204:207]\nv_exp_f32 v75,v75\nM v[172:175],a[204:207],v[104:107],v[172:175]\nv_exp_f32 v76,v76\nM v[140:143],a[204:207],v[96:99],v[140:143]\nbuffer_load_dwordx4 v22,s[40:43],s48 offen offset:2048 sc1 lds\nv_exp_f32 v77,v77\n" \
"M v[144:147],a[208:211],v[96:99],v[144:147]\nv_exp_f32 v78,v78\nM v[176:179],a[208:211],v[104:107],v[176:179]\nds_read_b128 a[196:199],v13 offset:34816\nds_read_b128 a[200:203],v13 offset:36864\nM v[208:211],a[208:211],v[112:115],v[208:211]\nv_exp_f32 v79,v79\nM v[240:243],a[208:211],v[120:123],v[240:243]\n" \
"buffer_load_dwordx4 v22,s[40:43],s48 offen offset:3072 sc1 lds\nv_exp_f32 v80,v80\nM v[244:247],a[212:215],v[120:123],v[244:247]\ns_add_u32 m0,s22,81920\nv_exp_f32 v81,v81\nM v[212:215],a[212:215],v[112:115],v[212:215]\ns_add_u32 s48,s48,s49\nds_read_b128 a[204:207],v13 offset:38912\n" \
"C v65,v66,v67\nM v[180:183],a[212:215],v[104:107],v[180:183]\ns_add_u32 s52,s52,1\nv_exp_f32 v82,v82\nM v[148:151],a[212:215],v[96:99],v[148:151]\nbuffer_load_dwordx4 v1,s[84:87],s46 offen offset:0 lds\nM v[152:155],a[216:219],v[96:99],v[152:155]\ns_add_u32 m0,s22,82816\n" \
"v_exp_f32 v83,v83\nM v[184:187],a[216:219],v[104:107],v[184:187]\nv_exp_f32 v84,v84\nM v[216:219],a[216:219],v[112:115],v[216:219]\nv_exp_f32 v85,v85\nM v[248:251],a[216:219],v[120:123],v[248:251]\nbuffer_load_dwordx4 v1,s[84:87],s46 offen offset:128 lds\nM v[252:255],a[220:223],v[120:123],v[252:255]\n" \
"s_add_u32 m0,s22,83968\nv_exp_f32 v86,v86\nM v[220:223],a[220:223],v[112:115],v[220:223]\ns_mov_b32 s46,983040\nv_exp_f32 v87,v87\nM v[188:191],a[220:223],v[104:107],v[188:191]\ns_cmp_eq_u32 s52,s56\nds_read_b128 a[208:211],v13 offset:33792\n" \
"C v66,v68,v69\nM v[156:159],a[220:223],v[96:99],v[156:159]\ns_waitcnt lgkmcnt(1)\nbuffer_load_dwordx4 v1,s[84:87],s46 offen offset:0 lds\nM v[96:99],a[192:195],a[128:131],v[32:35]\ns_cbranch_scc1 LEV_%=\nLEVR12_%=:\nC v67,v70,v71\n" \
"C v72,v72,v73\nM v[96:99],a[196:199],a[132:135],v[96:99]\ns_add_u32 m0,s22,84864\nds_read_b128 a[212:215],v13 offset:35840\nds_read_b128 a[216:219],v13 offset:37888\nM v[16:19],a[224:227],v[64:67],v[24:27]\nv_exp_f32 v88,v88\nM v[96:99],a[200:203],a[136:139],v[96:99]\n" \
"buffer_load_dwordx4 v1,s[84:87],s46 offen offset:128 lds\nM v[96:99],a[204:207],a[140:143],v[96:99]\ns_add_u32 m0,s22,114688\nds_read_b128 a[220:223],v13 offset:39936\nC v73,v74,v75\nM v[104:107],a[192:195],a[144:147],v[36:39]\ns_mov_b32 s46,1048576\nC v74,v76,v77\n" \
"C v75,v78,v79\nv_exp_f32 v89,v89\nM v[104:107],a[196:199],a[148:151],v[104:107]\nv_exp_f32 v90,v90\nM v[16:19],a[228:231],v[72:75],v[16:19]\nbuffer_load_dwordx4 v22,s[40:43],s45 offen offset:0 sc1 lds\nM v[104:107],a[200:203],a[152:155],v[104:107]\nv_exp_f32 v91,v91\n" \
"M v[104:107],a[204:207],a[156:159],v[104:107]\nv_exp_f32 v92,v92\nM v[112:115],a[192:195],a[160:163],v[40:43]\nC v80,v80,v81\nC v81,v82,v83\nM v[112:115],a[196:199],a[164:167],v[112:115]\nbuffer_load_dwordx4 v22,s[40:43],s45 offen offset:1024 sc1 lds\nM v[112:115],a[200:203],a[168:171],v[112:115]\n" \
"v_exp_f32 v93,v93\nM v[112:115],a[204:207],a[172:175],v[112:115]\nC v82,v84,v85\nC v83,v86,v87\nM v[120:123],a[192:195],a[176:179],v[44:47]\nv_exp_f32 v94,v94\nM v[120:123],a[196:199],a[180:183],v[120:123]\nbuffer_load_dwordx4 v22,s[40:43],s45 offen offset:2048 sc1 lds\n" \
"M v[120:123],a[200:203],a[184:187],v[120:123]\nC v88,v88,v89\nC v89,v90,v91\nM v[120:123],a[204:207],a[188:191],v[120:123]\nds_read_b64_tr_b16 a[80:81],v6 offset:0\nv_xor_b32 v14,0x10000,v14\nM v[16:19],a[232:235],v[80:83],v[16:19]\ns_waitcnt lgkmcnt(1)\n" \
"ds_read_b128 a[192:195],v14 offset:16384\nds_read_b128 a[196:199],v14 offset:17408\nM v[100:103],a[208:211],a[128:131],v[32:35]\nbuffer_load_dwordx4 v22,s[40:43],s45 offen offset:3072 sc1 lds\nM v[100:103],a[212:215],a[132:135],v[100:103]\ns_add_u32 m0,s22,0\nv_exp_f32 v95,v95\nM v[100:103],a[216:219],a[136:139],v[100:103]\n" \
"s_add_u32 s45,s45,s49\nds_read_b128 a[200:203],v14 offset:18432\nC v90,v92,v93\nM v[100:103],a[220:223],a[140:143],v[100:103]\nds_read_b64_tr_b16 a[82:83],v7 offset:0\nC v91,v94,v95\nM v[108:111],a[208:211],a[144:147],v[36:39]\nds_read_b64_tr_b16 a[84:85],v8 offset:0\n" \
"ds_read_b64_tr_b16 a[86:87],v9 offset:0\nM v[108:111],a[212:215],a[148:151],v[108:111]\nds_read_b64_tr_b16 a[88:89],v6 offset:2048\nv_xor_b32 v13,0x10000,v13\nM v[108:111],a[216:219],a[152:155],v[108:111]\nds_read_b64_tr_b16 a[90:91],v7 offset:2048\nM v[108:111],a[220:223],a[156:159],v[108:111]\nds_read_b64_tr_b16 a[92:93],v8 offset:2048\n" \
"ds_read_b64_tr_b16 a[94:95],v9 offset:2048\nM v[16:19],a[236:239],v[88:91],v[16:19]\nds_read_b128 a[204:207],v14 offset:19456\nM v[116:119],a[208:211],a[160:163],v[40:43]\nM v[116:119],a[212:215],a[164:167],v[116:119]\nv_max3_f32 v15,v16,v17,v18\nv_max_f32 v15,v15,v19\nM v[116:119],a[216:219],a[168:171],v[116:119]\n" \
"v_cmp_nge_f32 vcc,0x53800000,v15\nM v[116:119],a[220:223],a[172:175],v[116:119]\nM v[124:127],a[208:211],a[176:179],v[44:47]\nds_read_b128 a[208:211],v14 offset:20480\nM v[124:127],a[212:215],a[180:183],v[124:127]\ns_cbranch_vccnz Lcs55_%=\nLcr56_%=:\nds_read_b128 a[212:215],v14 offset:21504\n" \
"M v[124:127],a[216:219],a[184:187],v[124:127]\nds_read_b128 a[216:219],v14 offset:22528\nM v[124:127],a[220:223],a[188:191],v[124:127]\ns_waitcnt lgkmcnt(3)\nds_read_b128 a[220:223],v14 offset:23552\nv_xor_b32 v14,0x10000,v14\nM v[128:131],a[192:195],v[64:67],v[128:131]\nv_exp_f32 v96,v96\n" \
"M v[160:163],a[192:195],v[72:75],v[160:163]\nv_exp_f32 v97,v97\nM v[192:195],a[192:195],v[80:83],v[192:195]\nv_exp_f32 v98,v98\nM v[224:227],a[192:195],v[88:91],v[224:227]\nv_exp_f32 v99,v99\nM v[228:231],a[196:199],v[88:91],v[228:231]\nv_exp_f32 v100,v100\n" \
"M v[196:199],a[196:199],v[80:83],v[196:199]\nv_exp_f32 v101,v101\nM v[164:167],a[196:199],v[72:75],v[164:167]\ns_waitcnt vmcnt(8) lgkmcnt(0)\nv_exp_f32 v102,v102\nM v[132:135],a[196:199],v[64:67],v[132:135]\ns_barrier\nbuffer_load_dwordx4 v22,s[40:43],s48 offen offset:0 sc1 lds\n" \
"M v[136:139],a[200:203],v[64:67],v[136:139]\nv_exp_f32 v103,v103\nM v[168:171],a[200:203],v[72:75],v[168:171]\nv_exp_f32 v104,v104\nM v[200:203],a[200:203],v[80:83],v[200:203]\nv_exp_f32 v105,v105\nM v[232:235],a[200:203],v[88:91],v[232:235]\nbuffer_load_dwordx4 v22,s[40:43],s48 offen offset:1024 sc1 lds\n" \
"M v[236:239],a[204:207],v[88:91],v[236:239]\nds_read_b128 a[192:195],v13 offset:0\nC v96,v96,v97\nM v[204:207],a[204:207],v[80:83],v[204:207]\nv_exp_f32 v106,v106\nM v[172:175],a[204:207],v[72:75],v[172:175]\nv_exp_f32 v107,v107\nM v[140:143],a[204:207],v[64:67],v[140:143]\n" \
"buffer_load_dwordx4 v22,s[40:43],s48 offen offset:2048 sc1 lds\nv_exp_f32 v108,v108\nM v[144:147],a[208:211],v[64:67],v[144:147]\nv_exp_f32 v109,v109\nM v[176:179],a[208:211],v[72:75],v[176:179]\nds_read_b128 a[196:199],v13 offset:2048\nds_read_b128 a[200:203],v13 offset:4096\nM v[208:211],a[208:211],v[80:83],v[208:211]\n" \
"v_exp_f32 v110,v110\nM v[240:243],a[208:211],v[88:91],v[240:243]\nbuffer_load_dwordx4 v22,s[40:43],s48 offen offset:3072 sc1 lds\nv_exp_f32 v111,v111\nM v[244:247],a[212:215],v[88:91],v[244:247]\ns_add_u32 m0,s22,32768\nv_exp_f32 v112,v112\nM v[212:215],a[212:215],v[80:83],v[212:215]\n" \
"s_add_u32 s48,s48,s49\nds_read_b128 a[204:207],v13 offset:6144\nC v97,v98,v99\nM v[180:183],a[212:215],v[72:75],v[180:183]\nv_exp_f32 v113,v113\nM v[148:151],a[212:215],v[64:67],v[148:151]\nbuffer_load_dwordx4 v22,s[40:43],s45 offen offset:0 sc1 lds\nM v[152:155],a[216:219],v[64:67],v[152:155]\n" \
"v_exp_f32 v114,v114\nM v[184:187],a[216:219],v[72:75],v[184:187]\nv_exp_f32 v115,v115\nM v[216:219],a[216:219],v[80:83],v[216:219]\nv_exp_f32 v116,v116\nM v[248:251],a[216:219],v[88:91],v[248:251]\nbuffer_load_dwordx4 v22,s[40:43],s45 offen offset:1024 sc1 lds\nM v[252:255],a[220:223],v[88:91],v[252:255]\n" \
"v_exp_f32 v117,v117\nM v[220:223],a[220:223],v[80:83],v[220:223]\nC v98,v100,v101\nC v99,v102,v103\nM v[188:191],a[220:223],v[72:75],v[188:191]\ns_waitcnt lgkmcnt(0)\nv_exp_f32 v118,v118\nM v[156:159],a[220:223],v[64:67],v[156:159]\n" \
"buffer_load_dwordx4 v22,s[40:43],s45 offen offset:2048 sc1 lds\nM v[64:67],a[192:195],a[128:131],v[32:35]\nds_read_b128 a[208:211],v13 offset:1024\nC v104,v104,v105\nM v[64:67],a[196:199],a[132:135],v[64:67]\nds_read_b128 a[212:215],v13 offset:3072\nC v105,v106,v107\nM v[24:27],a[224:227],v[96:99],v[16:19]\n" \
"ds_read_b128 a[216:219],v13 offset:5120\nC v106,v108,v109\nM v[64:67],a[200:203],a[136:139],v[64:67]\nbuffer_load_dwordx2 v[10:11],v5,s[36:39],0 offen sc1\nM v[64:67],a[204:207],a[140:143],v[64:67]\nds_read_b128 a[220:223],v13 offset:7168\nC v107,v110,v111\nM v[72:75],a[192:195],a[144:147],v[36:39]\n" \
"buffer_load_dwordx4 v22,s[40:43],s45 offen offset:3072 sc1 lds\nM v[72:75],a[196:199],a[148:151],v[72:75]\ns_add_u32 m0,s22,16384\nv_exp_f32 v119,v119\nM v[24:27],a[228:231],v[104:107],v[24:27]\ns_add_u32 s45,s45,s49\nv_exp_f32 v120,v120\nM v[72:75],a[200:203],a[152:155],v[72:75]\n" \
"v_exp_f32 v121,v121\nM v[72:75],a[204:207],a[156:159],v[72:75]\nbuffer_load_dwordx4 v22,s[40:43],s48 offen offset:0 sc1 lds\nM v[80:83],a[192:195],a[160:163],v[40:43]\nv_exp_f32 v122,v122\nM v[80:83],a[196:199],a[164:167],v[80:83]\nv_exp_f32 v123,v123\nM v[80:83],a[200:203],a[168:171],v[80:83]\n" \
"C v112,v112,v113\nC v113,v114,v115\nM v[80:83],a[204:207],a[172:175],v[80:83]\nbuffer_load_dwordx4 v22,s[40:43],s48 offen offset:1024 sc1 lds\nM v[88:91],a[192:195],a[176:179],v[44:47]\nds_read_b128 a[192:195],v14 offset:32768\nC v114,v116,v117\nM v[88:91],a[196:199],a[180:183],v[88:91]\n" \
"ds_read_b128 a[196:199],v14 offset:33792\nC v115,v118,v119\nM v[88:91],a[200:203],a[184:187],v[88:91]\nv_exp_f32 v124,v124\nM v[88:91],a[204:207],a[188:191],v[88:91]\nbuffer_load_dwordx4 v22,s[40:43],s48 offen offset:2048 sc1 lds\nM v[24:27],a[232:235],v[112:115],v[24:27]\nLG19_%=:\n" \
"s_cbranch_execz LHR2_%=\ns_waitcnt lgkmcnt(2)\nds_read_b64_tr_b16 a[96:97],v6 offset:49152\nC v120,v120,v121\nM v[68:71],a[208:211],a[128:131],v[32:35]\nv_exp_f32 v125,v125\nM v[68:71],a[212:215],a[132:135],v[68:71]\nds_read_b128 a[200:203],v14 offset:34816\n" \
"ds_read_b64_tr_b16 a[98:99],v7 offset:49152\nM v[68:71],a[216:219],a[136:139],v[68:71]\nbuffer_load_dwordx4 v22,s[40:43],s48 offen offset:3072 sc1 lds\nM v[68:71],a[220:223],a[140:143],v[68:71]\ns_add_u32 m0,s22,49152\nv_exp_f32 v126,v126\nM v[76:79],a[208:211],a[144:147],v[36:39]\ns_add_u32 s48,s48,s49\n" \
"ds_read_b64_tr_b16 a[100:101],v8 offset:49152\nds_read_b64_tr_b16 a[102:103],v9 offset:49152\nM v[76:79],a[212:215],a[148:151],v[76:79]\nv_exp_f32 v127,v127\nM v[76:79],a[216:219],a[152:155],v[76:79]\nC v121,v122,v123\nC v122,v124,v125\nC v123,v126,v127\n" \
"M v[76:79],a[220:223],a[156:159],v[76:79]\nds_read_b64_tr_b16 a[104:105],v6 offset:51200\nds_read_b64_tr_b16 a[106:107],v7 offset:51200\nM v[24:27],a[236:239],v[120:123],v[24:27]\nds_read_b64_tr_b16 a[108:109],v8 offset:51200\nv_xor_b32 v6,0x10000,v6\nM v[84:87],a[208:211],a[160:163],v[40:43]\nds_read_b64_tr_b16 a[110:111],v9 offset:51200\n" \
"ds_read_b128 a[204:207],v14 offset:35840\nM v[84:87],a[212:215],a[164:167],v[84:87]\nv_max3_f32 v15,v24,v25,v26\nv_max_f32 v15,v15,v27\nM v[84:87],a[216:219],a[168:171],v[84:87]\nv_cmp_nge_f32 vcc,0x53800000,v15\nv_xor_b32 v7,0x10000,v7\nM v[84:87],a[220:223],a[172:175],v[84:87]\n" \
"v_xor_b32 v8,0x10000,v8\nv_xor_b32 v9,0x10000,v9\nM v[92:95],a[208:211],a[176:179],v[44:47]\nds_read_b128 a[208:211],v14 offset:36864\nM v[92:95],a[212:215],a[180:183],v[92:95]\ns_cbranch_vccnz Lcs57_%=\nLcr58_%=:\nds_read_b128 a[212:215],v14 offset:37888\n" \
"M v[92:95],a[216:219],a[184:187],v[92:95]\nds_read_b128 a[216:219],v14 offset:38912\nM v[92:95],a[220:223],a[188:191],v[92:95]\ns_waitcnt lgkmcnt(3)\nds_read_b128 a[220:223],v14 offset:39936\nM v[128:131],a[192:195],v[96:99],v[128:131]\nv_exp_f32 v64,v64\nM v[160:163],a[192:195],v[104:107],v[160:163]\n" \
"v_exp_f32 v65,v65\nM v[192:195],a[192:195],v[112:115],v[192:195]\nv_exp_f32 v66,v66\nM v[224:227],a[192:195],v[120:123],v[224:227]\nv_exp_f32 v67,v67\nM v[228:231],a[196:199],v[120:123],v[228:231]\nv_exp_f32 v68,v68\nM v[196:199],a[196:199],v[112:115],v[196:199]\n" \
"v_exp_f32 v69,v69\nM v[164:167],a[196:199],v[104:107],v[164:167]\ns_waitcnt vmcnt(9) lgkmcnt(0)\nv_exp_f32 v70,v70\nM v[132:135],a[196:199],v[96:99],v[132:135]\ns_barrier\nds_read_b128 a[192:195],v13 offset:49152\nM v[136:139],a[200:203],v[96:99],v[136:139]\n" \
"v_exp_f32 v71,v71\nM v[168:171],a[200:203],v[104:107],v[168:171]\nv_exp_f32 v72,v72\nM v[200:203],a[200:203],v[112:115],v[200:203]\nv_exp_f32 v73,v73\nM v[232:235],a[200:203],v[120:123],v[232:235]\nbuffer_load_dwordx4 v22,s[40:43],s45 offen offset:0 sc1 lds\nM v[236:239],a[204:207],v[120:123],v[236:239]\n" \
"v_exp_f32 v74,v74\nM v[204:207],a[204:207],v[112:115],v[204:207]\nv_exp_f32 v75,v75\nM v[172:175],a[204:207],v[104:107],v[172:175]\nv_exp_f32 v76,v76\nM v[140:143],a[204:207],v[96:99],v[140:143]\nbuffer_load_dwordx4 v22,s[40:43],s45 offen offset:1024 sc1 lds\nM v[144:147],a[208:211],v[96:99],v[144:147]\n" \
"v_exp_f32 v77,v77\nM v[176:179],a[208:211],v[104:107],v[176:179]\ns_waitcnt vmcnt(7)\nv_cmp_gt_u64 s[98:99],s[14:15],v[10:11]\nv_cmp_le_u64 s[18:19],s[16:17],v[10:11]\nv_exp_f32 v78,v78\nM v[208:211],a[208:211],v[112:115],v[208:211]\ns_or_b64 s[98:99],s[98:99],s[18:19]\n" \
"s_and_b32 s98,s98,0xffff\ns_cbranch_scc1 LPE_%=\nLPER_%=:\nv_exp_f32 v79,v79\nM v[240:243],a[208:211],v[120:123],v[240:243]\nbuffer_load_dwordx4 v22,s[40:43],s45 offen offset:2048 sc1 lds\nM v[244:247],a[212:215],v[120:123],v[244:247]\nds_read_b128 a[196:199],v13 offset:51200\n" \
"C v64,v64,v65\nM v[212:215],a[212:215],v[112:115],v[212:215]\nv_exp_f32 v80,v80\nM v[180:183],a[212:215],v[104:107],v[180:183]\ns_add_u32 s52,s52,1\nv_exp_f32 v81,v81\nM v[148:151],a[212:215],v[96:99],v[148:151]\nbuffer_load_dwordx4 v22,s[40:43],s45 offen offset:3072 sc1 lds\n" \
"M v[152:155],a[216:219],v[96:99],v[152:155]\ns_add_u32 m0,s22,65536\nds_read_b128 a[200:203],v13 offset:53248\nC v65,v66,v67\nv_exp_f32 v82,v82\nM v[184:187],a[216:219],v[104:107],v[184:187]\nds_read_b128 a[204:207],v13 offset:55296\nC v66,v68,v69\n" \
"M v[216:219],a[216:219],v[112:115],v[216:219]\nv_exp_f32 v83,v83\nM v[248:251],a[216:219],v[120:123],v[248:251]\nbuffer_load_dwordx4 v22,s[40:43],s48 offen offset:0 sc1 lds\nM v[252:255],a[220:223],v[120:123],v[252:255]\nv_exp_f32 v84,v84\nM v[220:223],a[220:223],v[112:115],v[220:223]\ns_waitcnt lgkmcnt(2)\n" \
"v_exp_f32 v85,v85\nM v[188:191],a[220:223],v[104:107],v[188:191]\ns_cmp_eq_u32 s52,s56\nC v72,v72,v73\nC v67,v70,v71\nM v[156:159],a[220:223],v[96:99],v[156:159]\nbuffer_load_dwordx4 v22,s[40:43],s48 offen offset:1024 sc1 lds\nM v[96:99],a[192:195],a[128:131],v[32:35]\n" \
"s_cbranch_scc1 LEV_%=\nLEVR13_%=:\nds_read_b128 a[208:211],v13 offset:50176\nC v73,v74,v75\nM v[96:99],a[196:199],a[132:135],v[96:99]\nds_read_b128 a[212:215],v13 offset:52224\nC v74,v76,v77\nM v[16:19],a[224:227],v[64:67],v[24:27]\n" \
"s_waitcnt lgkmcnt(2)\nds_read_b128 a[216:219],v13 offset:54272\nC v75,v78,v79\nM v[96:99],a[200:203],a[136:139],v[96:99]\nbuffer_load_dwordx4 v22,s[40:43],s48 offen offset:2048 sc1 lds\nv_exp_f32 v86,v86\nM v[96:99],a[204:207],a[140:143],v[96:99]\nds_read_b128 a[220:223],v13 offset:56320\n" \
"C v80,v80,v81\nM v[104:107],a[192:195],a[144:147],v[36:39]\nv_exp_f32 v87,v87\nM v[104:107],a[196:199],a[148:151],v[104:107]\nv_exp_f32 v88,v88\nM v[16:19],a[228:231],v[72:75],v[16:19]\nbuffer_load_dwordx4 v22,s[40:43],s48 offen offset:3072 sc1 lds\nM v[104:107],a[200:203],a[152:155],v[104:107]\n" \
"s_add_u32 m0,s22,98304\nv_exp_f32 v89,v89\nM v[104:107],a[204:207],a[156:159],v[104:107]\ns_add_u32 s48,s48,s49\nv_exp_f32 v90,v90\nM v[112:115],a[192:195],a[160:163],v[40:43]\nC v81,v82,v83\nC v82,v84,v85\n" \
"M v[112:115],a[196:199],a[164:167],v[112:115]\nbuffer_load_dwordx4 v1,s[84:87],s46 offen offset:0 lds\nM v[112:115],a[200:203],a[168:171],v[112:115]\ns_add_u32 m0,s22,99200\nv_exp_f32 v91,v91\nM v[112:115],a[204:207],a[172:175],v[112:115]\nC v83,v86,v87\nC v88,v88,v89\n" \
"M v[120:123],a[192:195],a[176:179],v[44:47]\nv_exp_f32 v92,v92\nM v[120:123],a[196:199],a[180:183],v[120:123]\nbuffer_load_dwordx4 v1,s[84:87],s46 offen offset:128 lds\nM v[120:123],a[200:203],a[184:187],v[120:123]\ns_add_u32 m0,s22,100352\nv_exp_f32 v93,v93\nM v[120:123],a[204:207],a[188:191],v[120:123]\n" \
"s_mov_b32 s46,1114112\nv_exp_f32 v94,v94\nM v[16:19],a[232:235],v[80:83],v[16:19]\ns_waitcnt lgkmcnt(0)\nC v89,v90,v91\nC v90,v92,v93\nM v[100:103],a[208:211],a[128:131],v[32:35]\nbuffer_load_dwordx4 v1,s[84:87],s46 offen offset:0 lds\n" \
"M v[100:103],a[212:215],a[132:135],v[100:103]\ns_add_u32 m0,s22,101248\nds_read_b64_tr_b16 a[112:113],v6 offset:16384\nv_xor_b32 v14,0x10000,v14\nM v[100:103],a[216:219],a[136:139],v[100:103]\nds_read_b128 a[192:195],v14 offset:0\nM v[100:103],a[220:223],a[140:143],v[100:103]\nds_read_b128 a[196:199],v14 offset:1024\n" \
"M v[108:111],a[208:211],a[144:147],v[36:39]\nbuffer_load_dwordx4 v1,s[84:87],s46 offen offset:128 lds\nM v[108:111],a[212:215],a[148:151],v[108:111]\ns_add_u32 m0,s22,81920\nv_exp_f32 v95,v95\nM v[108:111],a[216:219],a[152:155],v[108:111]\ns_mov_b32 s46,1179648\nds_read_b128 a[200:203],v14 offset:2048\n" \
"C v91,v94,v95\nM v[108:111],a[220:223],a[156:159],v[108:111]\nds_read_b64_tr_b16 a[114:115],v7 offset:16384\nds_read_b64_tr_b16 a[116:117],v8 offset:16384\nM v[16:19],a[236:239],v[88:91],v[16:19]\nds_read_b64_tr_b16 a[118:119],v9 offset:16384\nM v[116:119],a[208:211],a[160:163],v[40:43]\nds_read_b64_tr_b16 a[120:121],v6 offset:18432\n" \
"ds_read_b64_tr_b16 a[122:123],v7 offset:18432\nM v[116:119],a[212:215],a[164:167],v[116:119]\nds_read_b64_tr_b16 a[124:125],v8 offset:18432\nv_max3_f32 v15,v16,v17,v18\nM v[116:119],a[216:219],a[168:171],v[116:119]\nds_read_b64_tr_b16 a[126:127],v9 offset:18432\nv_max_f32 v15,v15,v19\nM v[116:119],a[220:223],a[172:175],v[116:119]\n" \
"ds_read_b128 a[204:207],v14 offset:3072\nv_cmp_nge_f32 vcc,0x53800000,v15\nM v[124:127],a[208:211],a[176:179],v[44:47]\ns_waitcnt lgkmcnt(6)\nds_read_b128 a[208:211],v14 offset:4096\nM v[124:127],a[212:215],a[180:183],v[124:127]\ns_cbranch_vccnz Lcs59_%=\nLcr60_%=:\n" \
"ds_read_b128 a[212:215],v14 offset:5120\nM v[124:127],a[216:219],a[184:187],v[124:127]\ns_cmp_eq_u32 s52,s55\nds_read_b128 a[216:219],v14 offset:6144\nM v[124:127],a[220:223],a[188:191],v[124:127]\ns_cbranch_scc1 LH1_%=\nLH1R14_%=:\nds_read_b128 a[220:223],v14 offset:7168\n" \
"v_xor_b32 v13,0x10000,v13\nM v[128:131],a[192:195],v[64:67],v[128:131]\nv_exp_f32 v96,v96\nM v[160:163],a[192:195],v[72:75],v[160:163]\nv_exp_f32 v97,v97\nM v[192:195],a[192:195],v[80:83],v[192:195]\nv_exp_f32 v98,v98\nM v[224:227],a[192:195],v[88:91],v[224:227]\n" \
"v_exp_f32 v99,v99\nM v[228:231],a[196:199],v[88:91],v[228:231]\nv_exp_f32 v100,v100\nM v[196:199],a[196:199],v[80:83],v[196:199]\nv_exp_f32 v101,v101\nM v[164:167],a[196:199],v[72:75],v[164:167]\ns_waitcnt vmcnt(12) lgkmcnt(0)\nv_exp_f32 v102,v102\n" \
"M v[132:135],a[196:199],v[64:67],v[132:135]\ns_barrier\nds_read_b128 a[192:195],v13 offset:32768\nM v[136:139],a[200:203],v[64:67],v[136:139]\nbuffer_load_dwordx4 v1,s[84:87],s46 offen offset:0 lds\nM v[168:171],a[200:203],v[72:75],v[168:171]\ns_add_u32 m0,s22,82816\nv_exp_f32 v103,v103\n" \
"M v[200:203],a[200:203],v[80:83],v[200:203]\nv_exp_f32 v104,v104\nM v[232:235],a[200:203],v[88:91],v[232:235]\nv_exp_f32 v105,v105\nM v[236:239],a[204:207],v[88:91],v[236:239]\nbuffer_load_dwordx4 v1,s[84:87],s46 offen offset:128 lds\nM v[204:207],a[204:207],v[80:83],v[204:207]\ns_mov_b32 s46,1245184\n" \
"ds_read_b128 a[196:199],v13 offset:34816\nC v96,v96,v97\nM v[172:175],a[204:207],v[72:75],v[172:175]\ns_add_u32 m0,s22,83968\nv_exp_f32 v106,v106\nM v[140:143],a[204:207],v[64:67],v[140:143]\nds_read_b128 a[200:203],v13 offset:36864\nds_read_b128 a[204:207],v13 offset:38912\n" \
"M v[144:147],a[208:211],v[64:67],v[144:147]\nbuffer_load_dwordx4 v1,s[84:87],s46 offen offset:0 lds\nM v[176:179],a[208:211],v[72:75],v[176:179]\ns_add_u32 m0,s22,84864\nv_exp_f32 v107,v107\nM v[208:211],a[208:211],v[80:83],v[208:211]\nv_exp_f32 v108,v108\nM v[240:243],a[208:211],v[88:91],v[240:243]\n" \
"v_exp_f32 v109,v109\nM v[244:247],a[212:215],v[88:91],v[244:247]\nbuffer_load_dwordx4 v1,s[84:87],s46 offen offset:128 lds\nM v[212:215],a[212:215],v[80:83],v[212:215]\ns_add_u32 m0,s22,114688\nv_exp_f32 v110,v110\nM v[180:183],a[212:215],v[72:75],v[180:183]\ns_mov_b32 s46,1310720\n" \
"v_exp_f32 v111,v111\nM v[148:151],a[212:215],v[64:67],v[148:151]\nv_exp_f32 v112,v112\nM v[152:155],a[216:219],v[64:67],v[152:155]\nbuffer_load_dwordx4 v22,s[40:43],s48 offen offset:0 sc1 lds\nM v[184:187],a[216:219],v[72:75],v[184:187]\nC v97,v98,v99\nC v98,v100,v101\n" \
"M v[216:219],a[216:219],v[80:83],v[216:219]\nv_exp_f32 v113,v113\nM v[248:251],a[216:219],v[88:91],v[248:251]\nC v99,v102,v103\nC v104,v104,v105\nM v[252:255],a[220:223],v[88:91],v[252:255]\nbuffer_load_dwordx4 v22,s[40:43],s48 offen offset:1024 sc1 lds\nM v[220:223],a[220:223],v[80:83],v[220:223]\n" \
"v_exp_f32 v114,v114\nM v[188:191],a[220:223],v[72:75],v[188:191]\nC v105,v106,v107\nC v106,v108,v109\nM v[156:159],a[220:223],v[64:67],v[156:159]\ns_waitcnt lgkmcnt(0)\nv_exp_f32 v115,v115\nM v[64:67],a[192:195],a[128:131],v[32:35]\n" \
"buffer_load_dwordx4 v22,s[40:43],s48 offen offset:2048 sc1 lds\nM v[64:67],a[196:199],a[132:135],v[64:67]\nds_read_b128 a[208:211],v13 offset:33792\nC v107,v110,v111\nM v[24:27],a[224:227],v[96:99],v[16:19]\nds_read_b128 a[212:215],v13 offset:35840\nC v112,v112,v113\nM v[64:67],a[200:203],a[136:139],v[64:67]\n" \
"ds_read_b128 a[216:219],v13 offset:37888\nC v113,v114,v115\nM v[64:67],a[204:207],a[140:143],v[64:67]\nbuffer_load_dwordx4 v22,s[40:43],s48 offen offset:3072 sc1 lds\nM v[72:75],a[192:195],a[144:147],v[36:39]\ns_add_u32 m0,s22,0\nds_read_b128 a[220:223],v13 offset:39936\nM v[72:75],a[196:199],a[148:151],v[72:75]\n" \
"v_exp_f32 v116,v116\nM v[24:27],a[228:231],v[104:107],v[24:27]\nv_exp_f32 v117,v117\nM v[72:75],a[200:203],a[152:155],v[72:75]\nbuffer_load_dwordx4 v2,s[36:39],s47 offen offset:0 sc1 lds\nM v[72:75],a[204:207],a[156:159],v[72:75]\nv_exp_f32 v118,v118\nM v[80:83],a[192:195],a[160:163],v[40:43]\n" \
"v_exp_f32 v119,v119\nM v[80:83],a[196:199],a[164:167],v[80:83]\nv_exp_f32 v120,v120\nM v[80:83],a[200:203],a[168:171],v[80:83]\nbuffer_load_dwordx4 v2,s[36:39],s47 offen offset:1024 sc1 lds\nM v[80:83],a[204:207],a[172:175],v[80:83]\nv_exp_f32 v121,v121\nM v[88:91],a[192:195],a[176:179],v[44:47]\n" \
"C v114,v116,v117\nC v115,v118,v119\nM v[88:91],a[196:199],a[180:183],v[88:91]\nv_exp_f32 v122,v122\nM v[88:91],a[200:203],a[184:187],v[88:91]\nbuffer_load_dwordx4 v2,s[36:39],s47 offen offset:2048 sc1 lds\nM v[88:91],a[204:207],a[188:191],v[88:91]\nds_read_b128 a[192:195],v14 offset:16384\n" \
"C v120,v120,v121\nM v[24:27],a[232:235],v[112:115],v[24:27]\ns_waitcnt lgkmcnt(1)\nds_read_b128 a[196:199],v14 offset:17408\nM v[68:71],a[208:211],a[128:131],v[32:35]\nv_exp_f32 v123,v123\nM v[68:71],a[212:215],a[132:135],v[68:71]\nbuffer_load_dwordx4 v2,s[36:39],s47 offen offset:3072 sc1 lds\n" \
"M v[68:71],a[216:219],a[136:139],v[68:71]\ns_add_u32 m0,s22,32768\nv_exp_f32 v124,v124\nM v[68:71],a[220:223],a[140:143],v[68:71]\ns_add_u32 s47,s28,8192\nv_exp_f32 v125,v125\nM v[76:79],a[208:211],a[144:147],v[36:39]\nv_exp_f32 v126,v126\n" \
"M v[76:79],a[212:215],a[148:151],v[76:79]\nv_exp_f32 v127,v127\nM v[76:79],a[216:219],a[152:155],v[76:79]\nC v121,v122,v123\nC v122,v124,v125\nC v123,v126,v127\nM v[76:79],a[220:223],a[156:159],v[76:79]\nds_read_b128 a[200:203],v14 offset:18432\n" \
"ds_read_b128 a[204:207],v14 offset:19456\nM v[24:27],a[236:239],v[120:123],v[24:27]\nM v[84:87],a[208:211],a[160:163],v[40:43]\nM v[84:87],a[212:215],a[164:167],v[84:87]\nv_max3_f32 v15,v24,v25,v26\nv_max_f32 v15,v15,v27\nM v[84:87],a[216:219],a[168:171],v[84:87]\nv_cmp_nge_f32 vcc,0x53800000,v15\n" \
"M v[84:87],a[220:223],a[172:175],v[84:87]\nM v[92:95],a[208:211],a[176:179],v[44:47]\nds_read_b128 a[208:211],v14 offset:20480\nM v[92:95],a[212:215],a[180:183],v[92:95]\ns_cbranch_vccnz Lcs61_%=\nLcr62_%=:\nds_read_b128 a[212:215],v14 offset:21504\nM v[92:95],a[216:219],a[184:187],v[92:95]\n" \
"ds_read_b128 a[216:219],v14 offset:22528\nM v[92:95],a[220:223],a[188:191],v[92:95]\ns_waitcnt lgkmcnt(3)\nds_read_b128 a[220:223],v14 offset:23552\nM v[128:131],a[192:195],v[96:99],v[128:131]\nv_exp_f32 v64,v64\nM v[160:163],a[192:195],v[104:107],v[160:163]\nv_exp_f32 v65,v65\n" \
"M v[192:195],a[192:195],v[112:115],v[192:195]\nv_exp_f32 v66,v66\nM v[224:227],a[192:195],v[120:123],v[224:227]\nv_exp_f32 v67,v67\nM v[228:231],a[196:199],v[120:123],v[228:231]\nv_exp_f32 v68,v68\nM v[196:199],a[196:199],v[112:115],v[196:199]\nv_exp_f32 v69,v69\n" \
"M v[164:167],a[196:199],v[104:107],v[164:167]\ns_waitcnt vmcnt(16) lgkmcnt(0)\nv_exp_f32 v70,v70\nM v[132:135],a[196:199],v[96:99],v[132:135]\ns_barrier\nds_read_b128 a[192:195],v13 offset:49152\nM v[136:139],a[200:203],v[96:99],v[136:139]\nbuffer_load_dwordx4 v1,s[84:87],s46 offen offset:0 lds\n" \
"M v[168:171],a[200:203],v[104:107],v[168:171]\ns_add_u32 m0,s22,33664\nv_exp_f32 v71,v71\nM v[200:203],a[200:203],v[112:115],v[200:203]\nv_exp_f32 v72,v72\nM v[232:235],a[200:203],v[120:123],v[232:235]\nv_exp_f32 v73,v73\nM v[236:239],a[204:207],v[120:123],v[236:239]\n" \
"buffer_load_dwordx4 v1,s[84:87],s46 offen offset:128 lds\nM v[204:207],a[204:207],v[112:115],v[204:207]\ns_add_u32 m0,s22,34816\nds_read_b128 a[196:199],v13 offset:51200\nC v64,v64,v65\nM v[172:175],a[204:207],v[104:107],v[172:175]\ns_mov_b32 s46,1376256\nv_exp_f32 v74,v74\n" \
"M v[140:143],a[204:207],v[96:99],v[140:143]\nds_read_b128 a[200:203],v13 offset:53248\nds_read_b128 a[204:207],v13 offset:55296\nM v[144:147],a[208:211],v[96:99],v[144:147]\nbuffer_load_dwordx4 v1,s[84:87],s46 offen offset:0 lds\nM v[176:179],a[208:211],v[104:107],v[176:179]\ns_add_u32 m0,s22,35712\nv_exp_f32 v75,v75\n" \
"M v[208:211],a[208:211],v[112:115],v[208:211]\nv_exp_f32 v76,v76\nM v[240:243],a[208:211],v[120:123],v[240:243]\nv_exp_f32 v77,v77\nM v[244:247],a[212:215],v[120:123],v[244:247]\nbuffer_load_dwordx4 v1,s[84:87],s46 offen offset:128 lds\nM v[212:215],a[212:215],v[112:115],v[212:215]\ns_add_u32 m0,s22,16384\n" \
"v_exp_f32 v78,v78\nM v[180:183],a[212:215],v[104:107],v[180:183]\ns_add_u32 s52,s52,1\nv_exp_f32 v79,v79\nM v[148:151],a[212:215],v[96:99],v[148:151]\ns_mov_b32 s46,1441792\nv_exp_f32 v80,v80\nM v[152:155],a[216:219],v[96:99],v[152:155]\n" \
"buffer_load_dwordx4 v1,s[84:87],s46 offen offset:0 lds\nM v[184:187],a[216:219],v[104:107],v[184:187]\ns_add_u32 m0,s22,17280\nv_exp_f32 v81,v81\nM v[216:219],a[216:219],v[112:115],v[216:219]\nv_exp_f32 v82,v82\nM v[248:251],a[216:219],v[120:123],v[248:251]\ns_waitcnt lgkmcnt(0)\n" \
"C v65,v66,v67\nC v66,v68,v69\nM v[252:255],a[220:223],v[120:123],v[252:255]\nbuffer_load_dwordx4 v1,s[84:87],s46 offen offset:128 lds\nM v[220:223],a[220:223],v[112:115],v[220:223]\ns_add_u32 m0,s22,18432\nv_exp_f32 v83,v83\nM v[188:191],a[220:223],v[104:107],v[188:191]\n" \
"s_cmp_eq_u32 s52,s56\nC v67,v70,v71\nC v72,v72,v73\nM v[156:159],a[220:223],v[96:99],v[156:159]\ns_cbranch_scc1 LEV_%=\nLEVR14_%=:\nv_exp_f32 v84,v84\nM v[96:99],a[192:195],a[128:131],v[32:35]\n" \
"buffer_load_dwordx2 v[10:11],v5,s[36:39],0 offen sc1\nM v[96:99],a[196:199],a[132:135],v[96:99]\ns_mov_b32 s46,1507328\nds_read_b128 a[208:211],v13 offset:50176\nds_read_b128 a[212:215],v13 offset:52224\nM v[16:19],a[224:227],v[64:67],v[24:27]\nbuffer_load_dwordx4 v1,s[84:87],s46 offen offset:0 lds\nM v[96:99],a[200:203],a[136:139],v[96:99]\n" \
"s_add_u32 m0,s22,19328\nC v73,v74,v75\nC v74,v76,v77\nM v[96:99],a[204:207],a[140:143],v[96:99]\nds_read_b128 a[216:219],v13 offset:54272\nC v75,v78,v79\nM v[104:107],a[192:195],a[144:147],v[36:39]\nds_read_b128 a[220:223],v13 offset:56320\n" \
"C v80,v80,v81\nM v[104:107],a[196:199],a[148:151],v[104:107]\nv_exp_f32 v85,v85\nM v[16:19],a[228:231],v[72:75],v[16:19]\nbuffer_load_dwordx4 v1,s[84:87],s46 offen offset:128 lds\nM v[104:107],a[200:203],a[152:155],v[104:107]\ns_add_u32 m0,s22,49152\nv_exp_f32 v86,v86\n" \
"M v[104:107],a[204:207],a[156:159],v[104:107]\ns_mov_b32 s46,1966080\nv_exp_f32 v87,v87\nM v[112:115],a[192:195],a[160:163],v[40:43]\nv_exp_f32 v88,v88\nM v[112:115],a[196:199],a[164:167],v[112:115]\nC v81,v82,v83\nC v82,v84,v85\n" \
"M v[112:115],a[200:203],a[168:171],v[112:115]\nv_exp_f32 v89,v89\nM v[112:115],a[204:207],a[172:175],v[112:115]\nC v83,v86,v87\nC v88,v88,v89\nM v[120:123],a[192:195],a[176:179],v[44:47]\nv_exp_f32 v90,v90\nM v[120:123],a[196:199],a[180:183],v[120:123]\n" \
"v_exp_f32 v91,v91\nM v[120:123],a[200:203],a[184:187],v[120:123]\nv_exp_f32 v92,v92\nM v[120:123],a[204:207],a[188:191],v[120:123]\nv_exp_f32 v93,v93\nM v[16:19],a[232:235],v[80:83],v[16:19]\ns_waitcnt lgkmcnt(0)\nv_exp_f32 v94,v94\n" \
"M v[100:103],a[208:211],a[128:131],v[32:35]\nC v89,v90,v91\nC v90,v92,v93\nM v[100:103],a[212:215],a[132:135],v[100:103]\nv_exp_f32 v95,v95\nM v[100:103],a[216:219],a[136:139],v[100:103]\nv_xor_b32 v14,0x10000,v14\nC v91,v94,v95\n" \
"M v[100:103],a[220:223],a[140:143],v[100:103]\nds_read_b128 a[192:195],v14 offset:0\nds_read_b128 a[196:199],v14 offset:1024\nM v[108:111],a[208:211],a[144:147],v[36:39]\nds_read_b128 a[200:203],v14 offset:2048\nv_add_u32 v20,0x10000,v4\nM v[108:111],a[212:215],a[148:151],v[108:111]\nds_read_b128 a[204:207],v14 offset:3072\n" \
"v_add_u32 v23,0x20000,v4\nM v[108:111],a[216:219],a[152:155],v[108:111]\nM v[108:111],a[220:223],a[156:159],v[108:111]\nM v[16:19],a[236:239],v[88:91],v[16:19]\nM v[116:119],a[208:211],a[160:163],v[40:43]\nM v[116:119],a[212:215],a[164:167],v[116:119]\nv_max3_f32 v15,v16,v17,v18\nv_max_f32 v15,v15,v19\n" \
"M v[116:119],a[216:219],a[168:171],v[116:119]\nv_cmp_nge_f32 vcc,0x53800000,v15\nM v[116:119],a[220:223],a[172:175],v[116:119]\nM v[124:127],a[208:211],a[176:179],v[44:47]\nds_read_b128 a[208:211],v14 offset:4096\nM v[124:127],a[212:215],a[180:183],v[124:127]\ns_cbranch_vccnz Lcs63_%=\nLcr64_%=:\n" \
"ds_read_b128 a[212:215],v14 offset:5120\nv_max3_f32 v60,v96,v97,v98\nM v[124:127],a[216:219],a[184:187],v[124:127]\ns_waitcnt lgkmcnt(2)\nds_read_b128 a[216:219],v14 offset:6144\nv_max3_f32 v61,v99,v104,v105\nM v[124:127],a[220:223],a[188:191],v[124:127]\nds_read_b128 a[220:223],v14 offset:7168\n" \
"v_max3_f32 v60,v60,v106,v107\nv_max3_f32 v61,v61,v112,v113\nv_max3_f32 v60,v60,v114,v115\nv_max3_f32 v61,v61,v120,v121\nv_max3_f32 v60,v60,v122,v123\nv_max3_f32 v61,v61,v100,v101\nv_max3_f32 v60,v60,v102,v103\nv_max3_f32 v61,v61,v108,v109\n" \
"v_max3_f32 v60,v60,v110,v111\nv_max3_f32 v61,v61,v116,v117\nv_max3_f32 v60,v60,v118,v119\nv_max3_f32 v61,v61,v124,v125\nv_max3_f32 v61,v61,v126,v127\nv_max_f32 v60,v60,v61\nv_cmp_nge_f32 vcc,0x41f00000,v60\ns_cbranch_vccnz LRS_%=\n" \
"LH1R15_%=:\nM v[128:131],a[192:195],v[64:67],v[128:131]\nLG20_%=:\ns_cbranch_execz LG22_%=\nv_exp_f32 v96,v96\nM v[160:163],a[192:195],v[72:75],v[160:163]\nv_exp_f32 v97,v97\nM v[192:195],a[192:195],v[80:83],v[192:195]\n" \
"v_exp_f32 v98,v98\nM v[224:227],a[192:195],v[88:91],v[224:227]\nv_exp_f32 v99,v99\nM v[228:231],a[196:199],v[88:91],v[228:231]\nv_exp_f32 v100,v100\nM v[196:199],a[196:199],v[80:83],v[196:199]\nv_exp_f32 v101,v101\nM v[164:167],a[196:199],v[72:75],v[164:167]\n" \
"s_waitcnt vmcnt(9) lgkmcnt(0)\nv_exp_f32 v102,v102\nM v[132:135],a[196:199],v[64:67],v[132:135]\ns_barrier\nbuffer_load_dwordx4 v1,s[84:87],s46 offen offset:0 lds\nM v[136:139],a[200:203],v[64:67],v[136:139]\ns_add_u32 m0,s22,50048\nds_read_b128 v[76:79],v4 offset:0\n" \
"C v96,v96,v97\nM v[168:171],a[200:203],v[72:75],v[168:171]\nv_exp_f32 v103,v103\nM v[200:203],a[200:203],v[80:83],v[200:203]\nv_exp_f32 v104,v104\nM v[232:235],a[200:203],v[88:91],v[232:235]\nbuffer_load_dwordx4 v1,s[84:87],s46 offen offset:128 lds\nM v[236:239],a[204:207],v[88:91],v[236:239]\n" \
"s_mov_b32 s46,2031616\ns_add_u32 m0,s22,51200\nds_read_b128 v[84:87],v4 offset:1024\nC v97,v98,v99\nM v[204:207],a[204:207],v[80:83],v[204:207]\nds_read_b128 v[92:95],v4 offset:2048\nC v98,v100,v101\nM v[172:175],a[204:207],v[72:75],v[172:175]\n" \
"s_waitcnt vmcnt(4)\nv_cmp_gt_u64 s[98:99],s[14:15],v[10:11]\nv_cmp_le_u64 s[18:19],s[16:17],v[10:11]\nM v[140:143],a[204:207],v[64:67],v[140:143]\nbuffer_load_dwordx4 v1,s[84:87],s46 offen offset:0 lds\nv_exp_f32 v105,v105\nM v[144:147],a[208:211],v[64:67],v[144:147]\ns_add_u32 m0,s22,52096\n" \
"v_exp_f32 v106,v106\nM v[176:179],a[208:211],v[72:75],v[176:179]\ns_or_b64 s[98:99],s[98:99],s[18:19]\ns_and_b32 s98,s98,0xff0000\ns_cbranch_scc1 LPF_%=\nLPFR_%=:\nv_exp_f32 v107,v107\nM v[208:211],a[208:211],v[80:83],v[208:211]\n" \
"v_exp_f32 v108,v108\nM v[240:243],a[208:211],v[88:91],v[240:243]\nbuffer_load_dwordx4 v1,s[84:87],s46 offen offset:128 lds\nv_exp_f32 v109,v109\nM v[244:247],a[212:215],v[88:91],v[244:247]\ns_add_u32 m0,s22,65536\nv_exp_f32 v110,v110\nM v[212:215],a[212:215],v[80:83],v[212:215]\n" \
"s_mov_b32 s46,1572864\nv_exp_f32 v111,v111\nM v[180:183],a[212:215],v[72:75],v[180:183]\nv_exp_f32 v112,v112\nM v[148:151],a[212:215],v[64:67],v[148:151]\nbuffer_load_dwordx4 v2,s[36:39],s47 offen offset:0 sc1 lds\nv_exp_f32 v113,v113\nM v[152:155],a[216:219],v[64:67],v[152:155]\n" \
"ds_read_b128 a[192:195],v14 offset:49152\nv_exp_f32 v114,v114\nM v[184:187],a[216:219],v[72:75],v[184:187]\nds_read_b128 a[196:199],v14 offset:50176\nv_exp_f32 v115,v115\nM v[216:219],a[216:219],v[80:83],v[216:219]\nds_read_b128 a[200:203],v14 offset:51200\nC v104,v104,v105\n" \
"v_exp_f32 v116,v116\nM v[248:251],a[216:219],v[88:91],v[248:251]\nbuffer_load_dwordx4 v2,s[36:39],s47 offen offset:1024 sc1 lds\nv_exp_f32 v117,v117\nM v[252:255],a[220:223],v[88:91],v[252:255]\nds_read_b128 a[204:207],v14 offset:52224\nC v99,v102,v103\nv_exp_f32 v118,v118\n" \
"M v[220:223],a[220:223],v[80:83],v[220:223]\nds_read_b128 a[208:211],v14 offset:53248\nC v105,v106,v107\nv_exp_f32 v119,v119\nM v[188:191],a[220:223],v[72:75],v[188:191]\ns_waitcnt lgkmcnt(4)\nds_read_b128 v[72:75],v4 offset:3072\nC v112,v112,v113\n" \
"v_exp_f32 v120,v120\nM v[156:159],a[220:223],v[64:67],v[156:159]\nbuffer_load_dwordx4 v2,s[36:39],s47 offen offset:2048 sc1 lds\nv_exp_f32 v121,v121\nM v[48:51],a[0:3],v[76:79],0\nv_exp_f32 v122,v122\nM v[48:51],a[8:11],v[84:87],v[48:51]\nds_read_b128 v[80:83],v4 offset:8192\n" \
"C v106,v108,v109\nv_exp_f32 v123,v123\nM v[48:51],a[16:19],v[92:95],v[48:51]\ns_waitcnt lgkmcnt(1)\nds_read_b128 v[88:91],v4 offset:9216\nC v107,v110,v111\nM v[48:51],a[24:27],v[72:75],v[48:51]\nbuffer_load_dwordx4 v2,s[36:39],s47 offen offset:3072 sc1 lds\n" \
"M v[52:55],a[28:31],v[72:75],0\ns_add_u32 m0,s22,98304\nds_read_b128 a[212:215],v14 offset:54272\nC v113,v114,v115\nM v[52:55],a[20:23],v[92:95],v[52:55]\ns_add_u32 s47,s29,0\nds_read_b128 v[72:75],v4 offset:10240\nds_read_b128 v[92:95],v4 offset:11264\n" \
"M v[52:55],a[12:15],v[84:87],v[52:55]\nv_exp_f32 v124,v124\nM v[52:55],a[4:7],v[76:79],v[52:55]\ns_waitcnt lgkmcnt(4)\nds_read_b128 a[216:219],v14 offset:55296\nC v120,v120,v121\nM v[64:67],a[0:3],v[80:83],0\ns_waitcnt lgkmcnt(4)\n" \
"ds_read_b64_tr_b16 a[128:129],v6 offset:32768\nC v121,v122,v123\nM v[64:67],a[8:11],v[88:91],v[64:67]\ns_waitcnt lgkmcnt(3)\nds_read_b64_tr_b16 a[130:131],v7 offset:32768\nds_read_b64_tr_b16 a[132:133],v8 offset:32768\nds_read_b64_tr_b16 a[134:135],v9 offset:32768\nds_read_b128 a[220:223],v14 offset:56320\n" \
"ds_read_b64_tr_b16 a[136:137],v6 offset:34816\nds_read_b64_tr_b16 a[138:139],v7 offset:34816\nds_read_b64_tr_b16 a[140:141],v8 offset:34816\nds_read_b64_tr_b16 a[142:143],v9 offset:34816\nds_read_b64_tr_b16 a[144:145],v6 offset:16384\nds_read_b64_tr_b16 a[146:147],v7 offset:16384\nds_read_b64_tr_b16 a[148:149],v8 offset:16384\nds_read_b64_tr_b16 a[150:151],v9 offset:16384\n" \
"ds_read_b64_tr_b16 a[152:153],v6 offset:18432\nds_read_b64_tr_b16 a[154:155],v7 offset:18432\nds_read_b64_tr_b16 a[156:157],v8 offset:18432\nds_read_b64_tr_b16 a[158:159],v9 offset:18432\nM v[64:67],a[16:19],v[72:75],v[64:67]\ns_waitcnt lgkmcnt(15)\nds_read_b128 v[84:87],v4 offset:4096\nv_xor_b32 v6,0x10000,v6\n" \
"M v[64:67],a[24:27],v[92:95],v[64:67]\nds_read_b128 v[76:79],v4 offset:5120\nv_xor_b32 v7,0x10000,v7\nM v[68:71],a[28:31],v[92:95],0\nds_read_b128 v[92:95],v4 offset:6144\nv_xor_b32 v8,0x10000,v8\nM v[68:71],a[20:23],v[72:75],v[68:71]\nds_read_b128 v[72:75],v4 offset:7168\n" \
"v_xor_b32 v9,0x10000,v9\nv_exp_f32 v125,v125\nM v[68:71],a[12:15],v[88:91],v[68:71]\ns_waitcnt lgkmcnt(3)\nv_exp_f32 v126,v126\nM v[68:71],a[4:7],v[80:83],v[68:71]\ns_waitcnt vmcnt(8) lgkmcnt(4)\nds_read_b128 v[88:91],v4 offset:12288\n" \
"C v114,v116,v117\nM v[24:27],a[224:227],v[96:99],v[16:19]\ns_barrier\nbuffer_load_dwordx4 v2,s[36:39],s47 offen offset:0 sc1 lds\nM v[48:51],a[32:35],v[84:87],v[48:51]\ns_waitcnt lgkmcnt(3)\nds_read_b128 v[80:83],v4 offset:13312\nC v115,v118,v119\n" \
"M v[48:51],a[40:43],v[76:79],v[48:51]\ns_waitcnt lgkmcnt(3)\nds_read_b64_tr_b16 a[160:161],v6 offset:32768\nC v122,v124,v125\nM v[48:51],a[48:51],v[92:95],v[48:51]\ns_waitcnt lgkmcnt(3)\nds_read_b64_tr_b16 a[162:163],v7 offset:32768\nM v[48:51],a[56:59],v[72:75],v[48:51]\n" \
"buffer_load_dwordx4 v2,s[36:39],s47 offen offset:1024 sc1 lds\nM v[52:55],a[60:63],v[72:75],v[52:55]\nds_read_b128 v[72:75],v4 offset:14336\nM v[52:55],a[52:55],v[92:95],v[52:55]\nds_read_b128 v[92:95],v4 offset:15360\nM v[52:55],a[44:47],v[76:79],v[52:55]\ns_waitcnt lgkmcnt(4)\nv_exp_f32 v127,v127\n" \
"M v[52:55],a[36:39],v[84:87],v[52:55]\nbuffer_load_dwordx4 v2,s[36:39],s47 offen offset:2048 sc1 lds\nM v[24:27],a[228:231],v[104:107],v[24:27]\ns_add_u32 s52,s52,1\nds_read_b64_tr_b16 a[164:165],v8 offset:32768\nC v123,v126,v127\nM v[64:67],a[32:35],v[88:91],v[64:67]\ns_waitcnt lgkmcnt(1)\n" \
"ds_read_b64_tr_b16 a[166:167],v9 offset:32768\nds_read_b64_tr_b16 a[168:169],v6 offset:34816\nM v[64:67],a[40:43],v[80:83],v[64:67]\ns_waitcnt lgkmcnt(4)\nds_read_b64_tr_b16 a[170:171],v7 offset:34816\nM v[64:67],a[48:51],v[72:75],v[64:67]\nbuffer_load_dwordx4 v2,s[36:39],s47 offen offset:3072 sc1 lds\nM v[64:67],a[56:59],v[92:95],v[64:67]\n" \
"s_add_u32 s47,s29,8192\nds_read_b64_tr_b16 a[172:173],v8 offset:34816\nM v[68:71],a[60:63],v[92:95],v[68:71]\ns_add_u32 m0,s22,81920\nds_read_b64_tr_b16 a[174:175],v9 offset:34816\nM v[68:71],a[52:55],v[72:75],v[68:71]\nds_read_b64_tr_b16 a[176:177],v6 offset:16384\nM v[68:71],a[44:47],v[80:83],v[68:71]\n" \
"s_cmp_eq_u32 s52,s56\nds_read_b64_tr_b16 a[178:179],v7 offset:16384\nds_read_b64_tr_b16 a[180:181],v8 offset:16384\nM v[68:71],a[36:39],v[88:91],v[68:71]\nbuffer_load_dwordx4 v2,s[36:39],s47 offen offset:0 sc1 lds\nM v[24:27],a[232:235],v[112:115],v[24:27]\ns_cbranch_scc1 LEV_%=\nLEVR15_%=:\n" \
"ds_read_b64_tr_b16 a[182:183],v9 offset:16384\nds_read_b64_tr_b16 a[184:185],v6 offset:18432\nds_read_b64_tr_b16 a[186:187],v7 offset:18432\nds_read_b64_tr_b16 a[188:189],v8 offset:18432\nds_read_b64_tr_b16 a[190:191],v9 offset:18432\nds_write2_b32 v57,v40,v44 offset1:1\nM v[24:27],a[236:239],v[120:123],v[24:27]\nM v[192:195],a[192:195],v[112:115],v[192:195]\n" \
"M v[196:199],a[196:199],v[112:115],v[196:199]\nds_write_b64 v57,v[26:27] offset:8\nM v[200:203],a[200:203],v[112:115],v[200:203]\nbuffer_load_dwordx4 v2,s[36:39],s47 offen offset:1024 sc1 lds\nM v[204:207],a[204:207],v[112:115],v[204:207]\nC v192,v192,v193\nC v193,v194,v195\nM v[208:211],a[208:211],v[112:115],v[208:211]\n" \
"s_waitcnt vmcnt(6) lgkmcnt(0)\nC v194,v196,v197\nC v195,v198,v199\nM v[212:215],a[212:215],v[112:115],v[212:215]\ns_barrier\nds_read_b128 v[28:31],v12 offset:0\nM v[216:219],a[216:219],v[112:115],v[216:219]\nds_write_b128 v12,v[192:195] offset:0\n" \
"M v[220:223],a[220:223],v[112:115],v[220:223]\nbuffer_load_dwordx4 v2,s[36:39],s47 offen offset:2048 sc1 lds\nM v[224:227],a[192:195],v[120:123],v[224:227]\nC v200,v200,v201\nC v201,v202,v203\nM v[228:231],a[196:199],v[120:123],v[228:231]\nds_read_b64_tr_b16 a[240:241],v6 offset:49152\nC v202,v204,v205\n" \
"M v[232:235],a[200:203],v[120:123],v[232:235]\nds_read_b64_tr_b16 a[242:243],v7 offset:49152\nC v203,v206,v207\nM v[236:239],a[204:207],v[120:123],v[236:239]\nbuffer_load_dwordx4 v2,s[36:39],s47 offen offset:3072 sc1 lds\nM v[240:243],a[208:211],v[120:123],v[240:243]\ns_add_u32 m0,s22,114688\nC v208,v208,v209\n" \
"C v209,v210,v211\nM v[244:247],a[212:215],v[120:123],v[244:247]\ns_add_u32 s47,s30,0\nds_write_b128 v12,v[200:203] offset:1024\nM v[248:251],a[216:219],v[120:123],v[248:251]\nC v210,v212,v213\nC v211,v214,v215\nM v[252:255],a[220:223],v[120:123],v[252:255]\n" \
"buffer_load_dwordx4 v2,s[36:39],s47 offen offset:0 sc1 lds\nM v[128:131],a[192:195],v[96:99],v[128:131]\nC v216,v216,v217\nC v217,v218,v219\nM v[132:135],a[196:199],v[96:99],v[132:135]\nC v218,v220,v221\nC v219,v222,v223\nM v[136:139],a[200:203],v[96:99],v[136:139]\n" \
"ds_write_b128 v12,v[208:211] offset:2048\nM v[140:143],a[204:207],v[96:99],v[140:143]\nbuffer_load_dwordx4 v2,s[36:39],s47 offen offset:1024 sc1 lds\nM v[144:147],a[208:211],v[96:99],v[144:147]\nC v224,v224,v225\nC v232,v232,v233\nM v[148:151],a[212:215],v[96:99],v[148:151]\nC v225,v226,v227\n" \
"C v233,v234,v235\nM v[152:155],a[216:219],v[96:99],v[152:155]\nds_write_b128 v12,v[216:219] offset:3072\nM v[156:159],a[220:223],v[96:99],v[156:159]\nbuffer_load_dwordx4 v2,s[36:39],s47 offen offset:2048 sc1 lds\nM v[160:163],a[192:195],v[104:107],v[160:163]\nC v226,v228,v229\nC v227,v230,v231\n" \
"M v[164:167],a[196:199],v[104:107],v[164:167]\nds_read_b128 v[76:79],v20 offset:0\nC v234,v236,v237\nM v[168:171],a[200:203],v[104:107],v[168:171]\nds_read_b128 v[84:87],v20 offset:1024\nC v235,v238,v239\nM v[172:175],a[204:207],v[104:107],v[172:175]\nds_read_b128 v[92:95],v20 offset:2048\n" \
"C v240,v240,v241\nM v[176:179],a[208:211],v[104:107],v[176:179]\ns_waitcnt lgkmcnt(3)\nds_read_b128 v[72:75],v20 offset:3072\nv_min_f32 v15,v32,v28\nM v[180:183],a[212:215],v[104:107],v[180:183]\nbuffer_load_dwordx4 v2,s[36:39],s47 offen offset:3072 sc1 lds\nM v[184:187],a[216:219],v[104:107],v[184:187]\n" \
"s_add_u32 m0,s22,0\nC v248,v248,v249\nC v241,v242,v243\nM v[188:191],a[220:223],v[104:107],v[188:191]\ns_waitcnt lgkmcnt(3)\ns_add_u32 s47,s30,8192\nC v249,v250,v251\nv_sub_f32 v16,v15,v32\n" \
"M v[48:51],a[64:67],v[76:79],v[48:51]\ns_waitcnt lgkmcnt(2)\nds_write_b128 v12,v[224:227] offset:4096\nM v[48:51],a[72:75],v[84:87],v[48:51]\ns_waitcnt lgkmcnt(2)\nds_read_b128 v[80:83],v20 offset:8192\nC v242,v244,v245\nM v[48:51],a[80:83],v[92:95],v[48:51]\n" \
"s_waitcnt lgkmcnt(2)\nds_read_b128 v[88:91],v20 offset:9216\nC v243,v246,v247\nM v[48:51],a[88:91],v[72:75],v[48:51]\nds_read_b128 v[60:63],v20 offset:10240\nC v250,v252,v253\nM v[52:55],a[92:95],v[72:75],v[52:55]\nds_read_b128 v[72:75],v20 offset:11264\n" \
"C v251,v254,v255\nM v[52:55],a[84:87],v[92:95],v[52:55]\nbuffer_load_dwordx4 v2,s[36:39],s47 offen offset:0 sc1 lds\nM v[52:55],a[76:79],v[84:87],v[52:55]\nv_exp_f32 v16,v16\nM v[52:55],a[68:71],v[76:79],v[52:55]\ns_waitcnt lgkmcnt(3)\nv_sub_f32 v18,v15,v28\n" \
"v_mul_f32 v15,v24,v16\nM v[64:67],a[64:67],v[80:83],v[64:67]\ns_waitcnt lgkmcnt(2)\nds_write_b128 v12,v[232:235] offset:5120\nM v[64:67],a[72:75],v[88:91],v[64:67]\ns_waitcnt lgkmcnt(2)\nds_read_b128 v[92:95],v20 offset:4096\nv_mov_b32 v28,s10\n" \
"M v[64:67],a[80:83],v[60:63],v[64:67]\ns_waitcnt lgkmcnt(2)\nds_read_b128 v[84:87],v20 offset:5120\nv_exp_f32 v18,v18\nM v[64:67],a[88:91],v[72:75],v[64:67]\nds_write_b128 v12,v[240:243] offset:6144\nds_write_b128 v12,v[248:251] offset:7168\nds_read_b64_tr_b16 a[244:245],v8 offset:49152\n" \
"ds_read_b64_tr_b16 a[246:247],v9 offset:49152\nds_read_b64_tr_b16 a[248:249],v6 offset:51200\nds_read_b64_tr_b16 a[250:251],v7 offset:51200\nds_read_b64_tr_b16 a[252:253],v8 offset:51200\nds_read_b64_tr_b16 a[254:255],v9 offset:51200\nds_read_b128 v[76:79],v20 offset:6144\nM v[68:71],a[92:95],v[72:75],v[68:71]\nds_read_b128 v[72:75],v20 offset:7168\n" \
"v_fmac_f32 v15,v30,v18\nv_rcp_f32 v15,v15\nM v[68:71],a[84:87],v[60:63],v[68:71]\nbuffer_load_dwordx4 v2,s[36:39],s47 offen offset:1024 sc1 lds\nM v[68:71],a[76:79],v[88:91],v[68:71]\nv_mul_f32 v16,v16,v15\nv_mul_f32 v18,v18,v15\nM v[68:71],a[68:71],v[80:83],v[68:71]\n" \
"s_waitcnt lgkmcnt(11)\nds_read_b128 v[60:63],v20 offset:12288\nv_mul_f32 v128,v16,v128\nM v[48:51],a[96:99],v[92:95],v[48:51]\ns_waitcnt lgkmcnt(11)\nds_read_b128 v[88:91],v20 offset:13312\nv_mul_f32 v129,v16,v129\nM v[48:51],a[104:107],v[84:87],v[48:51]\n" \
"s_waitcnt vmcnt(10) lgkmcnt(4)\ns_waitcnt lgkmcnt(3)\nds_read_b128 v[80:83],v20 offset:14336\nv_mul_f32 v130,v16,v130\nM v[48:51],a[112:115],v[76:79],v[48:51]\ns_barrier\ns_waitcnt lgkmcnt(3)\nv_mul_f32 v131,v16,v131\n" \
"v_mul_f32 v132,v16,v132\nM v[48:51],a[120:123],v[72:75],v[48:51]\nbuffer_load_dwordx4 v2,s[36:39],s47 offen offset:2048 sc1 lds\nv_mul_f32 v133,v16,v133\nM v[52:55],a[124:127],v[72:75],v[52:55]\nds_read_b128 v[72:75],v20 offset:15360\nv_mul_f32 v134,v16,v134\nv_mul_f32 v135,v16,v135\n" \
"M v[52:55],a[116:119],v[76:79],v[52:55]\ns_waitcnt lgkmcnt(1)\nds_read_b128 v[96:99],v57 offset:0\nv_mul_f32 v136,v16,v136\nv_mul_f32 v137,v16,v137\nM v[52:55],a[108:111],v[84:87],v[52:55]\ns_waitcnt lgkmcnt(3)\nds_read_b128 v[100:103],v57 offset:1024\n" \
"v_mul_f32 v138,v16,v138\nv_mul_f32 v139,v16,v139\nM v[52:55],a[100:103],v[92:95],v[52:55]\nbuffer_load_dwordx4 v2,s[36:39],s47 offen offset:3072 sc1 lds\nv_mul_f32 v140,v16,v140\nM v[64:67],a[96:99],v[60:63],v[64:67]\ns_add_u32 m0,s22,32768\nds_read_b128 v[104:107],v57 offset:2048\n" \
"v_mul_f32 v141,v16,v141\nv_mul_f32 v142,v16,v142\nM v[64:67],a[104:107],v[88:91],v[64:67]\ns_add_u32 s47,s28,16384\nds_read_b128 v[76:79],v20 offset:32768\nv_mul_f32 v143,v16,v143\nM v[64:67],a[112:115],v[80:83],v[64:67]\ns_waitcnt lgkmcnt(4)\n" \
"ds_read_b128 v[84:87],v20 offset:33792\nv_min_f32 v15,v36,v29\nM v[64:67],a[120:123],v[72:75],v[64:67]\nds_read_b128 v[92:95],v20 offset:34816\nv_mul_f32 v144,v16,v144\nM v[68:71],a[124:127],v[72:75],v[68:71]\ns_waitcnt lgkmcnt(4)\nds_read_b128 v[72:75],v20 offset:35840\n" \
"v_mul_f32 v145,v16,v145\nv_lshlrev_b32 v228,16,v96\nM v[68:71],a[116:119],v[80:83],v[68:71]\nbuffer_load_dwordx4 v2,s[36:39],s47 offen offset:0 sc1 lds\nv_and_b32 v96,0xffff0000,v96\nM v[68:71],a[108:111],v[88:91],v[68:71]\nv_fmac_f32 v128,v228,v18\nv_fmac_f32 v129,v96,v18\n" \
"v_lshlrev_b32 v229,16,v97\nM v[68:71],a[100:103],v[60:63],v[68:71]\ns_waitcnt lgkmcnt(3)\nds_read_b128 v[108:111],v57 offset:3072\nv_and_b32 v97,0xffff0000,v97\nv_fmac_f32 v130,v229,v18\nM v[32:35],a[0:3],v[76:79],0\ns_waitcnt lgkmcnt(3)\n" \
"v_fmac_f32 v131,v97,v18\nv_lshlrev_b32 v230,16,v98\nv_and_b32 v98,0xffff0000,v98\nM v[32:35],a[8:11],v[84:87],v[32:35]\ns_waitcnt lgkmcnt(2)\nds_read_b128 v[80:83],v20 offset:40960\nv_fmac_f32 v132,v230,v18\nv_fmac_f32 v133,v98,v18\n" \
"M v[32:35],a[16:19],v[92:95],v[32:35]\ns_waitcnt lgkmcnt(2)\nds_read_b128 v[88:91],v20 offset:41984\nv_lshlrev_b32 v231,16,v99\nv_and_b32 v99,0xffff0000,v99\nM v[32:35],a[24:27],v[72:75],v[32:35]\nds_read_b128 v[60:63],v20 offset:43008\nv_sub_f32 v17,v15,v36\n" \
"v_fmac_f32 v134,v231,v18\nM v[36:39],a[28:31],v[72:75],0\nds_read_b128 v[72:75],v20 offset:44032\nv_fmac_f32 v135,v99,v18\nC v128,v128,v129\nM v[36:39],a[20:23],v[92:95],v[36:39]\nbuffer_load_dwordx4 v2,s[36:39],s47 offen offset:1024 sc1 lds\nC v129,v130,v131\n" \
"M v[36:39],a[12:15],v[84:87],v[36:39]\nC v130,v132,v133\nC v131,v134,v135\nv_lshlrev_b32 v236,16,v100\nM v[36:39],a[4:7],v[76:79],v[36:39]\ns_waitcnt lgkmcnt(3)\nv_and_b32 v100,0xffff0000,v100\nv_fmac_f32 v136,v236,v18\n" \
"v_fmac_f32 v137,v100,v18\nM v[40:43],a[0:3],v[80:83],0\ns_waitcnt lgkmcnt(2)\nv_lshlrev_b32 v237,16,v101\nv_and_b32 v101,0xffff0000,v101\nv_fmac_f32 v138,v237,v18\nM v[40:43],a[8:11],v[88:91],v[40:43]\ns_waitcnt lgkmcnt(1)\n" \
"ds_read_b128 v[92:95],v20 offset:36864\nv_fmac_f32 v139,v101,v18\nv_lshlrev_b32 v238,16,v102\nM v[40:43],a[16:19],v[60:63],v[40:43]\ns_waitcnt lgkmcnt(1)\nds_read_b128 v[84:87],v20 offset:37888\nv_and_b32 v102,0xffff0000,v102\nv_fmac_f32 v140,v238,v18\n" \
"M v[40:43],a[24:27],v[72:75],v[40:43]\nbuffer_store_dwordx4 v[128:131],v4,s[36:39],s62 offen offset:0 sc1\nds_read_b128 v[112:115],v57 offset:4096\nds_read_b128 v[116:119],v57 offset:5120\nds_read_b128 v[120:123],v57 offset:6144\nds_read_b128 v[124:127],v57 offset:7168\nds_read_b128 v[76:79],v20 offset:38912\nM v[44:47],a[28:31],v[72:75],0\n" \
"ds_read_b128 v[72:75],v20 offset:39936\nv_fmac_f32 v141,v102,v18\nv_lshlrev_b32 v239,16,v103\nM v[44:47],a[20:23],v[60:63],v[44:47]\nv_and_b32 v103,0xffff0000,v103\nv_fmac_f32 v142,v239,v18\nv_fmac_f32 v143,v103,v18\nM v[44:47],a[12:15],v[88:91],v[44:47]\n" \
"s_waitcnt lgkmcnt(7)\nC v136,v136,v137\nC v137,v138,v139\nv_lshlrev_b32 v244,16,v104\nM v[44:47],a[4:7],v[80:83],v[44:47]\nbuffer_load_dwordx4 v2,s[36:39],s47 offen offset:2048 sc1 lds\nv_and_b32 v104,0xffff0000,v104\nM v[32:35],a[32:35],v[92:95],v[32:35]\n" \
"s_waitcnt lgkmcnt(6)\nv_lshlrev_b32 v245,16,v105\nC v138,v140,v141\nC v139,v142,v143\nM v[32:35],a[40:43],v[84:87],v[32:35]\ns_waitcnt vmcnt(12) lgkmcnt(2)\ns_waitcnt lgkmcnt(1)\nds_read_b128 v[60:63],v20 offset:45056\n" \
"v_lshlrev_b32 v246,16,v106\nv_mul_f32 v146,v16,v146\nM v[32:35],a[48:51],v[76:79],v[32:35]\ns_barrier\ns_waitcnt lgkmcnt(1)\nds_read_b128 v[88:91],v20 offset:46080\nv_mul_f32 v147,v16,v147\nM v[32:35],a[56:59],v[72:75],v[32:35]\n" \
"ds_read_b128 v[80:83],v20 offset:47104\nv_fmac_f32 v144,v244,v18\nv_and_b32 v105,0xffff0000,v105\nM v[36:39],a[60:63],v[72:75],v[36:39]\nds_read_b128 v[72:75],v20 offset:48128\nv_fmac_f32 v145,v104,v18\nv_lshlrev_b32 v247,16,v107\nM v[36:39],a[52:55],v[76:79],v[36:39]\n" \
"buffer_load_dwordx4 v2,s[36:39],s47 offen offset:3072 sc1 lds\nv_mul_f32 v148,v16,v148\nM v[36:39],a[44:47],v[84:87],v[36:39]\ns_add_u32 m0,s22,16384\nv_mul_f32 v149,v16,v149\nv_mul_f32 v150,v16,v150\nv_and_b32 v106,0xffff0000,v106\nM v[36:39],a[36:39],v[92:95],v[36:39]\n" \
"s_waitcnt lgkmcnt(3)\ns_add_u32 s47,s29,16384\nv_fmac_f32 v146,v245,v18\nv_fmac_f32 v147,v105,v18\nC v144,v144,v145\nM v[40:43],a[32:35],v[60:63],v[40:43]\ns_waitcnt lgkmcnt(2)\nv_mul_f32 v151,v16,v151\n" \
"v_and_b32 v107,0xffff0000,v107\nv_fmac_f32 v148,v246,v18\nM v[40:43],a[40:43],v[88:91],v[40:43]\ns_waitcnt lgkmcnt(1)\nds_read_b128 v[76:79],v20 offset:16384\nv_fmac_f32 v149,v106,v18\nv_fmac_f32 v150,v247,v18\nM v[40:43],a[48:51],v[80:83],v[40:43]\n" \
"s_waitcnt lgkmcnt(1)\nds_read_b128 v[84:87],v20 offset:17408\nC v145,v146,v147\nv_fmac_f32 v151,v107,v18\nM v[40:43],a[56:59],v[72:75],v[40:43]\nbuffer_store_dwordx4 v[136:139],v4,s[36:39],s62 offen offset:1024 sc1\nds_read_b128 v[92:95],v20 offset:18432\nM v[44:47],a[60:63],v[72:75],v[44:47]\n" \
"ds_read_b128 v[72:75],v20 offset:19456\nv_sub_f32 v19,v15,v29\nC v146,v148,v149\nM v[44:47],a[52:55],v[80:83],v[44:47]\nv_exp_f32 v17,v17\nv_lshlrev_b32 v252,16,v108\nM v[44:47],a[44:47],v[88:91],v[44:47]\ns_waitcnt lgkmcnt(3)\n" \
"C v147,v150,v151\nv_lshlrev_b32 v253,16,v109\nv_mul_f32 v152,v16,v152\nM v[44:47],a[36:39],v[60:63],v[44:47]\nbuffer_load_dwordx4 v2,s[36:39],s47 offen offset:0 sc1 lds\nv_mul_f32 v153,v16,v153\nM v[32:35],a[64:67],v[76:79],v[32:35]\ns_waitcnt lgkmcnt(2)\n" \
"v_exp_f32 v19,v19\nv_and_b32 v108,0xffff0000,v108\nM v[32:35],a[72:75],v[84:87],v[32:35]\ns_waitcnt lgkmcnt(1)\nds_read_b128 v[80:83],v20 offset:24576\nv_mul_f32 v15,v25,v17\nv_mul_f32 v154,v16,v154\nM v[32:35],a[80:83],v[92:95],v[32:35]\n" \
"s_waitcnt lgkmcnt(1)\nds_read_b128 v[88:91],v20 offset:25600\nv_lshlrev_b32 v254,16,v110\nv_mul_f32 v155,v16,v155\nM v[32:35],a[88:91],v[72:75],v[32:35]\nds_read_b128 v[60:63],v20 offset:26624\nv_and_b32 v109,0xffff0000,v109\nv_fmac_f32 v152,v252,v18\n" \
"M v[36:39],a[92:95],v[72:75],v[36:39]\nLG21_%=:\ns_cbranch_execz LG23_%=\nds_read_b128 v[72:75],v20 offset:27648\nv_fmac_f32 v153,v108,v18\nv_lshlrev_b32 v255,16,v111\nM v[36:39],a[84:87],v[92:95],v[36:39]\nbuffer_load_dwordx4 v2,s[36:39],s47 offen offset:1024 sc1 lds\n" \
"v_fmac_f32 v15,v31,v19\nM v[36:39],a[76:79],v[84:87],v[36:39]\nv_mul_f32 v156,v16,v156\nv_mul_f32 v157,v16,v157\nv_and_b32 v110,0xffff0000,v110\nM v[36:39],a[68:71],v[76:79],v[36:39]\ns_waitcnt lgkmcnt(3)\nv_fmac_f32 v154,v253,v18\n" \
"v_fmac_f32 v155,v109,v18\nv_mul_f32 v158,v16,v158\nM v[40:43],a[64:67],v[80:83],v[40:43]\ns_waitcnt lgkmcnt(2)\nC v152,v152,v153\nv_mul_f32 v159,v16,v159\nv_and_b32 v111,0xffff0000,v111\nM v[40:43],a[72:75],v[88:91],v[40:43]\n" \
"s_waitcnt lgkmcnt(1)\nds_read_b128 v[92:95],v20 offset:20480\nv_fmac_f32 v156,v254,v18\nv_rcp_f32 v15,v15\nM v[40:43],a[80:83],v[60:63],v[40:43]\ns_waitcnt lgkmcnt(1)\nds_read_b128 v[84:87],v20 offset:21504\nv_fmac_f32 v157,v110,v18\n" \
"C v153,v154,v155\nM v[40:43],a[88:91],v[72:75],v[40:43]\nbuffer_store_dwordx4 v[144:147],v4,s[36:39],s62 offen offset:2048 sc1\nds_read_b128 v[76:79],v20 offset:22528\nM v[44:47],a[92:95],v[72:75],v[44:47]\nds_read_b128 v[72:75],v20 offset:23552\nv_fmac_f32 v158,v255,v18\nv_fmac_f32 v159,v111,v18\n" \
"M v[44:47],a[84:87],v[60:63],v[44:47]\nv_mul_f32 v17,v17,v15\nC v154,v156,v157\nC v155,v158,v159\nM v[44:47],a[76:79],v[88:91],v[44:47]\ns_waitcnt lgkmcnt(3)\nv_lshlrev_b32 v228,16,v112\nv_mul_f32 v19,v19,v15\n" \
"v_lshlrev_b32 v229,16,v113\nM v[44:47],a[68:71],v[80:83],v[44:47]\nbuffer_load_dwordx2 v[10:11],v5,s[36:39],0 offen sc1\nv_mul_f32 v160,v17,v160\nM v[32:35],a[96:99],v[92:95],v[32:35]\ns_waitcnt lgkmcnt(2)\nv_mul_f32 v161,v17,v161\nv_and_b32 v112,0xffff0000,v112\n" \
"v_mul_f32 v162,v17,v162\nM v[32:35],a[104:107],v[84:87],v[32:35]\ns_waitcnt vmcnt(14) lgkmcnt(12)\ns_waitcnt lgkmcnt(1)\nds_read_b128 v[60:63],v20 offset:28672\nv_lshlrev_b32 v230,16,v114\nv_mul_f32 v163,v17,v163\nM v[32:35],a[112:115],v[76:79],v[32:35]\n" \
"s_barrier\ns_waitcnt lgkmcnt(1)\nds_read_b128 v[88:91],v20 offset:29696\nv_and_b32 v113,0xffff0000,v113\nM v[32:35],a[120:123],v[72:75],v[32:35]\nds_read_b128 v[80:83],v20 offset:30720\nv_fmac_f32 v160,v228,v19\nv_fmac_f32 v161,v112,v19\n" \
"M v[36:39],a[124:127],v[72:75],v[36:39]\nds_read_b128 v[72:75],v20 offset:31744\nv_mul_f32 v164,v17,v164\nv_mul_f32 v165,v17,v165\nM v[36:39],a[116:119],v[76:79],v[36:39]\nbuffer_load_dwordx4 v2,s[36:39],s47 offen offset:2048 sc1 lds\nv_and_b32 v114,0xffff0000,v114\nM v[36:39],a[108:111],v[84:87],v[36:39]\n" \
"v_lshlrev_b32 v231,16,v115\nv_mul_f32 v166,v17,v166\nv_fmac_f32 v162,v229,v19\nM v[36:39],a[100:103],v[92:95],v[36:39]\ns_waitcnt lgkmcnt(3)\nv_fmac_f32 v163,v113,v19\nC v160,v160,v161\nv_mul_f32 v167,v17,v167\n" \
"M v[40:43],a[96:99],v[60:63],v[40:43]\ns_waitcnt lgkmcnt(2)\nv_fmac_f32 v164,v230,v19\nv_fmac_f32 v165,v114,v19\nv_and_b32 v115,0xffff0000,v115\nM v[40:43],a[104:107],v[88:91],v[40:43]\ns_waitcnt lgkmcnt(1)\nds_read_b128 v[76:79],v20 offset:49152\n" \
"v_fmac_f32 v166,v231,v19\nC v161,v162,v163\nM v[40:43],a[112:115],v[80:83],v[40:43]\ns_waitcnt lgkmcnt(1)\nds_read_b128 v[84:87],v20 offset:50176\nv_fmac_f32 v167,v115,v19\nC v162,v164,v165\nM v[40:43],a[120:123],v[72:75],v[40:43]\n" \
"buffer_store_dwordx4 v[152:155],v4,s[36:39],s62 offen offset:3072 sc1\nds_read_b128 v[92:95],v20 offset:51200\nM v[44:47],a[124:127],v[72:75],v[44:47]\ns_add_u32 s62,s51,262144\nds_read_b128 v[72:75],v20 offset:52224\nC v163,v166,v167\nv_lshlrev_b32 v236,16,v116\nM v[44:47],a[116:119],v[80:83],v[44:47]\n" \
"s_lshl_b32 s59,s61,20\nv_mul_f32 v168,v17,v168\nv_mul_f32 v169,v17,v169\nv_and_b32 v116,0xffff0000,v116\nM v[44:47],a[108:111],v[88:91],v[44:47]\ns_lshl_b32 s58,s60,15\nv_lshlrev_b32 v237,16,v117\nv_lshlrev_b32 v238,16,v118\n" \
"M v[44:47],a[100:103],v[60:63],v[44:47]\ns_waitcnt lgkmcnt(3)\nbuffer_load_dwordx4 v2,s[36:39],s47 offen offset:3072 sc1 lds\nv_mul_f32 v170,v17,v170\nM v[192:195],a[0:3],v[76:79],0\ns_waitcnt lgkmcnt(2)\ns_add_u32 m0,s22,49152\nv_lshlrev_b32 v239,16,v119\n" \
"v_mul_f32 v171,v17,v171\nv_fmac_f32 v168,v236,v19\nM v[192:195],a[8:11],v[84:87],v[192:195]\ns_waitcnt lgkmcnt(1)\ns_add_u32 s47,s30,16384\nds_read_b128 v[80:83],v20 offset:57344\nv_fmac_f32 v169,v116,v19\nv_and_b32 v117,0xffff0000,v117\n" \
"M v[192:195],a[16:19],v[92:95],v[192:195]\ns_waitcnt lgkmcnt(1)\ns_add_u32 s94,s59,s58\nds_read_b128 v[88:91],v20 offset:58368\nds_read_b128 v[60:63],v20 offset:59392\nv_mul_f32 v172,v17,v172\nM v[192:195],a[24:27],v[72:75],v[192:195]\nbuffer_load_dwordx4 v2,s[36:39],s47 offen offset:0 sc1 lds\n" \
"v_mul_f32 v173,v17,v173\nM v[196:199],a[28:31],v[72:75],0\ns_add_u32 s58,s60,1\nds_read_b128 v[72:75],v20 offset:60416\nv_and_b32 v118,0xffff0000,v118\nv_mul_f32 v174,v17,v174\nM v[196:199],a[20:23],v[92:95],v[196:199]\ns_and_b32 s58,s58,3\n" \
"v_mul_f32 v175,v17,v175\nv_fmac_f32 v170,v237,v19\nv_and_b32 v119,0xffff0000,v119\nM v[196:199],a[12:15],v[84:87],v[196:199]\ns_and_b32 s59,s58,2\nv_fmac_f32 v171,v117,v19\nC v168,v168,v169\nM v[196:199],a[4:7],v[76:79],v[196:199]\n" \
"s_waitcnt lgkmcnt(3)\ns_add_u32 s59,s59,s58\nv_fmac_f32 v172,v238,v19\nv_fmac_f32 v173,v118,v19\nM v[200:203],a[0:3],v[80:83],0\ns_waitcnt lgkmcnt(1)\ns_add_u32 s94,s94,58720256\nv_fmac_f32 v174,v239,v19\n" \
"v_fmac_f32 v175,v119,v19\nM v[200:203],a[8:11],v[88:91],v[200:203]\ns_lshl_b32 s59,s59,17\nds_read_b128 v[92:95],v20 offset:53248\nds_read_b128 v[84:87],v20 offset:54272\nM v[200:203],a[16:19],v[60:63],v[200:203]\ns_waitcnt lgkmcnt(2)\nbuffer_load_dwordx4 v2,s[36:39],s47 offen offset:1024 sc1 lds\n" \
"C v169,v170,v171\nM v[200:203],a[24:27],v[72:75],v[200:203]\ns_add_u32 s32,s94,s59\nds_read_b128 v[76:79],v20 offset:55296\nC v170,v172,v173\nC v171,v174,v175\nM v[204:207],a[28:31],v[72:75],0\ns_lshl_b32 s58,s58,1\n" \
"ds_read_b128 v[72:75],v20 offset:56320\nv_lshlrev_b32 v244,16,v120\nM v[204:207],a[20:23],v[60:63],v[204:207]\ns_and_b32 s59,%1,0xf9\nv_lshlrev_b32 v245,16,v121\nv_mul_f32 v176,v17,v176\nM v[204:207],a[12:15],v[88:91],v[204:207]\nbuffer_store_dwordx4 v[160:163],v4,s[36:39],s62 offen offset:0 sc1\n" \
"M v[204:207],a[4:7],v[80:83],v[204:207]\ns_waitcnt lgkmcnt(2)\ns_or_b32 s58,s58,s59\nds_read_b128 v[60:63],v20 offset:61440\nv_mul_f32 v177,v17,v177\nM v[192:195],a[32:35],v[92:95],v[192:195]\ns_lshl_b32 s58,s58,2\nds_read_b128 v[88:91],v20 offset:62464\n" \
"v_and_b32 v120,0xffff0000,v120\nM v[192:195],a[40:43],v[84:87],v[192:195]\ns_waitcnt vmcnt(16) lgkmcnt(14)\ns_waitcnt lgkmcnt(3)\nds_read_b128 v[80:83],v20 offset:63488\nv_lshlrev_b32 v246,16,v122\nM v[192:195],a[48:51],v[76:79],v[192:195]\ns_add_u32 s58,s58,%2\n" \
"s_barrier\ns_waitcnt lgkmcnt(3)\nv_mul_f32 v178,v17,v178\nv_lshlrev_b32 v247,16,v123\nM v[192:195],a[56:59],v[72:75],v[192:195]\ns_mul_i32 s58,s58,12288\nv_mul_f32 v179,v17,v179\nv_and_b32 v121,0xffff0000,v121\n" \
"M v[196:199],a[60:63],v[72:75],v[196:199]\ns_add_u32 s44,s58,109058048\nds_read_b128 v[72:75],v20 offset:64512\nv_fmac_f32 v176,v244,v19\nv_fmac_f32 v177,v120,v19\nM v[196:199],a[52:55],v[76:79],v[196:199]\ns_add_u32 s58,s60,2\nv_mul_f32 v180,v17,v180\n" \
"v_mul_f32 v181,v17,v181\nM v[196:199],a[44:47],v[84:87],v[196:199]\ns_and_b32 s58,s58,3\nv_mul_f32 v182,v17,v182\nv_and_b32 v122,0xffff0000,v122\nM v[196:199],a[36:39],v[92:95],v[196:199]\ns_waitcnt lgkmcnt(1)\ns_and_b32 s59,s58,2\n" \
"v_mul_f32 v183,v17,v183\nv_and_b32 v123,0xffff0000,v123\nM v[200:203],a[32:35],v[60:63],v[200:203]\ns_add_u32 s59,s59,s58\nds_read_b128 v[76:79],v4 offset:0\nv_fmac_f32 v178,v245,v19\nv_fmac_f32 v179,v121,v19\nM v[200:203],a[40:43],v[88:91],v[200:203]\n" \
"s_lshl_b32 s59,s59,17\nds_read_b128 v[84:87],v4 offset:1024\nC v176,v176,v177\nM v[200:203],a[48:51],v[80:83],v[200:203]\ns_waitcnt lgkmcnt(2)\ns_add_u32 s33,s94,s59\nv_fmac_f32 v180,v246,v19\nv_fmac_f32 v181,v122,v19\n" \
"M v[200:203],a[56:59],v[72:75],v[200:203]\nbuffer_store_dwordx4 v[168:171],v4,s[36:39],s62 offen offset:1024 sc1\nds_read_b128 v[92:95],v4 offset:2048\nM v[204:207],a[60:63],v[72:75],v[204:207]\ns_lshl_b32 s58,s58,1\nds_read_b128 v[72:75],v4 offset:3072\nv_fmac_f32 v182,v247,v19\nv_fmac_f32 v183,v123,v19\n" \
"M v[204:207],a[52:55],v[80:83],v[204:207]\ns_and_b32 s59,%1,0xf9\nds_read_b128 v[80:83],v4 offset:8192\nC v177,v178,v179\nM v[204:207],a[44:47],v[88:91],v[204:207]\ns_or_b32 s58,s58,s59\nC v178,v180,v181\nC v179,v182,v183\n" \
"M v[204:207],a[36:39],v[60:63],v[204:207]\ns_waitcnt lgkmcnt(3)\nbuffer_load_dwordx4 v2,s[36:39],s47 offen offset:2048 sc1 lds\nv_lshlrev_b32 v252,16,v124\nM v[192:195],a[64:67],v[76:79],v[192:195]\ns_lshl_b32 s58,s58,2\nds_read_b128 v[88:91],v4 offset:9216\nv_mul_f32 v184,v17,v184\n" \
"M v[192:195],a[72:75],v[84:87],v[192:195]\ns_waitcnt lgkmcnt(3)\ns_add_u32 s58,s58,%2\nds_read_b128 v[60:63],v4 offset:10240\nv_mul_f32 v185,v17,v185\nM v[192:195],a[80:83],v[92:95],v[192:195]\ns_waitcnt lgkmcnt(3)\ns_mul_i32 s58,s58,12288\n" \
"v_lshlrev_b32 v253,16,v125\nv_and_b32 v124,0xffff0000,v124\nM v[192:195],a[88:91],v[72:75],v[192:195]\ns_add_u32 s45,s58,109060096\nv_lshlrev_b32 v254,16,v126\nv_mul_f32 v186,v17,v186\nM v[196:199],a[92:95],v[72:75],v[196:199]\ns_add_u32 s58,s60,3\n" \
"ds_read_b128 v[72:75],v4 offset:11264\nv_mul_f32 v187,v17,v187\nM v[196:199],a[84:87],v[92:95],v[196:199]\nbuffer_load_dwordx4 v2,s[36:39],s47 offen offset:3072 sc1 lds\nM v[196:199],a[76:79],v[84:87],v[196:199]\ns_add_u32 m0,s22,131072\nds_read_b128 v[92:95],v4 offset:4096\nv_fmac_f32 v184,v252,v19\n" \
"M v[196:199],a[68:71],v[76:79],v[196:199]\ns_waitcnt lgkmcnt(2)\ns_add_u32 s47,s31,0\nds_read_b128 v[84:87],v4 offset:5120\nv_and_b32 v125,0xffff0000,v125\nM v[200:203],a[64:67],v[80:83],v[200:203]\ns_and_b32 s58,s58,3\nds_read_b128 v[76:79],v4 offset:6144\n" \
"v_fmac_f32 v185,v124,v19\nM v[200:203],a[72:75],v[88:91],v[200:203]\ns_and_b32 s59,s58,2\nv_mul_f32 v188,v17,v188\nv_mul_f32 v189,v17,v189\nM v[200:203],a[80:83],v[60:63],v[200:203]\ns_waitcnt lgkmcnt(3)\ns_add_u32 s59,s59,s58\n" \
"v_and_b32 v126,0xffff0000,v126\nv_fmac_f32 v186,v253,v19\nM v[200:203],a[88:91],v[72:75],v[200:203]\nbuffer_store_dwordx4 v[176:179],v4,s[36:39],s62 offen offset:2048 sc1\nv_fmac_f32 v187,v125,v19\nM v[204:207],a[92:95],v[72:75],v[204:207]\ns_lshl_b32 s59,s59,17\nds_read_b128 v[72:75],v4 offset:7168\n" \
"v_lshlrev_b32 v255,16,v127\nC v184,v184,v185\nM v[204:207],a[84:87],v[60:63],v[204:207]\ns_add_u32 s34,s94,s59\nds_read_b128 v[60:63],v4 offset:12288\nv_mul_f32 v190,v17,v190\nM v[204:207],a[76:79],v[88:91],v[204:207]\ns_lshl_b32 s58,s58,1\n" \
"ds_read_b128 v[88:91],v4 offset:13312\nv_mul_f32 v191,v17,v191\nM v[204:207],a[68:71],v[80:83],v[204:207]\ns_waitcnt lgkmcnt(3)\ns_and_b32 s59,%1,0xf9\nv_fmac_f32 v188,v254,v19\nv_fmac_f32 v189,v126,v19\nM v[192:195],a[96:99],v[92:95],v[192:195]\n" \
"s_or_b32 s58,s58,s59\nv_and_b32 v127,0xffff0000,v127\nC v185,v186,v187\nM v[192:195],a[104:107],v[84:87],v[192:195]\ns_waitcnt vmcnt(15) lgkmcnt(14)\nds_read_b128 v[80:83],v4 offset:14336\nv_fmac_f32 v190,v255,v19\nM v[192:195],a[112:115],v[76:79],v[192:195]\n" \
"s_waitcnt vmcnt(10)\ns_barrier\ns_waitcnt lgkmcnt(3)\nv_cmp_gt_u64 s[98:99],s[14:15],v[10:11]\nv_cmp_le_u64 s[18:19],s[16:17],v[10:11]\nM v[192:195],a[120:123],v[72:75],v[192:195]\ns_or_b64 s[98:99],s[98:99],s[18:19]\ns_and_b32 s98,s98,0xff000000\n" \
"s_cbranch_scc1 LPM_%=\nLPMR_%=:\nbuffer_load_dwordx4 v2,s[36:39],s47 offen offset:0 sc1 lds\nv_fmac_f32 v191,v127,v19\nM v[196:199],a[124:127],v[72:75],v[196:199]\ns_lshl_b32 s58,s58,2\nds_read_b128 v[72:75],v4 offset:15360\nC v186,v188,v189\n" \
"C v187,v190,v191\nM v[196:199],a[116:119],v[76:79],v[196:199]\ns_add_u32 s58,s58,%2\nds_read_b128 v[76:79],v4 offset:32768\nv_mov_b32 v29,s11\nM v[196:199],a[108:111],v[84:87],v[196:199]\ns_mul_i32 s58,s58,12288\nds_read_b128 v[84:87],v4 offset:33792\n" \
"v_xor_b32 v6,0x20000,v6\nM v[196:199],a[100:103],v[92:95],v[196:199]\ns_waitcnt lgkmcnt(3)\nbuffer_load_dwordx4 v2,s[36:39],s47 offen offset:1024 sc1 lds\nM v[200:203],a[96:99],v[60:63],v[200:203]\ns_add_u32 s48,s58,109062144\nds_read_b128 v[92:95],v4 offset:34816\nv_xor_b32 v7,0x20000,v7\n" \
"M v[200:203],a[104:107],v[88:91],v[200:203]\ns_add_u32 s58,s60,4\nv_xor_b32 v8,0x20000,v8\nv_xor_b32 v9,0x20000,v9\nM v[200:203],a[112:115],v[80:83],v[200:203]\ns_waitcnt lgkmcnt(3)\ns_and_b32 s58,s58,3\nv_and_b32 v30,0xf0,v4\n" \
"v_lshrrev_b32 v15,2,v30\nM v[200:203],a[120:123],v[72:75],v[200:203]\nbuffer_store_dwordx4 v[184:187],v4,s[36:39],s62 offen offset:3072 sc1\nM v[204:207],a[124:127],v[72:75],v[204:207]\ns_and_b32 s59,s58,2\nds_read_b128 v[72:75],v4 offset:35840\nv_lshlrev_b32 v13,7,v30\nM v[204:207],a[116:119],v[80:83],v[204:207]\n" \
"s_add_u32 s59,s59,s58\nds_read_b128 v[80:83],v4 offset:40960\nv_lshl_add_u32 v114,v30,2,s22\nM v[204:207],a[108:111],v[88:91],v[204:207]\ns_and_b32 s58,%1,0xf9\nds_read_b128 v[88:91],v4 offset:41984\nv_bfe_u32 v30,v4,8,1\nM v[204:207],a[100:103],v[60:63],v[204:207]\n" \
"s_waitcnt lgkmcnt(3)\nbuffer_load_dwordx4 v2,s[36:39],s47 offen offset:2048 sc1 lds\nM v[48:51],a[128:131],v[76:79],v[48:51]\ns_lshl_b32 s59,s59,17\nds_read_b128 v[60:63],v4 offset:43008\nv_lshrrev_b32 v31,9,v4\nM v[48:51],a[136:139],v[84:87],v[48:51]\ns_lshl_b32 s58,s58,2\n" \
"v_lshl_add_u32 v13,v30,5,v13\nv_lshl_add_u32 v114,v30,3,v114\nM v[48:51],a[144:147],v[92:95],v[48:51]\ns_waitcnt lgkmcnt(3)\ns_add_u32 s35,s94,s59\nv_lshl_add_u32 v13,v31,4,v13\nv_lshl_add_u32 v114,v31,4,v114\nM v[48:51],a[152:155],v[72:75],v[48:51]\n" \
"buffer_load_dwordx4 v2,s[36:39],s47 offen offset:3072 sc1 lds\nM v[52:55],a[156:159],v[72:75],v[52:55]\ns_add_u32 m0,s22,147456\nds_read_b128 v[72:75],v4 offset:44032\nv_and_b32 v30,0x3c0,v4\nM v[52:55],a[148:151],v[92:95],v[52:55]\ns_add_u32 s47,s31,8192\nds_read_b128 v[92:95],v4 offset:36864\n" \
"v_and_b32 v31,0x30,v4\nM v[52:55],a[140:143],v[84:87],v[52:55]\ns_add_u32 s58,s58,%2\nds_read_b128 v[84:87],v4 offset:37888\nv_lshl_add_u32 v112,v30,5,v31\nM v[52:55],a[132:135],v[76:79],v[52:55]\ns_waitcnt lgkmcnt(3)\nbuffer_load_dwordx4 v1,s[84:87],s46 offen offset:0 lds\n" \
"M v[64:67],a[128:131],v[80:83],v[64:67]\ns_add_u32 m0,s22,148352\nds_read_b128 v[76:79],v4 offset:38912\nv_lshl_add_u32 v113,v30,1,v31\nM v[64:67],a[136:139],v[88:91],v[64:67]\ns_sub_u32 s59,3,s60\nv_lshrrev_b32 v31,1,v31\nv_add3_u32 v115,v30,v31,s22\n" \
"M v[64:67],a[144:147],v[60:63],v[64:67]\ns_waitcnt lgkmcnt(3)\ns_lshl_b32 s95,s58,5\nv_bfe_u32 v30,v4,7,2\nv_xor_b32 v114,0x20000,v114\nM v[64:67],a[152:155],v[72:75],v[64:67]\nbuffer_load_dwordx4 v1,s[84:87],s46 offen offset:128 lds\nM v[68:71],a[156:159],v[72:75],v[68:71]\n" \
"s_mov_b32 s46,1638400\ns_add_u32 m0,s22,149504\nds_read_b128 v[72:75],v4 offset:39936\nv_xor_b32 v115,0x20000,v115\nM v[68:71],a[148:151],v[60:63],v[68:71]\ns_and_b32 s59,s59,3\nds_read_b128 v[60:63],v4 offset:45056\nv_lshrrev_b32 v190,4,v4\n" \
"M v[68:71],a[140:143],v[88:91],v[68:71]\ns_mul_i32 s58,s58,12288\nds_read_b128 v[88:91],v4 offset:46080\nM v[68:71],a[132:135],v[80:83],v[68:71]\ns_waitcnt lgkmcnt(3)\nbuffer_load_dwordx4 v1,s[84:87],s46 offen offset:0 lds\nM v[48:51],a[160:163],v[92:95],v[48:51]\ns_add_u32 m0,s22,150400\n" \
"ds_read_b128 v[80:83],v4 offset:47104\nM v[48:51],a[168:171],v[84:87],v[48:51]\ns_waitcnt vmcnt(15) lgkmcnt(15)\nM v[48:51],a[176:179],v[76:79],v[48:51]\ns_lshl_b32 s59,s59,11\ns_barrier\ns_waitcnt lgkmcnt(3)\nds_read_b128 v[176:179],v4 offset:27648\n" \
"M v[48:51],a[184:187],v[72:75],v[48:51]\nbuffer_load_dwordx4 v1,s[84:87],s46 offen offset:128 lds\nM v[52:55],a[188:191],v[72:75],v[52:55]\ns_add_u32 m0,s22,65536\ns_mov_b32 s46,1703936\nds_read_b128 v[72:75],v4 offset:48128\nM v[52:55],a[180:183],v[76:79],v[52:55]\ns_add_u32 s59,s59,s58\n" \
"ds_read_b128 v[76:79],v4 offset:16384\nM v[52:55],a[172:175],v[84:87],v[52:55]\ns_add_u32 s59,s59,109051904\nds_read_b128 v[84:87],v4 offset:17408\nM v[52:55],a[164:167],v[92:95],v[52:55]\ns_waitcnt lgkmcnt(4)\nbuffer_load_dwordx4 v1,s[84:87],s46 offen offset:0 lds\nM v[64:67],a[160:163],v[60:63],v[64:67]\n" \
"s_add_u32 m0,s22,66432\nds_read_b128 v[92:95],v4 offset:18432\nM v[64:67],a[168:171],v[88:91],v[64:67]\ns_cmp_eq_u32 s60,0\ns_cselect_b32 s40,0x40000000,s59\nds_read_b128 v[180:183],v4 offset:20480\nM v[64:67],a[176:179],v[80:83],v[64:67]\ns_waitcnt lgkmcnt(4)\n" \
"s_sub_u32 s59,4,s60\nM v[64:67],a[184:187],v[72:75],v[64:67]\nbuffer_load_dwordx4 v1,s[84:87],s46 offen offset:128 lds\nM v[68:71],a[188:191],v[72:75],v[68:71]\ns_add_u32 m0,s22,67584\ns_mov_b32 s46,1769472\nds_read_b128 v[72:75],v4 offset:19456\nM v[68:71],a[180:183],v[80:83],v[68:71]\n" \
"s_and_b32 s59,s59,3\nds_read_b128 v[80:83],v4 offset:24576\nM v[68:71],a[172:175],v[88:91],v[68:71]\ns_lshl_b32 s59,s59,11\nds_read_b128 v[88:91],v4 offset:25600\nM v[68:71],a[164:167],v[60:63],v[68:71]\ns_waitcnt lgkmcnt(3)\nbuffer_load_dwordx4 v1,s[84:87],s46 offen offset:0 lds\n" \
"M v[32:35],a[128:131],v[76:79],v[32:35]\ns_add_u32 m0,s22,68480\nds_read_b128 v[60:63],v4 offset:26624\nM v[32:35],a[136:139],v[84:87],v[32:35]\ns_add_u32 s59,s59,s58\nM v[32:35],a[144:147],v[92:95],v[32:35]\ns_waitcnt lgkmcnt(3)\ns_add_u32 s59,s59,109150208\n" \
"M v[32:35],a[152:155],v[72:75],v[32:35]\nbuffer_load_dwordx4 v1,s[84:87],s46 offen offset:128 lds\nM v[36:39],a[156:159],v[72:75],v[36:39]\ns_add_u32 m0,s22,98304\nds_read_b128 v[72:75],v4 offset:21504\nM v[36:39],a[148:151],v[92:95],v[36:39]\ns_mov_b32 s46,1835008\nds_read_b128 v[92:95],v4 offset:22528\n" \
"M v[36:39],a[140:143],v[84:87],v[36:39]\ns_cmp_eq_u32 s60,1\ns_cselect_b32 s41,0x40000000,s59\nds_read_b128 v[84:87],v4 offset:23552\nM v[36:39],a[132:135],v[76:79],v[36:39]\ns_waitcnt lgkmcnt(3)\nbuffer_load_dwordx4 v2,s[36:39],s47 offen offset:0 sc1 lds\nM v[40:43],a[128:131],v[80:83],v[40:43]\n" \
"s_sub_u32 s59,5,s60\nds_read_b128 v[76:79],v4 offset:28672\nM v[40:43],a[136:139],v[88:91],v[40:43]\ns_and_b32 s59,s59,3\nM v[40:43],a[144:147],v[60:63],v[40:43]\ns_lshl_b32 s59,s59,11\nM v[40:43],a[152:155],v[176:179],v[40:43]\nbuffer_load_dwordx4 v2,s[36:39],s47 offen offset:1024 sc1 lds\n" \
"M v[44:47],a[156:159],v[176:179],v[44:47]\ns_add_u32 s59,s59,s58\nds_read_b128 v[176:179],v4 offset:29696\nM v[44:47],a[148:151],v[60:63],v[44:47]\ns_waitcnt vmcnt(16) lgkmcnt(13)\nds_read_b128 v[60:63],v4 offset:30720\nM v[44:47],a[140:143],v[88:91],v[44:47]\ns_add_u32 s59,s59,109248512\n" \
"s_barrier\nds_read_b128 v[88:91],v4 offset:31744\nM v[44:47],a[132:135],v[80:83],v[44:47]\nbuffer_load_dwordx4 v2,s[36:39],s47 offen offset:2048 sc1 lds\nM v[32:35],a[160:163],v[180:183],v[32:35]\ns_cmp_eq_u32 s60,2\ns_cselect_b32 s42,0x40000000,s59\ns_waitcnt lgkmcnt(3)\n" \
"ds_read_b128 v[80:83],v4 offset:49152\nM v[32:35],a[168:171],v[72:75],v[32:35]\ns_sub_u32 s59,6,s60\nM v[32:35],a[176:179],v[92:95],v[32:35]\ns_and_b32 s59,s59,3\nM v[32:35],a[184:187],v[84:87],v[32:35]\nbuffer_load_dwordx4 v2,s[36:39],s47 offen offset:3072 sc1 lds\nM v[36:39],a[188:191],v[84:87],v[36:39]\n" \
"s_add_u32 m0,s22,81920\ns_add_u32 s47,s31,16384\nds_read_b128 v[84:87],v4 offset:50176\nM v[36:39],a[180:183],v[92:95],v[36:39]\ns_lshl_b32 s59,s59,11\nds_read_b128 v[92:95],v4 offset:51200\nM v[36:39],a[172:175],v[72:75],v[36:39]\ns_add_u32 s59,s59,s58\n" \
"ds_read_b128 v[72:75],v4 offset:52224\nM v[36:39],a[164:167],v[180:183],v[36:39]\nbuffer_load_dwordx4 v2,s[36:39],s47 offen offset:0 sc1 lds\nM v[40:43],a[160:163],v[76:79],v[40:43]\ns_waitcnt lgkmcnt(3)\ns_add_u32 s59,s59,109346816\nds_read_b128 v[180:183],v4 offset:57344\nM v[40:43],a[168:171],v[176:179],v[40:43]\n" \
"s_and_b32 s58,s61,6\nM v[40:43],a[176:179],v[60:63],v[40:43]\ns_cmp_eq_u32 s60,3\ns_cselect_b32 s43,0x40000000,s59\nM v[40:43],a[184:187],v[88:91],v[40:43]\nbuffer_load_dwordx4 v2,s[36:39],s47 offen offset:1024 sc1 lds\nM v[44:47],a[188:191],v[88:91],v[44:47]\ns_add_u32 s58,s58,s61\n" \
"ds_read_b128 v[88:91],v4 offset:58368\nM v[44:47],a[180:183],v[60:63],v[44:47]\ns_lshl_b32 s59,s60,4\nds_read_b128 v[60:63],v4 offset:59392\nM v[44:47],a[172:175],v[176:179],v[44:47]\ns_add_u32 s92,s58,s59\nds_read_b128 v[176:179],v4 offset:60416\nM v[44:47],a[164:167],v[76:79],v[44:47]\n" \
"buffer_load_dwordx4 v2,s[36:39],s47 offen offset:2048 sc1 lds\nM v[192:195],a[128:131],v[80:83],v[192:195]\ns_waitcnt lgkmcnt(3)\ns_and_b32 s58,s60,2\nds_read_b128 v[76:79],v4 offset:53248\nM v[192:195],a[136:139],v[84:87],v[192:195]\ns_add_u32 s58,s58,s60\nM v[192:195],a[144:147],v[92:95],v[192:195]\n" \
"s_lshl_b32 s59,s61,3\nM v[192:195],a[152:155],v[72:75],v[192:195]\nbuffer_load_dwordx4 v2,s[36:39],s47 offen offset:3072 sc1 lds\nM v[196:199],a[156:159],v[72:75],v[196:199]\ns_add_u32 m0,s22,114688\nds_read_b128 v[72:75],v4 offset:54272\nM v[196:199],a[148:151],v[92:95],v[196:199]\ns_add_u32 s47,s28,24576\n" \
"ds_read_b128 v[92:95],v4 offset:55296\nM v[196:199],a[140:143],v[84:87],v[196:199]\ns_lshr_b32 s63,%1,5\nds_read_b128 v[84:87],v4 offset:56320\nM v[196:199],a[132:135],v[80:83],v[196:199]\nbuffer_load_dwordx4 v1,s[84:87],s46 offen offset:0 lds\nM v[200:203],a[128:131],v[180:183],v[200:203]\ns_waitcnt lgkmcnt(3)\n" \
"s_add_u32 m0,s22,115584\nds_read_b128 v[80:83],v4 offset:61440\nM v[200:203],a[136:139],v[88:91],v[200:203]\ns_add_u32 s58,s58,s59\nM v[200:203],a[144:147],v[60:63],v[200:203]\ns_lshl_b32 s63,s63,2\nM v[200:203],a[152:155],v[176:179],v[200:203]\nbuffer_load_dwordx4 v1,s[84:87],s46 offen offset:128 lds\n" \
"M v[204:207],a[156:159],v[176:179],v[204:207]\ns_add_u32 m0,s22,116736\nds_read_b128 v[176:179],v4 offset:62464\nM v[204:207],a[148:151],v[60:63],v[204:207]\nLG22_%=:\ns_cbranch_execz LG24_%=\ns_waitcnt vmcnt(18) lgkmcnt(14)\nds_read_b128 v[60:63],v4 offset:63488\n" \
"M v[204:207],a[140:143],v[88:91],v[204:207]\ns_mov_b32 s46,1900544\ns_barrier\nds_read_b128 v[88:91],v4 offset:64512\nM v[204:207],a[132:135],v[180:183],v[204:207]\nbuffer_load_dwordx4 v1,s[84:87],s46 offen offset:0 lds\nM v[192:195],a[160:163],v[76:79],v[192:195]\ns_waitcnt lgkmcnt(3)\n" \
"s_add_u32 m0,s22,117632\nds_read_b128 v[180:183],v23 offset:0\nM v[192:195],a[168:171],v[72:75],v[192:195]\ns_add_u32 s93,s58,64\nM v[192:195],a[176:179],v[92:95],v[192:195]\ns_add_u32 s63,s63,%2\nM v[192:195],a[184:187],v[84:87],v[192:195]\ns_waitcnt vmcnt(21)\n" \
"M v[196:199],a[188:191],v[84:87],v[196:199]\nbuffer_store_dwordx2 v[28:29],v56,s[36:39],s50 offen offset:0 sc1\nM v[196:199],a[180:183],v[92:95],v[196:199]\ns_lshl_b32 s58,s92,15\nds_read_b128 v[84:87],v23 offset:1024\nds_read_b128 v[92:95],v23 offset:2048\nM v[196:199],a[172:175],v[72:75],v[196:199]\nbuffer_load_dwordx4 v1,s[84:87],s46 offen offset:128 lds\n" \
"M v[196:199],a[164:167],v[76:79],v[196:199]\ns_add_u32 m0,s22,0\nM v[200:203],a[160:163],v[80:83],v[200:203]\ns_waitcnt lgkmcnt(2)\ns_lshl_b32 s59,s63,6\nds_read_b128 v[72:75],v23 offset:3072\nM v[200:203],a[168:171],v[176:179],v[200:203]\ns_add_u32 s80,s58,s59\n" \
"ds_read_b128 v[76:79],v23 offset:8192\nM v[200:203],a[176:179],v[60:63],v[200:203]\nbuffer_load_dwordx4 v2,s[36:39],s47 offen offset:0 sc1 lds\nM v[200:203],a[184:187],v[88:91],v[200:203]\ns_and_b32 s58,s58,0x3e0000\nM v[204:207],a[188:191],v[88:91],v[204:207]\ns_lshl_b32 s59,s63,12\nds_read_b128 v[88:91],v23 offset:9216\n" \
"M v[204:207],a[180:183],v[60:63],v[204:207]\ns_add_u32 s58,s58,s59\nds_read_b128 v[60:63],v23 offset:10240\nM v[204:207],a[172:175],v[176:179],v[204:207]\nbuffer_load_dwordx4 v2,s[36:39],s47 offen offset:1024 sc1 lds\nM v[204:207],a[164:167],v[80:83],v[204:207]\ns_and_b32 s59,s92,1\nds_read_b128 v[176:179],v23 offset:11264\n" \
"M v[208:211],a[0:3],v[180:183],0\ns_waitcnt lgkmcnt(3)\ns_lshl_b32 s59,s59,6\nds_read_b128 v[80:83],v23 offset:4096\nM v[208:211],a[8:11],v[84:87],v[208:211]\ns_add_u32 s58,s58,s59\nM v[208:211],a[16:19],v[92:95],v[208:211]\nbuffer_load_dwordx4 v2,s[36:39],s47 offen offset:2048 sc1 lds\n" \
"M v[208:211],a[24:27],v[72:75],v[208:211]\ns_add_u32 s82,s58,67108864\nM v[212:215],a[28:31],v[72:75],0\ns_lshl_b32 s58,s93,15\nds_read_b128 v[72:75],v23 offset:5120\nM v[212:215],a[20:23],v[92:95],v[212:215]\ns_lshl_b32 s59,s63,6\nds_read_b128 v[92:95],v23 offset:6144\n" \
"M v[212:215],a[12:15],v[84:87],v[212:215]\nbuffer_load_dwordx4 v2,s[36:39],s47 offen offset:3072 sc1 lds\nM v[212:215],a[4:7],v[180:183],v[212:215]\ns_add_u32 m0,s22,32768\ns_add_u32 s47,s29,24576\nds_read_b128 v[84:87],v23 offset:7168\nM v[216:219],a[0:3],v[76:79],0\ns_waitcnt lgkmcnt(3)\n" \
"s_add_u32 s81,s58,s59\nds_read_b128 v[180:183],v23 offset:12288\nM v[216:219],a[8:11],v[88:91],v[216:219]\ns_and_b32 s58,s58,0x3e0000\nM v[216:219],a[16:19],v[60:63],v[216:219]\nbuffer_load_dwordx4 v2,s[36:39],s47 offen offset:0 sc1 lds\nM v[216:219],a[24:27],v[176:179],v[216:219]\ns_lshl_b32 s59,s63,12\n" \
"M v[220:223],a[28:31],v[176:179],0\ns_add_u32 s58,s58,s59\nds_read_b128 v[176:179],v23 offset:13312\nM v[220:223],a[20:23],v[60:63],v[220:223]\ns_waitcnt vmcnt(10) lgkmcnt(14)\nds_read_b128 v[60:63],v23 offset:14336\nM v[220:223],a[12:15],v[88:91],v[220:223]\nbuffer_load_dwordx4 v2,s[36:39],s47 offen offset:1024 sc1 lds\n" \
"s_barrier\nM v[220:223],a[4:7],v[76:79],v[220:223]\ns_and_b32 s59,s93,1\nds_read_b128 v[88:91],v23 offset:15360\nM v[208:211],a[32:35],v[80:83],v[208:211]\ns_waitcnt lgkmcnt(3)\ns_lshl_b32 s59,s59,6\nds_read_b128 v[76:79],v20 offset:32768\n" \
"M v[208:211],a[40:43],v[72:75],v[208:211]\ns_add_u32 s58,s58,s59\nds_read_b64_tr_b16 a[192:193],v6 offset:16384\nM v[208:211],a[48:51],v[92:95],v[208:211]\nbuffer_load_dwordx4 v2,s[36:39],s47 offen offset:2048 sc1 lds\nM v[208:211],a[56:59],v[84:87],v[208:211]\ns_add_u32 s83,s58,67108864\nds_read_b64_tr_b16 a[194:195],v7 offset:16384\n" \
"M v[212:215],a[60:63],v[84:87],v[212:215]\ns_lshl_b32 s58,s61,10\nds_read_b128 v[84:87],v20 offset:33792\nM v[212:215],a[52:55],v[92:95],v[212:215]\ns_lshl_b32 s59,s60,6\nds_read_b128 v[92:95],v20 offset:34816\nM v[212:215],a[44:47],v[72:75],v[212:215]\nbuffer_load_dwordx4 v2,s[36:39],s47 offen offset:3072 sc1 lds\n" \
"M v[212:215],a[36:39],v[80:83],v[212:215]\ns_add_u32 m0,s22,16384\nds_read_b128 v[72:75],v20 offset:35840\nM v[216:219],a[32:35],v[180:183],v[216:219]\ns_waitcnt lgkmcnt(4)\ns_add_u32 s47,s30,24576\nds_read_b128 v[80:83],v20 offset:40960\nM v[216:219],a[40:43],v[176:179],v[216:219]\n" \
"s_add_u32 s58,s58,s59\nds_read_b64_tr_b16 a[200:201],v6 offset:18432\nv_xor_b32 v6,0x30000,v6\nM v[216:219],a[48:51],v[60:63],v[216:219]\nbuffer_load_dwordx4 v2,s[36:39],s47 offen offset:0 sc1 lds\nM v[216:219],a[56:59],v[88:91],v[216:219]\ns_add_u32 s95,s95,94437376\nM v[220:223],a[60:63],v[88:91],v[220:223]\n" \
"s_add_u32 s58,s58,94396416\nds_read_b128 v[88:91],v20 offset:41984\nv_lshl_add_u32 v58,v30,8,s58\nM v[220:223],a[52:55],v[60:63],v[220:223]\ns_mov_b64 s[64:65],s[12:13]\nds_read_b128 v[60:63],v20 offset:43008\nv_bfe_u32 v30,v4,4,3\nM v[220:223],a[44:47],v[176:179],v[220:223]\n" \
"buffer_load_dwordx4 v2,s[36:39],s47 offen offset:1024 sc1 lds\nM v[220:223],a[36:39],v[180:183],v[220:223]\ns_add_u32 s58,s95,24\nds_read_b128 v[176:179],v20 offset:44032\nv_lshl_add_u32 v58,v30,3,v58\nM v[208:211],a[64:67],v[76:79],v[208:211]\ns_waitcnt lgkmcnt(3)\ns_and_b32 s65,s65,0xffff\n" \
"ds_read_b128 v[180:183],v20 offset:36864\nv_and_b32 v30,0x30,v4\nM v[208:211],a[72:75],v[84:87],v[208:211]\ns_mov_b32 s66,4194304\nds_read_b64_tr_b16 a[196:197],v8 offset:16384\ns_mov_b32 exec_lo,0\nv_lshl_add_u32 v58,v30,4,s58\ns_mov_b32 exec_lo,-1\n" \
"s_nop 1\nM v[208:211],a[80:83],v[92:95],v[208:211]\ns_mov_b32 s67,0x00020000\nds_read_b64_tr_b16 a[202:203],v7 offset:18432\nv_xor_b32 v7,0x30000,v7\nM v[208:211],a[88:91],v[72:75],v[208:211]\nbuffer_load_dwordx4 v2,s[36:39],s47 offen offset:2048 sc1 lds\nM v[212:215],a[92:95],v[72:75],v[212:215]\n" \
"s_mov_b32 s58,0x300003\nds_read_b128 v[72:75],v20 offset:37888\nv_and_b32 v30,16,v4\nM v[212:215],a[84:87],v[92:95],v[212:215]\ns_mov_b32 s59,0x30000300\nds_read_b128 v[92:95],v20 offset:38912\nv_mov_b32 v31,0x3f80\nM v[212:215],a[76:79],v[84:87],v[212:215]\n" \
"s_lshl_b32 s92,s92,6\nds_read_b128 v[84:87],v20 offset:39936\nv_lshlrev_b32 v30,v30,v31\nM v[212:215],a[68:71],v[76:79],v[212:215]\nbuffer_load_dwordx4 v2,s[36:39],s47 offen offset:3072 sc1 lds\nM v[216:219],a[64:67],v[80:83],v[216:219]\ns_waitcnt lgkmcnt(3)\ns_add_u32 m0,s22,49152\n" \
"v_cndmask_b32 v186,0,v30,s[58:59]\nv_mov_b32 v188,0\nM v[216:219],a[72:75],v[88:91],v[216:219]\ns_add_u32 s47,s31,24576\nds_read_b128 v[76:79],v20 offset:45056\nv_mov_b32 v189,0\nM v[216:219],a[80:83],v[60:63],v[216:219]\ns_mov_b32 s58,0xc0000c\n" \
"ds_read_b64_tr_b16 a[198:199],v9 offset:16384\nds_read_b64_tr_b16 a[204:205],v8 offset:18432\nM v[216:219],a[88:91],v[176:179],v[216:219]\ns_mov_b32 s59,0xc0000c00\nds_read_b64_tr_b16 a[206:207],v9 offset:18432\nv_xor_b32 v8,0x30000,v8\nM v[220:223],a[92:95],v[176:179],v[220:223]\nbuffer_load_dwordx4 v2,s[36:39],s47 offen offset:0 sc1 lds\n" \
"M v[220:223],a[84:87],v[60:63],v[220:223]\ns_add_u32 s92,s92,71303168\nds_read_b128 v[176:179],v20 offset:46080\nv_xor_b32 v9,0x30000,v9\nM v[220:223],a[76:79],v[88:91],v[220:223]\ns_lshl_b32 s93,s93,6\nds_read_b128 v[60:63],v20 offset:47104\nv_cndmask_b32 v187,0,v30,s[58:59]\n" \
"M v[220:223],a[68:71],v[80:83],v[220:223]\ns_add_u32 s58,s60,1\nds_read_b128 v[88:91],v20 offset:48128\nv_add_u32 v191,s58,v190\nM v[208:211],a[96:99],v[180:183],v[208:211]\ns_waitcnt lgkmcnt(4)\nbuffer_load_dwordx4 v2,s[36:39],s47 offen offset:1024 sc1 lds\nM v[208:211],a[104:107],v[72:75],v[208:211]\n" \
"s_add_u32 s93,s93,71303168\nv_and_b32 v191,3,v191\nv_lshlrev_b32 v191,8,v191\nM v[208:211],a[112:115],v[92:95],v[208:211]\ns_lshl_b32 s58,s60,8\nds_read_b128 v[80:83],v20 offset:16384\nv_lshl_add_u32 v191,v190,3,v191\nM v[208:211],a[120:123],v[84:87],v[208:211]\n" \
"s_add_u32 s58,s58,s95\nds_read_b64_tr_b16 a[208:209],v6 offset:0\nv_add_u32 v190,1,v190\nM v[212:215],a[124:127],v[84:87],v[212:215]\ns_sub_u32 s49,s72,8192\nds_read_b128 v[84:87],v20 offset:17408\nv_lshrrev_b32 v190,2,v190\nM v[212:215],a[116:119],v[92:95],v[212:215]\n" \
"buffer_load_dwordx4 v2,s[36:39],s47 offen offset:2048 sc1 lds\nM v[212:215],a[108:111],v[72:75],v[212:215]\ns_add_u32 s63,s49,0\nds_read_b128 v[92:95],v20 offset:18432\nv_lshl_add_u32 v191,v190,27,v191\nM v[212:215],a[100:103],v[180:183],v[212:215]\ns_add_u32 s59,s80,65536\nds_read_b128 v[72:75],v20 offset:19456\n" \
"v_bfe_u32 v190,v4,4,2\nM v[216:219],a[96:99],v[76:79],v[216:219]\ns_waitcnt lgkmcnt(4)\nds_read_b128 v[180:183],v20 offset:24576\nv_lshl_add_u32 v190,v190,3,s58\nM v[216:219],a[104:107],v[176:179],v[216:219]\nbuffer_load_dwordx4 v2,s[36:39],s47 offen offset:3072 sc1 lds\nM v[216:219],a[112:115],v[60:63],v[216:219]\n" \
"s_add_u32 m0,s22,131072\nv_mov_b32 v184,0\nv_mov_b32 v185,0\nM v[216:219],a[120:123],v[88:91],v[216:219]\ns_add_u32 s47,s80,0\nds_read_b64_tr_b16 a[210:211],v7 offset:0\nds_read_b64_tr_b16 a[212:213],v8 offset:0\nM v[220:223],a[124:127],v[88:91],v[220:223]\n" \
"s_add_u32 s58,s80,0\nds_read_b128 v[88:91],v20 offset:25600\nM v[220:223],a[116:119],v[60:63],v[220:223]\nds_read_b128 v[60:63],v20 offset:26624\nM v[220:223],a[108:111],v[176:179],v[220:223]\nds_read_b128 v[176:179],v20 offset:27648\nM v[220:223],a[100:103],v[76:79],v[220:223]\nds_read_b64_tr_b16 a[214:215],v9 offset:0\n" \
"M v[208:211],a[128:131],v[80:83],v[208:211]\ns_waitcnt lgkmcnt(6)\nds_read_b64_tr_b16 a[216:217],v6 offset:2048\nM v[208:211],a[136:139],v[84:87],v[208:211]\nds_read_b64_tr_b16 a[218:219],v7 offset:2048\nds_read_b64_tr_b16 a[220:221],v8 offset:2048\nM v[208:211],a[144:147],v[92:95],v[208:211]\nds_read_b64_tr_b16 a[222:223],v9 offset:2048\n" \
"M v[208:211],a[152:155],v[72:75],v[208:211]\nds_read_b128 v[76:79],v20 offset:20480\nM v[212:215],a[156:159],v[72:75],v[212:215]\nds_read_b128 v[72:75],v20 offset:21504\nM v[212:215],a[148:151],v[92:95],v[212:215]\nds_read_b128 v[92:95],v20 offset:22528\nM v[212:215],a[140:143],v[84:87],v[212:215]\nds_read_b128 v[84:87],v20 offset:23552\n" \
"M v[212:215],a[132:135],v[80:83],v[212:215]\nds_read_b128 v[80:83],v20 offset:28672\nM v[216:219],a[128:131],v[180:183],v[216:219]\ns_waitcnt lgkmcnt(5)\nM v[216:219],a[136:139],v[88:91],v[216:219]\nM v[216:219],a[144:147],v[60:63],v[216:219]\nM v[216:219],a[152:155],v[176:179],v[216:219]\nM v[220:223],a[156:159],v[176:179],v[220:223]\n" \
"s_waitcnt lgkmcnt(1)\nds_read_b128 v[176:179],v20 offset:29696\nM v[220:223],a[148:151],v[60:63],v[220:223]\ns_waitcnt vmcnt(8) lgkmcnt(6)\nds_read_b128 v[60:63],v20 offset:30720\nM v[220:223],a[140:143],v[88:91],v[220:223]\ns_barrier\nds_read_b128 v[88:91],v20 offset:31744\n" \
"M v[220:223],a[132:135],v[180:183],v[220:223]\nbuffer_load_dwordx4 v112,s[64:67],s47 offen offset:0 lds\nM v[208:211],a[160:163],v[76:79],v[208:211]\ns_add_u32 m0,s22,132096\nds_read_b128 v[180:183],v4 offset:0\nM v[208:211],a[168:171],v[72:75],v[208:211]\ns_add_u32 s47,s80,65536\nds_read_b64_tr_b16 a[224:225],v6 offset:49152\n" \
"M v[208:211],a[176:179],v[92:95],v[208:211]\nds_read_b64_tr_b16 a[226:227],v7 offset:49152\nds_read_b64_tr_b16 a[232:233],v6 offset:51200\nM v[208:211],a[184:187],v[84:87],v[208:211]\nbuffer_load_dwordx4 v112,s[64:67],s47 offen offset:0 lds\nM v[212:215],a[188:191],v[84:87],v[212:215]\ns_add_u32 m0,s22,133120\nM v[212:215],a[180:183],v[92:95],v[212:215]\n" \
"s_add_u32 s47,s81,0\nds_read_b128 v[84:87],v4 offset:1024\nM v[212:215],a[172:175],v[72:75],v[212:215]\ns_waitcnt lgkmcnt(4)\nds_read_b128 v[92:95],v4 offset:2048\nM v[212:215],a[164:167],v[76:79],v[212:215]\nbuffer_load_dwordx4 v112,s[64:67],s47 offen offset:0 lds\nM v[216:219],a[160:163],v[80:83],v[216:219]\n" \
"s_add_u32 m0,s22,134144\nds_read_b128 v[72:75],v4 offset:3072\nM v[216:219],a[168:171],v[176:179],v[216:219]\ns_add_u32 s47,s81,65536\nds_read_b128 v[76:79],v4 offset:8192\nM v[216:219],a[176:179],v[60:63],v[216:219]\nds_read_b64_tr_b16 a[234:235],v7 offset:51200\nM v[216:219],a[184:187],v[88:91],v[216:219]\n" \
"buffer_load_dwordx4 v112,s[64:67],s47 offen offset:0 lds\nM v[220:223],a[188:191],v[88:91],v[220:223]\ns_add_u32 m0,s22,147456\nds_read_b128 v[88:91],v4 offset:9216\nM v[220:223],a[180:183],v[60:63],v[220:223]\ns_add_u32 s47,s32,0\nds_read_b128 v[60:63],v4 offset:10240\nM v[220:223],a[172:175],v[176:179],v[220:223]\n" \
"s_waitcnt lgkmcnt(4)\nds_read_b128 v[176:179],v4 offset:11264\nM v[220:223],a[164:167],v[80:83],v[220:223]\nbuffer_load_dwordx4 v113,s[36:39],s82 offen offset:0 sc1 lds\nM v[48:51],a[192:195],v[180:183],v[48:51]\ns_add_u32 m0,s22,146432\nM v[48:51],a[200:203],v[84:87],v[48:51]\nds_read_b128 v[80:83],v4 offset:4096\n" \
"M v[48:51],a[208:211],v[92:95],v[48:51]\nds_read_b64_tr_b16 a[236:237],v8 offset:51200\nM v[48:51],a[216:219],v[72:75],v[48:51]\nbuffer_load_dwordx4 v113,s[36:39],s82 offen offset:2048 sc1 lds\nM v[52:55],a[220:223],v[72:75],v[52:55]\ns_add_u32 m0,s22,149504\nds_read_b128 v[72:75],v4 offset:5120\nM v[52:55],a[212:215],v[92:95],v[52:55]\n" \
"ds_read_b64_tr_b16 a[238:239],v9 offset:51200\nM v[52:55],a[204:207],v[84:87],v[52:55]\ns_waitcnt lgkmcnt(4)\nds_read_b64_tr_b16 a[228:229],v8 offset:49152\nM v[52:55],a[196:199],v[180:183],v[52:55]\nbuffer_load_dwordx2 v[10:11],v58,s[36:39],0 offen sc1\nM v[64:67],a[192:195],v[76:79],v[64:67]\nds_read_b128 v[92:95],v4 offset:6144\n" \
"ds_read_b64_tr_b16 a[230:231],v9 offset:49152\nM v[64:67],a[200:203],v[88:91],v[64:67]\nbuffer_load_dwordx4 v113,s[36:39],s83 offen offset:0 sc1 lds\nM v[64:67],a[208:211],v[60:63],v[64:67]\ns_add_u32 m0,s22,148480\nM v[64:67],a[216:219],v[176:179],v[64:67]\nds_read_b128 v[84:87],v4 offset:7168\nM v[68:71],a[220:223],v[176:179],v[68:71]\n" \
"ds_read_b128 v[180:183],v4 offset:12288\nM v[68:71],a[212:215],v[60:63],v[68:71]\nbuffer_load_dwordx4 v113,s[36:39],s83 offen offset:2048 sc1 lds\nM v[68:71],a[204:207],v[88:91],v[68:71]\ns_add_u32 m0,s22,65536\nds_read_b128 v[176:179],v4 offset:13312\nM v[68:71],a[196:199],v[76:79],v[68:71]\ns_waitcnt lgkmcnt(3)\n" \
"ds_read_b128 v[60:63],v4 offset:14336\nM v[48:51],a[224:227],v[80:83],v[48:51]\nds_read_b128 v[88:91],v4 offset:15360\nM v[48:51],a[232:235],v[72:75],v[48:51]\nbuffer_load_dword v16,v15,s[36:39],s92 offen offset:0 sc1\nM v[48:51],a[240:243],v[92:95],v[48:51]\ns_waitcnt lgkmcnt(3)\nds_read_b128 v[76:79],v4 offset:32768\n" \
"M v[48:51],a[248:251],v[84:87],v[48:51]\nbuffer_load_dword v17,v15,s[36:39],s92 offen offset:128 sc1\nM v[52:55],a[252:255],v[84:87],v[52:55]\nds_read_b128 v[84:87],v4 offset:33792\nM v[52:55],a[244:247],v[92:95],v[52:55]\nbuffer_load_dword v18,v15,s[36:39],s93 offen offset:0 sc1\nM v[52:55],a[236:239],v[72:75],v[52:55]\nds_read_b128 v[92:95],v4 offset:34816\n" \
"ds_read_b128 v[72:75],v4 offset:35840\nM v[52:55],a[228:231],v[80:83],v[52:55]\nbuffer_load_dword v19,v15,s[36:39],s93 offen offset:128 sc1\nM v[64:67],a[224:227],v[180:183],v[64:67]\ns_waitcnt lgkmcnt(3)\nds_read_b128 v[80:83],v4 offset:40960\nC v48,v48,v49\nM v[64:67],a[232:235],v[176:179],v[64:67]\n" \
"C v49,v50,v51\nC v50,v52,v53\nM v[64:67],a[240:243],v[60:63],v[64:67]\nC v51,v54,v55\nv_cndmask_b32 v120,v120,v48,s[96:97]\nM v[64:67],a[248:251],v[88:91],v[64:67]\nv_cndmask_b32 v121,v121,v49,s[96:97]\nv_cndmask_b32 v122,v122,v50,s[96:97]\n" \
"M v[68:71],a[252:255],v[88:91],v[68:71]\nds_read_b128 v[88:91],v4 offset:41984\nv_cndmask_b32 v123,v123,v51,s[96:97]\nM v[68:71],a[244:247],v[60:63],v[68:71]\nds_read_b128 v[60:63],v4 offset:43008\nC v64,v64,v65\nM v[68:71],a[236:239],v[176:179],v[68:71]\nds_read_b128 v[176:179],v4 offset:44032\n" \
"C v65,v66,v67\nM v[68:71],a[228:231],v[180:183],v[68:71]\nds_read_b128 v[180:183],v4 offset:36864\nv_cndmask_b32 v124,v124,v64,s[96:97]\nM v[32:35],a[192:195],v[76:79],v[32:35]\ns_waitcnt lgkmcnt(4)\nv_cndmask_b32 v125,v125,v65,s[96:97]\nM v[32:35],a[200:203],v[84:87],v[32:35]\n" \
"C v66,v68,v69\nC v67,v70,v71\nM v[32:35],a[208:211],v[92:95],v[32:35]\nv_cndmask_b32 v126,v126,v66,s[96:97]\nv_cndmask_b32 v127,v127,v67,s[96:97]\nM v[32:35],a[216:219],v[72:75],v[32:35]\ns_cmp_eq_u32 s60,1\ns_cselect_b64 s[96:97],-1,0\n" \
"M v[36:39],a[220:223],v[72:75],v[36:39]\nds_read_b128 v[72:75],v4 offset:37888\nM v[36:39],a[212:215],v[92:95],v[36:39]\nds_read_b128 v[92:95],v4 offset:38912\nM v[36:39],a[204:207],v[84:87],v[36:39]\nds_read_b128 v[84:87],v4 offset:39936\nM v[36:39],a[196:199],v[76:79],v[36:39]\nds_read_b128 v[76:79],v4 offset:45056\n" \
"M v[40:43],a[192:195],v[80:83],v[40:43]\ns_waitcnt lgkmcnt(4)\nM v[40:43],a[200:203],v[88:91],v[40:43]\nM v[40:43],a[208:211],v[60:63],v[40:43]\nM v[40:43],a[216:219],v[176:179],v[40:43]\nM v[44:47],a[220:223],v[176:179],v[44:47]\nds_read_b128 v[176:179],v4 offset:46080\nM v[44:47],a[212:215],v[60:63],v[44:47]\n" \
"s_waitcnt vmcnt(13) lgkmcnt(14)\nds_read_b128 v[60:63],v4 offset:47104\nM v[44:47],a[204:207],v[88:91],v[44:47]\ns_barrier\nds_read_b128 v[88:91],v4 offset:48128\nM v[44:47],a[196:199],v[80:83],v[44:47]\nds_read_b128 v[80:83],v4 offset:16384\nM v[32:35],a[224:227],v[180:183],v[32:35]\n" \
"s_waitcnt lgkmcnt(4)\nM v[32:35],a[232:235],v[72:75],v[32:35]\nM v[32:35],a[240:243],v[92:95],v[32:35]\nM v[32:35],a[248:251],v[84:87],v[32:35]\nM v[36:39],a[252:255],v[84:87],v[36:39]\ns_waitcnt vmcnt(6)\nv_cmp_gt_u64 s[98:99],s[14:15],v[10:11]\nv_cmp_le_u64 s[18:19],s[16:17],v[10:11]\n" \
"M v[36:39],a[244:247],v[92:95],v[36:39]\ns_or_b64 s[98:99],s[98:99],s[18:19]\ns_cbranch_scc1 LPL_%=\nLPLR_%=:\nbuffer_load_dwordx4 v2,s[36:39],s47 offen offset:0 sc1 lds\nM v[36:39],a[236:239],v[72:75],v[36:39]\nds_read_b128 v[84:87],v4 offset:17408\nC v32,v32,v33\n" \
"M v[36:39],a[228:231],v[180:183],v[36:39]\nds_read_b128 v[92:95],v4 offset:18432\nC v33,v34,v35\nM v[40:43],a[224:227],v[76:79],v[40:43]\ns_waitcnt lgkmcnt(2)\nds_read_b128 v[72:75],v4 offset:19456\nv_cndmask_b32 v120,v120,v32,s[96:97]\nM v[40:43],a[232:235],v[176:179],v[40:43]\n" \
"buffer_load_dwordx4 v2,s[36:39],s47 offen offset:1024 sc1 lds\nM v[40:43],a[240:243],v[60:63],v[40:43]\nds_read_b128 v[180:183],v4 offset:24576\nC v34,v36,v37\nM v[40:43],a[248:251],v[88:91],v[40:43]\nC v35,v38,v39\nv_cndmask_b32 v121,v121,v33,s[96:97]\nM v[44:47],a[252:255],v[88:91],v[44:47]\n" \
"ds_read_b128 v[88:91],v4 offset:25600\nv_cndmask_b32 v122,v122,v34,s[96:97]\nM v[44:47],a[244:247],v[60:63],v[44:47]\nbuffer_load_dwordx4 v2,s[36:39],s47 offen offset:2048 sc1 lds\nM v[44:47],a[236:239],v[176:179],v[44:47]\nds_read_b128 v[60:63],v4 offset:26624\nC v40,v40,v41\nM v[44:47],a[228:231],v[76:79],v[44:47]\n" \
"ds_read_b128 v[176:179],v4 offset:27648\nC v41,v42,v43\nM v[192:195],a[192:195],v[80:83],v[192:195]\ns_waitcnt lgkmcnt(3)\nds_read_b128 v[76:79],v4 offset:20480\nv_cndmask_b32 v123,v123,v35,s[96:97]\nM v[192:195],a[200:203],v[84:87],v[192:195]\nbuffer_load_dwordx4 v2,s[36:39],s47 offen offset:3072 sc1 lds\n" \
"M v[192:195],a[208:211],v[92:95],v[192:195]\ns_add_u32 m0,s22,98304\ns_add_u32 s47,s32,8192\nC v42,v44,v45\nC v43,v46,v47\nM v[192:195],a[216:219],v[72:75],v[192:195]\nv_cndmask_b32 v124,v124,v40,s[96:97]\nv_cndmask_b32 v125,v125,v41,s[96:97]\n" \
"M v[196:199],a[220:223],v[72:75],v[196:199]\nds_read_b128 v[72:75],v4 offset:21504\nv_cndmask_b32 v126,v126,v42,s[96:97]\nM v[196:199],a[212:215],v[92:95],v[196:199]\nbuffer_load_dwordx4 v2,s[36:39],s47 offen offset:0 sc1 lds\nM v[196:199],a[204:207],v[84:87],v[196:199]\nds_read_b128 v[92:95],v4 offset:22528\nv_cndmask_b32 v127,v127,v43,s[96:97]\n" \
"M v[196:199],a[196:199],v[80:83],v[196:199]\ns_cmp_eq_u32 s60,2\ns_cselect_b64 s[96:97],-1,0\nds_read_b128 v[84:87],v4 offset:23552\nM v[200:203],a[192:195],v[180:183],v[200:203]\ns_waitcnt lgkmcnt(3)\nds_read_b128 v[80:83],v4 offset:28672\nM v[200:203],a[200:203],v[88:91],v[200:203]\n" \
"buffer_load_dwordx4 v2,s[36:39],s47 offen offset:1024 sc1 lds\nM v[200:203],a[208:211],v[60:63],v[200:203]\nM v[200:203],a[216:219],v[176:179],v[200:203]\nM v[204:207],a[220:223],v[176:179],v[204:207]\nds_read_b128 v[176:179],v4 offset:29696\nM v[204:207],a[212:215],v[60:63],v[204:207]\nbuffer_load_dwordx4 v2,s[36:39],s47 offen offset:2048 sc1 lds\nM v[204:207],a[204:207],v[88:91],v[204:207]\n" \
"ds_read_b128 v[60:63],v4 offset:30720\nM v[204:207],a[196:199],v[180:183],v[204:207]\nds_read_b128 v[88:91],v4 offset:31744\nM v[192:195],a[224:227],v[76:79],v[192:195]\ns_waitcnt lgkmcnt(3)\nds_read_b128 v[180:183],v4 offset:49152\nM v[192:195],a[232:235],v[72:75],v[192:195]\nbuffer_load_dwordx4 v2,s[36:39],s47 offen offset:3072 sc1 lds\n" \
"M v[192:195],a[240:243],v[92:95],v[192:195]\ns_add_u32 m0,s22,81920\nM v[192:195],a[248:251],v[84:87],v[192:195]\ns_add_u32 s47,s32,16384\nM v[196:199],a[252:255],v[84:87],v[196:199]\ns_waitcnt vmcnt(8)\nds_read_b128 v[84:87],v4 offset:50176\nv_sub_f32 v24,1.0,v16\n" \
"M v[196:199],a[244:247],v[92:95],v[196:199]\nbuffer_store_dwordx4 v[48:51],v4,s[36:39],s40 offen offset:0 sc1\nM v[196:199],a[236:239],v[72:75],v[196:199]\nds_read_b128 v[92:95],v4 offset:51200\nds_read_b128 v[72:75],v4 offset:52224\nM v[196:199],a[228:231],v[76:79],v[196:199]\nds_read_b128 v[76:79],v4 offset:57344\nC v192,v192,v193\n" \
"M v[200:203],a[224:227],v[80:83],v[200:203]\ns_waitcnt lgkmcnt(4)\nC v193,v194,v195\nv_cndmask_b32 v120,v120,v192,s[96:97]\nM v[200:203],a[232:235],v[176:179],v[200:203]\nLG23_%=:\ns_cbranch_execz LG25_%=\nbuffer_load_dwordx4 v2,s[36:39],s47 offen offset:0 sc1 lds\n" \
"M v[200:203],a[240:243],v[60:63],v[200:203]\nC v194,v196,v197\nC v195,v198,v199\nM v[200:203],a[248:251],v[88:91],v[200:203]\nv_cndmask_b32 v121,v121,v193,s[96:97]\nv_cndmask_b32 v122,v122,v194,s[96:97]\nM v[204:207],a[252:255],v[88:91],v[204:207]\nds_read_b128 v[88:91],v4 offset:58368\n" \
"v_cndmask_b32 v123,v123,v195,s[96:97]\nM v[204:207],a[244:247],v[60:63],v[204:207]\nbuffer_load_dwordx4 v2,s[36:39],s47 offen offset:1024 sc1 lds\nM v[204:207],a[236:239],v[176:179],v[204:207]\nds_read_b128 v[60:63],v4 offset:59392\nC v200,v200,v201\nM v[204:207],a[228:231],v[80:83],v[204:207]\nds_read_b128 v[176:179],v4 offset:60416\n" \
"C v201,v202,v203\nM v[208:211],a[192:195],v[180:183],v[208:211]\ns_waitcnt lgkmcnt(3)\nds_read_b128 v[80:83],v4 offset:53248\nv_cndmask_b32 v124,v124,v200,s[96:97]\nM v[208:211],a[200:203],v[84:87],v[208:211]\nbuffer_load_dwordx4 v2,s[36:39],s47 offen offset:2048 sc1 lds\nM v[208:211],a[208:211],v[92:95],v[208:211]\n" \
"C v202,v204,v205\nC v203,v206,v207\nM v[208:211],a[216:219],v[72:75],v[208:211]\nv_cndmask_b32 v125,v125,v201,s[96:97]\nv_cndmask_b32 v126,v126,v202,s[96:97]\nM v[212:215],a[220:223],v[72:75],v[212:215]\nds_read_b128 v[72:75],v4 offset:54272\nv_cndmask_b32 v127,v127,v203,s[96:97]\n" \
"M v[212:215],a[212:215],v[92:95],v[212:215]\nbuffer_store_dwordx4 v[64:67],v4,s[36:39],s40 offen offset:1024 sc1\nM v[212:215],a[204:207],v[84:87],v[212:215]\ns_cmp_eq_u32 s60,3\ns_cselect_b64 s[96:97],-1,0\nds_read_b128 v[92:95],v4 offset:55296\nds_read_b128 v[84:87],v4 offset:56320\nM v[212:215],a[196:199],v[180:183],v[212:215]\n" \
"ds_read_b128 v[180:183],v4 offset:61440\nC v24,v24,v24\nM v[216:219],a[192:195],v[76:79],v[216:219]\ns_waitcnt lgkmcnt(4)\nv_and_b32 v24,0xffff0000,v24\nv_sub_f32 v25,1.0,v17\nM v[216:219],a[200:203],v[88:91],v[216:219]\nbuffer_load_dwordx4 v2,s[36:39],s47 offen offset:3072 sc1 lds\n" \
"M v[216:219],a[208:211],v[60:63],v[216:219]\ns_add_u32 m0,s22,114688\ns_add_u32 s47,s32,24576\nC v25,v25,v25\nv_and_b32 v25,0xffff0000,v25\nM v[216:219],a[216:219],v[176:179],v[216:219]\nv_sub_f32 v26,1.0,v18\nv_sub_f32 v27,1.0,v19\n" \
"M v[220:223],a[220:223],v[176:179],v[220:223]\nds_read_b128 v[176:179],v4 offset:62464\nC v26,v26,v26\nM v[220:223],a[212:215],v[60:63],v[220:223]\ns_waitcnt vmcnt(6) lgkmcnt(14)\nbuffer_load_dwordx4 v2,s[36:39],s47 offen offset:0 sc1 lds\nM v[220:223],a[204:207],v[88:91],v[220:223]\ns_barrier\n" \
"ds_read_b128 v[60:63],v4 offset:63488\nM v[220:223],a[196:199],v[76:79],v[220:223]\nds_read_b128 v[88:91],v4 offset:64512\nC v27,v27,v27\nM v[208:211],a[224:227],v[80:83],v[208:211]\ns_waitcnt lgkmcnt(3)\nds_read_b128 v[76:79],v20 offset:0\nv_and_b32 v26,0xffff0000,v26\n" \
"M v[208:211],a[232:235],v[72:75],v[208:211]\nbuffer_load_dwordx4 v2,s[36:39],s47 offen offset:1024 sc1 lds\nM v[208:211],a[240:243],v[92:95],v[208:211]\nds_read_b64 v[160:161],v114 offset:0\nv_and_b32 v27,0xffff0000,v27\nM v[208:211],a[248:251],v[84:87],v[208:211]\nds_read_b64_tr_b16 v[96:97],v115 offset:16384\nM v[212:215],a[252:255],v[84:87],v[212:215]\n" \
"ds_read_b128 v[84:87],v20 offset:1024\nM v[212:215],a[244:247],v[92:95],v[212:215]\nbuffer_load_dwordx4 v2,s[36:39],s47 offen offset:2048 sc1 lds\nM v[212:215],a[236:239],v[72:75],v[212:215]\nds_read_b128 v[92:95],v20 offset:2048\nC v208,v208,v209\nM v[212:215],a[228:231],v[80:83],v[212:215]\nds_read_b128 v[72:75],v20 offset:3072\n" \
"C v209,v210,v211\nM v[216:219],a[224:227],v[180:183],v[216:219]\ns_waitcnt lgkmcnt(4)\nds_read_b128 v[80:83],v20 offset:8192\nv_cndmask_b32 v120,v120,v208,s[96:97]\nM v[216:219],a[232:235],v[176:179],v[216:219]\nbuffer_store_dwordx4 v[32:35],v4,s[36:39],s41 offen offset:0 sc1\nM v[216:219],a[240:243],v[60:63],v[216:219]\n" \
"ds_read_b64 v[162:163],v114 offset:32\nds_read_b64_tr_b16 v[98:99],v115 offset:17408\nM v[216:219],a[248:251],v[88:91],v[216:219]\nds_read_b64 v[164:165],v114 offset:1024\nC v210,v212,v213\nM v[220:223],a[252:255],v[88:91],v[220:223]\nds_read_b128 v[88:91],v20 offset:9216\nC v211,v214,v215\n" \
"M v[220:223],a[244:247],v[60:63],v[220:223]\nbuffer_load_dwordx4 v2,s[36:39],s47 offen offset:3072 sc1 lds\nM v[220:223],a[236:239],v[176:179],v[220:223]\ns_add_u32 m0,s22,0\nds_read_b128 v[60:63],v20 offset:10240\nC v216,v216,v217\nM v[220:223],a[228:231],v[180:183],v[220:223]\ns_add_u32 s47,s33,0\n" \
"ds_read_b128 v[176:179],v20 offset:11264\nC v217,v218,v219\nM v[224:227],a[0:3],v[76:79],0\ns_waitcnt lgkmcnt(4)\nds_read_b128 v[180:183],v20 offset:4096\nv_lshlrev_b32 v128,16,v160\nM v[224:227],a[8:11],v[84:87],v[224:227]\nbuffer_load_dwordx4 v2,s[36:39],s47 offen offset:0 sc1 lds\n" \
"M v[224:227],a[16:19],v[92:95],v[224:227]\nC v218,v220,v221\nC v219,v222,v223\nM v[224:227],a[24:27],v[72:75],v[224:227]\nds_read_b64_tr_b16 v[100:101],v115 offset:16416\nds_read_b64 v[166:167],v114 offset:1056\nM v[228:231],a[28:31],v[72:75],0\nds_read_b128 v[72:75],v20 offset:5120\n" \
"v_and_b32 v129,0xffff0000,v160\nM v[228:231],a[20:23],v[92:95],v[228:231]\nbuffer_store_dwordx4 v[40:43],v4,s[36:39],s41 offen offset:1024 sc1\nM v[228:231],a[12:15],v[84:87],v[228:231]\nds_read_b128 v[92:95],v20 offset:6144\nds_read_b128 v[84:87],v20 offset:7168\nM v[228:231],a[4:7],v[76:79],v[228:231]\nds_read_b128 v[76:79],v20 offset:12288\n" \
"v_lshlrev_b32 v130,16,v161\nM v[232:235],a[0:3],v[80:83],0\ns_waitcnt lgkmcnt(6)\nds_read_b64_tr_b16 v[102:103],v115 offset:17440\nv_and_b32 v131,0xffff0000,v161\nM v[232:235],a[8:11],v[88:91],v[232:235]\nbuffer_load_dwordx4 v2,s[36:39],s47 offen offset:1024 sc1 lds\nM v[232:235],a[16:19],v[60:63],v[232:235]\n" \
"v_lshlrev_b32 v132,16,v162\nv_and_b32 v133,0xffff0000,v162\nM v[232:235],a[24:27],v[176:179],v[232:235]\nds_read_b64 v[168:169],v114 offset:2048\nds_read_b64_tr_b16 v[104:105],v115 offset:18432\nM v[236:239],a[28:31],v[176:179],0\nds_read_b128 v[176:179],v20 offset:13312\nv_lshlrev_b32 v134,16,v163\n" \
"M v[236:239],a[20:23],v[60:63],v[236:239]\nbuffer_load_dwordx4 v2,s[36:39],s47 offen offset:2048 sc1 lds\nM v[236:239],a[12:15],v[88:91],v[236:239]\nv_and_b32 v135,0xffff0000,v163\nv_lshlrev_b32 v136,16,v164\nM v[236:239],a[4:7],v[80:83],v[236:239]\nds_read_b128 v[60:63],v20 offset:14336\nv_and_b32 v137,0xffff0000,v164\n" \
"M v[224:227],a[32:35],v[180:183],v[224:227]\ns_waitcnt lgkmcnt(4)\nds_read_b128 v[88:91],v20 offset:15360\nv_lshlrev_b32 v138,16,v165\nM v[224:227],a[40:43],v[72:75],v[224:227]\nbuffer_load_dwordx4 v2,s[36:39],s47 offen offset:3072 sc1 lds\nM v[224:227],a[48:51],v[92:95],v[224:227]\ns_add_u32 m0,s22,32768\n" \
"ds_read_b128 v[80:83],v20 offset:32768\nv_and_b32 v139,0xffff0000,v165\nM v[224:227],a[56:59],v[84:87],v[224:227]\ns_add_u32 s47,s33,8192\nds_read_b64 v[170:171],v114 offset:2080\nv_lshlrev_b32 v140,16,v166\nM v[228:231],a[60:63],v[84:87],v[228:231]\nds_read_b128 v[84:87],v20 offset:33792\n" \
"v_and_b32 v141,0xffff0000,v166\nM v[228:231],a[52:55],v[92:95],v[228:231]\nbuffer_load_dwordx4 v2,s[36:39],s47 offen offset:0 sc1 lds\nM v[228:231],a[44:47],v[72:75],v[228:231]\nds_read_b128 v[92:95],v20 offset:34816\nv_lshlrev_b32 v142,16,v167\nM v[228:231],a[36:39],v[180:183],v[228:231]\nds_read_b128 v[72:75],v20 offset:35840\n" \
"v_lshlrev_b32 v160,16,v96\nM v[232:235],a[32:35],v[76:79],v[232:235]\ns_waitcnt lgkmcnt(4)\nds_read_b128 v[180:183],v20 offset:40960\nv_and_b32 v143,0xffff0000,v167\nM v[232:235],a[40:43],v[176:179],v[232:235]\nbuffer_store_dwordx4 v[192:195],v4,s[36:39],s42 offen offset:0 sc1\nM v[232:235],a[48:51],v[60:63],v[232:235]\n" \
"ds_read_b64_tr_b16 v[106:107],v115 offset:19456\nds_read_b64 v[172:173],v114 offset:3072\nM v[232:235],a[56:59],v[88:91],v[232:235]\nds_read_b64_tr_b16 v[108:109],v115 offset:18464\nv_lshlrev_b32 v161,16,v97\nM v[236:239],a[60:63],v[88:91],v[236:239]\nds_read_b128 v[88:91],v20 offset:41984\nv_lshlrev_b32 v162,16,v98\n" \
"M v[236:239],a[52:55],v[60:63],v[236:239]\nbuffer_load_dwordx4 v2,s[36:39],s47 offen offset:1024 sc1 lds\nM v[236:239],a[44:47],v[176:179],v[236:239]\nds_read_b128 v[60:63],v20 offset:43008\nv_lshlrev_b32 v163,16,v99\nM v[236:239],a[36:39],v[76:79],v[236:239]\nds_read_b128 v[176:179],v20 offset:44032\nv_lshlrev_b32 v164,16,v100\n" \
"M v[224:227],a[64:67],v[80:83],v[224:227]\ns_waitcnt lgkmcnt(4)\nds_read_b64 v[174:175],v114 offset:3104\nv_lshlrev_b32 v165,16,v101\nM v[224:227],a[72:75],v[84:87],v[224:227]\nds_read_b64_tr_b16 v[110:111],v115 offset:19488\nv_lshlrev_b32 v166,16,v102\nM v[224:227],a[80:83],v[92:95],v[224:227]\n" \
"buffer_load_dwordx4 v2,s[36:39],s47 offen offset:2048 sc1 lds\nM v[224:227],a[88:91],v[72:75],v[224:227]\nds_read_b128 v[76:79],v20 offset:36864\nv_lshlrev_b32 v167,16,v103\nM v[228:231],a[92:95],v[72:75],v[228:231]\nds_read_b128 v[72:75],v20 offset:37888\nv_lshlrev_b32 v144,16,v168\nM v[228:231],a[84:87],v[92:95],v[228:231]\n" \
"s_waitcnt lgkmcnt(4)\nds_read_b128 v[92:95],v20 offset:38912\nv_and_b32 v96,0xffff0000,v96\nM v[228:231],a[76:79],v[84:87],v[228:231]\nbuffer_load_dwordx4 v2,s[36:39],s47 offen offset:3072 sc1 lds\nM v[228:231],a[68:71],v[80:83],v[228:231]\ns_add_u32 m0,s22,16384\nv_and_b32 v145,0xffff0000,v168\n" \
"v_and_b32 v97,0xffff0000,v97\nM v[232:235],a[64:67],v[180:183],v[232:235]\ns_add_u32 s47,s33,16384\nds_read_b128 v[84:87],v20 offset:39936\nv_and_b32 v98,0xffff0000,v98\nM v[232:235],a[72:75],v[88:91],v[232:235]\nds_read_b128 v[80:83],v20 offset:45056\nv_and_b32 v99,0xffff0000,v99\n" \
"M v[232:235],a[80:83],v[60:63],v[232:235]\nbuffer_store_dwordx4 v[200:203],v4,s[36:39],s42 offen offset:1024 sc1\nM v[232:235],a[88:91],v[176:179],v[232:235]\nv_lshlrev_b32 v146,16,v169\nv_and_b32 v147,0xffff0000,v169\nM v[236:239],a[92:95],v[176:179],v[236:239]\nds_read_b128 v[176:179],v20 offset:46080\nv_lshlrev_b32 v148,16,v170\n" \
"M v[236:239],a[84:87],v[60:63],v[236:239]\ns_waitcnt vmcnt(11) lgkmcnt(6)\nds_read_b128 v[60:63],v20 offset:47104\nv_and_b32 v149,0xffff0000,v170\nM v[236:239],a[76:79],v[88:91],v[236:239]\ns_barrier\nds_read_b128 v[88:91],v20 offset:48128\nM v[236:239],a[68:71],v[180:183],v[236:239]\n" \
"s_waitcnt lgkmcnt(3)\nds_read_b128 v[180:183],v20 offset:16384\nv_lshlrev_b32 v150,16,v171\nM v[224:227],a[96:99],v[76:79],v[224:227]\nv_and_b32 v100,0xffff0000,v100\nv_and_b32 v151,0xffff0000,v171\nM v[224:227],a[104:107],v[72:75],v[224:227]\nv_and_b32 v101,0xffff0000,v101\n" \
"v_and_b32 v102,0xffff0000,v102\nM v[224:227],a[112:115],v[92:95],v[224:227]\nv_and_b32 v103,0xffff0000,v103\nv_lshlrev_b32 v168,16,v104\nM v[224:227],a[120:123],v[84:87],v[224:227]\nv_mul_f32 v129,v129,v96\nv_mul_f32 v131,v131,v97\nM v[228:231],a[124:127],v[84:87],v[228:231]\n" \
"ds_read_b128 v[84:87],v20 offset:17408\nv_mul_f32 v133,v133,v98\nM v[228:231],a[116:119],v[92:95],v[228:231]\nds_read_b128 v[92:95],v20 offset:18432\nv_mul_f32 v135,v135,v99\nM v[228:231],a[108:111],v[72:75],v[228:231]\nds_read_b128 v[72:75],v20 offset:19456\nv_lshlrev_b32 v169,16,v105\n" \
"M v[228:231],a[100:103],v[76:79],v[228:231]\nbuffer_load_dwordx4 v2,s[36:39],s47 offen offset:0 sc1 lds\nM v[232:235],a[96:99],v[80:83],v[232:235]\ns_waitcnt lgkmcnt(3)\nds_read_b128 v[76:79],v20 offset:24576\nv_lshlrev_b32 v170,16,v106\nM v[232:235],a[104:107],v[176:179],v[232:235]\nv_lshlrev_b32 v171,16,v107\n" \
"v_lshlrev_b32 v152,16,v172\nM v[232:235],a[112:115],v[60:63],v[232:235]\nv_and_b32 v153,0xffff0000,v172\nv_lshlrev_b32 v154,16,v173\nM v[232:235],a[120:123],v[88:91],v[232:235]\nv_and_b32 v155,0xffff0000,v173\nv_lshlrev_b32 v156,16,v174\nM v[236:239],a[124:127],v[88:91],v[236:239]\n" \
"buffer_store_dwordx4 v[208:211],v4,s[36:39],s43 offen offset:0 sc1\nM v[236:239],a[116:119],v[60:63],v[236:239]\nds_read_b128 v[88:91],v20 offset:25600\nds_read_b128 v[60:63],v20 offset:26624\nM v[236:239],a[108:111],v[176:179],v[236:239]\nds_read_b128 v[176:179],v20 offset:27648\nv_mul_f32 v137,v137,v100\nM v[236:239],a[100:103],v[80:83],v[236:239]\n" \
"ds_read_b128 v[80:83],v20 offset:20480\nv_and_b32 v157,0xffff0000,v174\nM v[224:227],a[128:131],v[180:183],v[224:227]\ns_waitcnt lgkmcnt(4)\nv_mul_f32 v139,v139,v101\nv_mul_f32 v141,v141,v102\nM v[224:227],a[136:139],v[84:87],v[224:227]\nv_mul_f32 v143,v143,v103\n" \
"v_and_b32 v104,0xffff0000,v104\nM v[224:227],a[144:147],v[92:95],v[224:227]\nv_and_b32 v105,0xffff0000,v105\nv_and_b32 v106,0xffff0000,v106\nM v[224:227],a[152:155],v[72:75],v[224:227]\nv_and_b32 v107,0xffff0000,v107\nv_lshlrev_b32 v172,16,v108\nM v[228:231],a[156:159],v[72:75],v[228:231]\n" \
"ds_read_b128 v[72:75],v20 offset:21504\nv_lshlrev_b32 v158,16,v175\nM v[228:231],a[148:151],v[92:95],v[228:231]\nds_read_b128 v[92:95],v20 offset:22528\nv_and_b32 v159,0xffff0000,v175\nM v[228:231],a[140:143],v[84:87],v[228:231]\nds_read_b128 v[84:87],v20 offset:23552\nv_lshlrev_b32 v173,16,v109\n" \
"M v[228:231],a[132:135],v[180:183],v[228:231]\nds_read_b128 v[180:183],v20 offset:28672\nv_lshlrev_b32 v174,16,v110\nM v[232:235],a[128:131],v[76:79],v[232:235]\ns_waitcnt lgkmcnt(4)\nv_mul_f32 v145,v145,v104\nv_mul_f32 v147,v147,v105\nM v[232:235],a[136:139],v[88:91],v[232:235]\n" \
"buffer_load_dwordx4 v2,s[36:39],s47 offen offset:1024 sc1 lds\nM v[232:235],a[144:147],v[60:63],v[232:235]\nv_mul_f32 v149,v149,v106\nv_mul_f32 v151,v151,v107\nM v[232:235],a[152:155],v[176:179],v[232:235]\nv_and_b32 v108,0xffff0000,v108\nv_lshlrev_b32 v175,16,v111\nM v[236:239],a[156:159],v[176:179],v[236:239]\n" \
"ds_read_b128 v[176:179],v20 offset:29696\nv_and_b32 v109,0xffff0000,v109\nM v[236:239],a[148:151],v[60:63],v[236:239]\nbuffer_load_dwordx4 v2,s[36:39],s47 offen offset:2048 sc1 lds\nM v[236:239],a[140:143],v[88:91],v[236:239]\nds_read_b128 v[60:63],v20 offset:30720\nv_and_b32 v110,0xffff0000,v110\nM v[236:239],a[132:135],v[76:79],v[236:239]\n" \
"ds_read_b128 v[88:91],v20 offset:31744\nv_and_b32 v111,0xffff0000,v111\nM v[224:227],a[160:163],v[80:83],v[224:227]\ns_waitcnt lgkmcnt(3)\nds_read_b128 v[76:79],v20 offset:49152\nv_mul_f32 v153,v153,v108\nM v[224:227],a[168:171],v[72:75],v[224:227]\nbuffer_store_dwordx4 v[216:219],v4,s[36:39],s43 offen offset:1024 sc1\n" \
"M v[224:227],a[176:179],v[92:95],v[224:227]\nv_mul_f32 v155,v155,v109\nv_mul_f32 v157,v157,v110\nM v[224:227],a[184:187],v[84:87],v[224:227]\nv_mul_f32 v159,v159,v111\nv_cndmask_b32 v121,v121,v209,s[96:97]\nM v[228:231],a[188:191],v[84:87],v[228:231]\nds_read_b128 v[84:87],v20 offset:50176\n" \
"v_cndmask_b32 v122,v122,v210,s[96:97]\nM v[228:231],a[180:183],v[92:95],v[228:231]\nds_read_b128 v[92:95],v20 offset:51200\nv_cndmask_b32 v123,v123,v211,s[96:97]\nM v[228:231],a[172:175],v[72:75],v[228:231]\nds_read_b128 v[72:75],v20 offset:52224\nv_cndmask_b32 v124,v124,v216,s[96:97]\nM v[228:231],a[164:167],v[80:83],v[228:231]\n" \
"ds_read_b128 v[80:83],v20 offset:57344\nv_cndmask_b32 v125,v125,v217,s[96:97]\nM v[232:235],a[160:163],v[180:183],v[232:235]\ns_waitcnt lgkmcnt(4)\nv_cndmask_b32 v126,v126,v218,s[96:97]\nv_cndmask_b32 v127,v127,v219,s[96:97]\nM v[232:235],a[168:171],v[176:179],v[232:235]\nv_mul_f32 v128,v128,v160\n" \
"v_mul_f32 v130,v130,v161\nM v[232:235],a[176:179],v[60:63],v[232:235]\nC v129,v128,v129\nv_mul_f32 v132,v132,v162\nM v[232:235],a[184:187],v[88:91],v[232:235]\nC v131,v130,v131\nv_mul_f32 v134,v134,v163\nM v[236:239],a[188:191],v[88:91],v[236:239]\n" \
"buffer_load_dwordx4 v2,s[36:39],s47 offen offset:3072 sc1 lds\nM v[236:239],a[180:183],v[60:63],v[236:239]\ns_add_u32 m0,s22,49152\nds_read_b128 v[88:91],v20 offset:58368\nv_lshlrev_b32 v128,16,v129\nM v[236:239],a[172:175],v[176:179],v[236:239]\ns_add_u32 s47,s33,24576\nds_read_b128 v[60:63],v20 offset:59392\n" \
"C v133,v132,v133\nM v[236:239],a[164:167],v[180:183],v[236:239]\nds_read_b128 v[176:179],v20 offset:60416\nC v135,v134,v135\nM v[224:227],a[192:195],v[76:79],v[224:227]\ns_waitcnt lgkmcnt(3)\nds_read_b128 v[180:183],v20 offset:53248\nv_lshlrev_b32 v130,16,v131\n" \
"M v[224:227],a[200:203],v[84:87],v[224:227]\nbuffer_load_dwordx4 v2,s[36:39],s47 offen offset:0 sc1 lds\nM v[224:227],a[208:211],v[92:95],v[224:227]\nv_lshlrev_b32 v132,16,v133\nv_and_b32 v129,0xffff0000,v129\nM v[224:227],a[216:219],v[72:75],v[224:227]\nv_lshlrev_b32 v134,16,v135\nv_and_b32 v133,0xffff0000,v133\n" \
"M v[228:231],a[220:223],v[72:75],v[228:231]\nds_read_b128 v[72:75],v20 offset:54272\nv_and_b32 v131,0xffff0000,v131\nM v[228:231],a[212:215],v[92:95],v[228:231]\nbuffer_load_dwordx4 v2,s[36:39],s47 offen offset:1024 sc1 lds\nM v[228:231],a[204:207],v[84:87],v[228:231]\nds_read_b128 v[92:95],v20 offset:55296\nv_and_b32 v135,0xffff0000,v135\n" \
"M v[228:231],a[196:199],v[76:79],v[228:231]\nds_read_b128 v[84:87],v20 offset:56320\nv_mul_f32 v128,v16,v128\nM v[232:235],a[192:195],v[80:83],v[232:235]\ns_waitcnt lgkmcnt(3)\nds_read_b128 v[76:79],v20 offset:61440\nv_mul_f32 v129,v16,v129\nM v[232:235],a[200:203],v[88:91],v[232:235]\n" \
"v_mul_f32 v132,v16,v132\nv_mul_f32 v133,v16,v133\nM v[232:235],a[208:211],v[60:63],v[232:235]\nbuffer_load_dwordx4 v2,s[36:39],s47 offen offset:2048 sc1 lds\nM v[232:235],a[216:219],v[176:179],v[232:235]\nv_mul_f32 v130,v16,v130\nv_mul_f32 v131,v16,v131\nM v[236:239],a[220:223],v[176:179],v[236:239]\n" \
"s_waitcnt lgkmcnt(2)\nds_read_b128 v[176:179],v20 offset:62464\nv_mul_f32 v134,v16,v134\nM v[236:239],a[212:215],v[60:63],v[236:239]\ns_waitcnt vmcnt(10) lgkmcnt(14)\nds_read_b128 v[60:63],v20 offset:63488\nv_mul_f32 v135,v16,v135\nM v[236:239],a[204:207],v[88:91],v[236:239]\n" \
"s_barrier\nds_read_b128 v[88:91],v20 offset:64512\nM v[236:239],a[196:199],v[80:83],v[236:239]\nbuffer_load_dwordx4 v2,s[36:39],s47 offen offset:3072 sc1 lds\nM v[224:227],a[224:227],v[180:183],v[224:227]\ns_add_u32 m0,s22,131072\nds_read_b128 v[80:83],v4 offset:0\nv_mul_f32 v136,v136,v164\n" \
"M v[224:227],a[232:235],v[72:75],v[224:227]\ns_add_u32 s47,s34,0\nv_mul_f32 v138,v138,v165\nv_mul_f32 v140,v140,v166\nM v[224:227],a[240:243],v[92:95],v[224:227]\ns_waitcnt lgkmcnt(3)\nC v129,v128,v129\nv_mul_f32 v142,v142,v167\n" \
"M v[224:227],a[248:251],v[84:87],v[224:227]\nbuffer_load_dwordx4 v2,s[36:39],s47 offen offset:0 sc1 lds\nM v[228:231],a[252:255],v[84:87],v[228:231]\nds_read_b128 v[84:87],v4 offset:1024\nC v133,v132,v133\nM v[228:231],a[244:247],v[92:95],v[228:231]\nds_read_b128 v[92:95],v4 offset:2048\nC v224,v224,v225\n" \
"M v[228:231],a[236:239],v[72:75],v[228:231]\nds_read_b128 v[72:75],v4 offset:3072\nC v225,v226,v227\nM v[228:231],a[228:231],v[180:183],v[228:231]\nbuffer_load_dwordx4 v2,s[36:39],s47 offen offset:1024 sc1 lds\nM v[232:235],a[224:227],v[76:79],v[232:235]\nds_read_b128 v[180:183],v4 offset:8192\nC v131,v130,v131\n" \
"M v[232:235],a[232:235],v[176:179],v[232:235]\ns_waitcnt lgkmcnt(4)\nC v226,v228,v229\nC v227,v230,v231\nM v[232:235],a[240:243],v[60:63],v[232:235]\nC v135,v134,v135\nC v137,v136,v137\nM v[232:235],a[248:251],v[88:91],v[232:235]\n" \
"C v139,v138,v139\nC v141,v140,v141\nM v[236:239],a[252:255],v[88:91],v[236:239]\nbuffer_load_dwordx4 v2,s[36:39],s47 offen offset:2048 sc1 lds\nM v[236:239],a[244:247],v[60:63],v[236:239]\nds_read_b128 v[88:91],v4 offset:9216\nC v232,v232,v233\nM v[236:239],a[236:239],v[176:179],v[236:239]\n" \
"ds_read_b128 v[60:63],v4 offset:10240\nC v233,v234,v235\nM v[236:239],a[228:231],v[76:79],v[236:239]\ns_waitcnt lgkmcnt(2)\nds_read_b128 v[176:179],v4 offset:11264\nv_lshlrev_b32 v128,16,v129\nM v[240:243],a[0:3],v[80:83],0\nbuffer_load_dwordx4 v2,s[36:39],s47 offen offset:3072 sc1 lds\n" \
"M v[240:243],a[8:11],v[84:87],v[240:243]\ns_add_u32 m0,s22,147456\nds_read_b128 v[76:79],v4 offset:4096\nC v234,v236,v237\nM v[240:243],a[16:19],v[92:95],v[240:243]\ns_add_u32 s47,s34,8192\nC v235,v238,v239\nC v143,v142,v143\n" \
"M v[240:243],a[24:27],v[72:75],v[240:243]\nv_lshlrev_b32 v132,16,v133\nv_lshlrev_b32 v130,16,v131\nM v[244:247],a[28:31],v[72:75],0\nds_read_b128 v[72:75],v4 offset:5120\nv_lshlrev_b32 v134,16,v135\nM v[244:247],a[20:23],v[92:95],v[244:247]\nbuffer_load_dwordx4 v2,s[36:39],s47 offen offset:0 sc1 lds\n" \
"M v[244:247],a[12:15],v[84:87],v[244:247]\nds_read_b128 v[92:95],v4 offset:6144\nv_lshlrev_b32 v136,16,v137\nM v[244:247],a[4:7],v[80:83],v[244:247]\nds_read_b128 v[84:87],v4 offset:7168\nv_lshlrev_b32 v140,16,v141\nM v[248:251],a[0:3],v[180:183],0\ns_waitcnt lgkmcnt(3)\n" \
"ds_read_b128 v[80:83],v4 offset:12288\nv_lshlrev_b32 v138,16,v139\nM v[248:251],a[8:11],v[88:91],v[248:251]\nbuffer_store_dwordx4 v[224:227],v4,s[36:39],s44 offen offset:0 sc1\nM v[248:251],a[16:19],v[60:63],v[248:251]\nv_lshlrev_b32 v142,16,v143\nv_and_b32 v129,0xffff0000,v129\nM v[248:251],a[24:27],v[176:179],v[248:251]\n" \
"v_and_b32 v133,0xffff0000,v133\nv_and_b32 v131,0xffff0000,v131\nM v[252:255],a[28:31],v[176:179],0\nds_read_b128 v[176:179],v4 offset:13312\nv_and_b32 v135,0xffff0000,v135\nM v[252:255],a[20:23],v[60:63],v[252:255]\nds_read_b128 v[60:63],v4 offset:14336\nv_and_b32 v137,0xffff0000,v137\n" \
"M v[252:255],a[12:15],v[88:91],v[252:255]\nds_read_b128 v[88:91],v4 offset:15360\nv_and_b32 v141,0xffff0000,v141\nM v[252:255],a[4:7],v[180:183],v[252:255]\nds_read_b128 v[180:183],v4 offset:32768\nv_and_b32 v139,0xffff0000,v139\nM v[240:243],a[32:35],v[76:79],v[240:243]\ns_waitcnt lgkmcnt(4)\n" \
"v_and_b32 v143,0xffff0000,v143\nv_mul_f32 v136,v17,v136\nM v[240:243],a[40:43],v[72:75],v[240:243]\nv_mul_f32 v137,v17,v137\nv_mul_f32 v140,v17,v140\nM v[240:243],a[48:51],v[92:95],v[240:243]\nv_mul_f32 v141,v17,v141\nv_mul_f32 v138,v17,v138\n" \
"M v[240:243],a[56:59],v[84:87],v[240:243]\nv_mul_f32 v139,v17,v139\nv_mul_f32 v142,v17,v142\nM v[244:247],a[60:63],v[84:87],v[244:247]\nbuffer_store_dwordx4 v[232:235],v4,s[36:39],s44 offen offset:1024 sc1\nM v[244:247],a[52:55],v[92:95],v[244:247]\nds_read_b128 v[84:87],v4 offset:33792\nds_read_b128 v[92:95],v4 offset:34816\n" \
"M v[244:247],a[44:47],v[72:75],v[244:247]\nds_read_b128 v[72:75],v4 offset:35840\nv_mul_f32 v143,v17,v143\nM v[244:247],a[36:39],v[76:79],v[244:247]\nLG24_%=:\ns_cbranch_execz LHR3_%=\nds_read_b128 v[76:79],v4 offset:40960\nv_mul_f32 v144,v144,v168\n" \
"M v[248:251],a[32:35],v[80:83],v[248:251]\ns_waitcnt lgkmcnt(4)\nv_mul_f32 v148,v148,v170\nv_mul_f32 v146,v146,v169\nM v[248:251],a[40:43],v[176:179],v[248:251]\nv_mul_f32 v150,v150,v171\nC v137,v136,v137\nM v[248:251],a[48:51],v[60:63],v[248:251]\n" \
"C v141,v140,v141\nC v139,v138,v139\nM v[248:251],a[56:59],v[88:91],v[248:251]\nC v143,v142,v143\nC v145,v144,v145\nM v[252:255],a[60:63],v[88:91],v[252:255]\nds_read_b128 v[88:91],v4 offset:41984\nC v149,v148,v149\n" \
"M v[252:255],a[52:55],v[60:63],v[252:255]\nds_read_b128 v[60:63],v4 offset:43008\nC v147,v146,v147\nM v[252:255],a[44:47],v[176:179],v[252:255]\nds_read_b128 v[176:179],v4 offset:44032\nC v151,v150,v151\nM v[252:255],a[36:39],v[80:83],v[252:255]\nds_read_b128 v[80:83],v4 offset:36864\n" \
"v_lshlrev_b32 v136,16,v137\nM v[240:243],a[64:67],v[180:183],v[240:243]\ns_waitcnt lgkmcnt(4)\nv_lshlrev_b32 v140,16,v141\nv_lshlrev_b32 v138,16,v139\nM v[240:243],a[72:75],v[84:87],v[240:243]\nbuffer_load_dwordx4 v2,s[36:39],s47 offen offset:1024 sc1 lds\nM v[240:243],a[80:83],v[92:95],v[240:243]\n" \
"v_lshlrev_b32 v142,16,v143\nv_lshlrev_b32 v144,16,v145\nM v[240:243],a[88:91],v[72:75],v[240:243]\nv_lshlrev_b32 v148,16,v149\nv_lshlrev_b32 v146,16,v147\nM v[244:247],a[92:95],v[72:75],v[244:247]\nds_read_b128 v[72:75],v4 offset:37888\nv_lshlrev_b32 v150,16,v151\n" \
"M v[244:247],a[84:87],v[92:95],v[244:247]\nds_read_b128 v[92:95],v4 offset:38912\nv_and_b32 v137,0xffff0000,v137\nM v[244:247],a[76:79],v[84:87],v[244:247]\nbuffer_load_dwordx4 v2,s[36:39],s47 offen offset:2048 sc1 lds\nM v[244:247],a[68:71],v[180:183],v[244:247]\nds_read_b128 v[84:87],v4 offset:39936\nv_and_b32 v141,0xffff0000,v141\n" \
"M v[248:251],a[64:67],v[76:79],v[248:251]\ns_waitcnt lgkmcnt(3)\nds_read_b128 v[180:183],v4 offset:45056\nv_and_b32 v139,0xffff0000,v139\nM v[248:251],a[72:75],v[88:91],v[248:251]\nv_and_b32 v143,0xffff0000,v143\nv_and_b32 v145,0xffff0000,v145\nM v[248:251],a[80:83],v[60:63],v[248:251]\n" \
"buffer_load_dwordx4 v2,s[36:39],s47 offen offset:3072 sc1 lds\nM v[248:251],a[88:91],v[176:179],v[248:251]\ns_add_u32 m0,s22,65536\nv_and_b32 v149,0xffff0000,v149\nv_and_b32 v147,0xffff0000,v147\nM v[252:255],a[92:95],v[176:179],v[252:255]\ns_add_u32 s47,s34,16384\nds_read_b128 v[176:179],v4 offset:46080\n" \
"v_and_b32 v151,0xffff0000,v151\nM v[252:255],a[84:87],v[60:63],v[252:255]\ns_waitcnt vmcnt(10) lgkmcnt(14)\nds_read_b128 v[60:63],v4 offset:47104\nv_mul_f32 v144,v18,v144\nM v[252:255],a[76:79],v[88:91],v[252:255]\ns_barrier\nds_read_b128 v[88:91],v4 offset:48128\n" \
"M v[252:255],a[68:71],v[76:79],v[252:255]\nbuffer_load_dwordx4 v2,s[36:39],s47 offen offset:0 sc1 lds\nM v[240:243],a[96:99],v[80:83],v[240:243]\ns_waitcnt lgkmcnt(3)\nds_read_b128 v[76:79],v4 offset:16384\nv_mul_f32 v145,v18,v145\nM v[240:243],a[104:107],v[72:75],v[240:243]\nv_mul_f32 v148,v18,v148\n" \
"v_mul_f32 v149,v18,v149\nM v[240:243],a[112:115],v[92:95],v[240:243]\nv_mul_f32 v146,v18,v146\nv_mul_f32 v147,v18,v147\nM v[240:243],a[120:123],v[84:87],v[240:243]\nbuffer_load_dwordx4 v2,s[36:39],s47 offen offset:1024 sc1 lds\nM v[244:247],a[124:127],v[84:87],v[244:247]\nds_read_b128 v[84:87],v4 offset:17408\n" \
"v_mul_f32 v150,v18,v150\nM v[244:247],a[116:119],v[92:95],v[244:247]\ns_waitcnt vmcnt(17)\nds_read_b128 v[92:95],v4 offset:18432\nv_mul_f32 v151,v18,v151\nM v[244:247],a[108:111],v[72:75],v[244:247]\nds_read_b128 v[72:75],v4 offset:19456\nv_mul_f32 v152,v152,v172\n" \
"M v[244:247],a[100:103],v[80:83],v[244:247]\nbuffer_store_dwordx2 v[28:29],v191,s[36:39],s95 offen offset:0 sc1\nM v[248:251],a[96:99],v[180:183],v[248:251]\ns_waitcnt lgkmcnt(3)\nds_read_b128 v[80:83],v4 offset:24576\nv_mul_f32 v156,v156,v174\nM v[248:251],a[104:107],v[176:179],v[248:251]\nbuffer_load_dwordx4 v2,s[36:39],s47 offen offset:2048 sc1 lds\n" \
"M v[248:251],a[112:115],v[60:63],v[248:251]\nv_mul_f32 v154,v154,v173\nv_mul_f32 v158,v158,v175\nM v[248:251],a[120:123],v[88:91],v[248:251]\nC v145,v144,v145\nC v149,v148,v149\nM v[252:255],a[124:127],v[88:91],v[252:255]\nds_read_b128 v[88:91],v4 offset:25600\n" \
"C v147,v146,v147\nM v[252:255],a[116:119],v[60:63],v[252:255]\ns_waitcnt lgkmcnt(2)\nds_read_b128 v[60:63],v4 offset:26624\nC v151,v150,v151\nM v[252:255],a[108:111],v[176:179],v[252:255]\nbuffer_load_dwordx4 v2,s[36:39],s47 offen offset:3072 sc1 lds\nM v[252:255],a[100:103],v[180:183],v[252:255]\n" \
"s_add_u32 m0,s22,98304\nds_read_b128 v[176:179],v4 offset:27648\nC v153,v152,v153\nM v[240:243],a[128:131],v[76:79],v[240:243]\ns_add_u32 s47,s34,24576\nds_read_b128 v[180:183],v4 offset:20480\nC v157,v156,v157\nM v[240:243],a[136:139],v[84:87],v[240:243]\n" \
"C v155,v154,v155\nC v159,v158,v159\nM v[240:243],a[144:147],v[92:95],v[240:243]\nbuffer_load_dwordx4 v2,s[36:39],s47 offen offset:0 sc1 lds\nM v[240:243],a[152:155],v[72:75],v[240:243]\nv_lshlrev_b32 v144,16,v145\nv_lshlrev_b32 v148,16,v149\nM v[244:247],a[156:159],v[72:75],v[244:247]\n" \
"ds_read_b128 v[72:75],v4 offset:21504\nv_lshlrev_b32 v146,16,v147\nM v[244:247],a[148:151],v[92:95],v[244:247]\nds_read_b128 v[92:95],v4 offset:22528\nv_lshlrev_b32 v150,16,v151\nM v[244:247],a[140:143],v[84:87],v[244:247]\ns_waitcnt lgkmcnt(2)\nds_read_b128 v[84:87],v4 offset:23552\n" \
"v_lshlrev_b32 v152,16,v153\nM v[244:247],a[132:135],v[76:79],v[244:247]\nbuffer_load_dwordx4 v2,s[36:39],s47 offen offset:1024 sc1 lds\nM v[248:251],a[128:131],v[80:83],v[248:251]\nds_read_b128 v[76:79],v4 offset:28672\nv_lshlrev_b32 v156,16,v157\nM v[248:251],a[136:139],v[88:91],v[248:251]\nv_lshlrev_b32 v154,16,v155\n" \
"v_lshlrev_b32 v158,16,v159\nM v[248:251],a[144:147],v[60:63],v[248:251]\nv_and_b32 v145,0xffff0000,v145\nv_and_b32 v149,0xffff0000,v149\nM v[248:251],a[152:155],v[176:179],v[248:251]\nbuffer_load_dwordx4 v2,s[36:39],s47 offen offset:2048 sc1 lds\nM v[252:255],a[156:159],v[176:179],v[252:255]\nds_read_b128 v[176:179],v4 offset:29696\n" \
"v_and_b32 v147,0xffff0000,v147\nM v[252:255],a[148:151],v[60:63],v[252:255]\nds_read_b128 v[60:63],v4 offset:30720\nv_and_b32 v151,0xffff0000,v151\nM v[252:255],a[140:143],v[88:91],v[252:255]\nds_read_b128 v[88:91],v4 offset:31744\nv_and_b32 v153,0xffff0000,v153\nM v[252:255],a[132:135],v[80:83],v[252:255]\n" \
"s_waitcnt lgkmcnt(3)\nds_read_b128 v[80:83],v4 offset:49152\nv_and_b32 v157,0xffff0000,v157\nM v[240:243],a[160:163],v[180:183],v[240:243]\nbuffer_load_dwordx4 v2,s[36:39],s47 offen offset:3072 sc1 lds\nM v[240:243],a[168:171],v[72:75],v[240:243]\ns_add_u32 m0,s22,81920\nv_and_b32 v155,0xffff0000,v155\n" \
"v_and_b32 v159,0xffff0000,v159\nM v[240:243],a[176:179],v[92:95],v[240:243]\ns_add_u32 s47,s35,0\nv_mul_f32 v152,v19,v152\nv_mul_f32 v153,v19,v153\nM v[240:243],a[184:187],v[84:87],v[240:243]\nv_mul_f32 v156,v19,v156\nv_mul_f32 v157,v19,v157\n" \
"M v[244:247],a[188:191],v[84:87],v[244:247]\nbuffer_load_dwordx4 v2,s[36:39],s47 offen offset:0 sc1 lds\nM v[244:247],a[180:183],v[92:95],v[244:247]\nds_read_b128 v[84:87],v4 offset:50176\nv_mul_f32 v154,v19,v154\nM v[244:247],a[172:175],v[72:75],v[244:247]\nds_read_b128 v[92:95],v4 offset:51200\nv_mul_f32 v155,v19,v155\n" \
"M v[244:247],a[164:167],v[180:183],v[244:247]\nds_read_b128 v[72:75],v4 offset:52224\nv_mul_f32 v158,v19,v158\nM v[248:251],a[160:163],v[76:79],v[248:251]\ns_waitcnt lgkmcnt(3)\nds_read_b128 v[180:183],v4 offset:57344\nv_mul_f32 v159,v19,v159\nM v[248:251],a[168:171],v[176:179],v[248:251]\n" \
"buffer_load_dwordx4 v2,s[36:39],s47 offen offset:1024 sc1 lds\nM v[248:251],a[176:179],v[60:63],v[248:251]\nC v153,v152,v153\nC v157,v156,v157\nM v[248:251],a[184:187],v[88:91],v[248:251]\nC v155,v154,v155\nC v159,v158,v159\nM v[252:255],a[188:191],v[88:91],v[252:255]\n" \
"ds_read_b128 v[88:91],v4 offset:58368\nv_lshlrev_b32 v152,16,v153\nM v[252:255],a[180:183],v[60:63],v[252:255]\nbuffer_load_dwordx4 v2,s[36:39],s47 offen offset:2048 sc1 lds\nM v[252:255],a[172:175],v[176:179],v[252:255]\nds_read_b128 v[60:63],v4 offset:59392\nv_lshlrev_b32 v156,16,v157\nM v[252:255],a[164:167],v[76:79],v[252:255]\n" \
"ds_read_b128 v[176:179],v4 offset:60416\nv_lshlrev_b32 v154,16,v155\nM v[240:243],a[192:195],v[80:83],v[240:243]\ns_waitcnt lgkmcnt(3)\nds_read_b128 v[76:79],v4 offset:53248\nv_lshlrev_b32 v158,16,v159\nM v[240:243],a[200:203],v[84:87],v[240:243]\nv_and_b32 v153,0xffff0000,v153\n" \
"v_and_b32 v157,0xffff0000,v157\nM v[240:243],a[208:211],v[92:95],v[240:243]\nbuffer_load_dwordx4 v2,s[36:39],s47 offen offset:3072 sc1 lds\nM v[240:243],a[216:219],v[72:75],v[240:243]\ns_add_u32 m0,s22,114688\nv_and_b32 v155,0xffff0000,v155\nv_and_b32 v159,0xffff0000,v159\nM v[244:247],a[220:223],v[72:75],v[244:247]\n" \
"s_add_u32 s47,s35,8192\nds_read_b128 v[72:75],v4 offset:54272\nM v[244:247],a[212:215],v[92:95],v[244:247]\nds_read_b128 v[92:95],v4 offset:55296\nM v[244:247],a[204:207],v[84:87],v[244:247]\nbuffer_load_dwordx4 v2,s[36:39],s47 offen offset:0 sc1 lds\nM v[244:247],a[196:199],v[80:83],v[244:247]\nds_read_b128 v[84:87],v4 offset:56320\n" \
"M v[248:251],a[192:195],v[180:183],v[248:251]\ns_waitcnt lgkmcnt(3)\nds_read_b128 v[80:83],v4 offset:61440\nM v[248:251],a[200:203],v[88:91],v[248:251]\nM v[248:251],a[208:211],v[60:63],v[248:251]\nM v[248:251],a[216:219],v[176:179],v[248:251]\nbuffer_load_dwordx4 v2,s[36:39],s47 offen offset:1024 sc1 lds\nM v[252:255],a[220:223],v[176:179],v[252:255]\n" \
"ds_read_b128 v[176:179],v4 offset:62464\nM v[252:255],a[212:215],v[60:63],v[252:255]\ns_waitcnt vmcnt(15) lgkmcnt(14)\nds_read_b128 v[60:63],v4 offset:63488\nM v[252:255],a[204:207],v[88:91],v[252:255]\ns_barrier\nds_read_b128 v[88:91],v4 offset:64512\nM v[252:255],a[196:199],v[180:183],v[252:255]\n" \
"buffer_load_dwordx4 v2,s[36:39],s47 offen offset:2048 sc1 lds\nM v[240:243],a[224:227],v[76:79],v[240:243]\ns_waitcnt lgkmcnt(3)\nds_read_b128 v[180:183],v23 offset:0\nM v[240:243],a[232:235],v[72:75],v[240:243]\nM v[240:243],a[240:243],v[92:95],v[240:243]\nM v[240:243],a[248:251],v[84:87],v[240:243]\nbuffer_load_dwordx2 v[10:11],v190,s[36:39],0 offen sc1\n" \
"M v[244:247],a[252:255],v[84:87],v[244:247]\nds_read_b128 v[84:87],v23 offset:1024\nM v[244:247],a[244:247],v[92:95],v[244:247]\nbuffer_load_dwordx4 v2,s[36:39],s47 offen offset:3072 sc1 lds\nM v[244:247],a[236:239],v[72:75],v[244:247]\ns_add_u32 m0,s22,0\nds_read_b128 v[92:95],v23 offset:2048\nC v240,v240,v241\n" \
"M v[244:247],a[228:231],v[76:79],v[244:247]\ns_add_u32 s47,s35,16384\nds_read_b128 v[72:75],v23 offset:3072\nC v241,v242,v243\nM v[248:251],a[224:227],v[80:83],v[248:251]\ns_waitcnt lgkmcnt(3)\nds_read_b128 v[76:79],v23 offset:8192\nM v[248:251],a[232:235],v[176:179],v[248:251]\n" \
"C v242,v244,v245\nC v243,v246,v247\nM v[248:251],a[240:243],v[60:63],v[248:251]\nbuffer_store_dwordx4 v[240:243],v4,s[36:39],s45 offen offset:0 sc1\nM v[248:251],a[248:251],v[88:91],v[248:251]\nM v[252:255],a[252:255],v[88:91],v[252:255]\nds_read_b128 v[88:91],v23 offset:9216\nM v[252:255],a[244:247],v[60:63],v[252:255]\n" \
"ds_read_b128 v[60:63],v23 offset:10240\nC v248,v248,v249\nM v[252:255],a[236:239],v[176:179],v[252:255]\nds_read_b128 v[176:179],v23 offset:11264\nC v249,v250,v251\nM v[252:255],a[228:231],v[80:83],v[252:255]\nds_read_b128 v[80:83],v23 offset:4096\nM v[192:195],a[0:3],v[180:183],0\n" \
"s_waitcnt lgkmcnt(4)\nM v[192:195],a[8:11],v[84:87],v[192:195]\nC v250,v252,v253\nC v251,v254,v255\nM v[192:195],a[16:19],v[92:95],v[192:195]\nM v[192:195],a[24:27],v[72:75],v[192:195]\nM v[196:199],a[28:31],v[72:75],0\nds_read_b128 v[72:75],v23 offset:5120\n" \
"M v[196:199],a[20:23],v[92:95],v[196:199]\nds_read_b128 v[92:95],v23 offset:6144\nM v[196:199],a[12:15],v[84:87],v[196:199]\nds_read_b128 v[84:87],v23 offset:7168\nM v[196:199],a[4:7],v[180:183],v[196:199]\nbuffer_store_dwordx4 v[248:251],v4,s[36:39],s45 offen offset:1024 sc1\nM v[200:203],a[0:3],v[76:79],0\ns_waitcnt lgkmcnt(3)\n" \
"M v[200:203],a[8:11],v[88:91],v[200:203]\nM v[200:203],a[16:19],v[60:63],v[200:203]\nM v[200:203],a[24:27],v[176:179],v[200:203]\nM v[204:207],a[28:31],v[176:179],0\nM v[204:207],a[20:23],v[60:63],v[204:207]\nds_read_b128 v[60:63],v23 offset:12288\nM v[204:207],a[12:15],v[88:91],v[204:207]\nds_read_b128 v[88:91],v23 offset:13312\n" \
"M v[204:207],a[4:7],v[76:79],v[204:207]\nds_read_b128 v[76:79],v23 offset:14336\nM v[192:195],a[32:35],v[80:83],v[192:195]\ns_waitcnt lgkmcnt(3)\nM v[192:195],a[40:43],v[72:75],v[192:195]\nM v[192:195],a[48:51],v[92:95],v[192:195]\nM v[192:195],a[56:59],v[84:87],v[192:195]\nM v[196:199],a[60:63],v[84:87],v[196:199]\n" \
"ds_read_b128 v[84:87],v23 offset:15360\nM v[196:199],a[52:55],v[92:95],v[196:199]\nbuffer_load_dwordx4 v2,s[36:39],s47 offen offset:0 sc1 lds\nM v[196:199],a[44:47],v[72:75],v[196:199]\nds_read_b128 v[92:95],v23 offset:16384\nM v[196:199],a[36:39],v[80:83],v[196:199]\ns_waitcnt lgkmcnt(2)\nds_read_b128 v[72:75],v23 offset:17408\n" \
"M v[200:203],a[32:35],v[60:63],v[200:203]\nds_read_b128 v[80:83],v23 offset:18432\nM v[200:203],a[40:43],v[88:91],v[200:203]\nbuffer_load_dwordx4 v2,s[36:39],s47 offen offset:1024 sc1 lds\nM v[200:203],a[48:51],v[76:79],v[200:203]\ns_waitcnt lgkmcnt(3)\nM v[200:203],a[56:59],v[84:87],v[200:203]\nM v[204:207],a[60:63],v[84:87],v[204:207]\n" \
"ds_read_b128 v[84:87],v23 offset:19456\nM v[204:207],a[52:55],v[76:79],v[204:207]\nbuffer_load_dwordx4 v2,s[36:39],s47 offen offset:2048 sc1 lds\nM v[204:207],a[44:47],v[88:91],v[204:207]\nds_read_b128 v[76:79],v23 offset:24576\nM v[204:207],a[36:39],v[60:63],v[204:207]\ns_waitcnt lgkmcnt(2)\nds_read_b128 v[88:91],v23 offset:25600\n" \
"M v[192:195],a[64:67],v[92:95],v[192:195]\ns_waitcnt lgkmcnt(2)\nds_read_b128 v[60:63],v23 offset:26624\nM v[192:195],a[72:75],v[72:75],v[192:195]\nbuffer_load_dwordx4 v2,s[36:39],s47 offen offset:3072 sc1 lds\nM v[192:195],a[80:83],v[80:83],v[192:195]\ns_add_u32 m0,s22,32768\nM v[192:195],a[88:91],v[84:87],v[192:195]\n" \
"s_add_u32 s47,s35,24576\nM v[196:199],a[92:95],v[84:87],v[196:199]\ns_waitcnt lgkmcnt(2)\nds_read_b128 v[84:87],v23 offset:27648\nM v[196:199],a[84:87],v[80:83],v[196:199]\ns_waitcnt vmcnt(7)\nv_cmp_gt_u64 s[98:99],s[14:15],v[10:11]\nv_cmp_le_u64 s[18:19],s[16:17],v[10:11]\n" \
"M v[196:199],a[76:79],v[72:75],v[196:199]\ns_or_b64 s[98:99],s[98:99],s[18:19]\ns_and_b32 s98,s98,0x7\ns_cbranch_scc1 LPP_%=\nLPPR_%=:\nds_read_b128 v[80:83],v23 offset:20480\nds_read_b128 v[72:75],v23 offset:21504\nM v[196:199],a[68:71],v[92:95],v[196:199]\n" \
"buffer_load_dwordx4 v[96:99],v4,s[36:39],s63 offen offset:0 sc1\nM v[200:203],a[64:67],v[76:79],v[200:203]\ns_waitcnt lgkmcnt(3)\nds_read_b128 v[92:95],v23 offset:22528\nM v[200:203],a[72:75],v[88:91],v[200:203]\nM v[200:203],a[80:83],v[60:63],v[200:203]\ns_waitcnt lgkmcnt(3)\nM v[200:203],a[88:91],v[84:87],v[200:203]\n" \
"buffer_load_dwordx4 v[100:103],v4,s[36:39],s63 offen offset:1024 sc1\nM v[204:207],a[92:95],v[84:87],v[204:207]\nds_read_b128 v[84:87],v23 offset:23552\nM v[204:207],a[84:87],v[60:63],v[204:207]\nds_read_b128 v[60:63],v23 offset:28672\nM v[204:207],a[76:79],v[88:91],v[204:207]\ns_waitcnt lgkmcnt(2)\nds_read_b128 v[88:91],v23 offset:29696\n" \
"M v[204:207],a[68:71],v[76:79],v[204:207]\nbuffer_load_dwordx4 v[104:107],v4,s[36:39],s63 offen offset:2048 sc1\nM v[192:195],a[96:99],v[80:83],v[192:195]\ns_waitcnt lgkmcnt(2)\nds_read_b128 v[76:79],v23 offset:30720\nM v[192:195],a[104:107],v[72:75],v[192:195]\ns_waitcnt vmcnt(18) lgkmcnt(15)\nM v[192:195],a[112:115],v[92:95],v[192:195]\n" \
"s_barrier\nM v[192:195],a[120:123],v[84:87],v[192:195]\nbuffer_load_dwordx4 v[108:111],v4,s[36:39],s63 offen offset:3072 sc1\nM v[196:199],a[124:127],v[84:87],v[196:199]\ns_add_u32 s63,s49,4096\nds_read_b128 v[84:87],v23 offset:31744\nM v[196:199],a[116:119],v[92:95],v[196:199]\nds_read_b128 v[92:95],v20 offset:0\n" \
"M v[196:199],a[108:111],v[72:75],v[196:199]\nds_read_b128 v[72:75],v20 offset:1024\nM v[196:199],a[100:103],v[80:83],v[196:199]\ns_waitcnt lgkmcnt(3)\nds_read_b128 v[80:83],v20 offset:2048\nM v[200:203],a[96:99],v[60:63],v[200:203]\nbuffer_load_dwordx4 v[112:115],v4,s[36:39],s63 offen offset:0 sc1\nM v[200:203],a[104:107],v[88:91],v[200:203]\n" \
"M v[200:203],a[112:115],v[76:79],v[200:203]\ns_waitcnt lgkmcnt(3)\nM v[200:203],a[120:123],v[84:87],v[200:203]\nM v[204:207],a[124:127],v[84:87],v[204:207]\nds_read_b128 v[84:87],v20 offset:3072\nM v[204:207],a[116:119],v[76:79],v[204:207]\nbuffer_load_dwordx4 v[116:119],v4,s[36:39],s63 offen offset:1024 sc1\nM v[204:207],a[108:111],v[88:91],v[204:207]\n" \
"ds_read_b128 v[76:79],v20 offset:8192\nds_read_b128 v[88:91],v20 offset:9216\nM v[204:207],a[100:103],v[60:63],v[204:207]\ns_waitcnt lgkmcnt(3)\nds_read_b128 v[60:63],v20 offset:10240\nM v[192:195],a[128:131],v[92:95],v[192:195]\nM v[192:195],a[136:139],v[72:75],v[192:195]\nbuffer_load_dwordx4 v2,s[36:39],s47 offen offset:0 sc1 lds\n" \
"M v[192:195],a[144:147],v[80:83],v[192:195]\ns_waitcnt lgkmcnt(3)\nM v[192:195],a[152:155],v[84:87],v[192:195]\nM v[196:199],a[156:159],v[84:87],v[196:199]\nds_read_b128 v[84:87],v20 offset:11264\nM v[196:199],a[148:151],v[80:83],v[196:199]\nbuffer_load_dwordx4 v2,s[36:39],s47 offen offset:1024 sc1 lds\nM v[196:199],a[140:143],v[72:75],v[196:199]\n" \
"ds_read_b128 v[80:83],v20 offset:4096\nM v[196:199],a[132:135],v[92:95],v[196:199]\ns_waitcnt lgkmcnt(2)\nds_read_b128 v[72:75],v20 offset:5120\nM v[200:203],a[128:131],v[76:79],v[200:203]\nds_read_b128 v[92:95],v20 offset:6144\nM v[200:203],a[136:139],v[88:91],v[200:203]\ns_waitcnt lgkmcnt(3)\n" \
"M v[200:203],a[144:147],v[60:63],v[200:203]\nbuffer_load_dwordx4 v2,s[36:39],s47 offen offset:2048 sc1 lds\nM v[200:203],a[152:155],v[84:87],v[200:203]\nM v[204:207],a[156:159],v[84:87],v[204:207]\nds_read_b128 v[84:87],v20 offset:7168\nM v[204:207],a[148:151],v[60:63],v[204:207]\nds_read_b128 v[60:63],v20 offset:12288\nM v[204:207],a[140:143],v[88:91],v[204:207]\n" \
"buffer_load_dwordx4 v2,s[36:39],s47 offen offset:3072 sc1 lds\nM v[204:207],a[132:135],v[76:79],v[204:207]\ns_waitcnt lgkmcnt(2)\nds_read_b128 v[88:91],v20 offset:13312\nM v[192:195],a[160:163],v[80:83],v[192:195]\nds_read_b128 v[76:79],v20 offset:14336\nM v[192:195],a[168:171],v[72:75],v[192:195]\nM v[192:195],a[176:179],v[92:95],v[192:195]\n" \
"s_waitcnt lgkmcnt(3)\nM v[192:195],a[184:187],v[84:87],v[192:195]\nM v[196:199],a[188:191],v[84:87],v[196:199]\nds_read_b128 v[84:87],v20 offset:15360\nM v[196:199],a[180:183],v[92:95],v[196:199]\nds_read_b128 v[92:95],v20 offset:32768\nM v[196:199],a[172:175],v[72:75],v[196:199]\nds_read_b128 v[72:75],v20 offset:33792\n" \
"M v[196:199],a[164:167],v[80:83],v[196:199]\ns_waitcnt lgkmcnt(3)\nds_read_b128 v[80:83],v20 offset:34816\nM v[200:203],a[160:163],v[60:63],v[200:203]\nM v[200:203],a[168:171],v[88:91],v[200:203]\nM v[200:203],a[176:179],v[76:79],v[200:203]\ns_waitcnt lgkmcnt(3)\nM v[200:203],a[184:187],v[84:87],v[200:203]\n" \
"M v[204:207],a[188:191],v[84:87],v[204:207]\nds_read_b128 v[84:87],v20 offset:35840\nM v[204:207],a[180:183],v[76:79],v[204:207]\nds_read_b128 v[76:79],v20 offset:40960\nM v[204:207],a[172:175],v[88:91],v[204:207]\nds_read_b128 v[88:91],v20 offset:41984\nM v[204:207],a[164:167],v[60:63],v[204:207]\ns_waitcnt lgkmcnt(3)\n" \
"ds_read_b128 v[60:63],v20 offset:43008\nM v[192:195],a[192:195],v[92:95],v[192:195]\nM v[192:195],a[200:203],v[72:75],v[192:195]\nM v[192:195],a[208:211],v[80:83],v[192:195]\ns_waitcnt lgkmcnt(3)\nM v[192:195],a[216:219],v[84:87],v[192:195]\nM v[196:199],a[220:223],v[84:87],v[196:199]\nds_read_b128 v[84:87],v20 offset:44032\n" \
"M v[196:199],a[212:215],v[80:83],v[196:199]\nds_read_b128 v[80:83],v20 offset:36864\nM v[196:199],a[204:207],v[72:75],v[196:199]\nds_read_b128 v[72:75],v20 offset:37888\nM v[196:199],a[196:199],v[92:95],v[196:199]\ns_waitcnt lgkmcnt(3)\nds_read_b128 v[92:95],v20 offset:38912\nM v[200:203],a[192:195],v[76:79],v[200:203]\n" \
"M v[200:203],a[200:203],v[88:91],v[200:203]\nM v[200:203],a[208:211],v[60:63],v[200:203]\ns_waitcnt lgkmcnt(3)\nM v[200:203],a[216:219],v[84:87],v[200:203]\ns_add_u32 s63,s49,4096\nM v[204:207],a[220:223],v[84:87],v[204:207]\nds_read_b128 v[84:87],v20 offset:39936\nM v[204:207],a[212:215],v[60:63],v[204:207]\n" \
"buffer_load_dwordx4 v[176:179],v4,s[36:39],s63 offen offset:2048 sc1\nM v[204:207],a[204:207],v[88:91],v[204:207]\ns_waitcnt lgkmcnt(1)\nds_read_b128 v[60:63],v20 offset:45056\nds_read_b128 v[88:91],v20 offset:46080\nM v[204:207],a[196:199],v[76:79],v[204:207]\ns_waitcnt vmcnt(5)\nds_read_b128 v[76:79],v20 offset:47104\n" \
"M v[192:195],a[224:227],v[80:83],v[192:195]\ns_waitcnt lgkmcnt(3)\nM v[192:195],a[232:235],v[72:75],v[192:195]\ns_waitcnt vmcnt(17) lgkmcnt(15)\nM v[192:195],a[240:243],v[92:95],v[192:195]\ns_barrier\nM v[192:195],a[248:251],v[84:87],v[192:195]\nbuffer_load_dwordx4 v[180:183],v4,s[36:39],s63 offen offset:3072 sc1\n" \
"M v[196:199],a[252:255],v[84:87],v[196:199]\ns_add_u32 s63,s49,8192\nds_read_b128 v[84:87],v20 offset:48128\nM v[196:199],a[244:247],v[92:95],v[196:199]\nC v192,v192,v193\nC v193,v194,v195\nM v[196:199],a[236:239],v[72:75],v[196:199]\ns_waitcnt lgkmcnt(1)\n" \
"ds_read_b128 v[92:95],v20 offset:16384\nds_read_b128 v[72:75],v20 offset:17408\nM v[196:199],a[228:231],v[80:83],v[196:199]\nbuffer_load_dwordx4 v[32:35],v4,s[36:39],s63 offen offset:0 sc1\nM v[200:203],a[224:227],v[60:63],v[200:203]\nds_read_b128 v[80:83],v20 offset:18432\nM v[200:203],a[232:235],v[88:91],v[200:203]\nC v194,v196,v197\n" \
"C v195,v198,v199\nM v[200:203],a[240:243],v[76:79],v[200:203]\ns_waitcnt lgkmcnt(3)\nM v[200:203],a[248:251],v[84:87],v[200:203]\nbuffer_load_dwordx4 v[36:39],v4,s[36:39],s63 offen offset:1024 sc1\nM v[204:207],a[252:255],v[84:87],v[204:207]\nds_read_b128 v[84:87],v20 offset:19456\nM v[204:207],a[244:247],v[76:79],v[204:207]\n" \
"ds_read_b128 v[76:79],v20 offset:24576\nC v200,v200,v201\nM v[204:207],a[236:239],v[88:91],v[204:207]\nds_read_b128 v[88:91],v20 offset:25600\nC v201,v202,v203\nM v[204:207],a[228:231],v[60:63],v[204:207]\nbuffer_store_dwordx4 v[192:195],v4,s[36:39],s48 offen offset:0 sc1\nM v[48:51],v[186:189],v[120:123],0\n" \
"ds_read_b128 v[60:63],v20 offset:26624\nM v[52:55],v[184:187],v[120:123],0\nLG25_%=:\ns_cbranch_execz LHR3_%=\nC v202,v204,v205\nC v203,v206,v207\nM v[64:67],v[186:189],v[124:127],0\nM v[68:71],v[184:187],v[124:127],0\n" \
"M v[48:51],v[186:189],v[96:99],v[48:51]\nM v[52:55],v[184:187],v[96:99],v[52:55]\nM v[64:67],v[186:189],v[100:103],v[64:67]\nM v[68:71],v[184:187],v[100:103],v[68:71]\nM v[48:51],v[186:189],v[104:107],v[48:51]\nbuffer_store_dwordx4 v[200:203],v4,s[36:39],s48 offen offset:1024 sc1\nM v[52:55],v[184:187],v[104:107],v[52:55]\nM v[64:67],v[186:189],v[108:111],v[64:67]\n" \
"M v[68:71],v[184:187],v[108:111],v[68:71]\nM v[48:51],v[186:189],v[112:115],v[48:51]\nM v[52:55],v[184:187],v[112:115],v[52:55]\nM v[64:67],v[186:189],v[116:119],v[64:67]\nC v160,v48,v49\nC v161,v50,v51\nM v[68:71],v[184:187],v[116:119],v[68:71]\ns_waitcnt lgkmcnt(0)\n" \
"C v162,v52,v53\nC v163,v54,v55\nM v[208:211],a[0:3],v[92:95],0\nv_lshlrev_b32 v48,16,v160\nv_lshlrev_b32 v52,16,v162\nM v[208:211],a[8:11],v[72:75],v[208:211]\nv_and_b32 v49,0xffff0000,v160\nv_and_b32 v53,0xffff0000,v162\n" \
"M v[208:211],a[16:19],v[80:83],v[208:211]\nv_lshlrev_b32 v50,16,v161\nv_lshlrev_b32 v54,16,v163\nM v[208:211],a[24:27],v[84:87],v[208:211]\nv_and_b32 v51,0xffff0000,v161\nv_and_b32 v55,0xffff0000,v163\nM v[212:215],a[28:31],v[84:87],0\nds_read_b128 v[84:87],v20 offset:27648\n" \
"v_mul_f32 v48,v24,v48\nM v[212:215],a[20:23],v[80:83],v[212:215]\nds_read_b128 v[80:83],v20 offset:20480\nv_mul_f32 v52,v24,v52\nM v[212:215],a[12:15],v[72:75],v[212:215]\nds_read_b128 v[72:75],v20 offset:21504\nv_mul_f32 v49,v24,v49\nM v[212:215],a[4:7],v[92:95],v[212:215]\n" \
"ds_read_b128 v[92:95],v20 offset:22528\nv_mul_f32 v53,v24,v53\nM v[216:219],a[0:3],v[76:79],0\nv_mul_f32 v50,v24,v50\nv_mul_f32 v51,v24,v51\nM v[216:219],a[8:11],v[88:91],v[216:219]\nv_mul_f32 v54,v24,v54\nv_mul_f32 v55,v24,v55\n" \
"M v[216:219],a[16:19],v[60:63],v[216:219]\ns_waitcnt lgkmcnt(3)\nC v160,v48,v49\nC v162,v52,v53\nM v[216:219],a[24:27],v[84:87],v[216:219]\nC v161,v50,v51\nC v163,v54,v55\nM v[220:223],a[28:31],v[84:87],0\n" \
"ds_read_b128 v[84:87],v20 offset:23552\nv_lshlrev_b32 v48,16,v160\nM v[220:223],a[20:23],v[60:63],v[220:223]\nds_read_b128 v[60:63],v20 offset:28672\nv_lshlrev_b32 v52,16,v162\nM v[220:223],a[12:15],v[88:91],v[220:223]\nds_read_b128 v[88:91],v20 offset:29696\nv_and_b32 v49,0xffff0000,v160\n" \
"M v[220:223],a[4:7],v[76:79],v[220:223]\ns_waitcnt lgkmcnt(3)\nds_read_b128 v[76:79],v20 offset:30720\nv_lshlrev_b32 v50,16,v161\nM v[208:211],a[32:35],v[80:83],v[208:211]\nv_and_b32 v53,0xffff0000,v162\nv_lshlrev_b32 v54,16,v163\nM v[208:211],a[40:43],v[72:75],v[208:211]\n" \
"v_and_b32 v51,0xffff0000,v161\nv_and_b32 v55,0xffff0000,v163\nM v[208:211],a[48:51],v[92:95],v[208:211]\ns_waitcnt lgkmcnt(3)\nC v164,v64,v65\nC v166,v68,v69\nM v[208:211],a[56:59],v[84:87],v[208:211]\nC v165,v66,v67\n" \
"C v167,v70,v71\nM v[212:215],a[60:63],v[84:87],v[212:215]\ns_waitcnt vmcnt(2)\nds_read_b128 v[84:87],v20 offset:31744\nv_max_u32 v10,v176,v177\nM v[212:215],a[52:55],v[92:95],v[212:215]\nds_read_b128 v[92:95],v20 offset:49152\nv_max3_u32 v10,v10,v178,v179\n" \
"M v[212:215],a[44:47],v[72:75],v[212:215]\nds_read_b128 v[72:75],v20 offset:50176\nv_max3_u32 v10,v10,v180,v181\nM v[212:215],a[36:39],v[80:83],v[212:215]\ns_waitcnt lgkmcnt(3)\nds_read_b128 v[80:83],v20 offset:51200\nv_max3_u32 v10,v10,v182,v183\nM v[216:219],a[32:35],v[60:63],v[216:219]\n" \
"v_max3_u32 v10,v10,v32,v33\nv_max3_u32 v10,v10,v34,v35\nM v[216:219],a[40:43],v[88:91],v[216:219]\nv_max3_u32 v10,v10,v36,v37\nv_max3_u32 v10,v10,v38,v39\nM v[216:219],a[48:51],v[76:79],v[216:219]\ns_waitcnt lgkmcnt(3)\nv_cmp_eq_u32 vcc,-1,v10\n" \
"v_add_f32 v48,v48,v128\nM v[216:219],a[56:59],v[84:87],v[216:219]\nv_add_f32 v52,v52,v132\nv_add_f32 v49,v49,v129\nM v[220:223],a[60:63],v[84:87],v[220:223]\ns_cbranch_vccnz LIN_%=\nLINR_%=:\nds_read_b128 v[84:87],v20 offset:52224\n" \
"v_add_f32 v50,v50,v130\nM v[220:223],a[52:55],v[76:79],v[220:223]\nds_read_b128 v[76:79],v20 offset:57344\nv_add_f32 v53,v53,v133\nM v[220:223],a[44:47],v[88:91],v[220:223]\nds_read_b128 v[88:91],v20 offset:58368\nv_add_f32 v54,v54,v134\nM v[220:223],a[36:39],v[60:63],v[220:223]\n" \
"s_waitcnt lgkmcnt(3)\nds_read_b128 v[60:63],v20 offset:59392\nv_add_f32 v51,v51,v131\nM v[208:211],a[64:67],v[92:95],v[208:211]\nv_add_f32 v55,v55,v135\nv_lshlrev_b32 v64,16,v164\nM v[208:211],a[72:75],v[72:75],v[208:211]\nv_lshlrev_b32 v68,16,v166\n" \
"v_and_b32 v65,0xffff0000,v164\nM v[208:211],a[80:83],v[80:83],v[208:211]\ns_waitcnt lgkmcnt(3)\nv_and_b32 v69,0xffff0000,v166\nv_lshlrev_b32 v66,16,v165\nM v[208:211],a[88:91],v[84:87],v[208:211]\nv_lshlrev_b32 v70,16,v167\nv_and_b32 v67,0xffff0000,v165\n" \
"M v[212:215],a[92:95],v[84:87],v[212:215]\nds_read_b128 v[84:87],v20 offset:60416\nv_and_b32 v71,0xffff0000,v167\nM v[212:215],a[84:87],v[80:83],v[212:215]\nds_read_b128 v[80:83],v20 offset:53248\nC v160,v48,v49\nM v[212:215],a[76:79],v[72:75],v[212:215]\nds_read_b128 v[72:75],v20 offset:54272\n" \
"C v162,v52,v53\nM v[212:215],a[68:71],v[92:95],v[212:215]\ns_waitcnt lgkmcnt(3)\nds_read_b128 v[92:95],v20 offset:55296\nC v161,v50,v51\nM v[216:219],a[64:67],v[76:79],v[216:219]\nC v163,v54,v55\nv_mul_f32 v64,v25,v64\n" \
"M v[216:219],a[72:75],v[88:91],v[216:219]\nv_mul_f32 v68,v25,v68\nv_mul_f32 v65,v25,v65\nM v[216:219],a[80:83],v[60:63],v[216:219]\ns_waitcnt lgkmcnt(3)\nv_mul_f32 v69,v25,v69\nv_mul_f32 v66,v25,v66\nM v[216:219],a[88:91],v[84:87],v[216:219]\n" \
"v_mul_f32 v70,v25,v70\nv_mul_f32 v67,v25,v67\nM v[220:223],a[92:95],v[84:87],v[220:223]\nds_read_b128 v[84:87],v20 offset:56320\nv_mul_f32 v71,v25,v71\nM v[220:223],a[84:87],v[60:63],v[220:223]\nds_read_b128 v[60:63],v20 offset:61440\nC v164,v64,v65\n" \
"M v[220:223],a[76:79],v[88:91],v[220:223]\nds_read_b128 v[88:91],v20 offset:62464\nC v166,v68,v69\nM v[220:223],a[68:71],v[76:79],v[220:223]\ns_waitcnt lgkmcnt(3)\nds_read_b128 v[76:79],v20 offset:63488\nC v165,v66,v67\nM v[208:211],a[96:99],v[80:83],v[208:211]\n" \
"s_waitcnt lgkmcnt(3)\nv_permlane16_swap_b32 v160,v162\nM v[208:211],a[104:107],v[72:75],v[208:211]\ns_waitcnt vmcnt(6) lgkmcnt(15)\nv_permlane16_swap_b32 v161,v163\nM v[208:211],a[112:115],v[92:95],v[208:211]\ns_barrier\nC v167,v70,v71\n" \
"M v[208:211],a[120:123],v[84:87],v[208:211]\nv_lshlrev_b32 v64,16,v164\nv_lshlrev_b32 v68,16,v166\nM v[212:215],a[124:127],v[84:87],v[212:215]\nds_read_b128 v[84:87],v20 offset:64512\nv_and_b32 v65,0xffff0000,v164\nM v[212:215],a[116:119],v[92:95],v[212:215]\nds_read_b128 v[92:95],v4 offset:0\n" \
"v_and_b32 v69,0xffff0000,v166\nM v[212:215],a[108:111],v[72:75],v[212:215]\nds_read_b128 v[72:75],v4 offset:1024\nv_lshlrev_b32 v66,16,v165\nM v[212:215],a[100:103],v[80:83],v[212:215]\ns_waitcnt lgkmcnt(3)\nds_read_b128 v[80:83],v4 offset:2048\nv_lshlrev_b32 v70,16,v167\n" \
"M v[216:219],a[96:99],v[60:63],v[216:219]\nv_and_b32 v67,0xffff0000,v165\nv_and_b32 v71,0xffff0000,v167\nM v[216:219],a[104:107],v[88:91],v[216:219]\nv_add_f32 v64,v64,v136\nv_add_f32 v68,v68,v140\nM v[216:219],a[112:115],v[76:79],v[216:219]\ns_waitcnt lgkmcnt(3)\n" \
"v_add_f32 v65,v65,v137\nv_add_f32 v69,v69,v141\nM v[216:219],a[120:123],v[84:87],v[216:219]\nv_add_f32 v66,v66,v138\nv_add_f32 v70,v70,v142\nM v[220:223],a[124:127],v[84:87],v[220:223]\nds_read_b128 v[84:87],v4 offset:3072\nv_add_f32 v67,v67,v139\n" \
"M v[220:223],a[116:119],v[76:79],v[220:223]\nds_read_b128 v[76:79],v4 offset:8192\nv_add_f32 v71,v71,v143\nM v[220:223],a[108:111],v[88:91],v[220:223]\nds_read_b128 v[88:91],v4 offset:9216\nC v164,v64,v65\nM v[220:223],a[100:103],v[60:63],v[220:223]\ns_waitcnt lgkmcnt(3)\n" \
"ds_read_b128 v[60:63],v4 offset:10240\nC v166,v68,v69\nM v[208:211],a[128:131],v[92:95],v[208:211]\nC v165,v66,v67\nC v167,v70,v71\nM v[208:211],a[136:139],v[72:75],v[208:211]\nv_permlane16_swap_b32 v164,v166\nM v[208:211],a[144:147],v[80:83],v[208:211]\n" \
"s_waitcnt lgkmcnt(3)\nv_permlane16_swap_b32 v165,v167\nM v[208:211],a[152:155],v[84:87],v[208:211]\nM v[212:215],a[156:159],v[84:87],v[212:215]\nds_read_b128 v[84:87],v4 offset:11264\nM v[212:215],a[148:151],v[80:83],v[212:215]\nds_read_b128 v[80:83],v4 offset:4096\nM v[212:215],a[140:143],v[72:75],v[212:215]\n" \
"ds_read_b128 v[72:75],v4 offset:5120\nM v[212:215],a[132:135],v[92:95],v[212:215]\ns_waitcnt lgkmcnt(3)\nds_read_b128 v[92:95],v4 offset:6144\nM v[216:219],a[128:131],v[76:79],v[216:219]\nM v[216:219],a[136:139],v[88:91],v[216:219]\nM v[216:219],a[144:147],v[60:63],v[216:219]\ns_waitcnt lgkmcnt(3)\n" \
"M v[216:219],a[152:155],v[84:87],v[216:219]\nM v[220:223],a[156:159],v[84:87],v[220:223]\nds_read_b128 v[84:87],v4 offset:7168\nM v[220:223],a[148:151],v[60:63],v[220:223]\nds_read_b128 v[60:63],v4 offset:12288\nM v[220:223],a[140:143],v[88:91],v[220:223]\nds_read_b128 v[88:91],v4 offset:13312\nM v[220:223],a[132:135],v[76:79],v[220:223]\n" \
"s_waitcnt lgkmcnt(3)\nds_read_b128 v[76:79],v4 offset:14336\nM v[208:211],a[160:163],v[80:83],v[208:211]\nM v[208:211],a[168:171],v[72:75],v[208:211]\nM v[208:211],a[176:179],v[92:95],v[208:211]\ns_waitcnt lgkmcnt(3)\nM v[208:211],a[184:187],v[84:87],v[208:211]\nM v[212:215],a[188:191],v[84:87],v[212:215]\n" \
"ds_read_b128 v[84:87],v4 offset:15360\nM v[212:215],a[180:183],v[92:95],v[212:215]\ns_add_u32 s63,s49,8192\nds_read_b128 v[92:95],v4 offset:32768\nM v[212:215],a[172:175],v[72:75],v[212:215]\nbuffer_load_dwordx4 v[40:43],v4,s[36:39],s63 offen offset:2048 sc1\nM v[212:215],a[164:167],v[80:83],v[212:215]\ns_waitcnt lgkmcnt(2)\n" \
"ds_read_b128 v[72:75],v4 offset:33792\nds_read_b128 v[80:83],v4 offset:34816\nM v[216:219],a[160:163],v[60:63],v[216:219]\nM v[216:219],a[168:171],v[88:91],v[216:219]\ns_waitcnt lgkmcnt(3)\nM v[216:219],a[176:179],v[76:79],v[216:219]\nbuffer_load_dwordx4 v[44:47],v4,s[36:39],s63 offen offset:3072 sc1\nM v[216:219],a[184:187],v[84:87],v[216:219]\n" \
"M v[220:223],a[188:191],v[84:87],v[220:223]\nds_read_b128 v[84:87],v4 offset:35840\nM v[220:223],a[180:183],v[76:79],v[220:223]\ns_waitcnt lgkmcnt(1)\nds_read_b128 v[76:79],v4 offset:40960\nM v[220:223],a[172:175],v[88:91],v[220:223]\nbuffer_store_dwordx4 v[160:163],v13,s[88:91],s58 offen offset:0 sc1\nM v[220:223],a[164:167],v[60:63],v[220:223]\n" \
"s_add_u32 s58,s81,0\nds_read_b128 v[88:91],v4 offset:41984\nds_read_b128 v[60:63],v4 offset:43008\nM v[208:211],a[192:195],v[92:95],v[208:211]\nM v[208:211],a[200:203],v[72:75],v[208:211]\nM v[208:211],a[208:211],v[80:83],v[208:211]\ns_waitcnt lgkmcnt(3)\nM v[208:211],a[216:219],v[84:87],v[208:211]\n" \
"M v[212:215],a[220:223],v[84:87],v[212:215]\nds_read_b128 v[84:87],v4 offset:44032\nM v[212:215],a[212:215],v[80:83],v[212:215]\ns_waitcnt lgkmcnt(1)\nds_read_b128 v[80:83],v4 offset:36864\nM v[212:215],a[204:207],v[72:75],v[212:215]\nbuffer_store_dwordx4 v[164:167],v13,s[88:91],s59 offen offset:0 sc1\nM v[212:215],a[196:199],v[92:95],v[212:215]\n" \
"s_add_u32 s59,s81,65536\nds_read_b128 v[72:75],v4 offset:37888\nds_read_b128 v[92:95],v4 offset:38912\nM v[216:219],a[192:195],v[76:79],v[216:219]\nM v[216:219],a[200:203],v[88:91],v[216:219]\nM v[216:219],a[208:211],v[60:63],v[216:219]\ns_waitcnt lgkmcnt(3)\nM v[216:219],a[216:219],v[84:87],v[216:219]\n" \
"M v[220:223],a[220:223],v[84:87],v[220:223]\nds_read_b128 v[84:87],v4 offset:39936\nM v[220:223],a[212:215],v[60:63],v[220:223]\nds_read_b128 v[60:63],v4 offset:45056\nM v[220:223],a[204:207],v[88:91],v[220:223]\nds_read_b128 v[88:91],v4 offset:46080\nM v[220:223],a[196:199],v[76:79],v[220:223]\ns_waitcnt lgkmcnt(3)\n" \
"ds_read_b128 v[76:79],v4 offset:47104\nM v[208:211],a[224:227],v[80:83],v[208:211]\nM v[208:211],a[232:235],v[72:75],v[208:211]\nM v[208:211],a[240:243],v[92:95],v[208:211]\ns_waitcnt lgkmcnt(3)\nM v[208:211],a[248:251],v[84:87],v[208:211]\nM v[212:215],a[252:255],v[84:87],v[212:215]\nds_read_b128 v[84:87],v4 offset:48128\n" \
"M v[212:215],a[244:247],v[92:95],v[212:215]\nM v[212:215],a[236:239],v[72:75],v[212:215]\nM v[212:215],a[228:231],v[80:83],v[212:215]\ns_waitcnt lgkmcnt(1)\nM v[216:219],a[224:227],v[60:63],v[216:219]\nM v[216:219],a[232:235],v[88:91],v[216:219]\nM v[216:219],a[240:243],v[76:79],v[216:219]\ns_waitcnt lgkmcnt(0)\n" \
"M v[216:219],a[248:251],v[84:87],v[216:219]\nM v[220:223],a[252:255],v[84:87],v[220:223]\nM v[220:223],a[244:247],v[76:79],v[220:223]\nM v[220:223],a[236:239],v[88:91],v[220:223]\nM v[220:223],a[228:231],v[60:63],v[220:223]\nM v[208:211],v[186:189],v[176:179],v[208:211]\nM v[212:215],v[184:187],v[176:179],v[212:215]\nM v[216:219],v[186:189],v[180:183],v[216:219]\n" \
"s_waitcnt vmcnt(2)\nv_max_u32 v10,v40,v41\nv_max3_u32 v10,v10,v42,v43\nM v[220:223],v[184:187],v[180:183],v[220:223]\nv_max3_u32 v10,v10,v44,v45\nv_max3_u32 v10,v10,v46,v47\nM v[208:211],v[186:189],v[32:35],v[208:211]\nv_cmp_eq_u32 vcc,-1,v10\n" \
"M v[212:215],v[184:187],v[32:35],v[212:215]\nM v[216:219],v[186:189],v[36:39],v[216:219]\ns_cbranch_vccnz LIO_%=\nLIOR_%=:\nM v[220:223],v[184:187],v[36:39],v[220:223]\nM v[208:211],v[186:189],v[40:43],v[208:211]\nM v[212:215],v[184:187],v[40:43],v[212:215]\nM v[216:219],v[186:189],v[44:47],v[216:219]\n" \
"C v160,v208,v209\nC v161,v210,v211\nM v[220:223],v[184:187],v[44:47],v[220:223]\nC v162,v212,v213\nC v163,v214,v215\nv_lshlrev_b32 v208,16,v160\nv_lshlrev_b32 v210,16,v161\nv_lshlrev_b32 v212,16,v162\n" \
"v_lshlrev_b32 v214,16,v163\nv_and_b32 v209,0xffff0000,v160\nv_and_b32 v211,0xffff0000,v161\nv_and_b32 v213,0xffff0000,v162\nv_and_b32 v215,0xffff0000,v163\nv_pk_mul_f32 v[208:209],v[208:209],v[26:27] op_sel_hi:[1,0]\nv_pk_mul_f32 v[210:211],v[210:211],v[26:27] op_sel_hi:[1,0]\nv_pk_mul_f32 v[212:213],v[212:213],v[26:27] op_sel_hi:[1,0]\n" \
"v_pk_mul_f32 v[214:215],v[214:215],v[26:27] op_sel_hi:[1,0]\nC v160,v208,v209\nC v161,v210,v211\nC v162,v212,v213\nC v163,v214,v215\nv_lshlrev_b32 v208,16,v160\nv_lshlrev_b32 v210,16,v161\nv_lshlrev_b32 v212,16,v162\n" \
"v_lshlrev_b32 v214,16,v163\nv_and_b32 v209,0xffff0000,v160\nv_and_b32 v211,0xffff0000,v161\nv_and_b32 v213,0xffff0000,v162\nv_and_b32 v215,0xffff0000,v163\nv_pk_add_f32 v[208:209],v[208:209],v[144:145]\nv_pk_add_f32 v[210:211],v[210:211],v[146:147]\nv_pk_add_f32 v[212:213],v[212:213],v[148:149]\n" \
"v_pk_add_f32 v[214:215],v[214:215],v[150:151]\nC v160,v208,v209\nC v161,v210,v211\nC v162,v212,v213\nC v163,v214,v215\ns_nop 0\nv_permlane16_swap_b32 v160,v162\nv_permlane16_swap_b32 v161,v163\n" \
"buffer_store_dwordx4 v[160:163],v13,s[88:91],s58 offen offset:0 sc1\nC v164,v216,v217\nC v165,v218,v219\nC v166,v220,v221\nC v167,v222,v223\nv_lshlrev_b32 v216,16,v164\nv_lshlrev_b32 v218,16,v165\nv_lshlrev_b32 v220,16,v166\n" \
"v_lshlrev_b32 v222,16,v167\nv_and_b32 v217,0xffff0000,v164\nv_and_b32 v219,0xffff0000,v165\nv_and_b32 v221,0xffff0000,v166\nv_and_b32 v223,0xffff0000,v167\nv_pk_mul_f32 v[216:217],v[216:217],v[26:27] op_sel:[0,1] op_sel_hi:[1,1]\nv_pk_mul_f32 v[218:219],v[218:219],v[26:27] op_sel:[0,1] op_sel_hi:[1,1]\nv_pk_mul_f32 v[220:221],v[220:221],v[26:27] op_sel:[0,1] op_sel_hi:[1,1]\n" \
"v_pk_mul_f32 v[222:223],v[222:223],v[26:27] op_sel:[0,1] op_sel_hi:[1,1]\nC v164,v216,v217\nC v165,v218,v219\nC v166,v220,v221\nC v167,v222,v223\nv_lshlrev_b32 v216,16,v164\nv_lshlrev_b32 v218,16,v165\nv_lshlrev_b32 v220,16,v166\n" \
"v_lshlrev_b32 v222,16,v167\nv_and_b32 v217,0xffff0000,v164\nv_and_b32 v219,0xffff0000,v165\nv_and_b32 v221,0xffff0000,v166\nv_and_b32 v223,0xffff0000,v167\nv_pk_add_f32 v[216:217],v[216:217],v[152:153]\nv_pk_add_f32 v[218:219],v[218:219],v[154:155]\nv_pk_add_f32 v[220:221],v[220:221],v[156:157]\n" \
"v_pk_add_f32 v[222:223],v[222:223],v[158:159]\nC v164,v216,v217\nC v165,v218,v219\nC v166,v220,v221\nC v167,v222,v223\ns_nop 0\nv_permlane16_swap_b32 v164,v166\nv_permlane16_swap_b32 v165,v167\n" \
"buffer_store_dwordx4 v[164:167],v13,s[88:91],s59 offen offset:0 sc1\ns_endpgm\n.p2align 12\n.p2align 6\nLG26_%=:\ns_cbranch_execz LHR1_%=\nLH1_%=:\nds_read_b128 a[128:131],v21 offset:8192\n" \
"ds_read_b128 a[132:135],v21 offset:9216\nds_read_b128 a[136:139],v21 offset:10240\nds_read_b128 a[140:143],v21 offset:11264\nds_read_b128 a[144:147],v21 offset:12288\nds_read_b128 a[148:151],v21 offset:13312\nds_read_b128 a[152:155],v21 offset:14336\nds_read_b128 a[156:159],v21 offset:15360\nds_read_b128 a[160:163],v21 offset:0\n" \
"ds_read_b128 a[164:167],v21 offset:1024\nds_read_b128 a[168:171],v21 offset:2048\nds_read_b128 a[172:175],v21 offset:3072\nds_read_b128 a[176:179],v21 offset:4096\nds_read_b128 a[180:183],v21 offset:5120\nds_read_b128 a[184:187],v21 offset:6144\nds_read_b128 a[188:191],v21 offset:7168\nv_max3_f32 v60,v96,v97,v98\n" \
"v_max3_f32 v61,v99,v104,v105\nv_max3_f32 v60,v60,v106,v107\nv_max3_f32 v61,v61,v112,v113\nv_max3_f32 v60,v60,v114,v115\nv_max3_f32 v61,v61,v120,v121\nv_max3_f32 v60,v60,v122,v123\nv_max3_f32 v61,v61,v100,v101\nv_max3_f32 v60,v60,v102,v103\n" \
"v_max3_f32 v61,v61,v108,v109\nv_max3_f32 v60,v60,v110,v111\nv_max3_f32 v61,v61,v116,v117\nv_max3_f32 v60,v60,v118,v119\nv_max3_f32 v61,v61,v124,v125\nv_max3_f32 v61,v61,v126,v127\nv_max_f32 v60,v60,v61\ns_mov_b32 s75,1\n" \
"v_cmp_nge_f32 vcc,0x41f00000,v60\nv_xor_b32 v13,0x2000,v13\ns_cbranch_vccnz LRS_%=\nLH1X_%=:\nv_pk_mov_b32 v[32:33],v[28:29],v[28:29] op_sel:[0,0]\nv_pk_mov_b32 v[34:35],v[28:29],v[28:29] op_sel:[0,0]\nv_pk_mov_b32 v[36:37],v[28:29],v[28:29] op_sel:[1,1]\nv_pk_mov_b32 v[38:39],v[28:29],v[28:29] op_sel:[1,1]\n" \
"v_pk_mov_b32 v[40:41],v[30:31],v[30:31] op_sel:[0,0]\nv_pk_mov_b32 v[42:43],v[30:31],v[30:31] op_sel:[0,0]\nv_pk_mov_b32 v[44:45],v[30:31],v[30:31] op_sel:[1,1]\nv_pk_mov_b32 v[46:47],v[30:31],v[30:31] op_sel:[1,1]\ns_cmp_eq_u32 s52,-1\ns_cbranch_scc1 LH1R0_%=\ns_cmp_eq_u32 s52,0\ns_cbranch_scc1 LH1R1_%=\n" \
"s_cmp_eq_u32 s52,9\ns_cbranch_scc1 LH1R10_%=\ns_cmp_eq_u32 s52,11\ns_cbranch_scc1 LH1R12_%=\ns_cmp_eq_u32 s52,13\ns_cbranch_scc1 LH1R14_%=\ns_branch LH1R2_%=\n.p2align 6\n" \
"LG27_%=:\ns_cbranch_execz LHR1_%=\nLEV_%=:\ns_cmp_eq_u32 s52,s4\ns_cbranch_scc0 LEV3_%=\ns_cmp_lt_u32 %2,2\ns_cbranch_scc1 LEV2_%=\ns_mov_b64 exec,0\n" \
"s_cmp_eq_u32 %2,2\ns_cbranch_scc1 LG20_%=\ns_cmp_eq_u32 %2,3\ns_cbranch_scc1 LG21_%=\nLHR3_%=:\ns_mov_b64 exec,-1\ns_mov_b32 s4,50\ns_mov_b32 s56,50\n" \
"s_branch LEV4_%=\nLEV2_%=:\nv_rcp_f32 v60,v24\nv_rcp_f32 v61,v25\nv_rcp_f32 v62,v26\nv_rcp_f32 v63,v27\nv_xor_b32 v14,0x2000,v14\ns_mov_b32 s75,0\n" \
"s_mov_b32 s77,s68\nv_mov_b32 v24,0\nv_mov_b32 v25,0\nv_mov_b32 v26,0\nv_mov_b32 v27,0\nv_pk_mul_f32 v[128:129],v[128:129],v[60:61] op_sel_hi:[1,0]\nv_pk_mul_f32 v[130:131],v[130:131],v[60:61] op_sel_hi:[1,0]\nv_pk_mul_f32 v[132:133],v[132:133],v[60:61] op_sel_hi:[1,0]\n" \
"v_pk_mul_f32 v[134:135],v[134:135],v[60:61] op_sel_hi:[1,0]\nv_pk_mul_f32 v[136:137],v[136:137],v[60:61] op_sel_hi:[1,0]\nv_pk_mul_f32 v[138:139],v[138:139],v[60:61] op_sel_hi:[1,0]\nv_pk_mul_f32 v[140:141],v[140:141],v[60:61] op_sel_hi:[1,0]\nv_pk_mul_f32 v[142:143],v[142:143],v[60:61] op_sel_hi:[1,0]\nv_pk_mul_f32 v[144:145],v[144:145],v[60:61] op_sel_hi:[1,0]\nv_pk_mul_f32 v[146:147],v[146:147],v[60:61] op_sel_hi:[1,0]\nv_pk_mul_f32 v[148:149],v[148:149],v[60:61] op_sel_hi:[1,0]\n" \
"v_pk_mul_f32 v[150:151],v[150:151],v[60:61] op_sel_hi:[1,0]\nv_pk_mul_f32 v[152:153],v[152:153],v[60:61] op_sel_hi:[1,0]\nv_pk_mul_f32 v[154:155],v[154:155],v[60:61] op_sel_hi:[1,0]\nv_pk_mul_f32 v[156:157],v[156:157],v[60:61] op_sel_hi:[1,0]\nv_pk_mul_f32 v[158:159],v[158:159],v[60:61] op_sel_hi:[1,0]\nv_pk_mul_f32 v[160:161],v[160:161],v[60:61] op_sel:[0,1] op_sel_hi:[1,1]\nv_pk_mul_f32 v[162:163],v[162:163],v[60:61] op_sel:[0,1] op_sel_hi:[1,1]\nv_pk_mul_f32 v[164:165],v[164:165],v[60:61] op_sel:[0,1] op_sel_hi:[1,1]\n" \
"v_pk_mul_f32 v[166:167],v[166:167],v[60:61] op_sel:[0,1] op_sel_hi:[1,1]\nv_pk_mul_f32 v[168:169],v[168:169],v[60:61] op_sel:[0,1] op_sel_hi:[1,1]\nv_pk_mul_f32 v[170:171],v[170:171],v[60:61] op_sel:[0,1] op_sel_hi:[1,1]\nv_pk_mul_f32 v[172:173],v[172:173],v[60:61] op_sel:[0,1] op_sel_hi:[1,1]\nv_pk_mul_f32 v[174:175],v[174:175],v[60:61] op_sel:[0,1] op_sel_hi:[1,1]\nv_pk_mul_f32 v[176:177],v[176:177],v[60:61] op_sel:[0,1] op_sel_hi:[1,1]\nv_pk_mul_f32 v[178:179],v[178:179],v[60:61] op_sel:[0,1] op_sel_hi:[1,1]\nv_pk_mul_f32 v[180:181],v[180:181],v[60:61] op_sel:[0,1] op_sel_hi:[1,1]\n" \
"v_pk_mul_f32 v[182:183],v[182:183],v[60:61] op_sel:[0,1] op_sel_hi:[1,1]\nv_pk_mul_f32 v[184:185],v[184:185],v[60:61] op_sel:[0,1] op_sel_hi:[1,1]\nv_pk_mul_f32 v[186:187],v[186:187],v[60:61] op_sel:[0,1] op_sel_hi:[1,1]\nv_pk_mul_f32 v[188:189],v[188:189],v[60:61] op_sel:[0,1] op_sel_hi:[1,1]\nv_pk_mul_f32 v[190:191],v[190:191],v[60:61] op_sel:[0,1] op_sel_hi:[1,1]\nv_pk_mul_f32 v[192:193],v[192:193],v[62:63] op_sel_hi:[1,0]\nv_pk_mul_f32 v[194:195],v[194:195],v[62:63] op_sel_hi:[1,0]\nv_pk_mul_f32 v[196:197],v[196:197],v[62:63] op_sel_hi:[1,0]\n" \
"v_pk_mul_f32 v[198:199],v[198:199],v[62:63] op_sel_hi:[1,0]\nv_pk_mul_f32 v[200:201],v[200:201],v[62:63] op_sel_hi:[1,0]\nv_pk_mul_f32 v[202:203],v[202:203],v[62:63] op_sel_hi:[1,0]\nv_pk_mul_f32 v[204:205],v[204:205],v[62:63] op_sel_hi:[1,0]\nv_pk_mul_f32 v[206:207],v[206:207],v[62:63] op_sel_hi:[1,0]\nv_pk_mul_f32 v[208:209],v[208:209],v[62:63] op_sel_hi:[1,0]\nv_pk_mul_f32 v[210:211],v[210:211],v[62:63] op_sel_hi:[1,0]\nv_pk_mul_f32 v[212:213],v[212:213],v[62:63] op_sel_hi:[1,0]\n" \
"v_pk_mul_f32 v[214:215],v[214:215],v[62:63] op_sel_hi:[1,0]\nv_pk_mul_f32 v[216:217],v[216:217],v[62:63] op_sel_hi:[1,0]\nv_pk_mul_f32 v[218:219],v[218:219],v[62:63] op_sel_hi:[1,0]\nv_pk_mul_f32 v[220:221],v[220:221],v[62:63] op_sel_hi:[1,0]\nv_pk_mul_f32 v[222:223],v[222:223],v[62:63] op_sel_hi:[1,0]\nv_pk_mul_f32 v[224:225],v[224:225],v[62:63] op_sel:[0,1] op_sel_hi:[1,1]\nv_pk_mul_f32 v[226:227],v[226:227],v[62:63] op_sel:[0,1] op_sel_hi:[1,1]\nv_pk_mul_f32 v[228:229],v[228:229],v[62:63] op_sel:[0,1] op_sel_hi:[1,1]\n" \
"v_pk_mul_f32 v[230:231],v[230:231],v[62:63] op_sel:[0,1] op_sel_hi:[1,1]\nv_pk_mul_f32 v[232:233],v[232:233],v[62:63] op_sel:[0,1] op_sel_hi:[1,1]\nv_pk_mul_f32 v[234:235],v[234:235],v[62:63] op_sel:[0,1] op_sel_hi:[1,1]\nv_pk_mul_f32 v[236:237],v[236:237],v[62:63] op_sel:[0,1] op_sel_hi:[1,1]\nv_pk_mul_f32 v[238:239],v[238:239],v[62:63] op_sel:[0,1] op_sel_hi:[1,1]\nv_pk_mul_f32 v[240:241],v[240:241],v[62:63] op_sel:[0,1] op_sel_hi:[1,1]\nv_pk_mul_f32 v[242:243],v[242:243],v[62:63] op_sel:[0,1] op_sel_hi:[1,1]\nv_pk_mul_f32 v[244:245],v[244:245],v[62:63] op_sel:[0,1] op_sel_hi:[1,1]\n" \
"v_pk_mul_f32 v[246:247],v[246:247],v[62:63] op_sel:[0,1] op_sel_hi:[1,1]\nv_pk_mul_f32 v[248:249],v[248:249],v[62:63] op_sel:[0,1] op_sel_hi:[1,1]\nv_pk_mul_f32 v[250:251],v[250:251],v[62:63] op_sel:[0,1] op_sel_hi:[1,1]\nv_pk_mul_f32 v[252:253],v[252:253],v[62:63] op_sel:[0,1] op_sel_hi:[1,1]\nv_pk_mul_f32 v[254:255],v[254:255],v[62:63] op_sel:[0,1] op_sel_hi:[1,1]\nC v128,v128,v129\nC v129,v130,v131\nC v130,v132,v133\n" \
"C v131,v134,v135\nbuffer_store_dwordx4 v[128:131],v4,s[36:39],s77 offen offset:0 sc1\nC v136,v136,v137\nC v137,v138,v139\nC v138,v140,v141\nC v139,v142,v143\nbuffer_store_dwordx4 v[136:139],v4,s[36:39],s77 offen offset:1024 sc1\nC v144,v144,v145\n" \
"C v145,v146,v147\nC v146,v148,v149\nC v147,v150,v151\nv_mfma_f32_32x32x16_bf16 v[128:143],v[24:27],v[24:27],0\nbuffer_store_dwordx4 v[144:147],v4,s[36:39],s77 offen offset:2048 sc1\nC v152,v152,v153\nC v153,v154,v155\nC v154,v156,v157\n" \
"C v155,v158,v159\nbuffer_store_dwordx4 v[152:155],v4,s[36:39],s77 offen offset:3072 sc1\ns_add_u32 s77,s77,262144\nC v160,v160,v161\nC v161,v162,v163\nC v162,v164,v165\nC v163,v166,v167\nv_mfma_f32_32x32x16_bf16 v[144:159],v[24:27],v[24:27],0\n" \
"buffer_store_dwordx4 v[160:163],v4,s[36:39],s77 offen offset:0 sc1\nC v168,v168,v169\nC v169,v170,v171\nC v170,v172,v173\nC v171,v174,v175\nbuffer_store_dwordx4 v[168:171],v4,s[36:39],s77 offen offset:1024 sc1\nC v176,v176,v177\nC v177,v178,v179\n" \
"C v178,v180,v181\nC v179,v182,v183\nv_mfma_f32_32x32x16_bf16 v[160:175],v[24:27],v[24:27],0\nbuffer_store_dwordx4 v[176:179],v4,s[36:39],s77 offen offset:2048 sc1\nC v184,v184,v185\nC v185,v186,v187\nC v186,v188,v189\nC v187,v190,v191\n" \
"buffer_store_dwordx4 v[184:187],v4,s[36:39],s77 offen offset:3072 sc1\ns_add_u32 s77,s77,262144\nC v192,v192,v193\nC v193,v194,v195\nC v194,v196,v197\nC v195,v198,v199\nv_mfma_f32_32x32x16_bf16 v[176:191],v[24:27],v[24:27],0\nbuffer_store_dwordx4 v[192:195],v4,s[36:39],s77 offen offset:0 sc1\n" \
"C v200,v200,v201\nC v201,v202,v203\nC v202,v204,v205\nC v203,v206,v207\nbuffer_store_dwordx4 v[200:203],v4,s[36:39],s77 offen offset:1024 sc1\nC v208,v208,v209\nC v209,v210,v211\nC v210,v212,v213\n" \
"C v211,v214,v215\nv_mfma_f32_32x32x16_bf16 v[192:207],v[24:27],v[24:27],0\nbuffer_store_dwordx4 v[208:211],v4,s[36:39],s77 offen offset:2048 sc1\nC v216,v216,v217\nC v217,v218,v219\nC v218,v220,v221\nC v219,v222,v223\nbuffer_store_dwordx4 v[216:219],v4,s[36:39],s77 offen offset:3072 sc1\n" \
"s_add_u32 s77,s77,262144\nC v224,v224,v225\nC v225,v226,v227\nC v226,v228,v229\nC v227,v230,v231\nv_mfma_f32_32x32x16_bf16 v[208:223],v[24:27],v[24:27],0\nbuffer_store_dwordx4 v[224:227],v4,s[36:39],s77 offen offset:0 sc1\nC v232,v232,v233\n" \
"C v233,v234,v235\nC v234,v236,v237\nC v235,v238,v239\nbuffer_store_dwordx4 v[232:235],v4,s[36:39],s77 offen offset:1024 sc1\nC v240,v240,v241\nC v241,v242,v243\nC v242,v244,v245\nC v243,v246,v247\n" \
"v_mfma_f32_32x32x16_bf16 v[224:239],v[24:27],v[24:27],0\nbuffer_store_dwordx4 v[240:243],v4,s[36:39],s77 offen offset:2048 sc1\nC v248,v248,v249\nC v249,v250,v251\nC v250,v252,v253\nC v251,v254,v255\nbuffer_store_dwordx4 v[248:251],v4,s[36:39],s77 offen offset:3072 sc1\ns_add_u32 s77,s77,262144\n" \
"s_nop 0\nv_mfma_f32_32x32x16_bf16 v[240:255],v[24:27],v[24:27],0\ns_add_u32 s56,s4,1\ns_branch LEV4_%=\n.p2align 6\nLG28_%=:\ns_cbranch_execz LHR1_%=\nLEV3_%=:\n" \
"s_mov_b32 s56,s4\ns_cmp_lt_u32 s52,s4\ns_cbranch_scc1 LEV4_%=\ns_mov_b32 s56,50\ns_add_u32 s77,s4,1\ns_cmp_eq_u32 s52,s77\ns_cbranch_scc0 LEV4_%=\nv_lshrrev_b32 v62,5,v4\n" \
"v_mov_b32 v60,s10\nv_lshlrev_b32 v62,27,v62\nv_mov_b32 v61,s11\nv_lshl_add_u32 v62,v4,5,v62\ns_waitcnt vmcnt(16)\nbuffer_store_dwordx2 v[60:61],v62,s[36:39],s69 offen offset:0 sc1\nLEV4_%=:\ns_cmp_lt_u32 s52,8\n" \
"s_cbranch_scc0 LEV5_%=\ns_min_u32 s56,s56,8\nLEV5_%=:\ns_cmp_eq_u32 s52,8\ns_cbranch_scc0 LEV6_%=\ns_mov_b64 exec,0\ns_cmp_eq_u32 %2,0\ns_cbranch_scc1 LG16_%=\n" \
"s_cmp_eq_u32 %2,1\ns_cbranch_scc1 LG17_%=\ns_cmp_eq_u32 %2,2\ns_cbranch_scc1 LG18_%=\ns_cmp_eq_u32 %2,3\ns_cbranch_scc1 LG19_%=\nLHR2_%=:\ns_mov_b64 exec,-1\n" \
"LEV6_%=:\ns_cmp_eq_u32 s52,0\ns_cbranch_scc1 LEVR0_%=\ns_cmp_eq_u32 s52,8\ns_cbranch_scc1 LEVR8_%=\ns_cmp_eq_u32 s52,9\ns_cbranch_scc1 LEVR9_%=\ns_cmp_eq_u32 s52,10\n" \
"s_cbranch_scc1 LEVR10_%=\ns_cmp_eq_u32 s52,11\ns_cbranch_scc1 LEVR11_%=\ns_cmp_eq_u32 s52,12\ns_cbranch_scc1 LEVR12_%=\ns_cmp_eq_u32 s52,13\ns_cbranch_scc1 LEVR13_%=\ns_cmp_eq_u32 s52,14\n" \
"s_cbranch_scc1 LEVR14_%=\ns_cmp_eq_u32 s52,15\ns_cbranch_scc1 LEVR15_%=\ns_branch LEVR1_%=\nLRS_%=:\ns_nop 7\nv_max3_f32 v60,v96,v97,v98\nv_max3_f32 v61,v99,v100,v101\n" \
"v_max3_f32 v60,v60,v102,v103\nv_max3_f32 v60,v60,v61,0\nv_mov_b32 v61,v60\ns_nop 1\nv_permlane32_swap_b32 v60,v61\nv_max_f32 v60,v60,v61\nv_mov_b32 v61,v60\ns_nop 1\n" \
"v_permlane16_swap_b32 v60,v61\nv_max_f32 v60,v60,v61\nv_exp_f32 v62,-v60\nv_sub_f32 v96,v96,v60\nv_sub_f32 v97,v97,v60\nv_sub_f32 v98,v98,v60\nv_sub_f32 v99,v99,v60\nv_sub_f32 v100,v100,v60\n" \
"v_sub_f32 v101,v101,v60\nv_sub_f32 v102,v102,v60\nv_sub_f32 v103,v103,v60\nv_sub_f32 v32,v32,v60\nv_sub_f32 v33,v33,v60\nv_sub_f32 v34,v34,v60\nv_sub_f32 v35,v35,v60\nv_mul_f32 v16,v16,v62\n" \
"v_mul_f32 v128,v128,v62\nv_mul_f32 v129,v129,v62\nv_mul_f32 v130,v130,v62\nv_mul_f32 v131,v131,v62\nv_mul_f32 v132,v132,v62\nv_mul_f32 v133,v133,v62\nv_mul_f32 v134,v134,v62\nv_mul_f32 v135,v135,v62\n" \
"v_mul_f32 v136,v136,v62\nv_mul_f32 v137,v137,v62\nv_mul_f32 v138,v138,v62\nv_mul_f32 v139,v139,v62\nv_mul_f32 v140,v140,v62\nv_mul_f32 v141,v141,v62\nv_mul_f32 v142,v142,v62\nv_mul_f32 v143,v143,v62\n" \
"v_mul_f32 v144,v144,v62\nv_mul_f32 v145,v145,v62\nv_mul_f32 v146,v146,v62\nv_mul_f32 v147,v147,v62\nv_mul_f32 v148,v148,v62\nv_mul_f32 v149,v149,v62\nv_mul_f32 v150,v150,v62\nv_mul_f32 v151,v151,v62\n" \
"v_mul_f32 v152,v152,v62\nv_mul_f32 v153,v153,v62\nv_mul_f32 v154,v154,v62\nv_mul_f32 v155,v155,v62\nv_mul_f32 v156,v156,v62\nv_mul_f32 v157,v157,v62\nv_mul_f32 v158,v158,v62\nv_mul_f32 v159,v159,v62\n" \
"v_lshlrev_b32 v61,16,v64\nv_and_b32 v63,0xffff0000,v64\nv_mul_f32 v61,v61,v62\nv_mul_f32 v63,v63,v62\nC v64,v61,v63\nv_lshlrev_b32 v61,16,v65\nv_and_b32 v63,0xffff0000,v65\nv_mul_f32 v61,v61,v62\n" \
"v_mul_f32 v63,v63,v62\nC v65,v61,v63\nv_lshlrev_b32 v61,16,v66\nv_and_b32 v63,0xffff0000,v66\nv_mul_f32 v61,v61,v62\nv_mul_f32 v63,v63,v62\nC v66,v61,v63\nv_lshlrev_b32 v61,16,v67\n" \
"v_and_b32 v63,0xffff0000,v67\nv_mul_f32 v61,v61,v62\nv_mul_f32 v63,v63,v62\nC v67,v61,v63\nv_max3_f32 v60,v104,v105,v106\nv_max3_f32 v61,v107,v108,v109\nv_max3_f32 v60,v60,v110,v111\nv_max3_f32 v60,v60,v61,0\n" \
"v_mov_b32 v61,v60\ns_nop 1\nv_permlane32_swap_b32 v60,v61\nv_max_f32 v60,v60,v61\nv_mov_b32 v61,v60\ns_nop 1\nv_permlane16_swap_b32 v60,v61\nv_max_f32 v60,v60,v61\n" \
"v_exp_f32 v62,-v60\nv_sub_f32 v104,v104,v60\nv_sub_f32 v105,v105,v60\nv_sub_f32 v106,v106,v60\nv_sub_f32 v107,v107,v60\nv_sub_f32 v108,v108,v60\nv_sub_f32 v109,v109,v60\nv_sub_f32 v110,v110,v60\n" \
"v_sub_f32 v111,v111,v60\nv_sub_f32 v36,v36,v60\nv_sub_f32 v37,v37,v60\nv_sub_f32 v38,v38,v60\nv_sub_f32 v39,v39,v60\nv_mul_f32 v17,v17,v62\nv_mul_f32 v160,v160,v62\nv_mul_f32 v161,v161,v62\n" \
"v_mul_f32 v162,v162,v62\nv_mul_f32 v163,v163,v62\nv_mul_f32 v164,v164,v62\nv_mul_f32 v165,v165,v62\nv_mul_f32 v166,v166,v62\nv_mul_f32 v167,v167,v62\nv_mul_f32 v168,v168,v62\nv_mul_f32 v169,v169,v62\n" \
"v_mul_f32 v170,v170,v62\nv_mul_f32 v171,v171,v62\nv_mul_f32 v172,v172,v62\nv_mul_f32 v173,v173,v62\nv_mul_f32 v174,v174,v62\nv_mul_f32 v175,v175,v62\nv_mul_f32 v176,v176,v62\nv_mul_f32 v177,v177,v62\n" \
"v_mul_f32 v178,v178,v62\nv_mul_f32 v179,v179,v62\nv_mul_f32 v180,v180,v62\nv_mul_f32 v181,v181,v62\nv_mul_f32 v182,v182,v62\nv_mul_f32 v183,v183,v62\nv_mul_f32 v184,v184,v62\nv_mul_f32 v185,v185,v62\n" \
"v_mul_f32 v186,v186,v62\nv_mul_f32 v187,v187,v62\nv_mul_f32 v188,v188,v62\nv_mul_f32 v189,v189,v62\nv_mul_f32 v190,v190,v62\nv_mul_f32 v191,v191,v62\nv_lshlrev_b32 v61,16,v72\nv_and_b32 v63,0xffff0000,v72\n" \
"v_mul_f32 v61,v61,v62\nv_mul_f32 v63,v63,v62\nC v72,v61,v63\nv_lshlrev_b32 v61,16,v73\nv_and_b32 v63,0xffff0000,v73\nv_mul_f32 v61,v61,v62\nv_mul_f32 v63,v63,v62\nC v73,v61,v63\n" \
"v_lshlrev_b32 v61,16,v74\nv_and_b32 v63,0xffff0000,v74\nv_mul_f32 v61,v61,v62\nv_mul_f32 v63,v63,v62\nC v74,v61,v63\nv_lshlrev_b32 v61,16,v75\nv_and_b32 v63,0xffff0000,v75\nv_mul_f32 v61,v61,v62\n" \
"v_mul_f32 v63,v63,v62\nC v75,v61,v63\nv_max3_f32 v60,v112,v113,v114\nv_max3_f32 v61,v115,v116,v117\nv_max3_f32 v60,v60,v118,v119\nv_max3_f32 v60,v60,v61,0\nv_mov_b32 v61,v60\ns_nop 1\n" \
"v_permlane32_swap_b32 v60,v61\nv_max_f32 v60,v60,v61\nv_mov_b32 v61,v60\ns_nop 1\nv_permlane16_swap_b32 v60,v61\nv_max_f32 v60,v60,v61\nv_exp_f32 v62,-v60\nv_sub_f32 v112,v112,v60\n" \
"v_sub_f32 v113,v113,v60\nv_sub_f32 v114,v114,v60\nv_sub_f32 v115,v115,v60\nv_sub_f32 v116,v116,v60\nv_sub_f32 v117,v117,v60\nv_sub_f32 v118,v118,v60\nv_sub_f32 v119,v119,v60\nv_sub_f32 v40,v40,v60\n" \
"v_sub_f32 v41,v41,v60\nv_sub_f32 v42,v42,v60\nv_sub_f32 v43,v43,v60\nv_mul_f32 v18,v18,v62\nv_mul_f32 v192,v192,v62\nv_mul_f32 v193,v193,v62\nv_mul_f32 v194,v194,v62\nv_mul_f32 v195,v195,v62\n" \
"v_mul_f32 v196,v196,v62\nv_mul_f32 v197,v197,v62\nv_mul_f32 v198,v198,v62\nv_mul_f32 v199,v199,v62\nv_mul_f32 v200,v200,v62\nv_mul_f32 v201,v201,v62\nv_mul_f32 v202,v202,v62\nv_mul_f32 v203,v203,v62\n" \
"v_mul_f32 v204,v204,v62\nv_mul_f32 v205,v205,v62\nv_mul_f32 v206,v206,v62\nv_mul_f32 v207,v207,v62\nv_mul_f32 v208,v208,v62\nv_mul_f32 v209,v209,v62\nv_mul_f32 v210,v210,v62\nv_mul_f32 v211,v211,v62\n" \
"v_mul_f32 v212,v212,v62\nv_mul_f32 v213,v213,v62\nv_mul_f32 v214,v214,v62\nv_mul_f32 v215,v215,v62\nv_mul_f32 v216,v216,v62\nv_mul_f32 v217,v217,v62\nv_mul_f32 v218,v218,v62\nv_mul_f32 v219,v219,v62\n" \
"v_mul_f32 v220,v220,v62\nv_mul_f32 v221,v221,v62\nv_mul_f32 v222,v222,v62\nv_mul_f32 v223,v223,v62\nv_lshlrev_b32 v61,16,v80\nv_and_b32 v63,0xffff0000,v80\nv_mul_f32 v61,v61,v62\nv_mul_f32 v63,v63,v62\n" \
"C v80,v61,v63\nv_lshlrev_b32 v61,16,v81\nv_and_b32 v63,0xffff0000,v81\nv_mul_f32 v61,v61,v62\nv_mul_f32 v63,v63,v62\nC v81,v61,v63\nv_lshlrev_b32 v61,16,v82\nv_and_b32 v63,0xffff0000,v82\n" \
"v_mul_f32 v61,v61,v62\nv_mul_f32 v63,v63,v62\nC v82,v61,v63\nv_lshlrev_b32 v61,16,v83\nv_and_b32 v63,0xffff0000,v83\nv_mul_f32 v61,v61,v62\nv_mul_f32 v63,v63,v62\nC v83,v61,v63\n" \
"v_max3_f32 v60,v120,v121,v122\nv_max3_f32 v61,v123,v124,v125\nv_max3_f32 v60,v60,v126,v127\nv_max3_f32 v60,v60,v61,0\nv_mov_b32 v61,v60\ns_nop 1\nv_permlane32_swap_b32 v60,v61\nv_max_f32 v60,v60,v61\n" \
"v_mov_b32 v61,v60\ns_nop 1\nv_permlane16_swap_b32 v60,v61\nv_max_f32 v60,v60,v61\nv_exp_f32 v62,-v60\nv_sub_f32 v120,v120,v60\nv_sub_f32 v121,v121,v60\nv_sub_f32 v122,v122,v60\n" \
"v_sub_f32 v123,v123,v60\nv_sub_f32 v124,v124,v60\nv_sub_f32 v125,v125,v60\nv_sub_f32 v126,v126,v60\nv_sub_f32 v127,v127,v60\nv_sub_f32 v44,v44,v60\nv_sub_f32 v45,v45,v60\nv_sub_f32 v46,v46,v60\n" \
"v_sub_f32 v47,v47,v60\nv_mul_f32 v19,v19,v62\nv_mul_f32 v224,v224,v62\nv_mul_f32 v225,v225,v62\nv_mul_f32 v226,v226,v62\nv_mul_f32 v227,v227,v62\nv_mul_f32 v228,v228,v62\nv_mul_f32 v229,v229,v62\n" \
"v_mul_f32 v230,v230,v62\nv_mul_f32 v231,v231,v62\nv_mul_f32 v232,v232,v62\nv_mul_f32 v233,v233,v62\nv_mul_f32 v234,v234,v62\nv_mul_f32 v235,v235,v62\nv_mul_f32 v236,v236,v62\nv_mul_f32 v237,v237,v62\n" \
"v_mul_f32 v238,v238,v62\nv_mul_f32 v239,v239,v62\nv_mul_f32 v240,v240,v62\nv_mul_f32 v241,v241,v62\nv_mul_f32 v242,v242,v62\nv_mul_f32 v243,v243,v62\nv_mul_f32 v244,v244,v62\nv_mul_f32 v245,v245,v62\n" \
"v_mul_f32 v246,v246,v62\nv_mul_f32 v247,v247,v62\nv_mul_f32 v248,v248,v62\nv_mul_f32 v249,v249,v62\nv_mul_f32 v250,v250,v62\nv_mul_f32 v251,v251,v62\nv_mul_f32 v252,v252,v62\nv_mul_f32 v253,v253,v62\n" \
"v_mul_f32 v254,v254,v62\nv_mul_f32 v255,v255,v62\nv_lshlrev_b32 v61,16,v88\nv_and_b32 v63,0xffff0000,v88\nv_mul_f32 v61,v61,v62\nv_mul_f32 v63,v63,v62\nC v88,v61,v63\nv_lshlrev_b32 v61,16,v89\n" \
"v_and_b32 v63,0xffff0000,v89\nv_mul_f32 v61,v61,v62\nv_mul_f32 v63,v63,v62\nC v89,v61,v63\nv_lshlrev_b32 v61,16,v90\nv_and_b32 v63,0xffff0000,v90\nv_mul_f32 v61,v61,v62\nv_mul_f32 v63,v63,v62\n" \
"C v90,v61,v63\nv_lshlrev_b32 v61,16,v91\nv_and_b32 v63,0xffff0000,v91\nv_mul_f32 v61,v61,v62\nv_mul_f32 v63,v63,v62\nC v91,v61,v63\ns_cmp_eq_u32 s52,14\ns_cbranch_scc1 LH1R15_%=\n" \
"s_branch LH1X_%=\nLcs1_%=:\ns_add_u32 s9,s5,2\ns_mov_b32 s8,3\ns_mov_b32 s64,0\ns_branch LSLOW1_%=\nLcs3_%=:\ns_add_u32 s9,s5,1\n" \
"s_mov_b32 s8,2\ns_mov_b32 s64,0\ns_branch LSLOW0_%=\nLcs5_%=:\ns_add_u32 s9,s5,0\ns_mov_b32 s8,1\ns_mov_b32 s64,1\ns_branch LSLOW1_%=\n" \
"Lcs7_%=:\ns_lshl_b32 s9,s52,1\ns_add_u32 s9,s9,0\ns_sub_u32 s8,59,s9\ns_cmp_ge_u32 s9,s70\ns_cselect_b32 s9,s8,s9\ns_mov_b32 s8,0\ns_mov_b32 s64,1\n" \
"s_branch LSLOW0_%=\nLcs9_%=:\ns_lshl_b32 s9,s52,1\ns_add_u32 s9,s9,1\ns_sub_u32 s8,59,s9\ns_cmp_ge_u32 s9,s70\ns_cselect_b32 s9,s8,s9\ns_mov_b32 s8,0\n" \
"s_mov_b32 s64,2\ns_branch LSLOW1_%=\nLcs11_%=:\ns_lshl_b32 s9,s52,1\ns_add_u32 s9,s9,0\ns_sub_u32 s8,59,s9\ns_cmp_ge_u32 s9,s70\ns_cselect_b32 s9,s8,s9\n" \
"s_mov_b32 s8,0\ns_mov_b32 s64,2\ns_branch LSLOW0_%=\nLcs13_%=:\ns_lshl_b32 s9,s52,1\ns_add_u32 s9,s9,1\ns_sub_u32 s8,59,s9\ns_cmp_ge_u32 s9,s70\n" \
"s_cselect_b32 s9,s8,s9\ns_mov_b32 s8,0\ns_mov_b32 s64,3\ns_branch LSLOW1_%=\nLcs39_%=:\ns_lshl_b32 s9,s52,1\ns_add_u32 s9,s9,0\ns_sub_u32 s8,59,s9\n" \
"s_cmp_ge_u32 s9,s70\ns_cselect_b32 s9,s8,s9\ns_mov_b32 s8,0\ns_mov_b32 s64,3\ns_branch LSLOW0_%=\nLcs41_%=:\ns_lshl_b32 s9,s52,1\ns_add_u32 s9,s9,1\n" \
"s_sub_u32 s8,59,s9\ns_cmp_ge_u32 s9,s70\ns_cselect_b32 s9,s8,s9\ns_mov_b32 s8,0\ns_mov_b32 s64,4\ns_branch LSLOW1_%=\nLcs43_%=:\ns_lshl_b32 s9,s52,1\n" \
"s_add_u32 s9,s9,0\ns_sub_u32 s8,59,s9\ns_cmp_ge_u32 s9,s70\ns_cselect_b32 s9,s8,s9\ns_mov_b32 s8,0\ns_mov_b32 s64,4\ns_branch LSLOW0_%=\nLcs45_%=:\n" \
"s_lshl_b32 s9,s52,1\ns_add_u32 s9,s9,1\ns_sub_u32 s8,59,s9\ns_cmp_ge_u32 s9,s70\ns_cselect_b32 s9,s8,s9\ns_mov_b32 s8,0\ns_mov_b32 s64,5\ns_branch LSLOW1_%=\n" \
"Lcs47_%=:\ns_lshl_b32 s9,s52,1\ns_add_u32 s9,s9,0\ns_sub_u32 s8,59,s9\ns_cmp_ge_u32 s9,s70\ns_cselect_b32 s9,s8,s9\ns_mov_b32 s8,0\ns_mov_b32 s64,5\n" \
"s_branch LSLOW0_%=\nLcs49_%=:\ns_lshl_b32 s9,s52,1\ns_add_u32 s9,s9,1\ns_sub_u32 s8,59,s9\ns_cmp_ge_u32 s9,s70\ns_cselect_b32 s9,s8,s9\ns_mov_b32 s8,0\n" \
"s_mov_b32 s64,6\ns_branch LSLOW1_%=\nLcs51_%=:\ns_lshl_b32 s9,s52,1\ns_add_u32 s9,s9,0\ns_sub_u32 s8,59,s9\ns_cmp_ge_u32 s9,s70\ns_cselect_b32 s9,s8,s9\n" \
"s_mov_b32 s8,0\ns_mov_b32 s64,6\ns_branch LSLOW0_%=\nLcs53_%=:\ns_lshl_b32 s9,s52,1\ns_add_u32 s9,s9,1\ns_sub_u32 s8,59,s9\ns_cmp_ge_u32 s9,s70\n" \
"s_cselect_b32 s9,s8,s9\ns_mov_b32 s8,0\ns_mov_b32 s64,7\ns_branch LSLOW1_%=\nLcs55_%=:\ns_lshl_b32 s9,s52,1\ns_add_u32 s9,s9,0\ns_sub_u32 s8,59,s9\n" \
"s_cmp_ge_u32 s9,s70\ns_cselect_b32 s9,s8,s9\ns_mov_b32 s8,0\ns_mov_b32 s64,7\ns_branch LSLOW0_%=\nLcs57_%=:\ns_lshl_b32 s9,s52,1\ns_add_u32 s9,s9,1\n" \
"s_sub_u32 s8,59,s9\ns_cmp_ge_u32 s9,s70\ns_cselect_b32 s9,s8,s9\ns_mov_b32 s8,0\ns_mov_b32 s64,8\ns_branch LSLOW1_%=\nLcs59_%=:\ns_lshl_b32 s9,s52,1\n" \
"s_add_u32 s9,s9,0\ns_sub_u32 s8,59,s9\ns_cmp_ge_u32 s9,s70\ns_cselect_b32 s9,s8,s9\ns_mov_b32 s8,0\ns_mov_b32 s64,8\ns_branch LSLOW0_%=\nLcs61_%=:\n" \
"s_lshl_b32 s9,s52,1\ns_add_u32 s9,s9,1\ns_sub_u32 s8,59,s9\ns_cmp_ge_u32 s9,s70\ns_cselect_b32 s9,s8,s9\ns_mov_b32 s8,0\ns_mov_b32 s64,9\ns_branch LSLOW1_%=\n" \
"Lcs63_%=:\ns_lshl_b32 s9,s52,1\ns_add_u32 s9,s9,0\ns_sub_u32 s8,59,s9\ns_cmp_ge_u32 s9,s70\ns_cselect_b32 s9,s8,s9\ns_mov_b32 s8,0\ns_mov_b32 s64,9\n" \
"s_branch LSLOW0_%=\nLSLOW0_%=:\ns_lshl_b32 s9,s9,14\ns_add_u32 s9,s9,s71\ns_nop 7\ns_waitcnt lgkmcnt(0)\nbuffer_load_dwordx4 a[240:243],v4,s[36:39],s9 offen offset:0 sc1\nbuffer_load_dwordx4 a[244:247],v4,s[36:39],s9 offen offset:2048 sc1\n" \
"s_add_u32 s9,s9,4096\nbuffer_load_dwordx4 a[248:251],v4,s[36:39],s9 offen offset:0 sc1\nbuffer_load_dwordx4 a[252:255],v4,s[36:39],s9 offen offset:2048 sc1\ns_sub_u32 s9,s9,3072\ns_waitcnt vmcnt(0)\ns_cmp_gt_u32 s8,0\ns_cbranch_scc1 Lk65_%=\nM v[64:67],a[240:243],a[128:131],v[32:35]\n" \
"M v[64:67],a[244:247],a[132:135],v[64:67]\nM v[64:67],a[248:251],a[136:139],v[64:67]\nM v[64:67],a[252:255],a[140:143],v[64:67]\nLk65_%=:\ns_cmp_gt_u32 s8,1\ns_cbranch_scc1 Lk66_%=\nM v[72:75],a[240:243],a[144:147],v[36:39]\nM v[72:75],a[244:247],a[148:151],v[72:75]\n" \
"M v[72:75],a[248:251],a[152:155],v[72:75]\nM v[72:75],a[252:255],a[156:159],v[72:75]\nLk66_%=:\ns_cmp_gt_u32 s8,2\ns_cbranch_scc1 Lk67_%=\nM v[80:83],a[240:243],a[160:163],v[40:43]\nM v[80:83],a[244:247],a[164:167],v[80:83]\nM v[80:83],a[248:251],a[168:171],v[80:83]\n" \
"M v[80:83],a[252:255],a[172:175],v[80:83]\nLk67_%=:\ns_cmp_gt_u32 s8,3\ns_cbranch_scc1 Lk68_%=\nM v[88:91],a[240:243],a[176:179],v[44:47]\nM v[88:91],a[244:247],a[180:183],v[88:91]\nM v[88:91],a[248:251],a[184:187],v[88:91]\nM v[88:91],a[252:255],a[188:191],v[88:91]\n" \
"Lk68_%=:\ns_nop 7\nbuffer_load_dwordx4 a[240:243],v4,s[36:39],s9 offen offset:0 sc1\nbuffer_load_dwordx4 a[244:247],v4,s[36:39],s9 offen offset:2048 sc1\ns_add_u32 s9,s9,4096\nbuffer_load_dwordx4 a[248:251],v4,s[36:39],s9 offen offset:0 sc1\nbuffer_load_dwordx4 a[252:255],v4,s[36:39],s9 offen offset:2048 sc1\ns_sub_u32 s9,s9,3072\n" \
"s_waitcnt vmcnt(0)\ns_cmp_gt_u32 s8,0\ns_cbranch_scc1 Lk69_%=\nM v[68:71],a[240:243],a[128:131],v[32:35]\nM v[68:71],a[244:247],a[132:135],v[68:71]\nM v[68:71],a[248:251],a[136:139],v[68:71]\nM v[68:71],a[252:255],a[140:143],v[68:71]\nLk69_%=:\n" \
"s_cmp_gt_u32 s8,1\ns_cbranch_scc1 Lk70_%=\nM v[76:79],a[240:243],a[144:147],v[36:39]\nM v[76:79],a[244:247],a[148:151],v[76:79]\nM v[76:79],a[248:251],a[152:155],v[76:79]\nM v[76:79],a[252:255],a[156:159],v[76:79]\nLk70_%=:\ns_cmp_gt_u32 s8,2\n" \
"s_cbranch_scc1 Lk71_%=\nM v[84:87],a[240:243],a[160:163],v[40:43]\nM v[84:87],a[244:247],a[164:167],v[84:87]\nM v[84:87],a[248:251],a[168:171],v[84:87]\nM v[84:87],a[252:255],a[172:175],v[84:87]\nLk71_%=:\ns_cmp_gt_u32 s8,3\ns_cbranch_scc1 Lk72_%=\n" \
"M v[92:95],a[240:243],a[176:179],v[44:47]\nM v[92:95],a[244:247],a[180:183],v[92:95]\nM v[92:95],a[248:251],a[184:187],v[92:95]\nM v[92:95],a[252:255],a[188:191],v[92:95]\nLk72_%=:\ns_nop 7\ns_cmp_gt_u32 s8,0\ns_cbranch_scc0 Lk74_%=\n" \
"v_mov_b32 v16,v24\ns_cmp_eq_u32 s8,1\ns_cbranch_scc0 Lk73_%=\nM v[16:19],a[224:227],v[64:67],v[16:19]\ns_nop 7\ns_branch Lk73_%=\nLk74_%=:\nv_max3_f32 v62,v64,v65,v66\n" \
"v_max3_f32 v63,v67,v68,v69\nv_max3_f32 v62,v62,v70,v71\nv_max3_f32 v62,v62,v63,0\nv_mov_b32 v63,v62\ns_nop 1\nv_permlane32_swap_b32 v62,v63\nv_max_f32 v62,v62,v63\nv_mov_b32 v63,v62\n" \
"s_nop 1\nv_permlane16_swap_b32 v62,v63\nv_max_f32 v62,v62,v63\nv_exp_f32 v63,-v62\nv_sub_f32 v64,v64,v62\nv_sub_f32 v65,v65,v62\nv_sub_f32 v66,v66,v62\nv_sub_f32 v67,v67,v62\n" \
"v_sub_f32 v68,v68,v62\nv_sub_f32 v69,v69,v62\nv_sub_f32 v70,v70,v62\nv_sub_f32 v71,v71,v62\nv_exp_f32 v64,v64\nv_exp_f32 v65,v65\nv_exp_f32 v66,v66\nv_exp_f32 v67,v67\n" \
"v_exp_f32 v68,v68\nv_exp_f32 v69,v69\nv_exp_f32 v70,v70\nv_exp_f32 v71,v71\nv_mul_f32 v16,v24,v63\nC v64,v64,v65\nC v65,v66,v67\nC v66,v68,v69\n" \
"C v67,v70,v71\nv_sub_f32 v32,v32,v62\nv_sub_f32 v33,v33,v62\nv_sub_f32 v34,v34,v62\nv_sub_f32 v35,v35,v62\nM v[16:19],a[224:227],v[64:67],v[16:19]\nv_mul_f32 v128,v128,v63\nv_mul_f32 v129,v129,v63\n" \
"v_mul_f32 v130,v130,v63\nv_mul_f32 v131,v131,v63\nv_mul_f32 v132,v132,v63\nv_mul_f32 v133,v133,v63\nv_mul_f32 v134,v134,v63\nv_mul_f32 v135,v135,v63\nv_mul_f32 v136,v136,v63\nv_mul_f32 v137,v137,v63\n" \
"v_mul_f32 v138,v138,v63\nv_mul_f32 v139,v139,v63\nv_mul_f32 v140,v140,v63\nv_mul_f32 v141,v141,v63\nv_mul_f32 v142,v142,v63\nv_mul_f32 v143,v143,v63\nv_mul_f32 v144,v144,v63\nv_mul_f32 v145,v145,v63\n" \
"v_mul_f32 v146,v146,v63\nv_mul_f32 v147,v147,v63\nv_mul_f32 v148,v148,v63\nv_mul_f32 v149,v149,v63\nv_mul_f32 v150,v150,v63\nv_mul_f32 v151,v151,v63\nv_mul_f32 v152,v152,v63\nv_mul_f32 v153,v153,v63\n" \
"v_mul_f32 v154,v154,v63\nv_mul_f32 v155,v155,v63\nv_mul_f32 v156,v156,v63\nv_mul_f32 v157,v157,v63\nv_mul_f32 v158,v158,v63\nv_mul_f32 v159,v159,v63\ns_bitcmp1_b32 s64,6\ns_cbranch_scc1 Lk73_%=\n" \
"v_sub_f32 v96,v96,v62\nv_sub_f32 v97,v97,v62\nv_sub_f32 v98,v98,v62\nv_sub_f32 v99,v99,v62\nv_sub_f32 v100,v100,v62\nv_sub_f32 v101,v101,v62\nv_sub_f32 v102,v102,v62\nv_sub_f32 v103,v103,v62\n" \
"Lk73_%=:\ns_cmp_gt_u32 s8,1\ns_cbranch_scc0 Lk76_%=\nv_mov_b32 v17,v25\ns_cmp_eq_u32 s8,2\ns_cbranch_scc0 Lk75_%=\nM v[16:19],a[228:231],v[72:75],v[16:19]\ns_nop 7\n" \
"s_branch Lk75_%=\nLk76_%=:\nv_max3_f32 v62,v72,v73,v74\nv_max3_f32 v63,v75,v76,v77\nv_max3_f32 v62,v62,v78,v79\nv_max3_f32 v62,v62,v63,0\nv_mov_b32 v63,v62\ns_nop 1\n" \
"v_permlane32_swap_b32 v62,v63\nv_max_f32 v62,v62,v63\nv_mov_b32 v63,v62\ns_nop 1\nv_permlane16_swap_b32 v62,v63\nv_max_f32 v62,v62,v63\nv_exp_f32 v63,-v62\nv_sub_f32 v72,v72,v62\n" \
"v_sub_f32 v73,v73,v62\nv_sub_f32 v74,v74,v62\nv_sub_f32 v75,v75,v62\nv_sub_f32 v76,v76,v62\nv_sub_f32 v77,v77,v62\nv_sub_f32 v78,v78,v62\nv_sub_f32 v79,v79,v62\nv_exp_f32 v72,v72\n" \
"v_exp_f32 v73,v73\nv_exp_f32 v74,v74\nv_exp_f32 v75,v75\nv_exp_f32 v76,v76\nv_exp_f32 v77,v77\nv_exp_f32 v78,v78\nv_exp_f32 v79,v79\nv_mul_f32 v17,v25,v63\n" \
"C v72,v72,v73\nC v73,v74,v75\nC v74,v76,v77\nC v75,v78,v79\nv_sub_f32 v36,v36,v62\nv_sub_f32 v37,v37,v62\nv_sub_f32 v38,v38,v62\nv_sub_f32 v39,v39,v62\n" \
"M v[16:19],a[228:231],v[72:75],v[16:19]\nv_mul_f32 v160,v160,v63\nv_mul_f32 v161,v161,v63\nv_mul_f32 v162,v162,v63\nv_mul_f32 v163,v163,v63\nv_mul_f32 v164,v164,v63\nv_mul_f32 v165,v165,v63\nv_mul_f32 v166,v166,v63\n" \
"v_mul_f32 v167,v167,v63\nv_mul_f32 v168,v168,v63\nv_mul_f32 v169,v169,v63\nv_mul_f32 v170,v170,v63\nv_mul_f32 v171,v171,v63\nv_mul_f32 v172,v172,v63\nv_mul_f32 v173,v173,v63\nv_mul_f32 v174,v174,v63\n" \
"v_mul_f32 v175,v175,v63\nv_mul_f32 v176,v176,v63\nv_mul_f32 v177,v177,v63\nv_mul_f32 v178,v178,v63\nv_mul_f32 v179,v179,v63\nv_mul_f32 v180,v180,v63\nv_mul_f32 v181,v181,v63\nv_mul_f32 v182,v182,v63\n" \
"v_mul_f32 v183,v183,v63\nv_mul_f32 v184,v184,v63\nv_mul_f32 v185,v185,v63\nv_mul_f32 v186,v186,v63\nv_mul_f32 v187,v187,v63\nv_mul_f32 v188,v188,v63\nv_mul_f32 v189,v189,v63\nv_mul_f32 v190,v190,v63\n" \
"v_mul_f32 v191,v191,v63\ns_bitcmp1_b32 s64,6\ns_cbranch_scc1 Lk75_%=\nv_sub_f32 v104,v104,v62\nv_sub_f32 v105,v105,v62\nv_sub_f32 v106,v106,v62\nv_sub_f32 v107,v107,v62\nv_sub_f32 v108,v108,v62\n" \
"v_sub_f32 v109,v109,v62\nv_sub_f32 v110,v110,v62\nv_sub_f32 v111,v111,v62\nLk75_%=:\ns_cmp_gt_u32 s8,2\ns_cbranch_scc0 Lk78_%=\nv_mov_b32 v18,v26\ns_cmp_eq_u32 s8,3\n" \
"s_cbranch_scc0 Lk77_%=\nM v[16:19],a[232:235],v[80:83],v[16:19]\ns_nop 7\ns_branch Lk77_%=\nLk78_%=:\nv_max3_f32 v62,v80,v81,v82\nv_max3_f32 v63,v83,v84,v85\nv_max3_f32 v62,v62,v86,v87\n" \
"v_max3_f32 v62,v62,v63,0\nv_mov_b32 v63,v62\ns_nop 1\nv_permlane32_swap_b32 v62,v63\nv_max_f32 v62,v62,v63\nv_mov_b32 v63,v62\ns_nop 1\nv_permlane16_swap_b32 v62,v63\n" \
"v_max_f32 v62,v62,v63\nv_exp_f32 v63,-v62\nv_sub_f32 v80,v80,v62\nv_sub_f32 v81,v81,v62\nv_sub_f32 v82,v82,v62\nv_sub_f32 v83,v83,v62\nv_sub_f32 v84,v84,v62\nv_sub_f32 v85,v85,v62\n" \
"v_sub_f32 v86,v86,v62\nv_sub_f32 v87,v87,v62\nv_exp_f32 v80,v80\nv_exp_f32 v81,v81\nv_exp_f32 v82,v82\nv_exp_f32 v83,v83\nv_exp_f32 v84,v84\nv_exp_f32 v85,v85\n" \
"v_exp_f32 v86,v86\nv_exp_f32 v87,v87\nv_mul_f32 v18,v26,v63\nC v80,v80,v81\nC v81,v82,v83\nC v82,v84,v85\nC v83,v86,v87\nv_sub_f32 v40,v40,v62\n" \
"v_sub_f32 v41,v41,v62\nv_sub_f32 v42,v42,v62\nv_sub_f32 v43,v43,v62\nM v[16:19],a[232:235],v[80:83],v[16:19]\nv_mul_f32 v192,v192,v63\nv_mul_f32 v193,v193,v63\nv_mul_f32 v194,v194,v63\nv_mul_f32 v195,v195,v63\n" \
"v_mul_f32 v196,v196,v63\nv_mul_f32 v197,v197,v63\nv_mul_f32 v198,v198,v63\nv_mul_f32 v199,v199,v63\nv_mul_f32 v200,v200,v63\nv_mul_f32 v201,v201,v63\nv_mul_f32 v202,v202,v63\nv_mul_f32 v203,v203,v63\n" \
"v_mul_f32 v204,v204,v63\nv_mul_f32 v205,v205,v63\nv_mul_f32 v206,v206,v63\nv_mul_f32 v207,v207,v63\nv_mul_f32 v208,v208,v63\nv_mul_f32 v209,v209,v63\nv_mul_f32 v210,v210,v63\nv_mul_f32 v211,v211,v63\n" \
"v_mul_f32 v212,v212,v63\nv_mul_f32 v213,v213,v63\nv_mul_f32 v214,v214,v63\nv_mul_f32 v215,v215,v63\nv_mul_f32 v216,v216,v63\nv_mul_f32 v217,v217,v63\nv_mul_f32 v218,v218,v63\nv_mul_f32 v219,v219,v63\n" \
"v_mul_f32 v220,v220,v63\nv_mul_f32 v221,v221,v63\nv_mul_f32 v222,v222,v63\nv_mul_f32 v223,v223,v63\ns_bitcmp1_b32 s64,6\ns_cbranch_scc1 Lk77_%=\nv_sub_f32 v112,v112,v62\nv_sub_f32 v113,v113,v62\n" \
"v_sub_f32 v114,v114,v62\nv_sub_f32 v115,v115,v62\nv_sub_f32 v116,v116,v62\nv_sub_f32 v117,v117,v62\nv_sub_f32 v118,v118,v62\nv_sub_f32 v119,v119,v62\nLk77_%=:\ns_cmp_gt_u32 s8,3\n" \
"s_cbranch_scc0 Lk80_%=\nv_mov_b32 v19,v27\ns_cmp_eq_u32 s8,4\ns_cbranch_scc0 Lk79_%=\nM v[16:19],a[236:239],v[88:91],v[16:19]\ns_nop 7\ns_branch Lk79_%=\nLk80_%=:\n" \
"v_max3_f32 v62,v88,v89,v90\nv_max3_f32 v63,v91,v92,v93\nv_max3_f32 v62,v62,v94,v95\nv_max3_f32 v62,v62,v63,0\nv_mov_b32 v63,v62\ns_nop 1\nv_permlane32_swap_b32 v62,v63\nv_max_f32 v62,v62,v63\n" \
"v_mov_b32 v63,v62\ns_nop 1\nv_permlane16_swap_b32 v62,v63\nv_max_f32 v62,v62,v63\nv_exp_f32 v63,-v62\nv_sub_f32 v88,v88,v62\nv_sub_f32 v89,v89,v62\nv_sub_f32 v90,v90,v62\n" \
"v_sub_f32 v91,v91,v62\nv_sub_f32 v92,v92,v62\nv_sub_f32 v93,v93,v62\nv_sub_f32 v94,v94,v62\nv_sub_f32 v95,v95,v62\nv_exp_f32 v88,v88\nv_exp_f32 v89,v89\nv_exp_f32 v90,v90\n" \
"v_exp_f32 v91,v91\nv_exp_f32 v92,v92\nv_exp_f32 v93,v93\nv_exp_f32 v94,v94\nv_exp_f32 v95,v95\nv_mul_f32 v19,v27,v63\nC v88,v88,v89\nC v89,v90,v91\n" \
"C v90,v92,v93\nC v91,v94,v95\nv_sub_f32 v44,v44,v62\nv_sub_f32 v45,v45,v62\nv_sub_f32 v46,v46,v62\nv_sub_f32 v47,v47,v62\nM v[16:19],a[236:239],v[88:91],v[16:19]\nv_mul_f32 v224,v224,v63\n" \
"v_mul_f32 v225,v225,v63\nv_mul_f32 v226,v226,v63\nv_mul_f32 v227,v227,v63\nv_mul_f32 v228,v228,v63\nv_mul_f32 v229,v229,v63\nv_mul_f32 v230,v230,v63\nv_mul_f32 v231,v231,v63\nv_mul_f32 v232,v232,v63\n" \
"v_mul_f32 v233,v233,v63\nv_mul_f32 v234,v234,v63\nv_mul_f32 v235,v235,v63\nv_mul_f32 v236,v236,v63\nv_mul_f32 v237,v237,v63\nv_mul_f32 v238,v238,v63\nv_mul_f32 v239,v239,v63\nv_mul_f32 v240,v240,v63\n" \
"v_mul_f32 v241,v241,v63\nv_mul_f32 v242,v242,v63\nv_mul_f32 v243,v243,v63\nv_mul_f32 v244,v244,v63\nv_mul_f32 v245,v245,v63\nv_mul_f32 v246,v246,v63\nv_mul_f32 v247,v247,v63\nv_mul_f32 v248,v248,v63\n" \
"v_mul_f32 v249,v249,v63\nv_mul_f32 v250,v250,v63\nv_mul_f32 v251,v251,v63\nv_mul_f32 v252,v252,v63\nv_mul_f32 v253,v253,v63\nv_mul_f32 v254,v254,v63\nv_mul_f32 v255,v255,v63\ns_bitcmp1_b32 s64,6\n" \
"s_cbranch_scc1 Lk79_%=\nv_sub_f32 v120,v120,v62\nv_sub_f32 v121,v121,v62\nv_sub_f32 v122,v122,v62\nv_sub_f32 v123,v123,v62\nv_sub_f32 v124,v124,v62\nv_sub_f32 v125,v125,v62\nv_sub_f32 v126,v126,v62\n" \
"v_sub_f32 v127,v127,v62\nLk79_%=:\ns_nop 7\nLSR0_%=:\ns_cmp_eq_u32 s64,0\ns_cbranch_scc1 Lcr4_%=\ns_cmp_eq_u32 s64,1\ns_cbranch_scc1 Lcr8_%=\n" \
"s_cmp_eq_u32 s64,2\ns_cbranch_scc1 Lcr12_%=\ns_cmp_eq_u32 s64,3\ns_cbranch_scc1 Lcr40_%=\ns_cmp_eq_u32 s64,4\ns_cbranch_scc1 Lcr44_%=\ns_cmp_eq_u32 s64,5\ns_cbranch_scc1 Lcr48_%=\n" \
"s_cmp_eq_u32 s64,6\ns_cbranch_scc1 Lcr52_%=\ns_cmp_eq_u32 s64,7\ns_cbranch_scc1 Lcr56_%=\ns_cmp_eq_u32 s64,8\ns_cbranch_scc1 Lcr60_%=\ns_branch Lcr64_%=\nLSLOW1_%=:\n" \
"s_cmp_eq_u32 s75,1\ns_cbranch_scc1 LSR1_%=\ns_lshl_b32 s9,s9,14\ns_add_u32 s9,s9,s71\ns_nop 7\ns_waitcnt lgkmcnt(0)\nbuffer_load_dwordx4 a[240:243],v4,s[36:39],s9 offen offset:0 sc1\nbuffer_load_dwordx4 a[244:247],v4,s[36:39],s9 offen offset:2048 sc1\n" \
"s_add_u32 s9,s9,4096\nbuffer_load_dwordx4 a[248:251],v4,s[36:39],s9 offen offset:0 sc1\nbuffer_load_dwordx4 a[252:255],v4,s[36:39],s9 offen offset:2048 sc1\ns_sub_u32 s9,s9,3072\ns_waitcnt vmcnt(0)\ns_cmp_gt_u32 s8,0\ns_cbranch_scc1 Lk81_%=\nM v[96:99],a[240:243],a[128:131],v[32:35]\n" \
"M v[96:99],a[244:247],a[132:135],v[96:99]\nM v[96:99],a[248:251],a[136:139],v[96:99]\nM v[96:99],a[252:255],a[140:143],v[96:99]\nLk81_%=:\ns_cmp_gt_u32 s8,1\ns_cbranch_scc1 Lk82_%=\nM v[104:107],a[240:243],a[144:147],v[36:39]\nM v[104:107],a[244:247],a[148:151],v[104:107]\n" \
"M v[104:107],a[248:251],a[152:155],v[104:107]\nM v[104:107],a[252:255],a[156:159],v[104:107]\nLk82_%=:\ns_cmp_gt_u32 s8,2\ns_cbranch_scc1 Lk83_%=\nM v[112:115],a[240:243],a[160:163],v[40:43]\nM v[112:115],a[244:247],a[164:167],v[112:115]\nM v[112:115],a[248:251],a[168:171],v[112:115]\n" \
"M v[112:115],a[252:255],a[172:175],v[112:115]\nLk83_%=:\ns_cmp_gt_u32 s8,3\ns_cbranch_scc1 Lk84_%=\nM v[120:123],a[240:243],a[176:179],v[44:47]\nM v[120:123],a[244:247],a[180:183],v[120:123]\nM v[120:123],a[248:251],a[184:187],v[120:123]\nM v[120:123],a[252:255],a[188:191],v[120:123]\n" \
"Lk84_%=:\ns_nop 7\nbuffer_load_dwordx4 a[240:243],v4,s[36:39],s9 offen offset:0 sc1\nbuffer_load_dwordx4 a[244:247],v4,s[36:39],s9 offen offset:2048 sc1\ns_add_u32 s9,s9,4096\nbuffer_load_dwordx4 a[248:251],v4,s[36:39],s9 offen offset:0 sc1\nbuffer_load_dwordx4 a[252:255],v4,s[36:39],s9 offen offset:2048 sc1\ns_sub_u32 s9,s9,3072\n" \
"s_waitcnt vmcnt(0)\ns_cmp_gt_u32 s8,0\ns_cbranch_scc1 Lk85_%=\nM v[100:103],a[240:243],a[128:131],v[32:35]\nM v[100:103],a[244:247],a[132:135],v[100:103]\nM v[100:103],a[248:251],a[136:139],v[100:103]\nM v[100:103],a[252:255],a[140:143],v[100:103]\nLk85_%=:\n" \
"s_cmp_gt_u32 s8,1\ns_cbranch_scc1 Lk86_%=\nM v[108:111],a[240:243],a[144:147],v[36:39]\nM v[108:111],a[244:247],a[148:151],v[108:111]\nM v[108:111],a[248:251],a[152:155],v[108:111]\nM v[108:111],a[252:255],a[156:159],v[108:111]\nLk86_%=:\ns_cmp_gt_u32 s8,2\n" \
"s_cbranch_scc1 Lk87_%=\nM v[116:119],a[240:243],a[160:163],v[40:43]\nM v[116:119],a[244:247],a[164:167],v[116:119]\nM v[116:119],a[248:251],a[168:171],v[116:119]\nM v[116:119],a[252:255],a[172:175],v[116:119]\nLk87_%=:\ns_cmp_gt_u32 s8,3\ns_cbranch_scc1 Lk88_%=\n" \
"M v[124:127],a[240:243],a[176:179],v[44:47]\nM v[124:127],a[244:247],a[180:183],v[124:127]\nM v[124:127],a[248:251],a[184:187],v[124:127]\nM v[124:127],a[252:255],a[188:191],v[124:127]\nLk88_%=:\ns_nop 7\ns_cmp_gt_u32 s8,0\ns_cbranch_scc0 Lk90_%=\n" \
"v_mov_b32 v24,v16\ns_cmp_eq_u32 s8,1\ns_cbranch_scc0 Lk89_%=\nM v[24:27],a[224:227],v[96:99],v[24:27]\ns_nop 7\ns_branch Lk89_%=\nLk90_%=:\nv_max3_f32 v62,v96,v97,v98\n" \
"v_max3_f32 v63,v99,v100,v101\nv_max3_f32 v62,v62,v102,v103\nv_max3_f32 v62,v62,v63,0\nv_mov_b32 v63,v62\ns_nop 1\nv_permlane32_swap_b32 v62,v63\nv_max_f32 v62,v62,v63\nv_mov_b32 v63,v62\n" \
"s_nop 1\nv_permlane16_swap_b32 v62,v63\nv_max_f32 v62,v62,v63\nv_exp_f32 v63,-v62\nv_sub_f32 v96,v96,v62\nv_sub_f32 v97,v97,v62\nv_sub_f32 v98,v98,v62\nv_sub_f32 v99,v99,v62\n" \
"v_sub_f32 v100,v100,v62\nv_sub_f32 v101,v101,v62\nv_sub_f32 v102,v102,v62\nv_sub_f32 v103,v103,v62\nv_exp_f32 v96,v96\nv_exp_f32 v97,v97\nv_exp_f32 v98,v98\nv_exp_f32 v99,v99\n" \
"v_exp_f32 v100,v100\nv_exp_f32 v101,v101\nv_exp_f32 v102,v102\nv_exp_f32 v103,v103\nv_mul_f32 v24,v16,v63\nC v96,v96,v97\nC v97,v98,v99\nC v98,v100,v101\n" \
"C v99,v102,v103\nv_sub_f32 v32,v32,v62\nv_sub_f32 v33,v33,v62\nv_sub_f32 v34,v34,v62\nv_sub_f32 v35,v35,v62\nM v[24:27],a[224:227],v[96:99],v[24:27]\nv_mul_f32 v128,v128,v63\nv_mul_f32 v129,v129,v63\n" \
"v_mul_f32 v130,v130,v63\nv_mul_f32 v131,v131,v63\nv_mul_f32 v132,v132,v63\nv_mul_f32 v133,v133,v63\nv_mul_f32 v134,v134,v63\nv_mul_f32 v135,v135,v63\nv_mul_f32 v136,v136,v63\nv_mul_f32 v137,v137,v63\n" \
"v_mul_f32 v138,v138,v63\nv_mul_f32 v139,v139,v63\nv_mul_f32 v140,v140,v63\nv_mul_f32 v141,v141,v63\nv_mul_f32 v142,v142,v63\nv_mul_f32 v143,v143,v63\nv_mul_f32 v144,v144,v63\nv_mul_f32 v145,v145,v63\n" \
"v_mul_f32 v146,v146,v63\nv_mul_f32 v147,v147,v63\nv_mul_f32 v148,v148,v63\nv_mul_f32 v149,v149,v63\nv_mul_f32 v150,v150,v63\nv_mul_f32 v151,v151,v63\nv_mul_f32 v152,v152,v63\nv_mul_f32 v153,v153,v63\n" \
"v_mul_f32 v154,v154,v63\nv_mul_f32 v155,v155,v63\nv_mul_f32 v156,v156,v63\nv_mul_f32 v157,v157,v63\nv_mul_f32 v158,v158,v63\nv_mul_f32 v159,v159,v63\ns_bitcmp1_b32 s64,6\ns_cbranch_scc1 Lk89_%=\n" \
"v_sub_f32 v64,v64,v62\nv_sub_f32 v65,v65,v62\nv_sub_f32 v66,v66,v62\nv_sub_f32 v67,v67,v62\nv_sub_f32 v68,v68,v62\nv_sub_f32 v69,v69,v62\nv_sub_f32 v70,v70,v62\nv_sub_f32 v71,v71,v62\n" \
"Lk89_%=:\ns_cmp_gt_u32 s8,1\ns_cbranch_scc0 Lk92_%=\nv_mov_b32 v25,v17\ns_cmp_eq_u32 s8,2\ns_cbranch_scc0 Lk91_%=\nM v[24:27],a[228:231],v[104:107],v[24:27]\ns_nop 7\n" \
"s_branch Lk91_%=\nLk92_%=:\nv_max3_f32 v62,v104,v105,v106\nv_max3_f32 v63,v107,v108,v109\nv_max3_f32 v62,v62,v110,v111\nv_max3_f32 v62,v62,v63,0\nv_mov_b32 v63,v62\ns_nop 1\n" \
"v_permlane32_swap_b32 v62,v63\nv_max_f32 v62,v62,v63\nv_mov_b32 v63,v62\ns_nop 1\nv_permlane16_swap_b32 v62,v63\nv_max_f32 v62,v62,v63\nv_exp_f32 v63,-v62\nv_sub_f32 v104,v104,v62\n" \
"v_sub_f32 v105,v105,v62\nv_sub_f32 v106,v106,v62\nv_sub_f32 v107,v107,v62\nv_sub_f32 v108,v108,v62\nv_sub_f32 v109,v109,v62\nv_sub_f32 v110,v110,v62\nv_sub_f32 v111,v111,v62\nv_exp_f32 v104,v104\n" \
"v_exp_f32 v105,v105\nv_exp_f32 v106,v106\nv_exp_f32 v107,v107\nv_exp_f32 v108,v108\nv_exp_f32 v109,v109\nv_exp_f32 v110,v110\nv_exp_f32 v111,v111\nv_mul_f32 v25,v17,v63\n" \
"C v104,v104,v105\nC v105,v106,v107\nC v106,v108,v109\nC v107,v110,v111\nv_sub_f32 v36,v36,v62\nv_sub_f32 v37,v37,v62\nv_sub_f32 v38,v38,v62\nv_sub_f32 v39,v39,v62\n" \
"M v[24:27],a[228:231],v[104:107],v[24:27]\nv_mul_f32 v160,v160,v63\nv_mul_f32 v161,v161,v63\nv_mul_f32 v162,v162,v63\nv_mul_f32 v163,v163,v63\nv_mul_f32 v164,v164,v63\nv_mul_f32 v165,v165,v63\nv_mul_f32 v166,v166,v63\n" \
"v_mul_f32 v167,v167,v63\nv_mul_f32 v168,v168,v63\nv_mul_f32 v169,v169,v63\nv_mul_f32 v170,v170,v63\nv_mul_f32 v171,v171,v63\nv_mul_f32 v172,v172,v63\nv_mul_f32 v173,v173,v63\nv_mul_f32 v174,v174,v63\n" \
"v_mul_f32 v175,v175,v63\nv_mul_f32 v176,v176,v63\nv_mul_f32 v177,v177,v63\nv_mul_f32 v178,v178,v63\nv_mul_f32 v179,v179,v63\nv_mul_f32 v180,v180,v63\nv_mul_f32 v181,v181,v63\nv_mul_f32 v182,v182,v63\n" \
"v_mul_f32 v183,v183,v63\nv_mul_f32 v184,v184,v63\nv_mul_f32 v185,v185,v63\nv_mul_f32 v186,v186,v63\nv_mul_f32 v187,v187,v63\nv_mul_f32 v188,v188,v63\nv_mul_f32 v189,v189,v63\nv_mul_f32 v190,v190,v63\n" \
"v_mul_f32 v191,v191,v63\ns_bitcmp1_b32 s64,6\ns_cbranch_scc1 Lk91_%=\nv_sub_f32 v72,v72,v62\nv_sub_f32 v73,v73,v62\nv_sub_f32 v74,v74,v62\nv_sub_f32 v75,v75,v62\nv_sub_f32 v76,v76,v62\n" \
"v_sub_f32 v77,v77,v62\nv_sub_f32 v78,v78,v62\nv_sub_f32 v79,v79,v62\nLk91_%=:\ns_cmp_gt_u32 s8,2\ns_cbranch_scc0 Lk94_%=\nv_mov_b32 v26,v18\ns_cmp_eq_u32 s8,3\n" \
"s_cbranch_scc0 Lk93_%=\nM v[24:27],a[232:235],v[112:115],v[24:27]\ns_nop 7\ns_branch Lk93_%=\nLk94_%=:\nv_max3_f32 v62,v112,v113,v114\nv_max3_f32 v63,v115,v116,v117\nv_max3_f32 v62,v62,v118,v119\n" \
"v_max3_f32 v62,v62,v63,0\nv_mov_b32 v63,v62\ns_nop 1\nv_permlane32_swap_b32 v62,v63\nv_max_f32 v62,v62,v63\nv_mov_b32 v63,v62\ns_nop 1\nv_permlane16_swap_b32 v62,v63\n" \
"v_max_f32 v62,v62,v63\nv_exp_f32 v63,-v62\nv_sub_f32 v112,v112,v62\nv_sub_f32 v113,v113,v62\nv_sub_f32 v114,v114,v62\nv_sub_f32 v115,v115,v62\nv_sub_f32 v116,v116,v62\nv_sub_f32 v117,v117,v62\n" \
"v_sub_f32 v118,v118,v62\nv_sub_f32 v119,v119,v62\nv_exp_f32 v112,v112\nv_exp_f32 v113,v113\nv_exp_f32 v114,v114\nv_exp_f32 v115,v115\nv_exp_f32 v116,v116\nv_exp_f32 v117,v117\n" \
"v_exp_f32 v118,v118\nv_exp_f32 v119,v119\nv_mul_f32 v26,v18,v63\nC v112,v112,v113\nC v113,v114,v115\nC v114,v116,v117\nC v115,v118,v119\nv_sub_f32 v40,v40,v62\n" \
"v_sub_f32 v41,v41,v62\nv_sub_f32 v42,v42,v62\nv_sub_f32 v43,v43,v62\nM v[24:27],a[232:235],v[112:115],v[24:27]\nv_mul_f32 v192,v192,v63\nv_mul_f32 v193,v193,v63\nv_mul_f32 v194,v194,v63\nv_mul_f32 v195,v195,v63\n" \
"v_mul_f32 v196,v196,v63\nv_mul_f32 v197,v197,v63\nv_mul_f32 v198,v198,v63\nv_mul_f32 v199,v199,v63\nv_mul_f32 v200,v200,v63\nv_mul_f32 v201,v201,v63\nv_mul_f32 v202,v202,v63\nv_mul_f32 v203,v203,v63\n" \
"v_mul_f32 v204,v204,v63\nv_mul_f32 v205,v205,v63\nv_mul_f32 v206,v206,v63\nv_mul_f32 v207,v207,v63\nv_mul_f32 v208,v208,v63\nv_mul_f32 v209,v209,v63\nv_mul_f32 v210,v210,v63\nv_mul_f32 v211,v211,v63\n" \
"v_mul_f32 v212,v212,v63\nv_mul_f32 v213,v213,v63\nv_mul_f32 v214,v214,v63\nv_mul_f32 v215,v215,v63\nv_mul_f32 v216,v216,v63\nv_mul_f32 v217,v217,v63\nv_mul_f32 v218,v218,v63\nv_mul_f32 v219,v219,v63\n" \
"v_mul_f32 v220,v220,v63\nv_mul_f32 v221,v221,v63\nv_mul_f32 v222,v222,v63\nv_mul_f32 v223,v223,v63\ns_bitcmp1_b32 s64,6\ns_cbranch_scc1 Lk93_%=\nv_sub_f32 v80,v80,v62\nv_sub_f32 v81,v81,v62\n" \
"v_sub_f32 v82,v82,v62\nv_sub_f32 v83,v83,v62\nv_sub_f32 v84,v84,v62\nv_sub_f32 v85,v85,v62\nv_sub_f32 v86,v86,v62\nv_sub_f32 v87,v87,v62\nLk93_%=:\ns_cmp_gt_u32 s8,3\n" \
"s_cbranch_scc0 Lk96_%=\nv_mov_b32 v27,v19\ns_cmp_eq_u32 s8,4\ns_cbranch_scc0 Lk95_%=\nM v[24:27],a[236:239],v[120:123],v[24:27]\ns_nop 7\ns_branch Lk95_%=\nLk96_%=:\n" \
"v_max3_f32 v62,v120,v121,v122\nv_max3_f32 v63,v123,v124,v125\nv_max3_f32 v62,v62,v126,v127\nv_max3_f32 v62,v62,v63,0\nv_mov_b32 v63,v62\ns_nop 1\nv_permlane32_swap_b32 v62,v63\nv_max_f32 v62,v62,v63\n" \
"v_mov_b32 v63,v62\ns_nop 1\nv_permlane16_swap_b32 v62,v63\nv_max_f32 v62,v62,v63\nv_exp_f32 v63,-v62\nv_sub_f32 v120,v120,v62\nv_sub_f32 v121,v121,v62\nv_sub_f32 v122,v122,v62\n" \
"v_sub_f32 v123,v123,v62\nv_sub_f32 v124,v124,v62\nv_sub_f32 v125,v125,v62\nv_sub_f32 v126,v126,v62\nv_sub_f32 v127,v127,v62\nv_exp_f32 v120,v120\nv_exp_f32 v121,v121\nv_exp_f32 v122,v122\n" \
"v_exp_f32 v123,v123\nv_exp_f32 v124,v124\nv_exp_f32 v125,v125\nv_exp_f32 v126,v126\nv_exp_f32 v127,v127\nv_mul_f32 v27,v19,v63\nC v120,v120,v121\nC v121,v122,v123\n" \
"C v122,v124,v125\nC v123,v126,v127\nv_sub_f32 v44,v44,v62\nv_sub_f32 v45,v45,v62\nv_sub_f32 v46,v46,v62\nv_sub_f32 v47,v47,v62\nM v[24:27],a[236:239],v[120:123],v[24:27]\nv_mul_f32 v224,v224,v63\n" \
"v_mul_f32 v225,v225,v63\nv_mul_f32 v226,v226,v63\nv_mul_f32 v227,v227,v63\nv_mul_f32 v228,v228,v63\nv_mul_f32 v229,v229,v63\nv_mul_f32 v230,v230,v63\nv_mul_f32 v231,v231,v63\nv_mul_f32 v232,v232,v63\n" \
"v_mul_f32 v233,v233,v63\nv_mul_f32 v234,v234,v63\nv_mul_f32 v235,v235,v63\nv_mul_f32 v236,v236,v63\nv_mul_f32 v237,v237,v63\nv_mul_f32 v238,v238,v63\nv_mul_f32 v239,v239,v63\nv_mul_f32 v240,v240,v63\n" \
"v_mul_f32 v241,v241,v63\nv_mul_f32 v242,v242,v63\nv_mul_f32 v243,v243,v63\nv_mul_f32 v244,v244,v63\nv_mul_f32 v245,v245,v63\nv_mul_f32 v246,v246,v63\nv_mul_f32 v247,v247,v63\nv_mul_f32 v248,v248,v63\n" \
"v_mul_f32 v249,v249,v63\nv_mul_f32 v250,v250,v63\nv_mul_f32 v251,v251,v63\nv_mul_f32 v252,v252,v63\nv_mul_f32 v253,v253,v63\nv_mul_f32 v254,v254,v63\nv_mul_f32 v255,v255,v63\ns_bitcmp1_b32 s64,6\n" \
"s_cbranch_scc1 Lk95_%=\nv_sub_f32 v88,v88,v62\nv_sub_f32 v89,v89,v62\nv_sub_f32 v90,v90,v62\nv_sub_f32 v91,v91,v62\nv_sub_f32 v92,v92,v62\nv_sub_f32 v93,v93,v62\nv_sub_f32 v94,v94,v62\n" \
"v_sub_f32 v95,v95,v62\nLk95_%=:\ns_nop 7\nLSR1_%=:\ns_cmp_eq_u32 s64,0\ns_cbranch_scc1 Lcr2_%=\ns_cmp_eq_u32 s64,1\ns_cbranch_scc1 Lcr6_%=\n" \
"s_cmp_eq_u32 s64,2\ns_cbranch_scc1 Lcr10_%=\ns_cmp_eq_u32 s64,3\ns_cbranch_scc1 Lcr14_%=\ns_cmp_eq_u32 s64,4\ns_cbranch_scc1 Lcr42_%=\ns_cmp_eq_u32 s64,5\ns_cbranch_scc1 Lcr46_%=\n" \
"s_cmp_eq_u32 s64,6\ns_cbranch_scc1 Lcr50_%=\ns_cmp_eq_u32 s64,7\ns_cbranch_scc1 Lcr54_%=\ns_cmp_eq_u32 s64,8\ns_cbranch_scc1 Lcr58_%=\ns_branch Lcr62_%=\nLPU_%=:\n" \
"s_mov_b32 s25,0\nLPUL_%=:\ns_sleep 2\nbuffer_load_dwordx2 v[58:59],v57,s[36:39],0 offen sc1\ns_add_u32 s25,s25,1\ns_waitcnt vmcnt(0)\nv_cmp_gt_u64 s[98:99],s[14:15],v[58:59]\nv_cmp_le_u64 s[18:19],s[16:17],v[58:59]\n" \
"s_cmp_gt_u32 s25,262144\ns_cbranch_scc1 LPUR_%=\ns_or_b64 s[98:99],s[98:99],s[18:19]\ns_cbranch_scc1 LPUL_%=\ns_branch LPUR_%=\nLPK_%=:\ns_mov_b32 s25,0\nLPKL_%=:\n" \
"s_sleep 2\nbuffer_load_dwordx2 v[58:59],v57,s[36:39],0 offen sc1\ns_add_u32 s25,s25,1\ns_waitcnt vmcnt(0)\nv_cmp_gt_u64 s[98:99],s[14:15],v[58:59]\nv_cmp_le_u64 s[18:19],s[16:17],v[58:59]\ns_cmp_gt_u32 s25,262144\ns_cbranch_scc1 LPKR_%=\n" \
"s_or_b64 s[98:99],s[98:99],s[18:19]\ns_cbranch_scc1 LPKL_%=\ns_branch LPKR_%=\nLPZ_%=:\ns_mov_b32 s25,0\nLPZL_%=:\ns_sleep 2\nbuffer_load_dwordx2 v[10:11],v5,s[36:39],0 offen sc1\n" \
"s_add_u32 s25,s25,1\ns_waitcnt vmcnt(0)\nv_cmp_gt_u64 s[98:99],s[14:15],v[10:11]\nv_cmp_le_u64 s[18:19],s[16:17],v[10:11]\ns_cmp_gt_u32 s25,262144\ns_cbranch_scc1 LPZR_%=\ns_or_b64 s[98:99],s[98:99],s[18:19]\ns_cbranch_scc1 LPZL_%=\n" \
"s_branch LPZR_%=\nLPE_%=:\ns_mov_b32 s25,0\nLPEL_%=:\ns_sleep 2\nbuffer_load_dwordx2 v[10:11],v5,s[36:39],0 offen sc1\ns_add_u32 s25,s25,1\ns_waitcnt vmcnt(0)\n" \
"v_cmp_gt_u64 s[98:99],s[14:15],v[10:11]\nv_cmp_le_u64 s[18:19],s[16:17],v[10:11]\ns_cmp_gt_u32 s25,262144\ns_cbranch_scc1 LPER_%=\ns_or_b64 s[98:99],s[98:99],s[18:19]\ns_and_b32 s98,s98,0xffff\ns_cbranch_scc1 LPEL_%=\ns_branch LPER_%=\n" \
"LPF_%=:\ns_mov_b32 s25,0\nLPFL_%=:\ns_sleep 2\nbuffer_load_dwordx2 v[10:11],v5,s[36:39],0 offen sc1\ns_add_u32 s25,s25,1\ns_waitcnt vmcnt(0)\nv_cmp_gt_u64 s[98:99],s[14:15],v[10:11]\n" \
"v_cmp_le_u64 s[18:19],s[16:17],v[10:11]\ns_cmp_gt_u32 s25,262144\ns_cbranch_scc1 LPFR_%=\ns_or_b64 s[98:99],s[98:99],s[18:19]\ns_and_b32 s98,s98,0xff0000\ns_cbranch_scc1 LPFL_%=\ns_branch LPFR_%=\nLPM_%=:\n" \
"s_mov_b32 s25,0\nLPML_%=:\ns_sleep 2\nbuffer_load_dwordx2 v[10:11],v5,s[36:39],0 offen sc1\ns_add_u32 s25,s25,1\ns_waitcnt vmcnt(0)\nv_cmp_gt_u64 s[98:99],s[14:15],v[10:11]\nv_cmp_le_u64 s[18:19],s[16:17],v[10:11]\n" \
"s_cmp_gt_u32 s25,262144\ns_cbranch_scc1 LPMR_%=\ns_or_b64 s[98:99],s[98:99],s[18:19]\ns_and_b32 s98,s98,0xff000000\ns_cbranch_scc1 LPML_%=\ns_branch LPMR_%=\nLPL_%=:\ns_mov_b32 s25,0\n" \
"LPLL_%=:\ns_sleep 2\nbuffer_load_dwordx2 v[10:11],v58,s[36:39],0 offen sc1\ns_add_u32 s25,s25,1\ns_waitcnt vmcnt(0)\nv_cmp_gt_u64 s[98:99],s[14:15],v[10:11]\nv_cmp_le_u64 s[18:19],s[16:17],v[10:11]\ns_cmp_gt_u32 s25,262144\n" \
"s_cbranch_scc1 LPLR_%=\ns_or_b64 s[98:99],s[98:99],s[18:19]\ns_cbranch_scc1 LPLL_%=\ns_branch LPLR_%=\nLPP_%=:\ns_mov_b32 s25,0\nLPPL_%=:\ns_sleep 2\n" \
"buffer_load_dwordx2 v[10:11],v190,s[36:39],0 offen sc1\ns_add_u32 s25,s25,1\ns_waitcnt vmcnt(0)\nv_cmp_gt_u64 s[98:99],s[14:15],v[10:11]\nv_cmp_le_u64 s[18:19],s[16:17],v[10:11]\ns_cmp_gt_u32 s25,262144\ns_cbranch_scc1 LPPR_%=\ns_or_b64 s[98:99],s[98:99],s[18:19]\n" \
"s_and_b32 s98,s98,0x7\ns_cbranch_scc1 LPPL_%=\ns_branch LPPR_%=\nLIN_%=:\ns_mov_b32 s25,0\nLINL_%=:\ns_sleep 1\ns_add_u32 s57,s49,4096\n" \
"buffer_load_dwordx4 v[176:179],v4,s[36:39],s57 offen offset:2048 sc1\nbuffer_load_dwordx4 v[180:183],v4,s[36:39],s57 offen offset:3072 sc1\ns_add_u32 s57,s49,8192\nbuffer_load_dwordx4 v[32:35],v4,s[36:39],s57 offen offset:0 sc1\nbuffer_load_dwordx4 v[36:39],v4,s[36:39],s57 offen offset:1024 sc1\ns_add_u32 s25,s25,1\ns_waitcnt vmcnt(0)\nv_max_u32 v10,v176,v177\n" \
"v_max3_u32 v10,v10,v178,v179\nv_max3_u32 v10,v10,v180,v181\nv_max3_u32 v10,v10,v182,v183\nv_max3_u32 v10,v10,v32,v33\nv_max3_u32 v10,v10,v34,v35\nv_max3_u32 v10,v10,v36,v37\nv_max3_u32 v10,v10,v38,v39\nv_cmp_eq_u32 vcc,-1,v10\n" \
"s_cmp_gt_u32 s25,262144\ns_cbranch_scc1 LINR_%=\ns_cbranch_vccnz LINL_%=\ns_branch LINR_%=\nLIO_%=:\ns_mov_b32 s25,0\nLIOL_%=:\ns_sleep 1\n" \
"s_add_u32 s57,s49,8192\nbuffer_load_dwordx4 v[40:43],v4,s[36:39],s57 offen offset:2048 sc1\nbuffer_load_dwordx4 v[44:47],v4,s[36:39],s57 offen offset:3072 sc1\ns_add_u32 s25,s25,1\ns_waitcnt vmcnt(0)\nv_max_u32 v10,v40,v41\nv_max3_u32 v10,v10,v42,v43\nv_max3_u32 v10,v10,v44,v45\n" \
"v_max3_u32 v10,v10,v46,v47\nv_cmp_eq_u32 vcc,-1,v10\ns_cmp_gt_u32 s25,262144\ns_cbranch_scc1 LIOR_%=\ns_cbranch_vccnz LIOL_%=\ns_branch LIOR_%=\nLEND_%=:\ns_waitcnt vmcnt(0) lgkmcnt(0)\n" \
".purgem M\n.purgem C\n" \
""
constexpr int Q_LDS = 163840;
__global__ __launch_bounds__(256, 1) __attribute__((aligned(4096))) void fused_kernel(const void* x, const void* k, const void* v, const void* shk, const void* ssmk, const void* ssmD, const void* ckv,
                                                       const void* wq, const void* wuk, const void* wuv, const void* wo, const void* gw, void* out, void* ws) {
    const int w = __builtin_amdgcn_readfirstlane(threadIdx.x >> 6);
    const unsigned long long ka = (unsigned long long)__builtin_amdgcn_kernarg_segment_ptr();
    asm volatile(QKV_ASM_TEXT :: "s"(ka), "s"(blockIdx.x), "s"(w) : QKV_ASM_CLOBBERS);
}

extern "C" void kernel_launch(void* const* d_in, const int* in_sizes, int n_in, void* d_out, int out_size, void* d_ws, size_t ws_size,
                              hipStream_t stream) {
    (void)hipFuncSetAttribute((const void*)fused_kernel, hipFuncAttributeMaxDynamicSharedMemorySize, Q_LDS);
    fused_kernel<<<256, 256, Q_LDS, stream>>>(d_in[0], d_in[1], d_in[2], d_in[3], d_in[4], d_in[5], d_in[7], d_in[8], d_in[9], d_in[10], d_in[11], d_in[12], d_out, d_ws);
}
```

```cpp
#include <hip/hip_runtime.h>
#include <stdint.h>
#define QKV_ASM_CLOBBERS "v0", "v1", "v2", "v3", "v4", "v5", "v6", "v7", "v8", "v9", "v10", "v11", "v12", "v13", "v14", "v15", "v16", "v17", "v18", "v19", "v20", "v21", "v22", "v23", "v24", "v25", "v26", "v27", "v28", "v29", "v30", "v31", "v32", "v33", "v34", "v35", "v36", "v37", "v38", "v39", "v40", "v41", "v42", "v43", "v44", "v45", "v46", "v47", "v48", "v49", "v50", "v51", "v52", "v53", "v54", "v55", "v56", "v57", "v58", "v59", "v60", "v61", "v62", "v63", "v64", "v65", "v66", "v67", "v68", "v69", "v70", "v71", "v72", "v73", "v74", "v75", "v76", "v77", "v78", "v79", "v80", "v81", "v82", "v83", "v84", "v85", "v86", "v87", "v88", "v89", "v90", "v91", "v92", "v93", "v94", "v95", "v96", "v97", "v98", "v99", "v100", "v101", "v102", "v103", "v104", "v105", "v106", "v107", "v108", "v109", "v110", "v111", "v112", "v113", "v114", "v115", "v116", "v117", "v118", "v119", "v120", "v121", "v122", "v123", "v124", "v125", "v126", "v127", "v128", "v129", "v130", "v131", "v132", "v133", "v134", "v135", "v136", "v137", "v138", "v139", "v140", "v141", "v142", "v143", "v144", "v145", "v146", "v147", "v148", "v149", "v150", "v151", "v152", "v153", "v154", "v155", "v156", "v157", "v158", "v159", "v160", "v161", "v162", "v163", "v164", "v165", "v166", "v167", "v168", "v169", "v170", "v171", "v172", "v173", "v174", "v175", "v176", "v177", "v178", "v179", "v180", "v181", "v182", "v183", "v184", "v185", "v186", "v187", "v188", "v189", "v190", "v191", "v192", "v193", "v194", "v195", "v196", "v197", "v198", "v199", "v200", "v201", "v202", "v203", "v204", "v205", "v206", "v207", "v208", "v209", "v210", "v211", "v212", "v213", "v214", "v215", "v216", "v217", "v218", "v219", "v220", "v221", "v222", "v223", "v224", "v225", "v226", "v227", "v228", "v229", "v230", "v231", "v232", "v233", "v234", "v235", "v236", "v237", "v238", "v239", "v240", "v241", "v242", "v243", "v244", "v245", "v246", "v247", "v248", "v249", "v250", "v251", "v252", "v253", "v254", "v255", "a0", "a1", "a2", "a3", "a4", "a5", "a6", "a7", "a8", "a9", "a10", "a11", "a12", "a13", "a14", "a15", "a16", "a17", "a18", "a19", "a20", "a21", "a22", "a23", "a24", "a25", "a26", "a27", "a28", "a29", "a30", "a31", "a32", "a33", "a34", "a35", "a36", "a37", "a38", "a39", "a40", "a41", "a42", "a43", "a44", "a45", "a46", "a47", "a48", "a49", "a50", "a51", "a52", "a53", "a54", "a55", "a56", "a57", "a58", "a59", "a60", "a61", "a62", "a63", "a64", "a65", "a66", "a67", "a68", "a69", "a70", "a71", "a72", "a73", "a74", "a75", "a76", "a77", "a78", "a79", "a80", "a81", "a82", "a83", "a84", "a85", "a86", "a87", "a88", "a89", "a90", "a91", "a92", "a93", "a94", "a95", "a96", "a97", "a98", "a99", "a100", "a101", "a102", "a103", "a104", "a105", "a106", "a107", "a108", "a109", "a110", "a111", "a112", "a113", "a114", "a115", "a116", "a117", "a118", "a119", "a120", "a121", "a122", "a123", "a124", "a125", "a126", "a127", "a128", "a129", "a130", "a131", "a132", "a133", "a134", "a135", "a136", "a137", "a138", "a139", "a140", "a141", "a142", "a143", "a144", "a145", "a146", "a147", "a148", "a149", "a150", "a151", "a152", "a153", "a154", "a155", "a156", "a157", "a158", "a159", "a160", "a161", "a162", "a163", "a164", "a165", "a166", "a167", "a168", "a169", "a170", "a171", "a172", "a173", "a174", "a175", "a176", "a177", "a178", "a179", "a180", "a181", "a182", "a183", "a184", "a185", "a186", "a187", "a188", "a189", "a190", "a191", "a192", "a193", "a194", "a195", "a196", "a197", "a198", "a199", "a200", "a201", "a202", "a203", "a204", "a205", "a206", "a207", "a208", "a209", "a210", "a211", "a212", "a213", "a214", "a215", "a216", "a217", "a218", "a219", "a220", "a221", "a222", "a223", "a224", "a225", "a226", "a227", "a228", "a229", "a230", "a231", "a232", "a233", "a234", "a235", "a236", "a237", "a238", "a239", "a240", "a241", "a242", "a243", "a244", "a245", "a246", "a247", "a248", "a249", "a250", "a251", "a252", "a253", "a254", "a255", "s4", "s5", "s6", "s7", "s8", "s9", "s10", "s11", "s12", "s13", "s14", "s15", "s16", "s17", "s18", "s19", "s20", "s21", "s22", "s23", "s24", "s25", "s26", "s27", "s28", "s29", "s30", "s31", "s32", "s33", "s34", "s35", "s36", "s37", "s38", "s39", "s40", "s41", "s42", "s43", "s44", "s45", "s46", "s47", "s48", "s49", "s50", "s51", "s52", "s53", "s54", "s55", "s56", "s57", "s58", "s59", "s60", "s61", "s62", "s63", "s64", "s65", "s66", "s67", "s68", "s69", "s70", "s71", "s72", "s73", "s74", "s75", "s76", "s77", "s78", "s79", "s80", "s81", "s82", "s83", "s84", "s85", "s86", "s87", "s88", "s89", "s90", "s91", "s92", "s93", "s94", "s95", "s96", "s97", "s98", "s99", "s100", "s101", "vcc", "scc", "memory"
#define QKV_ASM_TEXT \
".macro M d,a,b,c\nv_mfma_f32_16x16x32_bf16 \\d,\\a,\\b,\\c\n.endm\n.macro C d,a,b\nv_cvt_pk_bf16_f32 \\d,\\a,\\b\n.endm\ns_memrealtime s[18:19]\ns_load_dwordx16 s[64:79],%0,0x0\n" \
"s_load_dwordx8 s[80:87],%0,0x40\ns_load_dwordx4 s[88:91],%0,0x60\nv_mbcnt_lo_u32_b32 v23,-1,0\nv_mbcnt_hi_u32_b32 v23,-1,v23\ns_bfe_u32 s7,%1,0x30003\ns_and_b32 s6,%1,7\ns_lshl_b32 s6,s6,2\ns_lshr_b32 s60,%1,6\n" \
"s_add_u32 s6,s6,s60\nv_lshlrev_b32 v4,4,v23\ns_getpc_b64 s[40:41]\nLCTBEG_%=:\ns_add_u32 s40,s40,LBEG_%=-LCTBEG_%=\ns_addc_u32 s41,s41,0\ns_mov_b64 s[100:101],s[40:41]\ns_and_b32 s41,s41,0xffff\n" \
"s_mov_b32 s42,LEND_%=-LBEG_%=\ns_mov_b32 s43,0x00020000\ns_lshr_b32 s9,%1,3\ns_lshl_b32 s9,s9,2\ns_add_u32 s9,s9,%2\ns_and_b32 s9,s9,31\ns_lshl_b32 s9,s9,13\nv_lshlrev_b32 v64,3,v4\n" \
"buffer_load_dword v64,v64,s[40:43],s9 offen\ns_lshl_b32 s60,%2,2\nv_lshrrev_b32 v24,4,v23\nv_add_u32 v25,s60,v24\nv_xor_b32 v25,v25,v23\nv_and_b32 v25,7,v25\ns_lshl_b32 s61,%2,3\nv_lshrrev_b32 v26,3,v23\n" \
"v_add_u32 v26,s61,v26\nv_lshlrev_b32 v26,10,v26\nv_lshl_add_u32 v13,v25,4,v26\nv_add_u32 v63,v13,v26\ns_lshl_b32 s58,s7,17\ns_sub_u32 s59,1966080,s58\nv_add_u32 v17,s59,v13\nv_add_u32 v13,s58,v13\n" \
"v_add_u32 v14,0x8000,v13\nv_add_u32 v18,0x8000,v17\nv_add_u32 v15,0x8000,v14\nv_add_u32 v19,0x8000,v18\nv_add_u32 v16,0x8000,v15\nv_add_u32 v20,0x8000,v19\nv_bfe_u32 v25,v23,2,3\nv_add_u32 v25,s61,v25\n" \
"v_lshlrev_b32 v25,13,v25\ns_lshl_b32 s61,%2,1\nv_and_b32 v26,1,v24\nv_add_u32 v26,s61,v26\nv_xor_b32 v26,v26,v23\nv_and_b32 v26,3,v26\nv_lshrrev_b32 v27,5,v23\nv_lshl_add_u32 v26,v27,2,v26\n" \
"v_lshl_add_u32 v21,v26,4,v25\ns_lshl_b32 s50,%2,10\ns_add_u32 s50,s50,65536\ns_lshl_b32 s22,%2,12\ns_lshl_b32 s51,%2,11\ns_mov_b32 s31,0x00020000\ns_mov_b32 s35,0x00020000\ns_mov_b32 s95,0x00020000\n" \
"s_mov_b32 s30,2097152\ns_lshl_b32 s98,s6,8\ns_sub_u32 s34,4194304,s98\ns_mov_b32 s94,s34\ns_sub_u32 s49,8388608,s98\ns_cmp_lt_u32 %2,2\ns_cselect_b32 s57,s58,s59\ns_lshr_b32 s57,s57,1\n" \
"s_lshl_b32 s60,s6,20\ns_add_u32 s57,s57,s60\ns_and_b32 s54,%2,1\ns_lshl_b32 s60,s54,13\ns_add_u32 s57,s57,s60\ns_add_u32 s57,s57,16777216\ns_bitcmp1_b32 %2,0\ns_cselect_b32 s20,1.0,0x3e0293ee\n" \
"s_lshl_b32 s54,s54,11\ns_lshl_b32 s24,s6,3\ns_add_u32 s24,s24,s7\ns_lshl_b32 s24,s24,3\ns_lshl_b32 s60,%2,27\ns_add_u32 s24,s24,s60\ns_waitcnt lgkmcnt(0)\ns_mov_b64 s[28:29],s[76:77]\n" \
"s_and_b32 s29,s29,0xffff\ns_add_u32 s32,s80,s98\ns_addc_u32 s33,s81,0\ns_and_b32 s33,s33,0xffff\ns_add_u32 s92,s82,s98\ns_addc_u32 s93,s83,0\ns_and_b32 s93,s93,0xffff\ns_mov_b32 s47,0\n" \
"s_add_u32 m0,s22,0\ns_add_u32 s24,s24,94375936\nbuffer_load_dwordx4 v21,s[32:35],s47 offen offset:0 lds\ns_and_b32 s8,%1,7\ns_lshl_b32 s23,s8,8\ns_add_u32 s23,s23,s60\ns_and_b32 s60,%1,0xf8\ns_add_u32 s23,s23,s60\n" \
"s_mov_b32 s46,0\ns_add_u32 m0,s50,65536\ns_add_u32 s23,s23,94380032\nbuffer_load_dwordx4 v13,s[28:31],s46 offen lds\ns_lshl_b32 s8,s8,5\ns_lshr_b32 s58,%1,3\ns_add_u32 s8,s8,s58\nv_lshrrev_b32 v23,4,v4\n" \
"v_and_b32 v24,15,v23\ns_add_u32 m0,s22,896\nv_lshrrev_b32 v25,4,v23\nbuffer_load_dwordx4 v21,s[32:35],s47 offen offset:128 lds\ns_lshr_b32 s58,%2,1\ns_lshl_b32 s58,s58,14\ns_add_u32 s58,s58,65536\nv_lshrrev_b32 v26,1,v24\n" \
"v_xor_b32 v26,v26,v25\ns_add_u32 m0,s50,69632\nv_lshlrev_b32 v26,4,v26\nbuffer_load_dwordx4 v14,s[28:31],s46 offen lds\nv_lshl_add_u32 v26,v24,7,v26\nv_add_u32 v5,s58,v26\nv_xor_b32 v6,64,v5\nv_add_u32 v7,0x10000,v5\n" \
"v_add_u32 v8,0x10000,v6\ns_add_u32 m0,s22,2048\nv_lshrrev_b32 v26,2,v24\nbuffer_load_dwordx4 v21,s[92:95],s47 offen offset:0 lds\nv_lshlrev_b32 v26,6,v26\nv_and_b32 v27,1,v23\nv_lshl_add_u32 v26,v27,3,v26\nv_bfe_u32 v27,v23,1,1\n" \
"v_lshlrev_b32 v28,1,v25\ns_add_u32 m0,s50,73728\nv_xor_b32 v27,v27,v28\nbuffer_load_dwordx4 v15,s[28:31],s46 offen lds\nv_and_b32 v27,3,v27\nv_lshl_add_u32 v26,v27,4,v26\nv_lshl_add_u32 v9,v25,12,v26\nv_add_u32 v9,s54,v9\n" \
"v_lshl_add_u32 v62,v25,11,s54\ns_add_u32 m0,s22,2944\nv_xor_b32 v10,0x110,v9\nbuffer_load_dwordx4 v21,s[92:95],s47 offen offset:128 lds\nv_xor_b32 v11,32,v9\nv_xor_b32 v12,0x130,v9\ns_and_b32 s62,%1,7\ns_lshl_b32 s62,s62,2\n" \
"s_add_u32 s62,s62,%2\ns_add_u32 m0,s50,77824\ns_lshl_b32 s62,s62,5\nbuffer_load_dwordx4 v16,s[28:31],s46 offen lds\ns_and_b32 s63,%1,0xf8\nv_bfe_u32 v24,v4,4,3\nv_lshrrev_b32 v23,7,v4\nv_lshl_add_u32 v24,v24,2,s62\n" \
"v_add_u32 v25,s63,v23\ns_mov_b32 s46,0\ns_add_u32 m0,s50,81920\nv_lshlrev_b32 v26,14,v25\nbuffer_load_dwordx4 v17,s[28:31],s46 offen lds\nv_lshl_add_u32 v59,v24,1,v26\nv_add_u32 v54,0xffffe800,v59\nv_add_u32 v55,0xfffff800,v59\n" \
"v_lshlrev_b32 v60,12,v24\nv_lshl_add_u32 v60,v25,4,v60\ns_add_u32 m0,s50,86016\nv_lshlrev_b32 v27,3,v24\nbuffer_load_dwordx4 v18,s[28:31],s46 offen lds\ns_lshl_b32 s62,%1,2\ns_add_u32 s62,s62,%2\ns_lshl_b32 s63,s62,12\n" \
"v_add_u32 v26,s63,v4\ns_lshl_b32 s62,s62,3\ns_add_u32 m0,s50,90112\ns_add_u32 s62,s62,71303168\nbuffer_load_dwordx4 v19,s[28:31],s46 offen lds\nv_and_b32 v61,4,v23\nv_add_u32 v61,s62,v61\nv_lshlrev_b32 v2,3,v4\n" \
"v_and_b32 v1,0x380,v2\nv_lshrrev_b32 v23,7,v4\ns_add_u32 m0,s50,94208\nv_lshl_add_u32 v1,v23,13,v1\nbuffer_load_dwordx4 v20,s[28:31],s46 offen lds\ns_lshr_b32 s58,%1,3\ns_lshl_b32 s58,s58,2\ns_add_u32 s58,s58,%2\n" \
"s_and_b32 s60,%1,7\ns_lshl_b32 s60,s60,10\ns_mov_b64 exec,0\ns_cmp_eq_u32 %2,0\ns_cbranch_scc1 LG0_%=\ns_cmp_eq_u32 %2,1\ns_cbranch_scc1 LG1_%=\ns_cmp_eq_u32 %2,2\n" \
"s_cbranch_scc1 LG4_%=\ns_cmp_eq_u32 %2,3\ns_cbranch_scc1 LG5_%=\nLHR0_%=:\ns_mov_b64 exec,-1\nLBEG_%=:\ns_bitcmp1_b32 %2,0\ns_cbranch_scc1 LAV_%=\n" \
"s_mov_b32 s47,262144\ns_add_u32 m0,s22,16384\ns_lshl_b32 s59,s58,14\nbuffer_load_dwordx4 v21,s[32:35],s47 offen offset:0 lds\ns_add_u32 s59,s59,s60\ns_mov_b64 s[4:5],s[90:91]\ns_lshl_b32 s99,s58,11\ns_lshl_b32 s96,s60,8\n" \
"s_add_u32 s99,s99,s96\ns_add_u32 m0,s22,17280\ns_add_u32 s96,s76,s99\ns_addc_u32 s97,s77,0\nbuffer_load_dwordx4 v21,s[32:35],s47 offen offset:128 lds\ns_add_u32 s76,s78,s59\ns_addc_u32 s77,s79,0\ns_and_b32 s59,s58,63\n" \
"s_lshl_b32 s59,s59,13\ns_add_u32 s59,s59,s60\ns_cmp_lt_u32 s58,64\ns_cselect_b64 s[84:85],s[80:81],s[82:83]\ns_add_u32 m0,s22,18432\ns_add_u32 s84,s84,s59\ns_addc_u32 s85,s85,0\nbuffer_load_dwordx4 v21,s[92:95],s47 offen offset:0 lds\n" \
"s_lshl_b32 s59,s8,2\ns_add_u32 s59,s59,%2\ns_lshl_b32 s59,s59,12\ns_add_u32 s88,s72,s59\ns_addc_u32 s89,s73,0\ns_add_u32 m0,s22,19328\ns_nop 0\nbuffer_load_dwordx4 v21,s[92:95],s47 offen offset:128 lds\n" \
"s_mov_b64 exec,0xff\nglobal_load_dword v0,v1,s[84:85]\ns_mov_b64 exec,-1\ns_add_u32 s84,s84,524288\ns_addc_u32 s85,s85,0\ns_mov_b64 exec,0xff\nglobal_load_dword v0,v1,s[84:85]\ns_mov_b64 exec,-1\n" \
"s_add_u32 s84,s84,524288\ns_addc_u32 s85,s85,0\ns_mov_b64 exec,0xff\nglobal_load_dword v0,v1,s[84:85]\ns_mov_b64 exec,-1\ns_mov_b64 exec,0xff\nglobal_load_dword v0,v2,s[96:97]\ns_mov_b64 exec,-1\n" \
"s_mov_b64 exec,0xff00\nglobal_load_dword v0,v2,s[96:97]\ns_mov_b64 exec,-1\ns_waitcnt vmcnt(9)\ns_barrier\nds_read_b64_tr_b16 a[32:33],v9 offset:0\nds_read_b64_tr_b16 a[34:35],v10 offset:0\nds_read_b64_tr_b16 a[36:37],v11 offset:0\n" \
"ds_read_b64_tr_b16 a[38:39],v12 offset:0\nds_read_b128 a[24:27],v7 offset:12288\nds_read_b128 a[28:31],v7 offset:14336\ns_waitcnt lgkmcnt(0)\nM v[128:131],a[32:35],a[24:27],0\ns_add_u32 m0,s22,32768\ns_mov_b32 s47,524288\ns_mov_b32 s46,128\n" \
"ds_read_b64_tr_b16 a[40:41],v9 offset:512\nds_read_b64_tr_b16 a[42:43],v10 offset:512\nds_read_b64_tr_b16 a[44:45],v11 offset:512\nds_read_b64_tr_b16 a[46:47],v12 offset:512\nds_read_b64_tr_b16 a[48:49],v9 offset:1024\nds_read_b64_tr_b16 a[50:51],v10 offset:1024\nds_read_b64_tr_b16 a[52:53],v11 offset:1024\nds_read_b64_tr_b16 a[54:55],v12 offset:1024\n" \
"M v[132:135],a[36:39],a[24:27],0\nbuffer_load_dwordx4 v21,s[32:35],s47 offen offset:0 lds\nds_read_b64_tr_b16 a[56:57],v9 offset:1536\nds_read_b64_tr_b16 a[58:59],v10 offset:1536\nds_read_b64_tr_b16 a[60:61],v11 offset:1536\nds_read_b64_tr_b16 a[62:63],v12 offset:1536\nM v[140:143],a[36:39],a[28:31],0\ns_add_u32 m0,s22,33664\n" \
"M v[136:139],a[32:35],a[28:31],0\ns_waitcnt lgkmcnt(8)\ns_mov_b64 s[40:41],s[66:67]\nds_read_b128 a[16:19],v7 offset:8192\nM v[152:155],a[40:43],a[28:31],0\ns_and_b32 s41,s41,0xffff\nds_read_b128 a[20:23],v7 offset:10240\nM v[156:159],a[44:47],a[28:31],0\n" \
"buffer_load_dwordx4 v21,s[32:35],s47 offen offset:128 lds\nM v[148:151],a[44:47],a[24:27],0\ns_add_u32 m0,s22,34816\nds_read_b128 a[0:3],v7 offset:0\nM v[144:147],a[40:43],a[24:27],0\ns_waitcnt lgkmcnt(3)\ns_mov_b32 s42,4194304\nds_read_b128 a[4:7],v7 offset:2048\n" \
"M v[160:163],a[48:51],a[24:27],0\ns_mov_b32 s43,0x00020000\nds_read_b128 a[8:11],v7 offset:4096\nM v[164:167],a[52:55],a[24:27],0\nbuffer_load_dwordx4 v21,s[92:95],s47 offen offset:0 lds\nM v[172:175],a[52:55],a[28:31],0\ns_add_u32 m0,s22,35712\nM v[168:171],a[48:51],a[28:31],0\n" \
"s_mov_b64 s[80:81],s[68:69]\nds_read_b128 a[12:15],v7 offset:6144\nM v[184:187],a[56:59],a[28:31],0\ns_mov_b32 s52,0\nds_read_b128 a[88:91],v8 offset:12288\nM v[188:191],a[60:63],a[28:31],0\nbuffer_load_dwordx4 v21,s[92:95],s47 offen offset:128 lds\nM v[180:183],a[60:63],a[24:27],0\n" \
"s_add_u32 m0,s50,0\nds_read_b128 a[92:95],v8 offset:14336\nM v[176:179],a[56:59],a[24:27],0\ns_waitcnt lgkmcnt(3)\ns_mov_b32 s47,786432\nds_read_b128 a[80:83],v8 offset:8192\nM v[64:67],a[32:35],a[16:19],0\ns_waitcnt vmcnt(9) lgkmcnt(9)\n" \
"ds_read_b128 a[84:87],v8 offset:10240\nM v[68:71],a[36:39],a[16:19],0\nbuffer_load_dwordx4 v13,s[28:31],s46 offen lds\ns_barrier\nM v[76:79],a[36:39],a[20:23],0\ns_add_u32 m0,s50,4096\nM v[72:75],a[32:35],a[20:23],0\ns_and_b32 s81,s81,0xffff\n" \
"ds_read_b64_tr_b16 a[96:97],v9 offset:16384\nds_read_b64_tr_b16 a[98:99],v10 offset:16384\nM v[88:91],a[40:43],a[20:23],0\ns_mov_b32 s82,4194304\nds_read_b64_tr_b16 a[100:101],v11 offset:16384\nM v[92:95],a[44:47],a[20:23],0\nbuffer_load_dwordx4 v14,s[28:31],s46 offen lds\nM v[84:87],a[44:47],a[16:19],0\n" \
"s_add_u32 m0,s50,8192\nds_read_b64_tr_b16 a[102:103],v12 offset:16384\nM v[80:83],a[40:43],a[16:19],0\ns_mov_b32 s83,0x00020000\nds_read_b64_tr_b16 a[104:105],v9 offset:16896\nM v[96:99],a[48:51],a[16:19],0\ns_lshl_b32 s60,s7,18\nds_read_b64_tr_b16 a[106:107],v10 offset:16896\n" \
"M v[100:103],a[52:55],a[16:19],0\nbuffer_load_dwordx4 v15,s[28:31],s46 offen lds\nM v[108:111],a[52:55],a[20:23],0\ns_add_u32 m0,s50,12288\nds_read_b64_tr_b16 a[108:109],v11 offset:16896\nM v[104:107],a[48:51],a[20:23],0\ns_sub_u32 s61,3932160,s60\nds_read_b64_tr_b16 a[110:111],v12 offset:16896\n" \
"M v[120:123],a[56:59],a[20:23],0\ns_add_u32 s44,s78,s98\ns_addc_u32 s45,s79,0\nds_read_b64_tr_b16 a[112:113],v9 offset:17408\nds_read_b64_tr_b16 a[114:115],v10 offset:17408\nM v[124:127],a[60:63],a[20:23],0\nbuffer_load_dwordx4 v16,s[28:31],s46 offen lds\nM v[116:119],a[60:63],a[16:19],0\n" \
"s_add_u32 m0,s50,16384\ns_mov_b32 s46,128\nM v[112:115],a[56:59],a[16:19],0\ns_and_b32 s45,s45,0xffff\nds_read_b64_tr_b16 a[116:117],v11 offset:17408\nds_read_b64_tr_b16 a[118:119],v12 offset:17408\nM v[192:195],a[32:35],a[0:3],0\ns_mov_b64 s[36:37],s[90:91]\n" \
"ds_read_b64_tr_b16 a[120:121],v9 offset:17920\nM v[196:199],a[36:39],a[0:3],0\nbuffer_load_dwordx4 v17,s[28:31],s46 offen lds\nM v[204:207],a[36:39],a[4:7],0\ns_add_u32 m0,s50,20480\nds_read_b64_tr_b16 a[122:123],v10 offset:17920\nM v[200:203],a[32:35],a[4:7],0\ns_and_b32 s37,s37,0xffff\n" \
"ds_read_b64_tr_b16 a[124:125],v11 offset:17920\nM v[216:219],a[40:43],a[4:7],0\ns_mov_b32 s38,134217728\nds_read_b64_tr_b16 a[126:127],v12 offset:17920\nM v[220:223],a[44:47],a[4:7],0\nbuffer_load_dwordx4 v18,s[28:31],s46 offen lds\nM v[212:215],a[44:47],a[0:3],0\ns_add_u32 m0,s50,24576\n" \
"ds_read_b128 a[64:67],v8 offset:0\nM v[208:211],a[40:43],a[0:3],0\ns_mov_b32 s39,0x00020000\nds_read_b128 a[68:71],v8 offset:2048\nM v[224:227],a[48:51],a[0:3],0\ns_lshr_b32 s62,%1,3\nds_read_b128 a[72:75],v8 offset:4096\nM v[228:231],a[52:55],a[0:3],0\n" \
"buffer_load_dwordx4 v19,s[28:31],s46 offen lds\nM v[236:239],a[52:55],a[4:7],0\ns_add_u32 m0,s50,28672\nM v[232:235],a[48:51],a[4:7],0\ns_lshl_b32 s62,s62,2\nds_read_b128 a[76:79],v8 offset:6144\nM v[248:251],a[56:59],a[4:7],0\ns_add_u32 s62,s62,%2\n" \
"M v[252:255],a[60:63],a[4:7],0\nbuffer_load_dwordx4 v20,s[28:31],s46 offen lds\nM v[244:247],a[60:63],a[0:3],0\ns_add_u32 m0,s22,49152\nM v[240:243],a[56:59],a[0:3],0\ns_waitcnt lgkmcnt(1)\ns_mov_b32 s46,256\nM a[128:131],a[32:35],a[8:11],0\n" \
"s_lshl_b32 s62,s62,10\nM a[132:135],a[36:39],a[8:11],0\nbuffer_load_dwordx4 v21,s[32:35],s47 offen offset:0 lds\nM a[140:143],a[36:39],a[12:15],0\ns_add_u32 m0,s22,50048\nM a[136:139],a[32:35],a[12:15],0\ns_min_u32 s62,s62,LEND_%=-LBEG_%=-1024\nM a[152:155],a[40:43],a[12:15],0\n" \
"s_add_u32 s100,s100,s62\ns_addc_u32 s101,s101,0\nM a[156:159],a[44:47],a[12:15],0\nbuffer_load_dwordx4 v21,s[32:35],s47 offen offset:128 lds\nM a[148:151],a[44:47],a[8:11],0\ns_add_u32 m0,s22,51200\nM a[144:147],a[40:43],a[8:11],0\ns_lshl_b32 s62,s8,2\n" \
"M a[160:163],a[48:51],a[8:11],0\ns_add_u32 s62,s62,%2\nM a[164:167],a[52:55],a[8:11],0\nbuffer_load_dwordx4 v21,s[92:95],s47 offen offset:0 lds\nM a[172:175],a[52:55],a[12:15],0\ns_add_u32 m0,s22,52096\nM a[168:171],a[48:51],a[12:15],0\ns_mov_b64 s[10:11],s[72:73]\n" \
"M a[184:187],a[56:59],a[12:15],0\ns_lshl_b32 s63,s62,12\nM a[188:191],a[60:63],a[12:15],0\nbuffer_load_dwordx4 v21,s[92:95],s47 offen offset:128 lds\nM a[180:183],a[60:63],a[8:11],0\ns_add_u32 m0,s50,32768\nM a[176:179],a[56:59],a[8:11],0\ns_mov_b32 s47,1048576\n" \
"M v[128:131],a[96:99],a[88:91],v[128:131]\ns_add_u32 s68,s10,s63\ns_addc_u32 s69,s11,0\nM v[132:135],a[100:103],a[88:91],v[132:135]\nbuffer_load_dwordx4 v13,s[28:31],s46 offen lds\nM v[140:143],a[100:103],a[92:95],v[140:143]\ns_add_u32 m0,s50,36864\nM v[136:139],a[96:99],a[92:95],v[136:139]\n" \
"s_and_b32 s69,s69,0xffff\nM v[152:155],a[104:107],a[92:95],v[152:155]\ns_add_u32 s96,s57,32768\nM v[156:159],a[108:111],a[92:95],v[156:159]\nbuffer_load_dwordx4 v14,s[28:31],s46 offen lds\nM v[148:151],a[108:111],a[88:91],v[148:151]\ns_add_u32 m0,s50,40960\nM v[144:147],a[104:107],a[88:91],v[144:147]\n" \
"s_lshl_b32 s62,s62,1\nM v[160:163],a[112:115],a[88:91],v[160:163]\ns_mov_b64 s[12:13],s[74:75]\nM v[164:167],a[116:119],a[88:91],v[164:167]\nbuffer_load_dwordx4 v15,s[28:31],s46 offen lds\nM v[172:175],a[116:119],a[92:95],v[172:175]\ns_add_u32 m0,s50,45056\nM v[168:171],a[112:115],a[92:95],v[168:171]\n" \
"s_lshr_b32 s21,s63,5\nM v[184:187],a[120:123],a[92:95],v[184:187]\ns_sub_u32 s14,s18,2000\ns_subb_u32 s15,s19,0\nM v[188:191],a[124:127],a[92:95],v[188:191]\nbuffer_load_dwordx4 v16,s[28:31],s46 offen lds\nM v[180:183],a[124:127],a[88:91],v[180:183]\ns_add_u32 m0,s50,49152\n" \
"M v[176:179],a[120:123],a[88:91],v[176:179]\ns_mov_b32 s46,256\nM v[64:67],a[96:99],a[80:83],v[64:67]\ns_mov_b64 s[4:5],s[90:91]\nM v[68:71],a[100:103],a[80:83],v[68:71]\nbuffer_load_dwordx4 v17,s[28:31],s46 offen lds\nM v[76:79],a[100:103],a[84:87],v[76:79]\ns_add_u32 m0,s50,53248\n" \
"M v[72:75],a[96:99],a[84:87],v[72:75]\ns_add_u32 s63,s63,79691776\nM v[88:91],a[104:107],a[84:87],v[88:91]\ns_waitcnt vmcnt(9) lgkmcnt(0)\nM v[92:95],a[108:111],a[84:87],v[92:95]\ns_add_u32 s16,s14,16777216\ns_addc_u32 s17,s15,0\ns_barrier\n" \
"ds_read_b64_tr_b16 a[32:33],v9 offset:32768\nM v[84:87],a[108:111],a[80:83],v[84:87]\nbuffer_load_dwordx4 v18,s[28:31],s46 offen lds\nM v[80:83],a[104:107],a[80:83],v[80:83]\ns_add_u32 m0,s50,57344\nds_read_b64_tr_b16 a[34:35],v10 offset:32768\nM v[96:99],a[112:115],a[80:83],v[96:99]\ns_add_u32 s72,s4,s63\n" \
"s_addc_u32 s73,s5,0\nds_read_b64_tr_b16 a[36:37],v11 offset:32768\nds_read_b64_tr_b16 a[38:39],v12 offset:32768\nM v[100:103],a[116:119],a[80:83],v[100:103]\ns_mov_b32 s63,65536\nds_read_b128 a[24:27],v5 offset:12288\nM v[108:111],a[116:119],a[84:87],v[108:111]\nbuffer_load_dwordx4 v19,s[28:31],s46 offen lds\n" \
"M v[104:107],a[112:115],a[84:87],v[104:107]\ns_add_u32 m0,s50,61440\nM v[120:123],a[120:123],a[84:87],v[120:123]\ns_cmp_lt_u32 %2,2\ns_cselect_b32 s63,32768,s63\nds_read_b128 a[28:31],v5 offset:14336\nM v[124:127],a[124:127],a[84:87],v[124:127]\ns_lshl_b32 s56,%2,14\n" \
"ds_read_b64_tr_b16 a[40:41],v9 offset:33280\nds_read_b64_tr_b16 a[42:43],v10 offset:33280\nM v[116:119],a[124:127],a[80:83],v[116:119]\nbuffer_load_dwordx4 v20,s[28:31],s46 offen lds\nM v[112:115],a[120:123],a[80:83],v[112:115]\ns_add_u32 m0,s22,0\nM v[192:195],a[96:99],a[64:67],v[192:195]\ns_mov_b32 s46,384\n" \
"ds_read_b64_tr_b16 a[44:45],v11 offset:33280\nds_read_b64_tr_b16 a[46:47],v12 offset:33280\nM v[196:199],a[100:103],a[64:67],v[196:199]\ns_add_u32 s56,s56,s63\nds_read_b64_tr_b16 a[48:49],v9 offset:33792\nM v[204:207],a[100:103],a[68:71],v[204:207]\nbuffer_load_dwordx4 v21,s[32:35],s47 offen offset:0 lds\nM v[200:203],a[96:99],a[68:71],v[200:203]\n" \
"s_add_u32 m0,s22,896\nds_read_b64_tr_b16 a[50:51],v10 offset:33792\nM v[216:219],a[104:107],a[68:71],v[216:219]\ns_and_b32 s73,s73,0xffff\nds_read_b64_tr_b16 a[52:53],v11 offset:33792\nM v[220:223],a[108:111],a[68:71],v[220:223]\ns_mov_b32 s74,4096\nds_read_b64_tr_b16 a[54:55],v12 offset:33792\n" \
"M v[212:215],a[108:111],a[64:67],v[212:215]\nbuffer_load_dwordx4 v21,s[32:35],s47 offen offset:128 lds\nM v[208:211],a[104:107],a[64:67],v[208:211]\ns_add_u32 m0,s22,2048\nds_read_b64_tr_b16 a[56:57],v9 offset:34304\nM v[224:227],a[112:115],a[64:67],v[224:227]\ns_mov_b32 s75,0x00020000\nds_read_b64_tr_b16 a[58:59],v10 offset:34304\n" \
"M v[228:231],a[116:119],a[64:67],v[228:231]\ns_add_u32 s84,s84,524288\ns_addc_u32 s85,s85,0\nds_read_b64_tr_b16 a[60:61],v11 offset:34304\nds_read_b64_tr_b16 a[62:63],v12 offset:34304\nM v[236:239],a[116:119],a[68:71],v[236:239]\nbuffer_load_dwordx4 v21,s[92:95],s47 offen offset:0 lds\nM v[232:235],a[112:115],a[68:71],v[232:235]\n" \
"s_add_u32 m0,s22,2944\nM v[248:251],a[120:123],a[68:71],v[248:251]\ns_add_u32 s21,s21,67108864\nds_read_b128 a[16:19],v5 offset:8192\nM v[252:255],a[124:127],a[68:71],v[252:255]\ns_mov_b32 s53,s22\nds_read_b128 a[20:23],v5 offset:10240\nM v[244:247],a[124:127],a[64:67],v[244:247]\n" \
"s_mov_b64 exec,0xff\nglobal_load_dword v0,v1,s[84:85]\ns_mov_b64 exec,-1\nM v[240:243],a[120:123],a[64:67],v[240:243]\ns_add_u32 s84,s84,524288\ns_addc_u32 s85,s85,0\nds_read_b128 a[0:3],v5 offset:0\nds_read_b128 a[4:7],v5 offset:2048\n" \
"M a[128:131],a[96:99],a[72:75],a[128:131]\ns_mov_b64 exec,0xff\nglobal_load_dword v0,v1,s[84:85]\ns_mov_b64 exec,-1\nM a[132:135],a[100:103],a[72:75],a[132:135]\ns_add_u32 s84,s84,524288\ns_addc_u32 s85,s85,0\nds_read_b128 a[8:11],v5 offset:4096\n" \
"ds_read_b128 a[12:15],v5 offset:6144\nM a[140:143],a[100:103],a[76:79],a[140:143]\nglobal_load_dwordx4 v[46:49],v4,s[86:87] offset:0\nM a[136:139],a[96:99],a[76:79],a[136:139]\nds_read_b128 a[88:91],v6 offset:12288\nds_read_b128 a[92:95],v6 offset:14336\nM a[152:155],a[104:107],a[76:79],a[152:155]\nds_read_b128 a[64:67],v6 offset:0\n" \
"M a[156:159],a[108:111],a[76:79],a[156:159]\nds_read_b128 a[68:71],v6 offset:2048\nM a[148:151],a[108:111],a[72:75],a[148:151]\nglobal_load_dwordx4 v[50:53],v4,s[86:87] offset:1024\nM a[144:147],a[104:107],a[72:75],a[144:147]\nds_read_b128 a[80:83],v6 offset:8192\nds_read_b128 a[84:87],v6 offset:10240\nM a[160:163],a[112:115],a[72:75],a[160:163]\n" \
"M a[164:167],a[116:119],a[72:75],a[164:167]\nM a[172:175],a[116:119],a[76:79],a[172:175]\nglobal_load_dwordx4 v[30:33],v26,s[64:65] offset:0\nM a[168:171],a[112:115],a[76:79],a[168:171]\nM a[184:187],a[120:123],a[76:79],a[184:187]\nM a[188:191],a[124:127],a[76:79],a[188:191]\nM a[180:183],a[124:127],a[72:75],a[180:183]\nglobal_load_dwordx4 v[34:37],v26,s[64:65] offset:1024\n" \
"M a[176:179],a[120:123],a[72:75],a[176:179]\ns_waitcnt lgkmcnt(0)\nds_read_b128 a[72:75],v6 offset:4096\nds_read_b128 a[76:79],v6 offset:6144\nM v[128:131],a[32:35],a[24:27],v[128:131]\nM v[132:135],a[36:39],a[24:27],v[132:135]\nM v[140:143],a[36:39],a[28:31],v[140:143]\nLG0_%=:\n" \
"s_cbranch_execz LG2_%=\nbuffer_load_dwordx4 v21,s[92:95],s47 offen offset:128 lds\nM v[136:139],a[32:35],a[28:31],v[136:139]\ns_add_u32 m0,s22,16384\nM v[152:155],a[40:43],a[28:31],v[152:155]\ns_mov_b32 s47,1310720\nM v[156:159],a[44:47],a[28:31],v[156:159]\nM v[148:151],a[44:47],a[24:27],v[148:151]\n" \
"buffer_load_dwordx4 v21,s[32:35],s47 offen offset:0 lds\nM v[144:147],a[40:43],a[24:27],v[144:147]\ns_add_u32 m0,s22,17280\nM v[160:163],a[48:51],a[24:27],v[160:163]\nM v[164:167],a[52:55],a[24:27],v[164:167]\nM v[172:175],a[52:55],a[28:31],v[172:175]\nbuffer_load_dwordx4 v21,s[32:35],s47 offen offset:128 lds\nM v[168:171],a[48:51],a[28:31],v[168:171]\n" \
"s_add_u32 m0,s22,18432\nM v[184:187],a[56:59],a[28:31],v[184:187]\ns_waitcnt vmcnt(20) lgkmcnt(14)\nM v[188:191],a[60:63],a[28:31],v[188:191]\ns_barrier\nds_read_b64_tr_b16 a[96:97],v9 offset:49152\nM v[180:183],a[60:63],a[24:27],v[180:183]\nbuffer_load_dwordx4 v21,s[92:95],s47 offen offset:0 lds\n" \
"M v[176:179],a[56:59],a[24:27],v[176:179]\ns_add_u32 m0,s22,19328\nds_read_b64_tr_b16 a[98:99],v10 offset:49152\nM v[64:67],a[32:35],a[16:19],v[64:67]\nds_read_b64_tr_b16 a[100:101],v11 offset:49152\nds_read_b64_tr_b16 a[102:103],v12 offset:49152\nM v[68:71],a[36:39],a[16:19],v[68:71]\nds_read_b64_tr_b16 a[104:105],v9 offset:49664\n" \
"M v[76:79],a[36:39],a[20:23],v[76:79]\nbuffer_load_dwordx4 v21,s[92:95],s47 offen offset:128 lds\nM v[72:75],a[32:35],a[20:23],v[72:75]\ns_add_u32 m0,s50,65536\nds_read_b64_tr_b16 a[106:107],v10 offset:49664\nM v[88:91],a[40:43],a[20:23],v[88:91]\ns_mov_b32 s47,1572864\nds_read_b64_tr_b16 a[108:109],v11 offset:49664\n" \
"M v[92:95],a[44:47],a[20:23],v[92:95]\nds_read_b64_tr_b16 a[110:111],v12 offset:49664\nM v[84:87],a[44:47],a[16:19],v[84:87]\nds_read_b64_tr_b16 a[112:113],v9 offset:50176\nds_read_b64_tr_b16 a[114:115],v10 offset:50176\nM v[80:83],a[40:43],a[16:19],v[80:83]\nds_read_b64_tr_b16 a[116:117],v11 offset:50176\nM v[96:99],a[48:51],a[16:19],v[96:99]\n" \
"ds_read_b64_tr_b16 a[118:119],v12 offset:50176\nM v[100:103],a[52:55],a[16:19],v[100:103]\nds_read_b64_tr_b16 a[120:121],v9 offset:50688\nds_read_b64_tr_b16 a[122:123],v10 offset:50688\nM v[108:111],a[52:55],a[20:23],v[108:111]\nds_read_b64_tr_b16 a[124:125],v11 offset:50688\nM v[104:107],a[48:51],a[20:23],v[104:107]\ns_mov_b64 exec,0\n" \
"s_cmp_lt_u32 %2,2\ns_cbranch_scc1 LG3_%=\ns_branch LG7_%=\nLHA155_%=:\ns_mov_b64 exec,-1\nds_read_b64_tr_b16 a[126:127],v12 offset:50688\nM v[120:123],a[56:59],a[20:23],v[120:123]\nM v[124:127],a[60:63],a[20:23],v[124:127]\n" \
"M v[116:119],a[60:63],a[16:19],v[116:119]\nM v[112:115],a[56:59],a[16:19],v[112:115]\nM v[192:195],a[32:35],a[0:3],v[192:195]\nM v[196:199],a[36:39],a[0:3],v[196:199]\nM v[204:207],a[36:39],a[4:7],v[204:207]\nM v[200:203],a[32:35],a[4:7],v[200:203]\nM v[216:219],a[40:43],a[4:7],v[216:219]\nbuffer_load_dwordx4 v13,s[28:31],s46 offen lds\n" \
"M v[220:223],a[44:47],a[4:7],v[220:223]\ns_add_u32 m0,s50,69632\nM v[212:215],a[44:47],a[0:3],v[212:215]\nM v[208:211],a[40:43],a[0:3],v[208:211]\nM v[224:227],a[48:51],a[0:3],v[224:227]\nbuffer_load_dwordx4 v14,s[28:31],s46 offen lds\nM v[228:231],a[52:55],a[0:3],v[228:231]\ns_add_u32 m0,s50,73728\n" \
"M v[236:239],a[52:55],a[4:7],v[236:239]\nM v[232:235],a[48:51],a[4:7],v[232:235]\nM v[248:251],a[56:59],a[4:7],v[248:251]\nbuffer_load_dwordx4 v15,s[28:31],s46 offen lds\nM v[252:255],a[60:63],a[4:7],v[252:255]\ns_add_u32 m0,s50,77824\nM v[244:247],a[60:63],a[0:3],v[244:247]\nM v[240:243],a[56:59],a[0:3],v[240:243]\n" \
"M a[128:131],a[32:35],a[8:11],a[128:131]\nbuffer_load_dwordx4 v16,s[28:31],s46 offen lds\nM a[132:135],a[36:39],a[8:11],a[132:135]\ns_add_u32 m0,s50,81920\nM a[140:143],a[36:39],a[12:15],a[140:143]\ns_mov_b32 s46,384\nM a[136:139],a[32:35],a[12:15],a[136:139]\nM a[152:155],a[40:43],a[12:15],a[152:155]\n" \
"buffer_load_dwordx4 v17,s[28:31],s46 offen lds\nM a[156:159],a[44:47],a[12:15],a[156:159]\ns_add_u32 m0,s50,86016\nM a[148:151],a[44:47],a[8:11],a[148:151]\nM a[144:147],a[40:43],a[8:11],a[144:147]\nM a[160:163],a[48:51],a[8:11],a[160:163]\nbuffer_load_dwordx4 v18,s[28:31],s46 offen lds\nM a[164:167],a[52:55],a[8:11],a[164:167]\n" \
"s_add_u32 m0,s50,90112\nM a[172:175],a[52:55],a[12:15],a[172:175]\nM a[168:171],a[48:51],a[12:15],a[168:171]\nM a[184:187],a[56:59],a[12:15],a[184:187]\nbuffer_load_dwordx4 v19,s[28:31],s46 offen lds\nM a[188:191],a[60:63],a[12:15],a[188:191]\ns_add_u32 m0,s50,94208\nM a[180:183],a[60:63],a[8:11],a[180:183]\n" \
"M a[176:179],a[56:59],a[8:11],a[176:179]\ns_waitcnt lgkmcnt(0)\nM v[128:131],a[96:99],a[88:91],v[128:131]\nbuffer_load_dwordx4 v20,s[28:31],s46 offen lds\nM v[132:135],a[100:103],a[88:91],v[132:135]\ns_add_u32 m0,s22,32768\nM v[140:143],a[100:103],a[92:95],v[140:143]\ns_waitcnt vmcnt(12) lgkmcnt(0)\n" \
"M v[136:139],a[96:99],a[92:95],v[136:139]\ns_mov_b32 s46,512\ns_barrier\nds_read_b64_tr_b16 a[32:33],v9 offset:0\nM v[152:155],a[104:107],a[92:95],v[152:155]\nbuffer_load_dwordx4 v21,s[32:35],s47 offen offset:0 lds\nM v[156:159],a[108:111],a[92:95],v[156:159]\ns_add_u32 m0,s22,33664\n" \
"ds_read_b64_tr_b16 a[34:35],v10 offset:0\nM v[148:151],a[108:111],a[88:91],v[148:151]\nds_read_b64_tr_b16 a[36:37],v11 offset:0\nds_read_b64_tr_b16 a[38:39],v12 offset:0\nM v[144:147],a[104:107],a[88:91],v[144:147]\nds_read_b64_tr_b16 a[40:41],v9 offset:512\nM v[160:163],a[112:115],a[88:91],v[160:163]\nbuffer_load_dwordx4 v21,s[32:35],s47 offen offset:128 lds\n" \
"M v[164:167],a[116:119],a[88:91],v[164:167]\ns_add_u32 m0,s22,34816\nds_read_b64_tr_b16 a[42:43],v10 offset:512\nM v[172:175],a[116:119],a[92:95],v[172:175]\nds_read_b64_tr_b16 a[44:45],v11 offset:512\nM v[168:171],a[112:115],a[92:95],v[168:171]\nds_read_b64_tr_b16 a[46:47],v12 offset:512\nM v[184:187],a[120:123],a[92:95],v[184:187]\n" \
"buffer_load_dwordx4 v21,s[92:95],s47 offen offset:0 lds\nM v[188:191],a[124:127],a[92:95],v[188:191]\ns_add_u32 m0,s22,35712\nds_read_b64_tr_b16 a[48:49],v9 offset:1024\nM v[180:183],a[124:127],a[88:91],v[180:183]\nds_read_b64_tr_b16 a[50:51],v10 offset:1024\nM v[176:179],a[120:123],a[88:91],v[176:179]\nds_read_b64_tr_b16 a[52:53],v11 offset:1024\n" \
"ds_read_b64_tr_b16 a[54:55],v12 offset:1024\nM v[64:67],a[96:99],a[80:83],v[64:67]\nbuffer_load_dwordx4 v21,s[92:95],s47 offen offset:128 lds\nM v[68:71],a[100:103],a[80:83],v[68:71]\ns_add_u32 m0,s22,49152\nM v[76:79],a[100:103],a[84:87],v[76:79]\ns_mov_b32 s47,1835008\nds_read_b64_tr_b16 a[56:57],v9 offset:1536\n" \
"ds_read_b64_tr_b16 a[58:59],v10 offset:1536\nM v[72:75],a[96:99],a[84:87],v[72:75]\nds_read_b64_tr_b16 a[60:61],v11 offset:1536\nM v[88:91],a[104:107],a[84:87],v[88:91]\nbuffer_load_dwordx4 v21,s[32:35],s47 offen offset:0 lds\nM v[92:95],a[108:111],a[84:87],v[92:95]\ns_add_u32 m0,s22,50048\nds_read_b64_tr_b16 a[62:63],v12 offset:1536\n" \
"M v[84:87],a[108:111],a[80:83],v[84:87]\nds_read_b128 a[24:27],v5 offset:45056\nM v[80:83],a[104:107],a[80:83],v[80:83]\nds_read_b128 a[28:31],v5 offset:47104\nM v[96:99],a[112:115],a[80:83],v[96:99]\nbuffer_load_dwordx4 v21,s[32:35],s47 offen offset:128 lds\nM v[100:103],a[116:119],a[80:83],v[100:103]\ns_add_u32 m0,s22,51200\n" \
"M v[108:111],a[116:119],a[84:87],v[108:111]\nds_read_b128 a[16:19],v5 offset:40960\nM v[104:107],a[112:115],a[84:87],v[104:107]\nds_read_b128 a[20:23],v5 offset:43008\nM v[120:123],a[120:123],a[84:87],v[120:123]\nbuffer_load_dwordx4 v21,s[92:95],s47 offen offset:0 lds\nM v[124:127],a[124:127],a[84:87],v[124:127]\ns_add_u32 m0,s22,52096\n" \
"ds_read_b128 a[0:3],v5 offset:32768\nM v[116:119],a[124:127],a[80:83],v[116:119]\nds_read_b128 a[4:7],v5 offset:34816\nM v[112:115],a[120:123],a[80:83],v[112:115]\nds_read_b128 a[8:11],v5 offset:36864\nM v[192:195],a[96:99],a[64:67],v[192:195]\nbuffer_load_dwordx4 v21,s[92:95],s47 offen offset:128 lds\nM v[196:199],a[100:103],a[64:67],v[196:199]\n" \
"s_add_u32 m0,s50,0\nM v[204:207],a[100:103],a[68:71],v[204:207]\ns_mov_b32 s47,2097152\nds_read_b128 a[12:15],v5 offset:38912\nM v[200:203],a[96:99],a[68:71],v[200:203]\nds_read_b128 a[80:83],v6 offset:40960\nM v[216:219],a[104:107],a[68:71],v[216:219]\nbuffer_load_dwordx4 v13,s[28:31],s46 offen lds\n" \
"M v[220:223],a[108:111],a[68:71],v[220:223]\ns_add_u32 m0,s50,4096\nds_read_b128 a[84:87],v6 offset:43008\nM v[212:215],a[108:111],a[64:67],v[212:215]\nds_read_b128 a[88:91],v6 offset:45056\nM v[208:211],a[104:107],a[64:67],v[208:211]\nds_read_b128 a[92:95],v6 offset:47104\nM v[224:227],a[112:115],a[64:67],v[224:227]\n" \
"buffer_load_dwordx4 v14,s[28:31],s46 offen lds\nM v[228:231],a[116:119],a[64:67],v[228:231]\ns_add_u32 m0,s50,8192\nM v[236:239],a[116:119],a[68:71],v[236:239]\nM v[232:235],a[112:115],a[68:71],v[232:235]\nM v[248:251],a[120:123],a[68:71],v[248:251]\nM v[252:255],a[124:127],a[68:71],v[252:255]\nbuffer_load_dwordx4 v15,s[28:31],s46 offen lds\n" \
"M v[244:247],a[124:127],a[64:67],v[244:247]\ns_add_u32 m0,s50,12288\nM v[240:243],a[120:123],a[64:67],v[240:243]\nds_read_b128 a[64:67],v6 offset:32768\nds_read_b128 a[68:71],v6 offset:34816\nM a[128:131],a[96:99],a[72:75],a[128:131]\nM a[132:135],a[100:103],a[72:75],a[132:135]\nbuffer_load_dwordx4 v16,s[28:31],s46 offen lds\n" \
"M a[140:143],a[100:103],a[76:79],a[140:143]\ns_add_u32 m0,s50,16384\nM a[136:139],a[96:99],a[76:79],a[136:139]\ns_mov_b32 s46,512\nM a[152:155],a[104:107],a[76:79],a[152:155]\nM a[156:159],a[108:111],a[76:79],a[156:159]\nM a[148:151],a[108:111],a[72:75],a[148:151]\ns_waitcnt vmcnt(20) lgkmcnt(14)\n" \
"M a[144:147],a[104:107],a[72:75],a[144:147]\ns_barrier\nds_read_b64_tr_b16 a[96:97],v9 offset:16384\nM a[160:163],a[112:115],a[72:75],a[160:163]\nbuffer_load_dwordx4 v17,s[28:31],s46 offen lds\nM a[164:167],a[116:119],a[72:75],a[164:167]\ns_add_u32 m0,s50,20480\nds_read_b64_tr_b16 a[98:99],v10 offset:16384\n" \
"M a[172:175],a[116:119],a[76:79],a[172:175]\nds_read_b64_tr_b16 a[100:101],v11 offset:16384\nds_read_b64_tr_b16 a[102:103],v12 offset:16384\nM a[168:171],a[112:115],a[76:79],a[168:171]\nds_read_b64_tr_b16 a[104:105],v9 offset:16896\nM a[184:187],a[120:123],a[76:79],a[184:187]\nbuffer_load_dwordx4 v18,s[28:31],s46 offen lds\nM a[188:191],a[124:127],a[76:79],a[188:191]\n" \
"s_add_u32 m0,s50,24576\nds_read_b64_tr_b16 a[106:107],v10 offset:16896\nM a[180:183],a[124:127],a[72:75],a[180:183]\nds_read_b64_tr_b16 a[108:109],v11 offset:16896\nM a[176:179],a[120:123],a[72:75],a[176:179]\ns_waitcnt lgkmcnt(5)\nds_read_b128 a[72:75],v6 offset:36864\nM v[128:131],a[32:35],a[24:27],v[128:131]\n" \
"buffer_load_dwordx4 v19,s[28:31],s46 offen lds\nM v[132:135],a[36:39],a[24:27],v[132:135]\ns_add_u32 m0,s50,28672\nds_read_b128 a[76:79],v6 offset:38912\nM v[140:143],a[36:39],a[28:31],v[140:143]\nds_read_b64_tr_b16 a[110:111],v12 offset:16896\nM v[136:139],a[32:35],a[28:31],v[136:139]\nds_read_b64_tr_b16 a[112:113],v9 offset:17408\n" \
"M v[152:155],a[40:43],a[28:31],v[152:155]\nbuffer_load_dwordx4 v20,s[28:31],s46 offen lds\nM v[156:159],a[44:47],a[28:31],v[156:159]\ns_add_u32 m0,s22,0\nds_read_b64_tr_b16 a[114:115],v10 offset:17408\nM v[148:151],a[44:47],a[24:27],v[148:151]\ns_mov_b32 s46,640\nds_read_b64_tr_b16 a[116:117],v11 offset:17408\n" \
"M v[144:147],a[40:43],a[24:27],v[144:147]\nds_read_b64_tr_b16 a[118:119],v12 offset:17408\nM v[160:163],a[48:51],a[24:27],v[160:163]\nbuffer_load_dwordx4 v21,s[32:35],s47 offen offset:0 lds\nM v[164:167],a[52:55],a[24:27],v[164:167]\ns_add_u32 m0,s22,896\nds_read_b64_tr_b16 a[120:121],v9 offset:17920\nM v[172:175],a[52:55],a[28:31],v[172:175]\n" \
"ds_read_b64_tr_b16 a[122:123],v10 offset:17920\nM v[168:171],a[48:51],a[28:31],v[168:171]\nds_read_b64_tr_b16 a[124:125],v11 offset:17920\nds_read_b64_tr_b16 a[126:127],v12 offset:17920\nM v[184:187],a[56:59],a[28:31],v[184:187]\nbuffer_load_dwordx4 v21,s[32:35],s47 offen offset:128 lds\nM v[188:191],a[60:63],a[28:31],v[188:191]\ns_add_u32 m0,s22,2048\n" \
"M v[180:183],a[60:63],a[24:27],v[180:183]\nM v[176:179],a[56:59],a[24:27],v[176:179]\nM v[64:67],a[32:35],a[16:19],v[64:67]\nM v[68:71],a[36:39],a[16:19],v[68:71]\nbuffer_load_dwordx4 v21,s[92:95],s47 offen offset:0 lds\nM v[76:79],a[36:39],a[20:23],v[76:79]\ns_add_u32 m0,s22,2944\nM v[72:75],a[32:35],a[20:23],v[72:75]\n" \
"M v[88:91],a[40:43],a[20:23],v[88:91]\nM v[92:95],a[44:47],a[20:23],v[92:95]\nbuffer_load_dwordx4 v21,s[92:95],s47 offen offset:128 lds\nM v[84:87],a[44:47],a[16:19],v[84:87]\ns_add_u32 m0,s22,16384\nM v[80:83],a[40:43],a[16:19],v[80:83]\ns_mov_b32 s47,2359296\nM v[96:99],a[48:51],a[16:19],v[96:99]\n" \
"M v[100:103],a[52:55],a[16:19],v[100:103]\nglobal_load_dwordx4 v[38:41],v26,s[64:65] offset:2048\nM v[108:111],a[52:55],a[20:23],v[108:111]\nM v[104:107],a[48:51],a[20:23],v[104:107]\nM v[120:123],a[56:59],a[20:23],v[120:123]\nM v[124:127],a[60:63],a[20:23],v[124:127]\nglobal_load_dwordx4 v[42:45],v26,s[64:65] offset:3072\nM v[116:119],a[60:63],a[16:19],v[116:119]\n" \
"M v[112:115],a[56:59],a[16:19],v[112:115]\nM v[192:195],a[32:35],a[0:3],v[192:195]\nM v[196:199],a[36:39],a[0:3],v[196:199]\ns_mov_b64 exec,0xff\nglobal_load_dword v0,v1,s[84:85]\ns_mov_b64 exec,-1\nM v[204:207],a[36:39],a[4:7],v[204:207]\ns_add_u32 s84,s84,524288\n" \
"s_addc_u32 s85,s85,0\nM v[200:203],a[32:35],a[4:7],v[200:203]\nglobal_load_dwordx4 a[230:233],v27,s[70:71] offset:0\nM v[216:219],a[40:43],a[4:7],v[216:219]\nM v[220:223],a[44:47],a[4:7],v[220:223]\nM v[212:215],a[44:47],a[0:3],v[212:215]\nM v[208:211],a[40:43],a[0:3],v[208:211]\nglobal_load_dwordx4 a[234:237],v27,s[70:71] offset:16\n" \
"M v[224:227],a[48:51],a[0:3],v[224:227]\ns_mov_b32 s70,4096\nM v[228:231],a[52:55],a[0:3],v[228:231]\ns_mov_b32 s71,0x00020000\nM v[236:239],a[52:55],a[4:7],v[236:239]\nM v[232:235],a[48:51],a[4:7],v[232:235]\nbuffer_load_dwordx2 a[192:193],v54,s[40:43],0 offen offset:0 nt\nM v[248:251],a[56:59],a[4:7],v[248:251]\n" \
"M v[252:255],a[60:63],a[4:7],v[252:255]\nM v[244:247],a[60:63],a[0:3],v[244:247]\nM v[240:243],a[56:59],a[0:3],v[240:243]\nbuffer_load_dwordx2 a[194:195],v54,s[40:43],0 offen offset:2048 nt\nM a[128:131],a[32:35],a[8:11],a[128:131]\nM a[132:135],a[36:39],a[8:11],a[132:135]\nM a[140:143],a[36:39],a[12:15],a[140:143]\nM a[136:139],a[32:35],a[12:15],a[136:139]\n" \
"buffer_load_dwordx2 a[196:197],v55,s[40:43],0 offen offset:0 nt\nM a[152:155],a[40:43],a[12:15],a[152:155]\nM a[156:159],a[44:47],a[12:15],a[156:159]\nM a[148:151],a[44:47],a[8:11],a[148:151]\ns_waitcnt vmcnt(24) lgkmcnt(0)\nM a[144:147],a[40:43],a[8:11],a[144:147]\ns_barrier\nds_read_b64_tr_b16 a[32:33],v9 offset:32768\n" \
"M a[160:163],a[48:51],a[8:11],a[160:163]\nbuffer_load_dwordx2 a[198:199],v59,s[40:43],s52 offen offset:0 nt\nM a[164:167],a[52:55],a[8:11],a[164:167]\nds_read_b64_tr_b16 a[34:35],v10 offset:32768\nds_read_b64_tr_b16 a[36:37],v11 offset:32768\nM a[172:175],a[52:55],a[12:15],a[172:175]\nds_read_b64_tr_b16 a[38:39],v12 offset:32768\nM a[168:171],a[48:51],a[12:15],a[168:171]\n" \
"ds_read_b64_tr_b16 a[40:41],v9 offset:33280\nds_read_b64_tr_b16 a[42:43],v10 offset:33280\nM a[184:187],a[56:59],a[12:15],a[184:187]\nbuffer_load_dwordx2 a[214:215],v59,s[80:83],s52 offen offset:0 nt\nM a[188:191],a[60:63],a[12:15],a[188:191]\nds_read_b64_tr_b16 a[44:45],v11 offset:33280\nds_read_b64_tr_b16 a[46:47],v12 offset:33280\nM a[180:183],a[60:63],a[8:11],a[180:183]\n" \
"ds_read_b64_tr_b16 a[48:49],v9 offset:33792\nM a[176:179],a[56:59],a[8:11],a[176:179]\nds_read_b64_tr_b16 a[50:51],v10 offset:33792\nds_read_b64_tr_b16 a[52:53],v11 offset:33792\nM v[128:131],a[96:99],a[88:91],v[128:131]\nbuffer_load_dwordx2 a[200:201],v59,s[40:43],s52 offen offset:2048 nt\nM v[132:135],a[100:103],a[88:91],v[132:135]\nds_read_b64_tr_b16 a[54:55],v12 offset:33792\n" \
"ds_read_b64_tr_b16 a[56:57],v9 offset:34304\nM v[140:143],a[100:103],a[92:95],v[140:143]\nds_read_b64_tr_b16 a[58:59],v10 offset:34304\nM v[136:139],a[96:99],a[92:95],v[136:139]\nds_read_b64_tr_b16 a[60:61],v11 offset:34304\nds_read_b64_tr_b16 a[62:63],v12 offset:34304\nM v[152:155],a[104:107],a[92:95],v[152:155]\nbuffer_load_dwordx2 a[216:217],v59,s[80:83],s52 offen offset:2048 nt\n" \
"M v[156:159],a[108:111],a[92:95],v[156:159]\ns_mov_b32 s52,4096\nds_read_b128 a[24:27],v7 offset:12288\nds_read_b128 a[28:31],v7 offset:14336\nM v[148:151],a[108:111],a[88:91],v[148:151]\nds_read_b128 a[16:19],v7 offset:8192\nM v[144:147],a[104:107],a[88:91],v[144:147]\nds_read_b128 a[20:23],v7 offset:10240\n" \
"M v[160:163],a[112:115],a[88:91],v[160:163]\nbuffer_load_dwordx2 a[202:203],v59,s[40:43],s52 offen offset:0 nt\nM v[164:167],a[116:119],a[88:91],v[164:167]\nds_read_b128 a[0:3],v7 offset:0\nds_read_b128 a[4:7],v7 offset:2048\nM v[172:175],a[116:119],a[92:95],v[172:175]\nds_read_b128 a[8:11],v7 offset:4096\nM v[168:171],a[112:115],a[92:95],v[168:171]\n" \
"ds_read_b128 a[12:15],v7 offset:6144\nM v[184:187],a[120:123],a[92:95],v[184:187]\nbuffer_load_dwordx2 a[218:219],v59,s[80:83],s52 offen offset:0 nt\nM v[188:191],a[124:127],a[92:95],v[188:191]\nM v[180:183],a[124:127],a[88:91],v[180:183]\nM v[176:179],a[120:123],a[88:91],v[176:179]\nds_read_b128 a[88:91],v8 offset:12288\nds_read_b128 a[92:95],v8 offset:14336\n" \
"M v[64:67],a[96:99],a[80:83],v[64:67]\nbuffer_load_dwordx2 a[204:205],v59,s[40:43],s52 offen offset:2048 nt\nM v[68:71],a[100:103],a[80:83],v[68:71]\nM v[76:79],a[100:103],a[84:87],v[76:79]\nM v[72:75],a[96:99],a[84:87],v[72:75]\nM v[88:91],a[104:107],a[84:87],v[88:91]\nbuffer_load_dwordx2 a[220:221],v59,s[80:83],s52 offen offset:2048 nt\nM v[92:95],a[108:111],a[84:87],v[92:95]\n" \
"s_mov_b32 s52,8192\nM v[84:87],a[108:111],a[80:83],v[84:87]\nM v[80:83],a[104:107],a[80:83],v[80:83]\nM v[96:99],a[112:115],a[80:83],v[96:99]\nM v[100:103],a[116:119],a[80:83],v[100:103]\nM v[108:111],a[116:119],a[84:87],v[108:111]\nM v[104:107],a[112:115],a[84:87],v[104:107]\nM v[120:123],a[120:123],a[84:87],v[120:123]\n" \
"M v[124:127],a[124:127],a[84:87],v[124:127]\nM v[116:119],a[124:127],a[80:83],v[116:119]\nM v[112:115],a[120:123],a[80:83],v[112:115]\nds_read_b128 a[80:83],v8 offset:8192\nds_read_b128 a[84:87],v8 offset:10240\nM v[192:195],a[96:99],a[64:67],v[192:195]\nM v[196:199],a[100:103],a[64:67],v[196:199]\nM v[204:207],a[100:103],a[68:71],v[204:207]\n" \
"M v[200:203],a[96:99],a[68:71],v[200:203]\nM v[216:219],a[104:107],a[68:71],v[216:219]\nM v[220:223],a[108:111],a[68:71],v[220:223]\nM v[212:215],a[108:111],a[64:67],v[212:215]\nM v[208:211],a[104:107],a[64:67],v[208:211]\nM v[224:227],a[112:115],a[64:67],v[224:227]\nM v[228:231],a[116:119],a[64:67],v[228:231]\nM v[236:239],a[116:119],a[68:71],v[236:239]\n" \
"M v[232:235],a[112:115],a[68:71],v[232:235]\nM v[248:251],a[120:123],a[68:71],v[248:251]\nM v[252:255],a[124:127],a[68:71],v[252:255]\nM v[244:247],a[124:127],a[64:67],v[244:247]\nM v[240:243],a[120:123],a[64:67],v[240:243]\nds_read_b128 a[64:67],v8 offset:0\nds_read_b128 a[68:71],v8 offset:2048\nM a[128:131],a[96:99],a[72:75],a[128:131]\n" \
"M a[132:135],a[100:103],a[72:75],a[132:135]\nM a[140:143],a[100:103],a[76:79],a[140:143]\nM a[136:139],a[96:99],a[76:79],a[136:139]\nM a[152:155],a[104:107],a[76:79],a[152:155]\nM a[156:159],a[108:111],a[76:79],a[156:159]\nM a[148:151],a[108:111],a[72:75],a[148:151]\ns_waitcnt vmcnt(28) lgkmcnt(14)\nM a[144:147],a[104:107],a[72:75],a[144:147]\n" \
"s_barrier\nds_read_b64_tr_b16 a[96:97],v9 offset:49152\nM a[160:163],a[112:115],a[72:75],a[160:163]\nds_read_b64_tr_b16 a[98:99],v10 offset:49152\nds_read_b64_tr_b16 a[100:101],v11 offset:49152\nM a[164:167],a[116:119],a[72:75],a[164:167]\nds_read_b64_tr_b16 a[102:103],v12 offset:49152\nM a[172:175],a[116:119],a[76:79],a[172:175]\n" \
"ds_read_b64_tr_b16 a[104:105],v9 offset:49664\nds_read_b64_tr_b16 a[106:107],v10 offset:49664\nM a[168:171],a[112:115],a[76:79],a[168:171]\nds_read_b64_tr_b16 a[108:109],v11 offset:49664\nM a[184:187],a[120:123],a[76:79],a[184:187]\nds_read_b64_tr_b16 a[110:111],v12 offset:49664\nM a[188:191],a[124:127],a[76:79],a[188:191]\nds_read_b64_tr_b16 a[112:113],v9 offset:50176\n" \
"ds_read_b64_tr_b16 a[114:115],v10 offset:50176\nM a[180:183],a[124:127],a[72:75],a[180:183]\nds_read_b64_tr_b16 a[116:117],v11 offset:50176\nM a[176:179],a[120:123],a[72:75],a[176:179]\ns_waitcnt lgkmcnt(7)\nds_read_b128 a[72:75],v8 offset:4096\nM v[128:131],a[32:35],a[24:27],v[128:131]\nds_read_b128 a[76:79],v8 offset:6144\n" \
"M v[132:135],a[36:39],a[24:27],v[132:135]\nbuffer_load_dwordx4 v21,s[32:35],s47 offen offset:0 lds\nM v[140:143],a[36:39],a[28:31],v[140:143]\ns_add_u32 m0,s22,17280\nds_read_b64_tr_b16 a[118:119],v12 offset:50176\nM v[136:139],a[32:35],a[28:31],v[136:139]\nds_read_b64_tr_b16 a[120:121],v9 offset:50688\nM v[152:155],a[40:43],a[28:31],v[152:155]\n" \
"ds_read_b64_tr_b16 a[122:123],v10 offset:50688\nM v[156:159],a[44:47],a[28:31],v[156:159]\nbuffer_load_dwordx4 v21,s[32:35],s47 offen offset:128 lds\nM v[148:151],a[44:47],a[24:27],v[148:151]\ns_add_u32 m0,s22,18432\nds_read_b64_tr_b16 a[124:125],v11 offset:50688\nM v[144:147],a[40:43],a[24:27],v[144:147]\nds_read_b64_tr_b16 a[126:127],v12 offset:50688\n" \
"M v[160:163],a[48:51],a[24:27],v[160:163]\nM v[164:167],a[52:55],a[24:27],v[164:167]\nbuffer_load_dwordx4 v21,s[92:95],s47 offen offset:0 lds\nM v[172:175],a[52:55],a[28:31],v[172:175]\ns_add_u32 m0,s22,19328\nM v[168:171],a[48:51],a[28:31],v[168:171]\nM v[184:187],a[56:59],a[28:31],v[184:187]\nM v[188:191],a[60:63],a[28:31],v[188:191]\n" \
"buffer_load_dwordx4 v21,s[92:95],s47 offen offset:128 lds\nM v[180:183],a[60:63],a[24:27],v[180:183]\ns_add_u32 m0,s50,32768\nM v[176:179],a[56:59],a[24:27],v[176:179]\ns_mov_b32 s47,2621440\nM v[64:67],a[32:35],a[16:19],v[64:67]\nM v[68:71],a[36:39],a[16:19],v[68:71]\nbuffer_load_dwordx4 v13,s[28:31],s46 offen lds\n" \
"M v[76:79],a[36:39],a[20:23],v[76:79]\ns_add_u32 m0,s50,36864\nM v[72:75],a[32:35],a[20:23],v[72:75]\nM v[88:91],a[40:43],a[20:23],v[88:91]\nM v[92:95],a[44:47],a[20:23],v[92:95]\nbuffer_load_dwordx4 v14,s[28:31],s46 offen lds\nM v[84:87],a[44:47],a[16:19],v[84:87]\ns_add_u32 m0,s50,40960\n" \
"M v[80:83],a[40:43],a[16:19],v[80:83]\nM v[96:99],a[48:51],a[16:19],v[96:99]\nM v[100:103],a[52:55],a[16:19],v[100:103]\nbuffer_load_dwordx4 v15,s[28:31],s46 offen lds\nM v[108:111],a[52:55],a[20:23],v[108:111]\nLG1_%=:\ns_cbranch_execz LAV_%=\ns_add_u32 m0,s50,45056\n" \
"M v[104:107],a[48:51],a[20:23],v[104:107]\nM v[120:123],a[56:59],a[20:23],v[120:123]\nM v[124:127],a[60:63],a[20:23],v[124:127]\nbuffer_load_dwordx4 v16,s[28:31],s46 offen lds\nM v[116:119],a[60:63],a[16:19],v[116:119]\ns_add_u32 m0,s50,49152\nM v[112:115],a[56:59],a[16:19],v[112:115]\ns_mov_b32 s46,640\n" \
"M v[192:195],a[32:35],a[0:3],v[192:195]\nM v[196:199],a[36:39],a[0:3],v[196:199]\nbuffer_load_dwordx4 v17,s[28:31],s46 offen lds\nM v[204:207],a[36:39],a[4:7],v[204:207]\ns_add_u32 m0,s50,53248\nM v[200:203],a[32:35],a[4:7],v[200:203]\nM v[216:219],a[40:43],a[4:7],v[216:219]\nM v[220:223],a[44:47],a[4:7],v[220:223]\n" \
"buffer_load_dwordx4 v18,s[28:31],s46 offen lds\nM v[212:215],a[44:47],a[0:3],v[212:215]\ns_add_u32 m0,s50,57344\nM v[208:211],a[40:43],a[0:3],v[208:211]\nM v[224:227],a[48:51],a[0:3],v[224:227]\nM v[228:231],a[52:55],a[0:3],v[228:231]\nbuffer_load_dwordx4 v19,s[28:31],s46 offen lds\nM v[236:239],a[52:55],a[4:7],v[236:239]\n" \
"s_add_u32 m0,s50,61440\nM v[232:235],a[48:51],a[4:7],v[232:235]\nM v[248:251],a[56:59],a[4:7],v[248:251]\nM v[252:255],a[60:63],a[4:7],v[252:255]\nbuffer_load_dwordx4 v20,s[28:31],s46 offen lds\nM v[244:247],a[60:63],a[0:3],v[244:247]\ns_add_u32 m0,s22,32768\nM v[240:243],a[56:59],a[0:3],v[240:243]\n" \
"s_mov_b32 s46,768\nM a[128:131],a[32:35],a[8:11],a[128:131]\nM a[132:135],a[36:39],a[8:11],a[132:135]\nbuffer_load_dwordx4 v21,s[32:35],s47 offen offset:0 lds\nM a[140:143],a[36:39],a[12:15],a[140:143]\ns_add_u32 m0,s22,33664\nM a[136:139],a[32:35],a[12:15],a[136:139]\nM a[152:155],a[40:43],a[12:15],a[152:155]\n" \
"M a[156:159],a[44:47],a[12:15],a[156:159]\nbuffer_load_dwordx4 v21,s[32:35],s47 offen offset:128 lds\nM a[148:151],a[44:47],a[8:11],a[148:151]\ns_waitcnt vmcnt(30) lgkmcnt(0)\nM a[144:147],a[40:43],a[8:11],a[144:147]\ns_add_u32 m0,s22,34816\ns_barrier\nds_read_b64_tr_b16 a[32:33],v9 offset:0\n" \
"M a[160:163],a[48:51],a[8:11],a[160:163]\nds_read_b64_tr_b16 a[34:35],v10 offset:0\nds_read_b64_tr_b16 a[36:37],v11 offset:0\nM a[164:167],a[52:55],a[8:11],a[164:167]\nbuffer_load_dwordx4 v21,s[92:95],s47 offen offset:0 lds\nM a[172:175],a[52:55],a[12:15],a[172:175]\ns_add_u32 m0,s22,35712\nds_read_b64_tr_b16 a[38:39],v12 offset:0\n" \
"M a[168:171],a[48:51],a[12:15],a[168:171]\nds_read_b64_tr_b16 a[40:41],v9 offset:512\nM a[184:187],a[56:59],a[12:15],a[184:187]\nds_read_b64_tr_b16 a[42:43],v10 offset:512\nM a[188:191],a[60:63],a[12:15],a[188:191]\nbuffer_load_dwordx4 v21,s[92:95],s47 offen offset:128 lds\nM a[180:183],a[60:63],a[8:11],a[180:183]\ns_add_u32 m0,s22,49152\n" \
"ds_read_b64_tr_b16 a[44:45],v11 offset:512\nM a[176:179],a[56:59],a[8:11],a[176:179]\ns_mov_b32 s47,2883584\nds_read_b64_tr_b16 a[46:47],v12 offset:512\nM v[128:131],a[96:99],a[88:91],v[128:131]\nds_read_b64_tr_b16 a[48:49],v9 offset:1024\nds_read_b64_tr_b16 a[50:51],v10 offset:1024\nM v[132:135],a[100:103],a[88:91],v[132:135]\n" \
"buffer_load_dwordx4 v21,s[32:35],s47 offen offset:0 lds\nM v[140:143],a[100:103],a[92:95],v[140:143]\ns_add_u32 m0,s22,50048\nM v[136:139],a[96:99],a[92:95],v[136:139]\nds_read_b64_tr_b16 a[52:53],v11 offset:1024\nds_read_b64_tr_b16 a[54:55],v12 offset:1024\nM v[152:155],a[104:107],a[92:95],v[152:155]\nds_read_b64_tr_b16 a[56:57],v9 offset:1536\n" \
"M v[156:159],a[108:111],a[92:95],v[156:159]\nbuffer_load_dwordx4 v21,s[32:35],s47 offen offset:128 lds\nM v[148:151],a[108:111],a[88:91],v[148:151]\ns_add_u32 m0,s22,51200\nds_read_b64_tr_b16 a[58:59],v10 offset:1536\nM v[144:147],a[104:107],a[88:91],v[144:147]\ns_waitcnt vmcnt(32)\nds_read_b64_tr_b16 a[60:61],v11 offset:1536\n" \
"v_lshlrev_b32 v25,16,v46\nM v[160:163],a[112:115],a[88:91],v[160:163]\nds_read_b64_tr_b16 a[62:63],v12 offset:1536\nv_lshlrev_b32 v26,16,v30\nM v[164:167],a[116:119],a[88:91],v[164:167]\nbuffer_load_dwordx4 v21,s[92:95],s47 offen offset:0 lds\nM v[172:175],a[116:119],a[92:95],v[172:175]\ns_add_u32 m0,s22,52096\n" \
"ds_read_b128 a[24:27],v5 offset:12288\nv_and_b32 v46,0xffff0000,v46\nM v[168:171],a[112:115],a[92:95],v[168:171]\nds_read_b128 a[28:31],v5 offset:14336\nv_mul_f32 v23,v26,v25\nM v[184:187],a[120:123],a[92:95],v[184:187]\nds_read_b128 a[16:19],v5 offset:8192\nv_and_b32 v30,0xffff0000,v30\n" \
"M v[188:191],a[124:127],a[92:95],v[188:191]\nds_read_b128 a[20:23],v5 offset:10240\nv_lshlrev_b32 v26,16,v38\nM v[180:183],a[124:127],a[88:91],v[180:183]\nbuffer_load_dwordx4 v21,s[92:95],s47 offen offset:128 lds\nM v[176:179],a[120:123],a[88:91],v[176:179]\ns_add_u32 m0,s50,65536\nv_fmac_f32 v23,v30,v46\n" \
"v_mul_f32 v24,v26,v25\nM v[64:67],a[96:99],a[80:83],v[64:67]\ns_mov_b32 s47,3145728\nds_read_b128 a[0:3],v5 offset:0\nv_lshlrev_b32 v25,16,v47\nM v[68:71],a[100:103],a[80:83],v[68:71]\nds_read_b128 a[4:7],v5 offset:2048\nv_lshlrev_b32 v26,16,v31\n" \
"M v[76:79],a[100:103],a[84:87],v[76:79]\nbuffer_load_dwordx4 v13,s[28:31],s46 offen lds\nM v[72:75],a[96:99],a[84:87],v[72:75]\ns_add_u32 m0,s50,69632\nds_read_b128 a[8:11],v5 offset:4096\nv_and_b32 v38,0xffff0000,v38\nM v[88:91],a[104:107],a[84:87],v[88:91]\nds_read_b128 a[12:15],v5 offset:6144\n" \
"v_fmac_f32 v23,v26,v25\nM v[92:95],a[108:111],a[84:87],v[92:95]\nds_read_b128 a[88:91],v6 offset:12288\nv_fmac_f32 v24,v38,v46\nM v[84:87],a[108:111],a[80:83],v[84:87]\nds_read_b128 a[92:95],v6 offset:14336\nv_and_b32 v47,0xffff0000,v47\nM v[80:83],a[104:107],a[80:83],v[80:83]\n" \
"buffer_load_dwordx4 v14,s[28:31],s46 offen lds\nM v[96:99],a[112:115],a[80:83],v[96:99]\ns_add_u32 m0,s50,73728\nv_and_b32 v31,0xffff0000,v31\nv_lshlrev_b32 v26,16,v39\nM v[100:103],a[116:119],a[80:83],v[100:103]\nv_fmac_f32 v23,v31,v47\nv_fmac_f32 v24,v26,v25\n" \
"M v[108:111],a[116:119],a[84:87],v[108:111]\nv_lshlrev_b32 v25,16,v48\nv_lshlrev_b32 v26,16,v32\nM v[104:107],a[112:115],a[84:87],v[104:107]\nbuffer_load_dwordx4 v15,s[28:31],s46 offen lds\nM v[120:123],a[120:123],a[84:87],v[120:123]\ns_add_u32 m0,s50,77824\nv_and_b32 v39,0xffff0000,v39\n" \
"v_fmac_f32 v23,v26,v25\nM v[124:127],a[124:127],a[84:87],v[124:127]\nv_fmac_f32 v24,v39,v47\nv_and_b32 v48,0xffff0000,v48\nM v[116:119],a[124:127],a[80:83],v[116:119]\nv_and_b32 v32,0xffff0000,v32\nv_lshlrev_b32 v26,16,v40\nM v[112:115],a[120:123],a[80:83],v[112:115]\n" \
"ds_read_b128 a[80:83],v6 offset:8192\nds_read_b128 a[84:87],v6 offset:10240\nM v[192:195],a[96:99],a[64:67],v[192:195]\nbuffer_load_dwordx4 v16,s[28:31],s46 offen lds\nM v[196:199],a[100:103],a[64:67],v[196:199]\ns_add_u32 m0,s50,81920\nv_fmac_f32 v23,v32,v48\nv_fmac_f32 v24,v26,v25\n" \
"M v[204:207],a[100:103],a[68:71],v[204:207]\ns_mov_b32 s46,768\nv_lshlrev_b32 v25,16,v49\nv_lshlrev_b32 v26,16,v33\nM v[200:203],a[96:99],a[68:71],v[200:203]\nv_and_b32 v40,0xffff0000,v40\nv_fmac_f32 v23,v26,v25\nM v[216:219],a[104:107],a[68:71],v[216:219]\n" \
"buffer_load_dwordx4 v17,s[28:31],s46 offen lds\nM v[220:223],a[108:111],a[68:71],v[220:223]\ns_add_u32 m0,s50,86016\nv_fmac_f32 v24,v40,v48\nv_and_b32 v49,0xffff0000,v49\nM v[212:215],a[108:111],a[64:67],v[212:215]\nv_and_b32 v33,0xffff0000,v33\nv_lshlrev_b32 v26,16,v41\n" \
"M v[208:211],a[104:107],a[64:67],v[208:211]\nv_fmac_f32 v23,v33,v49\nv_fmac_f32 v24,v26,v25\nM v[224:227],a[112:115],a[64:67],v[224:227]\nbuffer_load_dwordx2 a[206:207],v59,s[40:43],s52 offen offset:0 nt\nM v[228:231],a[116:119],a[64:67],v[228:231]\nv_lshlrev_b32 v25,16,v50\nv_lshlrev_b32 v26,16,v34\n" \
"M v[236:239],a[116:119],a[68:71],v[236:239]\nv_and_b32 v41,0xffff0000,v41\nv_fmac_f32 v23,v26,v25\nM v[232:235],a[112:115],a[68:71],v[232:235]\nv_fmac_f32 v24,v41,v49\nv_and_b32 v50,0xffff0000,v50\nM v[248:251],a[120:123],a[68:71],v[248:251]\nbuffer_load_dwordx2 a[222:223],v59,s[80:83],s52 offen offset:0 nt\n" \
"M v[252:255],a[124:127],a[68:71],v[252:255]\nv_and_b32 v34,0xffff0000,v34\nv_lshlrev_b32 v26,16,v42\nM v[244:247],a[124:127],a[64:67],v[244:247]\nv_fmac_f32 v23,v34,v50\nv_fmac_f32 v24,v26,v25\nM v[240:243],a[120:123],a[64:67],v[240:243]\nds_read_b128 a[64:67],v6 offset:0\n" \
"ds_read_b128 a[68:71],v6 offset:2048\nM a[128:131],a[96:99],a[72:75],a[128:131]\nbuffer_load_dwordx2 a[208:209],v59,s[40:43],s52 offen offset:2048 nt\nM a[132:135],a[100:103],a[72:75],a[132:135]\nv_lshlrev_b32 v25,16,v51\nv_lshlrev_b32 v26,16,v35\nM a[140:143],a[100:103],a[76:79],a[140:143]\nv_and_b32 v42,0xffff0000,v42\n" \
"v_fmac_f32 v23,v26,v25\nM a[136:139],a[96:99],a[76:79],a[136:139]\nv_fmac_f32 v24,v42,v50\nv_and_b32 v51,0xffff0000,v51\nM a[152:155],a[104:107],a[76:79],a[152:155]\nbuffer_load_dwordx2 a[224:225],v59,s[80:83],s52 offen offset:2048 nt\nM a[156:159],a[108:111],a[76:79],a[156:159]\ns_mov_b32 s52,12288\n" \
"v_and_b32 v35,0xffff0000,v35\nv_lshlrev_b32 v26,16,v43\nM a[148:151],a[108:111],a[72:75],a[148:151]\ns_waitcnt vmcnt(25) lgkmcnt(14)\nv_fmac_f32 v23,v35,v51\nv_fmac_f32 v24,v26,v25\nM a[144:147],a[104:107],a[72:75],a[144:147]\ns_barrier\n" \
"ds_read_b64_tr_b16 a[96:97],v9 offset:16384\nM a[160:163],a[112:115],a[72:75],a[160:163]\nbuffer_load_dwordx2 a[210:211],v59,s[40:43],s52 offen offset:0 nt\nM a[164:167],a[116:119],a[72:75],a[164:167]\nds_read_b64_tr_b16 a[98:99],v10 offset:16384\nds_read_b64_tr_b16 a[100:101],v11 offset:16384\nM a[172:175],a[116:119],a[76:79],a[172:175]\nds_read_b64_tr_b16 a[102:103],v12 offset:16384\n" \
"v_lshlrev_b32 v25,16,v52\nM a[168:171],a[112:115],a[76:79],a[168:171]\nds_read_b64_tr_b16 a[104:105],v9 offset:16896\nds_read_b64_tr_b16 a[106:107],v10 offset:16896\nM a[184:187],a[120:123],a[76:79],a[184:187]\nbuffer_load_dwordx2 a[226:227],v59,s[80:83],s52 offen offset:0 nt\nM a[188:191],a[124:127],a[76:79],a[188:191]\nds_read_b64_tr_b16 a[108:109],v11 offset:16896\n" \
"ds_read_b64_tr_b16 a[110:111],v12 offset:16896\nM a[180:183],a[124:127],a[72:75],a[180:183]\ns_waitcnt vmcnt(31)\nds_read_b64_tr_b16 a[112:113],v9 offset:17408\nv_lshlrev_b32 v26,16,v36\nM a[176:179],a[120:123],a[72:75],a[176:179]\ns_waitcnt lgkmcnt(6)\nds_read_b128 a[72:75],v6 offset:4096\n" \
"v_and_b32 v43,0xffff0000,v43\nM v[128:131],a[32:35],a[24:27],v[128:131]\nbuffer_load_dwordx2 a[212:213],v59,s[40:43],s52 offen offset:2048 nt\nM v[132:135],a[36:39],a[24:27],v[132:135]\nds_read_b128 a[76:79],v6 offset:6144\nds_read_b64_tr_b16 a[114:115],v10 offset:17408\nM v[140:143],a[36:39],a[28:31],v[140:143]\nds_read_b64_tr_b16 a[116:117],v11 offset:17408\n" \
"v_fmac_f32 v23,v26,v25\nM v[136:139],a[32:35],a[28:31],v[136:139]\nds_read_b64_tr_b16 a[118:119],v12 offset:17408\nv_fmac_f32 v24,v43,v51\nM v[152:155],a[40:43],a[28:31],v[152:155]\nbuffer_load_dwordx2 a[228:229],v59,s[80:83],s52 offen offset:2048 nt\nM v[156:159],a[44:47],a[28:31],v[156:159]\nds_read_b64_tr_b16 a[120:121],v9 offset:17920\n" \
"ds_read_b64_tr_b16 a[122:123],v10 offset:17920\nM v[148:151],a[44:47],a[24:27],v[148:151]\nds_read_b64_tr_b16 a[124:125],v11 offset:17920\nv_and_b32 v52,0xffff0000,v52\nM v[144:147],a[40:43],a[24:27],v[144:147]\nds_read_b64_tr_b16 a[126:127],v12 offset:17920\nv_and_b32 v36,0xffff0000,v36\nM v[160:163],a[48:51],a[24:27],v[160:163]\n" \
"s_mov_b64 exec,0xff\nglobal_load_dword v0,v1,s[84:85]\ns_mov_b64 exec,-1\nM v[164:167],a[52:55],a[24:27],v[164:167]\ns_add_u32 s84,s84,524288\ns_addc_u32 s85,s85,0\nv_lshlrev_b32 v26,16,v44\nv_fmac_f32 v23,v36,v52\n" \
"M v[172:175],a[52:55],a[28:31],v[172:175]\ns_mov_b64 exec,0xff\nglobal_load_dword v0,v1,s[84:85]\ns_mov_b64 exec,-1\nM v[168:171],a[48:51],a[28:31],v[168:171]\nv_fmac_f32 v24,v26,v25\nv_lshlrev_b32 v25,16,v53\nM v[184:187],a[56:59],a[28:31],v[184:187]\n" \
"s_mov_b64 exec,0xff\nglobal_load_dword v0,v1,s[76:77]\ns_mov_b64 exec,-1\nM v[188:191],a[60:63],a[28:31],v[188:191]\nv_lshlrev_b32 v26,16,v37\nv_and_b32 v44,0xffff0000,v44\nM v[180:183],a[60:63],a[24:27],v[180:183]\ns_mov_b64 exec,0xff00\n" \
"global_load_dword v0,v1,s[76:77]\ns_mov_b64 exec,-1\nM v[176:179],a[56:59],a[24:27],v[176:179]\ns_add_u32 s76,s76,2097152\ns_addc_u32 s77,s77,0\nv_fmac_f32 v23,v26,v25\nv_fmac_f32 v24,v44,v52\nM v[64:67],a[32:35],a[16:19],v[64:67]\n" \
"v_lshlrev_b32 v26,16,v45\nv_and_b32 v53,0xffff0000,v53\nM v[68:71],a[36:39],a[16:19],v[68:71]\nv_and_b32 v45,0xffff0000,v45\nv_fmac_f32 v24,v26,v25\nM v[76:79],a[36:39],a[20:23],v[76:79]\nv_fmac_f32 v24,v45,v53\nv_and_b32 v37,0xffff0000,v37\n" \
"M v[72:75],a[32:35],a[20:23],v[72:75]\nv_accvgpr_read_b32 v45,a231\nv_fmac_f32 v23,v37,v53\nM v[88:91],a[40:43],a[20:23],v[88:91]\nv_accvgpr_read_b32 v49,a233\nv_lshlrev_b32 v44,16,v45\nM v[92:95],a[44:47],a[20:23],v[92:95]\nv_accvgpr_read_b32 v36,a192\n" \
"v_accvgpr_read_b32 v43,a230\nM v[84:87],a[44:47],a[16:19],v[84:87]\nv_accvgpr_read_b32 v37,a194\nv_lshlrev_b32 v48,16,v49\nM v[80:83],a[40:43],a[16:19],v[80:83]\nv_lshlrev_b32 v30,16,v36\nv_and_b32 v45,0xffff0000,v45\nM v[96:99],a[48:51],a[16:19],v[96:99]\n" \
"v_accvgpr_read_b32 v47,a232\nv_lshlrev_b32 v31,16,v37\nM v[100:103],a[52:55],a[16:19],v[100:103]\nv_accvgpr_read_b32 v38,a196\nv_lshlrev_b32 v42,16,v43\nM v[108:111],a[52:55],a[20:23],v[108:111]\nv_and_b32 v49,0xffff0000,v49\nv_and_b32 v36,0xffff0000,v36\n" \
"M v[104:107],a[48:51],a[20:23],v[104:107]\nv_accvgpr_read_b32 v39,a198\nv_mul_f32 v25,v30,v45\nM v[120:123],a[56:59],a[20:23],v[120:123]\nv_lshlrev_b32 v46,16,v47\nv_and_b32 v37,0xffff0000,v37\nM v[124:127],a[60:63],a[20:23],v[124:127]\nv_lshlrev_b32 v32,16,v38\n" \
"v_and_b32 v43,0xffff0000,v43\nM v[116:119],a[60:63],a[16:19],v[116:119]\nv_mul_f32 v26,v36,v49\nv_lshlrev_b32 v33,16,v39\nM v[112:115],a[56:59],a[16:19],v[112:115]\nv_fmac_f32 v25,v31,v44\nv_and_b32 v47,0xffff0000,v47\nM v[192:195],a[32:35],a[0:3],v[192:195]\n" \
"v_and_b32 v38,0xffff0000,v38\nv_fmac_f32 v26,v37,v48\nM v[196:199],a[36:39],a[0:3],v[196:199]\nv_and_b32 v39,0xffff0000,v39\nv_fmac_f32 v25,v32,v43\nM v[204:207],a[36:39],a[4:7],v[204:207]\nv_fmac_f32 v26,v38,v47\nv_fmac_f32 v25,v33,v42\n" \
"M v[200:203],a[32:35],a[4:7],v[200:203]\nv_fmac_f32 v26,v39,v46\nv_accvgpr_read_b32 v28,a214\nM v[216:219],a[40:43],a[4:7],v[216:219]\nC v26,v25,v26\nv_lshlrev_b32 v27,16,v28\nM v[220:223],a[44:47],a[4:7],v[220:223]\nv_lshlrev_b32 v25,16,v26\n" \
"v_and_b32 v28,0xffff0000,v28\nM v[212:215],a[44:47],a[0:3],v[212:215]\nv_and_b32 v26,0xffff0000,v26\nv_mul_f32 v29,v25,v27\nM v[208:211],a[40:43],a[0:3],v[208:211]\nv_mul_f32 v58,v26,v28\nv_accvgpr_read_b32 v40,a200\nM v[224:227],a[48:51],a[0:3],v[224:227]\n" \
"v_mul_f32 v25,v31,v45\nv_mul_f32 v26,v37,v49\nM v[228:231],a[52:55],a[0:3],v[228:231]\nv_lshlrev_b32 v34,16,v40\nv_fmac_f32 v25,v32,v44\nM v[236:239],a[52:55],a[4:7],v[236:239]\nv_fmac_f32 v26,v38,v48\nv_fmac_f32 v25,v33,v43\n" \
"M v[232:235],a[48:51],a[4:7],v[232:235]\nv_and_b32 v40,0xffff0000,v40\nv_fmac_f32 v26,v39,v47\nM v[248:251],a[56:59],a[4:7],v[248:251]\nv_fmac_f32 v25,v34,v42\nv_fmac_f32 v26,v40,v46\nM v[252:255],a[60:63],a[4:7],v[252:255]\nv_accvgpr_read_b32 v28,a216\n" \
"C v26,v25,v26\nM v[244:247],a[60:63],a[0:3],v[244:247]\nv_lshlrev_b32 v27,16,v28\nv_lshlrev_b32 v25,16,v26\nM v[240:243],a[56:59],a[0:3],v[240:243]\nv_and_b32 v28,0xffff0000,v28\nv_and_b32 v26,0xffff0000,v26\nM a[128:131],a[32:35],a[8:11],a[128:131]\n" \
"v_mul_f32 v25,v25,v27\nv_mul_f32 v26,v26,v28\nM a[132:135],a[36:39],a[8:11],a[132:135]\nC v53,v25,v29\nC v57,v26,v58\nM a[140:143],a[36:39],a[12:15],a[140:143]\nv_accvgpr_read_b32 v41,a202\nv_lshlrev_b32 v35,16,v41\n" \
"M a[136:139],a[32:35],a[12:15],a[136:139]\nv_mul_f32 v25,v32,v45\nv_mul_f32 v26,v38,v49\nM a[152:155],a[40:43],a[12:15],a[152:155]\nv_fmac_f32 v25,v33,v44\nv_and_b32 v41,0xffff0000,v41\nM a[156:159],a[44:47],a[12:15],a[156:159]\nv_fmac_f32 v26,v39,v48\n" \
"v_fmac_f32 v25,v34,v43\nM a[148:151],a[44:47],a[8:11],a[148:151]\ns_waitcnt vmcnt(21) lgkmcnt(0)\nv_fmac_f32 v26,v40,v47\nv_fmac_f32 v25,v35,v42\nM a[144:147],a[40:43],a[8:11],a[144:147]\ns_barrier\nds_read_b64_tr_b16 a[32:33],v9 offset:32768\n" \
"M a[160:163],a[48:51],a[8:11],a[160:163]\nbuffer_load_dwordx4 v18,s[28:31],s46 offen lds\nM a[164:167],a[52:55],a[8:11],a[164:167]\ns_add_u32 m0,s50,90112\nds_read_b64_tr_b16 a[34:35],v10 offset:32768\nv_fmac_f32 v26,v41,v46\nM a[172:175],a[52:55],a[12:15],a[172:175]\nds_read_b64_tr_b16 a[36:37],v11 offset:32768\n" \
"ds_read_b64_tr_b16 a[38:39],v12 offset:32768\nM a[168:171],a[48:51],a[12:15],a[168:171]\nds_read_b64_tr_b16 a[40:41],v9 offset:33280\nv_accvgpr_read_b32 v28,a218\nM a[184:187],a[56:59],a[12:15],a[184:187]\nbuffer_load_dwordx4 v19,s[28:31],s46 offen lds\nM a[188:191],a[60:63],a[12:15],a[188:191]\ns_add_u32 m0,s50,94208\n" \
"ds_read_b64_tr_b16 a[42:43],v10 offset:33280\nC v26,v25,v26\nM a[180:183],a[60:63],a[8:11],a[180:183]\nds_read_b64_tr_b16 a[44:45],v11 offset:33280\nv_lshlrev_b32 v27,16,v28\nM a[176:179],a[56:59],a[8:11],a[176:179]\nds_read_b64_tr_b16 a[46:47],v12 offset:33280\nv_lshlrev_b32 v25,16,v26\n" \
"M v[128:131],a[96:99],a[88:91],v[128:131]\nbuffer_load_dwordx4 v20,s[28:31],s46 offen lds\nM v[132:135],a[100:103],a[88:91],v[132:135]\ns_add_u32 m0,s22,0\nds_read_b64_tr_b16 a[48:49],v9 offset:33792\nv_and_b32 v28,0xffff0000,v28\nM v[140:143],a[100:103],a[92:95],v[140:143]\ns_mov_b32 s46,896\n" \
"ds_read_b64_tr_b16 a[50:51],v10 offset:33792\nv_and_b32 v26,0xffff0000,v26\nM v[136:139],a[96:99],a[92:95],v[136:139]\nds_read_b64_tr_b16 a[52:53],v11 offset:33792\nds_read_b64_tr_b16 a[54:55],v12 offset:33792\nM v[152:155],a[104:107],a[92:95],v[152:155]\nbuffer_load_dwordx4 v21,s[32:35],s47 offen offset:0 lds\nM v[156:159],a[108:111],a[92:95],v[156:159]\n" \
"s_add_u32 m0,s22,896\nv_mul_f32 v29,v25,v27\nv_mul_f32 v58,v26,v28\nM v[148:151],a[108:111],a[88:91],v[148:151]\nds_read_b64_tr_b16 a[56:57],v9 offset:34304\nds_read_b64_tr_b16 a[58:59],v10 offset:34304\nM v[144:147],a[104:107],a[88:91],v[144:147]\nds_read_b64_tr_b16 a[60:61],v11 offset:34304\n" \
"v_accvgpr_read_b32 v36,a204\nM v[160:163],a[112:115],a[88:91],v[160:163]\nbuffer_load_dwordx4 v21,s[32:35],s47 offen offset:128 lds\nM v[164:167],a[116:119],a[88:91],v[164:167]\ns_add_u32 m0,s22,2048\nds_read_b64_tr_b16 a[62:63],v12 offset:34304\nv_mul_f32 v25,v33,v45\nM v[172:175],a[116:119],a[92:95],v[172:175]\n" \
"ds_read_b128 a[24:27],v5 offset:45056\nv_lshlrev_b32 v30,16,v36\nM v[168:171],a[112:115],a[92:95],v[168:171]\nds_read_b128 a[28:31],v5 offset:47104\nv_mul_f32 v26,v39,v49\nM v[184:187],a[120:123],a[92:95],v[184:187]\nbuffer_load_dwordx4 v21,s[92:95],s47 offen offset:0 lds\nM v[188:191],a[124:127],a[92:95],v[188:191]\n" \
"s_add_u32 m0,s22,2944\nv_fmac_f32 v25,v34,v44\nv_fmac_f32 v26,v40,v48\nM v[180:183],a[124:127],a[88:91],v[180:183]\nds_read_b128 a[16:19],v5 offset:40960\nv_and_b32 v36,0xffff0000,v36\nM v[176:179],a[120:123],a[88:91],v[176:179]\nds_read_b128 a[20:23],v5 offset:43008\n" \
"v_fmac_f32 v25,v35,v43\nM v[64:67],a[96:99],a[80:83],v[64:67]\nbuffer_load_dwordx4 v21,s[92:95],s47 offen offset:128 lds\nM v[68:71],a[100:103],a[80:83],v[68:71]\ns_add_u32 m0,s22,16384\nds_read_b128 a[0:3],v5 offset:32768\nv_fmac_f32 v26,v41,v47\nM v[76:79],a[100:103],a[84:87],v[76:79]\n" \
"s_mov_b32 s47,3407872\nds_read_b128 a[4:7],v5 offset:34816\nv_fmac_f32 v25,v30,v42\nM v[72:75],a[96:99],a[84:87],v[72:75]\nds_read_b128 a[8:11],v5 offset:36864\nv_fmac_f32 v26,v36,v46\nM v[88:91],a[104:107],a[84:87],v[88:91]\nbuffer_load_dwordx4 v21,s[32:35],s47 offen offset:0 lds\n" \
"M v[92:95],a[108:111],a[84:87],v[92:95]\ns_add_u32 m0,s22,17280\nv_accvgpr_read_b32 v28,a220\nC v26,v25,v26\nM v[84:87],a[108:111],a[80:83],v[84:87]\nds_read_b128 a[12:15],v5 offset:38912\nv_lshlrev_b32 v27,16,v28\nM v[80:83],a[104:107],a[80:83],v[80:83]\n" \
"ds_read_b128 a[88:91],v6 offset:45056\nv_lshlrev_b32 v25,16,v26\nM v[96:99],a[112:115],a[80:83],v[96:99]\nbuffer_load_dwordx4 v21,s[32:35],s47 offen offset:128 lds\nM v[100:103],a[116:119],a[80:83],v[100:103]\ns_add_u32 m0,s22,18432\nds_read_b128 a[92:95],v6 offset:47104\nv_and_b32 v28,0xffff0000,v28\n" \
"M v[108:111],a[116:119],a[84:87],v[108:111]\nv_and_b32 v26,0xffff0000,v26\nv_mul_f32 v25,v25,v27\nM v[104:107],a[112:115],a[84:87],v[104:107]\nv_mul_f32 v26,v26,v28\nC v52,v25,v29\nM v[120:123],a[120:123],a[84:87],v[120:123]\nbuffer_load_dwordx4 v21,s[92:95],s47 offen offset:0 lds\n" \
"M v[124:127],a[124:127],a[84:87],v[124:127]\ns_add_u32 m0,s22,19328\nC v56,v26,v58\nv_mov_b32 v59,s19\nM v[116:119],a[124:127],a[80:83],v[116:119]\nv_permlane32_swap_b32 v23,v24\nM v[112:115],a[120:123],a[80:83],v[112:115]\nds_read_b128 a[80:83],v6 offset:40960\n" \
"ds_read_b128 a[84:87],v6 offset:43008\nM v[192:195],a[96:99],a[64:67],v[192:195]\nbuffer_load_dwordx4 v21,s[92:95],s47 offen offset:128 lds\nM v[196:199],a[100:103],a[64:67],v[196:199]\ns_add_u32 m0,s50,0\nv_add_f32 v23,v23,v24\nv_mov_b32 v24,v23\nM v[204:207],a[100:103],a[68:71],v[204:207]\n" \
"s_mov_b32 s47,3670016\nv_permlane16_swap_b32 v23,v24\nM v[200:203],a[96:99],a[68:71],v[200:203]\nv_add_f32 v23,v23,v24\ns_nop 1\nv_add_f32 v23,v23,v23 row_ror:8 row_mask:0xf bank_mask:0xf\nM v[216:219],a[104:107],a[68:71],v[216:219]\nbuffer_load_dwordx4 v13,s[28:31],s46 offen lds\n" \
"M v[220:223],a[108:111],a[68:71],v[220:223]\ns_add_u32 m0,s50,4096\nv_add_u32 v13,s60,v63\nv_add_f32 v23,v23,v23 row_ror:4 row_mask:0xf bank_mask:0xf\nM v[212:215],a[108:111],a[64:67],v[212:215]\ns_nop 0\nv_add_f32 v23,v23,v23 row_ror:2 row_mask:0xf bank_mask:0xf\ns_nop 1\n" \
"v_add_f32 v23,v23,v23 row_ror:1 row_mask:0xf bank_mask:0xf\nM v[208:211],a[104:107],a[64:67],v[208:211]\nC v23,v23,v23\nv_and_b32 v23,0xffff0000,v23\nM v[224:227],a[112:115],a[64:67],v[224:227]\nLG2_%=:\ns_cbranch_execz LHR0_%=\nbuffer_load_dwordx4 v14,s[28:31],s46 offen lds\n" \
"M v[228:231],a[116:119],a[64:67],v[228:231]\ns_add_u32 m0,s50,8192\nv_add_u32 v14,0x10000,v13\nv_mul_f32 v23,0xbfb8aa3b,v23\nM v[236:239],a[116:119],a[68:71],v[236:239]\nv_exp_f32 v23,v23\nM v[232:235],a[112:115],a[68:71],v[232:235]\nv_add_f32 v23,1.0,v23\n" \
"v_add_u32 v24,992,v4\nM v[248:251],a[120:123],a[68:71],v[248:251]\nbuffer_load_dwordx4 v15,s[28:31],s46 offen lds\nM v[252:255],a[124:127],a[68:71],v[252:255]\ns_add_u32 m0,s50,12288\nv_add_u32 v15,0x10000,v14\nM v[244:247],a[124:127],a[64:67],v[244:247]\nv_rcp_f32 v23,v23\n" \
"M v[240:243],a[120:123],a[64:67],v[240:243]\nds_read_b128 a[64:67],v6 offset:32768\nds_read_b128 a[68:71],v6 offset:34816\nM a[128:131],a[96:99],a[72:75],a[128:131]\nbuffer_load_dwordx4 v16,s[28:31],s46 offen lds\nM a[132:135],a[100:103],a[72:75],a[132:135]\ns_add_u32 m0,s50,16384\nv_add_u32 v16,0x10000,v15\n" \
"C v23,v23,v23\nM a[140:143],a[100:103],a[76:79],a[140:143]\ns_mov_b32 s46,896\nv_and_b32 v23,0xffff0000,v23\nM a[136:139],a[96:99],a[76:79],a[136:139]\nM a[152:155],a[104:107],a[76:79],a[152:155]\nbuffer_load_dwordx4 v17,s[28:31],s46 offen lds\nM a[156:159],a[108:111],a[76:79],a[156:159]\n" \
"s_add_u32 m0,s50,20480\nv_add_u32 v17,s61,v63\nv_mov_b32 v63,0x3f803f80\nM a[148:151],a[108:111],a[72:75],a[148:151]\ns_waitcnt vmcnt(33) lgkmcnt(14)\nM a[144:147],a[104:107],a[72:75],a[144:147]\ns_barrier\nds_read_b64_tr_b16 a[96:97],v9 offset:49152\n" \
"M a[160:163],a[112:115],a[72:75],a[160:163]\nbuffer_store_dword v23,v61,s[36:39],0 offen sc1\nM a[164:167],a[116:119],a[72:75],a[164:167]\nds_read_b64_tr_b16 a[98:99],v10 offset:49152\nds_read_b64_tr_b16 a[100:101],v11 offset:49152\nM a[172:175],a[116:119],a[76:79],a[172:175]\nbuffer_load_dwordx4 v18,s[28:31],s46 offen lds\nM a[168:171],a[112:115],a[76:79],a[168:171]\n" \
"s_add_u32 m0,s50,24576\nds_read_b64_tr_b16 a[102:103],v12 offset:49152\nv_add_u32 v18,0x10000,v17\nM a[184:187],a[120:123],a[76:79],a[184:187]\nds_read_b64_tr_b16 a[104:105],v9 offset:49664\nv_add_u32 v23,-32,v4\nM a[188:191],a[124:127],a[76:79],a[188:191]\nds_read_b64_tr_b16 a[106:107],v10 offset:49664\n" \
"M a[180:183],a[124:127],a[72:75],a[180:183]\ns_waitcnt lgkmcnt(5)\nds_read_b64_tr_b16 a[108:109],v11 offset:49664\nds_read_b64_tr_b16 a[110:111],v12 offset:49664\nM a[176:179],a[120:123],a[72:75],a[176:179]\nbuffer_load_dwordx4 v19,s[28:31],s46 offen lds\nM v[128:131],a[32:35],a[24:27],v[128:131]\ns_add_u32 m0,s50,28672\n" \
"v_add_u32 v19,0x10000,v18\nM v[132:135],a[36:39],a[24:27],v[132:135]\nds_read_b128 a[72:75],v6 offset:36864\nM v[140:143],a[36:39],a[28:31],v[140:143]\nds_read_b128 a[76:79],v6 offset:38912\nM v[136:139],a[32:35],a[28:31],v[136:139]\nbuffer_load_dwordx4 v20,s[28:31],s46 offen lds\nM v[152:155],a[40:43],a[28:31],v[152:155]\n" \
"s_add_u32 m0,s22,32768\nds_read_b64_tr_b16 a[112:113],v9 offset:50176\nv_add_u32 v20,0x10000,v19\nM v[156:159],a[44:47],a[28:31],v[156:159]\ns_mov_b64 s[28:29],s[64:65]\nds_read_b64_tr_b16 a[114:115],v10 offset:50176\nM v[148:151],a[44:47],a[24:27],v[148:151]\ns_and_b32 s29,s29,0xffff\n" \
"ds_read_b64_tr_b16 a[116:117],v11 offset:50176\nds_read_b64_tr_b16 a[118:119],v12 offset:50176\nM v[144:147],a[40:43],a[24:27],v[144:147]\ns_mov_b32 s30,4194304\nds_read_b64_tr_b16 a[120:121],v9 offset:50688\nM v[160:163],a[48:51],a[24:27],v[160:163]\nbuffer_load_dwordx4 v21,s[32:35],s47 offen offset:0 lds\nM v[164:167],a[52:55],a[24:27],v[164:167]\n" \
"s_add_u32 m0,s22,33664\nds_read_b64_tr_b16 a[122:123],v10 offset:50688\nM v[172:175],a[52:55],a[28:31],v[172:175]\ns_mov_b32 s46,0\nds_read_b64_tr_b16 a[124:125],v11 offset:50688\nM v[168:171],a[48:51],a[28:31],v[168:171]\nds_read_b64_tr_b16 a[126:127],v12 offset:50688\nM v[184:187],a[56:59],a[28:31],v[184:187]\n" \
"buffer_load_dwordx4 v21,s[32:35],s47 offen offset:128 lds\nM v[188:191],a[60:63],a[28:31],v[188:191]\ns_add_u32 m0,s22,34816\nM v[180:183],a[60:63],a[24:27],v[180:183]\nM v[176:179],a[56:59],a[24:27],v[176:179]\nM v[64:67],a[32:35],a[16:19],v[64:67]\nM v[68:71],a[36:39],a[16:19],v[68:71]\nbuffer_load_dwordx4 v21,s[92:95],s47 offen offset:0 lds\n" \
"M v[76:79],a[36:39],a[20:23],v[76:79]\ns_add_u32 m0,s22,35712\nM v[72:75],a[32:35],a[20:23],v[72:75]\nM v[88:91],a[40:43],a[20:23],v[88:91]\nM v[92:95],a[44:47],a[20:23],v[92:95]\nbuffer_load_dwordx4 v21,s[92:95],s47 offen offset:128 lds\nM v[84:87],a[44:47],a[16:19],v[84:87]\ns_add_u32 m0,s22,49152\n" \
"M v[80:83],a[40:43],a[16:19],v[80:83]\ns_mov_b32 s47,3932160\nM v[96:99],a[48:51],a[16:19],v[96:99]\nM v[100:103],a[52:55],a[16:19],v[100:103]\nM v[108:111],a[52:55],a[20:23],v[108:111]\nM v[104:107],a[48:51],a[20:23],v[104:107]\nM v[120:123],a[56:59],a[20:23],v[120:123]\nM v[124:127],a[60:63],a[20:23],v[124:127]\n" \
"M v[116:119],a[60:63],a[16:19],v[116:119]\nM v[112:115],a[56:59],a[16:19],v[112:115]\nM v[192:195],a[32:35],a[0:3],v[192:195]\ns_mov_b64 exec,0xff\nglobal_load_dword v0,v2,s[88:89]\ns_mov_b64 exec,-1\nM v[196:199],a[36:39],a[0:3],v[196:199]\nM v[204:207],a[36:39],a[4:7],v[204:207]\n" \
"s_mov_b64 exec,0xff00\nglobal_load_dword v0,v2,s[88:89]\ns_mov_b64 exec,-1\nM v[200:203],a[32:35],a[4:7],v[200:203]\ns_add_u32 s88,s88,2048\ns_addc_u32 s89,s89,0\nM v[216:219],a[40:43],a[4:7],v[216:219]\ns_mov_b64 exec,0xff\n" \
"global_load_dword v0,v2,s[88:89]\ns_mov_b64 exec,-1\nM v[220:223],a[44:47],a[4:7],v[220:223]\nM v[212:215],a[44:47],a[0:3],v[212:215]\ns_mov_b64 exec,0xff00\nglobal_load_dword v0,v2,s[88:89]\ns_mov_b64 exec,-1\nM v[208:211],a[40:43],a[0:3],v[208:211]\n" \
"M v[224:227],a[48:51],a[0:3],v[224:227]\ns_mov_b64 exec,0xff\nglobal_load_dword v0,v1,s[76:77]\ns_mov_b64 exec,-1\nM v[228:231],a[52:55],a[0:3],v[228:231]\nM v[236:239],a[52:55],a[4:7],v[236:239]\ns_mov_b64 exec,0xff00\nglobal_load_dword v0,v1,s[76:77]\n" \
"s_mov_b64 exec,-1\nM v[232:235],a[48:51],a[4:7],v[232:235]\ns_add_u32 s76,s76,2097152\ns_addc_u32 s77,s77,0\nM v[248:251],a[56:59],a[4:7],v[248:251]\nM v[252:255],a[60:63],a[4:7],v[252:255]\nM v[244:247],a[60:63],a[0:3],v[244:247]\ns_waitcnt vmcnt(34)\n" \
"v_accvgpr_read_b32 v37,a206\nv_mul_f32 v25,v34,v45\nM v[240:243],a[56:59],a[0:3],v[240:243]\ns_mov_b32 s98,79691776\nv_mul_f32 v26,v40,v49\nv_lshlrev_b32 v31,16,v37\nM a[128:131],a[32:35],a[8:11],a[128:131]\ns_mov_b32 s99,79695872\n" \
"v_fmac_f32 v25,v35,v44\nv_fmac_f32 v26,v41,v48\nM a[132:135],a[36:39],a[8:11],a[132:135]\nv_and_b32 v37,0xffff0000,v37\nv_fmac_f32 v25,v30,v43\nM a[140:143],a[36:39],a[12:15],a[140:143]\nv_fmac_f32 v26,v36,v47\nv_fmac_f32 v25,v31,v42\n" \
"M a[136:139],a[32:35],a[12:15],a[136:139]\nv_fmac_f32 v26,v37,v46\nv_accvgpr_read_b32 v28,a222\nM a[152:155],a[40:43],a[12:15],a[152:155]\nC v26,v25,v26\nv_lshlrev_b32 v27,16,v28\nM a[156:159],a[44:47],a[12:15],a[156:159]\nv_lshlrev_b32 v25,16,v26\n" \
"v_and_b32 v28,0xffff0000,v28\nM a[148:151],a[44:47],a[8:11],a[148:151]\ns_waitcnt vmcnt(23) lgkmcnt(0)\nv_and_b32 v26,0xffff0000,v26\nv_mul_f32 v29,v25,v27\nM a[144:147],a[40:43],a[8:11],a[144:147]\ns_barrier\nv_mul_f32 v58,v26,v28\n" \
"M a[160:163],a[48:51],a[8:11],a[160:163]\nv_accvgpr_read_b32 v38,a208\nv_mul_f32 v25,v35,v45\nM a[164:167],a[52:55],a[8:11],a[164:167]\nv_lshlrev_b32 v32,16,v38\nv_mul_f32 v26,v41,v49\nM a[172:175],a[52:55],a[12:15],a[172:175]\nv_fmac_f32 v25,v30,v44\n" \
"v_and_b32 v38,0xffff0000,v38\nM a[168:171],a[48:51],a[12:15],a[168:171]\nv_fmac_f32 v26,v36,v48\nv_fmac_f32 v25,v31,v43\nM a[184:187],a[56:59],a[12:15],a[184:187]\nds_read_b64_tr_b16 a[32:33],v9 offset:0\nds_read_b64_tr_b16 a[34:35],v10 offset:0\nM a[188:191],a[60:63],a[12:15],a[188:191]\n" \
"ds_read_b64_tr_b16 a[36:37],v11 offset:0\nv_fmac_f32 v26,v37,v47\nM a[180:183],a[60:63],a[8:11],a[180:183]\nds_read_b64_tr_b16 a[38:39],v12 offset:0\nds_read_b64_tr_b16 a[40:41],v9 offset:512\nM a[176:179],a[56:59],a[8:11],a[176:179]\nds_read_b64_tr_b16 a[42:43],v10 offset:512\nv_fmac_f32 v25,v32,v42\n" \
"M v[128:131],a[96:99],a[88:91],v[128:131]\nv_fmac_f32 v26,v38,v46\nv_accvgpr_read_b32 v28,a224\nM v[132:135],a[100:103],a[88:91],v[132:135]\nds_read_b64_tr_b16 a[44:45],v11 offset:512\nC v26,v25,v26\nM v[140:143],a[100:103],a[92:95],v[140:143]\nds_read_b64_tr_b16 a[46:47],v12 offset:512\n" \
"v_lshlrev_b32 v27,16,v28\nM v[136:139],a[96:99],a[92:95],v[136:139]\nds_read_b64_tr_b16 a[48:49],v9 offset:1024\nds_read_b64_tr_b16 a[50:51],v10 offset:1024\nM v[152:155],a[104:107],a[92:95],v[152:155]\nds_read_b64_tr_b16 a[52:53],v11 offset:1024\nv_lshlrev_b32 v25,16,v26\nM v[156:159],a[108:111],a[92:95],v[156:159]\n" \
"ds_read_b64_tr_b16 a[54:55],v12 offset:1024\nds_read_b64_tr_b16 a[56:57],v9 offset:1536\nM v[148:151],a[108:111],a[88:91],v[148:151]\nds_read_b64_tr_b16 a[58:59],v10 offset:1536\nv_and_b32 v28,0xffff0000,v28\nM v[144:147],a[104:107],a[88:91],v[144:147]\nds_read_b64_tr_b16 a[60:61],v11 offset:1536\nv_and_b32 v26,0xffff0000,v26\n" \
"M v[160:163],a[112:115],a[88:91],v[160:163]\nds_read_b64_tr_b16 a[62:63],v12 offset:1536\nv_mul_f32 v25,v25,v27\nM v[164:167],a[116:119],a[88:91],v[164:167]\nds_read_b128 a[24:27],v7 offset:12288\nv_mul_f32 v26,v26,v28\nM v[172:175],a[116:119],a[92:95],v[172:175]\nds_read_b128 a[28:31],v7 offset:14336\n" \
"C v51,v25,v29\nM v[168:171],a[112:115],a[92:95],v[168:171]\nC v55,v26,v58\nv_accvgpr_read_b32 v39,a210\nM v[184:187],a[120:123],a[92:95],v[184:187]\nv_mul_f32 v25,v30,v45\nv_mul_f32 v26,v36,v49\nM v[188:191],a[124:127],a[92:95],v[188:191]\n" \
"v_lshlrev_b32 v33,16,v39\nv_fmac_f32 v25,v31,v44\nM v[180:183],a[124:127],a[88:91],v[180:183]\nv_fmac_f32 v26,v37,v48\nv_and_b32 v39,0xffff0000,v39\nM v[176:179],a[120:123],a[88:91],v[176:179]\nv_fmac_f32 v25,v32,v43\nv_fmac_f32 v26,v38,v47\n" \
"M v[64:67],a[96:99],a[80:83],v[64:67]\nv_fmac_f32 v25,v33,v42\nv_fmac_f32 v26,v39,v46\nM v[68:71],a[100:103],a[80:83],v[68:71]\nv_accvgpr_read_b32 v28,a226\nC v26,v25,v26\nM v[76:79],a[100:103],a[84:87],v[76:79]\nv_lshlrev_b32 v27,16,v28\n" \
"v_lshlrev_b32 v25,16,v26\nM v[72:75],a[96:99],a[84:87],v[72:75]\nv_and_b32 v28,0xffff0000,v28\nv_and_b32 v26,0xffff0000,v26\nM v[88:91],a[104:107],a[84:87],v[88:91]\nv_mul_f32 v29,v25,v27\nv_mul_f32 v58,v26,v28\nM v[92:95],a[108:111],a[84:87],v[92:95]\n" \
"v_accvgpr_read_b32 v40,a212\nv_lshlrev_b32 v34,16,v40\nM v[84:87],a[108:111],a[80:83],v[84:87]\nv_mul_f32 v25,v31,v45\nv_mul_f32 v26,v37,v49\nM v[80:83],a[104:107],a[80:83],v[80:83]\nds_read_b128 a[16:19],v7 offset:8192\nv_fmac_f32 v25,v32,v44\n" \
"M v[96:99],a[112:115],a[80:83],v[96:99]\nds_read_b128 a[20:23],v7 offset:10240\nv_and_b32 v40,0xffff0000,v40\nM v[100:103],a[116:119],a[80:83],v[100:103]\nv_fmac_f32 v26,v38,v48\nv_fmac_f32 v25,v33,v43\nM v[108:111],a[116:119],a[84:87],v[108:111]\nv_fmac_f32 v26,v39,v47\n" \
"v_fmac_f32 v25,v34,v42\nM v[104:107],a[112:115],a[84:87],v[104:107]\nv_fmac_f32 v26,v40,v46\nv_accvgpr_read_b32 v28,a228\nM v[120:123],a[120:123],a[84:87],v[120:123]\nC v26,v25,v26\nv_lshlrev_b32 v27,16,v28\nM v[124:127],a[124:127],a[84:87],v[124:127]\n" \
"v_lshlrev_b32 v25,16,v26\nv_mul_f32 v25,v25,v27\nM v[116:119],a[124:127],a[80:83],v[116:119]\nC v50,v25,v29\nv_accvgpr_read_b32 v45,a235\nM v[112:115],a[120:123],a[80:83],v[112:115]\nv_and_b32 v28,0xffff0000,v28\nv_and_b32 v26,0xffff0000,v26\n" \
"M v[192:195],a[96:99],a[64:67],v[192:195]\nv_lshlrev_b32 v44,16,v45\nv_accvgpr_read_b32 v49,a237\nM v[196:199],a[100:103],a[64:67],v[196:199]\nds_read_b128 a[0:3],v7 offset:0\nv_accvgpr_read_b32 v36,a193\nM v[204:207],a[100:103],a[68:71],v[204:207]\nds_read_b128 a[4:7],v7 offset:2048\n" \
"v_accvgpr_read_b32 v37,a195\nM v[200:203],a[96:99],a[68:71],v[200:203]\nv_mul_f32 v26,v26,v28\nv_accvgpr_read_b32 v43,a234\nM v[216:219],a[104:107],a[68:71],v[216:219]\nv_accvgpr_read_b32 v47,a236\nv_lshlrev_b32 v48,16,v49\nM v[220:223],a[108:111],a[68:71],v[220:223]\n" \
"v_and_b32 v45,0xffff0000,v45\nv_lshlrev_b32 v30,16,v36\nM v[212:215],a[108:111],a[64:67],v[212:215]\nbuffer_store_dwordx4 v[50:53],v60,s[36:39],s98 offen offset:0 sc1\nM v[208:211],a[104:107],a[64:67],v[208:211]\ns_mov_b32 s98,79699968\nv_lshlrev_b32 v31,16,v37\nv_accvgpr_read_b32 v38,a197\n" \
"M v[224:227],a[112:115],a[64:67],v[224:227]\nC v54,v26,v58\nv_accvgpr_read_b32 v39,a199\nM v[228:231],a[116:119],a[64:67],v[228:231]\nv_lshlrev_b32 v42,16,v43\nv_and_b32 v49,0xffff0000,v49\nM v[236:239],a[116:119],a[68:71],v[236:239]\nv_lshlrev_b32 v46,16,v47\n" \
"v_and_b32 v36,0xffff0000,v36\nM v[232:235],a[112:115],a[68:71],v[232:235]\nv_lshlrev_b32 v32,16,v38\nv_mul_f32 v25,v30,v45\nM v[248:251],a[120:123],a[68:71],v[248:251]\nds_read_b128 a[8:11],v7 offset:4096\nds_read_b128 a[12:15],v7 offset:6144\nM v[252:255],a[124:127],a[68:71],v[252:255]\n" \
"v_and_b32 v37,0xffff0000,v37\nv_lshlrev_b32 v33,16,v39\nM v[244:247],a[124:127],a[64:67],v[244:247]\nv_and_b32 v43,0xffff0000,v43\nv_mul_f32 v26,v36,v49\nM v[240:243],a[120:123],a[64:67],v[240:243]\nds_read_b128 a[64:67],v8 offset:0\nds_read_b128 a[68:71],v8 offset:2048\n" \
"M a[128:131],a[96:99],a[72:75],a[128:131]\nv_and_b32 v47,0xffff0000,v47\nv_and_b32 v38,0xffff0000,v38\nM a[132:135],a[100:103],a[72:75],a[132:135]\nds_read_b128 a[80:83],v8 offset:8192\nv_fmac_f32 v25,v31,v44\nM a[140:143],a[100:103],a[76:79],a[140:143]\nv_and_b32 v39,0xffff0000,v39\n" \
"v_fmac_f32 v26,v37,v48\nM a[136:139],a[96:99],a[76:79],a[136:139]\nv_fmac_f32 v25,v32,v43\nv_fmac_f32 v26,v38,v47\nM a[152:155],a[104:107],a[76:79],a[152:155]\nv_fmac_f32 v25,v33,v42\nv_fmac_f32 v26,v39,v46\nM a[156:159],a[108:111],a[76:79],a[156:159]\n" \
"ds_read_b128 a[84:87],v8 offset:10240\nv_accvgpr_read_b32 v28,a215\nM a[148:151],a[108:111],a[72:75],a[148:151]\ns_waitcnt vmcnt(20) lgkmcnt(12)\nds_read_b128 a[88:91],v8 offset:12288\nds_read_b128 a[92:95],v8 offset:14336\nM a[144:147],a[104:107],a[72:75],a[144:147]\ns_barrier\n" \
"ds_read_b64_tr_b16 a[96:97],v9 offset:16384\nM a[160:163],a[112:115],a[72:75],a[160:163]\nds_read_b64_tr_b16 a[98:99],v10 offset:16384\nC v26,v25,v26\nM a[164:167],a[116:119],a[72:75],a[164:167]\nds_read_b64_tr_b16 a[100:101],v11 offset:16384\nv_lshlrev_b32 v27,16,v28\nM a[172:175],a[116:119],a[76:79],a[172:175]\n" \
"ds_read_b64_tr_b16 a[102:103],v12 offset:16384\nds_read_b64_tr_b16 a[104:105],v9 offset:16896\nM a[168:171],a[112:115],a[76:79],a[168:171]\nds_read_b64_tr_b16 a[106:107],v10 offset:16896\nv_lshlrev_b32 v25,16,v26\nM a[184:187],a[120:123],a[76:79],a[184:187]\nds_read_b64_tr_b16 a[108:109],v11 offset:16896\nv_and_b32 v28,0xffff0000,v28\n" \
"M a[188:191],a[124:127],a[76:79],a[188:191]\ns_waitcnt lgkmcnt(6)\nds_read_b64_tr_b16 a[110:111],v12 offset:16896\nds_read_b64_tr_b16 a[112:113],v9 offset:17408\nM a[180:183],a[124:127],a[72:75],a[180:183]\nbuffer_store_dwordx4 v[54:57],v60,s[36:39],s99 offen offset:0 sc1\nM a[176:179],a[120:123],a[72:75],a[176:179]\ns_mov_b32 s99,79704064\n" \
"ds_read_b128 a[72:75],v8 offset:4096\nds_read_b128 a[76:79],v8 offset:6144\nM v[128:131],a[32:35],a[24:27],v[128:131]\nds_read_b64_tr_b16 a[114:115],v10 offset:17408\nv_and_b32 v26,0xffff0000,v26\nM v[132:135],a[36:39],a[24:27],v[132:135]\nds_read_b64_tr_b16 a[116:117],v11 offset:17408\nv_mul_f32 v29,v25,v27\n" \
"M v[140:143],a[36:39],a[28:31],v[140:143]\nds_read_b64_tr_b16 a[118:119],v12 offset:17408\nv_mul_f32 v58,v26,v28\nM v[136:139],a[32:35],a[28:31],v[136:139]\nds_read_b64_tr_b16 a[120:121],v9 offset:17920\nds_read_b64_tr_b16 a[122:123],v10 offset:17920\nM v[152:155],a[40:43],a[28:31],v[152:155]\nds_read_b64_tr_b16 a[124:125],v11 offset:17920\n" \
"v_accvgpr_read_b32 v40,a201\nM v[156:159],a[44:47],a[28:31],v[156:159]\nds_read_b64_tr_b16 a[126:127],v12 offset:17920\nv_lshlrev_b32 v34,16,v40\nM v[148:151],a[44:47],a[24:27],v[148:151]\nv_mul_f32 v25,v31,v45\nv_mul_f32 v26,v37,v49\nM v[144:147],a[40:43],a[24:27],v[144:147]\n" \
"v_fmac_f32 v25,v32,v44\nv_and_b32 v40,0xffff0000,v40\nM v[160:163],a[48:51],a[24:27],v[160:163]\nv_fmac_f32 v26,v38,v48\nv_fmac_f32 v25,v33,v43\nM v[164:167],a[52:55],a[24:27],v[164:167]\nv_fmac_f32 v26,v39,v47\nv_fmac_f32 v25,v34,v42\n" \
"M v[172:175],a[52:55],a[28:31],v[172:175]\nv_fmac_f32 v26,v40,v46\nv_accvgpr_read_b32 v28,a217\nM v[168:171],a[48:51],a[28:31],v[168:171]\nC v26,v25,v26\nv_lshlrev_b32 v27,16,v28\nM v[184:187],a[56:59],a[28:31],v[184:187]\nv_lshlrev_b32 v25,16,v26\n" \
"v_and_b32 v28,0xffff0000,v28\nM v[188:191],a[60:63],a[28:31],v[188:191]\nv_and_b32 v26,0xffff0000,v26\nv_mul_f32 v25,v25,v27\nM v[180:183],a[60:63],a[24:27],v[180:183]\nv_mul_f32 v26,v26,v28\nC v53,v25,v29\nM v[176:179],a[56:59],a[24:27],v[176:179]\n" \
"C v57,v26,v58\nv_accvgpr_read_b32 v41,a203\nM v[64:67],a[32:35],a[16:19],v[64:67]\nv_mul_f32 v25,v32,v45\nv_lshlrev_b32 v35,16,v41\nM v[68:71],a[36:39],a[16:19],v[68:71]\nv_mul_f32 v26,v38,v49\nv_fmac_f32 v25,v33,v44\n" \
"M v[76:79],a[36:39],a[20:23],v[76:79]\nv_fmac_f32 v26,v39,v48\nv_and_b32 v41,0xffff0000,v41\nM v[72:75],a[32:35],a[20:23],v[72:75]\nv_fmac_f32 v25,v34,v43\nv_fmac_f32 v26,v40,v47\nM v[88:91],a[40:43],a[20:23],v[88:91]\nv_fmac_f32 v25,v35,v42\n" \
"v_fmac_f32 v26,v41,v46\nM v[92:95],a[44:47],a[20:23],v[92:95]\nv_accvgpr_read_b32 v28,a219\nC v26,v25,v26\nM v[84:87],a[44:47],a[16:19],v[84:87]\ns_mov_b64 exec,0xff\nglobal_load_dword v0,v2,s[100:101]\ns_mov_b64 exec,-1\n" \
"M v[80:83],a[40:43],a[16:19],v[80:83]\nv_lshlrev_b32 v27,16,v28\nv_lshlrev_b32 v25,16,v26\nM v[96:99],a[48:51],a[16:19],v[96:99]\nbuffer_load_dwordx4 v21,s[32:35],s47 offen offset:0 lds\nM v[100:103],a[52:55],a[16:19],v[100:103]\ns_add_u32 m0,s22,50048\nv_and_b32 v28,0xffff0000,v28\n" \
"v_and_b32 v26,0xffff0000,v26\nM v[108:111],a[52:55],a[20:23],v[108:111]\nv_mul_f32 v29,v25,v27\nv_mul_f32 v58,v26,v28\nM v[104:107],a[48:51],a[20:23],v[104:107]\nv_accvgpr_read_b32 v36,a205\nv_lshlrev_b32 v30,16,v36\nM v[120:123],a[56:59],a[20:23],v[120:123]\n" \
"buffer_load_dwordx4 v21,s[32:35],s47 offen offset:128 lds\nM v[124:127],a[60:63],a[20:23],v[124:127]\ns_add_u32 m0,s22,51200\nv_mul_f32 v25,v33,v45\nv_mul_f32 v26,v39,v49\nM v[116:119],a[60:63],a[16:19],v[116:119]\ns_mov_b64 s[32:33],s[44:45]\nv_fmac_f32 v25,v34,v44\n" \
"v_and_b32 v36,0xffff0000,v36\nM v[112:115],a[56:59],a[16:19],v[112:115]\ns_mov_b32 s34,s49\nv_fmac_f32 v26,v40,v48\nv_fmac_f32 v25,v35,v43\nM v[192:195],a[32:35],a[0:3],v[192:195]\nbuffer_load_dwordx4 v21,s[92:95],s47 offen offset:0 lds\nM v[196:199],a[36:39],a[0:3],v[196:199]\n" \
"s_add_u32 m0,s22,52096\nv_fmac_f32 v26,v41,v47\nv_fmac_f32 v25,v30,v42\nM v[204:207],a[36:39],a[4:7],v[204:207]\nv_fmac_f32 v26,v36,v46\nv_accvgpr_read_b32 v28,a221\nM v[200:203],a[32:35],a[4:7],v[200:203]\nC v26,v25,v26\n" \
"v_lshlrev_b32 v27,16,v28\nM v[216:219],a[40:43],a[4:7],v[216:219]\nbuffer_load_dwordx4 v21,s[92:95],s47 offen offset:128 lds\nM v[220:223],a[44:47],a[4:7],v[220:223]\ns_add_u32 m0,s50,32768\nv_lshlrev_b32 v25,16,v26\nv_and_b32 v28,0xffff0000,v28\nM v[212:215],a[44:47],a[0:3],v[212:215]\n" \
"s_mov_b32 s47,0\nv_and_b32 v26,0xffff0000,v26\nv_mul_f32 v25,v25,v27\nM v[208:211],a[40:43],a[0:3],v[208:211]\nv_mul_f32 v26,v26,v28\nC v52,v25,v29\nM v[224:227],a[48:51],a[0:3],v[224:227]\nbuffer_load_dwordx4 v13,s[28:31],s46 offen lds\n" \
"M v[228:231],a[52:55],a[0:3],v[228:231]\ns_add_u32 m0,s50,36864\nC v56,v26,v58\nv_accvgpr_read_b32 v37,a207\nM v[236:239],a[52:55],a[4:7],v[236:239]\nv_mul_f32 v25,v34,v45\nv_lshlrev_b32 v31,16,v37\nM v[232:235],a[48:51],a[4:7],v[232:235]\n" \
"v_mul_f32 v26,v40,v49\nv_fmac_f32 v25,v35,v44\nM v[248:251],a[56:59],a[4:7],v[248:251]\nbuffer_load_dwordx4 v14,s[28:31],s46 offen lds\nM v[252:255],a[60:63],a[4:7],v[252:255]\ns_add_u32 m0,s50,40960\nv_fmac_f32 v26,v41,v48\nv_and_b32 v37,0xffff0000,v37\n" \
"M v[244:247],a[60:63],a[0:3],v[244:247]\nv_fmac_f32 v25,v30,v43\nv_fmac_f32 v26,v36,v47\nM v[240:243],a[56:59],a[0:3],v[240:243]\nv_fmac_f32 v25,v31,v42\nv_fmac_f32 v26,v37,v46\nM a[128:131],a[32:35],a[8:11],a[128:131]\nbuffer_load_dwordx4 v15,s[28:31],s46 offen lds\n" \
"M a[132:135],a[36:39],a[8:11],a[132:135]\ns_add_u32 m0,s50,45056\nv_accvgpr_read_b32 v28,a223\nC v26,v25,v26\nM a[140:143],a[36:39],a[12:15],a[140:143]\nv_lshlrev_b32 v27,16,v28\nv_lshlrev_b32 v25,16,v26\nM a[136:139],a[32:35],a[12:15],a[136:139]\n" \
"v_and_b32 v28,0xffff0000,v28\nv_and_b32 v26,0xffff0000,v26\nM a[152:155],a[40:43],a[12:15],a[152:155]\nbuffer_load_dwordx4 v16,s[28:31],s46 offen lds\nM a[156:159],a[44:47],a[12:15],a[156:159]\ns_add_u32 m0,s50,49152\nv_mul_f32 v29,v25,v27\nv_mul_f32 v58,v26,v28\n" \
"M a[148:151],a[44:47],a[8:11],a[148:151]\ns_waitcnt vmcnt(17) lgkmcnt(0)\nv_accvgpr_read_b32 v38,a209\nv_mul_f32 v25,v35,v45\nM a[144:147],a[40:43],a[8:11],a[144:147]\ns_mov_b32 s46,0\ns_barrier\nds_read_b64_tr_b16 a[32:33],v9 offset:32768\n" \
"M a[160:163],a[48:51],a[8:11],a[160:163]\nbuffer_load_dwordx4 v17,s[28:31],s46 offen lds\nM a[164:167],a[52:55],a[8:11],a[164:167]\ns_add_u32 m0,s50,53248\nds_read_b64_tr_b16 a[34:35],v10 offset:32768\nv_mul_f32 v26,v41,v49\nM a[172:175],a[52:55],a[12:15],a[172:175]\nLG3_%=:\n" \
"s_cbranch_execz LG10_%=\nds_read_b64_tr_b16 a[36:37],v11 offset:32768\nds_read_b64_tr_b16 a[38:39],v12 offset:32768\nM a[168:171],a[48:51],a[12:15],a[168:171]\nds_read_b64_tr_b16 a[40:41],v9 offset:33280\nv_lshlrev_b32 v32,16,v38\nM a[184:187],a[56:59],a[12:15],a[184:187]\nbuffer_load_dwordx4 v18,s[28:31],s46 offen lds\n" \
"M a[188:191],a[60:63],a[12:15],a[188:191]\ns_add_u32 m0,s50,57344\nds_read_b64_tr_b16 a[42:43],v10 offset:33280\nv_fmac_f32 v25,v30,v44\nM a[180:183],a[60:63],a[8:11],a[180:183]\nds_read_b64_tr_b16 a[44:45],v11 offset:33280\nv_fmac_f32 v26,v36,v48\nM a[176:179],a[56:59],a[8:11],a[176:179]\n" \
"ds_read_b64_tr_b16 a[46:47],v12 offset:33280\nv_and_b32 v38,0xffff0000,v38\nM v[128:131],a[96:99],a[88:91],v[128:131]\nbuffer_load_dwordx4 v19,s[28:31],s46 offen lds\nM v[132:135],a[100:103],a[88:91],v[132:135]\ns_add_u32 m0,s50,61440\nds_read_b64_tr_b16 a[48:49],v9 offset:33792\nv_fmac_f32 v25,v31,v43\n" \
"M v[140:143],a[100:103],a[92:95],v[140:143]\nds_read_b64_tr_b16 a[50:51],v10 offset:33792\nv_fmac_f32 v26,v37,v47\nM v[136:139],a[96:99],a[92:95],v[136:139]\nds_read_b64_tr_b16 a[52:53],v11 offset:33792\nds_read_b64_tr_b16 a[54:55],v12 offset:33792\nM v[152:155],a[104:107],a[92:95],v[152:155]\nbuffer_load_dwordx4 v20,s[28:31],s46 offen lds\n" \
"M v[156:159],a[108:111],a[92:95],v[156:159]\ns_add_u32 m0,s51,0\nv_fmac_f32 v25,v32,v42\nv_fmac_f32 v26,v38,v46\nM v[148:151],a[108:111],a[88:91],v[148:151]\ns_mov_b32 s46,128\nds_read_b64_tr_b16 a[56:57],v9 offset:34304\nds_read_b64_tr_b16 a[58:59],v10 offset:34304\n" \
"M v[144:147],a[104:107],a[88:91],v[144:147]\nds_read_b64_tr_b16 a[60:61],v11 offset:34304\nv_accvgpr_read_b32 v28,a225\nM v[160:163],a[112:115],a[88:91],v[160:163]\nbuffer_load_dwordx4 v21,s[32:35],s47 offen offset:0 lds\nM v[164:167],a[116:119],a[88:91],v[164:167]\ns_add_u32 m0,s51,896\nds_read_b64_tr_b16 a[62:63],v12 offset:34304\n" \
"C v26,v25,v26\nM v[172:175],a[116:119],a[92:95],v[172:175]\nds_read_b128 a[24:27],v5 offset:12288\nv_lshlrev_b32 v27,16,v28\nM v[168:171],a[112:115],a[92:95],v[168:171]\nds_read_b128 a[28:31],v5 offset:14336\nv_lshlrev_b32 v25,16,v26\nM v[184:187],a[120:123],a[92:95],v[184:187]\n" \
"buffer_load_dwordx4 v21,s[32:35],s47 offen offset:128 lds\nM v[188:191],a[124:127],a[92:95],v[188:191]\ns_add_u32 m0,s51,8192\nv_and_b32 v28,0xffff0000,v28\nv_and_b32 v26,0xffff0000,v26\nM v[180:183],a[124:127],a[88:91],v[180:183]\ns_mov_b32 s47,262144\nv_mul_f32 v25,v25,v27\n" \
"v_mul_f32 v26,v26,v28\nM v[176:179],a[120:123],a[88:91],v[176:179]\nC v51,v25,v29\nC v55,v26,v58\nM v[64:67],a[96:99],a[80:83],v[64:67]\nbuffer_load_dwordx4 v21,s[32:35],s47 offen offset:0 lds\nM v[68:71],a[100:103],a[80:83],v[68:71]\ns_add_u32 m0,s51,9088\n" \
"v_accvgpr_read_b32 v39,a211\nv_mul_f32 v25,v30,v45\nM v[76:79],a[100:103],a[84:87],v[76:79]\nv_lshlrev_b32 v33,16,v39\nv_mul_f32 v26,v36,v49\nM v[72:75],a[96:99],a[84:87],v[72:75]\nv_fmac_f32 v25,v31,v44\nv_fmac_f32 v26,v37,v48\n" \
"M v[88:91],a[104:107],a[84:87],v[88:91]\nbuffer_load_dwordx4 v21,s[32:35],s47 offen offset:128 lds\nM v[92:95],a[108:111],a[84:87],v[92:95]\ns_add_u32 m0,s51,16384\nv_and_b32 v39,0xffff0000,v39\nv_fmac_f32 v25,v32,v43\nM v[84:87],a[108:111],a[80:83],v[84:87]\ns_mov_b32 s47,524288\n" \
"v_fmac_f32 v26,v38,v47\nv_fmac_f32 v25,v33,v42\nM v[80:83],a[104:107],a[80:83],v[80:83]\nv_fmac_f32 v26,v39,v46\nv_accvgpr_read_b32 v28,a227\nM v[96:99],a[112:115],a[80:83],v[96:99]\nbuffer_load_dwordx4 v21,s[32:35],s47 offen offset:0 lds\nM v[100:103],a[116:119],a[80:83],v[100:103]\n" \
"s_add_u32 m0,s51,17280\nC v26,v25,v26\nv_lshlrev_b32 v27,16,v28\nM v[108:111],a[116:119],a[84:87],v[108:111]\nds_read_b128 a[16:19],v5 offset:8192\nv_lshlrev_b32 v25,16,v26\nM v[104:107],a[112:115],a[84:87],v[104:107]\nds_read_b128 a[20:23],v5 offset:10240\n" \
"v_and_b32 v28,0xffff0000,v28\nM v[120:123],a[120:123],a[84:87],v[120:123]\nbuffer_load_dwordx4 v21,s[32:35],s47 offen offset:128 lds\nM v[124:127],a[124:127],a[84:87],v[124:127]\ns_add_u32 m0,s51,24576\nv_and_b32 v26,0xffff0000,v26\nv_mul_f32 v29,v25,v27\nM v[116:119],a[124:127],a[80:83],v[116:119]\n" \
"s_mov_b32 s47,786432\nv_mul_f32 v58,v26,v28\nv_accvgpr_read_b32 v40,a213\nM v[112:115],a[120:123],a[80:83],v[112:115]\nv_lshlrev_b32 v34,16,v40\nv_mul_f32 v25,v31,v45\nM v[192:195],a[96:99],a[64:67],v[192:195]\ns_mov_b64 exec,0xff\n" \
"global_load_dword v0,v1,s[76:77]\ns_mov_b64 exec,-1\nM v[196:199],a[100:103],a[64:67],v[196:199]\nv_mul_f32 v26,v37,v49\nv_fmac_f32 v25,v32,v44\nM v[204:207],a[100:103],a[68:71],v[204:207]\ns_mov_b64 exec,0xff00\nglobal_load_dword v0,v1,s[76:77]\n" \
"s_mov_b64 exec,-1\nM v[200:203],a[96:99],a[68:71],v[200:203]\ns_add_u32 s76,s76,2097152\ns_addc_u32 s77,s77,0\nv_and_b32 v40,0xffff0000,v40\nv_fmac_f32 v26,v38,v48\nM v[216:219],a[104:107],a[68:71],v[216:219]\ns_mov_b64 exec,0xff\n" \
"global_load_dword v0,v1,s[76:77]\ns_mov_b64 exec,-1\nM v[220:223],a[108:111],a[68:71],v[220:223]\nv_fmac_f32 v25,v33,v43\nv_fmac_f32 v26,v39,v47\nM v[212:215],a[108:111],a[64:67],v[212:215]\ns_mov_b64 exec,0xff00\nglobal_load_dword v0,v1,s[76:77]\n" \
"s_mov_b64 exec,-1\nM v[208:211],a[104:107],a[64:67],v[208:211]\ns_mov_b32 s76,0xf149f2ca\nv_fmac_f32 v25,v34,v42\nv_fmac_f32 v26,v40,v46\nM v[224:227],a[112:115],a[64:67],v[224:227]\nv_accvgpr_read_b32 v28,a229\nC v26,v25,v26\n" \
"M v[228:231],a[116:119],a[64:67],v[228:231]\nv_lshlrev_b32 v27,16,v28\nv_lshlrev_b32 v25,16,v26\nM v[236:239],a[116:119],a[68:71],v[236:239]\nv_mul_f32 v25,v25,v27\nC v50,v25,v29\nM v[232:235],a[112:115],a[68:71],v[232:235]\nds_read_b128 a[0:3],v5 offset:0\n" \
"ds_read_b128 a[4:7],v5 offset:2048\nM v[248:251],a[120:123],a[68:71],v[248:251]\nds_read_b128 a[8:11],v5 offset:4096\nv_and_b32 v28,0xffff0000,v28\nM v[252:255],a[124:127],a[68:71],v[252:255]\nds_read_b128 a[12:15],v5 offset:6144\nv_and_b32 v26,0xffff0000,v26\nM v[244:247],a[124:127],a[64:67],v[244:247]\n" \
"v_mul_f32 v26,v26,v28\nC v54,v26,v58\nM v[240:243],a[120:123],a[64:67],v[240:243]\nds_read_b128 a[64:67],v6 offset:0\nds_read_b128 a[68:71],v6 offset:2048\nM a[128:131],a[96:99],a[72:75],a[128:131]\nds_read_b128 a[80:83],v6 offset:8192\nv_add_u32 v5,s54,v5\n" \
"M a[132:135],a[100:103],a[72:75],a[132:135]\nds_read_b128 a[84:87],v6 offset:10240\nv_add_u32 v7,s54,v7\nM a[140:143],a[100:103],a[76:79],a[140:143]\nds_read_b128 a[88:91],v6 offset:12288\nv_add_u32 v8,s54,v8\nM a[136:139],a[96:99],a[76:79],a[136:139]\nds_read_b128 a[92:95],v6 offset:14336\n" \
"v_mov_b32 v58,s18\nM a[152:155],a[104:107],a[76:79],a[152:155]\nv_add_u32 v25,-16,v4\nv_mov_b32 v29,s62\nM a[156:159],a[108:111],a[76:79],a[156:159]\ns_and_b32 s62,%1,7\nv_mov_b32 v28,0\nv_add_u32 v26,-512,v4\n" \
"M a[148:151],a[108:111],a[72:75],a[148:151]\ns_waitcnt vmcnt(18) lgkmcnt(14)\nv_add_u32 v27,512,v4\nM a[144:147],a[104:107],a[72:75],a[144:147]\ns_lshl_b32 s62,s62,8\ns_barrier\nds_read_b64_tr_b16 a[96:97],v9 offset:49152\nM a[160:163],a[112:115],a[72:75],a[160:163]\n" \
"s_add_u32 s62,s62,94380032\nds_read_b64_tr_b16 a[98:99],v10 offset:49152\nds_read_b64_tr_b16 a[100:101],v11 offset:49152\nM a[164:167],a[116:119],a[72:75],a[164:167]\nds_read_b64_tr_b16 a[102:103],v12 offset:49152\nM a[172:175],a[116:119],a[76:79],a[172:175]\nds_read_b64_tr_b16 a[104:105],v9 offset:49664\nds_read_b64_tr_b16 a[106:107],v10 offset:49664\n" \
"M a[168:171],a[112:115],a[76:79],a[168:171]\nds_read_b64_tr_b16 a[108:109],v11 offset:49664\nM a[184:187],a[120:123],a[76:79],a[184:187]\nds_read_b64_tr_b16 a[110:111],v12 offset:49664\nM a[188:191],a[124:127],a[76:79],a[188:191]\nds_read_b64_tr_b16 a[112:113],v9 offset:50176\nds_read_b64_tr_b16 a[114:115],v10 offset:50176\nM a[180:183],a[124:127],a[72:75],a[180:183]\n" \
"ds_read_b64_tr_b16 a[116:117],v11 offset:50176\nM a[176:179],a[120:123],a[72:75],a[176:179]\ns_waitcnt lgkmcnt(7)\nds_read_b128 a[72:75],v6 offset:4096\nM v[128:131],a[32:35],a[24:27],v[128:131]\nds_read_b128 a[76:79],v6 offset:6144\nv_add_u32 v6,s54,v6\nM v[132:135],a[36:39],a[24:27],v[132:135]\n" \
"ds_read_b64_tr_b16 a[118:119],v12 offset:50176\nM v[140:143],a[36:39],a[28:31],v[140:143]\nds_read_b64_tr_b16 a[120:121],v9 offset:50688\nds_read_b64_tr_b16 a[122:123],v10 offset:50688\nM v[136:139],a[32:35],a[28:31],v[136:139]\nds_read_b64_tr_b16 a[124:125],v11 offset:50688\nv_sub_u32 v9,v9,v62\nM v[152:155],a[40:43],a[28:31],v[152:155]\n" \
"ds_read_b64_tr_b16 a[126:127],v12 offset:50688\nv_sub_u32 v10,v10,v62\nM v[156:159],a[44:47],a[28:31],v[156:159]\nv_sub_u32 v11,v11,v62\nv_sub_u32 v12,v12,v62\nM v[148:151],a[44:47],a[24:27],v[148:151]\nM v[144:147],a[40:43],a[24:27],v[144:147]\nM v[160:163],a[48:51],a[24:27],v[160:163]\n" \
"M v[164:167],a[52:55],a[24:27],v[164:167]\nM v[172:175],a[52:55],a[28:31],v[172:175]\nM v[168:171],a[48:51],a[28:31],v[168:171]\nM v[184:187],a[56:59],a[28:31],v[184:187]\nM v[188:191],a[60:63],a[28:31],v[188:191]\nM v[180:183],a[60:63],a[24:27],v[180:183]\nbuffer_load_dwordx4 v21,s[32:35],s47 offen offset:0 lds\nM v[176:179],a[56:59],a[24:27],v[176:179]\n" \
"s_add_u32 m0,s51,25472\nM v[64:67],a[32:35],a[16:19],v[64:67]\nM v[68:71],a[36:39],a[16:19],v[68:71]\nM v[76:79],a[36:39],a[20:23],v[76:79]\nbuffer_store_dwordx4 v[50:53],v60,s[36:39],s98 offen offset:0 sc1\nM v[72:75],a[32:35],a[20:23],v[72:75]\nM v[88:91],a[40:43],a[20:23],v[88:91]\nM v[92:95],a[44:47],a[20:23],v[92:95]\n" \
"M v[84:87],a[44:47],a[16:19],v[84:87]\nM v[80:83],a[40:43],a[16:19],v[80:83]\nM v[96:99],a[48:51],a[16:19],v[96:99]\nM v[100:103],a[52:55],a[16:19],v[100:103]\nM v[108:111],a[52:55],a[20:23],v[108:111]\nM v[104:107],a[48:51],a[20:23],v[104:107]\nM v[120:123],a[56:59],a[20:23],v[120:123]\nM v[124:127],a[60:63],a[20:23],v[124:127]\n" \
"M v[116:119],a[60:63],a[16:19],v[116:119]\nM v[112:115],a[56:59],a[16:19],v[112:115]\nM v[192:195],a[32:35],a[0:3],v[192:195]\nM v[196:199],a[36:39],a[0:3],v[196:199]\nM v[204:207],a[36:39],a[4:7],v[204:207]\nM v[200:203],a[32:35],a[4:7],v[200:203]\nM v[216:219],a[40:43],a[4:7],v[216:219]\nM v[220:223],a[44:47],a[4:7],v[220:223]\n" \
"M v[212:215],a[44:47],a[0:3],v[212:215]\nM v[208:211],a[40:43],a[0:3],v[208:211]\nM v[224:227],a[48:51],a[0:3],v[224:227]\nM v[228:231],a[52:55],a[0:3],v[228:231]\nM v[236:239],a[52:55],a[4:7],v[236:239]\nbuffer_store_dwordx4 v[54:57],v60,s[36:39],s99 offen offset:0 sc1\nM v[232:235],a[48:51],a[4:7],v[232:235]\ns_nop 0\n" \
"v_lshlrev_b32 v56,23,v4\nv_bfe_u32 v57,v4,4,5\nM v[248:251],a[56:59],a[4:7],v[248:251]\nv_lshl_add_u32 v57,v57,3,s62\nv_add_u32 v55,s56,v4\nM v[252:255],a[60:63],a[4:7],v[252:255]\nM v[244:247],a[60:63],a[0:3],v[244:247]\nM v[240:243],a[56:59],a[0:3],v[240:243]\n" \
"M a[128:131],a[32:35],a[8:11],a[128:131]\nM a[132:135],a[36:39],a[8:11],a[132:135]\nM a[140:143],a[36:39],a[12:15],a[140:143]\nM a[136:139],a[32:35],a[12:15],a[136:139]\nM a[152:155],a[40:43],a[12:15],a[152:155]\nM a[156:159],a[44:47],a[12:15],a[156:159]\nM a[148:151],a[44:47],a[8:11],a[148:151]\ns_waitcnt vmcnt(9) lgkmcnt(0)\n" \
"M a[144:147],a[40:43],a[8:11],a[144:147]\ns_barrier\nds_read_b64_tr_b16 a[0:1],v9 offset:0\nM a[160:163],a[48:51],a[8:11],a[160:163]\nds_read_b64_tr_b16 a[2:3],v10 offset:0\nds_read_b64_tr_b16 a[4:5],v11 offset:0\nM a[164:167],a[52:55],a[8:11],a[164:167]\nds_read_b128 a[44:47],v5 offset:45056\n" \
"M a[172:175],a[52:55],a[12:15],a[172:175]\nds_read_b64_tr_b16 a[6:7],v12 offset:0\nM a[168:171],a[48:51],a[12:15],a[168:171]\nds_read_b64_tr_b16 a[16:17],v9 offset:1024\nds_read_b64_tr_b16 a[18:19],v10 offset:1024\nM a[184:187],a[56:59],a[12:15],a[184:187]\nds_read_b64_tr_b16 a[20:21],v11 offset:1024\nM a[188:191],a[60:63],a[12:15],a[188:191]\n" \
"ds_read_b64_tr_b16 a[22:23],v12 offset:1024\nM a[180:183],a[60:63],a[8:11],a[180:183]\nds_read_b64_tr_b16 a[24:25],v9 offset:1536\nds_read_b64_tr_b16 a[26:27],v10 offset:1536\nM a[176:179],a[56:59],a[8:11],a[176:179]\nds_read_b64_tr_b16 a[8:9],v9 offset:512\nM v[128:131],a[96:99],a[88:91],v[128:131]\nds_read_b64_tr_b16 a[10:11],v10 offset:512\n" \
"M v[132:135],a[100:103],a[88:91],v[132:135]\nds_read_b64_tr_b16 a[12:13],v11 offset:512\nds_read_b64_tr_b16 a[14:15],v12 offset:512\nM v[140:143],a[100:103],a[92:95],v[140:143]\nbuffer_load_dwordx4 v21,s[32:35],s47 offen offset:128 lds\nM v[136:139],a[96:99],a[92:95],v[136:139]\ns_add_u32 m0,s50,65536\nv_mul_f32 v128,s20,v128\n" \
"v_mul_f32 v129,s20,v129\nM v[152:155],a[104:107],a[92:95],v[152:155]\ns_mov_b32 s47,1048576\nv_mul_f32 v130,s20,v130\nv_mul_f32 v131,s20,v131\nM v[156:159],a[108:111],a[92:95],v[156:159]\nv_mul_f32 v132,s20,v132\nv_mul_f32 v133,s20,v133\n" \
"M v[148:151],a[108:111],a[88:91],v[148:151]\nbuffer_load_dwordx4 v13,s[28:31],s46 offen lds\nM v[144:147],a[104:107],a[88:91],v[144:147]\ns_add_u32 m0,s50,69632\nv_mul_f32 v134,s20,v134\nv_mul_f32 v135,s20,v135\nM v[160:163],a[112:115],a[88:91],v[160:163]\nv_mul_f32 v136,s20,v136\n" \
"v_mul_f32 v137,s20,v137\nM v[164:167],a[116:119],a[88:91],v[164:167]\nv_mul_f32 v138,s20,v138\nv_mul_f32 v139,s20,v139\nM v[172:175],a[116:119],a[92:95],v[172:175]\nbuffer_load_dwordx4 v14,s[28:31],s46 offen lds\nM v[168:171],a[112:115],a[92:95],v[168:171]\ns_add_u32 m0,s50,73728\n" \
"ds_read_b64_tr_b16 a[28:29],v11 offset:1536\nv_mul_f32 v140,s20,v140\nM v[184:187],a[120:123],a[92:95],v[184:187]\nds_read_b64_tr_b16 a[30:31],v12 offset:1536\nds_read_b128 a[40:43],v5 offset:40960\nM v[188:191],a[124:127],a[92:95],v[188:191]\nv_mul_f32 v141,s20,v141\nv_mul_f32 v142,s20,v142\n" \
"M v[180:183],a[124:127],a[88:91],v[180:183]\nbuffer_load_dwordx4 v15,s[28:31],s46 offen lds\nM v[176:179],a[120:123],a[88:91],v[176:179]\ns_add_u32 m0,s50,77824\nv_mul_f32 v143,s20,v143\nv_mul_f32 v144,s20,v144\nM v[64:67],a[96:99],a[80:83],v[64:67]\nv_mul_f32 v145,s20,v145\n" \
"v_mul_f32 v146,s20,v146\nM v[68:71],a[100:103],a[80:83],v[68:71]\nds_read_b128 a[32:35],v5 offset:32768\nv_mul_f32 v147,s20,v147\nM v[76:79],a[100:103],a[84:87],v[76:79]\nbuffer_load_dwordx4 v16,s[28:31],s46 offen lds\nM v[72:75],a[96:99],a[84:87],v[72:75]\ns_add_u32 m0,s50,81920\n" \
"s_mov_b32 s46,128\nv_mul_f32 v64,s20,v64\nv_mul_f32 v65,s20,v65\nM v[88:91],a[104:107],a[84:87],v[88:91]\nv_mul_f32 v66,s20,v66\nv_mul_f32 v67,s20,v67\nM v[92:95],a[108:111],a[84:87],v[92:95]\nC v64,v64,v65\n" \
"C v65,v66,v67\nM v[84:87],a[108:111],a[80:83],v[84:87]\nbuffer_load_dwordx4 v17,s[28:31],s46 offen lds\nM v[80:83],a[104:107],a[80:83],v[80:83]\ns_add_u32 m0,s50,86016\nv_mul_f32 v68,s20,v68\nv_mul_f32 v69,s20,v69\nM v[96:99],a[112:115],a[80:83],v[96:99]\n" \
"v_mul_f32 v148,s20,v148\nv_mul_f32 v149,s20,v149\nM v[100:103],a[116:119],a[80:83],v[100:103]\nv_mul_f32 v150,s20,v150\nv_mul_f32 v151,s20,v151\nM v[108:111],a[116:119],a[84:87],v[108:111]\nbuffer_load_dwordx4 v18,s[28:31],s46 offen lds\nM v[104:107],a[112:115],a[84:87],v[104:107]\n" \
"s_add_u32 m0,s50,90112\nv_mul_f32 v70,s20,v70\nv_mul_f32 v152,s20,v152\nM v[120:123],a[120:123],a[84:87],v[120:123]\nv_mul_f32 v71,s20,v71\nv_mul_f32 v153,s20,v153\nM v[124:127],a[124:127],a[84:87],v[124:127]\nv_mul_f32 v154,s20,v154\n" \
"v_mul_f32 v155,s20,v155\nM v[116:119],a[124:127],a[80:83],v[116:119]\nbuffer_load_dwordx4 v19,s[28:31],s46 offen lds\nM v[112:115],a[120:123],a[80:83],v[112:115]\ns_add_u32 m0,s50,94208\nv_mul_f32 v156,s20,v156\nv_mul_f32 v157,s20,v157\nM v[192:195],a[96:99],a[64:67],v[192:195]\n" \
"v_mul_f32 v158,s20,v158\nv_mul_f32 v159,s20,v159\nM v[196:199],a[100:103],a[64:67],v[196:199]\nv_mul_f32 v160,s20,v160\nv_mul_f32 v161,s20,v161\nM v[204:207],a[100:103],a[68:71],v[204:207]\nbuffer_load_dwordx4 v20,s[28:31],s46 offen lds\nM v[200:203],a[96:99],a[68:71],v[200:203]\n" \
"s_add_u32 m0,s51,32768\nds_read_b128 a[36:39],v5 offset:36864\nv_mul_f32 v162,s20,v162\nM v[216:219],a[104:107],a[68:71],v[216:219]\ns_mov_b32 s46,256\nv_mul_f32 v163,s20,v163\nC v66,v68,v69\nM v[220:223],a[108:111],a[68:71],v[220:223]\n" \
"v_mul_f32 v72,s20,v72\nv_mul_f32 v164,s20,v164\nM v[212:215],a[108:111],a[64:67],v[212:215]\nbuffer_load_dwordx4 v21,s[32:35],s47 offen offset:0 lds\nM v[208:211],a[104:107],a[64:67],v[208:211]\ns_add_u32 m0,s51,33664\nv_mul_f32 v73,s20,v73\nC v30,v128,v129\n" \
"M v[224:227],a[112:115],a[64:67],v[224:227]\nv_mul_f32 v165,s20,v165\nC v31,v130,v131\nM v[228:231],a[116:119],a[64:67],v[228:231]\nv_mul_f32 v166,s20,v166\nC v32,v132,v133\nM v[236:239],a[116:119],a[68:71],v[236:239]\nbuffer_load_dwordx4 v21,s[32:35],s47 offen offset:128 lds\n" \
"M v[232:235],a[112:115],a[68:71],v[232:235]\ns_add_u32 m0,s51,40960\nv_mul_f32 v167,s20,v167\nC v33,v134,v135\nM v[248:251],a[120:123],a[68:71],v[248:251]\ns_mov_b32 s47,1310720\nv_mul_f32 v168,s20,v168\nC v34,v136,v137\n" \
"M v[252:255],a[124:127],a[68:71],v[252:255]\nv_mul_f32 v169,s20,v169\nC v35,v138,v139\nM v[244:247],a[124:127],a[64:67],v[244:247]\nbuffer_load_dwordx4 v21,s[32:35],s47 offen offset:0 lds\nM v[240:243],a[120:123],a[64:67],v[240:243]\ns_add_u32 m0,s51,41856\nds_read_b64_tr_b16 a[48:49],v9 offset:8192\n" \
"v_mul_f32 v170,s20,v170\nM a[128:131],a[96:99],a[72:75],a[128:131]\nds_read_b64_tr_b16 a[50:51],v10 offset:8192\nds_read_b128 a[80:83],v6 offset:32768\nM a[132:135],a[100:103],a[72:75],a[132:135]\nds_read_b128 a[84:87],v6 offset:36864\nv_mul_f32 v171,s20,v171\nM a[140:143],a[100:103],a[76:79],a[140:143]\n" \
"buffer_load_dwordx4 v21,s[32:35],s47 offen offset:128 lds\nM a[136:139],a[96:99],a[76:79],a[136:139]\ns_add_u32 m0,s50,0\nv_mul_f32 v74,s20,v74\nC v36,v140,v141\nM a[152:155],a[104:107],a[76:79],a[152:155]\ns_mov_b32 s47,1572864\nds_read_b64_tr_b16 a[52:53],v11 offset:8192\n" \
"v_mul_f32 v75,s20,v75\nM a[156:159],a[108:111],a[76:79],a[156:159]\nds_read_b64_tr_b16 a[54:55],v12 offset:8192\nds_read_b64_tr_b16 a[56:57],v9 offset:8704\nM a[148:151],a[108:111],a[72:75],a[148:151]\nbuffer_load_dwordx4 v13,s[28:31],s46 offen lds\nM a[144:147],a[104:107],a[72:75],a[144:147]\ns_add_u32 m0,s50,4096\n" \
"C v37,v142,v143\nC v38,v144,v145\nM a[160:163],a[112:115],a[72:75],a[160:163]\nds_read_b64_tr_b16 a[58:59],v10 offset:8704\nds_read_b64_tr_b16 a[60:61],v11 offset:8704\nM a[164:167],a[116:119],a[72:75],a[164:167]\nds_read_b64_tr_b16 a[62:63],v12 offset:8704\nC v39,v146,v147\n" \
"M a[172:175],a[116:119],a[76:79],a[172:175]\nbuffer_load_dwordx4 v14,s[28:31],s46 offen lds\nM a[168:171],a[112:115],a[76:79],a[168:171]\ns_add_u32 m0,s50,8192\nds_read_b64_tr_b16 a[64:65],v9 offset:9216\nv_mul_f32 v172,s20,v172\nM a[184:187],a[120:123],a[76:79],a[184:187]\nds_read_b64_tr_b16 a[66:67],v10 offset:9216\n" \
"v_mul_f32 v173,s20,v173\nM a[188:191],a[124:127],a[76:79],a[188:191]\ns_waitcnt lgkmcnt(5)\nds_read_b64_tr_b16 a[68:69],v11 offset:9216\nv_mul_f32 v174,s20,v174\nM a[180:183],a[124:127],a[72:75],a[180:183]\nbuffer_load_dwordx4 v15,s[28:31],s46 offen lds\nM a[176:179],a[120:123],a[72:75],a[176:179]\n" \
"s_branch LC_%=\n.p2align 12\nLAV_%=:\ns_mov_b64 exec,-1\ns_mov_b32 s47,262144\ns_add_u32 m0,s22,16384\ns_lshl_b32 s59,s58,14\nbuffer_load_dwordx4 v21,s[32:35],s47 offen offset:0 lds\n" \
"s_add_u32 s59,s59,s60\ns_mov_b64 s[4:5],s[90:91]\ns_lshl_b32 s99,s58,11\ns_lshl_b32 s96,s60,8\ns_add_u32 s99,s99,s96\ns_add_u32 m0,s22,17280\ns_add_u32 s96,s76,s99\ns_addc_u32 s97,s77,0\n" \
"buffer_load_dwordx4 v21,s[32:35],s47 offen offset:128 lds\ns_add_u32 s76,s78,s59\ns_addc_u32 s77,s79,0\ns_and_b32 s59,s58,63\ns_lshl_b32 s59,s59,13\ns_add_u32 s59,s59,s60\ns_cmp_lt_u32 s58,64\ns_cselect_b64 s[84:85],s[80:81],s[82:83]\n" \
"s_add_u32 m0,s22,18432\ns_add_u32 s84,s84,s59\ns_addc_u32 s85,s85,0\nbuffer_load_dwordx4 v21,s[92:95],s47 offen offset:0 lds\ns_lshl_b32 s59,s8,2\ns_add_u32 s59,s59,%2\ns_lshl_b32 s59,s59,12\ns_add_u32 s88,s72,s59\n" \
"s_addc_u32 s89,s73,0\ns_add_u32 m0,s22,19328\ns_nop 0\nbuffer_load_dwordx4 v21,s[92:95],s47 offen offset:128 lds\ns_mov_b64 exec,0xff\nglobal_load_dword v0,v1,s[84:85]\ns_mov_b64 exec,-1\ns_add_u32 s84,s84,524288\n" \
"s_addc_u32 s85,s85,0\ns_mov_b64 exec,0xff\nglobal_load_dword v0,v1,s[84:85]\ns_mov_b64 exec,-1\ns_add_u32 s84,s84,524288\ns_addc_u32 s85,s85,0\ns_mov_b64 exec,0xff\nglobal_load_dword v0,v1,s[84:85]\n" \
"s_mov_b64 exec,-1\ns_mov_b64 exec,0xff\nglobal_load_dword v0,v2,s[96:97]\ns_mov_b64 exec,-1\ns_mov_b64 exec,0xff00\nglobal_load_dword v0,v2,s[96:97]\ns_mov_b64 exec,-1\ns_waitcnt vmcnt(9)\n" \
"s_barrier\nds_read_b64_tr_b16 a[32:33],v9 offset:0\nds_read_b64_tr_b16 a[34:35],v10 offset:0\nds_read_b64_tr_b16 a[36:37],v11 offset:0\nds_read_b64_tr_b16 a[38:39],v12 offset:0\nds_read_b128 a[24:27],v7 offset:12288\nds_read_b128 a[28:31],v7 offset:14336\ns_waitcnt lgkmcnt(0)\n" \
"M v[128:131],a[24:27],a[32:35],0\ns_add_u32 m0,s22,32768\ns_mov_b32 s47,524288\ns_mov_b32 s46,128\nds_read_b64_tr_b16 a[40:41],v9 offset:512\nds_read_b64_tr_b16 a[42:43],v10 offset:512\nds_read_b64_tr_b16 a[44:45],v11 offset:512\nds_read_b64_tr_b16 a[46:47],v12 offset:512\n" \
"ds_read_b64_tr_b16 a[48:49],v9 offset:1024\nds_read_b64_tr_b16 a[50:51],v10 offset:1024\nds_read_b64_tr_b16 a[52:53],v11 offset:1024\nds_read_b64_tr_b16 a[54:55],v12 offset:1024\nM v[132:135],a[28:31],a[32:35],0\nbuffer_load_dwordx4 v21,s[32:35],s47 offen offset:0 lds\nds_read_b64_tr_b16 a[56:57],v9 offset:1536\nds_read_b64_tr_b16 a[58:59],v10 offset:1536\n" \
"ds_read_b64_tr_b16 a[60:61],v11 offset:1536\nds_read_b64_tr_b16 a[62:63],v12 offset:1536\nM v[140:143],a[28:31],a[36:39],0\ns_add_u32 m0,s22,33664\nM v[136:139],a[24:27],a[36:39],0\ns_waitcnt lgkmcnt(8)\ns_mov_b64 s[40:41],s[66:67]\nds_read_b128 a[16:19],v7 offset:8192\n" \
"M v[152:155],a[24:27],a[44:47],0\ns_and_b32 s41,s41,0xffff\nds_read_b128 a[20:23],v7 offset:10240\nM v[156:159],a[28:31],a[44:47],0\nbuffer_load_dwordx4 v21,s[32:35],s47 offen offset:128 lds\nM v[148:151],a[28:31],a[40:43],0\ns_add_u32 m0,s22,34816\nds_read_b128 a[0:3],v7 offset:0\n" \
"M v[144:147],a[24:27],a[40:43],0\ns_waitcnt lgkmcnt(3)\ns_mov_b32 s42,4194304\nds_read_b128 a[4:7],v7 offset:2048\nM v[160:163],a[24:27],a[48:51],0\ns_mov_b32 s43,0x00020000\nds_read_b128 a[8:11],v7 offset:4096\nM v[164:167],a[28:31],a[48:51],0\n" \
"buffer_load_dwordx4 v21,s[92:95],s47 offen offset:0 lds\nM v[172:175],a[28:31],a[52:55],0\ns_add_u32 m0,s22,35712\nM v[168:171],a[24:27],a[52:55],0\ns_mov_b64 s[80:81],s[68:69]\nds_read_b128 a[12:15],v7 offset:6144\nM v[184:187],a[24:27],a[60:63],0\ns_mov_b32 s52,0\n" \
"ds_read_b128 a[88:91],v8 offset:12288\nM v[188:191],a[28:31],a[60:63],0\nbuffer_load_dwordx4 v21,s[92:95],s47 offen offset:128 lds\nM v[180:183],a[28:31],a[56:59],0\ns_add_u32 m0,s50,0\nds_read_b128 a[92:95],v8 offset:14336\nM v[176:179],a[24:27],a[56:59],0\ns_waitcnt lgkmcnt(3)\n" \
"s_mov_b32 s47,786432\nds_read_b128 a[80:83],v8 offset:8192\nM v[64:67],a[16:19],a[32:35],0\ns_waitcnt vmcnt(9) lgkmcnt(9)\nds_read_b128 a[84:87],v8 offset:10240\nM v[68:71],a[20:23],a[32:35],0\nbuffer_load_dwordx4 v13,s[28:31],s46 offen lds\ns_barrier\n" \
"M v[76:79],a[20:23],a[36:39],0\ns_add_u32 m0,s50,4096\nM v[72:75],a[16:19],a[36:39],0\ns_and_b32 s81,s81,0xffff\nds_read_b64_tr_b16 a[96:97],v9 offset:16384\nds_read_b64_tr_b16 a[98:99],v10 offset:16384\nM v[88:91],a[16:19],a[44:47],0\ns_mov_b32 s82,4194304\n" \
"ds_read_b64_tr_b16 a[100:101],v11 offset:16384\nM v[92:95],a[20:23],a[44:47],0\nbuffer_load_dwordx4 v14,s[28:31],s46 offen lds\nM v[84:87],a[20:23],a[40:43],0\ns_add_u32 m0,s50,8192\nds_read_b64_tr_b16 a[102:103],v12 offset:16384\nM v[80:83],a[16:19],a[40:43],0\ns_mov_b32 s83,0x00020000\n" \
"ds_read_b64_tr_b16 a[104:105],v9 offset:16896\nM v[96:99],a[16:19],a[48:51],0\ns_lshl_b32 s60,s7,18\nds_read_b64_tr_b16 a[106:107],v10 offset:16896\nM v[100:103],a[20:23],a[48:51],0\nbuffer_load_dwordx4 v15,s[28:31],s46 offen lds\nM v[108:111],a[20:23],a[52:55],0\ns_add_u32 m0,s50,12288\n" \
"ds_read_b64_tr_b16 a[108:109],v11 offset:16896\nM v[104:107],a[16:19],a[52:55],0\ns_sub_u32 s61,3932160,s60\nds_read_b64_tr_b16 a[110:111],v12 offset:16896\nM v[120:123],a[16:19],a[60:63],0\ns_add_u32 s44,s78,s98\ns_addc_u32 s45,s79,0\nds_read_b64_tr_b16 a[112:113],v9 offset:17408\n" \
"ds_read_b64_tr_b16 a[114:115],v10 offset:17408\nM v[124:127],a[20:23],a[60:63],0\nbuffer_load_dwordx4 v16,s[28:31],s46 offen lds\nM v[116:119],a[20:23],a[56:59],0\ns_add_u32 m0,s50,16384\ns_mov_b32 s46,128\nM v[112:115],a[16:19],a[56:59],0\ns_and_b32 s45,s45,0xffff\n" \
"ds_read_b64_tr_b16 a[116:117],v11 offset:17408\nds_read_b64_tr_b16 a[118:119],v12 offset:17408\nM v[192:195],a[0:3],a[32:35],0\ns_mov_b64 s[36:37],s[90:91]\nds_read_b64_tr_b16 a[120:121],v9 offset:17920\nM v[196:199],a[4:7],a[32:35],0\nbuffer_load_dwordx4 v17,s[28:31],s46 offen lds\nM v[204:207],a[4:7],a[36:39],0\n" \
"s_add_u32 m0,s50,20480\nds_read_b64_tr_b16 a[122:123],v10 offset:17920\nM v[200:203],a[0:3],a[36:39],0\ns_and_b32 s37,s37,0xffff\nds_read_b64_tr_b16 a[124:125],v11 offset:17920\nM v[216:219],a[0:3],a[44:47],0\ns_mov_b32 s38,134217728\nds_read_b64_tr_b16 a[126:127],v12 offset:17920\n" \
"M v[220:223],a[4:7],a[44:47],0\nbuffer_load_dwordx4 v18,s[28:31],s46 offen lds\nM v[212:215],a[4:7],a[40:43],0\ns_add_u32 m0,s50,24576\nds_read_b128 a[64:67],v8 offset:0\nM v[208:211],a[0:3],a[40:43],0\ns_mov_b32 s39,0x00020000\nds_read_b128 a[68:71],v8 offset:2048\n" \
"M v[224:227],a[0:3],a[48:51],0\ns_lshr_b32 s62,%1,3\nds_read_b128 a[72:75],v8 offset:4096\nM v[228:231],a[4:7],a[48:51],0\nbuffer_load_dwordx4 v19,s[28:31],s46 offen lds\nM v[236:239],a[4:7],a[52:55],0\ns_add_u32 m0,s50,28672\nM v[232:235],a[0:3],a[52:55],0\n" \
"s_lshl_b32 s62,s62,2\nds_read_b128 a[76:79],v8 offset:6144\nM v[248:251],a[0:3],a[60:63],0\ns_add_u32 s62,s62,%2\nM v[252:255],a[4:7],a[60:63],0\nbuffer_load_dwordx4 v20,s[28:31],s46 offen lds\nM v[244:247],a[4:7],a[56:59],0\ns_add_u32 m0,s22,49152\n" \
"M v[240:243],a[0:3],a[56:59],0\ns_waitcnt lgkmcnt(1)\ns_mov_b32 s46,256\nM a[128:131],a[8:11],a[32:35],0\ns_lshl_b32 s62,s62,10\nM a[132:135],a[12:15],a[32:35],0\nbuffer_load_dwordx4 v21,s[32:35],s47 offen offset:0 lds\nM a[140:143],a[12:15],a[36:39],0\n" \
"s_add_u32 m0,s22,50048\nM a[136:139],a[8:11],a[36:39],0\ns_min_u32 s62,s62,LEND_%=-LBEG_%=-1024\nM a[152:155],a[8:11],a[44:47],0\ns_add_u32 s100,s100,s62\ns_addc_u32 s101,s101,0\nM a[156:159],a[12:15],a[44:47],0\nbuffer_load_dwordx4 v21,s[32:35],s47 offen offset:128 lds\n" \
"M a[148:151],a[12:15],a[40:43],0\ns_add_u32 m0,s22,51200\nM a[144:147],a[8:11],a[40:43],0\ns_lshl_b32 s62,s8,2\nM a[160:163],a[8:11],a[48:51],0\ns_add_u32 s62,s62,%2\nM a[164:167],a[12:15],a[48:51],0\nbuffer_load_dwordx4 v21,s[92:95],s47 offen offset:0 lds\n" \
"M a[172:175],a[12:15],a[52:55],0\ns_add_u32 m0,s22,52096\nM a[168:171],a[8:11],a[52:55],0\ns_mov_b64 s[10:11],s[72:73]\nM a[184:187],a[8:11],a[60:63],0\ns_lshl_b32 s63,s62,12\nM a[188:191],a[12:15],a[60:63],0\nbuffer_load_dwordx4 v21,s[92:95],s47 offen offset:128 lds\n" \
"M a[180:183],a[12:15],a[56:59],0\ns_add_u32 m0,s50,32768\nM a[176:179],a[8:11],a[56:59],0\ns_mov_b32 s47,1048576\nM v[128:131],a[88:91],a[96:99],v[128:131]\ns_add_u32 s68,s10,s63\ns_addc_u32 s69,s11,0\nM v[132:135],a[92:95],a[96:99],v[132:135]\n" \
"buffer_load_dwordx4 v13,s[28:31],s46 offen lds\nM v[140:143],a[92:95],a[100:103],v[140:143]\ns_add_u32 m0,s50,36864\nM v[136:139],a[88:91],a[100:103],v[136:139]\ns_and_b32 s69,s69,0xffff\nM v[152:155],a[88:91],a[108:111],v[152:155]\ns_add_u32 s96,s57,32768\nM v[156:159],a[92:95],a[108:111],v[156:159]\n" \
"buffer_load_dwordx4 v14,s[28:31],s46 offen lds\nM v[148:151],a[92:95],a[104:107],v[148:151]\ns_add_u32 m0,s50,40960\nM v[144:147],a[88:91],a[104:107],v[144:147]\ns_lshl_b32 s62,s62,1\nM v[160:163],a[88:91],a[112:115],v[160:163]\ns_mov_b64 s[12:13],s[74:75]\nM v[164:167],a[92:95],a[112:115],v[164:167]\n" \
"buffer_load_dwordx4 v15,s[28:31],s46 offen lds\nM v[172:175],a[92:95],a[116:119],v[172:175]\ns_add_u32 m0,s50,45056\nM v[168:171],a[88:91],a[116:119],v[168:171]\ns_lshr_b32 s21,s63,5\nM v[184:187],a[88:91],a[124:127],v[184:187]\ns_sub_u32 s14,s18,2000\ns_subb_u32 s15,s19,0\n" \
"M v[188:191],a[92:95],a[124:127],v[188:191]\nbuffer_load_dwordx4 v16,s[28:31],s46 offen lds\nM v[180:183],a[92:95],a[120:123],v[180:183]\ns_add_u32 m0,s50,49152\nM v[176:179],a[88:91],a[120:123],v[176:179]\ns_mov_b32 s46,256\nM v[64:67],a[80:83],a[96:99],v[64:67]\ns_mov_b64 s[4:5],s[90:91]\n" \
"M v[68:71],a[84:87],a[96:99],v[68:71]\nbuffer_load_dwordx4 v17,s[28:31],s46 offen lds\nM v[76:79],a[84:87],a[100:103],v[76:79]\ns_add_u32 m0,s50,53248\nM v[72:75],a[80:83],a[100:103],v[72:75]\ns_add_u32 s63,s63,79691776\nM v[88:91],a[80:83],a[108:111],v[88:91]\ns_waitcnt vmcnt(9) lgkmcnt(0)\n" \
"M v[92:95],a[84:87],a[108:111],v[92:95]\ns_add_u32 s16,s14,16777216\ns_addc_u32 s17,s15,0\ns_barrier\nds_read_b64_tr_b16 a[32:33],v9 offset:32768\nM v[84:87],a[84:87],a[104:107],v[84:87]\nbuffer_load_dwordx4 v18,s[28:31],s46 offen lds\nM v[80:83],a[80:83],a[104:107],v[80:83]\n" \
"s_add_u32 m0,s50,57344\nds_read_b64_tr_b16 a[34:35],v10 offset:32768\nM v[96:99],a[80:83],a[112:115],v[96:99]\ns_add_u32 s72,s4,s63\ns_addc_u32 s73,s5,0\nds_read_b64_tr_b16 a[36:37],v11 offset:32768\nds_read_b64_tr_b16 a[38:39],v12 offset:32768\nM v[100:103],a[84:87],a[112:115],v[100:103]\n" \
"s_mov_b32 s63,65536\nds_read_b128 a[24:27],v5 offset:12288\nM v[108:111],a[84:87],a[116:119],v[108:111]\nbuffer_load_dwordx4 v19,s[28:31],s46 offen lds\nM v[104:107],a[80:83],a[116:119],v[104:107]\ns_add_u32 m0,s50,61440\nM v[120:123],a[80:83],a[124:127],v[120:123]\ns_cmp_lt_u32 %2,2\n" \
"s_cselect_b32 s63,32768,s63\nds_read_b128 a[28:31],v5 offset:14336\nM v[124:127],a[84:87],a[124:127],v[124:127]\ns_lshl_b32 s56,%2,14\nds_read_b64_tr_b16 a[40:41],v9 offset:33280\nds_read_b64_tr_b16 a[42:43],v10 offset:33280\nM v[116:119],a[84:87],a[120:123],v[116:119]\nbuffer_load_dwordx4 v20,s[28:31],s46 offen lds\n" \
"M v[112:115],a[80:83],a[120:123],v[112:115]\ns_add_u32 m0,s22,0\nM v[192:195],a[64:67],a[96:99],v[192:195]\ns_mov_b32 s46,384\nds_read_b64_tr_b16 a[44:45],v11 offset:33280\nds_read_b64_tr_b16 a[46:47],v12 offset:33280\nM v[196:199],a[68:71],a[96:99],v[196:199]\ns_add_u32 s56,s56,s63\n" \
"ds_read_b64_tr_b16 a[48:49],v9 offset:33792\nM v[204:207],a[68:71],a[100:103],v[204:207]\nbuffer_load_dwordx4 v21,s[32:35],s47 offen offset:0 lds\nM v[200:203],a[64:67],a[100:103],v[200:203]\ns_add_u32 m0,s22,896\nds_read_b64_tr_b16 a[50:51],v10 offset:33792\nM v[216:219],a[64:67],a[108:111],v[216:219]\ns_and_b32 s73,s73,0xffff\n" \
"ds_read_b64_tr_b16 a[52:53],v11 offset:33792\nM v[220:223],a[68:71],a[108:111],v[220:223]\ns_mov_b32 s74,4096\nds_read_b64_tr_b16 a[54:55],v12 offset:33792\nM v[212:215],a[68:71],a[104:107],v[212:215]\nbuffer_load_dwordx4 v21,s[32:35],s47 offen offset:128 lds\nM v[208:211],a[64:67],a[104:107],v[208:211]\ns_add_u32 m0,s22,2048\n" \
"ds_read_b64_tr_b16 a[56:57],v9 offset:34304\nM v[224:227],a[64:67],a[112:115],v[224:227]\ns_mov_b32 s75,0x00020000\nds_read_b64_tr_b16 a[58:59],v10 offset:34304\nM v[228:231],a[68:71],a[112:115],v[228:231]\ns_add_u32 s84,s84,524288\ns_addc_u32 s85,s85,0\nds_read_b64_tr_b16 a[60:61],v11 offset:34304\n" \
"ds_read_b64_tr_b16 a[62:63],v12 offset:34304\nM v[236:239],a[68:71],a[116:119],v[236:239]\nbuffer_load_dwordx4 v21,s[92:95],s47 offen offset:0 lds\nM v[232:235],a[64:67],a[116:119],v[232:235]\ns_add_u32 m0,s22,2944\nM v[248:251],a[64:67],a[124:127],v[248:251]\ns_add_u32 s21,s21,67108864\nds_read_b128 a[16:19],v5 offset:8192\n" \
"M v[252:255],a[68:71],a[124:127],v[252:255]\ns_mov_b32 s53,s22\nds_read_b128 a[20:23],v5 offset:10240\nM v[244:247],a[68:71],a[120:123],v[244:247]\ns_mov_b64 exec,0xff\nglobal_load_dword v0,v1,s[84:85]\ns_mov_b64 exec,-1\nM v[240:243],a[64:67],a[120:123],v[240:243]\n" \
"s_add_u32 s84,s84,524288\ns_addc_u32 s85,s85,0\nds_read_b128 a[0:3],v5 offset:0\nds_read_b128 a[4:7],v5 offset:2048\nM a[128:131],a[72:75],a[96:99],a[128:131]\ns_mov_b64 exec,0xff\nglobal_load_dword v0,v1,s[84:85]\ns_mov_b64 exec,-1\n" \
"M a[132:135],a[76:79],a[96:99],a[132:135]\ns_add_u32 s84,s84,524288\ns_addc_u32 s85,s85,0\nds_read_b128 a[8:11],v5 offset:4096\nds_read_b128 a[12:15],v5 offset:6144\nM a[140:143],a[76:79],a[100:103],a[140:143]\nglobal_load_dwordx4 v[46:49],v4,s[86:87] offset:0\nM a[136:139],a[72:75],a[100:103],a[136:139]\n" \
"ds_read_b128 a[88:91],v6 offset:12288\nds_read_b128 a[92:95],v6 offset:14336\nM a[152:155],a[72:75],a[108:111],a[152:155]\nds_read_b128 a[64:67],v6 offset:0\nM a[156:159],a[76:79],a[108:111],a[156:159]\nds_read_b128 a[68:71],v6 offset:2048\nM a[148:151],a[76:79],a[104:107],a[148:151]\nglobal_load_dwordx4 v[50:53],v4,s[86:87] offset:1024\n" \
"M a[144:147],a[72:75],a[104:107],a[144:147]\nds_read_b128 a[80:83],v6 offset:8192\nds_read_b128 a[84:87],v6 offset:10240\nM a[160:163],a[72:75],a[112:115],a[160:163]\nM a[164:167],a[76:79],a[112:115],a[164:167]\nM a[172:175],a[76:79],a[116:119],a[172:175]\nglobal_load_dwordx4 v[30:33],v26,s[64:65] offset:0\nM a[168:171],a[72:75],a[116:119],a[168:171]\n" \
"M a[184:187],a[72:75],a[124:127],a[184:187]\nM a[188:191],a[76:79],a[124:127],a[188:191]\nM a[180:183],a[76:79],a[120:123],a[180:183]\nglobal_load_dwordx4 v[34:37],v26,s[64:65] offset:1024\nM a[176:179],a[72:75],a[120:123],a[176:179]\ns_waitcnt lgkmcnt(0)\nds_read_b128 a[72:75],v6 offset:4096\nds_read_b128 a[76:79],v6 offset:6144\n" \
"M v[128:131],a[24:27],a[32:35],v[128:131]\nM v[132:135],a[28:31],a[32:35],v[132:135]\nM v[140:143],a[28:31],a[36:39],v[140:143]\nbuffer_load_dwordx4 v21,s[92:95],s47 offen offset:128 lds\nM v[136:139],a[24:27],a[36:39],v[136:139]\ns_add_u32 m0,s22,16384\nM v[152:155],a[24:27],a[44:47],v[152:155]\ns_mov_b32 s47,1310720\n" \
"M v[156:159],a[28:31],a[44:47],v[156:159]\nM v[148:151],a[28:31],a[40:43],v[148:151]\nbuffer_load_dwordx4 v21,s[32:35],s47 offen offset:0 lds\nM v[144:147],a[24:27],a[40:43],v[144:147]\ns_add_u32 m0,s22,17280\nM v[160:163],a[24:27],a[48:51],v[160:163]\nM v[164:167],a[28:31],a[48:51],v[164:167]\nM v[172:175],a[28:31],a[52:55],v[172:175]\n" \
"buffer_load_dwordx4 v21,s[32:35],s47 offen offset:128 lds\nM v[168:171],a[24:27],a[52:55],v[168:171]\ns_add_u32 m0,s22,18432\nM v[184:187],a[24:27],a[60:63],v[184:187]\ns_waitcnt vmcnt(20) lgkmcnt(14)\nM v[188:191],a[28:31],a[60:63],v[188:191]\ns_barrier\nds_read_b64_tr_b16 a[96:97],v9 offset:49152\n" \
"M v[180:183],a[28:31],a[56:59],v[180:183]\nbuffer_load_dwordx4 v21,s[92:95],s47 offen offset:0 lds\nM v[176:179],a[24:27],a[56:59],v[176:179]\ns_add_u32 m0,s22,19328\nds_read_b64_tr_b16 a[98:99],v10 offset:49152\nM v[64:67],a[16:19],a[32:35],v[64:67]\nds_read_b64_tr_b16 a[100:101],v11 offset:49152\nds_read_b64_tr_b16 a[102:103],v12 offset:49152\n" \
"M v[68:71],a[20:23],a[32:35],v[68:71]\nds_read_b64_tr_b16 a[104:105],v9 offset:49664\nM v[76:79],a[20:23],a[36:39],v[76:79]\nbuffer_load_dwordx4 v21,s[92:95],s47 offen offset:128 lds\nM v[72:75],a[16:19],a[36:39],v[72:75]\ns_add_u32 m0,s50,65536\nds_read_b64_tr_b16 a[106:107],v10 offset:49664\nM v[88:91],a[16:19],a[44:47],v[88:91]\n" \
"s_mov_b32 s47,1572864\nds_read_b64_tr_b16 a[108:109],v11 offset:49664\nM v[92:95],a[20:23],a[44:47],v[92:95]\nds_read_b64_tr_b16 a[110:111],v12 offset:49664\nM v[84:87],a[20:23],a[40:43],v[84:87]\nds_read_b64_tr_b16 a[112:113],v9 offset:50176\nds_read_b64_tr_b16 a[114:115],v10 offset:50176\nM v[80:83],a[16:19],a[40:43],v[80:83]\n" \
"ds_read_b64_tr_b16 a[116:117],v11 offset:50176\nM v[96:99],a[16:19],a[48:51],v[96:99]\nds_read_b64_tr_b16 a[118:119],v12 offset:50176\nM v[100:103],a[20:23],a[48:51],v[100:103]\nds_read_b64_tr_b16 a[120:121],v9 offset:50688\nds_read_b64_tr_b16 a[122:123],v10 offset:50688\nM v[108:111],a[20:23],a[52:55],v[108:111]\nds_read_b64_tr_b16 a[124:125],v11 offset:50688\n" \
"M v[104:107],a[16:19],a[52:55],v[104:107]\ns_mov_b64 exec,0\ns_cmp_lt_u32 %2,2\ns_cbranch_scc1 LG8_%=\ns_branch LG9_%=\nLHA155V_%=:\ns_mov_b64 exec,-1\nds_read_b64_tr_b16 a[126:127],v12 offset:50688\n" \
"M v[120:123],a[16:19],a[60:63],v[120:123]\nM v[124:127],a[20:23],a[60:63],v[124:127]\nM v[116:119],a[20:23],a[56:59],v[116:119]\nM v[112:115],a[16:19],a[56:59],v[112:115]\nM v[192:195],a[0:3],a[32:35],v[192:195]\nM v[196:199],a[4:7],a[32:35],v[196:199]\nM v[204:207],a[4:7],a[36:39],v[204:207]\nM v[200:203],a[0:3],a[36:39],v[200:203]\n" \
"M v[216:219],a[0:3],a[44:47],v[216:219]\nbuffer_load_dwordx4 v13,s[28:31],s46 offen lds\nM v[220:223],a[4:7],a[44:47],v[220:223]\ns_add_u32 m0,s50,69632\nM v[212:215],a[4:7],a[40:43],v[212:215]\nM v[208:211],a[0:3],a[40:43],v[208:211]\nM v[224:227],a[0:3],a[48:51],v[224:227]\nbuffer_load_dwordx4 v14,s[28:31],s46 offen lds\n" \
"M v[228:231],a[4:7],a[48:51],v[228:231]\ns_add_u32 m0,s50,73728\nM v[236:239],a[4:7],a[52:55],v[236:239]\nM v[232:235],a[0:3],a[52:55],v[232:235]\nM v[248:251],a[0:3],a[60:63],v[248:251]\nbuffer_load_dwordx4 v15,s[28:31],s46 offen lds\nM v[252:255],a[4:7],a[60:63],v[252:255]\ns_add_u32 m0,s50,77824\n" \
"M v[244:247],a[4:7],a[56:59],v[244:247]\nM v[240:243],a[0:3],a[56:59],v[240:243]\nM a[128:131],a[8:11],a[32:35],a[128:131]\nbuffer_load_dwordx4 v16,s[28:31],s46 offen lds\nM a[132:135],a[12:15],a[32:35],a[132:135]\ns_add_u32 m0,s50,81920\nM a[140:143],a[12:15],a[36:39],a[140:143]\ns_mov_b32 s46,384\n" \
"M a[136:139],a[8:11],a[36:39],a[136:139]\nM a[152:155],a[8:11],a[44:47],a[152:155]\nbuffer_load_dwordx4 v17,s[28:31],s46 offen lds\nM a[156:159],a[12:15],a[44:47],a[156:159]\ns_add_u32 m0,s50,86016\nM a[148:151],a[12:15],a[40:43],a[148:151]\nM a[144:147],a[8:11],a[40:43],a[144:147]\nM a[160:163],a[8:11],a[48:51],a[160:163]\n" \
"buffer_load_dwordx4 v18,s[28:31],s46 offen lds\nM a[164:167],a[12:15],a[48:51],a[164:167]\ns_add_u32 m0,s50,90112\nM a[172:175],a[12:15],a[52:55],a[172:175]\nM a[168:171],a[8:11],a[52:55],a[168:171]\nM a[184:187],a[8:11],a[60:63],a[184:187]\nbuffer_load_dwordx4 v19,s[28:31],s46 offen lds\nM a[188:191],a[12:15],a[60:63],a[188:191]\n" \
"s_add_u32 m0,s50,94208\nM a[180:183],a[12:15],a[56:59],a[180:183]\nM a[176:179],a[8:11],a[56:59],a[176:179]\ns_waitcnt lgkmcnt(0)\nM v[128:131],a[88:91],a[96:99],v[128:131]\nbuffer_load_dwordx4 v20,s[28:31],s46 offen lds\nM v[132:135],a[92:95],a[96:99],v[132:135]\ns_add_u32 m0,s22,32768\n" \
"M v[140:143],a[92:95],a[100:103],v[140:143]\ns_waitcnt vmcnt(12) lgkmcnt(0)\nM v[136:139],a[88:91],a[100:103],v[136:139]\ns_mov_b32 s46,512\ns_barrier\nds_read_b64_tr_b16 a[32:33],v9 offset:0\nM v[152:155],a[88:91],a[108:111],v[152:155]\nbuffer_load_dwordx4 v21,s[32:35],s47 offen offset:0 lds\n" \
"M v[156:159],a[92:95],a[108:111],v[156:159]\ns_add_u32 m0,s22,33664\nds_read_b64_tr_b16 a[34:35],v10 offset:0\nM v[148:151],a[92:95],a[104:107],v[148:151]\nds_read_b64_tr_b16 a[36:37],v11 offset:0\nds_read_b64_tr_b16 a[38:39],v12 offset:0\nM v[144:147],a[88:91],a[104:107],v[144:147]\nds_read_b64_tr_b16 a[40:41],v9 offset:512\n" \
"M v[160:163],a[88:91],a[112:115],v[160:163]\nbuffer_load_dwordx4 v21,s[32:35],s47 offen offset:128 lds\nM v[164:167],a[92:95],a[112:115],v[164:167]\ns_add_u32 m0,s22,34816\nds_read_b64_tr_b16 a[42:43],v10 offset:512\nM v[172:175],a[92:95],a[116:119],v[172:175]\nds_read_b64_tr_b16 a[44:45],v11 offset:512\nM v[168:171],a[88:91],a[116:119],v[168:171]\n" \
"ds_read_b64_tr_b16 a[46:47],v12 offset:512\nM v[184:187],a[88:91],a[124:127],v[184:187]\nbuffer_load_dwordx4 v21,s[92:95],s47 offen offset:0 lds\nM v[188:191],a[92:95],a[124:127],v[188:191]\ns_add_u32 m0,s22,35712\nds_read_b64_tr_b16 a[48:49],v9 offset:1024\nM v[180:183],a[92:95],a[120:123],v[180:183]\nds_read_b64_tr_b16 a[50:51],v10 offset:1024\n" \
"M v[176:179],a[88:91],a[120:123],v[176:179]\nds_read_b64_tr_b16 a[52:53],v11 offset:1024\nds_read_b64_tr_b16 a[54:55],v12 offset:1024\nM v[64:67],a[80:83],a[96:99],v[64:67]\nLG4_%=:\ns_cbranch_execz LG6_%=\nbuffer_load_dwordx4 v21,s[92:95],s47 offen offset:128 lds\nM v[68:71],a[84:87],a[96:99],v[68:71]\n" \
"s_add_u32 m0,s22,49152\nM v[76:79],a[84:87],a[100:103],v[76:79]\ns_mov_b32 s47,1835008\nds_read_b64_tr_b16 a[56:57],v9 offset:1536\nds_read_b64_tr_b16 a[58:59],v10 offset:1536\nM v[72:75],a[80:83],a[100:103],v[72:75]\nds_read_b64_tr_b16 a[60:61],v11 offset:1536\nM v[88:91],a[80:83],a[108:111],v[88:91]\n" \
"buffer_load_dwordx4 v21,s[32:35],s47 offen offset:0 lds\nM v[92:95],a[84:87],a[108:111],v[92:95]\ns_add_u32 m0,s22,50048\nds_read_b64_tr_b16 a[62:63],v12 offset:1536\nM v[84:87],a[84:87],a[104:107],v[84:87]\nds_read_b128 a[24:27],v5 offset:45056\nM v[80:83],a[80:83],a[104:107],v[80:83]\nds_read_b128 a[28:31],v5 offset:47104\n" \
"M v[96:99],a[80:83],a[112:115],v[96:99]\nbuffer_load_dwordx4 v21,s[32:35],s47 offen offset:128 lds\nM v[100:103],a[84:87],a[112:115],v[100:103]\ns_add_u32 m0,s22,51200\nM v[108:111],a[84:87],a[116:119],v[108:111]\nds_read_b128 a[16:19],v5 offset:40960\nM v[104:107],a[80:83],a[116:119],v[104:107]\nds_read_b128 a[20:23],v5 offset:43008\n" \
"M v[120:123],a[80:83],a[124:127],v[120:123]\nbuffer_load_dwordx4 v21,s[92:95],s47 offen offset:0 lds\nM v[124:127],a[84:87],a[124:127],v[124:127]\ns_add_u32 m0,s22,52096\nds_read_b128 a[0:3],v5 offset:32768\nM v[116:119],a[84:87],a[120:123],v[116:119]\nds_read_b128 a[4:7],v5 offset:34816\nM v[112:115],a[80:83],a[120:123],v[112:115]\n" \
"ds_read_b128 a[8:11],v5 offset:36864\nM v[192:195],a[64:67],a[96:99],v[192:195]\nbuffer_load_dwordx4 v21,s[92:95],s47 offen offset:128 lds\nM v[196:199],a[68:71],a[96:99],v[196:199]\ns_add_u32 m0,s50,0\nM v[204:207],a[68:71],a[100:103],v[204:207]\ns_mov_b32 s47,2097152\nds_read_b128 a[12:15],v5 offset:38912\n" \
"M v[200:203],a[64:67],a[100:103],v[200:203]\nds_read_b128 a[80:83],v6 offset:40960\nM v[216:219],a[64:67],a[108:111],v[216:219]\nbuffer_load_dwordx4 v13,s[28:31],s46 offen lds\nM v[220:223],a[68:71],a[108:111],v[220:223]\ns_add_u32 m0,s50,4096\nds_read_b128 a[84:87],v6 offset:43008\nM v[212:215],a[68:71],a[104:107],v[212:215]\n" \
"ds_read_b128 a[88:91],v6 offset:45056\nM v[208:211],a[64:67],a[104:107],v[208:211]\nds_read_b128 a[92:95],v6 offset:47104\nM v[224:227],a[64:67],a[112:115],v[224:227]\nbuffer_load_dwordx4 v14,s[28:31],s46 offen lds\nM v[228:231],a[68:71],a[112:115],v[228:231]\ns_add_u32 m0,s50,8192\nM v[236:239],a[68:71],a[116:119],v[236:239]\n" \
"M v[232:235],a[64:67],a[116:119],v[232:235]\nM v[248:251],a[64:67],a[124:127],v[248:251]\nM v[252:255],a[68:71],a[124:127],v[252:255]\nbuffer_load_dwordx4 v15,s[28:31],s46 offen lds\nM v[244:247],a[68:71],a[120:123],v[244:247]\ns_add_u32 m0,s50,12288\nM v[240:243],a[64:67],a[120:123],v[240:243]\nds_read_b128 a[64:67],v6 offset:32768\n" \
"ds_read_b128 a[68:71],v6 offset:34816\nM a[128:131],a[72:75],a[96:99],a[128:131]\nM a[132:135],a[76:79],a[96:99],a[132:135]\nbuffer_load_dwordx4 v16,s[28:31],s46 offen lds\nM a[140:143],a[76:79],a[100:103],a[140:143]\ns_add_u32 m0,s50,16384\nM a[136:139],a[72:75],a[100:103],a[136:139]\ns_mov_b32 s46,512\n" \
"M a[152:155],a[72:75],a[108:111],a[152:155]\nM a[156:159],a[76:79],a[108:111],a[156:159]\nM a[148:151],a[76:79],a[104:107],a[148:151]\ns_waitcnt vmcnt(20) lgkmcnt(14)\nM a[144:147],a[72:75],a[104:107],a[144:147]\ns_barrier\nds_read_b64_tr_b16 a[96:97],v9 offset:16384\nM a[160:163],a[72:75],a[112:115],a[160:163]\n" \
"buffer_load_dwordx4 v17,s[28:31],s46 offen lds\nM a[164:167],a[76:79],a[112:115],a[164:167]\ns_add_u32 m0,s50,20480\nds_read_b64_tr_b16 a[98:99],v10 offset:16384\nM a[172:175],a[76:79],a[116:119],a[172:175]\nds_read_b64_tr_b16 a[100:101],v11 offset:16384\nds_read_b64_tr_b16 a[102:103],v12 offset:16384\nM a[168:171],a[72:75],a[116:119],a[168:171]\n" \
"ds_read_b64_tr_b16 a[104:105],v9 offset:16896\nM a[184:187],a[72:75],a[124:127],a[184:187]\nbuffer_load_dwordx4 v18,s[28:31],s46 offen lds\nM a[188:191],a[76:79],a[124:127],a[188:191]\ns_add_u32 m0,s50,24576\nds_read_b64_tr_b16 a[106:107],v10 offset:16896\nM a[180:183],a[76:79],a[120:123],a[180:183]\nds_read_b64_tr_b16 a[108:109],v11 offset:16896\n" \
"M a[176:179],a[72:75],a[120:123],a[176:179]\ns_waitcnt lgkmcnt(5)\nds_read_b128 a[72:75],v6 offset:36864\nM v[128:131],a[24:27],a[32:35],v[128:131]\nbuffer_load_dwordx4 v19,s[28:31],s46 offen lds\nM v[132:135],a[28:31],a[32:35],v[132:135]\ns_add_u32 m0,s50,28672\nds_read_b128 a[76:79],v6 offset:38912\n" \
"M v[140:143],a[28:31],a[36:39],v[140:143]\nds_read_b64_tr_b16 a[110:111],v12 offset:16896\nM v[136:139],a[24:27],a[36:39],v[136:139]\nds_read_b64_tr_b16 a[112:113],v9 offset:17408\nM v[152:155],a[24:27],a[44:47],v[152:155]\nbuffer_load_dwordx4 v20,s[28:31],s46 offen lds\nM v[156:159],a[28:31],a[44:47],v[156:159]\ns_add_u32 m0,s22,0\n" \
"ds_read_b64_tr_b16 a[114:115],v10 offset:17408\nM v[148:151],a[28:31],a[40:43],v[148:151]\ns_mov_b32 s46,640\nds_read_b64_tr_b16 a[116:117],v11 offset:17408\nM v[144:147],a[24:27],a[40:43],v[144:147]\nds_read_b64_tr_b16 a[118:119],v12 offset:17408\nM v[160:163],a[24:27],a[48:51],v[160:163]\nbuffer_load_dwordx4 v21,s[32:35],s47 offen offset:0 lds\n" \
"M v[164:167],a[28:31],a[48:51],v[164:167]\ns_add_u32 m0,s22,896\nds_read_b64_tr_b16 a[120:121],v9 offset:17920\nM v[172:175],a[28:31],a[52:55],v[172:175]\nds_read_b64_tr_b16 a[122:123],v10 offset:17920\nM v[168:171],a[24:27],a[52:55],v[168:171]\nds_read_b64_tr_b16 a[124:125],v11 offset:17920\nds_read_b64_tr_b16 a[126:127],v12 offset:17920\n" \
"M v[184:187],a[24:27],a[60:63],v[184:187]\nbuffer_load_dwordx4 v21,s[32:35],s47 offen offset:128 lds\nM v[188:191],a[28:31],a[60:63],v[188:191]\ns_add_u32 m0,s22,2048\nM v[180:183],a[28:31],a[56:59],v[180:183]\nM v[176:179],a[24:27],a[56:59],v[176:179]\nM v[64:67],a[16:19],a[32:35],v[64:67]\nM v[68:71],a[20:23],a[32:35],v[68:71]\n" \
"buffer_load_dwordx4 v21,s[92:95],s47 offen offset:0 lds\nM v[76:79],a[20:23],a[36:39],v[76:79]\ns_add_u32 m0,s22,2944\nM v[72:75],a[16:19],a[36:39],v[72:75]\nM v[88:91],a[16:19],a[44:47],v[88:91]\nM v[92:95],a[20:23],a[44:47],v[92:95]\nbuffer_load_dwordx4 v21,s[92:95],s47 offen offset:128 lds\nM v[84:87],a[20:23],a[40:43],v[84:87]\n" \
"s_add_u32 m0,s22,16384\nM v[80:83],a[16:19],a[40:43],v[80:83]\ns_mov_b32 s47,2359296\nM v[96:99],a[16:19],a[48:51],v[96:99]\nM v[100:103],a[20:23],a[48:51],v[100:103]\nglobal_load_dwordx4 v[38:41],v26,s[64:65] offset:2048\nM v[108:111],a[20:23],a[52:55],v[108:111]\nM v[104:107],a[16:19],a[52:55],v[104:107]\n" \
"M v[120:123],a[16:19],a[60:63],v[120:123]\nM v[124:127],a[20:23],a[60:63],v[124:127]\nglobal_load_dwordx4 v[42:45],v26,s[64:65] offset:3072\nM v[116:119],a[20:23],a[56:59],v[116:119]\nM v[112:115],a[16:19],a[56:59],v[112:115]\nM v[192:195],a[0:3],a[32:35],v[192:195]\nM v[196:199],a[4:7],a[32:35],v[196:199]\ns_mov_b64 exec,0xff\n" \
"global_load_dword v0,v1,s[84:85]\ns_mov_b64 exec,-1\nM v[204:207],a[4:7],a[36:39],v[204:207]\ns_add_u32 s84,s84,524288\ns_addc_u32 s85,s85,0\nM v[200:203],a[0:3],a[36:39],v[200:203]\nglobal_load_dwordx4 a[230:233],v27,s[70:71] offset:0\nM v[216:219],a[0:3],a[44:47],v[216:219]\n" \
"M v[220:223],a[4:7],a[44:47],v[220:223]\nM v[212:215],a[4:7],a[40:43],v[212:215]\nM v[208:211],a[0:3],a[40:43],v[208:211]\nglobal_load_dwordx4 a[234:237],v27,s[70:71] offset:16\nM v[224:227],a[0:3],a[48:51],v[224:227]\ns_mov_b32 s70,4096\nM v[228:231],a[4:7],a[48:51],v[228:231]\ns_mov_b32 s71,0x00020000\n" \
"M v[236:239],a[4:7],a[52:55],v[236:239]\nM v[232:235],a[0:3],a[52:55],v[232:235]\nbuffer_load_dwordx2 a[192:193],v54,s[40:43],0 offen offset:0 nt\nM v[248:251],a[0:3],a[60:63],v[248:251]\nM v[252:255],a[4:7],a[60:63],v[252:255]\nM v[244:247],a[4:7],a[56:59],v[244:247]\nM v[240:243],a[0:3],a[56:59],v[240:243]\nbuffer_load_dwordx2 a[194:195],v54,s[40:43],0 offen offset:2048 nt\n" \
"M a[128:131],a[8:11],a[32:35],a[128:131]\nM a[132:135],a[12:15],a[32:35],a[132:135]\nM a[140:143],a[12:15],a[36:39],a[140:143]\nM a[136:139],a[8:11],a[36:39],a[136:139]\nbuffer_load_dwordx2 a[196:197],v55,s[40:43],0 offen offset:0 nt\nM a[152:155],a[8:11],a[44:47],a[152:155]\nM a[156:159],a[12:15],a[44:47],a[156:159]\nM a[148:151],a[12:15],a[40:43],a[148:151]\n" \
"s_waitcnt vmcnt(24) lgkmcnt(0)\nM a[144:147],a[8:11],a[40:43],a[144:147]\ns_barrier\nds_read_b64_tr_b16 a[32:33],v9 offset:32768\nM a[160:163],a[8:11],a[48:51],a[160:163]\nbuffer_load_dwordx2 a[198:199],v59,s[40:43],s52 offen offset:0 nt\nM a[164:167],a[12:15],a[48:51],a[164:167]\nds_read_b64_tr_b16 a[34:35],v10 offset:32768\n" \
"ds_read_b64_tr_b16 a[36:37],v11 offset:32768\nM a[172:175],a[12:15],a[52:55],a[172:175]\nds_read_b64_tr_b16 a[38:39],v12 offset:32768\nM a[168:171],a[8:11],a[52:55],a[168:171]\nds_read_b64_tr_b16 a[40:41],v9 offset:33280\nds_read_b64_tr_b16 a[42:43],v10 offset:33280\nM a[184:187],a[8:11],a[60:63],a[184:187]\nbuffer_load_dwordx2 a[214:215],v59,s[80:83],s52 offen offset:0 nt\n" \
"M a[188:191],a[12:15],a[60:63],a[188:191]\nds_read_b64_tr_b16 a[44:45],v11 offset:33280\nds_read_b64_tr_b16 a[46:47],v12 offset:33280\nM a[180:183],a[12:15],a[56:59],a[180:183]\nds_read_b64_tr_b16 a[48:49],v9 offset:33792\nM a[176:179],a[8:11],a[56:59],a[176:179]\nds_read_b64_tr_b16 a[50:51],v10 offset:33792\nds_read_b64_tr_b16 a[52:53],v11 offset:33792\n" \
"M v[128:131],a[88:91],a[96:99],v[128:131]\nbuffer_load_dwordx2 a[200:201],v59,s[40:43],s52 offen offset:2048 nt\nM v[132:135],a[92:95],a[96:99],v[132:135]\nds_read_b64_tr_b16 a[54:55],v12 offset:33792\nds_read_b64_tr_b16 a[56:57],v9 offset:34304\nM v[140:143],a[92:95],a[100:103],v[140:143]\nds_read_b64_tr_b16 a[58:59],v10 offset:34304\nM v[136:139],a[88:91],a[100:103],v[136:139]\n" \
"ds_read_b64_tr_b16 a[60:61],v11 offset:34304\nds_read_b64_tr_b16 a[62:63],v12 offset:34304\nM v[152:155],a[88:91],a[108:111],v[152:155]\nbuffer_load_dwordx2 a[216:217],v59,s[80:83],s52 offen offset:2048 nt\nM v[156:159],a[92:95],a[108:111],v[156:159]\ns_mov_b32 s52,4096\nds_read_b128 a[24:27],v7 offset:12288\nds_read_b128 a[28:31],v7 offset:14336\n" \
"M v[148:151],a[92:95],a[104:107],v[148:151]\nds_read_b128 a[16:19],v7 offset:8192\nM v[144:147],a[88:91],a[104:107],v[144:147]\nds_read_b128 a[20:23],v7 offset:10240\nM v[160:163],a[88:91],a[112:115],v[160:163]\nbuffer_load_dwordx2 a[202:203],v59,s[40:43],s52 offen offset:0 nt\nM v[164:167],a[92:95],a[112:115],v[164:167]\nds_read_b128 a[0:3],v7 offset:0\n" \
"ds_read_b128 a[4:7],v7 offset:2048\nM v[172:175],a[92:95],a[116:119],v[172:175]\nds_read_b128 a[8:11],v7 offset:4096\nM v[168:171],a[88:91],a[116:119],v[168:171]\nds_read_b128 a[12:15],v7 offset:6144\nM v[184:187],a[88:91],a[124:127],v[184:187]\nbuffer_load_dwordx2 a[218:219],v59,s[80:83],s52 offen offset:0 nt\nM v[188:191],a[92:95],a[124:127],v[188:191]\n" \
"M v[180:183],a[92:95],a[120:123],v[180:183]\nM v[176:179],a[88:91],a[120:123],v[176:179]\nds_read_b128 a[88:91],v8 offset:12288\nds_read_b128 a[92:95],v8 offset:14336\nM v[64:67],a[80:83],a[96:99],v[64:67]\nbuffer_load_dwordx2 a[204:205],v59,s[40:43],s52 offen offset:2048 nt\nM v[68:71],a[84:87],a[96:99],v[68:71]\nM v[76:79],a[84:87],a[100:103],v[76:79]\n" \
"M v[72:75],a[80:83],a[100:103],v[72:75]\nM v[88:91],a[80:83],a[108:111],v[88:91]\nbuffer_load_dwordx2 a[220:221],v59,s[80:83],s52 offen offset:2048 nt\nM v[92:95],a[84:87],a[108:111],v[92:95]\ns_mov_b32 s52,8192\nM v[84:87],a[84:87],a[104:107],v[84:87]\nM v[80:83],a[80:83],a[104:107],v[80:83]\nM v[96:99],a[80:83],a[112:115],v[96:99]\n" \
"M v[100:103],a[84:87],a[112:115],v[100:103]\nM v[108:111],a[84:87],a[116:119],v[108:111]\nM v[104:107],a[80:83],a[116:119],v[104:107]\nM v[120:123],a[80:83],a[124:127],v[120:123]\nM v[124:127],a[84:87],a[124:127],v[124:127]\nM v[116:119],a[84:87],a[120:123],v[116:119]\nM v[112:115],a[80:83],a[120:123],v[112:115]\nds_read_b128 a[80:83],v8 offset:8192\n" \
"ds_read_b128 a[84:87],v8 offset:10240\nM v[192:195],a[64:67],a[96:99],v[192:195]\nM v[196:199],a[68:71],a[96:99],v[196:199]\nM v[204:207],a[68:71],a[100:103],v[204:207]\nM v[200:203],a[64:67],a[100:103],v[200:203]\nM v[216:219],a[64:67],a[108:111],v[216:219]\nM v[220:223],a[68:71],a[108:111],v[220:223]\nM v[212:215],a[68:71],a[104:107],v[212:215]\n" \
"M v[208:211],a[64:67],a[104:107],v[208:211]\nM v[224:227],a[64:67],a[112:115],v[224:227]\nM v[228:231],a[68:71],a[112:115],v[228:231]\nM v[236:239],a[68:71],a[116:119],v[236:239]\nM v[232:235],a[64:67],a[116:119],v[232:235]\nM v[248:251],a[64:67],a[124:127],v[248:251]\nM v[252:255],a[68:71],a[124:127],v[252:255]\nM v[244:247],a[68:71],a[120:123],v[244:247]\n" \
"M v[240:243],a[64:67],a[120:123],v[240:243]\nds_read_b128 a[64:67],v8 offset:0\nds_read_b128 a[68:71],v8 offset:2048\nM a[128:131],a[72:75],a[96:99],a[128:131]\nM a[132:135],a[76:79],a[96:99],a[132:135]\nM a[140:143],a[76:79],a[100:103],a[140:143]\nM a[136:139],a[72:75],a[100:103],a[136:139]\nM a[152:155],a[72:75],a[108:111],a[152:155]\n" \
"M a[156:159],a[76:79],a[108:111],a[156:159]\nM a[148:151],a[76:79],a[104:107],a[148:151]\ns_waitcnt vmcnt(28) lgkmcnt(14)\nM a[144:147],a[72:75],a[104:107],a[144:147]\ns_barrier\nds_read_b64_tr_b16 a[96:97],v9 offset:49152\nM a[160:163],a[72:75],a[112:115],a[160:163]\nds_read_b64_tr_b16 a[98:99],v10 offset:49152\n" \
"ds_read_b64_tr_b16 a[100:101],v11 offset:49152\nM a[164:167],a[76:79],a[112:115],a[164:167]\nds_read_b64_tr_b16 a[102:103],v12 offset:49152\nM a[172:175],a[76:79],a[116:119],a[172:175]\nds_read_b64_tr_b16 a[104:105],v9 offset:49664\nds_read_b64_tr_b16 a[106:107],v10 offset:49664\nM a[168:171],a[72:75],a[116:119],a[168:171]\nds_read_b64_tr_b16 a[108:109],v11 offset:49664\n" \
"M a[184:187],a[72:75],a[124:127],a[184:187]\nds_read_b64_tr_b16 a[110:111],v12 offset:49664\nM a[188:191],a[76:79],a[124:127],a[188:191]\nds_read_b64_tr_b16 a[112:113],v9 offset:50176\nds_read_b64_tr_b16 a[114:115],v10 offset:50176\nM a[180:183],a[76:79],a[120:123],a[180:183]\nds_read_b64_tr_b16 a[116:117],v11 offset:50176\nM a[176:179],a[72:75],a[120:123],a[176:179]\n" \
"s_waitcnt lgkmcnt(7)\nds_read_b128 a[72:75],v8 offset:4096\nM v[128:131],a[24:27],a[32:35],v[128:131]\nds_read_b128 a[76:79],v8 offset:6144\nM v[132:135],a[28:31],a[32:35],v[132:135]\nbuffer_load_dwordx4 v21,s[32:35],s47 offen offset:0 lds\nM v[140:143],a[28:31],a[36:39],v[140:143]\ns_add_u32 m0,s22,17280\n" \
"ds_read_b64_tr_b16 a[118:119],v12 offset:50176\nM v[136:139],a[24:27],a[36:39],v[136:139]\nds_read_b64_tr_b16 a[120:121],v9 offset:50688\nM v[152:155],a[24:27],a[44:47],v[152:155]\nds_read_b64_tr_b16 a[122:123],v10 offset:50688\nM v[156:159],a[28:31],a[44:47],v[156:159]\nbuffer_load_dwordx4 v21,s[32:35],s47 offen offset:128 lds\nM v[148:151],a[28:31],a[40:43],v[148:151]\n" \
"s_add_u32 m0,s22,18432\nds_read_b64_tr_b16 a[124:125],v11 offset:50688\nM v[144:147],a[24:27],a[40:43],v[144:147]\nds_read_b64_tr_b16 a[126:127],v12 offset:50688\nM v[160:163],a[24:27],a[48:51],v[160:163]\nM v[164:167],a[28:31],a[48:51],v[164:167]\nbuffer_load_dwordx4 v21,s[92:95],s47 offen offset:0 lds\nM v[172:175],a[28:31],a[52:55],v[172:175]\n" \
"s_add_u32 m0,s22,19328\nM v[168:171],a[24:27],a[52:55],v[168:171]\nM v[184:187],a[24:27],a[60:63],v[184:187]\nM v[188:191],a[28:31],a[60:63],v[188:191]\nbuffer_load_dwordx4 v21,s[92:95],s47 offen offset:128 lds\nM v[180:183],a[28:31],a[56:59],v[180:183]\ns_add_u32 m0,s50,32768\nM v[176:179],a[24:27],a[56:59],v[176:179]\n" \
"s_mov_b32 s47,2621440\nM v[64:67],a[16:19],a[32:35],v[64:67]\nM v[68:71],a[20:23],a[32:35],v[68:71]\nbuffer_load_dwordx4 v13,s[28:31],s46 offen lds\nM v[76:79],a[20:23],a[36:39],v[76:79]\ns_add_u32 m0,s50,36864\nM v[72:75],a[16:19],a[36:39],v[72:75]\nM v[88:91],a[16:19],a[44:47],v[88:91]\n" \
"M v[92:95],a[20:23],a[44:47],v[92:95]\nbuffer_load_dwordx4 v14,s[28:31],s46 offen lds\nM v[84:87],a[20:23],a[40:43],v[84:87]\ns_add_u32 m0,s50,40960\nM v[80:83],a[16:19],a[40:43],v[80:83]\nM v[96:99],a[16:19],a[48:51],v[96:99]\nM v[100:103],a[20:23],a[48:51],v[100:103]\nbuffer_load_dwordx4 v15,s[28:31],s46 offen lds\n" \
"M v[108:111],a[20:23],a[52:55],v[108:111]\ns_add_u32 m0,s50,45056\nM v[104:107],a[16:19],a[52:55],v[104:107]\nM v[120:123],a[16:19],a[60:63],v[120:123]\nM v[124:127],a[20:23],a[60:63],v[124:127]\nbuffer_load_dwordx4 v16,s[28:31],s46 offen lds\nM v[116:119],a[20:23],a[56:59],v[116:119]\ns_add_u32 m0,s50,49152\n" \
"M v[112:115],a[16:19],a[56:59],v[112:115]\ns_mov_b32 s46,640\nM v[192:195],a[0:3],a[32:35],v[192:195]\nM v[196:199],a[4:7],a[32:35],v[196:199]\nbuffer_load_dwordx4 v17,s[28:31],s46 offen lds\nM v[204:207],a[4:7],a[36:39],v[204:207]\ns_add_u32 m0,s50,53248\nM v[200:203],a[0:3],a[36:39],v[200:203]\n" \
"M v[216:219],a[0:3],a[44:47],v[216:219]\nM v[220:223],a[4:7],a[44:47],v[220:223]\nbuffer_load_dwordx4 v18,s[28:31],s46 offen lds\nM v[212:215],a[4:7],a[40:43],v[212:215]\ns_add_u32 m0,s50,57344\nM v[208:211],a[0:3],a[40:43],v[208:211]\nM v[224:227],a[0:3],a[48:51],v[224:227]\nM v[228:231],a[4:7],a[48:51],v[228:231]\n" \
"buffer_load_dwordx4 v19,s[28:31],s46 offen lds\nM v[236:239],a[4:7],a[52:55],v[236:239]\ns_add_u32 m0,s50,61440\nM v[232:235],a[0:3],a[52:55],v[232:235]\nM v[248:251],a[0:3],a[60:63],v[248:251]\nM v[252:255],a[4:7],a[60:63],v[252:255]\nbuffer_load_dwordx4 v20,s[28:31],s46 offen lds\nM v[244:247],a[4:7],a[56:59],v[244:247]\n" \
"s_add_u32 m0,s22,32768\nM v[240:243],a[0:3],a[56:59],v[240:243]\ns_mov_b32 s46,768\nM a[128:131],a[8:11],a[32:35],a[128:131]\nM a[132:135],a[12:15],a[32:35],a[132:135]\nbuffer_load_dwordx4 v21,s[32:35],s47 offen offset:0 lds\nM a[140:143],a[12:15],a[36:39],a[140:143]\ns_add_u32 m0,s22,33664\n" \
"M a[136:139],a[8:11],a[36:39],a[136:139]\nM a[152:155],a[8:11],a[44:47],a[152:155]\nM a[156:159],a[12:15],a[44:47],a[156:159]\nbuffer_load_dwordx4 v21,s[32:35],s47 offen offset:128 lds\nM a[148:151],a[12:15],a[40:43],a[148:151]\ns_waitcnt vmcnt(30) lgkmcnt(0)\nM a[144:147],a[8:11],a[40:43],a[144:147]\ns_add_u32 m0,s22,34816\n" \
"s_barrier\nds_read_b64_tr_b16 a[32:33],v9 offset:0\nM a[160:163],a[8:11],a[48:51],a[160:163]\nds_read_b64_tr_b16 a[34:35],v10 offset:0\nds_read_b64_tr_b16 a[36:37],v11 offset:0\nM a[164:167],a[12:15],a[48:51],a[164:167]\nbuffer_load_dwordx4 v21,s[92:95],s47 offen offset:0 lds\nM a[172:175],a[12:15],a[52:55],a[172:175]\n" \
"s_add_u32 m0,s22,35712\nds_read_b64_tr_b16 a[38:39],v12 offset:0\nM a[168:171],a[8:11],a[52:55],a[168:171]\nds_read_b64_tr_b16 a[40:41],v9 offset:512\nM a[184:187],a[8:11],a[60:63],a[184:187]\nds_read_b64_tr_b16 a[42:43],v10 offset:512\nM a[188:191],a[12:15],a[60:63],a[188:191]\nbuffer_load_dwordx4 v21,s[92:95],s47 offen offset:128 lds\n" \
"M a[180:183],a[12:15],a[56:59],a[180:183]\ns_add_u32 m0,s22,49152\nds_read_b64_tr_b16 a[44:45],v11 offset:512\nM a[176:179],a[8:11],a[56:59],a[176:179]\ns_mov_b32 s47,2883584\nds_read_b64_tr_b16 a[46:47],v12 offset:512\nM v[128:131],a[88:91],a[96:99],v[128:131]\nds_read_b64_tr_b16 a[48:49],v9 offset:1024\n" \
"ds_read_b64_tr_b16 a[50:51],v10 offset:1024\nM v[132:135],a[92:95],a[96:99],v[132:135]\nbuffer_load_dwordx4 v21,s[32:35],s47 offen offset:0 lds\nM v[140:143],a[92:95],a[100:103],v[140:143]\ns_add_u32 m0,s22,50048\nM v[136:139],a[88:91],a[100:103],v[136:139]\nds_read_b64_tr_b16 a[52:53],v11 offset:1024\nds_read_b64_tr_b16 a[54:55],v12 offset:1024\n" \
"M v[152:155],a[88:91],a[108:111],v[152:155]\nds_read_b64_tr_b16 a[56:57],v9 offset:1536\nM v[156:159],a[92:95],a[108:111],v[156:159]\nbuffer_load_dwordx4 v21,s[32:35],s47 offen offset:128 lds\nM v[148:151],a[92:95],a[104:107],v[148:151]\ns_add_u32 m0,s22,51200\nds_read_b64_tr_b16 a[58:59],v10 offset:1536\nM v[144:147],a[88:91],a[104:107],v[144:147]\n" \
"s_waitcnt vmcnt(32)\nds_read_b64_tr_b16 a[60:61],v11 offset:1536\nv_lshlrev_b32 v25,16,v46\nM v[160:163],a[88:91],a[112:115],v[160:163]\nds_read_b64_tr_b16 a[62:63],v12 offset:1536\nv_lshlrev_b32 v26,16,v30\nM v[164:167],a[92:95],a[112:115],v[164:167]\nbuffer_load_dwordx4 v21,s[92:95],s47 offen offset:0 lds\n" \
"M v[172:175],a[92:95],a[116:119],v[172:175]\ns_add_u32 m0,s22,52096\nds_read_b128 a[24:27],v5 offset:12288\nv_and_b32 v46,0xffff0000,v46\nM v[168:171],a[88:91],a[116:119],v[168:171]\nds_read_b128 a[28:31],v5 offset:14336\nv_mul_f32 v23,v26,v25\nM v[184:187],a[88:91],a[124:127],v[184:187]\n" \
"ds_read_b128 a[16:19],v5 offset:8192\nv_and_b32 v30,0xffff0000,v30\nM v[188:191],a[92:95],a[124:127],v[188:191]\nds_read_b128 a[20:23],v5 offset:10240\nv_lshlrev_b32 v26,16,v38\nM v[180:183],a[92:95],a[120:123],v[180:183]\nbuffer_load_dwordx4 v21,s[92:95],s47 offen offset:128 lds\nM v[176:179],a[88:91],a[120:123],v[176:179]\n" \
"s_add_u32 m0,s50,65536\nv_fmac_f32 v23,v30,v46\nv_mul_f32 v24,v26,v25\nM v[64:67],a[80:83],a[96:99],v[64:67]\ns_mov_b32 s47,3145728\nds_read_b128 a[0:3],v5 offset:0\nv_lshlrev_b32 v25,16,v47\nM v[68:71],a[84:87],a[96:99],v[68:71]\n" \
"ds_read_b128 a[4:7],v5 offset:2048\nv_lshlrev_b32 v26,16,v31\nM v[76:79],a[84:87],a[100:103],v[76:79]\nbuffer_load_dwordx4 v13,s[28:31],s46 offen lds\nM v[72:75],a[80:83],a[100:103],v[72:75]\ns_add_u32 m0,s50,69632\nds_read_b128 a[8:11],v5 offset:4096\nv_and_b32 v38,0xffff0000,v38\n" \
"M v[88:91],a[80:83],a[108:111],v[88:91]\nds_read_b128 a[12:15],v5 offset:6144\nv_fmac_f32 v23,v26,v25\nM v[92:95],a[84:87],a[108:111],v[92:95]\nds_read_b128 a[88:91],v6 offset:12288\nv_fmac_f32 v24,v38,v46\nM v[84:87],a[84:87],a[104:107],v[84:87]\nds_read_b128 a[92:95],v6 offset:14336\n" \
"v_and_b32 v47,0xffff0000,v47\nM v[80:83],a[80:83],a[104:107],v[80:83]\nbuffer_load_dwordx4 v14,s[28:31],s46 offen lds\nM v[96:99],a[80:83],a[112:115],v[96:99]\ns_add_u32 m0,s50,73728\nv_and_b32 v31,0xffff0000,v31\nv_lshlrev_b32 v26,16,v39\nM v[100:103],a[84:87],a[112:115],v[100:103]\n" \
"v_fmac_f32 v23,v31,v47\nv_fmac_f32 v24,v26,v25\nM v[108:111],a[84:87],a[116:119],v[108:111]\nv_lshlrev_b32 v25,16,v48\nv_lshlrev_b32 v26,16,v32\nM v[104:107],a[80:83],a[116:119],v[104:107]\nbuffer_load_dwordx4 v15,s[28:31],s46 offen lds\nM v[120:123],a[80:83],a[124:127],v[120:123]\n" \
"s_add_u32 m0,s50,77824\nv_and_b32 v39,0xffff0000,v39\nv_fmac_f32 v23,v26,v25\nM v[124:127],a[84:87],a[124:127],v[124:127]\nLG5_%=:\ns_cbranch_execz LAV_%=\nv_fmac_f32 v24,v39,v47\nv_and_b32 v48,0xffff0000,v48\n" \
"M v[116:119],a[84:87],a[120:123],v[116:119]\nv_and_b32 v32,0xffff0000,v32\nv_lshlrev_b32 v26,16,v40\nM v[112:115],a[80:83],a[120:123],v[112:115]\nds_read_b128 a[80:83],v6 offset:8192\nds_read_b128 a[84:87],v6 offset:10240\nM v[192:195],a[64:67],a[96:99],v[192:195]\nbuffer_load_dwordx4 v16,s[28:31],s46 offen lds\n" \
"M v[196:199],a[68:71],a[96:99],v[196:199]\ns_add_u32 m0,s50,81920\nv_fmac_f32 v23,v32,v48\nv_fmac_f32 v24,v26,v25\nM v[204:207],a[68:71],a[100:103],v[204:207]\ns_mov_b32 s46,768\nv_lshlrev_b32 v25,16,v49\nv_lshlrev_b32 v26,16,v33\n" \
"M v[200:203],a[64:67],a[100:103],v[200:203]\nv_and_b32 v40,0xffff0000,v40\nv_fmac_f32 v23,v26,v25\nM v[216:219],a[64:67],a[108:111],v[216:219]\nbuffer_load_dwordx4 v17,s[28:31],s46 offen lds\nM v[220:223],a[68:71],a[108:111],v[220:223]\ns_add_u32 m0,s50,86016\nv_fmac_f32 v24,v40,v48\n" \
"v_and_b32 v49,0xffff0000,v49\nM v[212:215],a[68:71],a[104:107],v[212:215]\nv_and_b32 v33,0xffff0000,v33\nv_lshlrev_b32 v26,16,v41\nM v[208:211],a[64:67],a[104:107],v[208:211]\nv_fmac_f32 v23,v33,v49\nv_fmac_f32 v24,v26,v25\nM v[224:227],a[64:67],a[112:115],v[224:227]\n" \
"buffer_load_dwordx2 a[206:207],v59,s[40:43],s52 offen offset:0 nt\nM v[228:231],a[68:71],a[112:115],v[228:231]\nv_lshlrev_b32 v25,16,v50\nv_lshlrev_b32 v26,16,v34\nM v[236:239],a[68:71],a[116:119],v[236:239]\nv_and_b32 v41,0xffff0000,v41\nv_fmac_f32 v23,v26,v25\nM v[232:235],a[64:67],a[116:119],v[232:235]\n" \
"v_fmac_f32 v24,v41,v49\nv_and_b32 v50,0xffff0000,v50\nM v[248:251],a[64:67],a[124:127],v[248:251]\nbuffer_load_dwordx2 a[222:223],v59,s[80:83],s52 offen offset:0 nt\nM v[252:255],a[68:71],a[124:127],v[252:255]\nv_and_b32 v34,0xffff0000,v34\nv_lshlrev_b32 v26,16,v42\nM v[244:247],a[68:71],a[120:123],v[244:247]\n" \
"v_fmac_f32 v23,v34,v50\nv_fmac_f32 v24,v26,v25\nM v[240:243],a[64:67],a[120:123],v[240:243]\nds_read_b128 a[64:67],v6 offset:0\nds_read_b128 a[68:71],v6 offset:2048\nM a[128:131],a[72:75],a[96:99],a[128:131]\nbuffer_load_dwordx2 a[208:209],v59,s[40:43],s52 offen offset:2048 nt\nM a[132:135],a[76:79],a[96:99],a[132:135]\n" \
"v_lshlrev_b32 v25,16,v51\nv_lshlrev_b32 v26,16,v35\nM a[140:143],a[76:79],a[100:103],a[140:143]\nv_and_b32 v42,0xffff0000,v42\nv_fmac_f32 v23,v26,v25\nM a[136:139],a[72:75],a[100:103],a[136:139]\nv_fmac_f32 v24,v42,v50\nv_and_b32 v51,0xffff0000,v51\n" \
"M a[152:155],a[72:75],a[108:111],a[152:155]\nbuffer_load_dwordx2 a[224:225],v59,s[80:83],s52 offen offset:2048 nt\nM a[156:159],a[76:79],a[108:111],a[156:159]\ns_mov_b32 s52,12288\nv_and_b32 v35,0xffff0000,v35\nv_lshlrev_b32 v26,16,v43\nM a[148:151],a[76:79],a[104:107],a[148:151]\ns_waitcnt vmcnt(25) lgkmcnt(14)\n" \
"v_fmac_f32 v23,v35,v51\nv_fmac_f32 v24,v26,v25\nM a[144:147],a[72:75],a[104:107],a[144:147]\ns_barrier\nds_read_b64_tr_b16 a[96:97],v9 offset:16384\nM a[160:163],a[72:75],a[112:115],a[160:163]\nbuffer_load_dwordx2 a[210:211],v59,s[40:43],s52 offen offset:0 nt\nM a[164:167],a[76:79],a[112:115],a[164:167]\n" \
"ds_read_b64_tr_b16 a[98:99],v10 offset:16384\nds_read_b64_tr_b16 a[100:101],v11 offset:16384\nM a[172:175],a[76:79],a[116:119],a[172:175]\nds_read_b64_tr_b16 a[102:103],v12 offset:16384\nv_lshlrev_b32 v25,16,v52\nM a[168:171],a[72:75],a[116:119],a[168:171]\nds_read_b64_tr_b16 a[104:105],v9 offset:16896\nds_read_b64_tr_b16 a[106:107],v10 offset:16896\n" \
"M a[184:187],a[72:75],a[124:127],a[184:187]\nbuffer_load_dwordx2 a[226:227],v59,s[80:83],s52 offen offset:0 nt\nM a[188:191],a[76:79],a[124:127],a[188:191]\nds_read_b64_tr_b16 a[108:109],v11 offset:16896\nds_read_b64_tr_b16 a[110:111],v12 offset:16896\nM a[180:183],a[76:79],a[120:123],a[180:183]\ns_waitcnt vmcnt(31)\nds_read_b64_tr_b16 a[112:113],v9 offset:17408\n" \
"v_lshlrev_b32 v26,16,v36\nM a[176:179],a[72:75],a[120:123],a[176:179]\ns_waitcnt lgkmcnt(6)\nds_read_b128 a[72:75],v6 offset:4096\nv_and_b32 v43,0xffff0000,v43\nM v[128:131],a[24:27],a[32:35],v[128:131]\nbuffer_load_dwordx2 a[212:213],v59,s[40:43],s52 offen offset:2048 nt\nM v[132:135],a[28:31],a[32:35],v[132:135]\n" \
"ds_read_b128 a[76:79],v6 offset:6144\nds_read_b64_tr_b16 a[114:115],v10 offset:17408\nM v[140:143],a[28:31],a[36:39],v[140:143]\nds_read_b64_tr_b16 a[116:117],v11 offset:17408\nv_fmac_f32 v23,v26,v25\nM v[136:139],a[24:27],a[36:39],v[136:139]\nds_read_b64_tr_b16 a[118:119],v12 offset:17408\nv_fmac_f32 v24,v43,v51\n" \
"M v[152:155],a[24:27],a[44:47],v[152:155]\nbuffer_load_dwordx2 a[228:229],v59,s[80:83],s52 offen offset:2048 nt\nM v[156:159],a[28:31],a[44:47],v[156:159]\nds_read_b64_tr_b16 a[120:121],v9 offset:17920\nds_read_b64_tr_b16 a[122:123],v10 offset:17920\nM v[148:151],a[28:31],a[40:43],v[148:151]\nds_read_b64_tr_b16 a[124:125],v11 offset:17920\nv_and_b32 v52,0xffff0000,v52\n" \
"M v[144:147],a[24:27],a[40:43],v[144:147]\nds_read_b64_tr_b16 a[126:127],v12 offset:17920\nv_and_b32 v36,0xffff0000,v36\nM v[160:163],a[24:27],a[48:51],v[160:163]\ns_mov_b64 exec,0xff\nglobal_load_dword v0,v1,s[84:85]\ns_mov_b64 exec,-1\nM v[164:167],a[28:31],a[48:51],v[164:167]\n" \
"s_add_u32 s84,s84,524288\ns_addc_u32 s85,s85,0\nv_lshlrev_b32 v26,16,v44\nv_fmac_f32 v23,v36,v52\nM v[172:175],a[28:31],a[52:55],v[172:175]\ns_mov_b64 exec,0xff\nglobal_load_dword v0,v1,s[84:85]\ns_mov_b64 exec,-1\n" \
"M v[168:171],a[24:27],a[52:55],v[168:171]\nv_fmac_f32 v24,v26,v25\nv_lshlrev_b32 v25,16,v53\nM v[184:187],a[24:27],a[60:63],v[184:187]\ns_mov_b64 exec,0xff\nglobal_load_dword v0,v1,s[76:77]\ns_mov_b64 exec,-1\nM v[188:191],a[28:31],a[60:63],v[188:191]\n" \
"v_lshlrev_b32 v26,16,v37\nv_and_b32 v44,0xffff0000,v44\nM v[180:183],a[28:31],a[56:59],v[180:183]\ns_mov_b64 exec,0xff00\nglobal_load_dword v0,v1,s[76:77]\ns_mov_b64 exec,-1\nM v[176:179],a[24:27],a[56:59],v[176:179]\ns_add_u32 s76,s76,2097152\n" \
"s_addc_u32 s77,s77,0\nv_fmac_f32 v23,v26,v25\nv_fmac_f32 v24,v44,v52\nM v[64:67],a[16:19],a[32:35],v[64:67]\nv_lshlrev_b32 v26,16,v45\nv_and_b32 v53,0xffff0000,v53\nM v[68:71],a[20:23],a[32:35],v[68:71]\nv_and_b32 v45,0xffff0000,v45\n" \
"v_fmac_f32 v24,v26,v25\nM v[76:79],a[20:23],a[36:39],v[76:79]\nv_fmac_f32 v24,v45,v53\nv_and_b32 v37,0xffff0000,v37\nM v[72:75],a[16:19],a[36:39],v[72:75]\nv_accvgpr_read_b32 v45,a231\nv_fmac_f32 v23,v37,v53\nM v[88:91],a[16:19],a[44:47],v[88:91]\n" \
"v_accvgpr_read_b32 v49,a233\nv_lshlrev_b32 v44,16,v45\nM v[92:95],a[20:23],a[44:47],v[92:95]\nv_accvgpr_read_b32 v36,a192\nv_accvgpr_read_b32 v43,a230\nM v[84:87],a[20:23],a[40:43],v[84:87]\nv_accvgpr_read_b32 v37,a194\nv_lshlrev_b32 v48,16,v49\n" \
"M v[80:83],a[16:19],a[40:43],v[80:83]\nv_lshlrev_b32 v30,16,v36\nv_and_b32 v45,0xffff0000,v45\nM v[96:99],a[16:19],a[48:51],v[96:99]\nv_accvgpr_read_b32 v47,a232\nv_lshlrev_b32 v31,16,v37\nM v[100:103],a[20:23],a[48:51],v[100:103]\nv_accvgpr_read_b32 v38,a196\n" \
"v_lshlrev_b32 v42,16,v43\nM v[108:111],a[20:23],a[52:55],v[108:111]\nv_and_b32 v49,0xffff0000,v49\nv_and_b32 v36,0xffff0000,v36\nM v[104:107],a[16:19],a[52:55],v[104:107]\nv_accvgpr_read_b32 v39,a198\nv_mul_f32 v25,v30,v45\nM v[120:123],a[16:19],a[60:63],v[120:123]\n" \
"v_lshlrev_b32 v46,16,v47\nv_and_b32 v37,0xffff0000,v37\nM v[124:127],a[20:23],a[60:63],v[124:127]\nv_lshlrev_b32 v32,16,v38\nv_and_b32 v43,0xffff0000,v43\nM v[116:119],a[20:23],a[56:59],v[116:119]\nv_mul_f32 v26,v36,v49\nv_lshlrev_b32 v33,16,v39\n" \
"M v[112:115],a[16:19],a[56:59],v[112:115]\nv_fmac_f32 v25,v31,v44\nv_and_b32 v47,0xffff0000,v47\nM v[192:195],a[0:3],a[32:35],v[192:195]\nv_and_b32 v38,0xffff0000,v38\nv_fmac_f32 v26,v37,v48\nM v[196:199],a[4:7],a[32:35],v[196:199]\nv_and_b32 v39,0xffff0000,v39\n" \
"v_fmac_f32 v25,v32,v43\nM v[204:207],a[4:7],a[36:39],v[204:207]\nv_fmac_f32 v26,v38,v47\nv_fmac_f32 v25,v33,v42\nM v[200:203],a[0:3],a[36:39],v[200:203]\nv_fmac_f32 v26,v39,v46\nv_accvgpr_read_b32 v28,a214\nM v[216:219],a[0:3],a[44:47],v[216:219]\n" \
"C v26,v25,v26\nv_lshlrev_b32 v27,16,v28\nM v[220:223],a[4:7],a[44:47],v[220:223]\nv_lshlrev_b32 v25,16,v26\nv_and_b32 v28,0xffff0000,v28\nM v[212:215],a[4:7],a[40:43],v[212:215]\nv_and_b32 v26,0xffff0000,v26\nv_mul_f32 v29,v25,v27\n" \
"M v[208:211],a[0:3],a[40:43],v[208:211]\nv_mul_f32 v58,v26,v28\nv_accvgpr_read_b32 v40,a200\nM v[224:227],a[0:3],a[48:51],v[224:227]\nv_mul_f32 v25,v31,v45\nv_mul_f32 v26,v37,v49\nM v[228:231],a[4:7],a[48:51],v[228:231]\nv_lshlrev_b32 v34,16,v40\n" \
"v_fmac_f32 v25,v32,v44\nM v[236:239],a[4:7],a[52:55],v[236:239]\nv_fmac_f32 v26,v38,v48\nv_fmac_f32 v25,v33,v43\nM v[232:235],a[0:3],a[52:55],v[232:235]\nv_and_b32 v40,0xffff0000,v40\nv_fmac_f32 v26,v39,v47\nM v[248:251],a[0:3],a[60:63],v[248:251]\n" \
"v_fmac_f32 v25,v34,v42\nv_fmac_f32 v26,v40,v46\nM v[252:255],a[4:7],a[60:63],v[252:255]\nv_accvgpr_read_b32 v28,a216\nC v26,v25,v26\nM v[244:247],a[4:7],a[56:59],v[244:247]\nv_lshlrev_b32 v27,16,v28\nv_lshlrev_b32 v25,16,v26\n" \
"M v[240:243],a[0:3],a[56:59],v[240:243]\nv_and_b32 v28,0xffff0000,v28\nv_and_b32 v26,0xffff0000,v26\nM a[128:131],a[8:11],a[32:35],a[128:131]\nv_mul_f32 v25,v25,v27\nv_mul_f32 v26,v26,v28\nM a[132:135],a[12:15],a[32:35],a[132:135]\nC v53,v25,v29\n" \
"C v57,v26,v58\nM a[140:143],a[12:15],a[36:39],a[140:143]\nv_accvgpr_read_b32 v41,a202\nv_lshlrev_b32 v35,16,v41\nM a[136:139],a[8:11],a[36:39],a[136:139]\nv_mul_f32 v25,v32,v45\nv_mul_f32 v26,v38,v49\nM a[152:155],a[8:11],a[44:47],a[152:155]\n" \
"v_fmac_f32 v25,v33,v44\nv_and_b32 v41,0xffff0000,v41\nM a[156:159],a[12:15],a[44:47],a[156:159]\nv_fmac_f32 v26,v39,v48\nv_fmac_f32 v25,v34,v43\nM a[148:151],a[12:15],a[40:43],a[148:151]\ns_waitcnt vmcnt(21) lgkmcnt(0)\nv_fmac_f32 v26,v40,v47\n" \
"v_fmac_f32 v25,v35,v42\nM a[144:147],a[8:11],a[40:43],a[144:147]\ns_barrier\nds_read_b64_tr_b16 a[32:33],v9 offset:32768\nM a[160:163],a[8:11],a[48:51],a[160:163]\nbuffer_load_dwordx4 v18,s[28:31],s46 offen lds\nM a[164:167],a[12:15],a[48:51],a[164:167]\ns_add_u32 m0,s50,90112\n" \
"ds_read_b64_tr_b16 a[34:35],v10 offset:32768\nv_fmac_f32 v26,v41,v46\nM a[172:175],a[12:15],a[52:55],a[172:175]\nds_read_b64_tr_b16 a[36:37],v11 offset:32768\nds_read_b64_tr_b16 a[38:39],v12 offset:32768\nM a[168:171],a[8:11],a[52:55],a[168:171]\nds_read_b64_tr_b16 a[40:41],v9 offset:33280\nv_accvgpr_read_b32 v28,a218\n" \
"M a[184:187],a[8:11],a[60:63],a[184:187]\nbuffer_load_dwordx4 v19,s[28:31],s46 offen lds\nM a[188:191],a[12:15],a[60:63],a[188:191]\ns_add_u32 m0,s50,94208\nds_read_b64_tr_b16 a[42:43],v10 offset:33280\nC v26,v25,v26\nM a[180:183],a[12:15],a[56:59],a[180:183]\nds_read_b64_tr_b16 a[44:45],v11 offset:33280\n" \
"v_lshlrev_b32 v27,16,v28\nM a[176:179],a[8:11],a[56:59],a[176:179]\nds_read_b64_tr_b16 a[46:47],v12 offset:33280\nv_lshlrev_b32 v25,16,v26\nM v[128:131],a[88:91],a[96:99],v[128:131]\nbuffer_load_dwordx4 v20,s[28:31],s46 offen lds\nM v[132:135],a[92:95],a[96:99],v[132:135]\ns_add_u32 m0,s22,0\n" \
"ds_read_b64_tr_b16 a[48:49],v9 offset:33792\nv_and_b32 v28,0xffff0000,v28\nM v[140:143],a[92:95],a[100:103],v[140:143]\ns_mov_b32 s46,896\nds_read_b64_tr_b16 a[50:51],v10 offset:33792\nv_and_b32 v26,0xffff0000,v26\nM v[136:139],a[88:91],a[100:103],v[136:139]\nds_read_b64_tr_b16 a[52:53],v11 offset:33792\n" \
"ds_read_b64_tr_b16 a[54:55],v12 offset:33792\nM v[152:155],a[88:91],a[108:111],v[152:155]\nbuffer_load_dwordx4 v21,s[32:35],s47 offen offset:0 lds\nM v[156:159],a[92:95],a[108:111],v[156:159]\ns_add_u32 m0,s22,896\nv_mul_f32 v29,v25,v27\nv_mul_f32 v58,v26,v28\nM v[148:151],a[92:95],a[104:107],v[148:151]\n" \
"ds_read_b64_tr_b16 a[56:57],v9 offset:34304\nds_read_b64_tr_b16 a[58:59],v10 offset:34304\nM v[144:147],a[88:91],a[104:107],v[144:147]\nds_read_b64_tr_b16 a[60:61],v11 offset:34304\nv_accvgpr_read_b32 v36,a204\nM v[160:163],a[88:91],a[112:115],v[160:163]\nbuffer_load_dwordx4 v21,s[32:35],s47 offen offset:128 lds\nM v[164:167],a[92:95],a[112:115],v[164:167]\n" \
"s_add_u32 m0,s22,2048\nds_read_b64_tr_b16 a[62:63],v12 offset:34304\nv_mul_f32 v25,v33,v45\nM v[172:175],a[92:95],a[116:119],v[172:175]\nds_read_b128 a[24:27],v5 offset:45056\nv_lshlrev_b32 v30,16,v36\nM v[168:171],a[88:91],a[116:119],v[168:171]\nds_read_b128 a[28:31],v5 offset:47104\n" \
"v_mul_f32 v26,v39,v49\nM v[184:187],a[88:91],a[124:127],v[184:187]\nbuffer_load_dwordx4 v21,s[92:95],s47 offen offset:0 lds\nM v[188:191],a[92:95],a[124:127],v[188:191]\ns_add_u32 m0,s22,2944\nv_fmac_f32 v25,v34,v44\nv_fmac_f32 v26,v40,v48\nM v[180:183],a[92:95],a[120:123],v[180:183]\n" \
"ds_read_b128 a[16:19],v5 offset:40960\nv_and_b32 v36,0xffff0000,v36\nM v[176:179],a[88:91],a[120:123],v[176:179]\nds_read_b128 a[20:23],v5 offset:43008\nv_fmac_f32 v25,v35,v43\nM v[64:67],a[80:83],a[96:99],v[64:67]\nbuffer_load_dwordx4 v21,s[92:95],s47 offen offset:128 lds\nM v[68:71],a[84:87],a[96:99],v[68:71]\n" \
"s_add_u32 m0,s22,16384\nds_read_b128 a[0:3],v5 offset:32768\nv_fmac_f32 v26,v41,v47\nM v[76:79],a[84:87],a[100:103],v[76:79]\ns_mov_b32 s47,3407872\nds_read_b128 a[4:7],v5 offset:34816\nv_fmac_f32 v25,v30,v42\nM v[72:75],a[80:83],a[100:103],v[72:75]\n" \
"ds_read_b128 a[8:11],v5 offset:36864\nv_fmac_f32 v26,v36,v46\nM v[88:91],a[80:83],a[108:111],v[88:91]\nbuffer_load_dwordx4 v21,s[32:35],s47 offen offset:0 lds\nM v[92:95],a[84:87],a[108:111],v[92:95]\ns_add_u32 m0,s22,17280\nv_accvgpr_read_b32 v28,a220\nC v26,v25,v26\n" \
"M v[84:87],a[84:87],a[104:107],v[84:87]\nds_read_b128 a[12:15],v5 offset:38912\nv_lshlrev_b32 v27,16,v28\nM v[80:83],a[80:83],a[104:107],v[80:83]\nds_read_b128 a[88:91],v6 offset:45056\nv_lshlrev_b32 v25,16,v26\nM v[96:99],a[80:83],a[112:115],v[96:99]\nbuffer_load_dwordx4 v21,s[32:35],s47 offen offset:128 lds\n" \
"M v[100:103],a[84:87],a[112:115],v[100:103]\ns_add_u32 m0,s22,18432\nds_read_b128 a[92:95],v6 offset:47104\nv_and_b32 v28,0xffff0000,v28\nM v[108:111],a[84:87],a[116:119],v[108:111]\nv_and_b32 v26,0xffff0000,v26\nv_mul_f32 v25,v25,v27\nM v[104:107],a[80:83],a[116:119],v[104:107]\n" \
"v_mul_f32 v26,v26,v28\nC v52,v25,v29\nM v[120:123],a[80:83],a[124:127],v[120:123]\nbuffer_load_dwordx4 v21,s[92:95],s47 offen offset:0 lds\nM v[124:127],a[84:87],a[124:127],v[124:127]\ns_add_u32 m0,s22,19328\nC v56,v26,v58\nv_mov_b32 v59,s19\n" \
"M v[116:119],a[84:87],a[120:123],v[116:119]\nv_permlane32_swap_b32 v23,v24\nM v[112:115],a[80:83],a[120:123],v[112:115]\nds_read_b128 a[80:83],v6 offset:40960\nds_read_b128 a[84:87],v6 offset:43008\nM v[192:195],a[64:67],a[96:99],v[192:195]\nbuffer_load_dwordx4 v21,s[92:95],s47 offen offset:128 lds\nM v[196:199],a[68:71],a[96:99],v[196:199]\n" \
"s_add_u32 m0,s50,0\nv_add_f32 v23,v23,v24\nv_mov_b32 v24,v23\nM v[204:207],a[68:71],a[100:103],v[204:207]\ns_mov_b32 s47,3670016\nv_permlane16_swap_b32 v23,v24\nM v[200:203],a[64:67],a[100:103],v[200:203]\nv_add_f32 v23,v23,v24\n" \
"s_nop 1\nv_add_f32 v23,v23,v23 row_ror:8 row_mask:0xf bank_mask:0xf\nM v[216:219],a[64:67],a[108:111],v[216:219]\nbuffer_load_dwordx4 v13,s[28:31],s46 offen lds\nM v[220:223],a[68:71],a[108:111],v[220:223]\ns_add_u32 m0,s50,4096\nv_add_u32 v13,s60,v63\nv_add_f32 v23,v23,v23 row_ror:4 row_mask:0xf bank_mask:0xf\n" \
"M v[212:215],a[68:71],a[104:107],v[212:215]\ns_nop 0\nv_add_f32 v23,v23,v23 row_ror:2 row_mask:0xf bank_mask:0xf\ns_nop 1\nv_add_f32 v23,v23,v23 row_ror:1 row_mask:0xf bank_mask:0xf\nM v[208:211],a[64:67],a[104:107],v[208:211]\nC v23,v23,v23\nv_and_b32 v23,0xffff0000,v23\n" \
"M v[224:227],a[64:67],a[112:115],v[224:227]\nbuffer_load_dwordx4 v14,s[28:31],s46 offen lds\nM v[228:231],a[68:71],a[112:115],v[228:231]\ns_add_u32 m0,s50,8192\nv_add_u32 v14,0x10000,v13\nv_mul_f32 v23,0xbfb8aa3b,v23\nM v[236:239],a[68:71],a[116:119],v[236:239]\nv_exp_f32 v23,v23\n" \
"M v[232:235],a[64:67],a[116:119],v[232:235]\nv_add_f32 v23,1.0,v23\nv_add_u32 v24,992,v4\nM v[248:251],a[64:67],a[124:127],v[248:251]\nbuffer_load_dwordx4 v15,s[28:31],s46 offen lds\nM v[252:255],a[68:71],a[124:127],v[252:255]\ns_add_u32 m0,s50,12288\nv_add_u32 v15,0x10000,v14\n" \
"M v[244:247],a[68:71],a[120:123],v[244:247]\nv_rcp_f32 v23,v23\nM v[240:243],a[64:67],a[120:123],v[240:243]\nds_read_b128 a[64:67],v6 offset:32768\nds_read_b128 a[68:71],v6 offset:34816\nM a[128:131],a[72:75],a[96:99],a[128:131]\nbuffer_load_dwordx4 v16,s[28:31],s46 offen lds\nM a[132:135],a[76:79],a[96:99],a[132:135]\n" \
"s_add_u32 m0,s50,16384\nv_add_u32 v16,0x10000,v15\nC v23,v23,v23\nM a[140:143],a[76:79],a[100:103],a[140:143]\ns_mov_b32 s46,896\nv_and_b32 v23,0xffff0000,v23\nM a[136:139],a[72:75],a[100:103],a[136:139]\nM a[152:155],a[72:75],a[108:111],a[152:155]\n" \
"buffer_load_dwordx4 v17,s[28:31],s46 offen lds\nM a[156:159],a[76:79],a[108:111],a[156:159]\ns_add_u32 m0,s50,20480\nv_add_u32 v17,s61,v63\nv_mov_b32 v63,0x3f803f80\nM a[148:151],a[76:79],a[104:107],a[148:151]\ns_waitcnt vmcnt(33) lgkmcnt(14)\nM a[144:147],a[72:75],a[104:107],a[144:147]\n" \
"s_barrier\nds_read_b64_tr_b16 a[96:97],v9 offset:49152\nM a[160:163],a[72:75],a[112:115],a[160:163]\nbuffer_store_dword v23,v61,s[36:39],0 offen sc1\nM a[164:167],a[76:79],a[112:115],a[164:167]\nds_read_b64_tr_b16 a[98:99],v10 offset:49152\nds_read_b64_tr_b16 a[100:101],v11 offset:49152\nM a[172:175],a[76:79],a[116:119],a[172:175]\n" \
"buffer_load_dwordx4 v18,s[28:31],s46 offen lds\nM a[168:171],a[72:75],a[116:119],a[168:171]\ns_add_u32 m0,s50,24576\nds_read_b64_tr_b16 a[102:103],v12 offset:49152\nv_add_u32 v18,0x10000,v17\nM a[184:187],a[72:75],a[124:127],a[184:187]\nds_read_b64_tr_b16 a[104:105],v9 offset:49664\nv_add_u32 v23,-32,v4\n" \
"M a[188:191],a[76:79],a[124:127],a[188:191]\nds_read_b64_tr_b16 a[106:107],v10 offset:49664\nM a[180:183],a[76:79],a[120:123],a[180:183]\ns_waitcnt lgkmcnt(5)\nds_read_b64_tr_b16 a[108:109],v11 offset:49664\nds_read_b64_tr_b16 a[110:111],v12 offset:49664\nM a[176:179],a[72:75],a[120:123],a[176:179]\nbuffer_load_dwordx4 v19,s[28:31],s46 offen lds\n" \
"M v[128:131],a[24:27],a[32:35],v[128:131]\ns_add_u32 m0,s50,28672\nv_add_u32 v19,0x10000,v18\nM v[132:135],a[28:31],a[32:35],v[132:135]\nds_read_b128 a[72:75],v6 offset:36864\nM v[140:143],a[28:31],a[36:39],v[140:143]\nds_read_b128 a[76:79],v6 offset:38912\nM v[136:139],a[24:27],a[36:39],v[136:139]\n" \
"buffer_load_dwordx4 v20,s[28:31],s46 offen lds\nM v[152:155],a[24:27],a[44:47],v[152:155]\ns_add_u32 m0,s22,32768\nds_read_b64_tr_b16 a[112:113],v9 offset:50176\nv_add_u32 v20,0x10000,v19\nM v[156:159],a[28:31],a[44:47],v[156:159]\ns_mov_b64 s[28:29],s[64:65]\nds_read_b64_tr_b16 a[114:115],v10 offset:50176\n" \
"M v[148:151],a[28:31],a[40:43],v[148:151]\ns_and_b32 s29,s29,0xffff\nds_read_b64_tr_b16 a[116:117],v11 offset:50176\nds_read_b64_tr_b16 a[118:119],v12 offset:50176\nM v[144:147],a[24:27],a[40:43],v[144:147]\ns_mov_b32 s30,4194304\nds_read_b64_tr_b16 a[120:121],v9 offset:50688\nM v[160:163],a[24:27],a[48:51],v[160:163]\n" \
"buffer_load_dwordx4 v21,s[32:35],s47 offen offset:0 lds\nM v[164:167],a[28:31],a[48:51],v[164:167]\ns_add_u32 m0,s22,33664\nds_read_b64_tr_b16 a[122:123],v10 offset:50688\nM v[172:175],a[28:31],a[52:55],v[172:175]\ns_mov_b32 s46,0\nds_read_b64_tr_b16 a[124:125],v11 offset:50688\nM v[168:171],a[24:27],a[52:55],v[168:171]\n" \
"ds_read_b64_tr_b16 a[126:127],v12 offset:50688\nM v[184:187],a[24:27],a[60:63],v[184:187]\nbuffer_load_dwordx4 v21,s[32:35],s47 offen offset:128 lds\nM v[188:191],a[28:31],a[60:63],v[188:191]\ns_add_u32 m0,s22,34816\nM v[180:183],a[28:31],a[56:59],v[180:183]\nM v[176:179],a[24:27],a[56:59],v[176:179]\nM v[64:67],a[16:19],a[32:35],v[64:67]\n" \
"M v[68:71],a[20:23],a[32:35],v[68:71]\nbuffer_load_dwordx4 v21,s[92:95],s47 offen offset:0 lds\nM v[76:79],a[20:23],a[36:39],v[76:79]\ns_add_u32 m0,s22,35712\nM v[72:75],a[16:19],a[36:39],v[72:75]\nM v[88:91],a[16:19],a[44:47],v[88:91]\nM v[92:95],a[20:23],a[44:47],v[92:95]\nbuffer_load_dwordx4 v21,s[92:95],s47 offen offset:128 lds\n" \
"M v[84:87],a[20:23],a[40:43],v[84:87]\ns_add_u32 m0,s22,49152\nM v[80:83],a[16:19],a[40:43],v[80:83]\ns_mov_b32 s47,3932160\nM v[96:99],a[16:19],a[48:51],v[96:99]\nM v[100:103],a[20:23],a[48:51],v[100:103]\nM v[108:111],a[20:23],a[52:55],v[108:111]\nM v[104:107],a[16:19],a[52:55],v[104:107]\n" \
"M v[120:123],a[16:19],a[60:63],v[120:123]\nM v[124:127],a[20:23],a[60:63],v[124:127]\nM v[116:119],a[20:23],a[56:59],v[116:119]\nM v[112:115],a[16:19],a[56:59],v[112:115]\nM v[192:195],a[0:3],a[32:35],v[192:195]\ns_mov_b64 exec,0xff\nglobal_load_dword v0,v2,s[88:89]\ns_mov_b64 exec,-1\n" \
"M v[196:199],a[4:7],a[32:35],v[196:199]\nM v[204:207],a[4:7],a[36:39],v[204:207]\ns_mov_b64 exec,0xff00\nglobal_load_dword v0,v2,s[88:89]\ns_mov_b64 exec,-1\nM v[200:203],a[0:3],a[36:39],v[200:203]\ns_add_u32 s88,s88,2048\ns_addc_u32 s89,s89,0\n" \
"M v[216:219],a[0:3],a[44:47],v[216:219]\ns_mov_b64 exec,0xff\nglobal_load_dword v0,v2,s[88:89]\ns_mov_b64 exec,-1\nM v[220:223],a[4:7],a[44:47],v[220:223]\nM v[212:215],a[4:7],a[40:43],v[212:215]\ns_mov_b64 exec,0xff00\nglobal_load_dword v0,v2,s[88:89]\n" \
"s_mov_b64 exec,-1\nM v[208:211],a[0:3],a[40:43],v[208:211]\nM v[224:227],a[0:3],a[48:51],v[224:227]\ns_mov_b64 exec,0xff\nglobal_load_dword v0,v1,s[76:77]\ns_mov_b64 exec,-1\nM v[228:231],a[4:7],a[48:51],v[228:231]\nM v[236:239],a[4:7],a[52:55],v[236:239]\n" \
"s_mov_b64 exec,0xff00\nglobal_load_dword v0,v1,s[76:77]\ns_mov_b64 exec,-1\nM v[232:235],a[0:3],a[52:55],v[232:235]\nLG6_%=:\ns_cbranch_execz LHR0_%=\ns_add_u32 s76,s76,2097152\ns_addc_u32 s77,s77,0\n" \
"M v[248:251],a[0:3],a[60:63],v[248:251]\nM v[252:255],a[4:7],a[60:63],v[252:255]\nM v[244:247],a[4:7],a[56:59],v[244:247]\ns_waitcnt vmcnt(34)\nv_accvgpr_read_b32 v37,a206\nv_mul_f32 v25,v34,v45\nM v[240:243],a[0:3],a[56:59],v[240:243]\ns_mov_b32 s98,79691776\n" \
"v_mul_f32 v26,v40,v49\nv_lshlrev_b32 v31,16,v37\nM a[128:131],a[8:11],a[32:35],a[128:131]\ns_mov_b32 s99,79695872\nv_fmac_f32 v25,v35,v44\nv_fmac_f32 v26,v41,v48\nM a[132:135],a[12:15],a[32:35],a[132:135]\nv_and_b32 v37,0xffff0000,v37\n" \
"v_fmac_f32 v25,v30,v43\nM a[140:143],a[12:15],a[36:39],a[140:143]\nv_fmac_f32 v26,v36,v47\nv_fmac_f32 v25,v31,v42\nM a[136:139],a[8:11],a[36:39],a[136:139]\nv_fmac_f32 v26,v37,v46\nv_accvgpr_read_b32 v28,a222\nM a[152:155],a[8:11],a[44:47],a[152:155]\n" \
"C v26,v25,v26\nv_lshlrev_b32 v27,16,v28\nM a[156:159],a[12:15],a[44:47],a[156:159]\nv_lshlrev_b32 v25,16,v26\nv_and_b32 v28,0xffff0000,v28\nM a[148:151],a[12:15],a[40:43],a[148:151]\ns_waitcnt vmcnt(23) lgkmcnt(0)\nv_and_b32 v26,0xffff0000,v26\n" \
"v_mul_f32 v29,v25,v27\nM a[144:147],a[8:11],a[40:43],a[144:147]\ns_barrier\nv_mul_f32 v58,v26,v28\nM a[160:163],a[8:11],a[48:51],a[160:163]\nv_accvgpr_read_b32 v38,a208\nv_mul_f32 v25,v35,v45\nM a[164:167],a[12:15],a[48:51],a[164:167]\n" \
"v_lshlrev_b32 v32,16,v38\nv_mul_f32 v26,v41,v49\nM a[172:175],a[12:15],a[52:55],a[172:175]\nv_fmac_f32 v25,v30,v44\nv_and_b32 v38,0xffff0000,v38\nM a[168:171],a[8:11],a[52:55],a[168:171]\nv_fmac_f32 v26,v36,v48\nv_fmac_f32 v25,v31,v43\n" \
"M a[184:187],a[8:11],a[60:63],a[184:187]\nds_read_b64_tr_b16 a[32:33],v9 offset:0\nds_read_b64_tr_b16 a[34:35],v10 offset:0\nM a[188:191],a[12:15],a[60:63],a[188:191]\nds_read_b64_tr_b16 a[36:37],v11 offset:0\nv_fmac_f32 v26,v37,v47\nM a[180:183],a[12:15],a[56:59],a[180:183]\nds_read_b64_tr_b16 a[38:39],v12 offset:0\n" \
"ds_read_b64_tr_b16 a[40:41],v9 offset:512\nM a[176:179],a[8:11],a[56:59],a[176:179]\nds_read_b64_tr_b16 a[42:43],v10 offset:512\nv_fmac_f32 v25,v32,v42\nM v[128:131],a[88:91],a[96:99],v[128:131]\nv_fmac_f32 v26,v38,v46\nv_accvgpr_read_b32 v28,a224\nM v[132:135],a[92:95],a[96:99],v[132:135]\n" \
"ds_read_b64_tr_b16 a[44:45],v11 offset:512\nC v26,v25,v26\nM v[140:143],a[92:95],a[100:103],v[140:143]\nds_read_b64_tr_b16 a[46:47],v12 offset:512\nv_lshlrev_b32 v27,16,v28\nM v[136:139],a[88:91],a[100:103],v[136:139]\nds_read_b64_tr_b16 a[48:49],v9 offset:1024\nds_read_b64_tr_b16 a[50:51],v10 offset:1024\n" \
"M v[152:155],a[88:91],a[108:111],v[152:155]\nds_read_b64_tr_b16 a[52:53],v11 offset:1024\nv_lshlrev_b32 v25,16,v26\nM v[156:159],a[92:95],a[108:111],v[156:159]\nds_read_b64_tr_b16 a[54:55],v12 offset:1024\nds_read_b64_tr_b16 a[56:57],v9 offset:1536\nM v[148:151],a[92:95],a[104:107],v[148:151]\nds_read_b64_tr_b16 a[58:59],v10 offset:1536\n" \
"v_and_b32 v28,0xffff0000,v28\nM v[144:147],a[88:91],a[104:107],v[144:147]\nds_read_b64_tr_b16 a[60:61],v11 offset:1536\nv_and_b32 v26,0xffff0000,v26\nM v[160:163],a[88:91],a[112:115],v[160:163]\nds_read_b64_tr_b16 a[62:63],v12 offset:1536\nv_mul_f32 v25,v25,v27\nM v[164:167],a[92:95],a[112:115],v[164:167]\n" \
"ds_read_b128 a[24:27],v7 offset:12288\nv_mul_f32 v26,v26,v28\nM v[172:175],a[92:95],a[116:119],v[172:175]\nds_read_b128 a[28:31],v7 offset:14336\nC v51,v25,v29\nM v[168:171],a[88:91],a[116:119],v[168:171]\nC v55,v26,v58\nv_accvgpr_read_b32 v39,a210\n" \
"M v[184:187],a[88:91],a[124:127],v[184:187]\nv_mul_f32 v25,v30,v45\nv_mul_f32 v26,v36,v49\nM v[188:191],a[92:95],a[124:127],v[188:191]\nv_lshlrev_b32 v33,16,v39\nv_fmac_f32 v25,v31,v44\nM v[180:183],a[92:95],a[120:123],v[180:183]\nv_fmac_f32 v26,v37,v48\n" \
"v_and_b32 v39,0xffff0000,v39\nM v[176:179],a[88:91],a[120:123],v[176:179]\nv_fmac_f32 v25,v32,v43\nv_fmac_f32 v26,v38,v47\nM v[64:67],a[80:83],a[96:99],v[64:67]\nv_fmac_f32 v25,v33,v42\nv_fmac_f32 v26,v39,v46\nM v[68:71],a[84:87],a[96:99],v[68:71]\n" \
"v_accvgpr_read_b32 v28,a226\nC v26,v25,v26\nM v[76:79],a[84:87],a[100:103],v[76:79]\nv_lshlrev_b32 v27,16,v28\nv_lshlrev_b32 v25,16,v26\nM v[72:75],a[80:83],a[100:103],v[72:75]\nv_and_b32 v28,0xffff0000,v28\nv_and_b32 v26,0xffff0000,v26\n" \
"M v[88:91],a[80:83],a[108:111],v[88:91]\nv_mul_f32 v29,v25,v27\nv_mul_f32 v58,v26,v28\nM v[92:95],a[84:87],a[108:111],v[92:95]\nv_accvgpr_read_b32 v40,a212\nv_lshlrev_b32 v34,16,v40\nM v[84:87],a[84:87],a[104:107],v[84:87]\nv_mul_f32 v25,v31,v45\n" \
"v_mul_f32 v26,v37,v49\nM v[80:83],a[80:83],a[104:107],v[80:83]\nds_read_b128 a[16:19],v7 offset:8192\nv_fmac_f32 v25,v32,v44\nM v[96:99],a[80:83],a[112:115],v[96:99]\nds_read_b128 a[20:23],v7 offset:10240\nv_and_b32 v40,0xffff0000,v40\nM v[100:103],a[84:87],a[112:115],v[100:103]\n" \
"v_fmac_f32 v26,v38,v48\nv_fmac_f32 v25,v33,v43\nM v[108:111],a[84:87],a[116:119],v[108:111]\nv_fmac_f32 v26,v39,v47\nv_fmac_f32 v25,v34,v42\nM v[104:107],a[80:83],a[116:119],v[104:107]\nv_fmac_f32 v26,v40,v46\nv_accvgpr_read_b32 v28,a228\n" \
"M v[120:123],a[80:83],a[124:127],v[120:123]\nC v26,v25,v26\nv_lshlrev_b32 v27,16,v28\nM v[124:127],a[84:87],a[124:127],v[124:127]\nv_lshlrev_b32 v25,16,v26\nv_mul_f32 v25,v25,v27\nM v[116:119],a[84:87],a[120:123],v[116:119]\nC v50,v25,v29\n" \
"v_accvgpr_read_b32 v45,a235\nM v[112:115],a[80:83],a[120:123],v[112:115]\nv_and_b32 v28,0xffff0000,v28\nv_and_b32 v26,0xffff0000,v26\nM v[192:195],a[64:67],a[96:99],v[192:195]\nv_lshlrev_b32 v44,16,v45\nv_accvgpr_read_b32 v49,a237\nM v[196:199],a[68:71],a[96:99],v[196:199]\n" \
"ds_read_b128 a[0:3],v7 offset:0\nv_accvgpr_read_b32 v36,a193\nM v[204:207],a[68:71],a[100:103],v[204:207]\nds_read_b128 a[4:7],v7 offset:2048\nv_accvgpr_read_b32 v37,a195\nM v[200:203],a[64:67],a[100:103],v[200:203]\nv_mul_f32 v26,v26,v28\nv_accvgpr_read_b32 v43,a234\n" \
"M v[216:219],a[64:67],a[108:111],v[216:219]\nv_accvgpr_read_b32 v47,a236\nv_lshlrev_b32 v48,16,v49\nM v[220:223],a[68:71],a[108:111],v[220:223]\nv_and_b32 v45,0xffff0000,v45\nv_lshlrev_b32 v30,16,v36\nM v[212:215],a[68:71],a[104:107],v[212:215]\nbuffer_store_dwordx4 v[50:53],v60,s[36:39],s98 offen offset:0 sc1\n" \
"M v[208:211],a[64:67],a[104:107],v[208:211]\ns_mov_b32 s98,79699968\nv_lshlrev_b32 v31,16,v37\nv_accvgpr_read_b32 v38,a197\nM v[224:227],a[64:67],a[112:115],v[224:227]\nC v54,v26,v58\nv_accvgpr_read_b32 v39,a199\nM v[228:231],a[68:71],a[112:115],v[228:231]\n" \
"v_lshlrev_b32 v42,16,v43\nv_and_b32 v49,0xffff0000,v49\nM v[236:239],a[68:71],a[116:119],v[236:239]\nv_lshlrev_b32 v46,16,v47\nv_and_b32 v36,0xffff0000,v36\nM v[232:235],a[64:67],a[116:119],v[232:235]\nv_lshlrev_b32 v32,16,v38\nv_mul_f32 v25,v30,v45\n" \
"M v[248:251],a[64:67],a[124:127],v[248:251]\nds_read_b128 a[8:11],v7 offset:4096\nds_read_b128 a[12:15],v7 offset:6144\nM v[252:255],a[68:71],a[124:127],v[252:255]\nv_and_b32 v37,0xffff0000,v37\nv_lshlrev_b32 v33,16,v39\nM v[244:247],a[68:71],a[120:123],v[244:247]\nv_and_b32 v43,0xffff0000,v43\n" \
"v_mul_f32 v26,v36,v49\nM v[240:243],a[64:67],a[120:123],v[240:243]\nds_read_b128 a[64:67],v8 offset:0\nds_read_b128 a[68:71],v8 offset:2048\nM a[128:131],a[72:75],a[96:99],a[128:131]\nv_and_b32 v47,0xffff0000,v47\nv_and_b32 v38,0xffff0000,v38\nM a[132:135],a[76:79],a[96:99],a[132:135]\n" \
"ds_read_b128 a[80:83],v8 offset:8192\nv_fmac_f32 v25,v31,v44\nM a[140:143],a[76:79],a[100:103],a[140:143]\nv_and_b32 v39,0xffff0000,v39\nv_fmac_f32 v26,v37,v48\nM a[136:139],a[72:75],a[100:103],a[136:139]\nv_fmac_f32 v25,v32,v43\nv_fmac_f32 v26,v38,v47\n" \
"M a[152:155],a[72:75],a[108:111],a[152:155]\nv_fmac_f32 v25,v33,v42\nv_fmac_f32 v26,v39,v46\nM a[156:159],a[76:79],a[108:111],a[156:159]\nds_read_b128 a[84:87],v8 offset:10240\nv_accvgpr_read_b32 v28,a215\nM a[148:151],a[76:79],a[104:107],a[148:151]\ns_waitcnt vmcnt(20) lgkmcnt(12)\n" \
"ds_read_b128 a[88:91],v8 offset:12288\nds_read_b128 a[92:95],v8 offset:14336\nM a[144:147],a[72:75],a[104:107],a[144:147]\ns_barrier\nds_read_b64_tr_b16 a[96:97],v9 offset:16384\nM a[160:163],a[72:75],a[112:115],a[160:163]\nds_read_b64_tr_b16 a[98:99],v10 offset:16384\nC v26,v25,v26\n" \
"M a[164:167],a[76:79],a[112:115],a[164:167]\nds_read_b64_tr_b16 a[100:101],v11 offset:16384\nv_lshlrev_b32 v27,16,v28\nM a[172:175],a[76:79],a[116:119],a[172:175]\nds_read_b64_tr_b16 a[102:103],v12 offset:16384\nds_read_b64_tr_b16 a[104:105],v9 offset:16896\nM a[168:171],a[72:75],a[116:119],a[168:171]\nds_read_b64_tr_b16 a[106:107],v10 offset:16896\n" \
"v_lshlrev_b32 v25,16,v26\nM a[184:187],a[72:75],a[124:127],a[184:187]\nds_read_b64_tr_b16 a[108:109],v11 offset:16896\nv_and_b32 v28,0xffff0000,v28\nM a[188:191],a[76:79],a[124:127],a[188:191]\ns_waitcnt lgkmcnt(6)\nds_read_b64_tr_b16 a[110:111],v12 offset:16896\nds_read_b64_tr_b16 a[112:113],v9 offset:17408\n" \
"M a[180:183],a[76:79],a[120:123],a[180:183]\nbuffer_store_dwordx4 v[54:57],v60,s[36:39],s99 offen offset:0 sc1\nM a[176:179],a[72:75],a[120:123],a[176:179]\ns_mov_b32 s99,79704064\nds_read_b128 a[72:75],v8 offset:4096\nds_read_b128 a[76:79],v8 offset:6144\nM v[128:131],a[24:27],a[32:35],v[128:131]\nds_read_b64_tr_b16 a[114:115],v10 offset:17408\n" \
"v_and_b32 v26,0xffff0000,v26\nM v[132:135],a[28:31],a[32:35],v[132:135]\nds_read_b64_tr_b16 a[116:117],v11 offset:17408\nv_mul_f32 v29,v25,v27\nM v[140:143],a[28:31],a[36:39],v[140:143]\nds_read_b64_tr_b16 a[118:119],v12 offset:17408\nv_mul_f32 v58,v26,v28\nM v[136:139],a[24:27],a[36:39],v[136:139]\n" \
"ds_read_b64_tr_b16 a[120:121],v9 offset:17920\nds_read_b64_tr_b16 a[122:123],v10 offset:17920\nM v[152:155],a[24:27],a[44:47],v[152:155]\nds_read_b64_tr_b16 a[124:125],v11 offset:17920\nv_accvgpr_read_b32 v40,a201\nM v[156:159],a[28:31],a[44:47],v[156:159]\nds_read_b64_tr_b16 a[126:127],v12 offset:17920\nv_lshlrev_b32 v34,16,v40\n" \
"M v[148:151],a[28:31],a[40:43],v[148:151]\nv_mul_f32 v25,v31,v45\nv_mul_f32 v26,v37,v49\nM v[144:147],a[24:27],a[40:43],v[144:147]\nv_fmac_f32 v25,v32,v44\nv_and_b32 v40,0xffff0000,v40\nM v[160:163],a[24:27],a[48:51],v[160:163]\nv_fmac_f32 v26,v38,v48\n" \
"v_fmac_f32 v25,v33,v43\nM v[164:167],a[28:31],a[48:51],v[164:167]\nv_fmac_f32 v26,v39,v47\nv_fmac_f32 v25,v34,v42\nM v[172:175],a[28:31],a[52:55],v[172:175]\nv_fmac_f32 v26,v40,v46\nv_accvgpr_read_b32 v28,a217\nM v[168:171],a[24:27],a[52:55],v[168:171]\n" \
"C v26,v25,v26\nv_lshlrev_b32 v27,16,v28\nM v[184:187],a[24:27],a[60:63],v[184:187]\nv_lshlrev_b32 v25,16,v26\nv_and_b32 v28,0xffff0000,v28\nM v[188:191],a[28:31],a[60:63],v[188:191]\nv_and_b32 v26,0xffff0000,v26\nv_mul_f32 v25,v25,v27\n" \
"M v[180:183],a[28:31],a[56:59],v[180:183]\nv_mul_f32 v26,v26,v28\nC v53,v25,v29\nM v[176:179],a[24:27],a[56:59],v[176:179]\nC v57,v26,v58\nv_accvgpr_read_b32 v41,a203\nM v[64:67],a[16:19],a[32:35],v[64:67]\nv_mul_f32 v25,v32,v45\n" \
"v_lshlrev_b32 v35,16,v41\nM v[68:71],a[20:23],a[32:35],v[68:71]\nv_mul_f32 v26,v38,v49\nv_fmac_f32 v25,v33,v44\nM v[76:79],a[20:23],a[36:39],v[76:79]\nv_fmac_f32 v26,v39,v48\nv_and_b32 v41,0xffff0000,v41\nM v[72:75],a[16:19],a[36:39],v[72:75]\n" \
"v_fmac_f32 v25,v34,v43\nv_fmac_f32 v26,v40,v47\nM v[88:91],a[16:19],a[44:47],v[88:91]\nv_fmac_f32 v25,v35,v42\nv_fmac_f32 v26,v41,v46\nM v[92:95],a[20:23],a[44:47],v[92:95]\nv_accvgpr_read_b32 v28,a219\nC v26,v25,v26\n" \
"M v[84:87],a[20:23],a[40:43],v[84:87]\ns_mov_b64 exec,0xff\nglobal_load_dword v0,v2,s[100:101]\ns_mov_b64 exec,-1\nM v[80:83],a[16:19],a[40:43],v[80:83]\nv_lshlrev_b32 v27,16,v28\nv_lshlrev_b32 v25,16,v26\nM v[96:99],a[16:19],a[48:51],v[96:99]\n" \
"buffer_load_dwordx4 v21,s[32:35],s47 offen offset:0 lds\nM v[100:103],a[20:23],a[48:51],v[100:103]\ns_add_u32 m0,s22,50048\nv_and_b32 v28,0xffff0000,v28\nv_and_b32 v26,0xffff0000,v26\nM v[108:111],a[20:23],a[52:55],v[108:111]\nv_mul_f32 v29,v25,v27\nv_mul_f32 v58,v26,v28\n" \
"M v[104:107],a[16:19],a[52:55],v[104:107]\nv_accvgpr_read_b32 v36,a205\nv_lshlrev_b32 v30,16,v36\nM v[120:123],a[16:19],a[60:63],v[120:123]\nbuffer_load_dwordx4 v21,s[32:35],s47 offen offset:128 lds\nM v[124:127],a[20:23],a[60:63],v[124:127]\ns_add_u32 m0,s22,51200\nv_mul_f32 v25,v33,v45\n" \
"v_mul_f32 v26,v39,v49\nM v[116:119],a[20:23],a[56:59],v[116:119]\ns_mov_b64 s[32:33],s[44:45]\nv_fmac_f32 v25,v34,v44\nv_and_b32 v36,0xffff0000,v36\nM v[112:115],a[16:19],a[56:59],v[112:115]\ns_mov_b32 s34,s49\nv_fmac_f32 v26,v40,v48\n" \
"v_fmac_f32 v25,v35,v43\nM v[192:195],a[0:3],a[32:35],v[192:195]\nbuffer_load_dwordx4 v21,s[92:95],s47 offen offset:0 lds\nM v[196:199],a[4:7],a[32:35],v[196:199]\ns_add_u32 m0,s22,52096\nv_fmac_f32 v26,v41,v47\nv_fmac_f32 v25,v30,v42\nM v[204:207],a[4:7],a[36:39],v[204:207]\n" \
"v_fmac_f32 v26,v36,v46\nv_accvgpr_read_b32 v28,a221\nM v[200:203],a[0:3],a[36:39],v[200:203]\nC v26,v25,v26\nv_lshlrev_b32 v27,16,v28\nM v[216:219],a[0:3],a[44:47],v[216:219]\nbuffer_load_dwordx4 v21,s[92:95],s47 offen offset:128 lds\nM v[220:223],a[4:7],a[44:47],v[220:223]\n" \
"s_add_u32 m0,s50,32768\nv_lshlrev_b32 v25,16,v26\nv_and_b32 v28,0xffff0000,v28\nM v[212:215],a[4:7],a[40:43],v[212:215]\ns_mov_b32 s47,0\nv_and_b32 v26,0xffff0000,v26\nv_mul_f32 v25,v25,v27\nM v[208:211],a[0:3],a[40:43],v[208:211]\n" \
"v_mul_f32 v26,v26,v28\nC v52,v25,v29\nM v[224:227],a[0:3],a[48:51],v[224:227]\nbuffer_load_dwordx4 v13,s[28:31],s46 offen lds\nM v[228:231],a[4:7],a[48:51],v[228:231]\ns_add_u32 m0,s50,36864\nC v56,v26,v58\nv_accvgpr_read_b32 v37,a207\n" \
"M v[236:239],a[4:7],a[52:55],v[236:239]\nv_mul_f32 v25,v34,v45\nv_lshlrev_b32 v31,16,v37\nM v[232:235],a[0:3],a[52:55],v[232:235]\nv_mul_f32 v26,v40,v49\nv_fmac_f32 v25,v35,v44\nM v[248:251],a[0:3],a[60:63],v[248:251]\nbuffer_load_dwordx4 v14,s[28:31],s46 offen lds\n" \
"M v[252:255],a[4:7],a[60:63],v[252:255]\ns_add_u32 m0,s50,40960\nv_fmac_f32 v26,v41,v48\nv_and_b32 v37,0xffff0000,v37\nM v[244:247],a[4:7],a[56:59],v[244:247]\nv_fmac_f32 v25,v30,v43\nv_fmac_f32 v26,v36,v47\nM v[240:243],a[0:3],a[56:59],v[240:243]\n" \
"v_fmac_f32 v25,v31,v42\nv_fmac_f32 v26,v37,v46\nM a[128:131],a[8:11],a[32:35],a[128:131]\nbuffer_load_dwordx4 v15,s[28:31],s46 offen lds\nM a[132:135],a[12:15],a[32:35],a[132:135]\ns_add_u32 m0,s50,45056\nv_accvgpr_read_b32 v28,a223\nC v26,v25,v26\n" \
"M a[140:143],a[12:15],a[36:39],a[140:143]\nv_lshlrev_b32 v27,16,v28\nv_lshlrev_b32 v25,16,v26\nM a[136:139],a[8:11],a[36:39],a[136:139]\nv_and_b32 v28,0xffff0000,v28\nv_and_b32 v26,0xffff0000,v26\nM a[152:155],a[8:11],a[44:47],a[152:155]\nbuffer_load_dwordx4 v16,s[28:31],s46 offen lds\n" \
"M a[156:159],a[12:15],a[44:47],a[156:159]\ns_add_u32 m0,s50,49152\nv_mul_f32 v29,v25,v27\nv_mul_f32 v58,v26,v28\nM a[148:151],a[12:15],a[40:43],a[148:151]\ns_waitcnt vmcnt(17) lgkmcnt(0)\nv_accvgpr_read_b32 v38,a209\nv_mul_f32 v25,v35,v45\n" \
"M a[144:147],a[8:11],a[40:43],a[144:147]\ns_mov_b32 s46,0\ns_barrier\nds_read_b64_tr_b16 a[32:33],v9 offset:32768\nM a[160:163],a[8:11],a[48:51],a[160:163]\nbuffer_load_dwordx4 v17,s[28:31],s46 offen lds\nM a[164:167],a[12:15],a[48:51],a[164:167]\ns_add_u32 m0,s50,53248\n" \
"ds_read_b64_tr_b16 a[34:35],v10 offset:32768\nv_mul_f32 v26,v41,v49\nM a[172:175],a[12:15],a[52:55],a[172:175]\nds_read_b64_tr_b16 a[36:37],v11 offset:32768\nds_read_b64_tr_b16 a[38:39],v12 offset:32768\nM a[168:171],a[8:11],a[52:55],a[168:171]\nds_read_b64_tr_b16 a[40:41],v9 offset:33280\nv_lshlrev_b32 v32,16,v38\n" \
"M a[184:187],a[8:11],a[60:63],a[184:187]\nbuffer_load_dwordx4 v18,s[28:31],s46 offen lds\nM a[188:191],a[12:15],a[60:63],a[188:191]\ns_add_u32 m0,s50,57344\nds_read_b64_tr_b16 a[42:43],v10 offset:33280\nv_fmac_f32 v25,v30,v44\nM a[180:183],a[12:15],a[56:59],a[180:183]\nds_read_b64_tr_b16 a[44:45],v11 offset:33280\n" \
"v_fmac_f32 v26,v36,v48\nM a[176:179],a[8:11],a[56:59],a[176:179]\nds_read_b64_tr_b16 a[46:47],v12 offset:33280\nv_and_b32 v38,0xffff0000,v38\nM v[128:131],a[88:91],a[96:99],v[128:131]\nbuffer_load_dwordx4 v19,s[28:31],s46 offen lds\nM v[132:135],a[92:95],a[96:99],v[132:135]\ns_add_u32 m0,s50,61440\n" \
"ds_read_b64_tr_b16 a[48:49],v9 offset:33792\nv_fmac_f32 v25,v31,v43\nM v[140:143],a[92:95],a[100:103],v[140:143]\nds_read_b64_tr_b16 a[50:51],v10 offset:33792\nv_fmac_f32 v26,v37,v47\nM v[136:139],a[88:91],a[100:103],v[136:139]\nds_read_b64_tr_b16 a[52:53],v11 offset:33792\nds_read_b64_tr_b16 a[54:55],v12 offset:33792\n" \
"M v[152:155],a[88:91],a[108:111],v[152:155]\nbuffer_load_dwordx4 v20,s[28:31],s46 offen lds\nM v[156:159],a[92:95],a[108:111],v[156:159]\ns_add_u32 m0,s51,0\nv_fmac_f32 v25,v32,v42\nv_fmac_f32 v26,v38,v46\nM v[148:151],a[92:95],a[104:107],v[148:151]\ns_mov_b32 s46,128\n" \
"ds_read_b64_tr_b16 a[56:57],v9 offset:34304\nds_read_b64_tr_b16 a[58:59],v10 offset:34304\nM v[144:147],a[88:91],a[104:107],v[144:147]\nds_read_b64_tr_b16 a[60:61],v11 offset:34304\nv_accvgpr_read_b32 v28,a225\nM v[160:163],a[88:91],a[112:115],v[160:163]\nbuffer_load_dwordx4 v21,s[32:35],s47 offen offset:0 lds\nM v[164:167],a[92:95],a[112:115],v[164:167]\n" \
"s_add_u32 m0,s51,896\nds_read_b64_tr_b16 a[62:63],v12 offset:34304\nC v26,v25,v26\nM v[172:175],a[92:95],a[116:119],v[172:175]\nds_read_b128 a[24:27],v5 offset:12288\nv_lshlrev_b32 v27,16,v28\nM v[168:171],a[88:91],a[116:119],v[168:171]\nds_read_b128 a[28:31],v5 offset:14336\n" \
"v_lshlrev_b32 v25,16,v26\nM v[184:187],a[88:91],a[124:127],v[184:187]\nbuffer_load_dwordx4 v21,s[32:35],s47 offen offset:128 lds\nM v[188:191],a[92:95],a[124:127],v[188:191]\ns_add_u32 m0,s51,8192\nv_and_b32 v28,0xffff0000,v28\nv_and_b32 v26,0xffff0000,v26\nM v[180:183],a[92:95],a[120:123],v[180:183]\n" \
"s_mov_b32 s47,262144\nv_mul_f32 v25,v25,v27\nv_mul_f32 v26,v26,v28\nM v[176:179],a[88:91],a[120:123],v[176:179]\nC v51,v25,v29\nC v55,v26,v58\nM v[64:67],a[80:83],a[96:99],v[64:67]\nbuffer_load_dwordx4 v21,s[32:35],s47 offen offset:0 lds\n" \
"M v[68:71],a[84:87],a[96:99],v[68:71]\ns_add_u32 m0,s51,9088\nv_accvgpr_read_b32 v39,a211\nv_mul_f32 v25,v30,v45\nM v[76:79],a[84:87],a[100:103],v[76:79]\nv_lshlrev_b32 v33,16,v39\nv_mul_f32 v26,v36,v49\nM v[72:75],a[80:83],a[100:103],v[72:75]\n" \
"v_fmac_f32 v25,v31,v44\nv_fmac_f32 v26,v37,v48\nM v[88:91],a[80:83],a[108:111],v[88:91]\nbuffer_load_dwordx4 v21,s[32:35],s47 offen offset:128 lds\nM v[92:95],a[84:87],a[108:111],v[92:95]\ns_add_u32 m0,s51,16384\nv_and_b32 v39,0xffff0000,v39\nv_fmac_f32 v25,v32,v43\n" \
"M v[84:87],a[84:87],a[104:107],v[84:87]\ns_mov_b32 s47,524288\nv_fmac_f32 v26,v38,v47\nv_fmac_f32 v25,v33,v42\nM v[80:83],a[80:83],a[104:107],v[80:83]\nv_fmac_f32 v26,v39,v46\nv_accvgpr_read_b32 v28,a227\nM v[96:99],a[80:83],a[112:115],v[96:99]\n" \
"buffer_load_dwordx4 v21,s[32:35],s47 offen offset:0 lds\nM v[100:103],a[84:87],a[112:115],v[100:103]\ns_add_u32 m0,s51,17280\nC v26,v25,v26\nv_lshlrev_b32 v27,16,v28\nM v[108:111],a[84:87],a[116:119],v[108:111]\nds_read_b128 a[16:19],v5 offset:8192\nv_lshlrev_b32 v25,16,v26\n" \
"M v[104:107],a[80:83],a[116:119],v[104:107]\nds_read_b128 a[20:23],v5 offset:10240\nv_and_b32 v28,0xffff0000,v28\nM v[120:123],a[80:83],a[124:127],v[120:123]\nbuffer_load_dwordx4 v21,s[32:35],s47 offen offset:128 lds\nM v[124:127],a[84:87],a[124:127],v[124:127]\ns_add_u32 m0,s51,24576\nv_and_b32 v26,0xffff0000,v26\n" \
"v_mul_f32 v29,v25,v27\nM v[116:119],a[84:87],a[120:123],v[116:119]\ns_mov_b32 s47,786432\nv_mul_f32 v58,v26,v28\nv_accvgpr_read_b32 v40,a213\nM v[112:115],a[80:83],a[120:123],v[112:115]\nv_lshlrev_b32 v34,16,v40\nv_mul_f32 v25,v31,v45\n" \
"M v[192:195],a[64:67],a[96:99],v[192:195]\ns_mov_b64 exec,0xff\nglobal_load_dword v0,v1,s[76:77]\ns_mov_b64 exec,-1\nM v[196:199],a[68:71],a[96:99],v[196:199]\nv_mul_f32 v26,v37,v49\nv_fmac_f32 v25,v32,v44\nM v[204:207],a[68:71],a[100:103],v[204:207]\n" \
"s_mov_b64 exec,0xff00\nglobal_load_dword v0,v1,s[76:77]\ns_mov_b64 exec,-1\nM v[200:203],a[64:67],a[100:103],v[200:203]\ns_add_u32 s76,s76,2097152\ns_addc_u32 s77,s77,0\nv_and_b32 v40,0xffff0000,v40\nv_fmac_f32 v26,v38,v48\n" \
"M v[216:219],a[64:67],a[108:111],v[216:219]\ns_mov_b64 exec,0xff\nglobal_load_dword v0,v1,s[76:77]\ns_mov_b64 exec,-1\nM v[220:223],a[68:71],a[108:111],v[220:223]\nv_fmac_f32 v25,v33,v43\nv_fmac_f32 v26,v39,v47\nM v[212:215],a[68:71],a[104:107],v[212:215]\n" \
"s_mov_b64 exec,0xff00\nglobal_load_dword v0,v1,s[76:77]\ns_mov_b64 exec,-1\nM v[208:211],a[64:67],a[104:107],v[208:211]\ns_mov_b32 s76,0xf149f2ca\nv_fmac_f32 v25,v34,v42\nv_fmac_f32 v26,v40,v46\nM v[224:227],a[64:67],a[112:115],v[224:227]\n" \
"v_accvgpr_read_b32 v28,a229\nC v26,v25,v26\nM v[228:231],a[68:71],a[112:115],v[228:231]\nv_lshlrev_b32 v27,16,v28\nv_lshlrev_b32 v25,16,v26\nM v[236:239],a[68:71],a[116:119],v[236:239]\nv_mul_f32 v25,v25,v27\nC v50,v25,v29\n" \
"M v[232:235],a[64:67],a[116:119],v[232:235]\nds_read_b128 a[0:3],v5 offset:0\nds_read_b128 a[4:7],v5 offset:2048\nM v[248:251],a[64:67],a[124:127],v[248:251]\nds_read_b128 a[8:11],v5 offset:4096\nv_and_b32 v28,0xffff0000,v28\nM v[252:255],a[68:71],a[124:127],v[252:255]\nLG7_%=:\n" \
"s_cbranch_execz LG11_%=\nds_read_b128 a[12:15],v5 offset:6144\nv_and_b32 v26,0xffff0000,v26\nM v[244:247],a[68:71],a[120:123],v[244:247]\nv_mul_f32 v26,v26,v28\nC v54,v26,v58\nM v[240:243],a[64:67],a[120:123],v[240:243]\nds_read_b128 a[64:67],v6 offset:0\n" \
"ds_read_b128 a[68:71],v6 offset:2048\nM a[128:131],a[72:75],a[96:99],a[128:131]\nds_read_b128 a[80:83],v6 offset:8192\nv_add_u32 v5,s54,v5\nM a[132:135],a[76:79],a[96:99],a[132:135]\nds_read_b128 a[84:87],v6 offset:10240\nv_add_u32 v7,s54,v7\nM a[140:143],a[76:79],a[100:103],a[140:143]\n" \
"ds_read_b128 a[88:91],v6 offset:12288\nv_add_u32 v8,s54,v8\nM a[136:139],a[72:75],a[100:103],a[136:139]\nds_read_b128 a[92:95],v6 offset:14336\nv_mov_b32 v58,s18\nM a[152:155],a[72:75],a[108:111],a[152:155]\nv_add_u32 v25,-16,v4\nv_mov_b32 v29,s62\n" \
"M a[156:159],a[76:79],a[108:111],a[156:159]\ns_and_b32 s62,%1,7\nv_mov_b32 v28,0\nv_add_u32 v26,-512,v4\nM a[148:151],a[76:79],a[104:107],a[148:151]\ns_waitcnt vmcnt(18) lgkmcnt(14)\nv_add_u32 v27,512,v4\nM a[144:147],a[72:75],a[104:107],a[144:147]\n" \
"s_lshl_b32 s62,s62,8\ns_barrier\nds_read_b64_tr_b16 a[96:97],v9 offset:49152\nM a[160:163],a[72:75],a[112:115],a[160:163]\ns_add_u32 s62,s62,94380032\nds_read_b64_tr_b16 a[98:99],v10 offset:49152\nds_read_b64_tr_b16 a[100:101],v11 offset:49152\nM a[164:167],a[76:79],a[112:115],a[164:167]\n" \
"ds_read_b64_tr_b16 a[102:103],v12 offset:49152\nM a[172:175],a[76:79],a[116:119],a[172:175]\nds_read_b64_tr_b16 a[104:105],v9 offset:49664\nds_read_b64_tr_b16 a[106:107],v10 offset:49664\nM a[168:171],a[72:75],a[116:119],a[168:171]\nds_read_b64_tr_b16 a[108:109],v11 offset:49664\nM a[184:187],a[72:75],a[124:127],a[184:187]\nds_read_b64_tr_b16 a[110:111],v12 offset:49664\n" \
"M a[188:191],a[76:79],a[124:127],a[188:191]\nds_read_b64_tr_b16 a[112:113],v9 offset:50176\nds_read_b64_tr_b16 a[114:115],v10 offset:50176\nM a[180:183],a[76:79],a[120:123],a[180:183]\nds_read_b64_tr_b16 a[116:117],v11 offset:50176\nM a[176:179],a[72:75],a[120:123],a[176:179]\ns_waitcnt lgkmcnt(7)\nds_read_b128 a[72:75],v6 offset:4096\n" \
"M v[128:131],a[24:27],a[32:35],v[128:131]\nds_read_b128 a[76:79],v6 offset:6144\nv_add_u32 v6,s54,v6\nM v[132:135],a[28:31],a[32:35],v[132:135]\nds_read_b64_tr_b16 a[118:119],v12 offset:50176\nM v[140:143],a[28:31],a[36:39],v[140:143]\nds_read_b64_tr_b16 a[120:121],v9 offset:50688\nds_read_b64_tr_b16 a[122:123],v10 offset:50688\n" \
"M v[136:139],a[24:27],a[36:39],v[136:139]\nds_read_b64_tr_b16 a[124:125],v11 offset:50688\nv_sub_u32 v9,v9,v62\nM v[152:155],a[24:27],a[44:47],v[152:155]\nds_read_b64_tr_b16 a[126:127],v12 offset:50688\nv_sub_u32 v10,v10,v62\nM v[156:159],a[28:31],a[44:47],v[156:159]\nv_sub_u32 v11,v11,v62\n" \
"v_sub_u32 v12,v12,v62\nM v[148:151],a[28:31],a[40:43],v[148:151]\nM v[144:147],a[24:27],a[40:43],v[144:147]\nM v[160:163],a[24:27],a[48:51],v[160:163]\nM v[164:167],a[28:31],a[48:51],v[164:167]\nM v[172:175],a[28:31],a[52:55],v[172:175]\nM v[168:171],a[24:27],a[52:55],v[168:171]\nM v[184:187],a[24:27],a[60:63],v[184:187]\n" \
"M v[188:191],a[28:31],a[60:63],v[188:191]\nM v[180:183],a[28:31],a[56:59],v[180:183]\nbuffer_load_dwordx4 v21,s[32:35],s47 offen offset:0 lds\nM v[176:179],a[24:27],a[56:59],v[176:179]\ns_add_u32 m0,s51,25472\nM v[64:67],a[16:19],a[32:35],v[64:67]\nM v[68:71],a[20:23],a[32:35],v[68:71]\nM v[76:79],a[20:23],a[36:39],v[76:79]\n" \
"buffer_store_dwordx4 v[50:53],v60,s[36:39],s98 offen offset:0 sc1\nM v[72:75],a[16:19],a[36:39],v[72:75]\nM v[88:91],a[16:19],a[44:47],v[88:91]\nM v[92:95],a[20:23],a[44:47],v[92:95]\nM v[84:87],a[20:23],a[40:43],v[84:87]\nM v[80:83],a[16:19],a[40:43],v[80:83]\nM v[96:99],a[16:19],a[48:51],v[96:99]\nM v[100:103],a[20:23],a[48:51],v[100:103]\n" \
"M v[108:111],a[20:23],a[52:55],v[108:111]\nM v[104:107],a[16:19],a[52:55],v[104:107]\nM v[120:123],a[16:19],a[60:63],v[120:123]\nM v[124:127],a[20:23],a[60:63],v[124:127]\nM v[116:119],a[20:23],a[56:59],v[116:119]\nM v[112:115],a[16:19],a[56:59],v[112:115]\nM v[192:195],a[0:3],a[32:35],v[192:195]\nM v[196:199],a[4:7],a[32:35],v[196:199]\n" \
"M v[204:207],a[4:7],a[36:39],v[204:207]\nM v[200:203],a[0:3],a[36:39],v[200:203]\nM v[216:219],a[0:3],a[44:47],v[216:219]\nM v[220:223],a[4:7],a[44:47],v[220:223]\nM v[212:215],a[4:7],a[40:43],v[212:215]\nM v[208:211],a[0:3],a[40:43],v[208:211]\nM v[224:227],a[0:3],a[48:51],v[224:227]\nM v[228:231],a[4:7],a[48:51],v[228:231]\n" \
"M v[236:239],a[4:7],a[52:55],v[236:239]\nbuffer_store_dwordx4 v[54:57],v60,s[36:39],s99 offen offset:0 sc1\nM v[232:235],a[0:3],a[52:55],v[232:235]\ns_nop 0\nv_lshlrev_b32 v56,23,v4\nv_bfe_u32 v57,v4,4,5\nM v[248:251],a[0:3],a[60:63],v[248:251]\nv_lshl_add_u32 v57,v57,3,s62\n" \
"v_add_u32 v55,s56,v4\nM v[252:255],a[4:7],a[60:63],v[252:255]\nM v[244:247],a[4:7],a[56:59],v[244:247]\nM v[240:243],a[0:3],a[56:59],v[240:243]\nM a[128:131],a[8:11],a[32:35],a[128:131]\nM a[132:135],a[12:15],a[32:35],a[132:135]\nM a[140:143],a[12:15],a[36:39],a[140:143]\nM a[136:139],a[8:11],a[36:39],a[136:139]\n" \
"M a[152:155],a[8:11],a[44:47],a[152:155]\nM a[156:159],a[12:15],a[44:47],a[156:159]\nM a[148:151],a[12:15],a[40:43],a[148:151]\ns_waitcnt vmcnt(9) lgkmcnt(0)\nM a[144:147],a[8:11],a[40:43],a[144:147]\ns_barrier\nds_read_b64_tr_b16 a[0:1],v9 offset:0\nM a[160:163],a[8:11],a[48:51],a[160:163]\n" \
"ds_read_b64_tr_b16 a[2:3],v10 offset:0\nds_read_b64_tr_b16 a[4:5],v11 offset:0\nM a[164:167],a[12:15],a[48:51],a[164:167]\nds_read_b128 a[44:47],v5 offset:45056\nM a[172:175],a[12:15],a[52:55],a[172:175]\nds_read_b64_tr_b16 a[6:7],v12 offset:0\nM a[168:171],a[8:11],a[52:55],a[168:171]\nds_read_b64_tr_b16 a[16:17],v9 offset:1024\n" \
"ds_read_b64_tr_b16 a[18:19],v10 offset:1024\nM a[184:187],a[8:11],a[60:63],a[184:187]\nds_read_b64_tr_b16 a[20:21],v11 offset:1024\nM a[188:191],a[12:15],a[60:63],a[188:191]\nds_read_b64_tr_b16 a[22:23],v12 offset:1024\nM a[180:183],a[12:15],a[56:59],a[180:183]\nds_read_b64_tr_b16 a[24:25],v9 offset:1536\nds_read_b64_tr_b16 a[26:27],v10 offset:1536\n" \
"M a[176:179],a[8:11],a[56:59],a[176:179]\nds_read_b64_tr_b16 a[8:9],v9 offset:512\nM v[128:131],a[88:91],a[96:99],v[128:131]\nds_read_b64_tr_b16 a[10:11],v10 offset:512\nM v[132:135],a[92:95],a[96:99],v[132:135]\nds_read_b64_tr_b16 a[12:13],v11 offset:512\nds_read_b64_tr_b16 a[14:15],v12 offset:512\nM v[140:143],a[92:95],a[100:103],v[140:143]\n" \
"buffer_load_dwordx4 v21,s[32:35],s47 offen offset:128 lds\nM v[136:139],a[88:91],a[100:103],v[136:139]\ns_add_u32 m0,s50,65536\nv_mul_f32 v128,s20,v128\nv_mul_f32 v129,s20,v129\nM v[152:155],a[88:91],a[108:111],v[152:155]\ns_mov_b32 s47,1048576\nv_mul_f32 v130,s20,v130\n" \
"v_mul_f32 v131,s20,v131\nM v[156:159],a[92:95],a[108:111],v[156:159]\nv_mul_f32 v132,s20,v132\nv_mul_f32 v133,s20,v133\nM v[148:151],a[92:95],a[104:107],v[148:151]\nbuffer_load_dwordx4 v13,s[28:31],s46 offen lds\nM v[144:147],a[88:91],a[104:107],v[144:147]\ns_add_u32 m0,s50,69632\n" \
"v_mul_f32 v134,s20,v134\nv_mul_f32 v135,s20,v135\nM v[160:163],a[88:91],a[112:115],v[160:163]\nv_mul_f32 v136,s20,v136\nv_mul_f32 v137,s20,v137\nM v[164:167],a[92:95],a[112:115],v[164:167]\nv_mul_f32 v138,s20,v138\nv_mul_f32 v139,s20,v139\n" \
"M v[172:175],a[92:95],a[116:119],v[172:175]\nbuffer_load_dwordx4 v14,s[28:31],s46 offen lds\nM v[168:171],a[88:91],a[116:119],v[168:171]\ns_add_u32 m0,s50,73728\nds_read_b64_tr_b16 a[28:29],v11 offset:1536\nv_mul_f32 v140,s20,v140\nM v[184:187],a[88:91],a[124:127],v[184:187]\nds_read_b64_tr_b16 a[30:31],v12 offset:1536\n" \
"ds_read_b128 a[40:43],v5 offset:40960\nM v[188:191],a[92:95],a[124:127],v[188:191]\nv_mul_f32 v141,s20,v141\nv_mul_f32 v142,s20,v142\nM v[180:183],a[92:95],a[120:123],v[180:183]\nbuffer_load_dwordx4 v15,s[28:31],s46 offen lds\nM v[176:179],a[88:91],a[120:123],v[176:179]\ns_add_u32 m0,s50,77824\n" \
"v_mul_f32 v143,s20,v143\nv_mul_f32 v144,s20,v144\nM v[64:67],a[80:83],a[96:99],v[64:67]\nv_mul_f32 v145,s20,v145\nv_mul_f32 v146,s20,v146\nM v[68:71],a[84:87],a[96:99],v[68:71]\nds_read_b128 a[32:35],v5 offset:32768\nv_mul_f32 v147,s20,v147\n" \
"M v[76:79],a[84:87],a[100:103],v[76:79]\nbuffer_load_dwordx4 v16,s[28:31],s46 offen lds\nM v[72:75],a[80:83],a[100:103],v[72:75]\ns_add_u32 m0,s50,81920\ns_mov_b32 s46,128\nv_mul_f32 v64,s20,v64\nv_mul_f32 v65,s20,v65\nM v[88:91],a[80:83],a[108:111],v[88:91]\n" \
"v_mul_f32 v66,s20,v66\nv_mul_f32 v67,s20,v67\nM v[92:95],a[84:87],a[108:111],v[92:95]\nC v64,v64,v65\nC v65,v66,v67\nM v[84:87],a[84:87],a[104:107],v[84:87]\nbuffer_load_dwordx4 v17,s[28:31],s46 offen lds\nM v[80:83],a[80:83],a[104:107],v[80:83]\n" \
"s_add_u32 m0,s50,86016\nv_mul_f32 v68,s20,v68\nv_mul_f32 v69,s20,v69\nM v[96:99],a[80:83],a[112:115],v[96:99]\nv_mul_f32 v148,s20,v148\nv_mul_f32 v149,s20,v149\nM v[100:103],a[84:87],a[112:115],v[100:103]\nv_mul_f32 v150,s20,v150\n" \
"v_mul_f32 v151,s20,v151\nM v[108:111],a[84:87],a[116:119],v[108:111]\nbuffer_load_dwordx4 v18,s[28:31],s46 offen lds\nM v[104:107],a[80:83],a[116:119],v[104:107]\ns_add_u32 m0,s50,90112\nv_mul_f32 v70,s20,v70\nv_mul_f32 v152,s20,v152\nM v[120:123],a[80:83],a[124:127],v[120:123]\n" \
"v_mul_f32 v71,s20,v71\nv_mul_f32 v153,s20,v153\nM v[124:127],a[84:87],a[124:127],v[124:127]\nv_mul_f32 v154,s20,v154\nv_mul_f32 v155,s20,v155\nM v[116:119],a[84:87],a[120:123],v[116:119]\nbuffer_load_dwordx4 v19,s[28:31],s46 offen lds\nM v[112:115],a[80:83],a[120:123],v[112:115]\n" \
"s_add_u32 m0,s50,94208\nv_mul_f32 v156,s20,v156\nv_mul_f32 v157,s20,v157\nM v[192:195],a[64:67],a[96:99],v[192:195]\nv_mul_f32 v158,s20,v158\nv_mul_f32 v159,s20,v159\nM v[196:199],a[68:71],a[96:99],v[196:199]\nv_mul_f32 v160,s20,v160\n" \
"v_mul_f32 v161,s20,v161\nM v[204:207],a[68:71],a[100:103],v[204:207]\nbuffer_load_dwordx4 v20,s[28:31],s46 offen lds\nM v[200:203],a[64:67],a[100:103],v[200:203]\ns_add_u32 m0,s51,32768\nds_read_b128 a[36:39],v5 offset:36864\nv_mul_f32 v162,s20,v162\nM v[216:219],a[64:67],a[108:111],v[216:219]\n" \
"s_mov_b32 s46,256\nv_mul_f32 v163,s20,v163\nC v66,v68,v69\nM v[220:223],a[68:71],a[108:111],v[220:223]\nv_mul_f32 v72,s20,v72\nv_mul_f32 v164,s20,v164\nM v[212:215],a[68:71],a[104:107],v[212:215]\nbuffer_load_dwordx4 v21,s[32:35],s47 offen offset:0 lds\n" \
"M v[208:211],a[64:67],a[104:107],v[208:211]\ns_add_u32 m0,s51,33664\nv_mul_f32 v73,s20,v73\nC v30,v128,v129\nM v[224:227],a[64:67],a[112:115],v[224:227]\nv_mul_f32 v165,s20,v165\nC v31,v130,v131\nM v[228:231],a[68:71],a[112:115],v[228:231]\n" \
"v_mul_f32 v166,s20,v166\nC v32,v132,v133\nM v[236:239],a[68:71],a[116:119],v[236:239]\nbuffer_load_dwordx4 v21,s[32:35],s47 offen offset:128 lds\nM v[232:235],a[64:67],a[116:119],v[232:235]\ns_add_u32 m0,s51,40960\nv_mul_f32 v167,s20,v167\nC v33,v134,v135\n" \
"M v[248:251],a[64:67],a[124:127],v[248:251]\ns_mov_b32 s47,1310720\nv_mul_f32 v168,s20,v168\nC v34,v136,v137\nM v[252:255],a[68:71],a[124:127],v[252:255]\nv_mul_f32 v169,s20,v169\nC v35,v138,v139\nM v[244:247],a[68:71],a[120:123],v[244:247]\n" \
"buffer_load_dwordx4 v21,s[32:35],s47 offen offset:0 lds\nM v[240:243],a[64:67],a[120:123],v[240:243]\ns_add_u32 m0,s51,41856\nds_read_b64_tr_b16 a[48:49],v9 offset:8192\nv_mul_f32 v170,s20,v170\nM a[128:131],a[72:75],a[96:99],a[128:131]\nds_read_b64_tr_b16 a[50:51],v10 offset:8192\nds_read_b128 a[80:83],v6 offset:32768\n" \
"M a[132:135],a[76:79],a[96:99],a[132:135]\nds_read_b128 a[84:87],v6 offset:36864\nv_mul_f32 v171,s20,v171\nM a[140:143],a[76:79],a[100:103],a[140:143]\nbuffer_load_dwordx4 v21,s[32:35],s47 offen offset:128 lds\nM a[136:139],a[72:75],a[100:103],a[136:139]\ns_add_u32 m0,s50,0\nv_mul_f32 v74,s20,v74\n" \
"C v36,v140,v141\nM a[152:155],a[72:75],a[108:111],a[152:155]\ns_mov_b32 s47,1572864\nds_read_b64_tr_b16 a[52:53],v11 offset:8192\nv_mul_f32 v75,s20,v75\nM a[156:159],a[76:79],a[108:111],a[156:159]\nds_read_b64_tr_b16 a[54:55],v12 offset:8192\nds_read_b64_tr_b16 a[56:57],v9 offset:8704\n" \
"M a[148:151],a[76:79],a[104:107],a[148:151]\nbuffer_load_dwordx4 v13,s[28:31],s46 offen lds\nM a[144:147],a[72:75],a[104:107],a[144:147]\ns_add_u32 m0,s50,4096\nC v37,v142,v143\nC v38,v144,v145\nM a[160:163],a[72:75],a[112:115],a[160:163]\nds_read_b64_tr_b16 a[58:59],v10 offset:8704\n" \
"ds_read_b64_tr_b16 a[60:61],v11 offset:8704\nM a[164:167],a[76:79],a[112:115],a[164:167]\nds_read_b64_tr_b16 a[62:63],v12 offset:8704\nC v39,v146,v147\nM a[172:175],a[76:79],a[116:119],a[172:175]\nbuffer_load_dwordx4 v14,s[28:31],s46 offen lds\nM a[168:171],a[72:75],a[116:119],a[168:171]\ns_add_u32 m0,s50,8192\n" \
"ds_read_b64_tr_b16 a[64:65],v9 offset:9216\nv_mul_f32 v172,s20,v172\nM a[184:187],a[72:75],a[124:127],a[184:187]\nds_read_b64_tr_b16 a[66:67],v10 offset:9216\nv_mul_f32 v173,s20,v173\nM a[188:191],a[76:79],a[124:127],a[188:191]\ns_waitcnt lgkmcnt(5)\nds_read_b64_tr_b16 a[68:69],v11 offset:9216\n" \
"v_mul_f32 v174,s20,v174\nM a[180:183],a[76:79],a[120:123],a[180:183]\nbuffer_load_dwordx4 v15,s[28:31],s46 offen lds\nM a[176:179],a[72:75],a[120:123],a[176:179]\nLC_%=:\ns_add_u32 m0,s50,12288\nds_read_b64_tr_b16 a[70:71],v12 offset:9216\nC v40,v148,v149\n" \
"M a[192:195],a[0:3],a[44:47],0\nds_read_b64_tr_b16 a[72:73],v9 offset:9728\nv_mul_f32 v175,s20,v175\nM a[196:199],a[4:7],a[44:47],0\nds_read_b64_tr_b16 a[74:75],v10 offset:9728\nds_read_b64_tr_b16 a[76:77],v11 offset:9728\nM a[200:203],a[8:11],a[44:47],0\nbuffer_load_dwordx4 v16,s[28:31],s46 offen lds\n" \
"M a[204:207],a[12:15],a[44:47],0\ns_mov_b32 s46,256\nC v41,v150,v151\nC v67,v70,v71\nM a[208:211],a[16:19],a[44:47],0\ns_add_u32 m0,s50,16384\nds_read_b64_tr_b16 a[78:79],v12 offset:9728\nds_read_b128 a[88:91],v6 offset:40960\n" \
"M a[212:215],a[20:23],a[44:47],0\nds_read_b128 a[92:95],v6 offset:45056\nv_mul_f32 v76,s20,v76\nM a[216:219],a[24:27],a[44:47],0\nbuffer_load_dwordx4 v17,s[28:31],s46 offen lds\nM a[220:223],a[28:31],a[44:47],0\ns_add_u32 m0,s50,20480\nv_mul_f32 v77,s20,v77\n" \
"C v42,v152,v153\nM a[224:227],a[0:3],a[40:43],0\nC v43,v154,v155\nC v44,v156,v157\nM a[228:231],a[4:7],a[40:43],0\nC v45,v158,v159\nC v46,v160,v161\nM a[232:235],a[8:11],a[40:43],0\n" \
"buffer_load_dwordx4 v18,s[28:31],s46 offen lds\nM a[236:239],a[12:15],a[40:43],0\ns_add_u32 m0,s50,24576\nC v47,v162,v163\nC v68,v72,v73\nM a[240:243],a[16:19],a[40:43],0\nv_mul_f32 v176,s20,v176\nC v48,v164,v165\n" \
"M a[244:247],a[20:23],a[40:43],0\nv_mul_f32 v177,s20,v177\nC v49,v166,v167\nM a[248:251],a[24:27],a[40:43],0\nbuffer_load_dwordx4 v19,s[28:31],s46 offen lds\nM a[252:255],a[28:31],a[40:43],0\ns_add_u32 m0,s50,28672\nv_mul_f32 v178,s20,v178\n" \
"C v50,v168,v169\nM v[128:131],a[0:3],a[32:35],0\nv_mul_f32 v179,s20,v179\nC v51,v170,v171\nM v[132:135],a[4:7],a[32:35],0\nC v69,v74,v75\nv_mul_f32 v180,s20,v180\nM v[136:139],a[8:11],a[32:35],0\n" \
"buffer_load_dwordx4 v20,s[28:31],s46 offen lds\nM v[140:143],a[12:15],a[32:35],0\ns_add_u32 m0,s51,49152\nv_mul_f32 v181,s20,v181\nC v52,v172,v173\nM v[144:147],a[16:19],a[32:35],0\ns_mov_b32 s46,384\nC v53,v174,v175\n" \
"C v70,v76,v77\nM v[148:151],a[20:23],a[32:35],0\nv_mul_f32 v182,s20,v182\nv_mul_f32 v183,s20,v183\nM v[152:155],a[24:27],a[32:35],0\nbuffer_load_dwordx4 v21,s[32:35],s47 offen offset:0 lds\nM v[156:159],a[28:31],a[32:35],0\ns_add_u32 m0,s51,50048\n" \
"v_mul_f32 v184,s20,v184\nv_mul_f32 v78,s20,v78\nM v[160:163],a[0:3],a[36:39],0\ns_waitcnt vmcnt(13) lgkmcnt(0)\nv_mul_f32 v185,s20,v185\nv_mul_f32 v79,s20,v79\nM v[164:167],a[4:7],a[36:39],0\ns_barrier\n" \
"C v72,v176,v177\nM v[168:171],a[8:11],a[36:39],0\nbuffer_store_dwordx2 v[58:59],v56,s[36:39],s23 offen sc1\nM v[172:175],a[12:15],a[36:39],0\nC v73,v178,v179\nv_mul_f32 v186,s20,v186\nM v[176:179],a[16:19],a[36:39],0\nC v74,v180,v181\n" \
"C v75,v182,v183\nv_mul_f32 v187,s20,v187\nM v[180:183],a[20:23],a[36:39],0\nC v76,v184,v185\nC v77,v186,v187\nM v[184:187],a[24:27],a[36:39],0\nC v71,v78,v79\nv_mul_f32 v188,s20,v188\n" \
"v_mul_f32 v189,s20,v189\nv_mul_f32 v190,s20,v190\nv_mul_f32 v191,s20,v191\nC v78,v188,v189\nC v79,v190,v191\nM v[188:191],a[28:31],a[36:39],0\nbuffer_load_dwordx4 v21,s[32:35],s47 offen offset:128 lds\nM v[128:131],a[48:51],a[80:83],v[128:131]\n" \
"s_add_u32 m0,s51,57344\nds_read_b64_tr_b16 a[0:1],v9 offset:16384\nv_mul_f32 v80,s20,v80\nM v[160:163],a[48:51],a[84:87],v[160:163]\ns_mov_b32 s47,1835008\nds_read_b64_tr_b16 a[2:3],v10 offset:16384\nds_read_b128 a[32:35],v7 offset:0\nM a[224:227],a[48:51],a[88:91],a[224:227]\n" \
"ds_read_b128 a[36:39],v7 offset:4096\nv_mul_f32 v81,s20,v81\nM a[192:195],a[48:51],a[92:95],a[192:195]\nbuffer_load_dwordx4 v21,s[32:35],s47 offen offset:0 lds\nM a[196:199],a[52:55],a[92:95],a[196:199]\ns_add_u32 m0,s51,58240\nv_mul_f32 v112,s20,v112\nv_mul_f32 v82,s20,v82\n" \
"M a[228:231],a[52:55],a[88:91],a[228:231]\nds_read_b128 a[40:43],v7 offset:8192\nv_mul_f32 v113,s20,v113\nM v[164:167],a[52:55],a[84:87],v[164:167]\nds_read_b128 a[44:47],v7 offset:12288\nv_mul_f32 v83,s20,v83\nM v[132:135],a[52:55],a[80:83],v[132:135]\nbuffer_load_dwordx4 v21,s[32:35],s47 offen offset:128 lds\n" \
"M v[136:139],a[56:59],a[80:83],v[136:139]\ns_add_u32 m0,s50,32768\nds_read_b64_tr_b16 a[4:5],v11 offset:16384\nC v80,v80,v81\nM v[168:171],a[56:59],a[84:87],v[168:171]\ns_mov_b32 s47,2097152\nds_read_b64_tr_b16 a[6:7],v12 offset:16384\nv_mul_f32 v114,s20,v114\n" \
"M a[232:235],a[56:59],a[88:91],a[232:235]\nds_read_b64_tr_b16 a[8:9],v9 offset:16896\nv_mul_f32 v115,s20,v115\nM a[200:203],a[56:59],a[92:95],a[200:203]\nbuffer_load_dwordx4 v13,s[28:31],s46 offen lds\nM a[204:207],a[60:63],a[92:95],a[204:207]\ns_add_u32 m0,s50,36864\nds_read_b64_tr_b16 a[10:11],v10 offset:16896\n" \
"C v112,v112,v113\nM a[236:239],a[60:63],a[88:91],a[236:239]\nds_read_b64_tr_b16 a[12:13],v11 offset:16896\nC v81,v82,v83\nM v[172:175],a[60:63],a[84:87],v[172:175]\nds_read_b64_tr_b16 a[14:15],v12 offset:16896\nds_read_b64_tr_b16 a[16:17],v9 offset:17408\nM v[140:143],a[60:63],a[80:83],v[140:143]\n" \
"buffer_load_dwordx4 v14,s[28:31],s46 offen lds\nM v[144:147],a[64:67],a[80:83],v[144:147]\ns_add_u32 m0,s50,40960\nv_mul_f32 v84,s20,v84\nv_mul_f32 v85,s20,v85\nM v[176:179],a[64:67],a[84:87],v[176:179]\nds_read_b64_tr_b16 a[18:19],v10 offset:17408\nds_read_b64_tr_b16 a[20:21],v11 offset:17408\n" \
"M a[240:243],a[64:67],a[88:91],a[240:243]\nds_read_b64_tr_b16 a[22:23],v12 offset:17408\nC v113,v114,v115\nM a[208:211],a[64:67],a[92:95],a[208:211]\nbuffer_load_dwordx4 v15,s[28:31],s46 offen lds\nM a[212:215],a[68:71],a[92:95],a[212:215]\ns_add_u32 m0,s50,45056\nds_read_b64_tr_b16 a[24:25],v9 offset:17920\n" \
"v_mul_f32 v118,s20,v118\nM a[244:247],a[68:71],a[88:91],a[244:247]\nds_read_b64_tr_b16 a[26:27],v10 offset:17920\nv_mul_f32 v119,s20,v119\nM v[180:183],a[68:71],a[84:87],v[180:183]\nds_read_b64_tr_b16 a[28:29],v11 offset:17920\nv_mul_f32 v86,s20,v86\nM v[148:151],a[68:71],a[80:83],v[148:151]\n" \
"buffer_load_dwordx4 v16,s[28:31],s46 offen lds\nM v[152:155],a[72:75],a[80:83],v[152:155]\ns_add_u32 m0,s50,49152\nds_read_b64_tr_b16 a[30:31],v12 offset:17920\nv_mul_f32 v87,s20,v87\nM v[184:187],a[72:75],a[84:87],v[184:187]\ns_mov_b32 s46,384\nds_read_b64_tr_b16 a[48:49],v9 offset:24576\n" \
"C v82,v84,v85\nM a[248:251],a[72:75],a[88:91],a[248:251]\nds_read_b64_tr_b16 a[50:51],v10 offset:24576\nds_read_b64_tr_b16 a[52:53],v11 offset:24576\nM a[216:219],a[72:75],a[92:95],a[216:219]\nbuffer_load_dwordx4 v17,s[28:31],s46 offen lds\nM a[220:223],a[76:79],a[92:95],a[220:223]\ns_add_u32 m0,s50,53248\n" \
"v_mul_f32 v88,s20,v88\nv_mul_f32 v89,s20,v89\nM a[252:255],a[76:79],a[88:91],a[252:255]\nds_read_b64_tr_b16 a[54:55],v12 offset:24576\nds_read_b64_tr_b16 a[56:57],v9 offset:25088\nM v[188:191],a[76:79],a[84:87],v[188:191]\ns_waitcnt lgkmcnt(5)\nds_read_b128 a[84:87],v8 offset:4096\n" \
"v_mul_f32 v90,s20,v90\nM v[156:159],a[76:79],a[80:83],v[156:159]\nbuffer_load_dwordx4 v18,s[28:31],s46 offen lds\nM v[128:131],a[0:3],a[32:35],v[128:131]\ns_add_u32 m0,s50,57344\nv_mul_f32 v91,s20,v91\nC v115,v118,v119\nM v[160:163],a[0:3],a[36:39],v[160:163]\n" \
"ds_read_b128 a[80:83],v8 offset:0\nv_mul_f32 v124,s20,v124\nM a[224:227],a[0:3],a[40:43],a[224:227]\nds_read_b64_tr_b16 a[58:59],v10 offset:25088\nds_read_b64_tr_b16 a[60:61],v11 offset:25088\nM a[192:195],a[0:3],a[44:47],a[192:195]\nbuffer_load_dwordx4 v19,s[28:31],s46 offen lds\nM a[196:199],a[4:7],a[44:47],a[196:199]\n" \
"s_add_u32 m0,s50,61440\nC v83,v86,v87\nv_mul_f32 v125,s20,v125\nM a[228:231],a[4:7],a[40:43],a[228:231]\nds_read_b64_tr_b16 a[62:63],v12 offset:25088\nds_read_b64_tr_b16 a[64:65],v9 offset:25600\nM v[164:167],a[4:7],a[36:39],v[164:167]\nds_read_b64_tr_b16 a[66:67],v10 offset:25600\n" \
"v_mul_f32 v92,s20,v92\nM v[132:135],a[4:7],a[32:35],v[132:135]\nLG8_%=:\ns_cbranch_execz LG12_%=\nbuffer_load_dwordx4 v20,s[28:31],s46 offen lds\nM v[136:139],a[8:11],a[32:35],v[136:139]\ns_add_u32 m0,s51,0\nds_read_b64_tr_b16 a[68:69],v11 offset:25600\n" \
"v_mul_f32 v93,s20,v93\nM v[168:171],a[8:11],a[36:39],v[168:171]\ns_mov_b32 s46,512\nds_read_b64_tr_b16 a[70:71],v12 offset:25600\nC v84,v88,v89\nM a[232:235],a[8:11],a[40:43],a[232:235]\nds_read_b64_tr_b16 a[72:73],v9 offset:26112\nC v85,v90,v91\n" \
"M a[200:203],a[8:11],a[44:47],a[200:203]\nbuffer_load_dwordx4 v21,s[32:35],s47 offen offset:0 lds\nM a[204:207],a[12:15],a[44:47],a[204:207]\ns_add_u32 m0,s51,896\nds_read_b64_tr_b16 a[74:75],v10 offset:26112\nv_mul_f32 v94,s20,v94\nM a[236:239],a[12:15],a[40:43],a[236:239]\nds_read_b64_tr_b16 a[76:77],v11 offset:26112\n" \
"v_mul_f32 v95,s20,v95\nM v[172:175],a[12:15],a[36:39],v[172:175]\nds_read_b64_tr_b16 a[78:79],v12 offset:26112\nds_read_b128 a[88:91],v8 offset:8192\nM v[140:143],a[12:15],a[32:35],v[140:143]\nbuffer_load_dwordx4 v21,s[32:35],s47 offen offset:128 lds\nM v[144:147],a[16:19],a[32:35],v[144:147]\ns_add_u32 m0,s51,8192\n" \
"C v118,v124,v125\nv_mul_f32 v96,s20,v96\nM v[176:179],a[16:19],a[36:39],v[176:179]\ns_mov_b32 s47,2359296\nds_read_b128 a[92:95],v8 offset:12288\nv_mul_f32 v97,s20,v97\nM a[240:243],a[16:19],a[40:43],a[240:243]\nC v86,v92,v93\n" \
"v_mov_b32 v88,v30\nM a[208:211],a[16:19],a[44:47],a[208:211]\nbuffer_load_dwordx4 v21,s[32:35],s47 offen offset:0 lds\nM a[212:215],a[20:23],a[44:47],a[212:215]\ns_add_u32 m0,s51,9088\nv_mov_b32 v89,v31\nv_mov_b32 v90,v32\nM a[244:247],a[20:23],a[40:43],a[244:247]\n" \
"v_mov_b32 v91,v33\nC v87,v94,v95\nM v[180:183],a[20:23],a[36:39],v[180:183]\nv_mov_b32 v124,v50\nv_mul_f32 v98,s20,v98\nM v[148:151],a[20:23],a[32:35],v[148:151]\nbuffer_load_dwordx4 v21,s[32:35],s47 offen offset:128 lds\nM v[152:155],a[24:27],a[32:35],v[152:155]\n" \
"s_waitcnt vmcnt(17) lgkmcnt(0)\nv_mul_f32 v99,s20,v99\nC v96,v96,v97\nM v[184:187],a[24:27],a[36:39],v[184:187]\ns_add_u32 m0,s50,65536\ns_barrier\nds_read_b64_tr_b16 a[0:1],v9 offset:32768\nM a[248:251],a[24:27],a[40:43],a[248:251]\n" \
"s_mov_b32 s47,2621440\nds_read_b64_tr_b16 a[2:3],v10 offset:32768\nds_read_b64_tr_b16 a[4:5],v11 offset:32768\nM a[216:219],a[24:27],a[44:47],a[216:219]\nbuffer_load_dwordx4 v13,s[28:31],s46 offen lds\nM a[220:223],a[28:31],a[44:47],a[220:223]\ns_add_u32 m0,s50,69632\nds_read_b128 a[44:47],v5 offset:12288\n" \
"v_mov_b32 v92,v34\nM a[252:255],a[28:31],a[40:43],a[252:255]\nds_read_b128 a[40:43],v5 offset:8192\nv_mov_b32 v93,v35\nM v[188:191],a[28:31],a[36:39],v[188:191]\nds_read_b128 a[36:39],v5 offset:4096\nv_mov_b32 v94,v36\nM v[156:159],a[28:31],a[32:35],v[156:159]\n" \
"buffer_load_dwordx4 v14,s[28:31],s46 offen lds\nM v[128:131],a[48:51],a[80:83],v[128:131]\ns_add_u32 m0,s50,73728\nv_mov_b32 v95,v37\nC v97,v98,v99\nM v[160:163],a[48:51],a[84:87],v[160:163]\nds_read_b128 a[32:35],v5 offset:0\nv_mul_f32 v100,s20,v100\n" \
"M a[224:227],a[48:51],a[88:91],a[224:227]\nds_read_b64_tr_b16 a[6:7],v12 offset:32768\nv_mul_f32 v101,s20,v101\nM a[192:195],a[48:51],a[92:95],a[192:195]\nbuffer_load_dwordx4 v15,s[28:31],s46 offen lds\nM a[196:199],a[52:55],a[92:95],a[196:199]\ns_add_u32 m0,s50,77824\nds_read_b64_tr_b16 a[8:9],v9 offset:33280\n" \
"v_mov_b32 v125,v51\nM a[228:231],a[52:55],a[88:91],a[228:231]\nds_read_b64_tr_b16 a[10:11],v10 offset:33280\nC v98,v100,v101\nM v[164:167],a[52:55],a[84:87],v[164:167]\nds_read_b64_tr_b16 a[12:13],v11 offset:33280\nds_read_b64_tr_b16 a[14:15],v12 offset:33280\nM v[132:135],a[52:55],a[80:83],v[132:135]\n" \
"buffer_load_dwordx4 v16,s[28:31],s46 offen lds\nM v[136:139],a[56:59],a[80:83],v[136:139]\ns_mov_b32 s46,512\nv_mul_f32 v104,s20,v104\nv_mul_f32 v105,s20,v105\nM v[168:171],a[56:59],a[84:87],v[168:171]\ns_add_u32 m0,s50,81920\nds_read_b64_tr_b16 a[16:17],v9 offset:33792\n" \
"ds_read_b64_tr_b16 a[18:19],v10 offset:33792\nM a[232:235],a[56:59],a[88:91],a[232:235]\nds_read_b64_tr_b16 a[20:21],v11 offset:33792\nv_mul_f32 v106,s20,v106\nM a[200:203],a[56:59],a[92:95],a[200:203]\nbuffer_load_dwordx4 v17,s[28:31],s46 offen lds\nM a[204:207],a[60:63],a[92:95],a[204:207]\ns_add_u32 m0,s50,86016\n" \
"ds_read_b64_tr_b16 a[22:23],v12 offset:33792\nv_mul_f32 v107,s20,v107\nM a[236:239],a[60:63],a[88:91],a[236:239]\nds_read_b64_tr_b16 a[24:25],v9 offset:34304\nC v100,v104,v105\nM v[172:175],a[60:63],a[84:87],v[172:175]\nds_read_b64_tr_b16 a[26:27],v10 offset:34304\nC v101,v106,v107\n" \
"M v[140:143],a[60:63],a[80:83],v[140:143]\nbuffer_load_dwordx4 v18,s[28:31],s46 offen lds\nM v[144:147],a[64:67],a[80:83],v[144:147]\ns_add_u32 m0,s50,90112\nds_read_b64_tr_b16 a[28:29],v11 offset:34304\nv_mov_b32 v104,v38\nM v[176:179],a[64:67],a[84:87],v[176:179]\nds_read_b64_tr_b16 a[30:31],v12 offset:34304\n" \
"v_mov_b32 v105,v39\nM a[240:243],a[64:67],a[88:91],a[240:243]\nds_read_b64_tr_b16 a[48:49],v9 offset:40960\nds_read_b64_tr_b16 a[50:51],v10 offset:40960\nM a[208:211],a[64:67],a[92:95],a[208:211]\nbuffer_load_dwordx4 v19,s[28:31],s46 offen lds\nM a[212:215],a[68:71],a[92:95],a[212:215]\ns_add_u32 m0,s50,94208\n" \
"v_mov_b32 v106,v40\nv_mov_b32 v107,v41\nM a[244:247],a[68:71],a[88:91],a[244:247]\nds_read_b64_tr_b16 a[52:53],v11 offset:40960\nds_read_b64_tr_b16 a[54:55],v12 offset:40960\nM v[180:183],a[68:71],a[84:87],v[180:183]\nds_read_b64_tr_b16 a[56:57],v9 offset:41472\nv_mul_f32 v126,s20,v126\n" \
"M v[148:151],a[68:71],a[80:83],v[148:151]\nbuffer_load_dwordx4 v20,s[28:31],s46 offen lds\nM v[152:155],a[72:75],a[80:83],v[152:155]\ns_add_u32 m0,s51,16384\nds_read_b64_tr_b16 a[58:59],v10 offset:41472\nv_mul_f32 v127,s20,v127\nM v[184:187],a[72:75],a[84:87],v[184:187]\ns_mov_b32 s46,640\n" \
"ds_read_b64_tr_b16 a[60:61],v11 offset:41472\nC v119,v126,v127\nM a[248:251],a[72:75],a[88:91],a[248:251]\nds_read_b64_tr_b16 a[62:63],v12 offset:41472\nv_mul_f32 v102,s20,v102\nM a[216:219],a[72:75],a[92:95],a[216:219]\nbuffer_load_dwordx4 v21,s[32:35],s47 offen offset:0 lds\nM a[220:223],a[76:79],a[92:95],a[220:223]\n" \
"s_add_u32 m0,s51,17280\nds_read_b64_tr_b16 a[64:65],v9 offset:41984\nv_mul_f32 v103,s20,v103\nM a[252:255],a[76:79],a[88:91],a[252:255]\nds_read_b64_tr_b16 a[66:67],v10 offset:41984\nv_mov_b32 v126,v52\nM v[188:191],a[76:79],a[84:87],v[188:191]\ns_waitcnt lgkmcnt(4)\n" \
"ds_read_b128 a[84:87],v6 offset:4096\nC v99,v102,v103\nM v[156:159],a[76:79],a[80:83],v[156:159]\nbuffer_load_dwordx4 v21,s[32:35],s47 offen offset:128 lds\nM v[128:131],a[0:3],a[32:35],v[128:131]\ns_add_u32 m0,s51,24576\nds_read_b128 a[80:83],v6 offset:0\nv_mul_f32 v108,s20,v108\n" \
"M v[160:163],a[0:3],a[36:39],v[160:163]\ns_mov_b32 s47,2883584\nds_read_b64_tr_b16 a[68:69],v11 offset:41984\nv_mul_f32 v109,s20,v109\nM a[224:227],a[0:3],a[40:43],a[224:227]\nds_read_b64_tr_b16 a[70:71],v12 offset:41984\nv_mul_f32 v110,s20,v110\nM a[192:195],a[0:3],a[44:47],a[192:195]\n" \
"buffer_load_dwordx4 v21,s[32:35],s47 offen offset:0 lds\nM a[196:199],a[4:7],a[44:47],a[196:199]\ns_add_u32 m0,s51,25472\nds_read_b64_tr_b16 a[72:73],v9 offset:42496\nv_mul_f32 v111,s20,v111\nM a[228:231],a[4:7],a[40:43],a[228:231]\nds_read_b64_tr_b16 a[74:75],v10 offset:42496\nC v102,v108,v109\n" \
"M v[164:167],a[4:7],a[36:39],v[164:167]\nds_read_b64_tr_b16 a[76:77],v11 offset:42496\nds_read_b64_tr_b16 a[78:79],v12 offset:42496\nM v[132:135],a[4:7],a[32:35],v[132:135]\nbuffer_load_dwordx4 v21,s[32:35],s47 offen offset:128 lds\nM v[136:139],a[8:11],a[32:35],v[136:139]\ns_add_u32 m0,s50,0\nC v103,v110,v111\n" \
"v_mov_b32 v108,v42\nM v[168:171],a[8:11],a[36:39],v[168:171]\ns_mov_b32 s47,3145728\nds_read_b128 a[88:91],v6 offset:8192\nv_mov_b32 v109,v43\nM a[232:235],a[8:11],a[40:43],a[232:235]\nds_read_b128 a[92:95],v6 offset:12288\nv_mov_b32 v110,v44\n" \
"M a[200:203],a[8:11],a[44:47],a[200:203]\nbuffer_load_dwordx4 v[30:33],v23,s[68:71],0 offen offset:0\nM a[204:207],a[12:15],a[44:47],a[204:207]\nv_mov_b32 v111,v45\nv_mul_f32 v116,s20,v116\nM a[236:239],a[12:15],a[40:43],a[236:239]\nv_mul_f32 v117,s20,v117\nC v114,v116,v117\n" \
"M v[172:175],a[12:15],a[36:39],v[172:175]\nv_mov_b32 v127,v53\nv_mul_f32 v120,s20,v120\nM v[140:143],a[12:15],a[32:35],v[140:143]\nv_mul_f32 v121,s20,v121\nv_mul_f32 v122,s20,v122\nM v[144:147],a[16:19],a[32:35],v[144:147]\nv_mul_f32 v123,s20,v123\n" \
"C v116,v120,v121\nM v[176:179],a[16:19],a[36:39],v[176:179]\nbuffer_load_dword v50,v25,s[68:71],0 offen offset:0\nM a[240:243],a[16:19],a[40:43],a[240:243]\nC v117,v122,v123\nv_mov_b32 v120,v46\nM a[208:211],a[16:19],a[44:47],a[208:211]\nv_mov_b32 v121,v47\n" \
"v_mov_b32 v122,v48\nM a[212:215],a[20:23],a[44:47],a[212:215]\nv_mov_b32 v123,v49\nv_mul_f32 v192,s20,v192\nM a[244:247],a[20:23],a[40:43],a[244:247]\nv_mul_f32 v193,s20,v193\nv_mul_f32 v194,s20,v194\nM v[180:183],a[20:23],a[36:39],v[180:183]\n" \
"v_mul_f32 v195,s20,v195\nC v192,v192,v193\nM v[148:151],a[20:23],a[32:35],v[148:151]\nbuffer_load_dwordx4 v[34:37],v24,s[68:71],0 offen offset:0\nM v[152:155],a[24:27],a[32:35],v[152:155]\ns_waitcnt vmcnt(19) lgkmcnt(0)\nC v193,v194,v195\nv_mul_f32 v196,s20,v196\n" \
"M v[184:187],a[24:27],a[36:39],v[184:187]\ns_barrier\nds_read_b64_tr_b16 a[0:1],v9 offset:49152\nM a[248:251],a[24:27],a[40:43],a[248:251]\nds_read_b64_tr_b16 a[2:3],v10 offset:49152\nds_read_b64_tr_b16 a[4:5],v11 offset:49152\nM a[216:219],a[24:27],a[44:47],a[216:219]\nds_read_b64_tr_b16 a[6:7],v12 offset:49152\n" \
"v_mul_f32 v197,s20,v197\nM a[220:223],a[28:31],a[44:47],a[220:223]\nds_read_b128 a[44:47],v5 offset:45056\nv_mul_f32 v198,s20,v198\nM a[252:255],a[28:31],a[40:43],a[252:255]\nbuffer_store_dwordx4 v[64:67],v4,s[36:39],s96 offen offset:0 sc1\nM v[188:191],a[28:31],a[36:39],v[188:191]\nds_read_b128 a[36:39],v5 offset:36864\n" \
"ds_read_b128 a[40:43],v5 offset:40960\nM v[156:159],a[28:31],a[32:35],v[156:159]\nds_read_b128 a[32:35],v5 offset:32768\nv_mul_f32 v199,s20,v199\nM v[128:131],a[48:51],a[80:83],v[128:131]\nds_read_b64_tr_b16 a[8:9],v9 offset:49664\nC v194,v196,v197\nM v[160:163],a[48:51],a[84:87],v[160:163]\n" \
"buffer_load_dwordx2 v[58:59],v57,s[36:39],0 offen sc1\nM a[224:227],a[48:51],a[88:91],a[224:227]\nds_read_b64_tr_b16 a[10:11],v10 offset:49664\nds_read_b64_tr_b16 a[12:13],v11 offset:49664\nM a[192:195],a[48:51],a[92:95],a[192:195]\nbuffer_load_dword v51,v24,s[68:71],0 offen offset:16\nM a[196:199],a[52:55],a[92:95],a[196:199]\nds_read_b64_tr_b16 a[14:15],v12 offset:49664\n" \
"ds_read_b64_tr_b16 a[16:17],v9 offset:50176\nM a[228:231],a[52:55],a[88:91],a[228:231]\nds_read_b64_tr_b16 a[18:19],v10 offset:50176\nv_mul_f32 v200,s20,v200\nM v[164:167],a[52:55],a[84:87],v[164:167]\nds_read_b64_tr_b16 a[20:21],v11 offset:50176\nds_read_b64_tr_b16 a[22:23],v12 offset:50176\nM v[132:135],a[52:55],a[80:83],v[132:135]\n" \
"ds_read_b64_tr_b16 a[24:25],v9 offset:50688\nv_mul_f32 v201,s20,v201\nM v[136:139],a[56:59],a[80:83],v[136:139]\nds_read_b64_tr_b16 a[26:27],v10 offset:50688\nv_mul_f32 v202,s20,v202\nM v[168:171],a[56:59],a[84:87],v[168:171]\nbuffer_load_dwordx4 v[38:41],v24,s[68:71],0 offen offset:1024\nM a[232:235],a[56:59],a[88:91],a[232:235]\n" \
"ds_read_b64_tr_b16 a[28:29],v11 offset:50688\nds_read_b64_tr_b16 a[30:31],v12 offset:50688\nM a[200:203],a[56:59],a[92:95],a[200:203]\nds_read_b64_tr_b16 a[48:49],v9 offset:57344\nv_mul_f32 v203,s20,v203\nM a[204:207],a[60:63],a[92:95],a[204:207]\nds_read_b64_tr_b16 a[50:51],v10 offset:57344\nds_read_b64_tr_b16 a[52:53],v11 offset:57344\n" \
"M a[236:239],a[60:63],a[88:91],a[236:239]\nds_read_b64_tr_b16 a[54:55],v12 offset:57344\nC v195,v198,v199\nM v[172:175],a[60:63],a[84:87],v[172:175]\nds_read_b64_tr_b16 a[56:57],v9 offset:57856\nv_mul_f32 v204,s20,v204\nM v[140:143],a[60:63],a[80:83],v[140:143]\nbuffer_load_dwordx4 v13,s[28:31],s46 offen lds\n" \
"M v[144:147],a[64:67],a[80:83],v[144:147]\ns_add_u32 m0,s50,4096\nds_read_b64_tr_b16 a[58:59],v10 offset:57856\nv_mul_f32 v205,s20,v205\nM v[176:179],a[64:67],a[84:87],v[176:179]\nds_read_b64_tr_b16 a[60:61],v11 offset:57856\nC v196,v200,v201\nM a[240:243],a[64:67],a[88:91],a[240:243]\n" \
"ds_read_b64_tr_b16 a[62:63],v12 offset:57856\nv_mul_f32 v208,s20,v208\nM a[208:211],a[64:67],a[92:95],a[208:211]\nbuffer_load_dwordx4 v14,s[28:31],s46 offen lds\nM a[212:215],a[68:71],a[92:95],a[212:215]\ns_add_u32 m0,s50,8192\nds_read_b64_tr_b16 a[64:65],v9 offset:58368\nv_mul_f32 v209,s20,v209\n" \
"M a[244:247],a[68:71],a[88:91],a[244:247]\nds_read_b64_tr_b16 a[66:67],v10 offset:58368\nv_mul_f32 v210,s20,v210\nM v[180:183],a[68:71],a[84:87],v[180:183]\nC v197,v202,v203\nv_mul_f32 v211,s20,v211\nM v[148:151],a[68:71],a[80:83],v[148:151]\nbuffer_load_dwordx4 v15,s[28:31],s46 offen lds\n" \
"M v[152:155],a[72:75],a[80:83],v[152:155]\ns_add_u32 m0,s50,12288\nds_read_b64_tr_b16 a[68:69],v11 offset:58368\nv_mul_f32 v212,s20,v212\nM v[184:187],a[72:75],a[84:87],v[184:187]\nds_read_b64_tr_b16 a[70:71],v12 offset:58368\nv_mul_f32 v213,s20,v213\nM a[248:251],a[72:75],a[88:91],a[248:251]\n" \
"v_mul_f32 v206,s20,v206\nv_mul_f32 v207,s20,v207\nM a[216:219],a[72:75],a[92:95],a[216:219]\nbuffer_load_dwordx4 v16,s[28:31],s46 offen lds\nM a[220:223],a[76:79],a[92:95],a[220:223]\ns_mov_b32 s46,640\nds_read_b64_tr_b16 a[72:73],v9 offset:58880\nv_mul_f32 v214,s20,v214\n" \
"M a[252:255],a[76:79],a[88:91],a[252:255]\ns_add_u32 m0,s50,16384\nds_read_b64_tr_b16 a[74:75],v10 offset:58880\nds_read_b128 a[88:91],v6 offset:40960\nM v[188:191],a[76:79],a[84:87],v[188:191]\ns_waitcnt lgkmcnt(4)\nds_read_b128 a[84:87],v6 offset:36864\nv_mul_f32 v215,s20,v215\n" \
"M v[156:159],a[76:79],a[80:83],v[156:159]\nbuffer_load_dword v52,v24,s[68:71],0 offen offset:1040\nM v[128:131],a[0:3],a[32:35],v[128:131]\nds_read_b128 a[80:83],v6 offset:32768\nds_read_b64_tr_b16 a[76:77],v11 offset:58880\nM v[160:163],a[0:3],a[36:39],v[160:163]\nds_read_b64_tr_b16 a[78:79],v12 offset:58880\nC v198,v204,v205\n" \
"M a[224:227],a[0:3],a[40:43],a[224:227]\nds_read_b128 a[92:95],v6 offset:45056\nv_mul_f32 v216,s20,v216\nM a[192:195],a[0:3],a[44:47],a[192:195]\nv_mul_f32 v217,s20,v217\nC v200,v208,v209\nM a[196:199],a[4:7],a[44:47],a[196:199]\nv_mul_f32 v224,s20,v224\n" \
"v_mul_f32 v225,s20,v225\nM a[228:231],a[4:7],a[40:43],a[228:231]\nbuffer_load_dwordx4 v17,s[28:31],s46 offen lds\nM v[164:167],a[4:7],a[36:39],v[164:167]\ns_add_u32 m0,s50,20480\nv_mul_f32 v226,s20,v226\nv_mul_f32 v218,s20,v218\nM v[132:135],a[4:7],a[32:35],v[132:135]\n" \
"v_mul_f32 v227,s20,v227\nC v201,v210,v211\nM v[136:139],a[8:11],a[32:35],v[136:139]\nv_mul_f32 v219,s20,v219\nv_mul_f32 v228,s20,v228\nM v[168:171],a[8:11],a[36:39],v[168:171]\nbuffer_load_dwordx4 v18,s[28:31],s46 offen lds\nM a[232:235],a[8:11],a[40:43],a[232:235]\n" \
"s_add_u32 m0,s50,24576\nv_mul_f32 v229,s20,v229\nv_mul_f32 v230,s20,v230\nM a[200:203],a[8:11],a[44:47],a[200:203]\nv_mul_f32 v231,s20,v231\nC v202,v212,v213\nM a[204:207],a[12:15],a[44:47],a[204:207]\nv_mul_f32 v232,s20,v232\n" \
"v_mul_f32 v233,s20,v233\nM a[236:239],a[12:15],a[40:43],a[236:239]\nbuffer_load_dwordx4 v[42:45],v24,s[68:71],0 offen offset:2048\nM v[172:175],a[12:15],a[36:39],v[172:175]\nv_mul_f32 v234,s20,v234\nv_mul_f32 v235,s20,v235\nM v[140:143],a[12:15],a[32:35],v[140:143]\nC v199,v206,v207\n" \
"C v203,v214,v215\nM v[144:147],a[16:19],a[32:35],v[144:147]\nv_mul_f32 v220,s20,v220\nv_mul_f32 v221,s20,v221\nM v[176:179],a[16:19],a[36:39],v[176:179]\nv_mul_f32 v236,s20,v236\nv_mul_f32 v237,s20,v237\nM a[240:243],a[16:19],a[40:43],a[240:243]\n" \
"v_mul_f32 v238,s20,v238\nv_mul_f32 v239,s20,v239\nM a[208:211],a[16:19],a[44:47],a[208:211]\nbuffer_store_dwordx4 v[68:71],v4,s[36:39],s96 offen offset:1024 sc1\nM a[212:215],a[20:23],a[44:47],a[212:215]\nC v204,v216,v217\nv_mul_f32 v240,s20,v240\nM a[244:247],a[20:23],a[40:43],a[244:247]\n" \
"v_mul_f32 v241,s20,v241\nC v208,v224,v225\nM v[180:183],a[20:23],a[36:39],v[180:183]\nC v209,v226,v227\nC v205,v218,v219\nM v[148:151],a[20:23],a[32:35],v[148:151]\nbuffer_load_dword v53,v24,s[68:71],0 offen offset:2064\nM v[152:155],a[24:27],a[32:35],v[152:155]\n" \
"s_waitcnt vmcnt(21) lgkmcnt(0)\nC v210,v228,v229\nC v211,v230,v231\nM v[184:187],a[24:27],a[36:39],v[184:187]\ns_barrier\nds_read_b64_tr_b16 a[0:1],v9 offset:0\nM a[248:251],a[24:27],a[40:43],a[248:251]\nds_read_b64_tr_b16 a[2:3],v10 offset:0\n" \
"ds_read_b64_tr_b16 a[4:5],v11 offset:0\nM a[216:219],a[24:27],a[44:47],a[216:219]\nds_read_b64_tr_b16 a[6:7],v12 offset:0\nv_mul_f32 v242,s20,v242\nM a[220:223],a[28:31],a[44:47],a[220:223]\nds_read_b128 a[44:47],v7 offset:12288\nv_mul_f32 v243,s20,v243\nM a[252:255],a[28:31],a[40:43],a[252:255]\n" \
"buffer_load_dwordx4 v[46:49],v24,s[68:71],0 offen offset:3072\nM v[188:191],a[28:31],a[36:39],v[188:191]\nds_read_b128 a[36:39],v7 offset:4096\nds_read_b128 a[40:43],v7 offset:8192\nM v[156:159],a[28:31],a[32:35],v[156:159]\nds_read_b128 a[32:35],v7 offset:0\nv_mul_f32 v244,s20,v244\nM v[128:131],a[48:51],a[80:83],v[128:131]\n" \
"ds_read_b64_tr_b16 a[8:9],v9 offset:512\nv_mul_f32 v245,s20,v245\nM v[160:163],a[48:51],a[84:87],v[160:163]\nds_read_b64_tr_b16 a[10:11],v10 offset:512\nv_mul_f32 v222,s20,v222\nM a[224:227],a[48:51],a[88:91],a[224:227]\nds_read_b64_tr_b16 a[12:13],v11 offset:512\nv_mul_f32 v223,s20,v223\n" \
"M a[192:195],a[48:51],a[92:95],a[192:195]\nbuffer_load_dword v54,v24,s[68:71],0 offen offset:3088\nM a[196:199],a[52:55],a[92:95],a[196:199]\nds_read_b64_tr_b16 a[14:15],v12 offset:512\nds_read_b64_tr_b16 a[16:17],v9 offset:1024\nM a[228:231],a[52:55],a[88:91],a[228:231]\nds_read_b64_tr_b16 a[18:19],v10 offset:1024\nC v212,v232,v233\n" \
"M v[164:167],a[52:55],a[84:87],v[164:167]\nds_read_b64_tr_b16 a[20:21],v11 offset:1024\nds_read_b64_tr_b16 a[22:23],v12 offset:1024\nM v[132:135],a[52:55],a[80:83],v[132:135]\nds_read_b64_tr_b16 a[24:25],v9 offset:1536\nC v213,v234,v235\nM v[136:139],a[56:59],a[80:83],v[136:139]\nds_read_b64_tr_b16 a[26:27],v10 offset:1536\n" \
"v_mul_f32 v246,s20,v246\nM v[168:171],a[56:59],a[84:87],v[168:171]\nglobal_load_short_d16_hi v28,v29,s[12:13]\nM a[232:235],a[56:59],a[88:91],a[232:235]\nds_read_b64_tr_b16 a[28:29],v11 offset:1536\nds_read_b64_tr_b16 a[30:31],v12 offset:1536\nM a[200:203],a[56:59],a[92:95],a[200:203]\nds_read_b64_tr_b16 a[48:49],v9 offset:8192\n" \
"v_mul_f32 v247,s20,v247\nM a[204:207],a[60:63],a[92:95],a[204:207]\nds_read_b64_tr_b16 a[50:51],v10 offset:8192\nds_read_b64_tr_b16 a[52:53],v11 offset:8192\nM a[236:239],a[60:63],a[88:91],a[236:239]\nds_read_b64_tr_b16 a[54:55],v12 offset:8192\nC v206,v220,v221\nM v[172:175],a[60:63],a[84:87],v[172:175]\n" \
"ds_read_b64_tr_b16 a[56:57],v9 offset:8704\nC v214,v236,v237\nM v[140:143],a[60:63],a[80:83],v[140:143]\nbuffer_load_dwordx4 v19,s[28:31],s46 offen lds\nM v[144:147],a[64:67],a[80:83],v[144:147]\ns_add_u32 m0,s50,28672\nds_read_b64_tr_b16 a[58:59],v10 offset:8704\nC v215,v238,v239\n" \
"M v[176:179],a[64:67],a[84:87],v[176:179]\nds_read_b64_tr_b16 a[60:61],v11 offset:8704\nv_mul_f32 v248,s20,v248\nM a[240:243],a[64:67],a[88:91],a[240:243]\nds_read_b64_tr_b16 a[62:63],v12 offset:8704\nv_mul_f32 v249,s20,v249\nM a[208:211],a[64:67],a[92:95],a[208:211]\nbuffer_store_dwordx4 v[80:83],v4,s[36:39],s96 offen offset:2048 sc1\n" \
"M a[212:215],a[68:71],a[92:95],a[212:215]\nds_read_b64_tr_b16 a[64:65],v9 offset:9216\nds_read_b64_tr_b16 a[66:67],v10 offset:9216\nM a[244:247],a[68:71],a[88:91],a[244:247]\nv_mul_f32 v250,s20,v250\nv_mul_f32 v251,s20,v251\nM v[180:183],a[68:71],a[84:87],v[180:183]\nC v216,v240,v241\n" \
"C v217,v242,v243\nM v[148:151],a[68:71],a[80:83],v[148:151]\nbuffer_load_dwordx4 v20,s[28:31],s46 offen lds\nM v[152:155],a[72:75],a[80:83],v[152:155]\ns_add_u32 m0,s56,0\nds_read_b64_tr_b16 a[68:69],v11 offset:9216\nC v218,v244,v245\nM v[184:187],a[72:75],a[84:87],v[184:187]\n" \
"s_mov_b32 s46,768\nds_read_b64_tr_b16 a[70:71],v12 offset:9216\nC v207,v222,v223\nM a[248:251],a[72:75],a[88:91],a[248:251]\nC v219,v246,v247\nC v220,v248,v249\nM a[216:219],a[72:75],a[92:95],a[216:219]\nbuffer_load_dwordx4 v23,s[68:71],0 offen offset:0 lds\n" \
"M a[220:223],a[76:79],a[92:95],a[220:223]\ns_add_u32 m0,s56,1024\nds_read_b64_tr_b16 a[72:73],v9 offset:9728\nC v221,v250,v251\nM a[252:255],a[76:79],a[88:91],a[252:255]\nds_read_b64_tr_b16 a[74:75],v10 offset:9728\nds_read_b128 a[88:91],v8 offset:8192\nM v[188:191],a[76:79],a[84:87],v[188:191]\n" \
"s_waitcnt lgkmcnt(4)\nds_read_b128 a[84:87],v8 offset:4096\nv_mul_f32 v252,s20,v252\nM v[156:159],a[76:79],a[80:83],v[156:159]\ns_waitcnt vmcnt(19)\nbuffer_store_dwordx4 v[84:87],v4,s[36:39],s96 offen offset:3072 sc1\nM v[128:131],a[0:3],a[32:35],v[128:131]\ns_add_u32 s96,s57,36864\n" \
"v_cmp_gt_u64 s[98:99],s[14:15],v[58:59]\nv_cmp_le_u64 s[18:19],s[16:17],v[58:59]\nM v[160:163],a[0:3],a[36:39],v[160:163]\ns_or_b64 s[98:99],s[98:99],s[18:19]\ns_cbranch_scc1 LPU_%=\nLPUR_%=:\nds_read_b128 a[80:83],v8 offset:0\nds_read_b64_tr_b16 a[76:77],v11 offset:9728\n" \
"M a[224:227],a[0:3],a[40:43],a[224:227]\nds_read_b64_tr_b16 a[78:79],v12 offset:9728\nv_mul_f32 v253,s20,v253\nM a[192:195],a[0:3],a[44:47],a[192:195]\nbuffer_load_dwordx4 v24,s[68:71],0 offen offset:0 lds\nM a[196:199],a[4:7],a[44:47],a[196:199]\ns_add_u32 m0,s56,1024\nds_read_b128 a[92:95],v8 offset:12288\n" \
"v_mul_f32 v254,s20,v254\nM a[228:231],a[4:7],a[40:43],a[228:231]\nv_mul_f32 v255,s20,v255\nC v222,v252,v253\nM v[164:167],a[4:7],a[36:39],v[164:167]\nLG9_%=:\ns_cbranch_execz LG13_%=\nC v223,v254,v255\n" \
"M v[132:135],a[4:7],a[32:35],v[132:135]\nbuffer_load_dwordx4 v24,s[68:71],0 offen offset:1024 lds\nM v[136:139],a[8:11],a[32:35],v[136:139]\ns_add_u32 m0,s56,1024\nM v[168:171],a[8:11],a[36:39],v[168:171]\nM a[232:235],a[8:11],a[40:43],a[232:235]\nM a[200:203],a[8:11],a[44:47],a[200:203]\nbuffer_load_dwordx4 v24,s[68:71],0 offen offset:2048 lds\n" \
"M a[204:207],a[12:15],a[44:47],a[204:207]\ns_add_u32 m0,s56,1024\nM a[236:239],a[12:15],a[40:43],a[236:239]\nM v[172:175],a[12:15],a[36:39],v[172:175]\nM v[140:143],a[12:15],a[32:35],v[140:143]\nbuffer_load_dwordx4 v24,s[68:71],0 offen offset:3072 lds\nM v[144:147],a[16:19],a[32:35],v[144:147]\ns_add_u32 m0,s56,10304\n" \
"M v[176:179],a[16:19],a[36:39],v[176:179]\nM a[240:243],a[16:19],a[40:43],a[240:243]\nM a[208:211],a[16:19],a[44:47],a[208:211]\ns_mov_b64 exec,0xff\nglobal_load_dword v0,v2,s[100:101]\ns_mov_b64 exec,-1\nM a[212:215],a[20:23],a[44:47],a[212:215]\nM a[244:247],a[20:23],a[40:43],a[244:247]\n" \
"buffer_load_dwordx4 v26,s[72:75],0 offen offset:0 sc1 lds\nM v[180:183],a[20:23],a[36:39],v[180:183]\ns_add_u32 m0,s56,11328\nM v[148:151],a[20:23],a[32:35],v[148:151]\nM v[152:155],a[24:27],a[32:35],v[152:155]\ns_waitcnt vmcnt(8) lgkmcnt(0)\nM v[184:187],a[24:27],a[36:39],v[184:187]\ns_barrier\n" \
"buffer_store_dwordx4 v[96:99],v4,s[36:39],s96 offen offset:0 sc1\nM a[248:251],a[24:27],a[40:43],a[248:251]\nds_read_b64_tr_b16 a[0:1],v9 offset:16384\nds_read_b64_tr_b16 a[2:3],v10 offset:16384\nM a[216:219],a[24:27],a[44:47],a[216:219]\nds_read_b64_tr_b16 a[4:5],v11 offset:16384\nM a[220:223],a[28:31],a[44:47],a[220:223]\nds_read_b128 a[44:47],v5 offset:12288\n" \
"M a[252:255],a[28:31],a[40:43],a[252:255]\nbuffer_load_dwordx4 v27,s[72:75],0 offen offset:0 sc1 lds\nM v[188:191],a[28:31],a[36:39],v[188:191]\ns_add_u32 m0,s56,11328\nds_read_b128 a[36:39],v5 offset:4096\nM v[156:159],a[28:31],a[32:35],v[156:159]\nds_read_b128 a[32:35],v5 offset:0\nM v[128:131],a[48:51],a[80:83],v[128:131]\n" \
"ds_read_b128 a[40:43],v5 offset:8192\nM v[160:163],a[48:51],a[84:87],v[160:163]\nbuffer_load_dwordx4 v27,s[72:75],0 offen offset:1024 sc1 lds\nM a[224:227],a[48:51],a[88:91],a[224:227]\ns_add_u32 m0,s56,11328\nM a[192:195],a[48:51],a[92:95],a[192:195]\nds_read_b64_tr_b16 a[6:7],v12 offset:16384\nds_read_b64_tr_b16 a[8:9],v9 offset:16896\n" \
"M a[196:199],a[52:55],a[92:95],a[196:199]\nds_read_b64_tr_b16 a[10:11],v10 offset:16896\nM a[228:231],a[52:55],a[88:91],a[228:231]\nbuffer_load_dwordx4 v27,s[72:75],0 offen offset:2048 sc1 lds\nM v[164:167],a[52:55],a[84:87],v[164:167]\ns_add_u32 m0,s56,11328\nds_read_b64_tr_b16 a[12:13],v11 offset:16896\nM v[132:135],a[52:55],a[80:83],v[132:135]\n" \
"ds_read_b64_tr_b16 a[14:15],v12 offset:16896\nM v[136:139],a[56:59],a[80:83],v[136:139]\nds_read_b64_tr_b16 a[16:17],v9 offset:17408\nM v[168:171],a[56:59],a[84:87],v[168:171]\nbuffer_load_dwordx4 v27,s[72:75],0 offen offset:3072 sc1 lds\nM a[232:235],a[56:59],a[88:91],a[232:235]\ns_add_u32 m0,s51,0\nds_read_b64_tr_b16 a[18:19],v10 offset:17408\n" \
"M a[200:203],a[56:59],a[92:95],a[200:203]\ns_mov_b32 s74,0\nds_read_b64_tr_b16 a[20:21],v11 offset:17408\nM a[204:207],a[60:63],a[92:95],a[204:207]\nds_read_b64_tr_b16 a[22:23],v12 offset:17408\nds_read_b64_tr_b16 a[24:25],v9 offset:17920\nM a[236:239],a[60:63],a[88:91],a[236:239]\nbuffer_load_dwordx4 v21,s[32:35],s47 offen offset:0 lds\n" \
"M v[172:175],a[60:63],a[84:87],v[172:175]\ns_add_u32 m0,s51,896\nM v[140:143],a[60:63],a[80:83],v[140:143]\nds_read_b64_tr_b16 a[26:27],v10 offset:17920\nds_read_b64_tr_b16 a[28:29],v11 offset:17920\nM v[144:147],a[64:67],a[80:83],v[144:147]\nds_read_b64_tr_b16 a[30:31],v12 offset:17920\nM v[176:179],a[64:67],a[84:87],v[176:179]\n" \
"buffer_load_dwordx4 v21,s[32:35],s47 offen offset:128 lds\nM a[240:243],a[64:67],a[88:91],a[240:243]\ns_add_u32 m0,s51,8192\nds_read_b64_tr_b16 a[48:49],v9 offset:24576\nM a[208:211],a[64:67],a[92:95],a[208:211]\ns_mov_b32 s47,3407872\nds_read_b64_tr_b16 a[50:51],v10 offset:24576\nM a[212:215],a[68:71],a[92:95],a[212:215]\n" \
"ds_read_b64_tr_b16 a[52:53],v11 offset:24576\nM a[244:247],a[68:71],a[88:91],a[244:247]\nbuffer_store_dwordx4 v[100:103],v4,s[36:39],s96 offen offset:1024 sc1\nM v[180:183],a[68:71],a[84:87],v[180:183]\ns_waitcnt vmcnt(19)\nv_alignbyte_b32 v30,v31,v30,2\nv_alignbyte_b32 v31,v32,v31,2\nM v[148:151],a[68:71],a[80:83],v[148:151]\n" \
"v_alignbyte_b32 v32,v33,v32,2\nv_alignbyte_b32 v33,v50,v33,2\nM v[152:155],a[72:75],a[80:83],v[152:155]\nds_write_b128 v55,v[30:33] offset:5184\nM v[184:187],a[72:75],a[84:87],v[184:187]\nv_alignbyte_b32 v34,v35,v34,2\nv_alignbyte_b32 v35,v36,v35,2\nM a[248:251],a[72:75],a[88:91],a[248:251]\n" \
"v_alignbyte_b32 v36,v37,v36,2\nv_alignbyte_b32 v37,v51,v37,2\nM a[216:219],a[72:75],a[92:95],a[216:219]\nv_alignbyte_b32 v46,v47,v46,2\nv_alignbyte_b32 v47,v48,v47,2\nM a[220:223],a[76:79],a[92:95],a[220:223]\nds_write_b128 v55,v[34:37] offset:6208\nM a[252:255],a[76:79],a[88:91],a[252:255]\n" \
"v_lshrrev_b32 v30,4,v4\nv_alignbyte_b32 v48,v49,v48,2\nM v[188:191],a[76:79],a[84:87],v[188:191]\nv_and_b32 v31,15,v30\nv_alignbyte_b32 v49,v54,v49,2\nM v[156:159],a[76:79],a[80:83],v[156:159]\ns_waitcnt lgkmcnt(1)\nv_add_u32 v33,1,v31\n" \
"v_lshrrev_b32 v32,4,v30\nM v[128:131],a[0:3],a[32:35],v[128:131]\nv_and_b32 v34,1,v33\nv_and_b32 v33,30,v33\nM v[160:163],a[0:3],a[36:39],v[160:163]\nv_mul_u32_u24 v34,5184,v34\nv_lshl_add_u32 v33,v33,1,v34\nM a[224:227],a[0:3],a[40:43],a[224:227]\n" \
"ds_write_b128 v55,v[46:49] offset:9280\nM a[192:195],a[0:3],a[44:47],a[192:195]\nv_lshl_add_u32 v33,v32,4,v33\nv_add_u32 v46,s56,v33\nM a[196:199],a[4:7],a[44:47],a[196:199]\nv_lshlrev_b32 v33,1,v31\nv_sub_u32 v33,v33,v32\nM a[228:231],a[4:7],a[40:43],a[228:231]\n" \
"ds_read_b128 a[80:83],v6 offset:0\nv_lshlrev_b32 v33,4,v33\nv_add_u32 v33,10320,v33\nM v[164:167],a[4:7],a[36:39],v[164:167]\nds_read_b128 a[84:87],v6 offset:4096\nv_add_u32 v50,s56,v33\nv_bfe_u32 v33,v4,6,3\nM v[132:135],a[4:7],a[32:35],v[132:135]\n" \
"v_lshlrev_b32 v60,17,v33\nv_and_b32 v33,16,v4\nv_alignbyte_b32 v38,v39,v38,2\nM v[136:139],a[8:11],a[32:35],v[136:139]\nv_lshl_add_u32 v60,v33,2,v60\nv_and_b32 v33,32,v4\nv_alignbyte_b32 v39,v40,v39,2\nM v[168:171],a[8:11],a[36:39],v[168:171]\n" \
"v_add_u32 v60,v60,v33\nv_lshrrev_b32 v33,9,v4\nv_alignbyte_b32 v40,v41,v40,2\nM a[232:235],a[8:11],a[40:43],a[232:235]\nbuffer_load_dwordx4 v21,s[32:35],s47 offen offset:0 lds\nv_alignbyte_b32 v41,v52,v41,2\nM a[200:203],a[8:11],a[44:47],a[200:203]\ns_add_u32 m0,s51,9088\n" \
"v_lshl_add_u32 v60,v33,4,v60\nv_lshl_add_u32 v33,v32,3,v31\nv_alignbyte_b32 v42,v43,v42,2\nM a[204:207],a[12:15],a[44:47],a[204:207]\nv_sub_u32 v33,15,v33\nv_and_b32 v34,16,v4\nv_alignbyte_b32 v43,v44,v43,2\nM a[236:239],a[12:15],a[40:43],a[236:239]\n" \
"v_lshrrev_b32 v33,1,v33\nv_cmp_eq_u32 vcc,0,v33\nv_alignbyte_b32 v44,v45,v44,2\nM v[172:175],a[12:15],a[36:39],v[172:175]\nbuffer_load_dwordx4 v21,s[32:35],s47 offen offset:128 lds\nv_alignbyte_b32 v45,v53,v45,2\nM v[140:143],a[12:15],a[32:35],v[140:143]\ns_add_u32 m0,s50,65536\n" \
"ds_read_b64_tr_b16 a[54:55],v12 offset:24576\nv_lshrrev_b32 v34,v34,v28\nv_cndmask_b32 v52,0,v34,vcc\nM v[144:147],a[16:19],a[32:35],v[144:147]\ns_mov_b32 s47,3670016\nds_read_b64_tr_b16 a[56:57],v9 offset:25088\nds_read_b64_tr_b16 a[58:59],v10 offset:25088\nv_cmp_eq_u32 vcc,1,v33\n" \
"M v[176:179],a[16:19],a[36:39],v[176:179]\nds_read_b64_tr_b16 a[60:61],v11 offset:25088\nv_cndmask_b32 v53,0,v34,vcc\nM a[240:243],a[16:19],a[40:43],a[240:243]\nbuffer_store_dwordx4 v[112:115],v4,s[36:39],s96 offen offset:2048 sc1\nds_read_b64_tr_b16 a[62:63],v12 offset:25088\nds_read_b64_tr_b16 a[64:65],v9 offset:25600\nds_read_b64_tr_b16 a[66:67],v10 offset:25600\n" \
"ds_read_b64_tr_b16 a[68:69],v11 offset:25600\nds_read_b64_tr_b16 a[70:71],v12 offset:25600\nds_read_b64_tr_b16 a[72:73],v9 offset:26112\nds_read_b64_tr_b16 a[74:75],v10 offset:26112\nds_read_b64_tr_b16 a[76:77],v11 offset:26112\nds_read_b64_tr_b16 a[78:79],v12 offset:26112\nds_read_b128 a[88:91],v6 offset:8192\nds_read_b128 a[92:95],v6 offset:12288\n" \
"M a[208:211],a[16:19],a[44:47],a[208:211]\nv_cmp_eq_u32 vcc,2,v33\nv_add_u32 v47,1024,v46\nM a[212:215],a[20:23],a[44:47],a[212:215]\nv_cndmask_b32 v54,0,v34,vcc\nv_cmp_eq_u32 vcc,3,v33\nM a[244:247],a[20:23],a[40:43],a[244:247]\nds_write_b128 v55,v[38:41] offset:7232\n" \
"M v[180:183],a[20:23],a[36:39],v[180:183]\nbuffer_load_dwordx4 v13,s[28:31],s46 offen lds\nM v[148:151],a[20:23],a[32:35],v[148:151]\ns_add_u32 m0,s50,69632\nv_add_u32 v48,2048,v46\nv_add_u32 v49,3072,v46\nM v[152:155],a[24:27],a[32:35],v[152:155]\ns_waitcnt lgkmcnt(1)\n" \
"v_accvgpr_read_b32 v30,a128\nv_accvgpr_read_b32 v31,a129\nM v[184:187],a[24:27],a[36:39],v[184:187]\ns_barrier\nv_accvgpr_read_b32 v32,a130\nM a[248:251],a[24:27],a[40:43],a[248:251]\nbuffer_load_dwordx4 v14,s[28:31],s46 offen lds\nM a[216:219],a[24:27],a[44:47],a[216:219]\n" \
"s_add_u32 m0,s50,73728\nv_accvgpr_read_b32 v33,a131\nv_mul_f32 v30,s20,v30\nM a[220:223],a[28:31],a[44:47],a[220:223]\nds_write_b128 v55,v[42:45] offset:8256\nM a[252:255],a[28:31],a[40:43],a[252:255]\nv_cndmask_b32 v55,0,v34,vcc\nv_mul_f32 v31,s20,v31\n" \
"M v[188:191],a[28:31],a[36:39],v[188:191]\nbuffer_load_dwordx4 v15,s[28:31],s46 offen lds\nM v[156:159],a[28:31],a[32:35],v[156:159]\ns_add_u32 m0,s50,77824\nv_mul_f32 v32,s20,v32\nv_mul_f32 v33,s20,v33\nM v[128:131],a[48:51],a[80:83],v[128:131]\nC v30,v30,v31\n" \
"v_accvgpr_read_b32 v34,a132\nM v[160:163],a[48:51],a[84:87],v[160:163]\nv_accvgpr_read_b32 v35,a133\nv_accvgpr_read_b32 v36,a134\nM a[224:227],a[48:51],a[88:91],a[224:227]\ns_waitcnt vmcnt(9) lgkmcnt(0)\nds_read_b128 a[32:35],v50 offset:512\nds_read_b128 a[36:39],v50 offset:1024\n" \
"M a[192:195],a[48:51],a[92:95],a[192:195]\nbuffer_store_dwordx4 v[116:119],v4,s[36:39],s96 offen offset:3072 sc1\nM a[196:199],a[52:55],a[92:95],a[196:199]\ns_add_u32 s96,s57,0\nds_read_b128 a[40:43],v50 offset:1536\nds_read_b128 a[44:47],v50 offset:2048\nM a[228:231],a[52:55],a[88:91],a[228:231]\nds_read_b128 a[48:51],v50 offset:2560\n" \
"v_accvgpr_read_b32 v37,a135\nM v[164:167],a[52:55],a[84:87],v[164:167]\nds_read2_b32 a[0:1],v46 offset0:0 offset1:1\nC v31,v32,v33\nM v[132:135],a[52:55],a[80:83],v[132:135]\nds_read_b128 a[52:55],v50 offset:3072\nv_mul_f32 v34,s20,v34\nM v[136:139],a[56:59],a[80:83],v[136:139]\n" \
"ds_read2_b32 a[2:3],v46 offset0:2 offset1:3\nv_mul_f32 v35,s20,v35\nM v[168:171],a[56:59],a[84:87],v[168:171]\nds_read2_b32 a[4:5],v46 offset0:128 offset1:129\nv_mul_f32 v36,s20,v36\nM a[232:235],a[56:59],a[88:91],a[232:235]\nds_read2_b32 a[6:7],v46 offset0:130 offset1:131\nds_read2_b32 a[8:9],v47 offset0:0 offset1:1\n" \
"M a[200:203],a[56:59],a[92:95],a[200:203]\nds_read_b128 a[56:59],v50 offset:3584\nv_mul_f32 v37,s20,v37\nM a[204:207],a[60:63],a[92:95],a[204:207]\nbuffer_load_dwordx4 v16,s[28:31],s46 offen lds\nM a[236:239],a[60:63],a[88:91],a[236:239]\ns_add_u32 m0,s50,81920\nv_accvgpr_read_b32 v38,a136\n" \
"C v32,v34,v35\nM v[172:175],a[60:63],a[84:87],v[172:175]\ns_mov_b32 s46,768\nds_read2_b32 a[10:11],v47 offset0:2 offset1:3\nds_read2_b32 a[12:13],v47 offset0:128 offset1:129\nM v[140:143],a[60:63],a[80:83],v[140:143]\nds_read_b128 a[60:63],v50 offset:4096\nv_accvgpr_read_b32 v39,a137\n" \
"M v[144:147],a[64:67],a[80:83],v[144:147]\nds_read2_b32 a[14:15],v47 offset0:130 offset1:131\nC v33,v36,v37\nM v[176:179],a[64:67],a[84:87],v[176:179]\nbuffer_store_dwordx4 v[192:195],v4,s[36:39],s96 offen offset:0 sc1\nM a[240:243],a[64:67],a[88:91],a[240:243]\nds_read2_b32 a[16:17],v48 offset0:0 offset1:1\nds_read2_b32 a[18:19],v48 offset0:2 offset1:3\n" \
"M a[208:211],a[64:67],a[92:95],a[208:211]\nds_read2_b32 a[20:21],v48 offset0:128 offset1:129\nv_accvgpr_write_b32 a128,v30\nM a[212:215],a[68:71],a[92:95],a[212:215]\nds_read2_b32 a[22:23],v48 offset0:130 offset1:131\nds_read2_b32 a[24:25],v49 offset0:0 offset1:1\nM a[244:247],a[68:71],a[88:91],a[244:247]\nds_read2_b32 a[26:27],v49 offset0:2 offset1:3\n" \
"v_accvgpr_write_b32 a129,v31\nM v[180:183],a[68:71],a[84:87],v[180:183]\nds_read2_b32 a[28:29],v49 offset0:128 offset1:129\nv_accvgpr_read_b32 v40,a138\nM v[148:151],a[68:71],a[80:83],v[148:151]\nds_read2_b32 a[30:31],v49 offset0:130 offset1:131\nds_read_b128 a[96:99],v50 offset:448\nM v[152:155],a[72:75],a[80:83],v[152:155]\n" \
"ds_read2_b32 a[64:65],v46 offset0:16 offset1:17\nv_accvgpr_read_b32 v41,a139\nM v[184:187],a[72:75],a[84:87],v[184:187]\nds_read2_b32 a[66:67],v46 offset0:18 offset1:19\nv_mul_f32 v38,s20,v38\nM a[248:251],a[72:75],a[88:91],a[248:251]\nds_read_b128 a[100:103],v50 offset:960\nv_mul_f32 v39,s20,v39\n" \
"M a[216:219],a[72:75],a[92:95],a[216:219]\nds_read2_b32 a[68:69],v46 offset0:144 offset1:145\nv_accvgpr_write_b32 a130,v32\nM a[220:223],a[76:79],a[92:95],a[220:223]\nds_read2_b32 a[70:71],v46 offset0:146 offset1:147\nds_read_b128 a[104:107],v50 offset:1472\nM a[252:255],a[76:79],a[88:91],a[252:255]\nds_read2_b32 a[72:73],v47 offset0:16 offset1:17\n" \
"v_accvgpr_write_b32 a131,v33\nM v[188:191],a[76:79],a[84:87],v[188:191]\nbuffer_store_dwordx4 v[196:199],v4,s[36:39],s96 offen offset:1024 sc1\nM v[156:159],a[76:79],a[80:83],v[156:159]\ns_waitcnt lgkmcnt(5)\nds_read2_b32 a[74:75],v47 offset0:18 offset1:19\nds_read_b128 a[108:111],v50 offset:1984\nM v[224:227],v[52:55],a[32:35],0\n" \
"ds_read2_b32 a[76:77],v47 offset0:144 offset1:145\nv_accvgpr_read_b32 v30,a144\nM v[228:231],v[52:55],a[36:39],0\nds_read2_b32 a[78:79],v47 offset0:146 offset1:147\nv_accvgpr_read_b32 v31,a145\nM v[232:235],v[52:55],a[40:43],0\nbuffer_load_dwordx4 v17,s[28:31],s46 offen lds\nM v[236:239],v[52:55],a[44:47],0\n" \
"s_add_u32 m0,s50,86016\nds_read_b128 a[112:115],v50 offset:2496\nv_mul_f32 v40,s20,v40\nM v[240:243],v[52:55],a[48:51],0\nds_read2_b32 a[80:81],v48 offset0:16 offset1:17\nv_mul_f32 v41,s20,v41\nM v[244:247],v[52:55],a[52:55],0\nds_read2_b32 a[82:83],v48 offset0:18 offset1:19\n" \
"C v38,v38,v39\nM v[248:251],v[52:55],a[56:59],0\nds_read_b128 a[116:119],v50 offset:3008\nv_accvgpr_read_b32 v42,a140\nM v[252:255],v[52:55],a[60:63],0\nbuffer_store_dwordx4 v[200:203],v4,s[36:39],s96 offen offset:2048 sc1\nM v[224:227],a[0:3],a[32:35],v[224:227]\nds_read2_b32 a[84:85],v48 offset0:144 offset1:145\n" \
"ds_read2_b32 a[86:87],v48 offset0:146 offset1:147\nM v[228:231],a[0:3],a[36:39],v[228:231]\nds_read_b128 a[120:123],v50 offset:3520\nv_accvgpr_read_b32 v43,a141\nM v[228:231],a[4:7],a[32:35],v[228:231]\nds_read2_b32 a[88:89],v49 offset0:16 offset1:17\nv_accvgpr_read_b32 v44,a142\nM v[232:235],a[0:3],a[40:43],v[232:235]\n" \
"buffer_load_dwordx4 v18,s[28:31],s46 offen lds\nM v[232:235],a[4:7],a[36:39],v[232:235]\ns_add_u32 m0,s50,90112\nds_read2_b32 a[90:91],v49 offset0:18 offset1:19\nv_accvgpr_read_b32 v45,a143\nM v[232:235],a[8:11],a[32:35],v[232:235]\nds_read_b128 a[124:127],v50 offset:4032\nv_accvgpr_read_b32 v32,a146\n" \
"M v[236:239],a[0:3],a[44:47],v[236:239]\nds_read2_b32 a[92:93],v49 offset0:144 offset1:145\nv_accvgpr_read_b32 v33,a147\nM v[236:239],a[4:7],a[40:43],v[236:239]\nbuffer_load_dwordx4 v19,s[28:31],s46 offen lds\nM v[236:239],a[8:11],a[36:39],v[236:239]\ns_add_u32 m0,s50,94208\nds_read2_b32 a[94:95],v49 offset0:146 offset1:147\n" \
"v_mul_f32 v30,s20,v30\nM v[236:239],a[12:15],a[32:35],v[236:239]\nv_mul_f32 v31,s20,v31\nC v39,v40,v41\nM v[240:243],a[0:3],a[48:51],v[240:243]\nv_mul_f32 v42,s20,v42\nv_mul_f32 v43,s20,v43\nM v[240:243],a[4:7],a[44:47],v[240:243]\n" \
"buffer_load_dwordx4 v20,s[28:31],s46 offen lds\nM v[240:243],a[8:11],a[40:43],v[240:243]\ns_add_u32 m0,s51,16384\nv_mul_f32 v44,s20,v44\nv_mul_f32 v45,s20,v45\nM v[240:243],a[12:15],a[36:39],v[240:243]\ns_mov_b32 s46,896\nv_mul_f32 v32,s20,v32\n" \
"v_mul_f32 v33,s20,v33\nM v[240:243],a[16:19],a[32:35],v[240:243]\nC v30,v30,v31\nv_accvgpr_read_b32 v34,a148\nM v[244:247],a[0:3],a[52:55],v[244:247]\nbuffer_load_dwordx4 v21,s[32:35],s47 offen offset:0 lds\nM v[244:247],a[4:7],a[48:51],v[244:247]\ns_add_u32 m0,s51,17280\n" \
"v_accvgpr_read_b32 v35,a149\nv_accvgpr_read_b32 v36,a150\nM v[244:247],a[8:11],a[44:47],v[244:247]\nv_accvgpr_read_b32 v37,a151\nC v40,v42,v43\nM v[244:247],a[12:15],a[40:43],v[244:247]\nC v41,v44,v45\nv_accvgpr_write_b32 a132,v38\n" \
"M v[244:247],a[16:19],a[36:39],v[244:247]\nbuffer_load_dwordx4 v21,s[32:35],s47 offen offset:128 lds\nM v[244:247],a[20:23],a[32:35],v[244:247]\ns_add_u32 m0,s51,24576\nv_accvgpr_write_b32 a133,v39\nC v31,v32,v33\nM v[248:251],a[0:3],a[56:59],v[248:251]\ns_mov_b32 s47,3932160\n" \
"v_mul_f32 v34,s20,v34\nv_mul_f32 v35,s20,v35\nM v[248:251],a[4:7],a[52:55],v[248:251]\nv_mul_f32 v36,s20,v36\nv_mul_f32 v37,s20,v37\nM v[248:251],a[8:11],a[48:51],v[248:251]\nv_accvgpr_write_b32 a134,v40\nv_accvgpr_write_b32 a135,v41\n" \
"M v[248:251],a[12:15],a[44:47],v[248:251]\nbuffer_store_dwordx4 v[204:207],v4,s[36:39],s96 offen offset:3072 sc1\nM v[248:251],a[16:19],a[40:43],v[248:251]\ns_add_u32 s96,s57,4096\nv_accvgpr_read_b32 v38,a152\nv_accvgpr_read_b32 v39,a153\nM v[248:251],a[20:23],a[36:39],v[248:251]\nC v32,v34,v35\n" \
"C v33,v36,v37\nM v[248:251],a[24:27],a[32:35],v[248:251]\nv_accvgpr_read_b32 v40,a154\nv_accvgpr_read_b32 v41,a155\nM v[252:255],a[0:3],a[60:63],v[252:255]\nbuffer_load_dwordx4 v21,s[32:35],s47 offen offset:0 lds\nM v[252:255],a[4:7],a[56:59],v[252:255]\ns_add_u32 m0,s51,25472\n" \
"ds_read2_b32 a[0:1],v46 offset0:32 offset1:33\nv_mul_f32 v38,s20,v38\nM v[252:255],a[8:11],a[52:55],v[252:255]\nds_read2_b32 a[2:3],v46 offset0:34 offset1:35\nds_read2_b32 a[4:5],v46 offset0:160 offset1:161\nM v[252:255],a[12:15],a[48:51],v[252:255]\nds_read2_b32 a[6:7],v46 offset0:162 offset1:163\nv_mul_f32 v39,s20,v39\n" \
"M v[252:255],a[16:19],a[44:47],v[252:255]\nds_read2_b32 a[8:9],v47 offset0:32 offset1:33\nv_accvgpr_write_b32 a136,v30\nM v[252:255],a[20:23],a[40:43],v[252:255]\nbuffer_store_dwordx4 v[208:211],v4,s[36:39],s96 offen offset:0 sc1\nM v[252:255],a[24:27],a[36:39],v[252:255]\nds_read_b128 a[36:39],v50 offset:896\nds_read_b128 a[40:43],v50 offset:1408\n" \
"M v[252:255],a[28:31],a[32:35],v[252:255]\nds_read_b128 a[32:35],v50 offset:384\nv_accvgpr_write_b32 a137,v31\nM v[224:227],a[64:67],a[96:99],v[224:227]\ns_waitcnt lgkmcnt(5)\nds_read2_b32 a[10:11],v47 offset0:34 offset1:35\nv_accvgpr_write_b32 a138,v32\nM v[228:231],a[64:67],a[100:103],v[228:231]\n" \
"buffer_load_dwordx4 v21,s[32:35],s47 offen offset:128 lds\nM v[228:231],a[68:71],a[96:99],v[228:231]\ns_add_u32 m0,s50,0\nv_accvgpr_write_b32 a139,v33\nv_mul_f32 v40,s20,v40\nM v[232:235],a[64:67],a[104:107],v[232:235]\ns_mov_b32 s47,4194304\nds_read_b128 a[44:47],v50 offset:1920\n" \
"v_mul_f32 v41,s20,v41\nM v[232:235],a[68:71],a[100:103],v[232:235]\nds_read2_b32 a[12:13],v47 offset0:160 offset1:161\nds_read2_b32 a[14:15],v47 offset0:162 offset1:163\nM v[232:235],a[72:75],a[96:99],v[232:235]\nbuffer_load_dwordx4 v13,s[28:31],s46 offen lds\nM v[236:239],a[64:67],a[108:111],v[236:239]\ns_add_u32 m0,s50,4096\n" \
"C v38,v38,v39\nv_accvgpr_read_b32 v42,a156\nM v[236:239],a[68:71],a[104:107],v[236:239]\nds_read_b128 a[48:51],v50 offset:2432\nv_accvgpr_read_b32 v43,a157\nM v[236:239],a[72:75],a[100:103],v[236:239]\nds_read2_b32 a[16:17],v48 offset0:32 offset1:33\nds_read2_b32 a[18:19],v48 offset0:34 offset1:35\n" \
"M v[236:239],a[76:79],a[96:99],v[236:239]\nbuffer_load_dwordx4 v14,s[28:31],s46 offen lds\nM v[240:243],a[64:67],a[112:115],v[240:243]\ns_add_u32 m0,s50,8192\nv_accvgpr_read_b32 v44,a158\nC v39,v40,v41\nM v[240:243],a[68:71],a[108:111],v[240:243]\nds_read_b128 a[52:55],v50 offset:2944\n" \
"v_accvgpr_read_b32 v45,a159\nM v[240:243],a[72:75],a[104:107],v[240:243]\nds_read2_b32 a[20:21],v48 offset0:160 offset1:161\nds_read2_b32 a[22:23],v48 offset0:162 offset1:163\nM v[240:243],a[76:79],a[100:103],v[240:243]\nds_read_b128 a[56:59],v50 offset:3456\nv_mul_f32 v42,s20,v42\nM v[240:243],a[80:83],a[96:99],v[240:243]\n" \
"buffer_store_dwordx4 v[212:215],v4,s[36:39],s96 offen offset:1024 sc1\nM v[244:247],a[64:67],a[116:119],v[244:247]\nds_read2_b32 a[24:25],v49 offset0:32 offset1:33\nds_read2_b32 a[26:27],v49 offset0:34 offset1:35\nM v[244:247],a[68:71],a[112:115],v[244:247]\nds_read_b128 a[60:63],v50 offset:3968\nv_mul_f32 v43,s20,v43\nM v[244:247],a[72:75],a[108:111],v[244:247]\n" \
"ds_read2_b32 a[28:29],v49 offset0:160 offset1:161\nv_mul_f32 v44,s20,v44\nM v[244:247],a[76:79],a[104:107],v[244:247]\nbuffer_load_dwordx4 v15,s[28:31],s46 offen lds\nM v[244:247],a[80:83],a[100:103],v[244:247]\ns_add_u32 m0,s50,12288\nds_read2_b32 a[30:31],v49 offset0:162 offset1:163\nv_mul_f32 v45,s20,v45\n" \
"M v[244:247],a[84:87],a[96:99],v[244:247]\nC v40,v42,v43\nC v41,v44,v45\nM v[248:251],a[64:67],a[120:123],v[248:251]\nv_accvgpr_read_b32 v30,a160\nv_accvgpr_read_b32 v31,a161\nM v[248:251],a[68:71],a[116:119],v[248:251]\nv_accvgpr_write_b32 a140,v38\n" \
"v_accvgpr_read_b32 v32,a162\nM v[248:251],a[72:75],a[112:115],v[248:251]\nbuffer_store_dwordx4 v[216:219],v4,s[36:39],s96 offen offset:2048 sc1\nM v[248:251],a[76:79],a[108:111],v[248:251]\nLG10_%=:\ns_cbranch_execz LHA155_%=\nv_accvgpr_write_b32 a141,v39\nv_accvgpr_write_b32 a142,v40\n" \
"M v[248:251],a[80:83],a[104:107],v[248:251]\nv_accvgpr_write_b32 a143,v41\nv_accvgpr_read_b32 v33,a163\nM v[248:251],a[84:87],a[100:103],v[248:251]\nv_mul_f32 v30,s20,v30\nv_mul_f32 v31,s20,v31\nM v[248:251],a[88:91],a[96:99],v[248:251]\nbuffer_load_dwordx4 v16,s[28:31],s46 offen lds\n" \
"M v[252:255],a[64:67],a[124:127],v[252:255]\ns_add_u32 m0,s50,16384\nds_read2_b32 a[64:65],v46 offset0:48 offset1:49\nv_mul_f32 v32,s20,v32\nM v[252:255],a[68:71],a[120:123],v[252:255]\ns_mov_b32 s46,896\nds_read2_b32 a[66:67],v46 offset0:50 offset1:51\nds_read2_b32 a[68:69],v46 offset0:176 offset1:177\n" \
"M v[252:255],a[72:75],a[116:119],v[252:255]\nds_read2_b32 a[70:71],v46 offset0:178 offset1:179\nv_mul_f32 v33,s20,v33\nM v[252:255],a[76:79],a[112:115],v[252:255]\nds_read2_b32 a[72:73],v47 offset0:48 offset1:49\nC v30,v30,v31\nM v[252:255],a[80:83],a[108:111],v[252:255]\nbuffer_store_dwordx4 v[220:223],v4,s[36:39],s96 offen offset:3072 sc1\n" \
"M v[252:255],a[84:87],a[104:107],v[252:255]\ns_add_u32 s96,s57,16384\nds_read_b128 a[104:107],v50 offset:1344\nds_read2_b32 a[74:75],v47 offset0:50 offset1:51\nM v[252:255],a[88:91],a[100:103],v[252:255]\nds_read_b128 a[100:103],v50 offset:832\nv_accvgpr_read_b32 v34,a164\nM v[252:255],a[92:95],a[96:99],v[252:255]\n" \
"s_waitcnt lgkmcnt(5)\nds_read_b128 a[96:99],v50 offset:320\nv_accvgpr_read_b32 v35,a165\nM v[224:227],a[0:3],a[32:35],v[224:227]\nbuffer_load_dwordx4 v17,s[28:31],s46 offen lds\nM v[228:231],a[0:3],a[36:39],v[228:231]\ns_add_u32 m0,s50,20480\nv_accvgpr_read_b32 v36,a166\n" \
"v_accvgpr_read_b32 v37,a167\nM v[228:231],a[4:7],a[32:35],v[228:231]\nds_read_b128 a[108:111],v50 offset:1856\nv_accvgpr_read_b32 v38,a168\nM v[232:235],a[0:3],a[40:43],v[232:235]\nds_read2_b32 a[76:77],v47 offset0:176 offset1:177\nds_read2_b32 a[78:79],v47 offset0:178 offset1:179\nM v[232:235],a[4:7],a[36:39],v[232:235]\n" \
"ds_read_b128 a[112:115],v50 offset:2368\nC v31,v32,v33\nM v[232:235],a[8:11],a[32:35],v[232:235]\nbuffer_store_dwordx4 a[128:131],v4,s[36:39],s96 offen offset:0 sc1\nM v[236:239],a[0:3],a[44:47],v[236:239]\nds_read2_b32 a[80:81],v48 offset0:48 offset1:49\nds_read2_b32 a[82:83],v48 offset0:50 offset1:51\nM v[236:239],a[4:7],a[40:43],v[236:239]\n" \
"ds_read_b128 a[116:119],v50 offset:2880\nv_accvgpr_read_b32 v39,a169\nM v[236:239],a[8:11],a[36:39],v[236:239]\nds_read2_b32 a[84:85],v48 offset0:176 offset1:177\nv_mul_f32 v34,s20,v34\nM v[236:239],a[12:15],a[32:35],v[236:239]\nds_read2_b32 a[86:87],v48 offset0:178 offset1:179\nds_read_b128 a[120:123],v50 offset:3392\n" \
"M v[240:243],a[0:3],a[48:51],v[240:243]\nds_read2_b32 a[88:89],v49 offset0:48 offset1:49\nv_mul_f32 v35,s20,v35\nM v[240:243],a[4:7],a[44:47],v[240:243]\nds_read2_b32 a[90:91],v49 offset0:50 offset1:51\nv_mul_f32 v36,s20,v36\nM v[240:243],a[8:11],a[40:43],v[240:243]\nds_read_b128 a[124:127],v50 offset:3904\n" \
"v_mul_f32 v37,s20,v37\nM v[240:243],a[12:15],a[36:39],v[240:243]\nds_read2_b32 a[92:93],v49 offset0:176 offset1:177\nv_accvgpr_read_b32 v40,a170\nM v[240:243],a[16:19],a[32:35],v[240:243]\nds_read2_b32 a[94:95],v49 offset0:178 offset1:179\nv_accvgpr_read_b32 v41,a171\nM v[244:247],a[0:3],a[52:55],v[244:247]\n" \
"v_mul_f32 v38,s20,v38\nv_mul_f32 v39,s20,v39\nM v[244:247],a[4:7],a[48:51],v[244:247]\nC v32,v34,v35\nC v33,v36,v37\nM v[244:247],a[8:11],a[44:47],v[244:247]\nv_mul_f32 v40,s20,v40\nv_mul_f32 v41,s20,v41\n" \
"M v[244:247],a[12:15],a[40:43],v[244:247]\nbuffer_store_dwordx4 a[132:135],v4,s[36:39],s96 offen offset:1024 sc1\nM v[244:247],a[16:19],a[36:39],v[244:247]\nC v38,v38,v39\nv_accvgpr_read_b32 v42,a172\nM v[244:247],a[20:23],a[32:35],v[244:247]\nv_accvgpr_read_b32 v43,a173\nv_accvgpr_read_b32 v44,a174\n" \
"M v[248:251],a[0:3],a[56:59],v[248:251]\nv_accvgpr_read_b32 v45,a175\nv_accvgpr_write_b32 a144,v30\nM v[248:251],a[4:7],a[52:55],v[248:251]\nbuffer_load_dwordx4 v18,s[28:31],s46 offen lds\nM v[248:251],a[8:11],a[48:51],v[248:251]\ns_add_u32 m0,s50,24576\nv_accvgpr_write_b32 a145,v31\n" \
"v_accvgpr_write_b32 a146,v32\nM v[248:251],a[12:15],a[44:47],v[248:251]\nv_accvgpr_write_b32 a147,v33\nC v39,v40,v41\nM v[248:251],a[16:19],a[40:43],v[248:251]\nv_mul_f32 v42,s20,v42\nv_mul_f32 v43,s20,v43\nM v[248:251],a[20:23],a[36:39],v[248:251]\n" \
"v_mul_f32 v44,s20,v44\nv_mul_f32 v45,s20,v45\nM v[248:251],a[24:27],a[32:35],v[248:251]\nbuffer_store_dwordx4 a[136:139],v4,s[36:39],s96 offen offset:2048 sc1\nM v[252:255],a[0:3],a[60:63],v[252:255]\nds_read2_b32 a[0:1],v46 offset0:64 offset1:65\nds_read2_b32 a[2:3],v46 offset0:66 offset1:67\nM v[252:255],a[4:7],a[56:59],v[252:255]\n" \
"ds_read2_b32 a[4:5],v46 offset0:192 offset1:193\nC v40,v42,v43\nM v[252:255],a[8:11],a[52:55],v[252:255]\nds_read2_b32 a[6:7],v46 offset0:194 offset1:195\nds_read2_b32 a[8:9],v47 offset0:64 offset1:65\nM v[252:255],a[12:15],a[48:51],v[252:255]\nds_read2_b32 a[10:11],v47 offset0:66 offset1:67\nC v41,v44,v45\n" \
"M v[252:255],a[16:19],a[44:47],v[252:255]\nds_read_b128 a[44:47],v50 offset:1792\nv_accvgpr_write_b32 a148,v38\nM v[252:255],a[20:23],a[40:43],v[252:255]\nds_read_b128 a[40:43],v50 offset:1280\nv_accvgpr_write_b32 a149,v39\nM v[252:255],a[24:27],a[36:39],v[252:255]\nds_read_b128 a[36:39],v50 offset:768\n" \
"v_accvgpr_write_b32 a150,v40\nM v[252:255],a[28:31],a[32:35],v[252:255]\ns_waitcnt lgkmcnt(6)\nds_read_b128 a[32:35],v50 offset:256\nv_accvgpr_write_b32 a151,v41\nM v[224:227],a[64:67],a[96:99],v[224:227]\nds_read2_b32 a[12:13],v47 offset0:192 offset1:193\nv_accvgpr_read_b32 v30,a176\n" \
"M v[228:231],a[64:67],a[100:103],v[228:231]\nds_read2_b32 a[14:15],v47 offset0:194 offset1:195\nds_read_b128 a[48:51],v50 offset:2304\nM v[228:231],a[68:71],a[96:99],v[228:231]\nds_read2_b32 a[16:17],v48 offset0:64 offset1:65\nv_accvgpr_read_b32 v31,a177\nM v[232:235],a[64:67],a[104:107],v[232:235]\nds_read2_b32 a[18:19],v48 offset0:66 offset1:67\n" \
"v_accvgpr_read_b32 v32,a178\nM v[232:235],a[68:71],a[100:103],v[232:235]\nbuffer_store_dwordx4 a[140:143],v4,s[36:39],s96 offen offset:3072 sc1\nM v[232:235],a[72:75],a[96:99],v[232:235]\ns_add_u32 s96,s57,20480\nds_read_b128 a[52:55],v50 offset:2816\nds_read2_b32 a[20:21],v48 offset0:192 offset1:193\nM v[236:239],a[64:67],a[108:111],v[236:239]\n" \
"ds_read2_b32 a[22:23],v48 offset0:194 offset1:195\nv_accvgpr_read_b32 v33,a179\nM v[236:239],a[68:71],a[104:107],v[236:239]\nds_read_b128 a[56:59],v50 offset:3328\nv_mul_f32 v30,s20,v30\nM v[236:239],a[72:75],a[100:103],v[236:239]\nds_read2_b32 a[24:25],v49 offset0:64 offset1:65\nv_mul_f32 v31,s20,v31\n" \
"M v[236:239],a[76:79],a[96:99],v[236:239]\nds_read2_b32 a[26:27],v49 offset0:66 offset1:67\nds_read_b128 a[60:63],v50 offset:3840\nM v[240:243],a[64:67],a[112:115],v[240:243]\nds_read2_b32 a[28:29],v49 offset0:192 offset1:193\nv_mul_f32 v32,s20,v32\nM v[240:243],a[68:71],a[108:111],v[240:243]\nds_read2_b32 a[30:31],v49 offset0:194 offset1:195\n" \
"v_mul_f32 v33,s20,v33\nM v[240:243],a[72:75],a[104:107],v[240:243]\nC v30,v30,v31\nv_accvgpr_read_b32 v34,a180\nM v[240:243],a[76:79],a[100:103],v[240:243]\nv_accvgpr_read_b32 v35,a181\nv_accvgpr_read_b32 v36,a182\nM v[240:243],a[80:83],a[96:99],v[240:243]\n" \
"C v31,v32,v33\nv_accvgpr_read_b32 v37,a183\nM v[244:247],a[64:67],a[116:119],v[244:247]\nv_mul_f32 v34,s20,v34\nv_mul_f32 v35,s20,v35\nM v[244:247],a[68:71],a[112:115],v[244:247]\nv_mul_f32 v36,s20,v36\nv_mul_f32 v37,s20,v37\n" \
"M v[244:247],a[72:75],a[108:111],v[244:247]\nbuffer_store_dwordx4 a[144:147],v4,s[36:39],s96 offen offset:0 sc1\nM v[244:247],a[76:79],a[104:107],v[244:247]\nC v32,v34,v35\nC v33,v36,v37\nM v[244:247],a[80:83],a[100:103],v[244:247]\nv_accvgpr_write_b32 a152,v30\nv_accvgpr_write_b32 a153,v31\n" \
"M v[244:247],a[84:87],a[96:99],v[244:247]\nv_accvgpr_write_b32 a154,v32\nv_accvgpr_write_b32 a155,v33\nM v[248:251],a[64:67],a[120:123],v[248:251]\nv_accvgpr_read_b32 v38,a184\nv_accvgpr_read_b32 v39,a185\nM v[248:251],a[68:71],a[116:119],v[248:251]\nv_accvgpr_read_b32 v40,a186\n" \
"v_accvgpr_read_b32 v41,a187\nM v[248:251],a[72:75],a[112:115],v[248:251]\nv_mul_f32 v38,s20,v38\nv_mul_f32 v39,s20,v39\nM v[248:251],a[76:79],a[108:111],v[248:251]\nv_mul_f32 v40,s20,v40\nv_mul_f32 v41,s20,v41\nM v[248:251],a[80:83],a[104:107],v[248:251]\n" \
"C v38,v38,v39\nv_accvgpr_read_b32 v42,a188\nM v[248:251],a[84:87],a[100:103],v[248:251]\nv_accvgpr_read_b32 v43,a189\nv_accvgpr_read_b32 v44,a190\nM v[248:251],a[88:91],a[96:99],v[248:251]\nC v39,v40,v41\nv_accvgpr_read_b32 v45,a191\n" \
"M v[252:255],a[64:67],a[124:127],v[252:255]\nds_read2_b32 a[64:65],v46 offset0:80 offset1:81\nds_read2_b32 a[66:67],v46 offset0:82 offset1:83\nM v[252:255],a[68:71],a[120:123],v[252:255]\nds_read2_b32 a[68:69],v46 offset0:208 offset1:209\nv_mul_f32 v42,s20,v42\nM v[252:255],a[72:75],a[116:119],v[252:255]\nbuffer_store_dwordx4 a[148:151],v4,s[36:39],s96 offen offset:1024 sc1\n" \
"M v[252:255],a[76:79],a[112:115],v[252:255]\nds_read2_b32 a[70:71],v46 offset0:210 offset1:211\nds_read2_b32 a[72:73],v47 offset0:80 offset1:81\nM v[252:255],a[80:83],a[108:111],v[252:255]\nds_read2_b32 a[74:75],v47 offset0:82 offset1:83\nv_mul_f32 v43,s20,v43\nM v[252:255],a[84:87],a[104:107],v[252:255]\nds_read_b128 a[104:107],v50 offset:1216\n" \
"v_mul_f32 v44,s20,v44\nM v[252:255],a[88:91],a[100:103],v[252:255]\nds_read_b128 a[100:103],v50 offset:704\nv_mul_f32 v45,s20,v45\nM v[252:255],a[92:95],a[96:99],v[252:255]\ns_waitcnt lgkmcnt(5)\nds_read_b128 a[96:99],v50 offset:192\nC v40,v42,v43\n" \
"M v[224:227],a[0:3],a[32:35],v[224:227]\nds_read_b128 a[108:111],v50 offset:1728\nC v41,v44,v45\nM v[228:231],a[0:3],a[36:39],v[228:231]\nds_read2_b32 a[76:77],v47 offset0:208 offset1:209\nds_read2_b32 a[78:79],v47 offset0:210 offset1:211\nM v[228:231],a[4:7],a[32:35],v[228:231]\nds_read_b128 a[112:115],v50 offset:2240\n" \
"v_accvgpr_write_b32 a156,v38\nM v[232:235],a[0:3],a[40:43],v[232:235]\nds_read2_b32 a[80:81],v48 offset0:80 offset1:81\nv_accvgpr_write_b32 a157,v39\nM v[232:235],a[4:7],a[36:39],v[232:235]\nds_read2_b32 a[82:83],v48 offset0:82 offset1:83\nv_accvgpr_write_b32 a158,v40\nM v[232:235],a[8:11],a[32:35],v[232:235]\n" \
"ds_read_b128 a[116:119],v50 offset:2752\nv_accvgpr_write_b32 a159,v41\nM v[236:239],a[0:3],a[44:47],v[236:239]\nds_read2_b32 a[84:85],v48 offset0:208 offset1:209\nds_read2_b32 a[86:87],v48 offset0:210 offset1:211\nM v[236:239],a[4:7],a[40:43],v[236:239]\nbuffer_store_dwordx4 a[152:155],v4,s[36:39],s96 offen offset:2048 sc1\nM v[236:239],a[8:11],a[36:39],v[236:239]\n" \
"ds_read_b128 a[120:123],v50 offset:3264\nds_read2_b32 a[88:89],v49 offset0:80 offset1:81\nM v[236:239],a[12:15],a[32:35],v[236:239]\nds_read2_b32 a[90:91],v49 offset0:82 offset1:83\nM v[240:243],a[0:3],a[48:51],v[240:243]\nds_read_b128 a[124:127],v50 offset:3776\nM v[240:243],a[4:7],a[44:47],v[240:243]\nbuffer_load_dwordx4 v19,s[28:31],s46 offen lds\n" \
"M v[240:243],a[8:11],a[40:43],v[240:243]\ns_add_u32 m0,s50,28672\nds_read2_b32 a[92:93],v49 offset0:208 offset1:209\nM v[240:243],a[12:15],a[36:39],v[240:243]\nds_read2_b32 a[94:95],v49 offset0:210 offset1:211\nM v[240:243],a[16:19],a[32:35],v[240:243]\nM v[244:247],a[0:3],a[52:55],v[244:247]\nM v[244:247],a[4:7],a[48:51],v[244:247]\n" \
"buffer_store_dwordx4 a[156:159],v4,s[36:39],s96 offen offset:3072 sc1\nM v[244:247],a[8:11],a[44:47],v[244:247]\ns_add_u32 s96,s57,49152\nM v[244:247],a[12:15],a[40:43],v[244:247]\nM v[244:247],a[16:19],a[36:39],v[244:247]\nM v[244:247],a[20:23],a[32:35],v[244:247]\nM v[248:251],a[0:3],a[56:59],v[248:251]\nM v[248:251],a[4:7],a[52:55],v[248:251]\n" \
"M v[248:251],a[8:11],a[48:51],v[248:251]\nM v[248:251],a[12:15],a[44:47],v[248:251]\nM v[248:251],a[16:19],a[40:43],v[248:251]\nM v[248:251],a[20:23],a[36:39],v[248:251]\nM v[248:251],a[24:27],a[32:35],v[248:251]\nM v[252:255],a[0:3],a[60:63],v[252:255]\nds_read2_b32 a[0:1],v46 offset0:96 offset1:97\nds_read2_b32 a[2:3],v46 offset0:98 offset1:99\n" \
"M v[252:255],a[4:7],a[56:59],v[252:255]\nbuffer_store_dwordx4 v[88:91],v4,s[36:39],s96 offen offset:0 sc1\nM v[252:255],a[8:11],a[52:55],v[252:255]\nds_read2_b32 a[4:5],v46 offset0:224 offset1:225\nds_read2_b32 a[6:7],v46 offset0:226 offset1:227\nM v[252:255],a[12:15],a[48:51],v[252:255]\nds_read2_b32 a[8:9],v47 offset0:96 offset1:97\nM v[252:255],a[16:19],a[44:47],v[252:255]\n" \
"ds_read2_b32 a[10:11],v47 offset0:98 offset1:99\nds_read_b128 a[44:47],v50 offset:1664\nM v[252:255],a[20:23],a[40:43],v[252:255]\nds_read_b128 a[40:43],v50 offset:1152\nM v[252:255],a[24:27],a[36:39],v[252:255]\nds_read_b128 a[36:39],v50 offset:640\nM v[252:255],a[28:31],a[32:35],v[252:255]\ns_waitcnt lgkmcnt(7)\n" \
"ds_read_b128 a[32:35],v50 offset:128\nM v[224:227],a[64:67],a[96:99],v[224:227]\nds_read2_b32 a[12:13],v47 offset0:224 offset1:225\nM v[228:231],a[64:67],a[100:103],v[228:231]\nds_read2_b32 a[14:15],v47 offset0:226 offset1:227\nM v[228:231],a[68:71],a[96:99],v[228:231]\nds_read_b128 a[48:51],v50 offset:2176\nM v[232:235],a[64:67],a[104:107],v[232:235]\n" \
"ds_read2_b32 a[16:17],v48 offset0:96 offset1:97\nM v[232:235],a[68:71],a[100:103],v[232:235]\nds_read2_b32 a[18:19],v48 offset0:98 offset1:99\nds_read_b128 a[52:55],v50 offset:2688\nM v[232:235],a[72:75],a[96:99],v[232:235]\nds_read2_b32 a[20:21],v48 offset0:224 offset1:225\nM v[236:239],a[64:67],a[108:111],v[236:239]\nbuffer_store_dwordx4 v[92:95],v4,s[36:39],s96 offen offset:1024 sc1\n" \
"M v[236:239],a[68:71],a[104:107],v[236:239]\nds_read2_b32 a[22:23],v48 offset0:226 offset1:227\nds_read_b128 a[56:59],v50 offset:3200\nM v[236:239],a[72:75],a[100:103],v[236:239]\nds_read2_b32 a[24:25],v49 offset0:96 offset1:97\nM v[236:239],a[76:79],a[96:99],v[236:239]\nds_read2_b32 a[26:27],v49 offset0:98 offset1:99\nM v[240:243],a[64:67],a[112:115],v[240:243]\n" \
"ds_read_b128 a[60:63],v50 offset:3712\nM v[240:243],a[68:71],a[108:111],v[240:243]\nds_read2_b32 a[28:29],v49 offset0:224 offset1:225\nds_read2_b32 a[30:31],v49 offset0:226 offset1:227\nM v[240:243],a[72:75],a[104:107],v[240:243]\nM v[240:243],a[76:79],a[100:103],v[240:243]\nM v[240:243],a[80:83],a[96:99],v[240:243]\nM v[244:247],a[64:67],a[116:119],v[244:247]\n" \
"M v[244:247],a[68:71],a[112:115],v[244:247]\nM v[244:247],a[72:75],a[108:111],v[244:247]\nM v[244:247],a[76:79],a[104:107],v[244:247]\nM v[244:247],a[80:83],a[100:103],v[244:247]\nbuffer_store_dwordx4 v[104:107],v4,s[36:39],s96 offen offset:2048 sc1\nM v[244:247],a[84:87],a[96:99],v[244:247]\nM v[248:251],a[64:67],a[120:123],v[248:251]\nM v[248:251],a[68:71],a[116:119],v[248:251]\n" \
"M v[248:251],a[72:75],a[112:115],v[248:251]\nM v[248:251],a[76:79],a[108:111],v[248:251]\nM v[248:251],a[80:83],a[104:107],v[248:251]\nM v[248:251],a[84:87],a[100:103],v[248:251]\nM v[248:251],a[88:91],a[96:99],v[248:251]\nM v[252:255],a[64:67],a[124:127],v[252:255]\nds_read2_b32 a[64:65],v46 offset0:112 offset1:113\nds_read2_b32 a[66:67],v46 offset0:114 offset1:115\n" \
"M v[252:255],a[68:71],a[120:123],v[252:255]\nds_read2_b32 a[68:69],v46 offset0:240 offset1:241\nM v[252:255],a[72:75],a[116:119],v[252:255]\nbuffer_load_dwordx4 v20,s[28:31],s46 offen lds\nM v[252:255],a[76:79],a[112:115],v[252:255]\ns_add_u32 m0,s51,32768\nds_read2_b32 a[70:71],v46 offset0:242 offset1:243\nM v[252:255],a[80:83],a[108:111],v[252:255]\n" \
"s_mov_b32 s46,1024\nds_read2_b32 a[72:73],v47 offset0:112 offset1:113\nM v[252:255],a[84:87],a[104:107],v[252:255]\nds_read_b128 a[104:107],v50 offset:1088\nM v[252:255],a[88:91],a[100:103],v[252:255]\ns_waitcnt lgkmcnt(4)\nds_read_b128 a[100:103],v50 offset:576\nM v[252:255],a[92:95],a[96:99],v[252:255]\n" \
"buffer_store_dwordx4 v[108:111],v4,s[36:39],s96 offen offset:3072 sc1\nM v[224:227],a[0:3],a[32:35],v[224:227]\ns_add_u32 s96,s57,53248\nds_read_b128 a[96:99],v50 offset:64\nds_read2_b32 a[74:75],v47 offset0:114 offset1:115\nM v[228:231],a[0:3],a[36:39],v[228:231]\nds_read_b128 a[108:111],v50 offset:1600\nM v[228:231],a[4:7],a[32:35],v[228:231]\n" \
"ds_read2_b32 a[76:77],v47 offset0:240 offset1:241\nM v[232:235],a[0:3],a[40:43],v[232:235]\nds_read2_b32 a[78:79],v47 offset0:242 offset1:243\nM v[232:235],a[4:7],a[36:39],v[232:235]\nds_read_b128 a[112:115],v50 offset:2112\nM v[232:235],a[8:11],a[32:35],v[232:235]\nds_read2_b32 a[80:81],v48 offset0:112 offset1:113\nds_read2_b32 a[82:83],v48 offset0:114 offset1:115\n" \
"M v[236:239],a[0:3],a[44:47],v[236:239]\nds_read_b128 a[116:119],v50 offset:2624\nM v[236:239],a[4:7],a[40:43],v[236:239]\nds_read2_b32 a[84:85],v48 offset0:240 offset1:241\nM v[236:239],a[8:11],a[36:39],v[236:239]\nds_read2_b32 a[86:87],v48 offset0:242 offset1:243\nM v[236:239],a[12:15],a[32:35],v[236:239]\nds_read_b128 a[120:123],v50 offset:3136\n" \
"M v[240:243],a[0:3],a[48:51],v[240:243]\nds_read2_b32 a[88:89],v49 offset0:112 offset1:113\nds_read2_b32 a[90:91],v49 offset0:114 offset1:115\nM v[240:243],a[4:7],a[44:47],v[240:243]\nds_read_b128 a[124:127],v50 offset:3648\nM v[240:243],a[8:11],a[40:43],v[240:243]\nds_read2_b32 a[92:93],v49 offset0:240 offset1:241\nM v[240:243],a[12:15],a[36:39],v[240:243]\n" \
"ds_read2_b32 a[94:95],v49 offset0:242 offset1:243\nM v[240:243],a[16:19],a[32:35],v[240:243]\nM v[244:247],a[0:3],a[52:55],v[244:247]\nM v[244:247],a[4:7],a[48:51],v[244:247]\nM v[244:247],a[8:11],a[44:47],v[244:247]\nM v[244:247],a[12:15],a[40:43],v[244:247]\nM v[244:247],a[16:19],a[36:39],v[244:247]\nM v[244:247],a[20:23],a[32:35],v[244:247]\n" \
"M v[248:251],a[0:3],a[56:59],v[248:251]\nM v[248:251],a[4:7],a[52:55],v[248:251]\nM v[248:251],a[8:11],a[48:51],v[248:251]\nM v[248:251],a[12:15],a[44:47],v[248:251]\nM v[248:251],a[16:19],a[40:43],v[248:251]\nM v[248:251],a[20:23],a[36:39],v[248:251]\nM v[248:251],a[24:27],a[32:35],v[248:251]\nM v[252:255],a[0:3],a[60:63],v[252:255]\n" \
"M v[252:255],a[4:7],a[56:59],v[252:255]\nM v[252:255],a[8:11],a[52:55],v[252:255]\nM v[252:255],a[12:15],a[48:51],v[252:255]\nM v[252:255],a[16:19],a[44:47],v[252:255]\nM v[252:255],a[20:23],a[40:43],v[252:255]\nM v[252:255],a[24:27],a[36:39],v[252:255]\nM v[252:255],a[28:31],a[32:35],v[252:255]\ns_waitcnt lgkmcnt(0)\n" \
"M v[224:227],a[64:67],a[96:99],v[224:227]\ns_waitcnt vmcnt(29) lgkmcnt(0)\nM v[228:231],a[64:67],a[100:103],v[228:231]\ns_barrier\nds_read_b64_tr_b16 a[0:1],v9 offset:0\nM v[228:231],a[68:71],a[96:99],v[228:231]\nbuffer_load_dwordx4 v21,s[32:35],s47 offen offset:0 lds\nM v[232:235],a[64:67],a[104:107],v[232:235]\n" \
"s_add_u32 m0,s51,33664\nC v224,v224,v225\nC v225,v226,v227\nM v[232:235],a[68:71],a[100:103],v[232:235]\nC v226,v228,v229\nC v227,v230,v231\nM v[232:235],a[72:75],a[96:99],v[232:235]\nv_permlane16_swap_b32 v224,v226\n" \
"M v[236:239],a[64:67],a[108:111],v[236:239]\nbuffer_load_dwordx4 v21,s[32:35],s47 offen offset:128 lds\nM v[236:239],a[68:71],a[104:107],v[236:239]\ns_mov_b32 s47,4456448\nv_permlane16_swap_b32 v225,v227\nM v[236:239],a[72:75],a[100:103],v[236:239]\ns_add_u32 m0,s51,40960\nds_read_b64_tr_b16 a[2:3],v10 offset:0\n" \
"C v232,v232,v233\nM v[236:239],a[76:79],a[96:99],v[236:239]\nds_read_b128 a[32:35],v7 offset:0\nC v233,v234,v235\nM v[240:243],a[64:67],a[112:115],v[240:243]\nbuffer_load_dwordx4 v21,s[32:35],s47 offen offset:0 lds\nM v[240:243],a[68:71],a[108:111],v[240:243]\ns_add_u32 m0,s51,41856\n" \
"ds_read_b128 a[36:39],v7 offset:4096\nC v234,v236,v237\nM v[240:243],a[72:75],a[104:107],v[240:243]\nds_read_b128 a[40:43],v7 offset:8192\nC v235,v238,v239\nM v[240:243],a[76:79],a[100:103],v[240:243]\nds_read_b128 a[44:47],v7 offset:12288\nM v[240:243],a[80:83],a[96:99],v[240:243]\n" \
"buffer_load_dwordx4 v21,s[32:35],s47 offen offset:128 lds\nM v[244:247],a[64:67],a[116:119],v[244:247]\ns_add_u32 m0,s50,32768\nv_permlane16_swap_b32 v232,v234\nM v[244:247],a[68:71],a[112:115],v[244:247]\ns_mov_b32 s47,4718592\nds_read_b64_tr_b16 a[4:5],v11 offset:0\nds_read_b64_tr_b16 a[6:7],v12 offset:0\n" \
"M v[244:247],a[72:75],a[108:111],v[244:247]\nv_permlane16_swap_b32 v233,v235\nM v[244:247],a[76:79],a[104:107],v[244:247]\nbuffer_load_dwordx4 v13,s[28:31],s46 offen lds\nM v[244:247],a[80:83],a[100:103],v[244:247]\ns_add_u32 m0,s50,36864\nds_read_b64_tr_b16 a[8:9],v9 offset:512\nC v240,v240,v241\n" \
"M v[244:247],a[84:87],a[96:99],v[244:247]\nds_read_b64_tr_b16 a[10:11],v10 offset:512\nds_read_b64_tr_b16 a[12:13],v11 offset:512\nM v[248:251],a[64:67],a[120:123],v[248:251]\nds_read_b64_tr_b16 a[14:15],v12 offset:512\nC v241,v242,v243\nM v[248:251],a[68:71],a[116:119],v[248:251]\nbuffer_load_dwordx4 v14,s[28:31],s46 offen lds\n" \
"M v[248:251],a[72:75],a[112:115],v[248:251]\ns_add_u32 m0,s50,40960\nds_read_b64_tr_b16 a[16:17],v9 offset:1024\nC v242,v244,v245\nM v[248:251],a[76:79],a[108:111],v[248:251]\nds_read_b64_tr_b16 a[18:19],v10 offset:1024\nC v243,v246,v247\nM v[248:251],a[80:83],a[104:107],v[248:251]\n" \
"ds_read_b64_tr_b16 a[20:21],v11 offset:1024\nM v[248:251],a[84:87],a[100:103],v[248:251]\nbuffer_load_dwordx4 v15,s[28:31],s46 offen lds\nM v[248:251],a[88:91],a[96:99],v[248:251]\ns_add_u32 m0,s50,45056\nds_read_b64_tr_b16 a[22:23],v12 offset:1024\nM v[252:255],a[64:67],a[124:127],v[252:255]\nds_read_b64_tr_b16 a[24:25],v9 offset:1536\n" \
"M v[252:255],a[68:71],a[120:123],v[252:255]\nds_read_b64_tr_b16 a[26:27],v10 offset:1536\nds_read_b64_tr_b16 a[28:29],v11 offset:1536\nM v[252:255],a[72:75],a[116:119],v[252:255]\nbuffer_load_dwordx4 v16,s[28:31],s46 offen lds\nM v[252:255],a[76:79],a[112:115],v[252:255]\ns_mov_b32 s46,1024\nv_permlane16_swap_b32 v240,v242\n" \
"M v[252:255],a[80:83],a[108:111],v[252:255]\ns_add_u32 m0,s50,49152\nds_read_b64_tr_b16 a[30:31],v12 offset:1536\nds_read_b64_tr_b16 a[48:49],v9 offset:8192\nM v[252:255],a[84:87],a[104:107],v[252:255]\nLG11_%=:\ns_cbranch_execz LHA155_%=\ns_waitcnt lgkmcnt(5)\n" \
"ds_read_b64_tr_b16 a[50:51],v10 offset:8192\nC v248,v248,v249\nM v[252:255],a[88:91],a[100:103],v[252:255]\nbuffer_load_dwordx4 v17,s[28:31],s46 offen lds\nM v[252:255],a[92:95],a[96:99],v[252:255]\ns_add_u32 m0,s50,53248\nds_read_b128 a[80:83],v8 offset:0\nC v249,v250,v251\n" \
"M v[128:131],a[0:3],a[32:35],v[128:131]\ns_load_dwordx2 s[84:85],%0,0x50\nds_read_b128 a[84:87],v8 offset:4096\nM v[160:163],a[0:3],a[36:39],v[160:163]\ns_load_dwordx2 s[88:89],%0,0x60\nds_read_b64_tr_b16 a[52:53],v11 offset:8192\nC v250,v252,v253\nM a[224:227],a[0:3],a[40:43],a[224:227]\n" \
"buffer_load_dwordx4 v18,s[28:31],s46 offen lds\nM a[192:195],a[0:3],a[44:47],a[192:195]\ns_add_u32 m0,s50,57344\nv_permlane16_swap_b32 v241,v243\nM a[196:199],a[4:7],a[44:47],a[196:199]\ns_load_dwordx2 s[12:13],%0,0x0\nds_read_b64_tr_b16 a[54:55],v12 offset:8192\nds_read_b64_tr_b16 a[56:57],v9 offset:8704\n" \
"M a[228:231],a[4:7],a[40:43],a[228:231]\ns_memrealtime s[10:11]\nds_read_b64_tr_b16 a[58:59],v10 offset:8704\nC v251,v254,v255\nM v[164:167],a[4:7],a[36:39],v[164:167]\nbuffer_load_dwordx4 v19,s[28:31],s46 offen lds\nM v[132:135],a[4:7],a[32:35],v[132:135]\ns_add_u32 m0,s50,61440\n" \
"ds_read_b64_tr_b16 a[60:61],v11 offset:8704\nM v[136:139],a[8:11],a[32:35],v[136:139]\nds_read_b64_tr_b16 a[62:63],v12 offset:8704\nM v[168:171],a[8:11],a[36:39],v[168:171]\nds_read_b64_tr_b16 a[64:65],v9 offset:9216\nM a[232:235],a[8:11],a[40:43],a[232:235]\nbuffer_load_dwordx4 v20,s[28:31],s46 offen lds\nM a[200:203],a[8:11],a[44:47],a[200:203]\n" \
"s_add_u32 m0,s51,49152\nds_read_b64_tr_b16 a[66:67],v10 offset:9216\nM a[204:207],a[12:15],a[44:47],a[204:207]\ns_mov_b32 s46,1152\nds_read_b64_tr_b16 a[68:69],v11 offset:9216\nM a[236:239],a[12:15],a[40:43],a[236:239]\nds_read_b64_tr_b16 a[70:71],v12 offset:9216\nds_read_b64_tr_b16 a[72:73],v9 offset:9728\n" \
"M v[172:175],a[12:15],a[36:39],v[172:175]\nbuffer_load_dwordx4 v21,s[32:35],s47 offen offset:0 lds\nM v[140:143],a[12:15],a[32:35],v[140:143]\ns_add_u32 m0,s51,50048\nv_permlane16_swap_b32 v248,v250\nM v[144:147],a[16:19],a[32:35],v[144:147]\nds_read_b64_tr_b16 a[74:75],v10 offset:9728\nds_read_b64_tr_b16 a[76:77],v11 offset:9728\n" \
"M v[176:179],a[16:19],a[36:39],v[176:179]\nds_read_b64_tr_b16 a[78:79],v12 offset:9728\nv_mov_b32 v58,s10\nM a[240:243],a[16:19],a[40:43],a[240:243]\nds_read_b128 a[88:91],v8 offset:8192\nds_read_b128 a[92:95],v8 offset:12288\nM a[208:211],a[16:19],a[44:47],a[208:211]\nbuffer_load_dwordx4 v21,s[32:35],s47 offen offset:128 lds\n" \
"M a[212:215],a[20:23],a[44:47],a[212:215]\ns_add_u32 m0,s51,57344\nv_permlane16_swap_b32 v249,v251\nM a[244:247],a[20:23],a[40:43],a[244:247]\ns_mov_b32 s47,4980736\nv_mov_b32 v59,s11\nM v[180:183],a[20:23],a[36:39],v[180:183]\ns_waitcnt lgkmcnt(3)\n" \
"M v[148:151],a[20:23],a[32:35],v[148:151]\nbuffer_load_dwordx4 v21,s[32:35],s47 offen offset:0 lds\nM v[152:155],a[24:27],a[32:35],v[152:155]\ns_waitcnt vmcnt(16) lgkmcnt(0)\nM v[184:187],a[24:27],a[36:39],v[184:187]\ns_add_u32 m0,s51,58240\ns_barrier\nds_read_b64_tr_b16 a[0:1],v9 offset:16384\n" \
"M a[248:251],a[24:27],a[40:43],a[248:251]\ns_lshl_b32 s62,s6,20\nds_read_b64_tr_b16 a[2:3],v10 offset:16384\nds_read_b64_tr_b16 a[4:5],v11 offset:16384\nM a[216:219],a[24:27],a[44:47],a[216:219]\nbuffer_store_dwordx4 v[224:227],v60,s[36:39],s21 offen offset:0 sc1\nM a[220:223],a[28:31],a[44:47],a[220:223]\ns_add_u32 s21,s21,1048576\n" \
"ds_read_b128 a[44:47],v5 offset:12288\nds_read_b64_tr_b16 a[6:7],v12 offset:16384\nM a[252:255],a[28:31],a[40:43],a[252:255]\ns_add_u32 s62,s62,16777216\nds_read_b128 a[40:43],v5 offset:8192\nv_bfe_u32 v57,v4,4,3\nM v[188:191],a[28:31],a[36:39],v[188:191]\ns_add_u32 s40,s4,s62\n" \
"s_addc_u32 s41,s5,0\nds_read_b128 a[36:39],v5 offset:4096\nv_bfe_u32 v61,v4,4,4\nM v[156:159],a[28:31],a[32:35],v[156:159]\ns_lshl_b32 s62,s7,16\nds_read_b128 a[32:35],v5 offset:0\nv_mov_b32 v24,0\nM v[128:131],a[48:51],a[80:83],v[128:131]\n" \
"s_sub_u32 s63,983040,s62\nds_read_b64_tr_b16 a[8:9],v9 offset:16896\nv_mov_b32 v25,0\nM v[160:163],a[48:51],a[84:87],v[160:163]\ns_cmp_lt_u32 %2,2\ns_cselect_b32 s44,s63,s62\ns_cselect_b32 s42,1048576,s63\nds_read_b64_tr_b16 a[10:11],v10 offset:16896\n" \
"v_mov_b32 v26,0\nM a[224:227],a[48:51],a[88:91],a[224:227]\ns_and_b32 s63,%2,1\nds_read_b64_tr_b16 a[12:13],v11 offset:16896\nds_read_b64_tr_b16 a[14:15],v12 offset:16896\nM a[192:195],a[48:51],a[92:95],a[192:195]\ns_lshl_b32 s63,s63,12\nds_read_b64_tr_b16 a[16:17],v9 offset:17408\n" \
"v_add_u32 v22,s63,v4\nM a[196:199],a[52:55],a[92:95],a[196:199]\ns_and_b32 s41,s41,0xffff\nds_read_b64_tr_b16 a[18:19],v10 offset:17408\nv_mov_b32 v27,0\nM a[228:231],a[52:55],a[88:91],a[228:231]\ns_add_u32 s61,s44,32768\nds_read_b64_tr_b16 a[20:21],v11 offset:17408\n" \
"ds_read_b64_tr_b16 a[22:23],v12 offset:17408\nM v[164:167],a[52:55],a[84:87],v[164:167]\ns_lshl_b32 s62,s6,6\nds_read_b64_tr_b16 a[24:25],v9 offset:17920\nM v[132:135],a[52:55],a[80:83],v[132:135]\ns_add_u32 s62,s62,94375936\nds_read_b64_tr_b16 a[26:27],v10 offset:17920\nv_lshl_add_u32 v57,v57,3,s62\n" \
"M v[136:139],a[56:59],a[80:83],v[136:139]\ns_lshl_b32 s62,s7,1\nds_read_b64_tr_b16 a[28:29],v11 offset:17920\nds_read_b64_tr_b16 a[30:31],v12 offset:17920\nM v[168:171],a[56:59],a[84:87],v[168:171]\ns_mov_b32 s4,s62\nds_read_b64_tr_b16 a[48:49],v9 offset:24576\nM a[232:235],a[56:59],a[88:91],a[232:235]\n" \
"s_cmp_lt_u32 %2,2\ns_cselect_b32 s62,s62,50\ns_cselect_b32 s54,8192,0\nds_read_b64_tr_b16 a[50:51],v10 offset:24576\nM a[200:203],a[56:59],a[92:95],a[200:203]\nbuffer_load_dwordx4 v21,s[32:35],s47 offen offset:128 lds\nM a[204:207],a[60:63],a[92:95],a[204:207]\ns_add_u32 m0,s50,65536\n" \
"ds_read_b64_tr_b16 a[52:53],v11 offset:24576\nM a[236:239],a[60:63],a[88:91],a[236:239]\ns_mov_b32 s47,5242880\nds_read_b64_tr_b16 a[54:55],v12 offset:24576\nM v[172:175],a[60:63],a[84:87],v[172:175]\ns_sub_u32 s55,s62,1\nds_read_b64_tr_b16 a[56:57],v9 offset:25088\nds_read_b64_tr_b16 a[58:59],v10 offset:25088\n" \
"M v[140:143],a[60:63],a[80:83],v[140:143]\nbuffer_load_dwordx4 v13,s[28:31],s46 offen lds\nM v[144:147],a[64:67],a[80:83],v[144:147]\ns_add_u32 m0,s50,69632\nM v[176:179],a[64:67],a[84:87],v[176:179]\ns_lshl_b32 s70,s62,1\nds_read_b64_tr_b16 a[60:61],v11 offset:25088\nds_read_b64_tr_b16 a[62:63],v12 offset:25088\n" \
"M a[240:243],a[64:67],a[88:91],a[240:243]\ns_cmp_eq_u32 s62,0\ns_cselect_b64 s[66:67],-1,0\nM a[208:211],a[64:67],a[92:95],a[208:211]\nbuffer_load_dwordx4 v14,s[28:31],s46 offen lds\nM a[212:215],a[68:71],a[92:95],a[212:215]\ns_add_u32 m0,s50,73728\nds_read_b64_tr_b16 a[64:65],v9 offset:25600\n" \
"M a[244:247],a[68:71],a[88:91],a[244:247]\ns_lshl_b32 s62,s7,2\nds_read_b64_tr_b16 a[66:67],v10 offset:25600\nM v[180:183],a[68:71],a[84:87],v[180:183]\ns_sub_u32 s63,60,s62\nM v[148:151],a[68:71],a[80:83],v[148:151]\nbuffer_load_dwordx4 v15,s[28:31],s46 offen lds\nM v[152:155],a[72:75],a[80:83],v[152:155]\n" \
"s_add_u32 m0,s50,77824\nds_read_b64_tr_b16 a[68:69],v11 offset:25600\nM v[184:187],a[72:75],a[84:87],v[184:187]\ns_cmp_lt_u32 %2,2\ns_cselect_b32 s5,s62,s63\nds_read_b64_tr_b16 a[70:71],v12 offset:25600\nM a[248:251],a[72:75],a[88:91],a[248:251]\ns_and_b32 s63,%2,1\n" \
"M a[216:219],a[72:75],a[92:95],a[216:219]\nbuffer_load_dwordx4 v16,s[28:31],s46 offen lds\nM a[220:223],a[76:79],a[92:95],a[220:223]\ns_mov_b32 s46,1152\nds_read_b64_tr_b16 a[72:73],v9 offset:26112\nM a[252:255],a[76:79],a[88:91],a[252:255]\ns_add_u32 m0,s50,81920\nds_read_b64_tr_b16 a[74:75],v10 offset:26112\n" \
"ds_read_b128 a[88:91],v6 offset:8192\nM v[188:191],a[76:79],a[84:87],v[188:191]\ns_add_u32 s69,s62,s63\nds_read_b128 a[84:87],v6 offset:4096\nM v[156:159],a[76:79],a[80:83],v[156:159]\ns_waitcnt lgkmcnt(4)\nbuffer_load_dwordx4 v17,s[28:31],s46 offen lds\nM v[128:131],a[0:3],a[32:35],v[128:131]\n" \
"s_add_u32 m0,s50,86016\nM v[160:163],a[0:3],a[36:39],v[160:163]\ns_add_u32 s68,s69,s62\nds_read_b128 a[80:83],v6 offset:0\nM a[224:227],a[0:3],a[40:43],a[224:227]\ns_bfe_u32 s62,%1,0x20001\nds_read_b64_tr_b16 a[76:77],v11 offset:26112\nM a[192:195],a[0:3],a[44:47],a[192:195]\n" \
"buffer_store_dwordx4 v[232:235],v60,s[36:39],s21 offen offset:0 sc1\nM a[196:199],a[4:7],a[44:47],a[196:199]\ns_add_u32 s21,s21,1048576\nds_read_b64_tr_b16 a[78:79],v12 offset:26112\nds_read_b128 a[92:95],v6 offset:12288\nM a[228:231],a[4:7],a[40:43],a[228:231]\ns_lshr_b32 s63,%1,5\nM v[164:167],a[4:7],a[36:39],v[164:167]\n" \
"s_lshl_b32 s62,s62,21\nM v[132:135],a[4:7],a[32:35],v[132:135]\ns_lshl_b32 s63,s63,8\nM v[136:139],a[8:11],a[32:35],v[136:139]\ns_add_u32 s62,s62,s63\nM v[168:171],a[8:11],a[36:39],v[168:171]\ns_sub_u32 s86,8388608,s62\nM a[232:235],a[8:11],a[40:43],a[232:235]\n" \
"s_add_u32 s84,s84,s62\ns_addc_u32 s85,s85,0\nM a[200:203],a[8:11],a[44:47],a[200:203]\ns_and_b32 s63,%2,1\nM a[204:207],a[12:15],a[44:47],a[204:207]\ns_lshl_b32 s62,s63,4\nM a[236:239],a[12:15],a[40:43],a[236:239]\ns_and_b32 s63,%2,1\n" \
"M v[172:175],a[12:15],a[36:39],v[172:175]\ns_lshl_b32 s63,s63,14\nM v[140:143],a[12:15],a[32:35],v[140:143]\ns_add_u32 s63,s63,0x20000\nM v[144:147],a[16:19],a[32:35],v[144:147]\ns_cmp_lt_u32 %2,2\ns_cselect_b32 s65,0,-4096\nM v[176:179],a[16:19],a[36:39],v[176:179]\n" \
"s_cmp_lt_u32 %2,2\ns_cselect_b32 s49,1,-1\ns_cselect_b32 s45,-1,60\nM a[240:243],a[16:19],a[40:43],a[240:243]\ns_lshl_b32 s45,s45,14\nM a[208:211],a[16:19],a[44:47],a[208:211]\nbuffer_load_dwordx4 v18,s[28:31],s46 offen lds\nM a[212:215],a[20:23],a[44:47],a[212:215]\n" \
"s_add_u32 m0,s50,90112\nM a[244:247],a[20:23],a[40:43],a[244:247]\ns_lshl_b32 s49,s49,14\nM v[180:183],a[20:23],a[36:39],v[180:183]\ns_add_u32 s48,s45,8192\nM v[148:151],a[20:23],a[32:35],v[148:151]\nbuffer_load_dwordx4 v19,s[28:31],s46 offen lds\nM v[152:155],a[24:27],a[32:35],v[152:155]\n" \
"s_waitcnt vmcnt(13) lgkmcnt(0)\nM v[184:187],a[24:27],a[36:39],v[184:187]\ns_add_u32 m0,s50,94208\ns_barrier\nds_read_b64_tr_b16 a[0:1],v9 offset:32768\nM a[248:251],a[24:27],a[40:43],a[248:251]\ns_add_u32 s45,s45,s49\nds_read_b64_tr_b16 a[2:3],v10 offset:32768\n" \
"ds_read_b64_tr_b16 a[4:5],v11 offset:32768\nM a[216:219],a[24:27],a[44:47],a[216:219]\nbuffer_load_dwordx4 v20,s[28:31],s46 offen lds\nM a[220:223],a[28:31],a[44:47],a[220:223]\ns_add_u32 m0,s51,0\nds_read_b128 a[44:47],v5 offset:45056\nM a[252:255],a[28:31],a[40:43],a[252:255]\ns_mov_b32 s46,1280\n" \
"ds_read_b128 a[40:43],v5 offset:40960\nM v[188:191],a[28:31],a[36:39],v[188:191]\ns_add_u32 s48,s48,s49\nds_read_b128 a[36:39],v5 offset:36864\nM v[156:159],a[28:31],a[32:35],v[156:159]\nbuffer_load_dwordx4 v21,s[32:35],s47 offen offset:0 lds\nM v[128:131],a[48:51],a[80:83],v[128:131]\ns_add_u32 m0,s51,896\n" \
"M v[160:163],a[48:51],a[84:87],v[160:163]\ns_mov_b32 s52,-1\nds_read_b128 a[32:35],v5 offset:32768\nM a[224:227],a[48:51],a[88:91],a[224:227]\ns_min_u32 s56,s4,8\nds_read_b64_tr_b16 a[6:7],v12 offset:32768\nM a[192:195],a[48:51],a[92:95],a[192:195]\nbuffer_load_dwordx4 v21,s[32:35],s47 offen offset:128 lds\n" \
"M a[196:199],a[52:55],a[92:95],a[196:199]\ns_add_u32 m0,s51,8192\nds_read_b64_tr_b16 a[8:9],v9 offset:33280\nM a[228:231],a[52:55],a[88:91],a[228:231]\ns_mov_b32 s47,5505024\nds_read_b64_tr_b16 a[10:11],v10 offset:33280\nM v[164:167],a[52:55],a[84:87],v[164:167]\ns_mov_b32 s87,0x00020000\n" \
"ds_read_b64_tr_b16 a[12:13],v11 offset:33280\nds_read_b64_tr_b16 a[14:15],v12 offset:33280\nM v[132:135],a[52:55],a[80:83],v[132:135]\nbuffer_load_dwordx4 v21,s[32:35],s47 offen offset:0 lds\nM v[136:139],a[56:59],a[80:83],v[136:139]\ns_add_u32 m0,s51,9088\nM v[168:171],a[56:59],a[84:87],v[168:171]\ns_and_b32 s85,s85,0xffff\n" \
"ds_read_b64_tr_b16 a[16:17],v9 offset:33792\nds_read_b64_tr_b16 a[18:19],v10 offset:33792\nM a[232:235],a[56:59],a[88:91],a[232:235]\ns_lshl_b32 s72,%1,2\nds_read_b64_tr_b16 a[20:21],v11 offset:33792\nM a[200:203],a[56:59],a[92:95],a[200:203]\nbuffer_load_dwordx4 v21,s[32:35],s47 offen offset:128 lds\nM a[204:207],a[60:63],a[92:95],a[204:207]\n" \
"s_add_u32 m0,s50,0\nds_read_b64_tr_b16 a[22:23],v12 offset:33792\nM a[236:239],a[60:63],a[88:91],a[236:239]\ns_mov_b32 s47,5767168\nds_read_b64_tr_b16 a[24:25],v9 offset:34304\nM v[172:175],a[60:63],a[84:87],v[172:175]\ns_add_u32 s72,s72,%2\nds_read_b64_tr_b16 a[26:27],v10 offset:34304\n" \
"M v[140:143],a[60:63],a[80:83],v[140:143]\nbuffer_load_dwordx4 v13,s[28:31],s46 offen lds\nM v[144:147],a[64:67],a[80:83],v[144:147]\ns_add_u32 m0,s50,4096\nds_read_b64_tr_b16 a[28:29],v11 offset:34304\nM v[176:179],a[64:67],a[84:87],v[176:179]\ns_mul_i32 s72,s72,12288\nds_read_b64_tr_b16 a[30:31],v12 offset:34304\n" \
"M a[240:243],a[64:67],a[88:91],a[240:243]\ns_add_u32 s72,s72,109060096\nds_read_b64_tr_b16 a[48:49],v9 offset:40960\nds_read_b64_tr_b16 a[50:51],v10 offset:40960\nM a[208:211],a[64:67],a[92:95],a[208:211]\nbuffer_load_dwordx4 v14,s[28:31],s46 offen lds\nM a[212:215],a[68:71],a[92:95],a[212:215]\ns_add_u32 m0,s50,8192\n" \
"M a[244:247],a[68:71],a[88:91],a[244:247]\ns_lshl_b32 s68,s68,5\nds_read_b64_tr_b16 a[52:53],v11 offset:40960\nds_read_b64_tr_b16 a[54:55],v12 offset:40960\nM v[180:183],a[68:71],a[84:87],v[180:183]\ns_add_u32 s68,s68,s6\nds_read_b64_tr_b16 a[56:57],v9 offset:41472\nM v[148:151],a[68:71],a[80:83],v[148:151]\n" \
"buffer_load_dwordx4 v15,s[28:31],s46 offen lds\nM v[152:155],a[72:75],a[80:83],v[152:155]\ns_add_u32 m0,s50,12288\nds_read_b64_tr_b16 a[58:59],v10 offset:41472\nM v[184:187],a[72:75],a[84:87],v[184:187]\ns_lshl_b32 s68,s68,12\nds_read_b64_tr_b16 a[60:61],v11 offset:41472\nM a[248:251],a[72:75],a[88:91],a[248:251]\n" \
"s_add_u32 s68,s68,50331648\nds_read_b64_tr_b16 a[62:63],v12 offset:41472\nM a[216:219],a[72:75],a[92:95],a[216:219]\nbuffer_load_dwordx4 v16,s[28:31],s46 offen lds\nM a[220:223],a[76:79],a[92:95],a[220:223]\ns_mov_b32 s46,1280\nds_read_b64_tr_b16 a[64:65],v9 offset:41984\nM a[252:255],a[76:79],a[88:91],a[252:255]\n" \
"s_add_u32 m0,s50,16384\nds_read_b64_tr_b16 a[66:67],v10 offset:41984\nM v[188:191],a[76:79],a[84:87],v[188:191]\ns_lshl_b32 s69,s69,5\nds_read_b128 a[84:87],v6 offset:36864\nM v[156:159],a[76:79],a[80:83],v[156:159]\ns_waitcnt lgkmcnt(4)\nbuffer_store_dwordx4 v[240:243],v60,s[36:39],s21 offen offset:0 sc1\n" \
"M v[128:131],a[0:3],a[32:35],v[128:131]\ns_add_u32 s21,s21,1048576\nds_read_b128 a[80:83],v6 offset:32768\nds_read_b64_tr_b16 a[68:69],v11 offset:41984\nM v[160:163],a[0:3],a[36:39],v[160:163]\ns_add_u32 s69,s69,s6\nds_read_b64_tr_b16 a[70:71],v12 offset:41984\nM a[224:227],a[0:3],a[40:43],a[224:227]\n" \
"s_lshl_b32 s69,s69,3\nds_read_b64_tr_b16 a[72:73],v9 offset:42496\nM a[192:195],a[0:3],a[44:47],a[192:195]\ns_add_u32 s69,s69,94388224\nds_read_b64_tr_b16 a[74:75],v10 offset:42496\nds_read_b64_tr_b16 a[76:77],v11 offset:42496\nM a[196:199],a[4:7],a[44:47],a[196:199]\ns_and_b32 s89,s89,0xffff\n" \
"ds_read_b64_tr_b16 a[78:79],v12 offset:42496\nM a[228:231],a[4:7],a[40:43],a[228:231]\ns_mov_b32 s90,4194304\nds_read_b128 a[88:91],v6 offset:40960\nM v[164:167],a[4:7],a[36:39],v[164:167]\ns_mov_b32 s91,0x00020000\nds_read_b128 a[92:95],v6 offset:45056\nM v[132:135],a[4:7],a[32:35],v[132:135]\n" \
"s_lshl_b32 s71,s6,20\nM v[136:139],a[8:11],a[32:35],v[136:139]\ns_mov_b32 s75,0\nM v[168:171],a[8:11],a[36:39],v[168:171]\ns_add_u32 s71,s71,16777216\nM a[232:235],a[8:11],a[40:43],a[232:235]\nM a[200:203],a[8:11],a[44:47],a[200:203]\nM a[204:207],a[12:15],a[44:47],a[204:207]\n" \
"M a[236:239],a[12:15],a[40:43],a[236:239]\nM v[172:175],a[12:15],a[36:39],v[172:175]\nM v[140:143],a[12:15],a[32:35],v[140:143]\nbuffer_load_dwordx4 v17,s[28:31],s46 offen lds\nM v[144:147],a[16:19],a[32:35],v[144:147]\ns_add_u32 m0,s50,20480\nM v[176:179],a[16:19],a[36:39],v[176:179]\nM a[240:243],a[16:19],a[40:43],a[240:243]\n" \
"M a[208:211],a[16:19],a[44:47],a[208:211]\nbuffer_load_dwordx4 v18,s[28:31],s46 offen lds\nM a[212:215],a[20:23],a[44:47],a[212:215]\ns_add_u32 m0,s50,24576\nM a[244:247],a[20:23],a[40:43],a[244:247]\nM v[180:183],a[20:23],a[36:39],v[180:183]\nM v[148:151],a[20:23],a[32:35],v[148:151]\nbuffer_load_dwordx4 v19,s[28:31],s46 offen lds\n" \
"M v[152:155],a[24:27],a[32:35],v[152:155]\ns_waitcnt vmcnt(12) lgkmcnt(0)\nM v[184:187],a[24:27],a[36:39],v[184:187]\ns_add_u32 m0,s50,28672\ns_barrier\nds_read_b64_tr_b16 a[0:1],v9 offset:49152\nM a[248:251],a[24:27],a[40:43],a[248:251]\nds_read_b64_tr_b16 a[2:3],v10 offset:49152\n" \
"ds_read_b64_tr_b16 a[4:5],v11 offset:49152\nM a[216:219],a[24:27],a[44:47],a[216:219]\nbuffer_load_dwordx4 v20,s[28:31],s46 offen lds\nM a[220:223],a[28:31],a[44:47],a[220:223]\ns_add_u32 m0,s51,16384\nds_read_b128 a[44:47],v7 offset:12288\nM a[252:255],a[28:31],a[40:43],a[252:255]\ns_mov_b32 s46,1408\n" \
"ds_read_b128 a[40:43],v7 offset:8192\nM v[188:191],a[28:31],a[36:39],v[188:191]\nds_read_b128 a[36:39],v7 offset:4096\nM v[156:159],a[28:31],a[32:35],v[156:159]\nbuffer_load_dwordx4 v21,s[32:35],s47 offen offset:0 lds\nM v[128:131],a[48:51],a[80:83],v[128:131]\ns_add_u32 m0,s51,17280\nM v[160:163],a[48:51],a[84:87],v[160:163]\n" \
"ds_read_b128 a[32:35],v7 offset:0\nM a[224:227],a[48:51],a[88:91],a[224:227]\nds_read_b64_tr_b16 a[6:7],v12 offset:49152\nM a[192:195],a[48:51],a[92:95],a[192:195]\nbuffer_load_dwordx4 v21,s[32:35],s47 offen offset:128 lds\nM a[196:199],a[52:55],a[92:95],a[196:199]\ns_add_u32 m0,s51,24576\nds_read_b64_tr_b16 a[8:9],v9 offset:49664\n" \
"M a[228:231],a[52:55],a[88:91],a[228:231]\ns_mov_b32 s47,6029312\nds_read_b64_tr_b16 a[10:11],v10 offset:49664\nM v[164:167],a[52:55],a[84:87],v[164:167]\nds_read_b64_tr_b16 a[12:13],v11 offset:49664\nds_read_b64_tr_b16 a[14:15],v12 offset:49664\nM v[132:135],a[52:55],a[80:83],v[132:135]\nbuffer_load_dwordx4 v21,s[32:35],s47 offen offset:0 lds\n" \
"M v[136:139],a[56:59],a[80:83],v[136:139]\ns_add_u32 m0,s51,25472\nM v[168:171],a[56:59],a[84:87],v[168:171]\nds_read_b64_tr_b16 a[16:17],v9 offset:50176\nds_read_b64_tr_b16 a[18:19],v10 offset:50176\nM a[232:235],a[56:59],a[88:91],a[232:235]\nds_read_b64_tr_b16 a[20:21],v11 offset:50176\nM a[200:203],a[56:59],a[92:95],a[200:203]\n" \
"buffer_store_dwordx4 v[120:123],v4,s[36:39],s96 offen offset:0 sc1\nM a[204:207],a[60:63],a[92:95],a[204:207]\nds_read_b64_tr_b16 a[22:23],v12 offset:50176\nds_read_b64_tr_b16 a[24:25],v9 offset:50688\nM a[236:239],a[60:63],a[88:91],a[236:239]\nds_read_b64_tr_b16 a[26:27],v10 offset:50688\nM v[172:175],a[60:63],a[84:87],v[172:175]\nds_read_b64_tr_b16 a[28:29],v11 offset:50688\n" \
"ds_read_b64_tr_b16 a[30:31],v12 offset:50688\nM v[140:143],a[60:63],a[80:83],v[140:143]\nbuffer_load_dwordx4 v21,s[32:35],s47 offen offset:128 lds\nM v[144:147],a[64:67],a[80:83],v[144:147]\ns_add_u32 m0,s50,32768\nM v[176:179],a[64:67],a[84:87],v[176:179]\ns_mov_b32 s47,6291456\nds_read_b64_tr_b16 a[48:49],v9 offset:57344\n" \
"ds_read_b64_tr_b16 a[50:51],v10 offset:57344\nM a[240:243],a[64:67],a[88:91],a[240:243]\nds_read_b64_tr_b16 a[52:53],v11 offset:57344\nM a[208:211],a[64:67],a[92:95],a[208:211]\nbuffer_load_dwordx4 v13,s[28:31],s46 offen lds\nM a[212:215],a[68:71],a[92:95],a[212:215]\ns_add_u32 m0,s50,36864\nds_read_b64_tr_b16 a[54:55],v12 offset:57344\n" \
"M a[244:247],a[68:71],a[88:91],a[244:247]\nds_read_b64_tr_b16 a[56:57],v9 offset:57856\nM v[180:183],a[68:71],a[84:87],v[180:183]\nds_read_b64_tr_b16 a[58:59],v10 offset:57856\nM v[148:151],a[68:71],a[80:83],v[148:151]\nbuffer_load_dwordx4 v14,s[28:31],s46 offen lds\nM v[152:155],a[72:75],a[80:83],v[152:155]\ns_add_u32 m0,s50,40960\n" \
"ds_read_b64_tr_b16 a[60:61],v11 offset:57856\nM v[184:187],a[72:75],a[84:87],v[184:187]\nds_read_b64_tr_b16 a[62:63],v12 offset:57856\nM a[248:251],a[72:75],a[88:91],a[248:251]\nds_read_b64_tr_b16 a[64:65],v9 offset:58368\nds_read_b64_tr_b16 a[66:67],v10 offset:58368\nM a[216:219],a[72:75],a[92:95],a[216:219]\nbuffer_load_dwordx4 v15,s[28:31],s46 offen lds\n" \
"M a[220:223],a[76:79],a[92:95],a[220:223]\ns_add_u32 m0,s50,45056\nM a[252:255],a[76:79],a[88:91],a[252:255]\nds_read_b64_tr_b16 a[68:69],v11 offset:58368\nds_read_b64_tr_b16 a[70:71],v12 offset:58368\nM v[188:191],a[76:79],a[84:87],v[188:191]\ns_waitcnt lgkmcnt(5)\nds_read_b128 a[84:87],v8 offset:4096\n" \
"M v[156:159],a[76:79],a[80:83],v[156:159]\nbuffer_load_dwordx4 v16,s[28:31],s46 offen lds\nM v[128:131],a[0:3],a[32:35],v[128:131]\ns_mov_b32 s46,1408\nM v[160:163],a[0:3],a[36:39],v[160:163]\ns_add_u32 m0,s50,49152\nds_read_b128 a[80:83],v8 offset:0\nM a[224:227],a[0:3],a[40:43],a[224:227]\n" \
"ds_read_b64_tr_b16 a[72:73],v9 offset:58880\nds_read_b64_tr_b16 a[74:75],v10 offset:58880\nM a[192:195],a[0:3],a[44:47],a[192:195]\nbuffer_load_dwordx4 v17,s[28:31],s46 offen lds\nM a[196:199],a[4:7],a[44:47],a[196:199]\ns_add_u32 m0,s50,53248\nM a[228:231],a[4:7],a[40:43],a[228:231]\nds_read_b64_tr_b16 a[76:77],v11 offset:58880\n" \
"ds_read_b64_tr_b16 a[78:79],v12 offset:58880\nM v[164:167],a[4:7],a[36:39],v[164:167]\nds_read_b128 a[88:91],v8 offset:8192\nM v[132:135],a[4:7],a[32:35],v[132:135]\nbuffer_load_dwordx4 v18,s[28:31],s46 offen lds\nM v[136:139],a[8:11],a[32:35],v[136:139]\ns_add_u32 m0,s50,57344\nM v[168:171],a[8:11],a[36:39],v[168:171]\n" \
"ds_read_b128 a[92:95],v8 offset:12288\nM a[232:235],a[8:11],a[40:43],a[232:235]\nM a[200:203],a[8:11],a[44:47],a[200:203]\nLG12_%=:\ns_cbranch_execz LHA155V_%=\nbuffer_load_dwordx4 v19,s[28:31],s46 offen lds\nM a[204:207],a[12:15],a[44:47],a[204:207]\ns_add_u32 m0,s50,61440\n" \
"M a[236:239],a[12:15],a[40:43],a[236:239]\nM v[172:175],a[12:15],a[36:39],v[172:175]\nM v[140:143],a[12:15],a[32:35],v[140:143]\nbuffer_load_dwordx4 v20,s[28:31],s46 offen lds\nM v[144:147],a[16:19],a[32:35],v[144:147]\ns_add_u32 m0,s51,32768\nM v[176:179],a[16:19],a[36:39],v[176:179]\ns_mov_b32 s46,1536\n" \
"M a[240:243],a[16:19],a[40:43],a[240:243]\nM a[208:211],a[16:19],a[44:47],a[208:211]\nbuffer_load_dwordx4 v21,s[32:35],s47 offen offset:0 lds\nM a[212:215],a[20:23],a[44:47],a[212:215]\ns_add_u32 m0,s51,33664\nM a[244:247],a[20:23],a[40:43],a[244:247]\nM v[180:183],a[20:23],a[36:39],v[180:183]\nM v[148:151],a[20:23],a[32:35],v[148:151]\n" \
"buffer_store_dwordx4 v[248:251],v60,s[36:39],s21 offen offset:0 sc1\nM v[152:155],a[24:27],a[32:35],v[152:155]\ns_waitcnt vmcnt(15) lgkmcnt(0)\nv_lshrrev_b32 v60,8,v4\nv_lshlrev_b32 v60,2,v60\nM v[184:187],a[24:27],a[36:39],v[184:187]\ns_barrier\nds_read_b64_tr_b16 a[0:1],v9 offset:0\n" \
"M a[248:251],a[24:27],a[40:43],a[248:251]\nds_read_b64_tr_b16 a[2:3],v10 offset:0\nds_read_b64_tr_b16 a[4:5],v11 offset:0\nM a[216:219],a[24:27],a[44:47],a[216:219]\nds_read_b64_tr_b16 a[6:7],v12 offset:0\nv_sub_u32 v60,v60,v61\nM a[220:223],a[28:31],a[44:47],a[220:223]\nds_read_b128 a[44:47],v5 offset:12288\n" \
"v_subrev_u32 v60,s62,v60\nM a[252:255],a[28:31],a[40:43],a[252:255]\ns_xor_b32 s62,s22,0x2000\nds_read_b128 a[40:43],v5 offset:8192\nv_mov_b32 v61,0xff800000\nM v[188:191],a[28:31],a[36:39],v[188:191]\ns_add_u32 s62,s62,84992\nds_read_b128 a[36:39],v5 offset:4096\n" \
"v_add_u32 v23,s62,v4\nM v[156:159],a[28:31],a[32:35],v[156:159]\ns_and_b32 s62,%2,1\nds_read_b128 a[32:35],v5 offset:0\nv_cmp_lt_i32 vcc,0,v60\nM v[128:131],a[48:51],a[80:83],v[128:131]\ns_lshl_b32 s62,s62,15\nds_read_b64_tr_b16 a[8:9],v9 offset:512\n" \
"ds_read_b64_tr_b16 a[10:11],v10 offset:512\nM v[160:163],a[48:51],a[84:87],v[160:163]\ns_add_u32 s62,s62,s54\nds_read_b64_tr_b16 a[12:13],v11 offset:512\nv_cndmask_b32 v48,0,v61,vcc\nM a[224:227],a[48:51],a[88:91],a[224:227]\ns_add_u32 s62,s62,32768\nds_read_b64_tr_b16 a[14:15],v12 offset:512\n" \
"v_add_u32 v1,s62,v4\nM a[192:195],a[48:51],a[92:95],a[192:195]\nds_read_b64_tr_b16 a[16:17],v9 offset:1024\nds_read_b64_tr_b16 a[18:19],v10 offset:1024\nM a[196:199],a[52:55],a[92:95],a[196:199]\nds_read_b64_tr_b16 a[20:21],v11 offset:1024\nv_cmp_lt_i32 vcc,-1,v60\nM a[228:231],a[52:55],a[88:91],a[228:231]\n" \
"ds_read_b64_tr_b16 a[22:23],v12 offset:1024\nv_cndmask_b32 v49,0,v61,vcc\nM v[164:167],a[52:55],a[84:87],v[164:167]\nds_read_b64_tr_b16 a[24:25],v9 offset:1536\nds_read_b64_tr_b16 a[26:27],v10 offset:1536\nM v[132:135],a[52:55],a[80:83],v[132:135]\nbuffer_load_dwordx4 v21,s[32:35],s47 offen offset:128 lds\nM v[136:139],a[56:59],a[80:83],v[136:139]\n" \
"s_add_u32 m0,s51,40960\nv_cmp_lt_i32 vcc,-2,v60\nM v[168:171],a[56:59],a[84:87],v[168:171]\ns_mov_b32 s47,6553600\nds_read_b64_tr_b16 a[28:29],v11 offset:1536\nds_read_b64_tr_b16 a[30:31],v12 offset:1536\nM a[232:235],a[56:59],a[88:91],a[232:235]\nds_read_b64_tr_b16 a[48:49],v9 offset:8192\n" \
"v_cndmask_b32 v50,0,v61,vcc\nM a[200:203],a[56:59],a[92:95],a[200:203]\ns_mov_b64 exec,0xff\nglobal_load_dword v0,v2,s[100:101]\ns_mov_b64 exec,-1\nM a[204:207],a[60:63],a[92:95],a[204:207]\nds_read_b64_tr_b16 a[50:51],v10 offset:8192\nds_read_b64_tr_b16 a[52:53],v11 offset:8192\n" \
"M a[236:239],a[60:63],a[88:91],a[236:239]\nbuffer_load_dwordx4 v21,s[32:35],s47 offen offset:0 lds\nM v[172:175],a[60:63],a[84:87],v[172:175]\ns_add_u32 m0,s51,41856\nds_read_b64_tr_b16 a[54:55],v12 offset:8192\nv_cmp_lt_i32 vcc,-3,v60\nM v[140:143],a[60:63],a[80:83],v[140:143]\nds_read_b64_tr_b16 a[56:57],v9 offset:8704\n" \
"v_cndmask_b32 v51,0,v61,vcc\nM v[144:147],a[64:67],a[80:83],v[144:147]\nds_read_b64_tr_b16 a[58:59],v10 offset:8704\nv_cmp_lt_i32 vcc,-16,v60\nM v[176:179],a[64:67],a[84:87],v[176:179]\nbuffer_load_dwordx4 v21,s[32:35],s47 offen offset:128 lds\nM a[240:243],a[64:67],a[88:91],a[240:243]\ns_add_u32 m0,s50,65536\n" \
"ds_read_b64_tr_b16 a[60:61],v11 offset:8704\nv_cndmask_b32 v52,0,v61,vcc\nM a[208:211],a[64:67],a[92:95],a[208:211]\ns_mov_b32 s47,6815744\nds_read_b64_tr_b16 a[62:63],v12 offset:8704\nv_cmp_lt_i32 vcc,-17,v60\nM a[212:215],a[68:71],a[92:95],a[212:215]\nds_read_b64_tr_b16 a[64:65],v9 offset:9216\n" \
"ds_read_b64_tr_b16 a[66:67],v10 offset:9216\nM a[244:247],a[68:71],a[88:91],a[244:247]\nbuffer_load_dwordx4 v13,s[28:31],s46 offen lds\nM v[180:183],a[68:71],a[84:87],v[180:183]\ns_add_u32 m0,s50,69632\nv_cndmask_b32 v53,0,v61,vcc\nv_cmp_lt_i32 vcc,-18,v60\nM v[148:151],a[68:71],a[80:83],v[148:151]\n" \
"ds_read_b64_tr_b16 a[68:69],v11 offset:9216\nds_read_b64_tr_b16 a[70:71],v12 offset:9216\nM v[152:155],a[72:75],a[80:83],v[152:155]\nv_cndmask_b32 v54,0,v61,vcc\nv_cmp_lt_i32 vcc,-19,v60\nM v[184:187],a[72:75],a[84:87],v[184:187]\nbuffer_load_dwordx4 v14,s[28:31],s46 offen lds\nM a[248:251],a[72:75],a[88:91],a[248:251]\n" \
"s_add_u32 m0,s50,73728\nv_cndmask_b32 v55,0,v61,vcc\nM a[216:219],a[72:75],a[92:95],a[216:219]\nds_read_b64_tr_b16 a[72:73],v9 offset:9728\nds_read_b64_tr_b16 a[74:75],v10 offset:9728\nM a[220:223],a[76:79],a[92:95],a[220:223]\nds_read_b128 a[92:95],v6 offset:12288\nM a[252:255],a[76:79],a[88:91],a[252:255]\n" \
"buffer_load_dwordx4 v15,s[28:31],s46 offen lds\nM v[188:191],a[76:79],a[84:87],v[188:191]\ns_add_u32 m0,s50,77824\nds_read_b128 a[84:87],v6 offset:4096\nM v[156:159],a[76:79],a[80:83],v[156:159]\ns_waitcnt lgkmcnt(4)\nds_read_b128 a[80:83],v6 offset:0\nM v[128:131],a[0:3],a[32:35],v[128:131]\n" \
"ds_read_b64_tr_b16 a[76:77],v11 offset:9728\nM v[160:163],a[0:3],a[36:39],v[160:163]\nbuffer_load_dwordx4 v16,s[28:31],s46 offen lds\nM a[224:227],a[0:3],a[40:43],a[224:227]\ns_add_u32 m0,s50,81920\nM a[192:195],a[0:3],a[44:47],a[192:195]\ns_mov_b32 s46,1536\nds_read_b64_tr_b16 a[78:79],v12 offset:9728\n" \
"ds_read_b128 a[88:91],v6 offset:8192\nM a[196:199],a[4:7],a[44:47],a[196:199]\nM a[228:231],a[4:7],a[40:43],a[228:231]\nbuffer_load_dwordx4 v17,s[28:31],s46 offen lds\nM v[164:167],a[4:7],a[36:39],v[164:167]\ns_add_u32 m0,s50,86016\nM v[132:135],a[4:7],a[32:35],v[132:135]\nM v[136:139],a[8:11],a[32:35],v[136:139]\n" \
"M v[168:171],a[8:11],a[36:39],v[168:171]\nbuffer_load_dwordx4 v18,s[28:31],s46 offen lds\nM a[232:235],a[8:11],a[40:43],a[232:235]\ns_add_u32 m0,s50,90112\nM a[200:203],a[8:11],a[44:47],a[200:203]\nM a[204:207],a[12:15],a[44:47],a[204:207]\nM a[236:239],a[12:15],a[40:43],a[236:239]\nbuffer_load_dwordx4 v19,s[28:31],s46 offen lds\n" \
"M v[172:175],a[12:15],a[36:39],v[172:175]\ns_add_u32 m0,s50,94208\nM v[140:143],a[12:15],a[32:35],v[140:143]\nM v[144:147],a[16:19],a[32:35],v[144:147]\nM v[176:179],a[16:19],a[36:39],v[176:179]\nbuffer_load_dwordx4 v20,s[28:31],s46 offen lds\nM a[240:243],a[16:19],a[40:43],a[240:243]\ns_add_u32 m0,s51,49152\n" \
"M a[208:211],a[16:19],a[44:47],a[208:211]\ns_mov_b32 s46,1664\nM a[212:215],a[20:23],a[44:47],a[212:215]\nM a[244:247],a[20:23],a[40:43],a[244:247]\nbuffer_load_dwordx4 v21,s[32:35],s47 offen offset:0 lds\nM v[180:183],a[20:23],a[36:39],v[180:183]\ns_add_u32 m0,s51,50048\nM v[148:151],a[20:23],a[32:35],v[148:151]\n" \
"M v[152:155],a[24:27],a[32:35],v[152:155]\ns_waitcnt vmcnt(15) lgkmcnt(0)\nM v[184:187],a[24:27],a[36:39],v[184:187]\ns_barrier\nds_read_b64_tr_b16 a[0:1],v9 offset:16384\nM a[248:251],a[24:27],a[40:43],a[248:251]\nbuffer_load_dwordx4 v21,s[32:35],s47 offen offset:128 lds\nM a[216:219],a[24:27],a[44:47],a[216:219]\n" \
"s_add_u32 m0,s51,57344\nds_read_b64_tr_b16 a[2:3],v10 offset:16384\nM a[220:223],a[28:31],a[44:47],a[220:223]\ns_mov_b32 s47,7077888\nds_read_b128 a[44:47],v5 offset:45056\nM a[252:255],a[28:31],a[40:43],a[252:255]\nds_read_b128 a[40:43],v5 offset:40960\nM v[188:191],a[28:31],a[36:39],v[188:191]\n" \
"buffer_load_dwordx4 v21,s[32:35],s47 offen offset:0 lds\nM v[156:159],a[28:31],a[32:35],v[156:159]\ns_add_u32 m0,s51,58240\nds_read_b128 a[32:35],v5 offset:32768\nM v[128:131],a[48:51],a[80:83],v[128:131]\nds_read_b128 a[36:39],v5 offset:36864\nM v[160:163],a[48:51],a[84:87],v[160:163]\nds_read_b64_tr_b16 a[4:5],v11 offset:16384\n" \
"M a[224:227],a[48:51],a[88:91],a[224:227]\nbuffer_load_dwordx4 v21,s[32:35],s47 offen offset:128 lds\nM a[192:195],a[48:51],a[92:95],a[192:195]\ns_add_u32 m0,s50,0\nds_read_b64_tr_b16 a[6:7],v12 offset:16384\nM a[196:199],a[52:55],a[92:95],a[196:199]\ns_mov_b32 s47,7340032\nds_read_b64_tr_b16 a[8:9],v9 offset:16896\n" \
"M a[228:231],a[52:55],a[88:91],a[228:231]\nds_read_b64_tr_b16 a[10:11],v10 offset:16896\nM v[164:167],a[52:55],a[84:87],v[164:167]\nbuffer_load_dwordx4 v13,s[28:31],s46 offen lds\nM v[132:135],a[52:55],a[80:83],v[132:135]\ns_add_u32 m0,s50,4096\nds_read_b64_tr_b16 a[12:13],v11 offset:16896\nM v[136:139],a[56:59],a[80:83],v[136:139]\n" \
"ds_read_b64_tr_b16 a[14:15],v12 offset:16896\nM v[168:171],a[56:59],a[84:87],v[168:171]\nds_read_b64_tr_b16 a[16:17],v9 offset:17408\nds_read_b64_tr_b16 a[18:19],v10 offset:17408\nM a[232:235],a[56:59],a[88:91],a[232:235]\nbuffer_load_dwordx4 v14,s[28:31],s46 offen lds\nM a[200:203],a[56:59],a[92:95],a[200:203]\ns_add_u32 m0,s50,8192\n" \
"M a[204:207],a[60:63],a[92:95],a[204:207]\nds_read_b64_tr_b16 a[20:21],v11 offset:17408\nds_read_b64_tr_b16 a[22:23],v12 offset:17408\nM a[236:239],a[60:63],a[88:91],a[236:239]\nds_read_b64_tr_b16 a[24:25],v9 offset:17920\nM v[172:175],a[60:63],a[84:87],v[172:175]\nbuffer_load_dwordx4 v15,s[28:31],s46 offen lds\nM v[140:143],a[60:63],a[80:83],v[140:143]\n" \
"s_add_u32 m0,s50,12288\nds_read_b64_tr_b16 a[26:27],v10 offset:17920\nM v[144:147],a[64:67],a[80:83],v[144:147]\nds_read_b64_tr_b16 a[28:29],v11 offset:17920\nM v[176:179],a[64:67],a[84:87],v[176:179]\nds_read_b64_tr_b16 a[30:31],v12 offset:17920\nM a[240:243],a[64:67],a[88:91],a[240:243]\nbuffer_load_dwordx4 v16,s[28:31],s46 offen lds\n" \
"M a[208:211],a[64:67],a[92:95],a[208:211]\ns_add_u32 m0,s50,16384\nds_read_b64_tr_b16 a[48:49],v9 offset:24576\nM a[212:215],a[68:71],a[92:95],a[212:215]\ns_mov_b32 s46,1664\nds_read_b64_tr_b16 a[50:51],v10 offset:24576\nM a[244:247],a[68:71],a[88:91],a[244:247]\nds_read_b64_tr_b16 a[52:53],v11 offset:24576\n" \
"ds_read_b64_tr_b16 a[54:55],v12 offset:24576\nM v[180:183],a[68:71],a[84:87],v[180:183]\nbuffer_load_dwordx4 v17,s[28:31],s46 offen lds\nM v[148:151],a[68:71],a[80:83],v[148:151]\ns_add_u32 m0,s50,20480\nM v[152:155],a[72:75],a[80:83],v[152:155]\nds_read_b64_tr_b16 a[56:57],v9 offset:25088\nds_read_b64_tr_b16 a[58:59],v10 offset:25088\n" \
"M v[184:187],a[72:75],a[84:87],v[184:187]\nds_read_b64_tr_b16 a[60:61],v11 offset:25088\nM a[248:251],a[72:75],a[88:91],a[248:251]\nbuffer_load_dwordx4 v18,s[28:31],s46 offen lds\nM a[216:219],a[72:75],a[92:95],a[216:219]\ns_add_u32 m0,s50,24576\nds_read_b64_tr_b16 a[62:63],v12 offset:25088\nM a[220:223],a[76:79],a[92:95],a[220:223]\n" \
"ds_read_b64_tr_b16 a[64:65],v9 offset:25600\nM a[252:255],a[76:79],a[88:91],a[252:255]\ns_waitcnt lgkmcnt(5)\nds_read_b64_tr_b16 a[66:67],v10 offset:25600\nM v[188:191],a[76:79],a[84:87],v[188:191]\nbuffer_load_dwordx4 v19,s[28:31],s46 offen lds\nM v[156:159],a[76:79],a[80:83],v[156:159]\ns_add_u32 m0,s50,28672\n" \
"ds_read_b128 a[80:83],v6 offset:32768\nM v[128:131],a[0:3],a[32:35],v[128:131]\nds_read_b128 a[84:87],v6 offset:36864\nM v[160:163],a[0:3],a[36:39],v[160:163]\nds_read_b64_tr_b16 a[68:69],v11 offset:25600\nM a[224:227],a[0:3],a[40:43],a[224:227]\nbuffer_load_dwordx4 v20,s[28:31],s46 offen lds\nM a[192:195],a[0:3],a[44:47],a[192:195]\n" \
"s_add_u32 m0,s51,0\nds_read_b64_tr_b16 a[70:71],v12 offset:25600\nM a[196:199],a[4:7],a[44:47],a[196:199]\ns_mov_b32 s46,1792\nds_read_b64_tr_b16 a[72:73],v9 offset:26112\nM a[228:231],a[4:7],a[40:43],a[228:231]\nds_read_b64_tr_b16 a[74:75],v10 offset:26112\nM v[164:167],a[4:7],a[36:39],v[164:167]\n" \
"buffer_load_dwordx4 v21,s[32:35],s47 offen offset:0 lds\nM v[132:135],a[4:7],a[32:35],v[132:135]\ns_add_u32 m0,s51,896\nds_read_b64_tr_b16 a[76:77],v11 offset:26112\nM v[136:139],a[8:11],a[32:35],v[136:139]\nds_read_b64_tr_b16 a[78:79],v12 offset:26112\nM v[168:171],a[8:11],a[36:39],v[168:171]\nds_read_b128 a[88:91],v6 offset:40960\n" \
"M a[232:235],a[8:11],a[40:43],a[232:235]\nbuffer_load_dwordx4 v21,s[32:35],s47 offen offset:128 lds\nM a[200:203],a[8:11],a[44:47],a[200:203]\ns_add_u32 m0,s51,8192\nds_read_b128 a[92:95],v6 offset:45056\nM a[204:207],a[12:15],a[44:47],a[204:207]\ns_mov_b32 s47,7602176\nM a[236:239],a[12:15],a[40:43],a[236:239]\n" \
"M v[172:175],a[12:15],a[36:39],v[172:175]\nbuffer_load_dwordx4 v21,s[32:35],s47 offen offset:0 lds\nM v[140:143],a[12:15],a[32:35],v[140:143]\ns_add_u32 m0,s51,9088\nM v[144:147],a[16:19],a[32:35],v[144:147]\nM v[176:179],a[16:19],a[36:39],v[176:179]\nM a[240:243],a[16:19],a[40:43],a[240:243]\nM a[208:211],a[16:19],a[44:47],a[208:211]\n" \
"buffer_load_dwordx4 v21,s[32:35],s47 offen offset:128 lds\nM a[212:215],a[20:23],a[44:47],a[212:215]\ns_add_u32 m0,s50,32768\nM a[244:247],a[20:23],a[40:43],a[244:247]\ns_mov_b32 s47,7864320\nM v[180:183],a[20:23],a[36:39],v[180:183]\nM v[148:151],a[20:23],a[32:35],v[148:151]\nM v[152:155],a[24:27],a[32:35],v[152:155]\n" \
"s_waitcnt vmcnt(16) lgkmcnt(0)\nM v[184:187],a[24:27],a[36:39],v[184:187]\ns_barrier\nds_read_b64_tr_b16 a[0:1],v9 offset:32768\nM a[248:251],a[24:27],a[40:43],a[248:251]\nbuffer_load_dwordx4 v13,s[28:31],s46 offen lds\nM a[216:219],a[24:27],a[44:47],a[216:219]\ns_add_u32 m0,s50,36864\n" \
"ds_read_b64_tr_b16 a[2:3],v10 offset:32768\nM a[220:223],a[28:31],a[44:47],a[220:223]\nds_read_b128 a[44:47],v7 offset:12288\nM a[252:255],a[28:31],a[40:43],a[252:255]\nds_read_b128 a[40:43],v7 offset:8192\nM v[188:191],a[28:31],a[36:39],v[188:191]\nbuffer_load_dwordx4 v14,s[28:31],s46 offen lds\nM v[156:159],a[28:31],a[32:35],v[156:159]\n" \
"s_add_u32 m0,s50,40960\nds_read_b128 a[32:35],v7 offset:0\nM v[128:131],a[48:51],a[80:83],v[128:131]\nds_read_b128 a[36:39],v7 offset:4096\nM v[160:163],a[48:51],a[84:87],v[160:163]\nds_read_b64_tr_b16 a[4:5],v11 offset:32768\nM a[224:227],a[48:51],a[88:91],a[224:227]\nbuffer_load_dwordx4 v15,s[28:31],s46 offen lds\n" \
"M a[192:195],a[48:51],a[92:95],a[192:195]\ns_add_u32 m0,s50,45056\nds_read_b64_tr_b16 a[6:7],v12 offset:32768\nM a[196:199],a[52:55],a[92:95],a[196:199]\nds_read_b64_tr_b16 a[8:9],v9 offset:33280\nM a[228:231],a[52:55],a[88:91],a[228:231]\nds_read_b64_tr_b16 a[10:11],v10 offset:33280\nM v[164:167],a[52:55],a[84:87],v[164:167]\n" \
"buffer_load_dwordx4 v16,s[28:31],s46 offen lds\nM v[132:135],a[52:55],a[80:83],v[132:135]\ns_add_u32 m0,s50,49152\nds_read_b64_tr_b16 a[12:13],v11 offset:33280\nM v[136:139],a[56:59],a[80:83],v[136:139]\ns_mov_b32 s46,1792\nds_read_b64_tr_b16 a[14:15],v12 offset:33280\nM v[168:171],a[56:59],a[84:87],v[168:171]\n" \
"ds_read_b64_tr_b16 a[16:17],v9 offset:33792\nds_read_b64_tr_b16 a[18:19],v10 offset:33792\nM a[232:235],a[56:59],a[88:91],a[232:235]\nbuffer_load_dwordx4 v17,s[28:31],s46 offen lds\nM a[200:203],a[56:59],a[92:95],a[200:203]\ns_add_u32 m0,s50,53248\nM a[204:207],a[60:63],a[92:95],a[204:207]\nds_read_b64_tr_b16 a[20:21],v11 offset:33792\n" \
"ds_read_b64_tr_b16 a[22:23],v12 offset:33792\nM a[236:239],a[60:63],a[88:91],a[236:239]\nds_read_b64_tr_b16 a[24:25],v9 offset:34304\nM v[172:175],a[60:63],a[84:87],v[172:175]\nbuffer_load_dwordx4 v18,s[28:31],s46 offen lds\nM v[140:143],a[60:63],a[80:83],v[140:143]\ns_add_u32 m0,s50,57344\nds_read_b64_tr_b16 a[26:27],v10 offset:34304\n" \
"M v[144:147],a[64:67],a[80:83],v[144:147]\nds_read_b64_tr_b16 a[28:29],v11 offset:34304\nM v[176:179],a[64:67],a[84:87],v[176:179]\nds_read_b64_tr_b16 a[30:31],v12 offset:34304\nM a[240:243],a[64:67],a[88:91],a[240:243]\nbuffer_load_dwordx4 v19,s[28:31],s46 offen lds\nM a[208:211],a[64:67],a[92:95],a[208:211]\ns_add_u32 m0,s50,61440\n" \
"ds_read_b64_tr_b16 a[48:49],v9 offset:40960\nM a[212:215],a[68:71],a[92:95],a[212:215]\nds_read_b64_tr_b16 a[50:51],v10 offset:40960\nM a[244:247],a[68:71],a[88:91],a[244:247]\nds_read_b64_tr_b16 a[52:53],v11 offset:40960\nds_read_b64_tr_b16 a[54:55],v12 offset:40960\nM v[180:183],a[68:71],a[84:87],v[180:183]\nbuffer_load_dwordx4 v20,s[28:31],s46 offen lds\n" \
"M v[148:151],a[68:71],a[80:83],v[148:151]\ns_add_u32 m0,s51,16384\nM v[152:155],a[72:75],a[80:83],v[152:155]\ns_mov_b32 s46,1920\nds_read_b64_tr_b16 a[56:57],v9 offset:41472\nds_read_b64_tr_b16 a[58:59],v10 offset:41472\nM v[184:187],a[72:75],a[84:87],v[184:187]\nds_read_b64_tr_b16 a[60:61],v11 offset:41472\n" \
"M a[248:251],a[72:75],a[88:91],a[248:251]\nbuffer_load_dwordx4 v21,s[32:35],s47 offen offset:0 lds\nM a[216:219],a[72:75],a[92:95],a[216:219]\ns_add_u32 m0,s51,17280\nds_read_b64_tr_b16 a[62:63],v12 offset:41472\nM a[220:223],a[76:79],a[92:95],a[220:223]\nds_read_b64_tr_b16 a[64:65],v9 offset:41984\nM a[252:255],a[76:79],a[88:91],a[252:255]\n" \
"s_waitcnt lgkmcnt(5)\nds_read_b64_tr_b16 a[66:67],v10 offset:41984\nM v[188:191],a[76:79],a[84:87],v[188:191]\nbuffer_load_dwordx4 v21,s[32:35],s47 offen offset:128 lds\nM v[156:159],a[76:79],a[80:83],v[156:159]\ns_add_u32 m0,s51,24576\nds_read_b128 a[80:83],v8 offset:0\nM v[128:131],a[0:3],a[32:35],v[128:131]\n" \
"s_mov_b32 s47,8126464\nds_read_b128 a[84:87],v8 offset:4096\nM v[160:163],a[0:3],a[36:39],v[160:163]\nds_read_b64_tr_b16 a[68:69],v11 offset:41984\nM a[224:227],a[0:3],a[40:43],a[224:227]\nbuffer_load_dwordx4 v21,s[32:35],s47 offen offset:0 lds\nM a[192:195],a[0:3],a[44:47],a[192:195]\ns_add_u32 m0,s51,25472\n" \
"ds_read_b64_tr_b16 a[70:71],v12 offset:41984\nM a[196:199],a[4:7],a[44:47],a[196:199]\nds_read_b64_tr_b16 a[72:73],v9 offset:42496\nM a[228:231],a[4:7],a[40:43],a[228:231]\nds_read_b64_tr_b16 a[74:75],v10 offset:42496\nM v[164:167],a[4:7],a[36:39],v[164:167]\nds_read_b64_tr_b16 a[76:77],v11 offset:42496\nds_read_b64_tr_b16 a[78:79],v12 offset:42496\n" \
"M v[132:135],a[4:7],a[32:35],v[132:135]\nbuffer_load_dwordx4 v21,s[32:35],s47 offen offset:128 lds\nM v[136:139],a[8:11],a[32:35],v[136:139]\ns_add_u32 m0,s50,65536\nv_add_u32 v21,s63,v4\nM v[168:171],a[8:11],a[36:39],v[168:171]\ns_add_u32 s63,s22,16384\nds_read_b128 a[88:91],v8 offset:8192\n" \
"v_add_u32 v62,s63,v4\nM a[232:235],a[8:11],a[40:43],a[232:235]\ns_cmp_lt_u32 %2,2\ns_cselect_b64 s[62:63],-1,0\nds_read_b128 a[92:95],v8 offset:12288\nM a[200:203],a[8:11],a[44:47],a[200:203]\nM a[204:207],a[12:15],a[44:47],a[204:207]\nM a[236:239],a[12:15],a[40:43],a[236:239]\n" \
"M v[172:175],a[12:15],a[36:39],v[172:175]\nM v[140:143],a[12:15],a[32:35],v[140:143]\nM v[144:147],a[16:19],a[32:35],v[144:147]\nbuffer_store_dwordx4 v[124:127],v4,s[36:39],s96 offen offset:1024 sc1\nM v[176:179],a[16:19],a[36:39],v[176:179]\nM a[240:243],a[16:19],a[40:43],a[240:243]\nM a[208:211],a[16:19],a[44:47],a[208:211]\nM a[212:215],a[20:23],a[44:47],a[212:215]\n" \
"M a[244:247],a[20:23],a[40:43],a[244:247]\nM v[180:183],a[20:23],a[36:39],v[180:183]\nM v[148:151],a[20:23],a[32:35],v[148:151]\nM v[152:155],a[24:27],a[32:35],v[152:155]\ns_waitcnt vmcnt(17) lgkmcnt(0)\nM v[184:187],a[24:27],a[36:39],v[184:187]\ns_barrier\nds_read_b64_tr_b16 a[0:1],v9 offset:49152\n" \
"M a[248:251],a[24:27],a[40:43],a[248:251]\nbuffer_load_dwordx4 v13,s[28:31],s46 offen lds\nM a[216:219],a[24:27],a[44:47],a[216:219]\ns_add_u32 m0,s50,69632\nds_read_b64_tr_b16 a[2:3],v10 offset:49152\nv_add_u32 v13,s54,v4\nM a[220:223],a[28:31],a[44:47],a[220:223]\nds_read_b128 a[44:47],v5 offset:12288\n" \
"M a[252:255],a[28:31],a[40:43],a[252:255]\nds_read_b128 a[40:43],v5 offset:8192\nM v[188:191],a[28:31],a[36:39],v[188:191]\nbuffer_load_dwordx4 v14,s[28:31],s46 offen lds\nM v[156:159],a[28:31],a[32:35],v[156:159]\ns_add_u32 m0,s50,73728\nds_read_b128 a[32:35],v5 offset:0\nv_add_u32 v14,0x10000,v13\n" \
"M v[128:131],a[48:51],a[80:83],v[128:131]\nds_read_b128 a[36:39],v5 offset:4096\nM v[160:163],a[48:51],a[84:87],v[160:163]\nds_read_b64_tr_b16 a[4:5],v11 offset:49152\nM a[224:227],a[48:51],a[88:91],a[224:227]\nbuffer_load_dwordx4 v15,s[28:31],s46 offen lds\nM a[192:195],a[48:51],a[92:95],a[192:195]\ns_add_u32 m0,s50,77824\n" \
"ds_read_b64_tr_b16 a[6:7],v12 offset:49152\nM a[196:199],a[52:55],a[92:95],a[196:199]\nds_read_b64_tr_b16 a[8:9],v9 offset:49664\nM a[228:231],a[52:55],a[88:91],a[228:231]\nds_read_b64_tr_b16 a[10:11],v10 offset:49664\nM v[164:167],a[52:55],a[84:87],v[164:167]\nbuffer_load_dwordx4 v16,s[28:31],s46 offen lds\nM v[132:135],a[52:55],a[80:83],v[132:135]\n" \
"s_add_u32 m0,s50,81920\nds_read_b64_tr_b16 a[12:13],v11 offset:49664\nM v[136:139],a[56:59],a[80:83],v[136:139]\ns_mov_b32 s46,1920\nds_read_b64_tr_b16 a[14:15],v12 offset:49664\nM v[168:171],a[56:59],a[84:87],v[168:171]\nds_read_b64_tr_b16 a[16:17],v9 offset:50176\nds_read_b64_tr_b16 a[18:19],v10 offset:50176\n" \
"M a[232:235],a[56:59],a[88:91],a[232:235]\nbuffer_store_dwordx4 v[72:75],v4,s[36:39],s96 offen offset:2048 sc1\nM a[200:203],a[56:59],a[92:95],a[200:203]\nds_read_b64_tr_b16 a[20:21],v11 offset:50176\nds_read_b64_tr_b16 a[22:23],v12 offset:50176\nM a[204:207],a[60:63],a[92:95],a[204:207]\nds_read_b64_tr_b16 a[24:25],v9 offset:50688\nM a[236:239],a[60:63],a[88:91],a[236:239]\n" \
"ds_read_b64_tr_b16 a[26:27],v10 offset:50688\nds_read_b64_tr_b16 a[28:29],v11 offset:50688\nM v[172:175],a[60:63],a[84:87],v[172:175]\nbuffer_load_dwordx4 v17,s[28:31],s46 offen lds\nM v[140:143],a[60:63],a[80:83],v[140:143]\ns_add_u32 m0,s50,86016\nM v[144:147],a[64:67],a[80:83],v[144:147]\nds_read_b64_tr_b16 a[30:31],v12 offset:50688\n" \
"ds_read_b64_tr_b16 a[48:49],v9 offset:57344\nM v[176:179],a[64:67],a[84:87],v[176:179]\nLG13_%=:\ns_cbranch_execz LHA155V_%=\nds_read_b64_tr_b16 a[50:51],v10 offset:57344\nM a[240:243],a[64:67],a[88:91],a[240:243]\nbuffer_load_dwordx4 v18,s[28:31],s46 offen lds\nM a[208:211],a[64:67],a[92:95],a[208:211]\n" \
"s_add_u32 m0,s50,90112\nds_read_b64_tr_b16 a[52:53],v11 offset:57344\nM a[212:215],a[68:71],a[92:95],a[212:215]\nds_read_b64_tr_b16 a[54:55],v12 offset:57344\nM a[244:247],a[68:71],a[88:91],a[244:247]\nds_read_b64_tr_b16 a[56:57],v9 offset:57856\nM v[180:183],a[68:71],a[84:87],v[180:183]\nbuffer_load_dwordx4 v19,s[28:31],s46 offen lds\n" \
"M v[148:151],a[68:71],a[80:83],v[148:151]\ns_add_u32 m0,s50,94208\nds_read_b64_tr_b16 a[58:59],v10 offset:57856\nM v[152:155],a[72:75],a[80:83],v[152:155]\nds_read_b64_tr_b16 a[60:61],v11 offset:57856\nM v[184:187],a[72:75],a[84:87],v[184:187]\nds_read_b64_tr_b16 a[62:63],v12 offset:57856\nds_read_b64_tr_b16 a[64:65],v9 offset:58368\n" \
"M a[248:251],a[72:75],a[88:91],a[248:251]\nbuffer_load_dwordx4 v20,s[28:31],s46 offen lds\nM a[216:219],a[72:75],a[92:95],a[216:219]\ns_add_u32 m0,s22,49152\nv_add_u32 v20,12288,v21\nv_cndmask_b32 v20,v20,v62,s[62:63]\nM a[220:223],a[76:79],a[92:95],a[220:223]\ns_lshr_b32 s62,%1,3\n" \
"ds_read_b64_tr_b16 a[66:67],v10 offset:58368\nds_read_b64_tr_b16 a[68:69],v11 offset:58368\nM a[252:255],a[76:79],a[88:91],a[252:255]\ns_lshl_b32 s62,s62,2\nds_read_b64_tr_b16 a[70:71],v12 offset:58368\nM v[188:191],a[76:79],a[84:87],v[188:191]\nbuffer_store_dwordx4 v[76:79],v4,s[36:39],s96 offen offset:3072 sc1\nM v[156:159],a[76:79],a[80:83],v[156:159]\n" \
"s_waitcnt lgkmcnt(3)\ns_add_u32 s62,s62,%2\nds_read_b128 a[80:83],v6 offset:0\nds_read_b128 a[84:87],v6 offset:4096\nM v[128:131],a[0:3],a[32:35],v[128:131]\ns_lshr_b32 s63,%1,5\nds_read_b64_tr_b16 a[72:73],v9 offset:58880\nM v[160:163],a[0:3],a[36:39],v[160:163]\n" \
"s_lshl_b32 s62,s62,14\nds_read_b64_tr_b16 a[74:75],v10 offset:58880\nM a[224:227],a[0:3],a[40:43],a[224:227]\ns_lshl_b32 s63,s63,8\nds_read_b64_tr_b16 a[76:77],v11 offset:58880\nM a[192:195],a[0:3],a[44:47],a[192:195]\ns_sub_u32 s62,s62,s63\nds_read_b64_tr_b16 a[78:79],v12 offset:58880\n" \
"ds_read_b128 a[88:91],v6 offset:8192\nM a[196:199],a[4:7],a[44:47],a[196:199]\ns_mov_b32 s46,0\nds_read_b128 a[92:95],v6 offset:12288\nM a[228:231],a[4:7],a[40:43],a[228:231]\nM v[164:167],a[4:7],a[36:39],v[164:167]\nM v[132:135],a[4:7],a[32:35],v[132:135]\nM v[136:139],a[8:11],a[32:35],v[136:139]\n" \
"M v[168:171],a[8:11],a[36:39],v[168:171]\nM a[232:235],a[8:11],a[40:43],a[232:235]\nM a[200:203],a[8:11],a[44:47],a[200:203]\nM a[204:207],a[12:15],a[44:47],a[204:207]\nM a[236:239],a[12:15],a[40:43],a[236:239]\nM v[172:175],a[12:15],a[36:39],v[172:175]\nM v[140:143],a[12:15],a[32:35],v[140:143]\nM v[144:147],a[16:19],a[32:35],v[144:147]\n" \
"M v[176:179],a[16:19],a[36:39],v[176:179]\nM a[240:243],a[16:19],a[40:43],a[240:243]\nM a[208:211],a[16:19],a[44:47],a[208:211]\nM a[212:215],a[20:23],a[44:47],a[212:215]\nM a[244:247],a[20:23],a[40:43],a[244:247]\nM v[180:183],a[20:23],a[36:39],v[180:183]\nM v[148:151],a[20:23],a[32:35],v[148:151]\nM v[152:155],a[24:27],a[32:35],v[152:155]\n" \
"s_waitcnt vmcnt(15) lgkmcnt(0)\nM v[184:187],a[24:27],a[36:39],v[184:187]\ns_barrier\nds_read_b64_tr_b16 a[0:1],v9 offset:0\nM a[248:251],a[24:27],a[40:43],a[248:251]\nbuffer_load_dwordx4 v22,s[40:43],s61 offen offset:0 sc1 lds\nM a[216:219],a[24:27],a[44:47],a[216:219]\nds_read_b64_tr_b16 a[2:3],v10 offset:0\n" \
"M a[220:223],a[28:31],a[44:47],a[220:223]\nds_read_b64_tr_b16 a[4:5],v11 offset:0\nds_read_b64_tr_b16 a[6:7],v12 offset:0\nM a[252:255],a[28:31],a[40:43],a[252:255]\nds_read_b64_tr_b16 a[8:9],v9 offset:512\nM v[188:191],a[28:31],a[36:39],v[188:191]\nbuffer_load_dwordx4 v22,s[40:43],s61 offen offset:1024 sc1 lds\nM v[156:159],a[28:31],a[32:35],v[156:159]\n" \
"ds_read_b128 a[32:35],v5 offset:45056\nM v[128:131],a[48:51],a[80:83],v[128:131]\nds_read_b64_tr_b16 a[10:11],v10 offset:512\nM v[160:163],a[48:51],a[84:87],v[160:163]\nds_read_b64_tr_b16 a[12:13],v11 offset:512\nM a[224:227],a[48:51],a[88:91],a[224:227]\nbuffer_load_dwordx4 v22,s[40:43],s61 offen offset:2048 sc1 lds\nM a[192:195],a[48:51],a[92:95],a[192:195]\n" \
"ds_read_b64_tr_b16 a[14:15],v12 offset:512\nM a[196:199],a[52:55],a[92:95],a[196:199]\nds_read_b64_tr_b16 a[16:17],v9 offset:1024\nM a[228:231],a[52:55],a[88:91],a[228:231]\nds_read_b64_tr_b16 a[18:19],v10 offset:1024\nM v[164:167],a[52:55],a[84:87],v[164:167]\nbuffer_load_dwordx4 v22,s[40:43],s61 offen offset:3072 sc1 lds\nM v[132:135],a[52:55],a[80:83],v[132:135]\n" \
"s_add_u32 m0,s22,81920\nds_read_b64_tr_b16 a[20:21],v11 offset:1024\nM v[136:139],a[56:59],a[80:83],v[136:139]\ns_add_u32 s61,s44,40960\nds_read_b64_tr_b16 a[22:23],v12 offset:1024\nM v[168:171],a[56:59],a[84:87],v[168:171]\nds_read_b64_tr_b16 a[24:25],v9 offset:1536\nds_read_b64_tr_b16 a[26:27],v10 offset:1536\n" \
"M a[232:235],a[56:59],a[88:91],a[232:235]\nbuffer_load_dwordx4 v22,s[40:43],s61 offen offset:0 sc1 lds\nM a[200:203],a[56:59],a[92:95],a[200:203]\nM a[204:207],a[60:63],a[92:95],a[204:207]\nds_read_b64_tr_b16 a[28:29],v11 offset:1536\nds_read_b64_tr_b16 a[30:31],v12 offset:1536\nM a[236:239],a[60:63],a[88:91],a[236:239]\nds_read_b128 a[44:47],v6 offset:45056\n" \
"M v[172:175],a[60:63],a[84:87],v[172:175]\nbuffer_load_dwordx4 v22,s[40:43],s61 offen offset:1024 sc1 lds\nM v[140:143],a[60:63],a[80:83],v[140:143]\nM v[144:147],a[64:67],a[80:83],v[144:147]\nds_read_b64_tr_b16 a[48:49],v9 offset:8192\nds_read_b64_tr_b16 a[50:51],v10 offset:8192\nM v[176:179],a[64:67],a[84:87],v[176:179]\nds_read_b64_tr_b16 a[52:53],v11 offset:8192\n" \
"M a[240:243],a[64:67],a[88:91],a[240:243]\ns_waitcnt vmcnt(7)\nds_read_b64_tr_b16 a[54:55],v12 offset:8192\nM a[208:211],a[64:67],a[92:95],a[208:211]\ns_barrier\nds_read_b128 a[36:39],v7 offset:12288\nM a[212:215],a[68:71],a[92:95],a[212:215]\nbuffer_load_dwordx4 v22,s[40:43],s61 offen offset:2048 sc1 lds\n" \
"M a[244:247],a[68:71],a[88:91],a[244:247]\nds_read_b64_tr_b16 a[96:97],v9 offset:16384\nM v[180:183],a[68:71],a[84:87],v[180:183]\nds_read_b64_tr_b16 a[98:99],v10 offset:16384\nM v[148:151],a[68:71],a[80:83],v[148:151]\nds_read_b64_tr_b16 a[100:101],v11 offset:16384\nds_read_b64_tr_b16 a[102:103],v12 offset:16384\nM v[152:155],a[72:75],a[80:83],v[152:155]\n" \
"ds_read_b64_tr_b16 a[104:105],v9 offset:16896\nM v[184:187],a[72:75],a[84:87],v[184:187]\nds_read_b64_tr_b16 a[106:107],v10 offset:16896\nM a[248:251],a[72:75],a[88:91],a[248:251]\nds_read_b64_tr_b16 a[108:109],v11 offset:16896\nds_read_b64_tr_b16 a[110:111],v12 offset:16896\nM a[216:219],a[72:75],a[92:95],a[216:219]\nds_read_b64_tr_b16 a[112:113],v9 offset:17408\n" \
"M a[220:223],a[76:79],a[92:95],a[220:223]\nds_read_b64_tr_b16 a[114:115],v10 offset:17408\nM a[252:255],a[76:79],a[88:91],a[252:255]\nds_read_b64_tr_b16 a[116:117],v11 offset:17408\nds_read_b64_tr_b16 a[118:119],v12 offset:17408\nM v[188:191],a[76:79],a[84:87],v[188:191]\nds_read_b64_tr_b16 a[120:121],v9 offset:17920\nM v[156:159],a[76:79],a[80:83],v[156:159]\n" \
"s_waitcnt lgkmcnt(7)\nds_read_b64_tr_b16 a[122:123],v10 offset:17920\nM a[192:195],a[0:3],a[32:35],a[192:195]\nds_read_b64_tr_b16 a[124:125],v11 offset:17920\nds_read_b64_tr_b16 a[126:127],v12 offset:17920\nM a[196:199],a[4:7],a[32:35],a[196:199]\nds_read_b128 a[40:43],v8 offset:12288\nM a[200:203],a[8:11],a[32:35],a[200:203]\n" \
"ds_read_b64_tr_b16 a[128:129],v9 offset:24576\nM a[204:207],a[12:15],a[32:35],a[204:207]\nds_read_b64_tr_b16 a[130:131],v10 offset:24576\nM a[208:211],a[16:19],a[32:35],a[208:211]\nds_read_b64_tr_b16 a[132:133],v11 offset:24576\nds_read_b64_tr_b16 a[134:135],v12 offset:24576\nM a[212:215],a[20:23],a[32:35],a[212:215]\nds_read_b64_tr_b16 a[136:137],v9 offset:25088\n" \
"M a[216:219],a[24:27],a[32:35],a[216:219]\nds_read_b64_tr_b16 a[138:139],v10 offset:25088\nM a[220:223],a[28:31],a[32:35],a[220:223]\nds_read_b64_tr_b16 a[140:141],v11 offset:25088\nds_read_b64_tr_b16 a[142:143],v12 offset:25088\nM a[192:195],a[96:99],a[36:39],a[192:195]\nds_read_b64_tr_b16 a[144:145],v9 offset:25600\nM a[196:199],a[100:103],a[36:39],a[196:199]\n" \
"ds_read_b64_tr_b16 a[146:147],v10 offset:25600\nM a[200:203],a[104:107],a[36:39],a[200:203]\ns_waitcnt lgkmcnt(6)\nds_read_b64_tr_b16 a[148:149],v11 offset:25600\nds_read_b64_tr_b16 a[150:151],v12 offset:25600\nM a[204:207],a[108:111],a[36:39],a[204:207]\nds_read_b64_tr_b16 a[152:153],v9 offset:26112\nds_read_b64_tr_b16 a[154:155],v10 offset:26112\n" \
"M a[208:211],a[112:115],a[36:39],a[208:211]\nds_read_b64_tr_b16 a[156:157],v11 offset:26112\nds_read_b64_tr_b16 a[158:159],v12 offset:26112\nM a[212:215],a[116:119],a[36:39],a[212:215]\nbuffer_load_dwordx4 v22,s[40:43],s61 offen offset:3072 sc1 lds\nM a[216:219],a[120:123],a[36:39],a[216:219]\ns_add_u32 m0,s22,98304\nM a[220:223],a[124:127],a[36:39],a[220:223]\n" \
"s_add_u32 s61,s44,16384\nds_read_b64_tr_b16 a[56:57],v9 offset:8704\nds_read_b64_tr_b16 a[58:59],v10 offset:8704\nM a[192:195],a[128:131],a[40:43],a[192:195]\nds_read_b64_tr_b16 a[60:61],v11 offset:8704\nds_read_b64_tr_b16 a[62:63],v12 offset:8704\nM a[196:199],a[132:135],a[40:43],a[196:199]\ns_waitcnt lgkmcnt(6)\n" \
"ds_read_b64_tr_b16 a[64:65],v9 offset:9216\nds_read_b64_tr_b16 a[66:67],v10 offset:9216\nM a[200:203],a[136:139],a[40:43],a[200:203]\nds_read_b128 a[80:83],v5 offset:40960\nds_read_b128 v[240:243],v5 offset:36864\nds_read_b128 v[224:227],v5 offset:32768\nds_read_b128 a[84:87],v7 offset:8192\nds_read_b128 v[244:247],v7 offset:4096\n" \
"ds_read_b128 v[228:231],v7 offset:0\nds_read_b128 a[88:91],v8 offset:8192\nds_read_b128 v[248:251],v8 offset:4096\nds_read_b128 v[232:235],v8 offset:0\nds_read_b64_tr_b16 a[68:69],v11 offset:9216\nds_read_b64_tr_b16 a[70:71],v12 offset:9216\nds_read_b64_tr_b16 a[72:73],v9 offset:9728\nds_read_b64_tr_b16 a[74:75],v10 offset:9728\n" \
"ds_read_b64_tr_b16 a[76:77],v11 offset:9728\nds_read_b64_tr_b16 a[78:79],v12 offset:9728\nds_read_b128 a[92:95],v6 offset:40960\nds_read_b128 v[252:255],v6 offset:36864\nds_read_b128 v[236:239],v6 offset:32768\nM a[204:207],a[140:143],a[40:43],a[204:207]\nM a[208:211],a[144:147],a[40:43],a[208:211]\ns_mov_b64 exec,0\n" \
"s_cmp_eq_u32 %2,0\ns_cbranch_scc1 LG14_%=\ns_cmp_eq_u32 %2,1\ns_cbranch_scc1 LG15_%=\ns_cmp_eq_u32 %2,2\ns_cbranch_scc1 LG26_%=\ns_cmp_eq_u32 %2,3\ns_cbranch_scc1 LG27_%=\n" \
"LHR1_%=:\ns_mov_b64 exec,-1\nM a[212:215],a[148:151],a[40:43],a[212:215]\nds_write_b128 v1,v[88:91] offset:0\nM a[216:219],a[152:155],a[40:43],a[216:219]\ns_waitcnt lgkmcnt(15)\nM a[220:223],a[156:159],a[40:43],a[220:223]\nM a[192:195],a[48:51],a[44:47],a[192:195]\n" \
"s_waitcnt vmcnt(8) lgkmcnt(1)\nM a[196:199],a[52:55],a[44:47],a[196:199]\ns_barrier\nM a[200:203],a[56:59],a[44:47],a[200:203]\nbuffer_store_dwordx2 v[58:59],v56,s[36:39],s24 offen sc1\nM a[204:207],a[60:63],a[44:47],a[204:207]\nds_write_b128 v1,v[92:95] offset:1024\nM a[208:211],a[64:67],a[44:47],a[208:211]\n" \
"buffer_load_dwordx4 v22,s[40:43],s61 offen offset:0 sc1 lds\nM a[212:215],a[68:71],a[44:47],a[212:215]\nv_accvgpr_read_b32 v5,a192\nv_accvgpr_read_b32 v10,a193\nv_accvgpr_read_b32 v11,a194\nM a[216:219],a[72:75],a[44:47],a[216:219]\nv_accvgpr_read_b32 v12,a195\nC v6,v5,v10\n" \
"C v7,v11,v12\nM a[220:223],a[76:79],a[44:47],a[220:223]\nds_write_b128 v1,v[104:107] offset:2048\nv_accvgpr_read_b32 v5,a196\nM a[224:227],a[0:3],a[80:83],a[224:227]\nbuffer_load_dwordx4 v22,s[40:43],s61 offen offset:1024 sc1 lds\nv_accvgpr_read_b32 v10,a197\nM a[224:227],a[96:99],a[84:87],a[224:227]\n" \
"v_accvgpr_read_b32 v11,a198\nv_accvgpr_read_b32 v12,a199\nC v8,v5,v10\nM a[224:227],a[128:131],a[88:91],a[224:227]\nC v9,v11,v12\nv_accvgpr_read_b32 v32,a200\nv_accvgpr_read_b32 v33,a201\nM a[224:227],a[48:51],a[92:95],a[224:227]\n" \
"ds_write_b128 v20,v[6:9] offset:0\nv_accvgpr_read_b32 v34,a202\nM a[228:231],a[52:55],a[92:95],a[228:231]\nbuffer_load_dwordx4 v22,s[40:43],s61 offen offset:2048 sc1 lds\nv_accvgpr_read_b32 v35,a203\nM a[228:231],a[132:135],a[88:91],a[228:231]\nC v36,v32,v33\nC v37,v34,v35\n" \
"v_accvgpr_read_b32 v32,a204\nM a[228:231],a[100:103],a[84:87],a[228:231]\nv_accvgpr_read_b32 v33,a205\nv_accvgpr_read_b32 v34,a206\nv_accvgpr_read_b32 v35,a207\nM a[228:231],a[4:7],a[80:83],a[228:231]\nC v38,v32,v33\nC v39,v34,v35\n" \
"v_accvgpr_read_b32 v5,a208\nM a[232:235],a[8:11],a[80:83],a[232:235]\nbuffer_load_dwordx4 v22,s[40:43],s61 offen offset:3072 sc1 lds\nv_accvgpr_read_b32 v10,a209\nM a[232:235],a[104:107],a[84:87],a[232:235]\ns_add_u32 m0,s22,114688\nv_accvgpr_read_b32 v11,a210\nv_accvgpr_read_b32 v12,a211\n" \
"C v6,v5,v10\nM a[232:235],a[136:139],a[88:91],a[232:235]\ns_add_u32 s61,s44,0\nds_write_b128 v1,v[108:111] offset:3072\nC v7,v11,v12\nM a[232:235],a[56:59],a[92:95],a[232:235]\nv_accvgpr_read_b32 v5,a212\nv_accvgpr_read_b32 v10,a213\n" \
"v_accvgpr_read_b32 v11,a214\nM a[236:239],a[60:63],a[92:95],a[236:239]\nbuffer_load_dwordx4 v22,s[40:43],s61 offen offset:0 sc1 lds\nv_accvgpr_read_b32 v12,a215\nM a[236:239],a[140:143],a[88:91],a[236:239]\nC v8,v5,v10\nC v9,v11,v12\nv_accvgpr_read_b32 v32,a216\n" \
"M a[236:239],a[108:111],a[84:87],a[236:239]\nv_accvgpr_read_b32 v33,a217\nv_accvgpr_read_b32 v34,a218\nv_accvgpr_read_b32 v35,a219\nM a[236:239],a[12:15],a[80:83],a[236:239]\nds_write_b128 v20,v[36:39] offset:1024\nC v36,v32,v33\nM a[240:243],a[16:19],a[80:83],a[240:243]\n" \
"buffer_load_dwordx4 v22,s[40:43],s61 offen offset:1024 sc1 lds\nC v37,v34,v35\nM a[240:243],a[112:115],a[84:87],a[240:243]\nv_accvgpr_read_b32 v32,a220\nv_accvgpr_read_b32 v33,a221\nv_accvgpr_read_b32 v34,a222\nM a[240:243],a[144:147],a[88:91],a[240:243]\nv_accvgpr_read_b32 v35,a223\n" \
"C v38,v32,v33\nC v39,v34,v35\nM a[240:243],a[64:67],a[92:95],a[240:243]\nds_write_b128 v20,v[6:9] offset:2048\nM a[244:247],a[68:71],a[92:95],a[244:247]\nv_accvgpr_read_b32 v5,a224\nv_accvgpr_read_b32 v10,a225\nM a[244:247],a[148:151],a[88:91],a[244:247]\n" \
"v_accvgpr_read_b32 v11,a226\nv_accvgpr_read_b32 v12,a227\nM a[244:247],a[116:119],a[84:87],a[244:247]\nds_write_b128 v1,v[120:123] offset:4096\nds_write_b128 v1,v[124:127] offset:5120\nds_write_b128 v1,v[72:75] offset:6144\nds_write_b128 v1,v[76:79] offset:7168\nM a[244:247],a[20:23],a[80:83],a[244:247]\n" \
"C v6,v5,v10\nC v7,v11,v12\nM a[248:251],a[24:27],a[80:83],a[248:251]\nv_accvgpr_read_b32 v32,a232\nv_accvgpr_read_b32 v33,a233\nM a[248:251],a[120:123],a[84:87],a[248:251]\nv_accvgpr_read_b32 v34,a234\nv_accvgpr_read_b32 v35,a235\n" \
"M a[248:251],a[152:155],a[88:91],a[248:251]\nbuffer_load_dwordx4 v22,s[40:43],s61 offen offset:2048 sc1 lds\nM a[248:251],a[72:75],a[92:95],a[248:251]\nv_accvgpr_read_b32 v5,a228\nv_accvgpr_read_b32 v10,a229\nM a[252:255],a[76:79],a[92:95],a[252:255]\ns_waitcnt lgkmcnt(0)\nv_accvgpr_read_b32 v11,a230\n" \
"v_accvgpr_read_b32 v12,a231\nM a[252:255],a[156:159],a[88:91],a[252:255]\ns_barrier\nC v8,v5,v10\nM a[252:255],a[124:127],a[84:87],a[252:255]\nds_write_b128 v20,v[36:39] offset:3072\nM a[252:255],a[28:31],a[80:83],a[252:255]\nC v36,v32,v33\n" \
"C v37,v34,v35\nM v[160:163],a[0:3],v[240:243],v[160:163]\nbuffer_load_dwordx4 v22,s[40:43],s61 offen offset:3072 sc1 lds\nM v[160:163],a[96:99],v[244:247],v[160:163]\ns_add_u32 m0,s22,0\nC v9,v11,v12\nv_accvgpr_read_b32 v32,a236\nM v[164:167],a[100:103],v[244:247],v[164:167]\n" \
"s_add_u32 s61,s44,24576\nds_read_b128 a[176:179],v20 offset:0\nv_accvgpr_read_b32 v33,a237\nM v[164:167],a[4:7],v[240:243],v[164:167]\nds_read_b128 a[180:183],v20 offset:1024\nv_accvgpr_read_b32 v34,a238\nM v[168:171],a[8:11],v[240:243],v[168:171]\nds_read_b128 a[184:187],v20 offset:2048\n" \
"v_accvgpr_read_b32 v35,a239\nM v[168:171],a[104:107],v[244:247],v[168:171]\nds_read_b128 a[192:195],v13 offset:32768\nC v38,v32,v33\nM v[172:175],a[108:111],v[244:247],v[172:175]\nds_read_b128 a[196:199],v13 offset:34816\nC v39,v34,v35\nM v[172:175],a[12:15],v[240:243],v[172:175]\n" \
"ds_read_b128 a[200:203],v13 offset:36864\nv_accvgpr_read_b32 v5,a240\nM v[176:179],a[16:19],v[240:243],v[176:179]\nds_read_b128 a[188:191],v20 offset:3072\nds_read_b128 a[204:207],v13 offset:38912\nv_add_u32 v20,s65,v20\nM v[176:179],a[112:115],v[244:247],v[176:179]\nds_read_b128 a[208:211],v13 offset:33792\n" \
"v_accvgpr_read_b32 v10,a241\nM v[180:183],a[116:119],v[244:247],v[180:183]\nds_read_b128 a[212:215],v13 offset:35840\nv_accvgpr_read_b32 v11,a242\nM v[180:183],a[20:23],v[240:243],v[180:183]\nds_read_b128 a[216:219],v13 offset:37888\nv_accvgpr_read_b32 v12,a243\nM v[184:187],a[24:27],v[240:243],v[184:187]\n" \
"ds_read_b128 a[220:223],v13 offset:39936\nv_accvgpr_read_b32 v32,a248\nM v[184:187],a[120:123],v[244:247],v[184:187]\nbuffer_load_dwordx4 v22,s[40:43],s61 offen offset:0 sc1 lds\nM v[188:191],a[124:127],v[244:247],v[188:191]\nv_accvgpr_read_b32 v33,a249\nv_accvgpr_read_b32 v34,a250\nM v[188:191],a[28:31],v[240:243],v[188:191]\n" \
"s_waitcnt lgkmcnt(3)\nv_accvgpr_read_b32 v35,a251\nM v[88:91],a[192:195],a[176:179],v[48:51]\nds_write_b128 v20,v[6:9] offset:0\nC v6,v5,v10\nM v[88:91],a[196:199],a[180:183],v[88:91]\nC v7,v11,v12\nv_accvgpr_read_b32 v5,a244\n" \
"M v[88:91],a[200:203],a[184:187],v[88:91]\nbuffer_load_dwordx4 v22,s[40:43],s61 offen offset:1024 sc1 lds\nM v[88:91],a[204:207],a[188:191],v[88:91]\nv_accvgpr_read_b32 v10,a245\nv_accvgpr_read_b32 v11,a246\nM v[92:95],a[208:211],a[176:179],v[52:55]\ns_waitcnt lgkmcnt(1)\nv_accvgpr_read_b32 v12,a247\n" \
"C v8,v5,v10\nM v[92:95],a[212:215],a[180:183],v[92:95]\nds_write_b128 v20,v[36:39] offset:1024\nC v36,v32,v33\nM v[92:95],a[216:219],a[184:187],v[92:95]\nC v37,v34,v35\nC v9,v11,v12\nM v[92:95],a[220:223],a[188:191],v[92:95]\n" \
"buffer_load_dwordx4 v22,s[40:43],s61 offen offset:2048 sc1 lds\nM v[160:163],a[128:131],v[248:251],v[160:163]\nv_accvgpr_read_b32 v32,a252\nv_accvgpr_read_b32 v33,a253\nM v[160:163],a[48:51],v[252:255],v[160:163]\nv_accvgpr_read_b32 v34,a254\nv_accvgpr_read_b32 v35,a255\nM v[164:167],a[52:55],v[252:255],v[164:167]\n" \
"v_max3_f32 v60,v88,v89,v90\nC v38,v32,v33\nM v[164:167],a[132:135],v[248:251],v[164:167]\nC v39,v34,v35\nv_max3_f32 v61,v91,v92,v93\nM v[168:171],a[136:139],v[248:251],v[168:171]\nds_write_b128 v20,v[6:9] offset:2048\nv_max3_f32 v60,v60,v94,v95\n" \
"M v[168:171],a[56:59],v[252:255],v[168:171]\nv_max3_f32 v60,v60,v61,s76\nv_mov_b32 v61,v60\nM v[172:175],a[60:63],v[252:255],v[172:175]\ns_nop 0\nv_permlane32_swap_b32 v60,v61\nv_max_f32 v60,v60,v61\nM v[172:175],a[140:143],v[248:251],v[172:175]\n" \
"v_mov_b32 v61,v60\nC v160,v160,v161\nM v[176:179],a[144:147],v[248:251],v[176:179]\ns_waitcnt vmcnt(12)\nds_write_b128 v20,v[36:39] offset:3072\nM v[176:179],a[64:67],v[252:255],v[176:179]\ns_barrier\nC v161,v162,v163\n" \
"M v[180:183],a[68:71],v[252:255],v[180:183]\nv_permlane16_swap_b32 v60,v61\nv_min_f32 v44,-v60,-v61\nM v[180:183],a[148:151],v[248:251],v[180:183]\nds_read_b128 a[160:163],v20 offset:0\nv_add_f32 v88,v88,v44\nM v[184:187],a[152:155],v[248:251],v[184:187]\nds_read_b128 a[164:167],v20 offset:1024\n" \
"v_exp_f32 v88,v88\nM v[184:187],a[72:75],v[252:255],v[184:187]\nds_read_b128 a[168:171],v20 offset:2048\nv_add_f32 v89,v89,v44\nM v[188:191],a[76:79],v[252:255],v[188:191]\nds_read_b128 a[172:175],v20 offset:3072\nv_add_f32 v90,v90,v44\nM v[188:191],a[156:159],v[248:251],v[188:191]\n" \
"v_exp_f32 v89,v89\nM v[128:131],a[128:131],v[232:235],v[128:131]\nbuffer_load_dwordx4 v22,s[40:43],s61 offen offset:3072 sc1 lds\nM v[128:131],a[0:3],v[224:227],v[128:131]\ns_add_u32 m0,s22,32768\nds_read_b128 v[192:195],v13 offset:49152\nC v168,v168,v169\nM v[132:135],a[4:7],v[224:227],v[132:135]\n" \
"s_add_u32 s61,s44,8192\nds_read_b128 v[196:199],v13 offset:51200\nC v176,v176,v177\nM v[132:135],a[132:135],v[232:235],v[132:135]\nds_read_b128 v[200:203],v13 offset:53248\nv_add_u32 v20,s65,v20\nM v[136:139],a[136:139],v[232:235],v[136:139]\nds_read_b128 v[204:207],v13 offset:55296\n" \
"C v162,v164,v165\nM v[136:139],a[8:11],v[224:227],v[136:139]\nds_read_b128 v[208:211],v13 offset:50176\nC v163,v166,v167\nM v[140:143],a[12:15],v[224:227],v[140:143]\nds_read_b128 v[212:215],v13 offset:52224\nC v169,v170,v171\nM v[140:143],a[140:143],v[232:235],v[140:143]\n" \
"ds_read_b128 v[216:219],v13 offset:54272\nv_add_f32 v91,v91,v44\nM v[144:147],a[144:147],v[232:235],v[144:147]\nv_exp_f32 v90,v90\nM v[144:147],a[16:19],v[224:227],v[144:147]\nds_read_b128 v[220:223],v13 offset:56320\nC v177,v178,v179\nM v[148:151],a[20:23],v[224:227],v[148:151]\n" \
"ds_write_b128 v20,v[160:163] offset:0\nM v[148:151],a[148:151],v[232:235],v[148:151]\nC v170,v172,v173\nC v171,v174,v175\nM v[152:155],a[152:155],v[232:235],v[152:155]\nC v178,v180,v181\nC v179,v182,v183\nM v[152:155],a[24:27],v[224:227],v[152:155]\n" \
"LG14_%=:\ns_cbranch_execz LG28_%=\nv_exp_f32 v91,v91\nM v[156:159],a[28:31],v[224:227],v[156:159]\nv_add_f32 v92,v92,v44\nv_min_f32 v45,-v60,-v61\nM v[156:159],a[156:159],v[232:235],v[156:159]\ns_waitcnt lgkmcnt(1)\n" \
"ds_write_b128 v20,v[168:171] offset:1024\nM v[112:115],v[192:195],a[160:163],v[48:51]\nC v184,v184,v185\nv_min_f32 v46,-v60,-v61\nM v[112:115],v[196:199],a[164:167],v[112:115]\nv_min_f32 v47,-v60,-v61\nC v185,v186,v187\nM v[112:115],v[200:203],a[168:171],v[112:115]\n" \
"v_exp_f32 v92,v92\nM v[112:115],v[204:207],a[172:175],v[112:115]\nds_write_b128 v20,v[176:179] offset:2048\nM v[116:119],v[208:211],a[160:163],v[52:55]\nv_min_f32 v29,-v60,-v61\nv_add_f32 v93,v93,v44\nM v[116:119],v[212:215],a[164:167],v[116:119]\nC v186,v188,v189\n" \
"C v187,v190,v191\nM v[116:119],v[216:219],a[168:171],v[116:119]\nds_read_b128 a[192:195],v14 offset:0\nv_max3_f32 v60,v112,v113,v114\nM v[116:119],v[220:223],a[172:175],v[116:119]\nds_read_b128 a[196:199],v14 offset:1024\nv_add_f32 v94,v94,v44\nM v[120:123],v[192:195],a[176:179],v[44:47]\n" \
"v_exp_f32 v93,v93\nM v[120:123],v[196:199],a[180:183],v[120:123]\nds_write_b128 v20,v[184:187] offset:3072\nM v[120:123],v[200:203],a[184:187],v[120:123]\nv_max3_f32 v61,v115,v116,v117\nv_max3_f32 v60,v60,v118,v119\nM v[120:123],v[204:207],a[188:191],v[120:123]\nv_max3_f32 v60,v60,v61,s76\n" \
"v_mov_b32 v61,v60\nM v[124:127],v[208:211],a[176:179],v[44:47]\nds_read_b128 a[200:203],v14 offset:2048\nv_add_f32 v95,v95,v44\nM v[124:127],v[212:215],a[180:183],v[124:127]\nv_permlane32_swap_b32 v60,v61\nv_max_f32 v60,v60,v61\nM v[124:127],v[216:219],a[184:187],v[124:127]\n" \
"ds_read_b128 a[204:207],v14 offset:3072\nv_mov_b32 v61,v60\nv_exp_f32 v94,v94\nM v[124:127],v[220:223],a[188:191],v[124:127]\nv_permlane16_swap_b32 v60,v61\nv_min_f32 v40,-v60,-v61\nM v[128:131],a[96:99],v[228:231],v[128:131]\nv_add_f32 v112,v112,v40\n" \
"v_add_f32 v113,v113,v40\nv_add_f32 v114,v114,v40\nM v[128:131],a[48:51],v[236:239],v[128:131]\nv_exp_f32 v112,v112\nv_add_f32 v115,v115,v40\nM v[132:135],a[52:55],v[236:239],v[132:135]\ns_waitcnt vmcnt(8) lgkmcnt(15)\nv_exp_f32 v113,v113\n" \
"v_add_f32 v116,v116,v40\nM v[132:135],a[100:103],v[228:231],v[132:135]\ns_barrier\nv_add_f32 v117,v117,v40\nv_exp_f32 v114,v114\nM v[136:139],a[104:107],v[228:231],v[136:139]\nbuffer_load_dwordx2 v[58:59],v57,s[36:39],0 offen sc1\nv_add_f32 v118,v118,v40\n" \
"M v[136:139],a[56:59],v[236:239],v[136:139]\nv_exp_f32 v115,v115\nv_exp_f32 v95,v95\nM v[140:143],a[60:63],v[236:239],v[140:143]\nds_read_b128 a[208:211],v14 offset:4096\nv_exp_f32 v116,v116\nM v[140:143],a[108:111],v[228:231],v[140:143]\nds_read_b128 a[212:215],v14 offset:5120\n" \
"v_exp_f32 v117,v117\nM v[144:147],a[112:115],v[228:231],v[144:147]\nds_read_b128 a[216:219],v14 offset:6144\nv_add_f32 v119,v119,v40\nv_exp_f32 v118,v118\nM v[144:147],a[64:67],v[236:239],v[144:147]\nds_read_b128 a[220:223],v14 offset:7168\nC v88,v88,v89\n" \
"v_exp_f32 v119,v119\nM v[148:151],a[68:71],v[236:239],v[148:151]\nds_read_b128 a[144:147],v20 offset:0\nC v89,v90,v91\nv_exp_f32 v120,v120\nM v[148:151],a[116:119],v[228:231],v[148:151]\nv_exp_f32 v121,v121\nv_xor_b32 v13,0x10000,v13\n" \
"M v[152:155],a[120:123],v[228:231],v[152:155]\nds_read_b128 a[148:151],v20 offset:1024\nds_read_b128 a[152:155],v20 offset:2048\nv_exp_f32 v122,v122\nM v[152:155],a[72:75],v[236:239],v[152:155]\nC v128,v128,v129\nC v90,v92,v93\nC v91,v94,v95\n" \
"M v[156:159],a[76:79],v[236:239],v[156:159]\nds_read_b128 a[0:3],v13 offset:32768\nds_read_b128 a[156:159],v20 offset:3072\nC v136,v136,v137\nM v[156:159],a[124:127],v[228:231],v[156:159]\ns_waitcnt lgkmcnt(5)\nds_read_b128 a[4:7],v13 offset:34816\nC v129,v130,v131\n" \
"C v144,v144,v145\nM v[224:227],a[192:195],v[88:91],0\nds_read_b128 a[8:11],v13 offset:36864\nC v137,v138,v139\nv_exp_f32 v123,v123\nM v[228:231],a[196:199],v[88:91],0\nds_read_b128 a[12:15],v13 offset:38912\nv_add_u32 v20,s65,v20\n" \
"M v[232:235],a[200:203],v[88:91],0\nds_read_b128 a[16:19],v13 offset:33792\nC v130,v132,v133\nC v131,v134,v135\nM v[236:239],a[204:207],v[88:91],0\nds_read_b128 a[20:23],v13 offset:35840\nC v145,v146,v147\nM v[240:243],a[208:211],v[88:91],0\n" \
"ds_read_b128 a[24:27],v13 offset:37888\nC v138,v140,v141\nM v[244:247],a[212:215],v[88:91],0\nds_read_b128 a[28:31],v13 offset:39936\nC v152,v152,v153\nM v[248:251],a[216:219],v[88:91],0\ns_waitcnt lgkmcnt(5)\nv_exp_f32 v124,v124\n" \
"M v[252:255],a[220:223],v[88:91],0\nds_write_b128 v20,v[128:131] offset:0\ns_waitcnt vmcnt(0)\nv_cmp_gt_u64 s[98:99],s[14:15],v[58:59]\nv_cmp_le_u64 s[18:19],s[16:17],v[58:59]\ns_or_b64 s[98:99],s[98:99],s[18:19]\ns_cbranch_scc1 LPK_%=\nLPKR_%=:\n" \
"v_and_b32 v58,0xf0,v4\nv_lshl_add_u32 v57,v58,3,s62\ns_mov_b32 s62,0x11111111\ns_mov_b32 s63,s62\nv_cndmask_b32 v62,0,v63,s[62:63]\nv_accvgpr_write_b32 a224,v62\nv_accvgpr_write_b32 a225,v62\nv_accvgpr_write_b32 a226,v62\n" \
"v_accvgpr_write_b32 a227,v62\ns_mov_b32 s62,0x22222222\ns_mov_b32 s63,s62\nv_cndmask_b32 v62,0,v63,s[62:63]\nv_accvgpr_write_b32 a228,v62\nv_accvgpr_write_b32 a229,v62\nv_accvgpr_write_b32 a230,v62\nv_accvgpr_write_b32 a231,v62\n" \
"s_mov_b32 s62,0x44444444\ns_mov_b32 s63,s62\nv_cndmask_b32 v62,0,v63,s[62:63]\nv_accvgpr_write_b32 a232,v62\nv_accvgpr_write_b32 a233,v62\nv_accvgpr_write_b32 a234,v62\nv_accvgpr_write_b32 a235,v62\ns_mov_b32 s62,0x88888888\n" \
"s_mov_b32 s63,s62\nv_cndmask_b32 v62,0,v63,s[62:63]\nv_accvgpr_write_b32 a236,v62\nv_accvgpr_write_b32 a237,v62\nv_accvgpr_write_b32 a238,v62\nv_accvgpr_write_b32 a239,v62\ns_nop 1\nM v[16:19],a[236:239],v[88:91],v[24:27]\n" \
"s_waitcnt vmcnt(5) lgkmcnt(13)\nC v112,v112,v113\nC v139,v142,v143\nM v[72:75],a[0:3],a[144:147],v[48:51]\ns_barrier\nC v146,v148,v149\nM v[72:75],a[4:7],a[148:151],v[72:75]\nC v147,v150,v151\n" \
"v_min_f32 v41,-v60,-v61\nM v[72:75],a[8:11],a[152:155],v[72:75]\ns_waitcnt lgkmcnt(1)\nC v153,v154,v155\nv_min_f32 v42,-v60,-v61\nM v[72:75],a[12:15],a[156:159],v[72:75]\nv_min_f32 v43,-v60,-v61\nv_min_f32 v28,-v60,-v61\n" \
"M v[76:79],a[16:19],a[144:147],v[52:55]\nds_write_b128 v20,v[136:139] offset:1024\nM v[76:79],a[20:23],a[148:151],v[76:79]\nbuffer_load_dwordx4 v22,s[40:43],s45 offen offset:0 sc1 lds\nM v[76:79],a[24:27],a[152:155],v[76:79]\nv_exp_f32 v125,v125\nM v[76:79],a[28:31],a[156:159],v[76:79]\nds_read_b128 a[96:99],v14 offset:16384\n" \
"C v113,v114,v115\nC v154,v156,v157\nM v[80:83],a[0:3],a[160:163],v[40:43]\nC v155,v158,v159\nv_max3_f32 v60,v72,v73,v74\nM v[80:83],a[4:7],a[164:167],v[80:83]\nds_write_b128 v20,v[144:147] offset:2048\nM v[80:83],a[8:11],a[168:171],v[80:83]\n" \
"v_exp_f32 v126,v126\nM v[80:83],a[12:15],a[172:175],v[80:83]\nC v114,v116,v117\nC v120,v120,v121\nM v[88:91],a[0:3],a[176:179],v[44:47]\nds_read_b128 a[100:103],v14 offset:17408\nC v115,v118,v119\nv_max3_f32 v61,v75,v76,v77\n" \
"M v[88:91],a[4:7],a[180:183],v[88:91]\nv_max3_f32 v60,v60,v78,v79\nv_max3_f32 v60,v60,v61,s76\nv_mov_b32 v61,v60\nM v[88:91],a[8:11],a[184:187],v[88:91]\nds_write_b128 v20,v[152:155] offset:3072\nC v121,v122,v123\nM v[24:27],a[232:235],v[112:115],v[16:19]\n" \
"v_exp_f32 v127,v127\nM v[88:91],a[12:15],a[188:191],v[88:91]\nC v122,v124,v125\nC v123,v126,v127\nM v[84:87],a[16:19],a[160:163],v[40:43]\nv_permlane32_swap_b32 v60,v61\nv_max_f32 v60,v60,v61\nM v[24:27],a[236:239],v[120:123],v[24:27]\n" \
"ds_read_b128 a[104:107],v14 offset:18432\nds_read_b128 a[108:111],v14 offset:19456\nv_mov_b32 v61,v60\nM v[84:87],a[20:23],a[164:167],v[84:87]\nbuffer_load_dwordx4 v22,s[40:43],s45 offen offset:1024 sc1 lds\nM v[84:87],a[24:27],a[168:171],v[84:87]\nv_permlane16_swap_b32 v60,v61\nv_min_f32 v36,-v60,-v61\n" \
"M v[84:87],a[28:31],a[172:175],v[84:87]\nv_max3_f32 v15,v24,v25,v26\nv_max_f32 v15,v15,v27\nv_cmp_nge_f32 vcc,0x53800000,v15\nM v[92:95],a[16:19],a[176:179],v[44:47]\nv_add_f32 v72,v72,v36\nv_add_f32 v73,v73,v36\nv_add_f32 v74,v74,v36\n" \
"M v[92:95],a[20:23],a[180:183],v[92:95]\nbuffer_load_dwordx4 v22,s[40:43],s45 offen offset:2048 sc1 lds\nds_read_b128 a[128:131],v20 offset:0\nds_read_b128 a[132:135],v20 offset:1024\nds_read_b128 a[136:139],v20 offset:2048\nds_read_b128 a[140:143],v20 offset:3072\nds_read_b128 a[112:115],v14 offset:20480\ns_cbranch_vccnz Lcs1_%=\n" \
"Lcr2_%=:\nM v[92:95],a[24:27],a[184:187],v[92:95]\nv_exp_f32 v72,v72\nv_add_f32 v75,v75,v36\nM v[92:95],a[28:31],a[188:191],v[92:95]\ns_waitcnt lgkmcnt(5)\nds_read_b128 a[116:119],v14 offset:21504\nv_add_f32 v76,v76,v36\n" \
"v_exp_f32 v73,v73\nM v[192:195],a[96:99],v[112:115],0\nds_read_b128 a[120:123],v14 offset:22528\nds_read_b128 a[124:127],v14 offset:23552\nv_exp_f32 v74,v74\nM v[224:227],a[96:99],v[120:123],v[224:227]\nbuffer_load_dwordx4 v22,s[40:43],s45 offen offset:3072 sc1 lds\nv_exp_f32 v75,v75\n" \
"M v[228:231],a[100:103],v[120:123],v[228:231]\ns_add_u32 s45,s45,s49\ns_add_u32 m0,s22,49152\nv_exp_f32 v76,v76\nv_add_f32 v77,v77,v36\nM v[196:199],a[100:103],v[112:115],0\ns_waitcnt lgkmcnt(4)\nv_exp_f32 v77,v77\n" \
"v_add_f32 v78,v78,v36\nM v[200:203],a[104:107],v[112:115],0\nds_read_b128 a[48:51],v13 offset:49152\nv_exp_f32 v78,v78\nM v[232:235],a[104:107],v[120:123],v[232:235]\nbuffer_load_dwordx4 v22,s[40:43],s45 offen offset:0 sc1 lds\nv_add_f32 v79,v79,v36\nM v[236:239],a[108:111],v[120:123],v[236:239]\n" \
"s_waitcnt vmcnt(6) lgkmcnt(1)\nds_read_b128 a[52:55],v13 offset:51200\nv_min_f32 v37,-v60,-v61\nv_exp_f32 v79,v79\nM v[204:207],a[108:111],v[112:115],0\ns_barrier\nds_read_b128 a[56:59],v13 offset:53248\nv_exp_f32 v80,v80\n" \
"M v[208:211],a[112:115],v[112:115],0\nds_read_b128 a[60:63],v13 offset:55296\nv_min_f32 v38,-v60,-v61\nv_exp_f32 v81,v81\nM v[240:243],a[112:115],v[120:123],v[240:243]\nbuffer_load_dwordx4 v22,s[40:43],s45 offen offset:1024 sc1 lds\nds_read_b128 a[64:67],v13 offset:50176\nM v[244:247],a[116:119],v[120:123],v[244:247]\n" \
"ds_read_b128 a[68:71],v13 offset:52224\nv_min_f32 v39,-v60,-v61\nv_exp_f32 v82,v82\nM v[212:215],a[116:119],v[112:115],0\nds_read_b128 a[72:75],v13 offset:54272\nv_min_f32 v31,-v60,-v61\nv_exp_f32 v83,v83\nM v[216:219],a[120:123],v[112:115],0\n" \
"ds_read_b128 a[76:79],v13 offset:56320\nC v72,v72,v73\nv_exp_f32 v84,v84\nM v[248:251],a[120:123],v[120:123],v[248:251]\nbuffer_load_dwordx4 v22,s[40:43],s45 offen offset:2048 sc1 lds\nM v[252:255],a[124:127],v[120:123],v[252:255]\nv_exp_f32 v85,v85\nM v[220:223],a[124:127],v[112:115],0\n" \
"s_waitcnt lgkmcnt(3)\nv_exp_f32 v86,v86\nM v[96:99],a[48:51],a[128:131],v[48:51]\nv_exp_f32 v87,v87\nM v[96:99],a[52:55],a[132:135],v[96:99]\nbuffer_load_dwordx4 v22,s[40:43],s45 offen offset:3072 sc1 lds\nM v[96:99],a[56:59],a[136:139],v[96:99]\ns_add_u32 m0,s22,65536\n" \
"s_add_u32 s45,s45,s49\nv_exp_f32 v88,v88\nM v[96:99],a[60:63],a[140:143],v[96:99]\nv_exp_f32 v89,v89\nM v[100:103],a[64:67],a[128:131],v[52:55]\ns_waitcnt lgkmcnt(0)\nv_exp_f32 v90,v90\nM v[100:103],a[68:71],a[132:135],v[100:103]\n" \
"buffer_load_dwordx4 v22,s[40:43],s48 offen offset:0 sc1 lds\nM v[100:103],a[72:75],a[136:139],v[100:103]\nv_exp_f32 v91,v91\nM v[100:103],a[76:79],a[140:143],v[100:103]\nv_exp_f32 v92,v92\nM v[104:107],a[48:51],a[144:147],v[36:39]\nv_max3_f32 v60,v96,v97,v98\nC v73,v74,v75\n" \
"M v[104:107],a[52:55],a[148:151],v[104:107]\nbuffer_load_dwordx4 v22,s[40:43],s48 offen offset:1024 sc1 lds\nM v[104:107],a[56:59],a[152:155],v[104:107]\nv_max3_f32 v61,v99,v100,v101\nv_max3_f32 v60,v60,v102,v103\nM v[104:107],a[60:63],a[156:159],v[104:107]\nv_max3_f32 v60,v60,v61,s76\nv_mov_b32 v61,v60\n" \
"M v[112:115],a[48:51],a[160:163],v[40:43]\nv_exp_f32 v93,v93\nM v[112:115],a[52:55],a[164:167],v[112:115]\nbuffer_load_dwordx4 v22,s[40:43],s48 offen offset:2048 sc1 lds\nM v[112:115],a[56:59],a[168:171],v[112:115]\nv_permlane32_swap_b32 v60,v61\nM v[112:115],a[60:63],a[172:175],v[112:115]\nC v80,v80,v81\n" \
"C v88,v88,v89\nM v[120:123],a[48:51],a[176:179],v[44:47]\nv_max_f32 v60,v60,v61\nC v74,v76,v77\nM v[120:123],a[52:55],a[180:183],v[120:123]\nbuffer_load_dwordx4 v22,s[40:43],s48 offen offset:3072 sc1 lds\nv_exp_f32 v94,v94\nM v[120:123],a[56:59],a[184:187],v[120:123]\n" \
"s_add_u32 m0,s22,98304\nC v75,v78,v79\nC v81,v82,v83\nM v[120:123],a[60:63],a[188:191],v[120:123]\ns_add_u32 s48,s48,s49\nv_mov_b32 v61,v60\nC v89,v90,v91\nM v[108:111],a[64:67],a[144:147],v[36:39]\n" \
"v_xor_b32 v14,0x10000,v14\nC v82,v84,v85\nM v[16:19],a[228:231],v[72:75],v[24:27]\nbuffer_load_dwordx4 v22,s[40:43],s45 offen offset:0 sc1 lds\nv_exp_f32 v95,v95\nM v[108:111],a[68:71],a[148:151],v[108:111]\nds_read_b128 a[0:3],v14 offset:0\nC v83,v86,v87\n" \
"M v[108:111],a[72:75],a[152:155],v[108:111]\nv_permlane16_swap_b32 v60,v61\nM v[16:19],a[232:235],v[80:83],v[16:19]\nds_read_b128 a[4:7],v14 offset:1024\nC v90,v92,v93\nM v[108:111],a[76:79],a[156:159],v[108:111]\nbuffer_load_dwordx4 v22,s[40:43],s45 offen offset:1024 sc1 lds\nC v91,v94,v95\n" \
"M v[116:119],a[64:67],a[160:163],v[40:43]\nds_read_b128 a[8:11],v14 offset:2048\nv_min_f32 v32,-v60,-v61\nM v[16:19],a[236:239],v[88:91],v[16:19]\nv_xor_b32 v13,s54,v13\nv_min_f32 v33,-v60,-v61\nM v[116:119],a[68:71],a[164:167],v[116:119]\nds_read_b128 a[12:15],v14 offset:3072\n" \
"v_min_f32 v34,-v60,-v61\nM v[116:119],a[72:75],a[168:171],v[116:119]\nbuffer_load_dwordx4 v22,s[40:43],s45 offen offset:2048 sc1 lds\nv_min_f32 v35,-v60,-v61\nM v[116:119],a[76:79],a[172:175],v[116:119]\nv_max3_f32 v15,v16,v17,v18\nv_max_f32 v15,v15,v19\nv_cmp_nge_f32 vcc,0x53800000,v15\n" \
"M v[124:127],a[64:67],a[176:179],v[44:47]\ns_waitcnt lgkmcnt(2)\nv_min_f32 v30,-v60,-v61\nv_add_f32 v96,v96,v32\nv_add_f32 v97,v97,v32\nM v[124:127],a[68:71],a[180:183],v[124:127]\ns_cbranch_vccnz Lcs3_%=\nLcr4_%=:\n" \
"v_add_f32 v98,v98,v32\nv_add_f32 v99,v99,v32\nv_add_f32 v100,v100,v32\nv_add_f32 v101,v101,v32\nv_add_f32 v102,v102,v32\nv_add_f32 v103,v103,v32\nM v[124:127],a[72:75],a[184:187],v[124:127]\ns_cmp_eq_u32 s52,s55\n" \
"buffer_load_dwordx4 v22,s[40:43],s45 offen offset:3072 sc1 lds\nds_write_b128 v23,v[28:31] offset:0\nds_read_b128 a[16:19],v14 offset:4096\nds_read_b128 a[20:23],v14 offset:5120\nds_read_b128 a[24:27],v14 offset:6144\nds_read_b128 a[28:31],v14 offset:7168\nM v[124:127],a[76:79],a[188:191],v[124:127]\ns_cbranch_scc1 LH1_%=\n" \
"LH1R0_%=:\ns_add_u32 m0,s22,16384\nv_exp_f32 v96,v96\nM v[160:163],a[0:3],v[72:75],0\ns_add_u32 s45,s45,s49\nv_exp_f32 v97,v97\nM v[192:195],a[0:3],v[80:83],v[192:195]\nv_exp_f32 v98,v98\n" \
"M v[224:227],a[0:3],v[88:91],v[224:227]\nbuffer_load_dwordx4 v22,s[40:43],s61 offen offset:0 sc1 lds\nM v[228:231],a[4:7],v[88:91],v[228:231]\nv_exp_f32 v99,v99\nM v[196:199],a[4:7],v[80:83],v[196:199]\ns_waitcnt vmcnt(13) lgkmcnt(0)\nv_exp_f32 v100,v100\nM v[164:167],a[4:7],v[72:75],0\n" \
"s_barrier\nv_xor_b32 v59,0x2000,v23\nM v[168:171],a[8:11],v[72:75],0\nbuffer_load_dwordx4 v22,s[40:43],s61 offen offset:1024 sc1 lds\nM v[200:203],a[8:11],v[80:83],v[200:203]\nds_read_b128 v[28:31],v59 offset:0\nv_xor_b32 v13,0x10000,v13\nv_exp_f32 v101,v101\n" \
"M v[232:235],a[8:11],v[88:91],v[232:235]\nv_exp_f32 v102,v102\nM v[236:239],a[12:15],v[88:91],v[236:239]\nv_exp_f32 v103,v103\nM v[204:207],a[12:15],v[80:83],v[204:207]\nbuffer_load_dwordx4 v22,s[40:43],s61 offen offset:2048 sc1 lds\nv_exp_f32 v104,v104\nM v[172:175],a[12:15],v[72:75],0\n" \
"v_exp_f32 v105,v105\nM v[176:179],a[16:19],v[72:75],0\nv_exp_f32 v106,v106\nM v[208:211],a[16:19],v[80:83],v[208:211]\ns_waitcnt lgkmcnt(0)\nds_read_b128 a[192:195],v13 offset:32768\nds_read_b128 a[196:199],v13 offset:34816\nv_exp_f32 v107,v107\n" \
"M v[240:243],a[16:19],v[88:91],v[240:243]\nbuffer_load_dwordx4 v22,s[40:43],s61 offen offset:3072 sc1 lds\nv_exp_f32 v108,v108\nM v[244:247],a[20:23],v[88:91],v[244:247]\ns_add_u32 m0,s22,81920\nv_exp_f32 v109,v109\nM v[212:215],a[20:23],v[80:83],v[212:215]\nds_read_b128 a[200:203],v13 offset:36864\n" \
"C v96,v96,v97\nv_exp_f32 v110,v110\nM v[180:183],a[20:23],v[72:75],0\nds_read_b128 a[204:207],v13 offset:38912\nC v97,v98,v99\nv_exp_f32 v111,v111\nM v[184:187],a[24:27],v[72:75],0\nv_exp_f32 v112,v112\n" \
"M v[216:219],a[24:27],v[80:83],v[216:219]\nv_exp_f32 v113,v113\nM v[248:251],a[24:27],v[88:91],v[248:251]\nv_exp_f32 v114,v114\nM v[252:255],a[28:31],v[88:91],v[252:255]\nbuffer_load_dwordx4 v22,s[40:43],s48 offen offset:0 sc1 lds\nM v[220:223],a[28:31],v[80:83],v[220:223]\nv_cndmask_b32 v32,v32,v28,s[66:67]\n" \
"v_cndmask_b32 v33,v33,v28,s[66:67]\nv_cndmask_b32 v34,v34,v28,s[66:67]\nv_cndmask_b32 v35,v35,v28,s[66:67]\nM v[188:191],a[28:31],v[72:75],0\ns_waitcnt lgkmcnt(0)\nds_read_b128 a[208:211],v13 offset:33792\nds_read_b128 a[212:215],v13 offset:35840\nM v[64:67],a[192:195],a[128:131],v[32:35]\n" \
"C v98,v100,v101\nC v99,v102,v103\nv_exp_f32 v115,v115\nM v[64:67],a[196:199],a[132:135],v[64:67]\nds_read_b128 a[216:219],v13 offset:37888\nC v104,v104,v105\nM v[24:27],a[224:227],v[96:99],v[16:19]\nds_read_b128 a[220:223],v13 offset:39936\n" \
"C v105,v106,v107\nM v[64:67],a[200:203],a[136:139],v[64:67]\nv_cndmask_b32 v36,v36,v29,s[66:67]\nv_cndmask_b32 v37,v37,v29,s[66:67]\nv_cndmask_b32 v38,v38,v29,s[66:67]\nv_cndmask_b32 v39,v39,v29,s[66:67]\nM v[64:67],a[204:207],a[140:143],v[64:67]\nv_exp_f32 v116,v116\n" \
"M v[72:75],a[192:195],a[144:147],v[36:39]\nC v106,v108,v109\nC v107,v110,v111\nM v[72:75],a[196:199],a[148:151],v[72:75]\nbuffer_load_dwordx4 v22,s[40:43],s48 offen offset:1024 sc1 lds\nM v[24:27],a[228:231],v[104:107],v[24:27]\nv_exp_f32 v117,v117\nM v[72:75],a[200:203],a[152:155],v[72:75]\n" \
"v_cndmask_b32 v40,v40,v30,s[66:67]\nv_cndmask_b32 v41,v41,v30,s[66:67]\nv_cndmask_b32 v42,v42,v30,s[66:67]\nv_cndmask_b32 v43,v43,v30,s[66:67]\nM v[72:75],a[204:207],a[156:159],v[72:75]\ns_waitcnt vmcnt(2)\nv_exp_f32 v118,v118\nM v[80:83],a[192:195],a[160:163],v[40:43]\n" \
"s_barrier\nv_cndmask_b32 v44,v44,v31,s[66:67]\nM v[80:83],a[196:199],a[164:167],v[80:83]\nbuffer_load_dwordx4 v22,s[40:43],s48 offen offset:2048 sc1 lds\nM v[80:83],a[200:203],a[168:171],v[80:83]\nv_cndmask_b32 v45,v45,v31,s[66:67]\nv_cndmask_b32 v46,v46,v31,s[66:67]\nv_cndmask_b32 v47,v47,v31,s[66:67]\n" \
"M v[80:83],a[204:207],a[172:175],v[80:83]\nv_exp_f32 v119,v119\nM v[88:91],a[192:195],a[176:179],v[44:47]\nC v112,v112,v113\nC v113,v114,v115\nM v[88:91],a[196:199],a[180:183],v[88:91]\nbuffer_load_dwordx4 v22,s[40:43],s48 offen offset:3072 sc1 lds\nM v[88:91],a[200:203],a[184:187],v[88:91]\n" \
"s_add_u32 m0,s22,114688\nC v114,v116,v117\nC v115,v118,v119\nM v[88:91],a[204:207],a[188:191],v[88:91]\ns_add_u32 s48,s48,s49\nv_exp_f32 v120,v120\nM v[24:27],a[232:235],v[112:115],v[24:27]\ns_waitcnt lgkmcnt(0)\n" \
"v_exp_f32 v121,v121\nM v[68:71],a[208:211],a[128:131],v[32:35]\nv_exp_f32 v122,v122\nM v[68:71],a[212:215],a[132:135],v[68:71]\nv_exp_f32 v123,v123\nM v[68:71],a[216:219],a[136:139],v[68:71]\nv_exp_f32 v124,v124\nM v[68:71],a[220:223],a[140:143],v[68:71]\n" \
"v_exp_f32 v125,v125\nM v[76:79],a[208:211],a[144:147],v[36:39]\nv_exp_f32 v126,v126\nM v[76:79],a[212:215],a[148:151],v[76:79]\nv_exp_f32 v127,v127\nM v[76:79],a[216:219],a[152:155],v[76:79]\nds_read_b128 a[192:195],v14 offset:16384\nC v120,v120,v121\n" \
"C v121,v122,v123\nC v122,v124,v125\nC v123,v126,v127\nM v[76:79],a[220:223],a[156:159],v[76:79]\nbuffer_load_dwordx4 v22,s[40:43],s45 offen offset:0 sc1 lds\nM v[24:27],a[236:239],v[120:123],v[24:27]\nds_read_b128 a[196:199],v14 offset:17408\nv_lshrrev_b32 v58,8,v4\n" \
"M v[84:87],a[208:211],a[160:163],v[40:43]\nds_read_b128 a[200:203],v14 offset:18432\nv_lshl_add_u32 v57,v58,27,v57\nM v[84:87],a[212:215],a[164:167],v[84:87]\nds_read_b128 a[204:207],v14 offset:19456\nv_max3_f32 v15,v24,v25,v26\nM v[84:87],a[216:219],a[168:171],v[84:87]\nbuffer_load_dwordx4 v22,s[40:43],s45 offen offset:1024 sc1 lds\n" \
"M v[84:87],a[220:223],a[172:175],v[84:87]\nv_max_f32 v15,v15,v27\nv_cmp_nge_f32 vcc,0x53800000,v15\nM v[92:95],a[208:211],a[176:179],v[44:47]\nds_read_b128 a[208:211],v14 offset:20480\nM v[92:95],a[212:215],a[180:183],v[92:95]\nLG15_%=:\ns_cbranch_execz LHR1_%=\n" \
"s_cbranch_vccnz Lcs5_%=\nLcr6_%=:\nds_read_b128 a[212:215],v14 offset:21504\nM v[92:95],a[216:219],a[184:187],v[92:95]\nbuffer_load_dwordx4 v22,s[40:43],s45 offen offset:2048 sc1 lds\nM v[92:95],a[220:223],a[188:191],v[92:95]\ns_waitcnt lgkmcnt(2)\nds_read_b128 a[216:219],v14 offset:22528\n" \
"M v[128:131],a[192:195],v[96:99],0\nds_read_b128 a[220:223],v14 offset:23552\nv_xor_b32 v14,s54,v14\nM v[160:163],a[192:195],v[104:107],v[160:163]\nv_exp_f32 v64,v64\nM v[192:195],a[192:195],v[112:115],v[192:195]\nbuffer_load_dwordx4 v22,s[40:43],s45 offen offset:3072 sc1 lds\nM v[224:227],a[192:195],v[120:123],v[224:227]\n" \
"s_add_u32 m0,s22,0\nv_exp_f32 v65,v65\nM v[228:231],a[196:199],v[120:123],v[228:231]\ns_add_u32 s45,s45,s49\nv_exp_f32 v66,v66\nM v[196:199],a[196:199],v[112:115],v[196:199]\nv_exp_f32 v67,v67\nM v[164:167],a[196:199],v[104:107],v[164:167]\n" \
"s_waitcnt vmcnt(16) lgkmcnt(0)\nv_exp_f32 v68,v68\nM v[132:135],a[196:199],v[96:99],0\ns_barrier\nds_read_b128 a[192:195],v13 offset:49152\nM v[136:139],a[200:203],v[96:99],0\nbuffer_load_dwordx4 v22,s[40:43],s48 offen offset:0 sc1 lds\nM v[168:171],a[200:203],v[104:107],v[168:171]\n" \
"v_exp_f32 v69,v69\nM v[200:203],a[200:203],v[112:115],v[200:203]\nv_exp_f32 v70,v70\nM v[232:235],a[200:203],v[120:123],v[232:235]\nv_exp_f32 v71,v71\nM v[236:239],a[204:207],v[120:123],v[236:239]\nbuffer_load_dwordx4 v22,s[40:43],s48 offen offset:1024 sc1 lds\nM v[204:207],a[204:207],v[112:115],v[204:207]\n" \
"ds_read_b128 a[196:199],v13 offset:51200\nC v64,v64,v65\nM v[172:175],a[204:207],v[104:107],v[172:175]\nv_exp_f32 v72,v72\nM v[140:143],a[204:207],v[96:99],0\nds_read_b128 a[200:203],v13 offset:53248\nds_read_b128 a[204:207],v13 offset:55296\nM v[144:147],a[208:211],v[96:99],0\n" \
"buffer_load_dwordx4 v22,s[40:43],s48 offen offset:2048 sc1 lds\nM v[176:179],a[208:211],v[104:107],v[176:179]\nv_exp_f32 v73,v73\nM v[208:211],a[208:211],v[112:115],v[208:211]\nv_exp_f32 v74,v74\nM v[240:243],a[208:211],v[120:123],v[240:243]\nv_exp_f32 v75,v75\nM v[244:247],a[212:215],v[120:123],v[244:247]\n" \
"buffer_load_dwordx4 v22,s[40:43],s48 offen offset:3072 sc1 lds\nM v[212:215],a[212:215],v[112:115],v[212:215]\ns_add_u32 m0,s53,32768\nv_exp_f32 v76,v76\nM v[180:183],a[212:215],v[104:107],v[180:183]\ns_add_u32 s52,s52,1\nv_exp_f32 v77,v77\nM v[148:151],a[212:215],v[96:99],0\n" \
"s_add_u32 s48,s48,s49\nv_exp_f32 v78,v78\nM v[152:155],a[216:219],v[96:99],0\nbuffer_load_dwordx4 v22,s[40:43],s45 offen offset:0 sc1 lds\nM v[184:187],a[216:219],v[104:107],v[184:187]\nv_exp_f32 v79,v79\nM v[216:219],a[216:219],v[112:115],v[216:219]\nv_exp_f32 v80,v80\n" \
"M v[248:251],a[216:219],v[120:123],v[248:251]\nC v65,v66,v67\nC v66,v68,v69\nM v[252:255],a[220:223],v[120:123],v[252:255]\nbuffer_load_dwordx4 v22,s[40:43],s45 offen offset:1024 sc1 lds\nM v[220:223],a[220:223],v[112:115],v[220:223]\ns_waitcnt lgkmcnt(0)\nv_exp_f32 v81,v81\n" \
"M v[188:191],a[220:223],v[104:107],v[188:191]\ns_cmp_eq_u32 s52,s56\nC v67,v70,v71\nC v72,v72,v73\nM v[156:159],a[220:223],v[96:99],0\ns_cbranch_scc1 LEV_%=\nLEVR0_%=:\nds_read_b128 a[208:211],v13 offset:50176\n" \
"ds_read_b128 a[212:215],v13 offset:52224\nM v[96:99],a[192:195],a[128:131],v[32:35]\nbuffer_load_dwordx4 v22,s[40:43],s45 offen offset:2048 sc1 lds\nM v[96:99],a[196:199],a[132:135],v[96:99]\nv_exp_f32 v82,v82\nM v[16:19],a[224:227],v[64:67],v[24:27]\nds_read_b128 a[216:219],v13 offset:54272\nC v73,v74,v75\n" \
"M v[96:99],a[200:203],a[136:139],v[96:99]\nds_read_b128 a[220:223],v13 offset:56320\nC v74,v76,v77\nM v[96:99],a[204:207],a[140:143],v[96:99]\nv_exp_f32 v83,v83\nM v[104:107],a[192:195],a[144:147],v[36:39]\nC v75,v78,v79\nC v80,v80,v81\n" \
"M v[104:107],a[196:199],a[148:151],v[104:107]\nv_exp_f32 v84,v84\nM v[16:19],a[228:231],v[72:75],v[16:19]\nbuffer_load_dwordx4 v22,s[40:43],s45 offen offset:3072 sc1 lds\nM v[104:107],a[200:203],a[152:155],v[104:107]\ns_add_u32 m0,s53,16384\nv_exp_f32 v85,v85\nM v[104:107],a[204:207],a[156:159],v[104:107]\n" \
"s_add_u32 s45,s45,s49\nv_exp_f32 v86,v86\nM v[112:115],a[192:195],a[160:163],v[40:43]\nv_exp_f32 v87,v87\nM v[112:115],a[196:199],a[164:167],v[112:115]\nbuffer_load_dwordx4 v22,s[40:43],s48 offen offset:0 sc1 lds\nM v[112:115],a[200:203],a[168:171],v[112:115]\nv_exp_f32 v88,v88\n" \
"M v[112:115],a[204:207],a[172:175],v[112:115]\nC v81,v82,v83\nC v82,v84,v85\nM v[120:123],a[192:195],a[176:179],v[44:47]\nv_exp_f32 v89,v89\nM v[120:123],a[196:199],a[180:183],v[120:123]\nbuffer_load_dwordx4 v22,s[40:43],s48 offen offset:1024 sc1 lds\nM v[120:123],a[200:203],a[184:187],v[120:123]\n" \
"C v83,v86,v87\nC v88,v88,v89\nM v[120:123],a[204:207],a[188:191],v[120:123]\nv_exp_f32 v90,v90\nM v[16:19],a[232:235],v[80:83],v[16:19]\ns_waitcnt lgkmcnt(0)\nv_exp_f32 v91,v91\nM v[100:103],a[208:211],a[128:131],v[32:35]\n" \
"buffer_load_dwordx4 v22,s[40:43],s48 offen offset:2048 sc1 lds\nM v[100:103],a[212:215],a[132:135],v[100:103]\nv_exp_f32 v92,v92\nM v[100:103],a[216:219],a[136:139],v[100:103]\nv_exp_f32 v93,v93\nM v[100:103],a[220:223],a[140:143],v[100:103]\nv_exp_f32 v94,v94\nM v[108:111],a[208:211],a[144:147],v[36:39]\n" \
"buffer_load_dwordx4 v22,s[40:43],s48 offen offset:3072 sc1 lds\nM v[108:111],a[212:215],a[148:151],v[108:111]\ns_add_u32 m0,s53,49152\nv_exp_f32 v95,v95\nv_xor_b32 v14,0x10000,v14\nM v[108:111],a[216:219],a[152:155],v[108:111]\ns_xor_b32 s53,s53,0x10000\nds_read_b128 a[192:195],v14 offset:0\n" \
"C v89,v90,v91\nC v90,v92,v93\nC v91,v94,v95\nM v[108:111],a[220:223],a[156:159],v[108:111]\ns_add_u32 s48,s48,s49\nds_read_b128 a[196:199],v14 offset:1024\nds_read_b128 a[200:203],v14 offset:2048\nM v[16:19],a[236:239],v[88:91],v[16:19]\n" \
"ds_read_b128 a[204:207],v14 offset:3072\nM v[116:119],a[208:211],a[160:163],v[40:43]\nM v[116:119],a[212:215],a[164:167],v[116:119]\nv_max3_f32 v15,v16,v17,v18\nv_max_f32 v15,v15,v19\nM v[116:119],a[216:219],a[168:171],v[116:119]\nv_cmp_nge_f32 vcc,0x53800000,v15\nM v[116:119],a[220:223],a[172:175],v[116:119]\n" \
"M v[124:127],a[208:211],a[176:179],v[44:47]\ns_waitcnt lgkmcnt(1)\nds_read_b128 a[208:211],v14 offset:4096\nM v[124:127],a[212:215],a[180:183],v[124:127]\ns_cbranch_vccnz Lcs7_%=\nLcr8_%=:\nds_read_b128 a[212:215],v14 offset:5120\nM v[124:127],a[216:219],a[184:187],v[124:127]\n" \
"s_cmp_eq_u32 s52,s55\nds_read_b128 a[216:219],v14 offset:6144\nM v[124:127],a[220:223],a[188:191],v[124:127]\ns_cbranch_scc1 LH1_%=\nLH1R1_%=:\nds_read_b128 a[220:223],v14 offset:7168\nv_xor_b32 v13,0x10000,v13\nM v[128:131],a[192:195],v[64:67],v[128:131]\n" \
"v_exp_f32 v96,v96\nM v[160:163],a[192:195],v[72:75],v[160:163]\nv_exp_f32 v97,v97\nM v[192:195],a[192:195],v[80:83],v[192:195]\nv_exp_f32 v98,v98\nM v[224:227],a[192:195],v[88:91],v[224:227]\nv_exp_f32 v99,v99\nM v[228:231],a[196:199],v[88:91],v[228:231]\n" \
"v_exp_f32 v100,v100\nM v[196:199],a[196:199],v[80:83],v[196:199]\nv_exp_f32 v101,v101\nM v[164:167],a[196:199],v[72:75],v[164:167]\ns_waitcnt vmcnt(16) lgkmcnt(0)\nv_exp_f32 v102,v102\nM v[132:135],a[196:199],v[64:67],v[132:135]\ns_barrier\n" \
"ds_read_b128 a[192:195],v13 offset:32768\nM v[136:139],a[200:203],v[64:67],v[136:139]\nbuffer_load_dwordx4 v22,s[40:43],s45 offen offset:0 sc1 lds\nM v[168:171],a[200:203],v[72:75],v[168:171]\nv_exp_f32 v103,v103\nM v[200:203],a[200:203],v[80:83],v[200:203]\nv_exp_f32 v104,v104\nM v[232:235],a[200:203],v[88:91],v[232:235]\n" \
"v_exp_f32 v105,v105\nM v[236:239],a[204:207],v[88:91],v[236:239]\nbuffer_load_dwordx4 v22,s[40:43],s45 offen offset:1024 sc1 lds\nM v[204:207],a[204:207],v[80:83],v[204:207]\nds_read_b128 a[196:199],v13 offset:34816\nC v96,v96,v97\nM v[172:175],a[204:207],v[72:75],v[172:175]\nv_exp_f32 v106,v106\n" \
"M v[140:143],a[204:207],v[64:67],v[140:143]\nds_read_b128 a[200:203],v13 offset:36864\nds_read_b128 a[204:207],v13 offset:38912\nM v[144:147],a[208:211],v[64:67],v[144:147]\nbuffer_load_dwordx4 v22,s[40:43],s45 offen offset:2048 sc1 lds\nM v[176:179],a[208:211],v[72:75],v[176:179]\nv_exp_f32 v107,v107\nM v[208:211],a[208:211],v[80:83],v[208:211]\n" \
"v_exp_f32 v108,v108\nM v[240:243],a[208:211],v[88:91],v[240:243]\nv_exp_f32 v109,v109\nM v[244:247],a[212:215],v[88:91],v[244:247]\nbuffer_load_dwordx4 v22,s[40:43],s45 offen offset:3072 sc1 lds\nM v[212:215],a[212:215],v[80:83],v[212:215]\ns_add_u32 m0,s53,0\nv_exp_f32 v110,v110\n" \
"M v[180:183],a[212:215],v[72:75],v[180:183]\ns_add_u32 s45,s45,s49\nv_exp_f32 v111,v111\nM v[148:151],a[212:215],v[64:67],v[148:151]\nv_exp_f32 v112,v112\nM v[152:155],a[216:219],v[64:67],v[152:155]\nbuffer_load_dwordx4 v22,s[40:43],s48 offen offset:0 sc1 lds\nM v[184:187],a[216:219],v[72:75],v[184:187]\n" \
"v_exp_f32 v113,v113\nM v[216:219],a[216:219],v[80:83],v[216:219]\nC v97,v98,v99\nC v98,v100,v101\nM v[248:251],a[216:219],v[88:91],v[248:251]\nv_exp_f32 v114,v114\nM v[252:255],a[220:223],v[88:91],v[252:255]\nbuffer_load_dwordx4 v22,s[40:43],s48 offen offset:1024 sc1 lds\n" \
"M v[220:223],a[220:223],v[80:83],v[220:223]\nC v99,v102,v103\nC v104,v104,v105\nM v[188:191],a[220:223],v[72:75],v[188:191]\nv_exp_f32 v115,v115\nM v[156:159],a[220:223],v[64:67],v[156:159]\ns_waitcnt lgkmcnt(0)\nv_exp_f32 v116,v116\n" \
"M v[64:67],a[192:195],a[128:131],v[32:35]\nbuffer_load_dwordx4 v22,s[40:43],s48 offen offset:2048 sc1 lds\nM v[64:67],a[196:199],a[132:135],v[64:67]\nds_read_b128 a[208:211],v13 offset:33792\nC v105,v106,v107\nM v[24:27],a[224:227],v[96:99],v[16:19]\nds_read_b128 a[212:215],v13 offset:35840\nC v106,v108,v109\n" \
"M v[64:67],a[200:203],a[136:139],v[64:67]\nds_read_b128 a[216:219],v13 offset:37888\nC v107,v110,v111\nM v[64:67],a[204:207],a[140:143],v[64:67]\nbuffer_load_dwordx4 v22,s[40:43],s48 offen offset:3072 sc1 lds\nM v[72:75],a[192:195],a[144:147],v[36:39]\ns_add_u32 m0,s53,32768\nds_read_b128 a[220:223],v13 offset:39936\n" \
"C v112,v112,v113\nM v[72:75],a[196:199],a[148:151],v[72:75]\ns_add_u32 s48,s48,s49\nv_exp_f32 v117,v117\nM v[24:27],a[228:231],v[104:107],v[24:27]\nv_exp_f32 v118,v118\nM v[72:75],a[200:203],a[152:155],v[72:75]\nv_exp_f32 v119,v119\n" \
"M v[72:75],a[204:207],a[156:159],v[72:75]\nv_exp_f32 v120,v120\nM v[80:83],a[192:195],a[160:163],v[40:43]\nv_exp_f32 v121,v121\nM v[80:83],a[196:199],a[164:167],v[80:83]\nC v113,v114,v115\nC v114,v116,v117\nM v[80:83],a[200:203],a[168:171],v[80:83]\n" \
"v_exp_f32 v122,v122\nM v[80:83],a[204:207],a[172:175],v[80:83]\nC v115,v118,v119\nC v120,v120,v121\nM v[88:91],a[192:195],a[176:179],v[44:47]\nv_exp_f32 v123,v123\nM v[88:91],a[196:199],a[180:183],v[88:91]\nv_exp_f32 v124,v124\n" \
"M v[88:91],a[200:203],a[184:187],v[88:91]\nv_exp_f32 v125,v125\nM v[88:91],a[204:207],a[188:191],v[88:91]\nds_read_b128 a[192:195],v14 offset:16384\nC v121,v122,v123\nM v[24:27],a[232:235],v[112:115],v[24:27]\ns_waitcnt lgkmcnt(1)\nv_exp_f32 v126,v126\n" \
"M v[68:71],a[208:211],a[128:131],v[32:35]\nds_read_b128 a[196:199],v14 offset:17408\nC v122,v124,v125\nM v[68:71],a[212:215],a[132:135],v[68:71]\nv_exp_f32 v127,v127\nM v[68:71],a[216:219],a[136:139],v[68:71]\nds_read_b128 a[200:203],v14 offset:18432\nC v123,v126,v127\n" \
"M v[68:71],a[220:223],a[140:143],v[68:71]\nds_read_b128 a[204:207],v14 offset:19456\nM v[76:79],a[208:211],a[144:147],v[36:39]\nM v[76:79],a[212:215],a[148:151],v[76:79]\nM v[76:79],a[216:219],a[152:155],v[76:79]\nM v[76:79],a[220:223],a[156:159],v[76:79]\nM v[24:27],a[236:239],v[120:123],v[24:27]\nM v[84:87],a[208:211],a[160:163],v[40:43]\n" \
"s_mov_b64 exec,0xff\nglobal_load_dword v0,v2,s[100:101]\ns_mov_b64 exec,-1\nM v[84:87],a[212:215],a[164:167],v[84:87]\nv_max3_f32 v15,v24,v25,v26\nv_max_f32 v15,v15,v27\nM v[84:87],a[216:219],a[168:171],v[84:87]\nv_cmp_nge_f32 vcc,0x53800000,v15\n" \
"M v[84:87],a[220:223],a[172:175],v[84:87]\nM v[92:95],a[208:211],a[176:179],v[44:47]\nds_read_b128 a[208:211],v14 offset:20480\nM v[92:95],a[212:215],a[180:183],v[92:95]\ns_cbranch_vccnz Lcs9_%=\nLcr10_%=:\nds_read_b128 a[212:215],v14 offset:21504\nM v[92:95],a[216:219],a[184:187],v[92:95]\n" \
"ds_read_b128 a[216:219],v14 offset:22528\nM v[92:95],a[220:223],a[188:191],v[92:95]\ns_waitcnt lgkmcnt(3)\nds_read_b128 a[220:223],v14 offset:23552\nM v[128:131],a[192:195],v[96:99],v[128:131]\nv_exp_f32 v64,v64\nM v[160:163],a[192:195],v[104:107],v[160:163]\nv_exp_f32 v65,v65\n" \
"M v[192:195],a[192:195],v[112:115],v[192:195]\nbuffer_load_dword v58,v57,s[84:87],s74 offen\nM v[224:227],a[192:195],v[120:123],v[224:227]\ns_add_u32 s74,s74,2048\nv_exp_f32 v66,v66\nM v[228:231],a[196:199],v[120:123],v[228:231]\nv_exp_f32 v67,v67\nM v[196:199],a[196:199],v[112:115],v[196:199]\n" \
"v_exp_f32 v68,v68\nM v[164:167],a[196:199],v[104:107],v[164:167]\ns_waitcnt vmcnt(18) lgkmcnt(0)\nv_exp_f32 v69,v69\nM v[132:135],a[196:199],v[96:99],v[132:135]\ns_barrier\nds_read_b128 a[192:195],v13 offset:49152\nM v[136:139],a[200:203],v[96:99],v[136:139]\n" \
"buffer_load_dwordx4 v22,s[40:43],s45 offen offset:0 sc1 lds\nM v[168:171],a[200:203],v[104:107],v[168:171]\nv_exp_f32 v70,v70\nM v[200:203],a[200:203],v[112:115],v[200:203]\nv_exp_f32 v71,v71\nM v[232:235],a[200:203],v[120:123],v[232:235]\nv_exp_f32 v72,v72\nM v[236:239],a[204:207],v[120:123],v[236:239]\n" \
"buffer_load_dwordx4 v22,s[40:43],s45 offen offset:1024 sc1 lds\nM v[204:207],a[204:207],v[112:115],v[204:207]\nds_read_b128 a[196:199],v13 offset:51200\nC v64,v64,v65\nM v[172:175],a[204:207],v[104:107],v[172:175]\nv_exp_f32 v73,v73\nM v[140:143],a[204:207],v[96:99],v[140:143]\nds_read_b128 a[200:203],v13 offset:53248\n" \
"ds_read_b128 a[204:207],v13 offset:55296\nM v[144:147],a[208:211],v[96:99],v[144:147]\nbuffer_load_dwordx4 v22,s[40:43],s45 offen offset:2048 sc1 lds\nM v[176:179],a[208:211],v[104:107],v[176:179]\nv_exp_f32 v74,v74\nM v[208:211],a[208:211],v[112:115],v[208:211]\nv_exp_f32 v75,v75\nM v[240:243],a[208:211],v[120:123],v[240:243]\n" \
"v_exp_f32 v76,v76\nM v[244:247],a[212:215],v[120:123],v[244:247]\nbuffer_load_dwordx4 v22,s[40:43],s45 offen offset:3072 sc1 lds\nM v[212:215],a[212:215],v[112:115],v[212:215]\ns_add_u32 m0,s53,16384\nv_exp_f32 v77,v77\nM v[180:183],a[212:215],v[104:107],v[180:183]\ns_add_u32 s52,s52,1\n" \
"v_exp_f32 v78,v78\nM v[148:151],a[212:215],v[96:99],v[148:151]\ns_add_u32 s45,s45,s49\nv_exp_f32 v79,v79\nM v[152:155],a[216:219],v[96:99],v[152:155]\nbuffer_load_dwordx4 v22,s[40:43],s48 offen offset:0 sc1 lds\nM v[184:187],a[216:219],v[104:107],v[184:187]\nv_exp_f32 v80,v80\n" \
"M v[216:219],a[216:219],v[112:115],v[216:219]\nC v65,v66,v67\nC v66,v68,v69\nM v[248:251],a[216:219],v[120:123],v[248:251]\nv_exp_f32 v81,v81\nM v[252:255],a[220:223],v[120:123],v[252:255]\nbuffer_load_dwordx4 v22,s[40:43],s48 offen offset:1024 sc1 lds\nM v[220:223],a[220:223],v[112:115],v[220:223]\n" \
"s_waitcnt lgkmcnt(0)\nC v67,v70,v71\nC v72,v72,v73\nM v[188:191],a[220:223],v[104:107],v[188:191]\ns_cmp_eq_u32 s52,s56\nv_exp_f32 v82,v82\nM v[156:159],a[220:223],v[96:99],v[156:159]\ns_cbranch_scc1 LEV_%=\n" \
"LEVR1_%=:\nds_read_b128 a[208:211],v13 offset:50176\nds_read_b128 a[212:215],v13 offset:52224\nM v[96:99],a[192:195],a[128:131],v[32:35]\nbuffer_load_dwordx4 v22,s[40:43],s48 offen offset:2048 sc1 lds\nM v[96:99],a[196:199],a[132:135],v[96:99]\nv_exp_f32 v83,v83\nM v[16:19],a[224:227],v[64:67],v[24:27]\n" \
"ds_read_b128 a[216:219],v13 offset:54272\nC v73,v74,v75\nM v[96:99],a[200:203],a[136:139],v[96:99]\nds_read_b128 a[220:223],v13 offset:56320\nC v74,v76,v77\nM v[96:99],a[204:207],a[140:143],v[96:99]\nbuffer_load_dwordx4 v22,s[40:43],s48 offen offset:3072 sc1 lds\nM v[104:107],a[192:195],a[144:147],v[36:39]\n" \
"s_add_u32 m0,s53,49152\nC v75,v78,v79\nC v80,v80,v81\nM v[104:107],a[196:199],a[148:151],v[104:107]\ns_xor_b32 s53,s53,0x10000\nv_exp_f32 v84,v84\nM v[16:19],a[228:231],v[72:75],v[16:19]\ns_add_u32 s48,s48,s49\n" \
"v_exp_f32 v85,v85\nM v[104:107],a[200:203],a[152:155],v[104:107]\nv_exp_f32 v86,v86\nM v[104:107],a[204:207],a[156:159],v[104:107]\nv_exp_f32 v87,v87\nM v[112:115],a[192:195],a[160:163],v[40:43]\nv_exp_f32 v88,v88\nM v[112:115],a[196:199],a[164:167],v[112:115]\n" \
"C v81,v82,v83\nC v82,v84,v85\nM v[112:115],a[200:203],a[168:171],v[112:115]\nv_exp_f32 v89,v89\nM v[112:115],a[204:207],a[172:175],v[112:115]\nC v83,v86,v87\nC v88,v88,v89\nM v[120:123],a[192:195],a[176:179],v[44:47]\n" \
"v_exp_f32 v90,v90\nM v[120:123],a[196:199],a[180:183],v[120:123]\nv_exp_f32 v91,v91\nM v[120:123],a[200:203],a[184:187],v[120:123]\nv_exp_f32 v92,v92\nM v[120:123],a[204:207],a[188:191],v[120:123]\nv_exp_f32 v93,v93\nM v[16:19],a[232:235],v[80:83],v[16:19]\n" \
"s_waitcnt lgkmcnt(0)\nv_exp_f32 v94,v94\nM v[100:103],a[208:211],a[128:131],v[32:35]\nC v89,v90,v91\nC v90,v92,v93\nM v[100:103],a[212:215],a[132:135],v[100:103]\nv_exp_f32 v95,v95\nM v[100:103],a[216:219],a[136:139],v[100:103]\n" \
"v_xor_b32 v14,0x10000,v14\nC v91,v94,v95\nM v[100:103],a[220:223],a[140:143],v[100:103]\nds_read_b128 a[192:195],v14 offset:0\nds_read_b128 a[196:199],v14 offset:1024\nM v[108:111],a[208:211],a[144:147],v[36:39]\nds_read_b128 a[200:203],v14 offset:2048\nM v[108:111],a[212:215],a[148:151],v[108:111]\n" \
"ds_read_b128 a[204:207],v14 offset:3072\nM v[108:111],a[216:219],a[152:155],v[108:111]\nM v[108:111],a[220:223],a[156:159],v[108:111]\nM v[16:19],a[236:239],v[88:91],v[16:19]\nM v[116:119],a[208:211],a[160:163],v[40:43]\nM v[116:119],a[212:215],a[164:167],v[116:119]\nv_max3_f32 v15,v16,v17,v18\nv_max_f32 v15,v15,v19\n" \
"M v[116:119],a[216:219],a[168:171],v[116:119]\nv_cmp_nge_f32 vcc,0x53800000,v15\nM v[116:119],a[220:223],a[172:175],v[116:119]\nM v[124:127],a[208:211],a[176:179],v[44:47]\ns_waitcnt lgkmcnt(0)\nds_read_b128 a[208:211],v14 offset:4096\nM v[124:127],a[212:215],a[180:183],v[124:127]\ns_cbranch_vccnz Lcs11_%=\n" \
"Lcr12_%=:\nds_read_b128 a[212:215],v14 offset:5120\nM v[124:127],a[216:219],a[184:187],v[124:127]\ns_cmp_eq_u32 s52,s55\nds_read_b128 a[216:219],v14 offset:6144\nM v[124:127],a[220:223],a[188:191],v[124:127]\ns_cbranch_scc1 LH1_%=\nLH1R2_%=:\n" \
"ds_read_b128 a[220:223],v14 offset:7168\nv_xor_b32 v13,0x10000,v13\nM v[128:131],a[192:195],v[64:67],v[128:131]\nv_exp_f32 v96,v96\nM v[160:163],a[192:195],v[72:75],v[160:163]\nv_exp_f32 v97,v97\nM v[192:195],a[192:195],v[80:83],v[192:195]\nv_exp_f32 v98,v98\n" \
"M v[224:227],a[192:195],v[88:91],v[224:227]\nv_exp_f32 v99,v99\nM v[228:231],a[196:199],v[88:91],v[228:231]\nv_exp_f32 v100,v100\nM v[196:199],a[196:199],v[80:83],v[196:199]\nv_exp_f32 v101,v101\nM v[164:167],a[196:199],v[72:75],v[164:167]\ns_waitcnt vmcnt(18) lgkmcnt(0)\n" \
"v_exp_f32 v102,v102\nM v[132:135],a[196:199],v[64:67],v[132:135]\ns_barrier\nds_read_b128 a[192:195],v13 offset:32768\nM v[136:139],a[200:203],v[64:67],v[136:139]\nbuffer_load_dwordx4 v22,s[40:43],s45 offen offset:0 sc1 lds\nM v[168:171],a[200:203],v[72:75],v[168:171]\nv_exp_f32 v103,v103\n" \
"M v[200:203],a[200:203],v[80:83],v[200:203]\nv_exp_f32 v104,v104\nM v[232:235],a[200:203],v[88:91],v[232:235]\nv_exp_f32 v105,v105\nM v[236:239],a[204:207],v[88:91],v[236:239]\nbuffer_load_dwordx4 v22,s[40:43],s45 offen offset:1024 sc1 lds\nM v[204:207],a[204:207],v[80:83],v[204:207]\nds_read_b128 a[196:199],v13 offset:34816\n" \
"C v96,v96,v97\nM v[172:175],a[204:207],v[72:75],v[172:175]\nv_exp_f32 v106,v106\nM v[140:143],a[204:207],v[64:67],v[140:143]\nds_read_b128 a[200:203],v13 offset:36864\nds_read_b128 a[204:207],v13 offset:38912\nM v[144:147],a[208:211],v[64:67],v[144:147]\nbuffer_load_dwordx4 v22,s[40:43],s45 offen offset:2048 sc1 lds\n" \
"M v[176:179],a[208:211],v[72:75],v[176:179]\nv_exp_f32 v107,v107\nM v[208:211],a[208:211],v[80:83],v[208:211]\nv_exp_f32 v108,v108\nM v[240:243],a[208:211],v[88:91],v[240:243]\nv_exp_f32 v109,v109\nM v[244:247],a[212:215],v[88:91],v[244:247]\nbuffer_load_dwordx4 v22,s[40:43],s45 offen offset:3072 sc1 lds\n" \
"M v[212:215],a[212:215],v[80:83],v[212:215]\ns_add_u32 m0,s53,0\nv_exp_f32 v110,v110\nM v[180:183],a[212:215],v[72:75],v[180:183]\ns_add_u32 s45,s45,s49\nv_exp_f32 v111,v111\nM v[148:151],a[212:215],v[64:67],v[148:151]\nv_exp_f32 v112,v112\n" \
"M v[152:155],a[216:219],v[64:67],v[152:155]\nbuffer_load_dwordx4 v22,s[40:43],s48 offen offset:0 sc1 lds\nM v[184:187],a[216:219],v[72:75],v[184:187]\nC v97,v98,v99\nC v98,v100,v101\nM v[216:219],a[216:219],v[80:83],v[216:219]\nv_exp_f32 v113,v113\nM v[248:251],a[216:219],v[88:91],v[248:251]\n" \
"C v99,v102,v103\nC v104,v104,v105\nM v[252:255],a[220:223],v[88:91],v[252:255]\nbuffer_load_dwordx4 v22,s[40:43],s48 offen offset:1024 sc1 lds\nM v[220:223],a[220:223],v[80:83],v[220:223]\nv_exp_f32 v114,v114\nM v[188:191],a[220:223],v[72:75],v[188:191]\nv_exp_f32 v115,v115\n" \
"M v[156:159],a[220:223],v[64:67],v[156:159]\ns_waitcnt lgkmcnt(0)\nds_read_b128 a[208:211],v13 offset:33792\nds_read_b128 a[212:215],v13 offset:35840\nM v[64:67],a[192:195],a[128:131],v[32:35]\nbuffer_load_dwordx4 v22,s[40:43],s48 offen offset:2048 sc1 lds\nM v[64:67],a[196:199],a[132:135],v[64:67]\nC v105,v106,v107\n" \
"C v106,v108,v109\nM v[24:27],a[224:227],v[96:99],v[16:19]\nds_read_b128 a[216:219],v13 offset:37888\nC v107,v110,v111\nM v[64:67],a[200:203],a[136:139],v[64:67]\nds_read_b128 a[220:223],v13 offset:39936\nC v112,v112,v113\nM v[64:67],a[204:207],a[140:143],v[64:67]\n" \
"buffer_load_dwordx4 v22,s[40:43],s48 offen offset:3072 sc1 lds\nM v[72:75],a[192:195],a[144:147],v[36:39]\ns_add_u32 m0,s53,32768\nv_exp_f32 v116,v116\nM v[72:75],a[196:199],a[148:151],v[72:75]\ns_add_u32 s48,s48,s49\nv_exp_f32 v117,v117\nM v[24:27],a[228:231],v[104:107],v[24:27]\n" \
"v_exp_f32 v118,v118\nM v[72:75],a[200:203],a[152:155],v[72:75]\nv_exp_f32 v119,v119\nM v[72:75],a[204:207],a[156:159],v[72:75]\nv_exp_f32 v120,v120\nM v[80:83],a[192:195],a[160:163],v[40:43]\nLG16_%=:\ns_cbranch_execz LHR2_%=\n" \
"C v113,v114,v115\nC v114,v116,v117\nM v[80:83],a[196:199],a[164:167],v[80:83]\nv_exp_f32 v121,v121\nM v[80:83],a[200:203],a[168:171],v[80:83]\nC v115,v118,v119\nC v120,v120,v121\nM v[80:83],a[204:207],a[172:175],v[80:83]\n" \
"v_exp_f32 v122,v122\nM v[88:91],a[192:195],a[176:179],v[44:47]\nv_exp_f32 v123,v123\nM v[88:91],a[196:199],a[180:183],v[88:91]\nv_exp_f32 v124,v124\nM v[88:91],a[200:203],a[184:187],v[88:91]\nv_exp_f32 v125,v125\nM v[88:91],a[204:207],a[188:191],v[88:91]\n" \
"ds_read_b128 a[192:195],v14 offset:16384\nC v121,v122,v123\nM v[24:27],a[232:235],v[112:115],v[24:27]\ns_waitcnt lgkmcnt(1)\nv_exp_f32 v126,v126\nM v[68:71],a[208:211],a[128:131],v[32:35]\nds_read_b128 a[196:199],v14 offset:17408\nC v122,v124,v125\n" \
"M v[68:71],a[212:215],a[132:135],v[68:71]\nv_exp_f32 v127,v127\nM v[68:71],a[216:219],a[136:139],v[68:71]\nds_read_b128 a[200:203],v14 offset:18432\nC v123,v126,v127\nM v[68:71],a[220:223],a[140:143],v[68:71]\nds_read_b128 a[204:207],v14 offset:19456\nM v[76:79],a[208:211],a[144:147],v[36:39]\n" \
"M v[76:79],a[212:215],a[148:151],v[76:79]\nM v[76:79],a[216:219],a[152:155],v[76:79]\nM v[76:79],a[220:223],a[156:159],v[76:79]\nM v[24:27],a[236:239],v[120:123],v[24:27]\nM v[84:87],a[208:211],a[160:163],v[40:43]\ns_mov_b64 exec,0xff\nglobal_load_dword v0,v2,s[100:101]\ns_mov_b64 exec,-1\n" \
"M v[84:87],a[212:215],a[164:167],v[84:87]\nv_max3_f32 v15,v24,v25,v26\nv_max_f32 v15,v15,v27\nM v[84:87],a[216:219],a[168:171],v[84:87]\nv_cmp_nge_f32 vcc,0x53800000,v15\nM v[84:87],a[220:223],a[172:175],v[84:87]\nM v[92:95],a[208:211],a[176:179],v[44:47]\nds_read_b128 a[208:211],v14 offset:20480\n" \
"M v[92:95],a[212:215],a[180:183],v[92:95]\ns_cbranch_vccnz Lcs13_%=\nLcr14_%=:\nds_read_b128 a[212:215],v14 offset:21504\nM v[92:95],a[216:219],a[184:187],v[92:95]\nds_read_b128 a[216:219],v14 offset:22528\nM v[92:95],a[220:223],a[188:191],v[92:95]\ns_waitcnt lgkmcnt(3)\n" \
"ds_read_b128 a[220:223],v14 offset:23552\nM v[128:131],a[192:195],v[96:99],v[128:131]\nv_exp_f32 v64,v64\nM v[160:163],a[192:195],v[104:107],v[160:163]\nv_exp_f32 v65,v65\nM v[192:195],a[192:195],v[112:115],v[192:195]\nbuffer_load_dword v58,v57,s[84:87],s74 offen\nM v[224:227],a[192:195],v[120:123],v[224:227]\n" \
"s_add_u32 s74,s74,2048\nv_exp_f32 v66,v66\nM v[228:231],a[196:199],v[120:123],v[228:231]\nv_exp_f32 v67,v67\nM v[196:199],a[196:199],v[112:115],v[196:199]\nv_exp_f32 v68,v68\nM v[164:167],a[196:199],v[104:107],v[164:167]\ns_waitcnt vmcnt(20) lgkmcnt(0)\n" \
"v_exp_f32 v69,v69\nM v[132:135],a[196:199],v[96:99],v[132:135]\ns_barrier\nds_read_b128 a[192:195],v13 offset:49152\nM v[136:139],a[200:203],v[96:99],v[136:139]\nbuffer_load_dwordx4 v22,s[40:43],s45 offen offset:0 sc1 lds\nM v[168:171],a[200:203],v[104:107],v[168:171]\nv_exp_f32 v70,v70\n" \
"M v[200:203],a[200:203],v[112:115],v[200:203]\nv_exp_f32 v71,v71\nM v[232:235],a[200:203],v[120:123],v[232:235]\nv_exp_f32 v72,v72\nM v[236:239],a[204:207],v[120:123],v[236:239]\nbuffer_load_dwordx4 v22,s[40:43],s45 offen offset:1024 sc1 lds\nM v[204:207],a[204:207],v[112:115],v[204:207]\nds_read_b128 a[196:199],v13 offset:51200\n" \
"C v64,v64,v65\nM v[172:175],a[204:207],v[104:107],v[172:175]\nv_exp_f32 v73,v73\nM v[140:143],a[204:207],v[96:99],v[140:143]\nds_read_b128 a[200:203],v13 offset:53248\nds_read_b128 a[204:207],v13 offset:55296\nM v[144:147],a[208:211],v[96:99],v[144:147]\nbuffer_load_dwordx4 v22,s[40:43],s45 offen offset:2048 sc1 lds\n" \
"M v[176:179],a[208:211],v[104:107],v[176:179]\nv_exp_f32 v74,v74\nM v[208:211],a[208:211],v[112:115],v[208:211]\nv_exp_f32 v75,v75\nM v[240:243],a[208:211],v[120:123],v[240:243]\nv_exp_f32 v76,v76\nM v[244:247],a[212:215],v[120:123],v[244:247]\nbuffer_load_dwordx4 v22,s[40:43],s45 offen offset:3072 sc1 lds\n" \
"M v[212:215],a[212:215],v[112:115],v[212:215]\ns_add_u32 m0,s53,16384\nv_exp_f32 v77,v77\nM v[180:183],a[212:215],v[104:107],v[180:183]\ns_add_u32 s52,s52,1\nv_exp_f32 v78,v78\nM v[148:151],a[212:215],v[96:99],v[148:151]\ns_add_u32 s45,s45,s49\n" \
"v_exp_f32 v79,v79\nM v[152:155],a[216:219],v[96:99],v[152:155]\nbuffer_load_dwordx4 v22,s[40:43],s48 offen offset:0 sc1 lds\nM v[184:187],a[216:219],v[104:107],v[184:187]\nv_exp_f32 v80,v80\nM v[216:219],a[216:219],v[112:115],v[216:219]\nC v65,v66,v67\nC v66,v68,v69\n" \
"M v[248:251],a[216:219],v[120:123],v[248:251]\nv_exp_f32 v81,v81\nM v[252:255],a[220:223],v[120:123],v[252:255]\nbuffer_load_dwordx4 v22,s[40:43],s48 offen offset:1024 sc1 lds\nM v[220:223],a[220:223],v[112:115],v[220:223]\ns_waitcnt lgkmcnt(0)\nC v67,v70,v71\nC v72,v72,v73\n" \
"M v[188:191],a[220:223],v[104:107],v[188:191]\ns_cmp_eq_u32 s52,s56\nv_exp_f32 v82,v82\nM v[156:159],a[220:223],v[96:99],v[156:159]\ns_cbranch_scc0 LEVR1_%=\ns_branch LEV_%=\nLEVR8_%=:\nds_read_b128 a[208:211],v13 offset:50176\n" \
"ds_read_b128 a[212:215],v13 offset:52224\nM v[96:99],a[192:195],a[128:131],v[32:35]\nbuffer_load_dwordx4 v22,s[40:43],s48 offen offset:2048 sc1 lds\nM v[96:99],a[196:199],a[132:135],v[96:99]\ns_lshl_b32 s58,%2,2\nv_exp_f32 v83,v83\nM v[16:19],a[224:227],v[64:67],v[24:27]\ns_lshl_b32 s59,%2,1\n" \
"ds_read_b128 a[216:219],v13 offset:54272\nC v73,v74,v75\nM v[96:99],a[200:203],a[136:139],v[96:99]\nds_read_b128 a[220:223],v13 offset:56320\nC v74,v76,v77\nM v[96:99],a[204:207],a[140:143],v[96:99]\nbuffer_load_dwordx4 v22,s[40:43],s48 offen offset:3072 sc1 lds\nM v[104:107],a[192:195],a[144:147],v[36:39]\n" \
"s_add_u32 m0,s22,49152\nC v75,v78,v79\nC v80,v80,v81\nM v[104:107],a[196:199],a[148:151],v[104:107]\ns_add_u32 s48,s48,s49\nv_exp_f32 v84,v84\nM v[16:19],a[228:231],v[72:75],v[16:19]\nv_exp_f32 v85,v85\n" \
"M v[104:107],a[200:203],a[152:155],v[104:107]\nv_exp_f32 v86,v86\nM v[104:107],a[204:207],a[156:159],v[104:107]\nv_exp_f32 v87,v87\nM v[112:115],a[192:195],a[160:163],v[40:43]\nv_exp_f32 v88,v88\nM v[112:115],a[196:199],a[164:167],v[112:115]\nC v81,v82,v83\n" \
"C v82,v84,v85\nM v[112:115],a[200:203],a[168:171],v[112:115]\nv_exp_f32 v89,v89\nM v[112:115],a[204:207],a[172:175],v[112:115]\nC v83,v86,v87\nC v88,v88,v89\nM v[120:123],a[192:195],a[176:179],v[44:47]\nv_exp_f32 v90,v90\n" \
"M v[120:123],a[196:199],a[180:183],v[120:123]\nv_exp_f32 v91,v91\nM v[120:123],a[200:203],a[184:187],v[120:123]\nv_exp_f32 v92,v92\nM v[120:123],a[204:207],a[188:191],v[120:123]\nv_exp_f32 v93,v93\nM v[16:19],a[232:235],v[80:83],v[16:19]\ns_waitcnt lgkmcnt(0)\n" \
"v_exp_f32 v94,v94\nM v[100:103],a[208:211],a[128:131],v[32:35]\nC v89,v90,v91\nv_xor_b32 v14,0x10000,v14\nM v[100:103],a[212:215],a[132:135],v[100:103]\nds_read_b128 a[192:195],v14 offset:0\nds_read_b128 a[196:199],v14 offset:1024\nM v[100:103],a[216:219],a[136:139],v[100:103]\n" \
"v_exp_f32 v95,v95\nM v[100:103],a[220:223],a[140:143],v[100:103]\nC v90,v92,v93\nC v91,v94,v95\nM v[108:111],a[208:211],a[144:147],v[36:39]\nds_read_b128 a[200:203],v14 offset:2048\nv_bfe_u32 v52,v4,6,2\nM v[108:111],a[212:215],a[148:151],v[108:111]\n" \
"v_add_u32 v52,s58,v52\nv_lshrrev_b32 v5,1,v4\nM v[108:111],a[216:219],a[152:155],v[108:111]\ns_lshl_b32 s58,%2,9\nds_read_b128 a[204:207],v14 offset:3072\nv_xor_b32 v13,0x10000,v13\nM v[108:111],a[220:223],a[156:159],v[108:111]\ns_add_u32 s58,s58,94375936\n" \
"v_add_u32 v5,s58,v5\nv_bfe_u32 v53,v4,8,1\nM v[16:19],a[236:239],v[88:91],v[16:19]\ns_lshl_b32 s58,%2,9\nv_lshl_add_u32 v52,v53,4,v52\nv_add_u32 v53,s59,v53\nM v[116:119],a[208:211],a[160:163],v[40:43]\nv_lshrrev_b32 v54,4,v4\n" \
"v_xor_b32 v53,v53,v54\nM v[116:119],a[212:215],a[164:167],v[116:119]\nbuffer_load_dwordx2 v[10:11],v5,s[36:39],0 offen sc1\nM v[116:119],a[216:219],a[168:171],v[116:119]\nv_max3_f32 v15,v16,v17,v18\nv_max_f32 v15,v15,v19\nM v[116:119],a[220:223],a[172:175],v[116:119]\nv_cmp_nge_f32 vcc,0x53800000,v15\n" \
"v_lshlrev_b32 v52,11,v52\nM v[124:127],a[208:211],a[176:179],v[44:47]\nds_read_b128 a[208:211],v14 offset:4096\nv_and_b32 v53,3,v53\nM v[124:127],a[212:215],a[180:183],v[124:127]\ns_cbranch_vccnz Lcs39_%=\nLcr40_%=:\nds_read_b128 a[212:215],v14 offset:5120\n" \
"v_lshrrev_b32 v54,9,v4\nM v[124:127],a[216:219],a[184:187],v[124:127]\nds_read_b128 a[216:219],v14 offset:6144\nv_lshl_add_u32 v53,v54,2,v53\nM v[124:127],a[220:223],a[188:191],v[124:127]\ns_waitcnt lgkmcnt(3)\nds_read_b128 a[220:223],v14 offset:7168\nv_lshl_add_u32 v1,v53,4,v52\n" \
"M v[128:131],a[192:195],v[64:67],v[128:131]\nv_exp_f32 v96,v96\nM v[160:163],a[192:195],v[72:75],v[160:163]\nv_exp_f32 v97,v97\nM v[192:195],a[192:195],v[80:83],v[192:195]\nv_exp_f32 v98,v98\nM v[224:227],a[192:195],v[88:91],v[224:227]\nv_exp_f32 v99,v99\n" \
"M v[228:231],a[196:199],v[88:91],v[228:231]\nv_exp_f32 v100,v100\nM v[196:199],a[196:199],v[80:83],v[196:199]\nv_exp_f32 v101,v101\nM v[164:167],a[196:199],v[72:75],v[164:167]\ns_waitcnt vmcnt(19) lgkmcnt(0)\nv_exp_f32 v102,v102\nM v[132:135],a[196:199],v[64:67],v[132:135]\n" \
"s_barrier\nds_read_b128 a[192:195],v13 offset:32768\nM v[136:139],a[200:203],v[64:67],v[136:139]\nbuffer_load_dwordx4 v1,s[84:87],s46 offen offset:0 lds\nM v[168:171],a[200:203],v[72:75],v[168:171]\ns_add_u32 m0,s22,50048\nv_exp_f32 v103,v103\nM v[200:203],a[200:203],v[80:83],v[200:203]\n" \
"v_exp_f32 v104,v104\nM v[232:235],a[200:203],v[88:91],v[232:235]\nv_exp_f32 v105,v105\nM v[236:239],a[204:207],v[88:91],v[236:239]\nbuffer_load_dwordx4 v1,s[84:87],s46 offen offset:128 lds\nM v[204:207],a[204:207],v[80:83],v[204:207]\ns_add_u32 m0,s22,51200\nv_exp_f32 v106,v106\n" \
"M v[172:175],a[204:207],v[72:75],v[172:175]\ns_mov_b32 s46,65536\nds_read_b128 a[196:199],v13 offset:34816\nds_read_b128 a[200:203],v13 offset:36864\nM v[140:143],a[204:207],v[64:67],v[140:143]\nv_exp_f32 v107,v107\nM v[144:147],a[208:211],v[64:67],v[144:147]\nbuffer_load_dwordx4 v1,s[84:87],s46 offen offset:0 lds\n" \
"M v[176:179],a[208:211],v[72:75],v[176:179]\ns_add_u32 m0,s22,52096\nds_read_b128 a[204:207],v13 offset:38912\nC v96,v96,v97\nM v[208:211],a[208:211],v[80:83],v[208:211]\nv_exp_f32 v108,v108\nM v[240:243],a[208:211],v[88:91],v[240:243]\nv_exp_f32 v109,v109\n" \
"M v[244:247],a[212:215],v[88:91],v[244:247]\nbuffer_load_dwordx4 v1,s[84:87],s46 offen offset:128 lds\nM v[212:215],a[212:215],v[80:83],v[212:215]\ns_add_u32 m0,s22,65536\nv_exp_f32 v110,v110\nM v[180:183],a[212:215],v[72:75],v[180:183]\ns_mov_b32 s46,131072\nv_exp_f32 v111,v111\n" \
"M v[148:151],a[212:215],v[64:67],v[148:151]\ns_and_b32 s61,%1,1\nv_exp_f32 v112,v112\nM v[152:155],a[216:219],v[64:67],v[152:155]\nbuffer_load_dwordx4 v1,s[84:87],s46 offen offset:0 lds\nM v[184:187],a[216:219],v[72:75],v[184:187]\ns_add_u32 m0,s22,66432\nv_exp_f32 v113,v113\n" \
"M v[216:219],a[216:219],v[80:83],v[216:219]\ns_lshl_b32 s61,s61,2\nv_exp_f32 v114,v114\nM v[248:251],a[216:219],v[88:91],v[248:251]\ns_bfe_u32 s60,%1,0x20001\nC v97,v98,v99\nC v98,v100,v101\nM v[252:255],a[220:223],v[88:91],v[252:255]\n" \
"buffer_load_dwordx4 v1,s[84:87],s46 offen offset:128 lds\nM v[220:223],a[220:223],v[80:83],v[220:223]\ns_add_u32 m0,s22,67584\nv_exp_f32 v115,v115\nM v[188:191],a[220:223],v[72:75],v[188:191]\ns_mov_b32 s46,196608\nC v99,v102,v103\nC v104,v104,v105\n" \
"M v[156:159],a[220:223],v[64:67],v[156:159]\ns_cmp_eq_u32 s60,0\ns_cselect_b64 s[96:97],-1,0\ns_waitcnt lgkmcnt(0)\nv_exp_f32 v116,v116\nM v[64:67],a[192:195],a[128:131],v[32:35]\nbuffer_load_dwordx4 v1,s[84:87],s46 offen offset:0 lds\nM v[64:67],a[196:199],a[132:135],v[64:67]\n" \
"s_waitcnt vmcnt(7)\nv_cmp_gt_u64 s[98:99],s[14:15],v[10:11]\nv_cmp_le_u64 s[18:19],s[16:17],v[10:11]\nM v[24:27],a[224:227],v[96:99],v[16:19]\ns_or_b64 s[98:99],s[98:99],s[18:19]\ns_cbranch_scc1 LPZ_%=\nLPZR_%=:\nds_read_b128 a[208:211],v13 offset:33792\n" \
"ds_read_b128 a[212:215],v13 offset:35840\nM v[64:67],a[200:203],a[136:139],v[64:67]\ns_add_u32 m0,s22,68480\nds_read_b128 a[216:219],v13 offset:37888\nC v105,v106,v107\nM v[64:67],a[204:207],a[140:143],v[64:67]\nbuffer_load_dwordx4 v1,s[84:87],s46 offen offset:128 lds\nM v[72:75],a[192:195],a[144:147],v[36:39]\n" \
"s_add_u32 m0,s22,98304\nC v106,v108,v109\nC v107,v110,v111\nv_exp_f32 v117,v117\nM v[72:75],a[196:199],a[148:151],v[72:75]\ns_mov_b32 s46,262144\nds_read_b128 a[220:223],v13 offset:39936\nC v112,v112,v113\n" \
"M v[24:27],a[228:231],v[104:107],v[24:27]\ns_add_u32 s14,s14,3000\ns_addc_u32 s15,s15,0\nv_exp_f32 v118,v118\nM v[72:75],a[200:203],a[152:155],v[72:75]\ns_sub_u32 s73,s72,2048\nv_exp_f32 v119,v119\nM v[72:75],a[204:207],a[156:159],v[72:75]\n" \
"s_lshl_b32 s95,%1,2\nv_exp_f32 v120,v120\nM v[80:83],a[192:195],a[160:163],v[40:43]\ns_add_u32 s95,s95,%2\nv_exp_f32 v121,v121\nM v[80:83],a[196:199],a[164:167],v[80:83]\ns_lshl_b32 s95,s95,5\nC v113,v114,v115\n" \
"C v114,v116,v117\nM v[80:83],a[200:203],a[168:171],v[80:83]\ns_add_u32 s95,s95,94437376\nv_exp_f32 v122,v122\nM v[80:83],a[204:207],a[172:175],v[80:83]\nC v115,v118,v119\nC v120,v120,v121\nM v[88:91],a[192:195],a[176:179],v[44:47]\n" \
"v_exp_f32 v123,v123\nM v[88:91],a[196:199],a[180:183],v[88:91]\nv_exp_f32 v124,v124\nM v[88:91],a[200:203],a[184:187],v[88:91]\nv_exp_f32 v125,v125\nM v[88:91],a[204:207],a[188:191],v[88:91]\nds_read_b128 a[192:195],v14 offset:16384\nC v121,v122,v123\n" \
"M v[24:27],a[232:235],v[112:115],v[24:27]\ns_waitcnt lgkmcnt(1)\nv_exp_f32 v126,v126\nM v[68:71],a[208:211],a[128:131],v[32:35]\nds_read_b128 a[196:199],v14 offset:17408\nC v122,v124,v125\nM v[68:71],a[212:215],a[132:135],v[68:71]\nv_exp_f32 v127,v127\n" \
"M v[68:71],a[216:219],a[136:139],v[68:71]\nds_read_b128 a[200:203],v14 offset:18432\nC v123,v126,v127\nM v[68:71],a[220:223],a[140:143],v[68:71]\nds_read_b128 a[204:207],v14 offset:19456\nv_and_b32 v52,0xc0,v4\nM v[76:79],a[208:211],a[144:147],v[36:39]\nv_bfe_u32 v53,v4,4,1\n" \
"v_lshl_add_u32 v52,v53,3,v52\nM v[76:79],a[212:215],a[148:151],v[76:79]\nv_lshrrev_b32 v54,7,v4\nv_bfe_u32 v53,v4,5,1\nM v[76:79],a[216:219],a[152:155],v[76:79]\nv_and_b32 v54,2,v54\nv_xor_b32 v53,v53,v54\nM v[76:79],a[220:223],a[156:159],v[76:79]\n" \
"v_lshl_add_u32 v52,v53,4,v52\nv_lshrrev_b32 v53,8,v4\nM v[24:27],a[236:239],v[120:123],v[24:27]\nv_lshl_add_u32 v52,v53,12,v52\nv_add_u32 v6,s58,v52\nM v[84:87],a[208:211],a[160:163],v[40:43]\ns_bfe_u32 s58,%1,0x20003\nv_xor_b32 v7,0x110,v6\n" \
"v_xor_b32 v8,32,v6\nM v[84:87],a[212:215],a[164:167],v[84:87]\ns_add_u32 s61,s61,s58\nv_max3_f32 v15,v24,v25,v26\nv_max_f32 v15,v15,v27\nM v[84:87],a[216:219],a[168:171],v[84:87]\ns_and_b32 s58,s61,6\nv_cmp_nge_f32 vcc,0x53800000,v15\n" \
"v_xor_b32 v9,0x130,v6\nM v[84:87],a[220:223],a[172:175],v[84:87]\ns_add_u32 s58,s58,s61\nv_bfe_u32 v52,v4,7,2\nv_mov_b32 v48,-1\nM v[92:95],a[208:211],a[176:179],v[44:47]\ns_lshl_b32 s58,s58,2\nds_read_b128 a[208:211],v14 offset:20480\n" \
"v_mov_b32 v49,-1\nM v[92:95],a[212:215],a[180:183],v[92:95]\ns_cbranch_vccnz Lcs41_%=\nLcr42_%=:\nds_read_b128 a[212:215],v14 offset:21504\nv_mov_b32 v50,-1\nM v[92:95],a[216:219],a[184:187],v[92:95]\ns_add_u32 s58,s58,s60\n" \
"ds_read_b128 a[216:219],v14 offset:22528\nv_mov_b32 v51,-1\nM v[92:95],a[220:223],a[188:191],v[92:95]\ns_waitcnt lgkmcnt(3)\nbuffer_store_dwordx4 v[48:51],v4,s[36:39],s72 offen offset:0 sc1\nM v[128:131],a[192:195],v[96:99],v[128:131]\ns_lshl_b32 s58,s58,15\nds_read_b128 a[220:223],v14 offset:23552\n" \
"v_mov_b32 v53,s11\nM v[160:163],a[192:195],v[104:107],v[160:163]\ns_add_u32 s28,s58,50331648\nv_exp_f32 v64,v64\nM v[192:195],a[192:195],v[112:115],v[192:195]\ns_add_u32 s29,s58,52428800\nv_exp_f32 v65,v65\nM v[224:227],a[192:195],v[120:123],v[224:227]\n" \
"s_add_u32 s30,s58,54525952\nv_exp_f32 v66,v66\nM v[228:231],a[196:199],v[120:123],v[228:231]\ns_add_u32 s31,s58,56623104\nv_exp_f32 v67,v67\nM v[196:199],a[196:199],v[112:115],v[196:199]\ns_lshr_b32 s58,%2,1\nv_exp_f32 v68,v68\n" \
"M v[164:167],a[196:199],v[104:107],v[164:167]\ns_waitcnt vmcnt(20) lgkmcnt(0)\nv_exp_f32 v69,v69\nM v[132:135],a[196:199],v[96:99],v[132:135]\ns_lshl_b32 s58,s58,18\ns_barrier\nds_read_b128 a[192:195],v13 offset:49152\nM v[136:139],a[200:203],v[96:99],v[136:139]\n" \
"buffer_store_dwordx4 v[48:51],v4,s[36:39],s72 offen offset:1024 sc1\nM v[168:171],a[200:203],v[104:107],v[168:171]\ns_add_u32 s47,s28,0\nv_exp_f32 v70,v70\nM v[200:203],a[200:203],v[112:115],v[200:203]\nv_exp_f32 v71,v71\nM v[232:235],a[200:203],v[120:123],v[232:235]\nv_exp_f32 v72,v72\n" \
"M v[236:239],a[204:207],v[120:123],v[236:239]\nv_exp_f32 v73,v73\nM v[204:207],a[204:207],v[112:115],v[204:207]\nv_exp_f32 v74,v74\nM v[172:175],a[204:207],v[104:107],v[172:175]\nds_read_b128 a[196:199],v13 offset:51200\nds_read_b128 a[200:203],v13 offset:53248\nM v[140:143],a[204:207],v[96:99],v[140:143]\n" \
"v_exp_f32 v75,v75\nM v[144:147],a[208:211],v[96:99],v[144:147]\nds_read_b128 a[204:207],v13 offset:55296\nC v64,v64,v65\nM v[176:179],a[208:211],v[104:107],v[176:179]\nv_exp_f32 v76,v76\nM v[208:211],a[208:211],v[112:115],v[208:211]\nv_exp_f32 v77,v77\n" \
"M v[240:243],a[208:211],v[120:123],v[240:243]\nbuffer_load_dwordx4 v22,s[40:43],s45 offen offset:0 sc1 lds\nM v[244:247],a[212:215],v[120:123],v[244:247]\nv_exp_f32 v78,v78\nM v[212:215],a[212:215],v[112:115],v[212:215]\nv_exp_f32 v79,v79\nM v[180:183],a[212:215],v[104:107],v[180:183]\ns_add_u32 s52,s52,1\n" \
"v_exp_f32 v80,v80\nM v[148:151],a[212:215],v[96:99],v[148:151]\nv_exp_f32 v81,v81\nM v[152:155],a[216:219],v[96:99],v[152:155]\nbuffer_load_dwordx4 v22,s[40:43],s45 offen offset:1024 sc1 lds\nM v[184:187],a[216:219],v[104:107],v[184:187]\nC v65,v66,v67\nC v66,v68,v69\n" \
"M v[216:219],a[216:219],v[112:115],v[216:219]\nv_exp_f32 v82,v82\nM v[248:251],a[216:219],v[120:123],v[248:251]\nC v67,v70,v71\nC v72,v72,v73\nM v[252:255],a[220:223],v[120:123],v[252:255]\nbuffer_load_dwordx4 v22,s[40:43],s45 offen offset:2048 sc1 lds\nM v[220:223],a[220:223],v[112:115],v[220:223]\n" \
"s_waitcnt lgkmcnt(0)\nv_exp_f32 v83,v83\nM v[188:191],a[220:223],v[104:107],v[188:191]\ns_cmp_eq_u32 s52,s56\nv_exp_f32 v84,v84\nM v[156:159],a[220:223],v[96:99],v[156:159]\ns_cbranch_scc1 LEV_%=\nLEVR9_%=:\n" \
"C v73,v74,v75\nC v74,v76,v77\nM v[96:99],a[192:195],a[128:131],v[32:35]\nds_read_b128 a[208:211],v13 offset:50176\nds_read_b128 a[212:215],v13 offset:52224\nM v[96:99],a[196:199],a[132:135],v[96:99]\nbuffer_load_dwordx4 v22,s[40:43],s45 offen offset:3072 sc1 lds\nM v[16:19],a[224:227],v[64:67],v[24:27]\n" \
"s_add_u32 m0,s22,81920\nv_exp_f32 v85,v85\nM v[96:99],a[200:203],a[136:139],v[96:99]\ns_add_u32 s45,s45,s49\nds_read_b128 a[216:219],v13 offset:54272\nC v75,v78,v79\nM v[96:99],a[204:207],a[140:143],v[96:99]\nds_read_b128 a[220:223],v13 offset:56320\n" \
"C v80,v80,v81\nM v[104:107],a[192:195],a[144:147],v[36:39]\nbuffer_load_dwordx4 v22,s[40:43],s48 offen offset:0 sc1 lds\nM v[104:107],a[196:199],a[148:151],v[104:107]\nv_exp_f32 v86,v86\nM v[16:19],a[228:231],v[72:75],v[16:19]\nv_exp_f32 v87,v87\nM v[104:107],a[200:203],a[152:155],v[104:107]\n" \
"v_exp_f32 v88,v88\nM v[104:107],a[204:207],a[156:159],v[104:107]\nC v81,v82,v83\nC v82,v84,v85\nM v[112:115],a[192:195],a[160:163],v[40:43]\nbuffer_store_dwordx4 v[48:51],v4,s[36:39],s72 offen offset:2048 sc1\nM v[112:115],a[196:199],a[164:167],v[112:115]\nv_exp_f32 v89,v89\n" \
"M v[112:115],a[200:203],a[168:171],v[112:115]\nC v83,v86,v87\nC v88,v88,v89\nM v[112:115],a[204:207],a[172:175],v[112:115]\nv_exp_f32 v90,v90\nM v[120:123],a[192:195],a[176:179],v[44:47]\nv_exp_f32 v91,v91\nM v[120:123],a[196:199],a[180:183],v[120:123]\n" \
"v_exp_f32 v92,v92\nM v[120:123],a[200:203],a[184:187],v[120:123]\nv_exp_f32 v93,v93\nM v[120:123],a[204:207],a[188:191],v[120:123]\nv_exp_f32 v94,v94\nM v[16:19],a[232:235],v[80:83],v[16:19]\ns_waitcnt lgkmcnt(0)\nC v89,v90,v91\n" \
"C v90,v92,v93\nM v[100:103],a[208:211],a[128:131],v[32:35]\nv_exp_f32 v95,v95\nM v[100:103],a[212:215],a[132:135],v[100:103]\nv_xor_b32 v14,0x10000,v14\nC v91,v94,v95\nM v[100:103],a[216:219],a[136:139],v[100:103]\nds_read_b128 a[192:195],v14 offset:0\n" \
"ds_read_b128 a[196:199],v14 offset:1024\nM v[100:103],a[220:223],a[140:143],v[100:103]\nds_read_b128 a[200:203],v14 offset:2048\nv_add_u32 v2,s58,v22\nM v[108:111],a[208:211],a[144:147],v[36:39]\nbuffer_store_dwordx4 v[48:51],v4,s[36:39],s72 offen offset:3072 sc1\nM v[108:111],a[212:215],a[148:151],v[108:111]\ns_lshl_b32 s58,s61,2\n" \
"ds_read_b128 a[204:207],v14 offset:3072\nM v[108:111],a[216:219],a[152:155],v[108:111]\ns_add_u32 s58,s58,s60\nM v[108:111],a[220:223],a[156:159],v[108:111]\ns_lshl_b32 s58,s58,6\nM v[16:19],a[236:239],v[88:91],v[16:19]\ns_add_u32 s58,s58,94388224\nv_lshl_add_u32 v5,v52,11,s58\n" \
"v_bfe_u32 v52,v4,4,3\nM v[116:119],a[208:211],a[160:163],v[40:43]\ns_lshl_b32 s58,%2,13\nv_lshl_add_u32 v5,v52,3,v5\nv_mov_b32 v52,s10\nM v[116:119],a[212:215],a[164:167],v[116:119]\ns_add_u32 s58,s58,0x20000\nv_max3_f32 v15,v16,v17,v18\n" \
"v_max_f32 v15,v15,v19\nM v[116:119],a[216:219],a[168:171],v[116:119]\nLG17_%=:\ns_cbranch_execz LHR2_%=\nv_cmp_nge_f32 vcc,0x53800000,v15\nv_add_u32 v12,s58,v4\nM v[116:119],a[220:223],a[172:175],v[116:119]\ns_lshl_b32 s58,s7,1\n" \
"v_xor_b32 v57,0x4000,v12\nM v[124:127],a[208:211],a[176:179],v[44:47]\ns_sub_u32 s58,15,s58\nds_read_b128 a[208:211],v14 offset:4096\nM v[124:127],a[212:215],a[180:183],v[124:127]\ns_cbranch_vccnz Lcs43_%=\nLcr44_%=:\nds_read_b128 a[212:215],v14 offset:5120\n" \
"M v[124:127],a[216:219],a[184:187],v[124:127]\ns_cmp_eq_u32 s52,s55\nds_read_b128 a[216:219],v14 offset:6144\nM v[124:127],a[220:223],a[188:191],v[124:127]\ns_cbranch_scc1 LH1_%=\nLH1R10_%=:\ns_waitcnt lgkmcnt(3)\nds_read_b128 a[220:223],v14 offset:7168\n" \
"v_xor_b32 v13,0x10000,v13\nM v[128:131],a[192:195],v[64:67],v[128:131]\nbuffer_store_dwordx4 v[48:51],v4,s[36:39],s73 offen offset:0 sc1\nM v[160:163],a[192:195],v[72:75],v[160:163]\ns_lshl_b32 s59,s58,20\nv_exp_f32 v96,v96\nM v[192:195],a[192:195],v[80:83],v[192:195]\ns_add_u32 s51,s68,s59\n" \
"v_exp_f32 v97,v97\nM v[224:227],a[192:195],v[88:91],v[224:227]\ns_lshl_b32 s59,s54,6\nv_exp_f32 v98,v98\nM v[228:231],a[196:199],v[88:91],v[228:231]\nbuffer_load_dwordx4 v22,s[40:43],s48 offen offset:1024 sc1 lds\nM v[196:199],a[196:199],v[80:83],v[196:199]\ns_add_u32 s51,s51,s59\n" \
"v_exp_f32 v99,v99\nM v[164:167],a[196:199],v[72:75],v[164:167]\ns_waitcnt vmcnt(15) lgkmcnt(0)\nv_exp_f32 v100,v100\nM v[132:135],a[196:199],v[64:67],v[132:135]\ns_add_u32 s62,s51,0\ns_barrier\nds_read_b128 a[192:195],v13 offset:32768\n" \
"v_exp_f32 v101,v101\nM v[136:139],a[200:203],v[64:67],v[136:139]\ns_lshl_b32 s59,s58,10\nv_exp_f32 v102,v102\nM v[168:171],a[200:203],v[72:75],v[168:171]\ns_add_u32 s50,s69,s59\nv_exp_f32 v103,v103\nM v[200:203],a[200:203],v[80:83],v[200:203]\n" \
"buffer_load_dwordx4 v22,s[40:43],s48 offen offset:2048 sc1 lds\nv_exp_f32 v104,v104\nM v[232:235],a[200:203],v[88:91],v[232:235]\ns_lshr_b32 s59,s54,4\nv_exp_f32 v105,v105\nM v[236:239],a[204:207],v[88:91],v[236:239]\ns_add_u32 s50,s50,s59\nv_exp_f32 v106,v106\n" \
"M v[204:207],a[204:207],v[80:83],v[204:207]\nv_exp_f32 v107,v107\nM v[172:175],a[204:207],v[72:75],v[172:175]\nbuffer_load_dwordx4 v22,s[40:43],s48 offen offset:3072 sc1 lds\nv_exp_f32 v108,v108\nM v[140:143],a[204:207],v[64:67],v[140:143]\ns_add_u32 m0,s22,114688\nv_exp_f32 v109,v109\n" \
"M v[144:147],a[208:211],v[64:67],v[144:147]\ns_add_u32 s48,s48,s49\nv_exp_f32 v110,v110\nM v[176:179],a[208:211],v[72:75],v[176:179]\nv_exp_f32 v111,v111\nM v[208:211],a[208:211],v[80:83],v[208:211]\nv_exp_f32 v112,v112\nM v[240:243],a[208:211],v[88:91],v[240:243]\n" \
"v_exp_f32 v113,v113\nM v[244:247],a[212:215],v[88:91],v[244:247]\nv_exp_f32 v114,v114\nM v[212:215],a[212:215],v[80:83],v[212:215]\nv_exp_f32 v115,v115\nM v[180:183],a[212:215],v[72:75],v[180:183]\nds_read_b128 a[196:199],v13 offset:34816\nds_read_b128 a[200:203],v13 offset:36864\n" \
"v_exp_f32 v116,v116\nM v[148:151],a[212:215],v[64:67],v[148:151]\nv_exp_f32 v117,v117\nM v[152:155],a[216:219],v[64:67],v[152:155]\nbuffer_load_dwordx4 v1,s[84:87],s46 offen offset:0 lds\nM v[184:187],a[216:219],v[72:75],v[184:187]\ns_add_u32 m0,s22,115584\nds_read_b128 a[204:207],v13 offset:38912\n" \
"C v96,v96,v97\nv_exp_f32 v118,v118\nM v[216:219],a[216:219],v[80:83],v[216:219]\nv_exp_f32 v119,v119\nM v[248:251],a[216:219],v[88:91],v[248:251]\nC v97,v98,v99\nC v104,v104,v105\nM v[252:255],a[220:223],v[88:91],v[252:255]\n" \
"buffer_load_dwordx4 v1,s[84:87],s46 offen offset:128 lds\nM v[220:223],a[220:223],v[80:83],v[220:223]\ns_add_u32 m0,s22,116736\nds_read_b128 a[208:211],v13 offset:33792\nC v98,v100,v101\nM v[188:191],a[220:223],v[72:75],v[188:191]\ns_mov_b32 s46,327680\nds_read_b128 a[212:215],v13 offset:35840\n" \
"C v99,v102,v103\nM v[156:159],a[220:223],v[64:67],v[156:159]\ns_waitcnt lgkmcnt(2)\nv_exp_f32 v120,v120\nM v[64:67],a[192:195],a[128:131],v[32:35]\nbuffer_load_dwordx4 v1,s[84:87],s46 offen offset:0 lds\nM v[64:67],a[196:199],a[132:135],v[64:67]\ns_add_u32 m0,s22,117632\n" \
"ds_read_b128 a[216:219],v13 offset:37888\nC v105,v106,v107\nM v[24:27],a[224:227],v[96:99],v[16:19]\nds_read_b128 a[220:223],v13 offset:39936\nC v106,v108,v109\nM v[64:67],a[200:203],a[136:139],v[64:67]\nv_exp_f32 v121,v121\nM v[64:67],a[204:207],a[140:143],v[64:67]\n" \
"buffer_load_dwordx4 v1,s[84:87],s46 offen offset:128 lds\nM v[72:75],a[192:195],a[144:147],v[36:39]\ns_add_u32 m0,s22,0\nC v107,v110,v111\nC v112,v112,v113\nM v[72:75],a[196:199],a[148:151],v[72:75]\ns_mov_b32 s46,393216\nv_exp_f32 v122,v122\n" \
"M v[24:27],a[228:231],v[104:107],v[24:27]\nv_exp_f32 v123,v123\nM v[72:75],a[200:203],a[152:155],v[72:75]\nbuffer_load_dwordx4 v22,s[40:43],s45 offen offset:0 sc1 lds\nM v[72:75],a[204:207],a[156:159],v[72:75]\nv_exp_f32 v124,v124\nM v[80:83],a[192:195],a[160:163],v[40:43]\nC v113,v114,v115\n" \
"C v114,v116,v117\nM v[80:83],a[196:199],a[164:167],v[80:83]\nC v115,v118,v119\nC v120,v120,v121\nM v[80:83],a[200:203],a[168:171],v[80:83]\nbuffer_load_dwordx4 v22,s[40:43],s45 offen offset:1024 sc1 lds\nM v[80:83],a[204:207],a[172:175],v[80:83]\nv_exp_f32 v125,v125\n" \
"M v[88:91],a[192:195],a[176:179],v[44:47]\nds_read_b128 a[192:195],v14 offset:16384\nC v121,v122,v123\nM v[88:91],a[196:199],a[180:183],v[88:91]\nv_exp_f32 v126,v126\nM v[88:91],a[200:203],a[184:187],v[88:91]\nbuffer_load_dwordx4 v22,s[40:43],s45 offen offset:2048 sc1 lds\nM v[88:91],a[204:207],a[188:191],v[88:91]\n" \
"ds_read_b128 a[196:199],v14 offset:17408\nC v122,v124,v125\nM v[24:27],a[232:235],v[112:115],v[24:27]\ns_waitcnt lgkmcnt(2)\nds_read_b128 a[200:203],v14 offset:18432\nv_xor_b32 v13,0x10000,v13\nM v[68:71],a[208:211],a[128:131],v[32:35]\nds_read_b64_tr_b16 a[0:1],v6 offset:49152\n" \
"M v[68:71],a[212:215],a[132:135],v[68:71]\nbuffer_load_dwordx4 v22,s[40:43],s45 offen offset:3072 sc1 lds\nM v[68:71],a[216:219],a[136:139],v[68:71]\ns_add_u32 m0,s22,32768\nds_read_b64_tr_b16 a[2:3],v7 offset:49152\nM v[68:71],a[220:223],a[140:143],v[68:71]\ns_add_u32 s45,s45,s49\nds_read_b64_tr_b16 a[4:5],v8 offset:49152\n" \
"M v[76:79],a[208:211],a[144:147],v[36:39]\nv_exp_f32 v127,v127\nM v[76:79],a[212:215],a[148:151],v[76:79]\nbuffer_store_dwordx4 v[48:51],v4,s[36:39],s73 offen offset:1024 sc1\nM v[76:79],a[216:219],a[152:155],v[76:79]\nds_read_b64_tr_b16 a[6:7],v9 offset:49152\nC v123,v126,v127\nM v[76:79],a[220:223],a[156:159],v[76:79]\n" \
"ds_read_b64_tr_b16 a[8:9],v6 offset:51200\nds_read_b64_tr_b16 a[10:11],v7 offset:51200\nM v[24:27],a[236:239],v[120:123],v[24:27]\nds_read_b64_tr_b16 a[12:13],v8 offset:51200\nv_xor_b32 v6,0x10000,v6\nM v[84:87],a[208:211],a[160:163],v[40:43]\nds_read_b64_tr_b16 a[14:15],v9 offset:51200\nds_read_b128 a[204:207],v14 offset:19456\n" \
"M v[84:87],a[212:215],a[164:167],v[84:87]\nv_max3_f32 v15,v24,v25,v26\nv_max_f32 v15,v15,v27\nM v[84:87],a[216:219],a[168:171],v[84:87]\nv_cmp_nge_f32 vcc,0x53800000,v15\nv_xor_b32 v7,0x10000,v7\nM v[84:87],a[220:223],a[172:175],v[84:87]\nv_xor_b32 v8,0x10000,v8\n" \
"v_xor_b32 v9,0x10000,v9\nM v[92:95],a[208:211],a[176:179],v[44:47]\nds_read_b128 a[208:211],v14 offset:20480\nM v[92:95],a[212:215],a[180:183],v[92:95]\ns_cbranch_vccnz Lcs45_%=\nLcr46_%=:\nds_read_b128 a[212:215],v14 offset:21504\nM v[92:95],a[216:219],a[184:187],v[92:95]\n" \
"ds_read_b128 a[216:219],v14 offset:22528\nM v[92:95],a[220:223],a[188:191],v[92:95]\ns_waitcnt lgkmcnt(3)\nds_read_b128 a[220:223],v14 offset:23552\nM v[128:131],a[192:195],v[96:99],v[128:131]\nv_exp_f32 v64,v64\nM v[160:163],a[192:195],v[104:107],v[160:163]\nv_exp_f32 v65,v65\n" \
"M v[192:195],a[192:195],v[112:115],v[192:195]\nv_exp_f32 v66,v66\nM v[224:227],a[192:195],v[120:123],v[224:227]\nv_exp_f32 v67,v67\nM v[228:231],a[196:199],v[120:123],v[228:231]\nv_exp_f32 v68,v68\nM v[196:199],a[196:199],v[112:115],v[196:199]\nv_exp_f32 v69,v69\n" \
"M v[164:167],a[196:199],v[104:107],v[164:167]\ns_waitcnt vmcnt(9) lgkmcnt(0)\nv_exp_f32 v70,v70\nM v[132:135],a[196:199],v[96:99],v[132:135]\ns_barrier\nbuffer_load_dwordx4 v22,s[40:43],s48 offen offset:0 sc1 lds\nM v[136:139],a[200:203],v[96:99],v[136:139]\nv_exp_f32 v71,v71\n" \
"M v[168:171],a[200:203],v[104:107],v[168:171]\nv_exp_f32 v72,v72\nM v[200:203],a[200:203],v[112:115],v[200:203]\nv_exp_f32 v73,v73\nM v[232:235],a[200:203],v[120:123],v[232:235]\nbuffer_load_dwordx4 v22,s[40:43],s48 offen offset:1024 sc1 lds\nM v[236:239],a[204:207],v[120:123],v[236:239]\nds_read_b128 a[192:195],v13 offset:32768\n" \
"C v64,v64,v65\nM v[204:207],a[204:207],v[112:115],v[204:207]\nv_exp_f32 v74,v74\nM v[172:175],a[204:207],v[104:107],v[172:175]\nv_exp_f32 v75,v75\nM v[140:143],a[204:207],v[96:99],v[140:143]\nbuffer_load_dwordx4 v22,s[40:43],s48 offen offset:2048 sc1 lds\nv_exp_f32 v76,v76\n" \
"M v[144:147],a[208:211],v[96:99],v[144:147]\nv_exp_f32 v77,v77\nM v[176:179],a[208:211],v[104:107],v[176:179]\nds_read_b128 a[196:199],v13 offset:34816\nds_read_b128 a[200:203],v13 offset:36864\nM v[208:211],a[208:211],v[112:115],v[208:211]\nv_exp_f32 v78,v78\nM v[240:243],a[208:211],v[120:123],v[240:243]\n" \
"buffer_load_dwordx4 v22,s[40:43],s48 offen offset:3072 sc1 lds\nv_exp_f32 v79,v79\nM v[244:247],a[212:215],v[120:123],v[244:247]\ns_add_u32 m0,s22,16384\nv_exp_f32 v80,v80\nM v[212:215],a[212:215],v[112:115],v[212:215]\ns_add_u32 s48,s48,s49\nds_read_b128 a[204:207],v13 offset:38912\n" \
"C v65,v66,v67\nM v[180:183],a[212:215],v[104:107],v[180:183]\ns_add_u32 s52,s52,1\nv_exp_f32 v81,v81\nM v[148:151],a[212:215],v[96:99],v[148:151]\nbuffer_load_dwordx4 v1,s[84:87],s46 offen offset:0 lds\nM v[152:155],a[216:219],v[96:99],v[152:155]\ns_add_u32 m0,s22,17280\n" \
"v_exp_f32 v82,v82\nM v[184:187],a[216:219],v[104:107],v[184:187]\nv_exp_f32 v83,v83\nM v[216:219],a[216:219],v[112:115],v[216:219]\nv_exp_f32 v84,v84\nM v[248:251],a[216:219],v[120:123],v[248:251]\nbuffer_load_dwordx4 v1,s[84:87],s46 offen offset:128 lds\nM v[252:255],a[220:223],v[120:123],v[252:255]\n" \
"s_add_u32 m0,s22,18432\nv_exp_f32 v85,v85\nM v[220:223],a[220:223],v[112:115],v[220:223]\ns_mov_b32 s46,458752\nv_exp_f32 v86,v86\nM v[188:191],a[220:223],v[104:107],v[188:191]\ns_cmp_eq_u32 s52,s56\nC v66,v68,v69\n" \
"C v67,v70,v71\nM v[156:159],a[220:223],v[96:99],v[156:159]\ns_waitcnt lgkmcnt(0)\nbuffer_load_dwordx4 v1,s[84:87],s46 offen offset:0 lds\nM v[96:99],a[192:195],a[128:131],v[32:35]\ns_cbranch_scc1 LEV_%=\nLEVR10_%=:\nds_read_b128 a[208:211],v13 offset:33792\n" \
"C v72,v72,v73\nM v[96:99],a[196:199],a[132:135],v[96:99]\ns_add_u32 m0,s22,19328\nds_read_b128 a[212:215],v13 offset:35840\nC v73,v74,v75\nM v[16:19],a[224:227],v[64:67],v[24:27]\nds_read_b128 a[216:219],v13 offset:37888\nC v74,v76,v77\n" \
"M v[96:99],a[200:203],a[136:139],v[96:99]\nv_exp_f32 v87,v87\nM v[96:99],a[204:207],a[140:143],v[96:99]\nbuffer_load_dwordx4 v1,s[84:87],s46 offen offset:128 lds\nM v[104:107],a[192:195],a[144:147],v[36:39]\ns_add_u32 m0,s22,49152\nds_read_b128 a[220:223],v13 offset:39936\nC v75,v78,v79\n" \
"v_exp_f32 v88,v88\nM v[104:107],a[196:199],a[148:151],v[104:107]\ns_mov_b32 s46,524288\nv_exp_f32 v89,v89\nM v[16:19],a[228:231],v[72:75],v[16:19]\nv_exp_f32 v90,v90\nM v[104:107],a[200:203],a[152:155],v[104:107]\nbuffer_load_dwordx4 v22,s[40:43],s45 offen offset:0 sc1 lds\n" \
"M v[104:107],a[204:207],a[156:159],v[104:107]\nv_exp_f32 v91,v91\nM v[112:115],a[192:195],a[160:163],v[40:43]\nv_exp_f32 v92,v92\nM v[112:115],a[196:199],a[164:167],v[112:115]\nC v80,v80,v81\nC v81,v82,v83\nM v[112:115],a[200:203],a[168:171],v[112:115]\n" \
"buffer_load_dwordx4 v22,s[40:43],s45 offen offset:1024 sc1 lds\nM v[112:115],a[204:207],a[172:175],v[112:115]\nv_exp_f32 v93,v93\nM v[120:123],a[192:195],a[176:179],v[44:47]\nC v82,v84,v85\nC v83,v86,v87\nM v[120:123],a[196:199],a[180:183],v[120:123]\nv_exp_f32 v94,v94\n" \
"M v[120:123],a[200:203],a[184:187],v[120:123]\nbuffer_load_dwordx4 v22,s[40:43],s45 offen offset:2048 sc1 lds\nM v[120:123],a[204:207],a[188:191],v[120:123]\nC v88,v88,v89\nC v89,v90,v91\nM v[16:19],a[232:235],v[80:83],v[16:19]\ns_waitcnt lgkmcnt(0)\nds_read_b64_tr_b16 a[16:17],v6 offset:0\n" \
"ds_read_b64_tr_b16 a[18:19],v7 offset:0\nM v[100:103],a[208:211],a[128:131],v[32:35]\nds_read_b64_tr_b16 a[20:21],v8 offset:0\nC v90,v92,v93\nM v[100:103],a[212:215],a[132:135],v[100:103]\nbuffer_load_dwordx4 v22,s[40:43],s45 offen offset:3072 sc1 lds\nM v[100:103],a[216:219],a[136:139],v[100:103]\ns_add_u32 m0,s22,65536\n" \
"v_exp_f32 v95,v95\nM v[100:103],a[220:223],a[140:143],v[100:103]\ns_add_u32 s45,s45,s49\nds_read_b64_tr_b16 a[22:23],v9 offset:0\nds_read_b64_tr_b16 a[24:25],v6 offset:2048\nM v[108:111],a[208:211],a[144:147],v[36:39]\nds_read_b64_tr_b16 a[26:27],v7 offset:2048\nv_xor_b32 v14,0x10000,v14\n" \
"M v[108:111],a[212:215],a[148:151],v[108:111]\nds_read_b128 a[192:195],v14 offset:16384\nC v91,v94,v95\nM v[108:111],a[216:219],a[152:155],v[108:111]\nds_read_b128 a[196:199],v14 offset:17408\nv_xor_b32 v13,0x10000,v13\nM v[108:111],a[220:223],a[156:159],v[108:111]\nds_read_b128 a[200:203],v14 offset:18432\n" \
"M v[16:19],a[236:239],v[88:91],v[16:19]\nds_read_b64_tr_b16 a[28:29],v8 offset:2048\nds_read_b64_tr_b16 a[30:31],v9 offset:2048\nM v[116:119],a[208:211],a[160:163],v[40:43]\nds_read_b128 a[204:207],v14 offset:19456\nM v[116:119],a[212:215],a[164:167],v[116:119]\nv_max3_f32 v15,v16,v17,v18\nv_max_f32 v15,v15,v19\n" \
"M v[116:119],a[216:219],a[168:171],v[116:119]\nv_cmp_nge_f32 vcc,0x53800000,v15\nM v[116:119],a[220:223],a[172:175],v[116:119]\nM v[124:127],a[208:211],a[176:179],v[44:47]\nds_read_b128 a[208:211],v14 offset:20480\nM v[124:127],a[212:215],a[180:183],v[124:127]\ns_cbranch_vccnz Lcs47_%=\nLcr48_%=:\n" \
"ds_read_b128 a[212:215],v14 offset:21504\nM v[124:127],a[216:219],a[184:187],v[124:127]\nds_read_b128 a[216:219],v14 offset:22528\nM v[124:127],a[220:223],a[188:191],v[124:127]\ns_waitcnt lgkmcnt(3)\nds_read_b128 a[220:223],v14 offset:23552\nv_xor_b32 v14,0x10000,v14\nM v[128:131],a[192:195],v[64:67],v[128:131]\n" \
"v_exp_f32 v96,v96\nM v[160:163],a[192:195],v[72:75],v[160:163]\nv_exp_f32 v97,v97\nM v[192:195],a[192:195],v[80:83],v[192:195]\nv_exp_f32 v98,v98\nM v[224:227],a[192:195],v[88:91],v[224:227]\nv_exp_f32 v99,v99\nM v[228:231],a[196:199],v[88:91],v[228:231]\n" \
"v_exp_f32 v100,v100\nM v[196:199],a[196:199],v[80:83],v[196:199]\nv_exp_f32 v101,v101\nM v[164:167],a[196:199],v[72:75],v[164:167]\ns_waitcnt vmcnt(8) lgkmcnt(0)\nv_exp_f32 v102,v102\nM v[132:135],a[196:199],v[64:67],v[132:135]\ns_barrier\n" \
"buffer_load_dwordx4 v22,s[40:43],s48 offen offset:0 sc1 lds\nM v[136:139],a[200:203],v[64:67],v[136:139]\nv_exp_f32 v103,v103\nM v[168:171],a[200:203],v[72:75],v[168:171]\nv_exp_f32 v104,v104\nM v[200:203],a[200:203],v[80:83],v[200:203]\nv_exp_f32 v105,v105\nM v[232:235],a[200:203],v[88:91],v[232:235]\n" \
"buffer_load_dwordx4 v22,s[40:43],s48 offen offset:1024 sc1 lds\nM v[236:239],a[204:207],v[88:91],v[236:239]\nds_read_b128 a[192:195],v13 offset:0\nC v96,v96,v97\nM v[204:207],a[204:207],v[80:83],v[204:207]\nv_exp_f32 v106,v106\nM v[172:175],a[204:207],v[72:75],v[172:175]\nds_read_b128 a[196:199],v13 offset:2048\n" \
"ds_read_b128 a[200:203],v13 offset:4096\nM v[140:143],a[204:207],v[64:67],v[140:143]\nbuffer_load_dwordx4 v22,s[40:43],s48 offen offset:2048 sc1 lds\nM v[144:147],a[208:211],v[64:67],v[144:147]\nv_exp_f32 v107,v107\nM v[176:179],a[208:211],v[72:75],v[176:179]\nds_read_b128 a[204:207],v13 offset:6144\nC v97,v98,v99\n" \
"M v[208:211],a[208:211],v[80:83],v[208:211]\nv_exp_f32 v108,v108\nM v[240:243],a[208:211],v[88:91],v[240:243]\nbuffer_load_dwordx4 v22,s[40:43],s48 offen offset:3072 sc1 lds\nv_exp_f32 v109,v109\nM v[244:247],a[212:215],v[88:91],v[244:247]\ns_add_u32 m0,s22,98304\nv_exp_f32 v110,v110\n" \
"M v[212:215],a[212:215],v[80:83],v[212:215]\ns_add_u32 s48,s48,s49\nv_exp_f32 v111,v111\nM v[180:183],a[212:215],v[72:75],v[180:183]\nv_exp_f32 v112,v112\nM v[148:151],a[212:215],v[64:67],v[148:151]\nbuffer_load_dwordx4 v1,s[84:87],s46 offen offset:0 lds\nM v[152:155],a[216:219],v[64:67],v[152:155]\n" \
"s_add_u32 m0,s22,99200\nv_exp_f32 v113,v113\nM v[184:187],a[216:219],v[72:75],v[184:187]\nv_exp_f32 v114,v114\nM v[216:219],a[216:219],v[80:83],v[216:219]\nv_exp_f32 v115,v115\nM v[248:251],a[216:219],v[88:91],v[248:251]\nbuffer_load_dwordx4 v1,s[84:87],s46 offen offset:128 lds\n" \
"M v[252:255],a[220:223],v[88:91],v[252:255]\ns_add_u32 m0,s22,100352\nv_exp_f32 v116,v116\nM v[220:223],a[220:223],v[80:83],v[220:223]\ns_mov_b32 s46,589824\nC v98,v100,v101\nC v99,v102,v103\nM v[188:191],a[220:223],v[72:75],v[188:191]\n" \
"s_waitcnt lgkmcnt(0)\nv_exp_f32 v117,v117\nM v[156:159],a[220:223],v[64:67],v[156:159]\nbuffer_load_dwordx4 v1,s[84:87],s46 offen offset:0 lds\nM v[64:67],a[192:195],a[128:131],v[32:35]\ns_add_u32 m0,s22,101248\nds_read_b128 a[208:211],v13 offset:1024\nC v104,v104,v105\n" \
"M v[64:67],a[196:199],a[132:135],v[64:67]\nds_read_b128 a[212:215],v13 offset:3072\nC v105,v106,v107\nM v[24:27],a[224:227],v[96:99],v[16:19]\nds_read_b128 a[216:219],v13 offset:5120\nC v106,v108,v109\nM v[64:67],a[200:203],a[136:139],v[64:67]\nbuffer_load_dwordx4 v1,s[84:87],s46 offen offset:128 lds\n" \
"M v[64:67],a[204:207],a[140:143],v[64:67]\ns_add_u32 m0,s22,81920\nds_read_b128 a[220:223],v13 offset:7168\nC v107,v110,v111\nM v[72:75],a[192:195],a[144:147],v[36:39]\ns_mov_b32 s46,655360\nv_exp_f32 v118,v118\nM v[72:75],a[196:199],a[148:151],v[72:75]\n" \
"v_exp_f32 v119,v119\nM v[24:27],a[228:231],v[104:107],v[24:27]\nbuffer_load_dwordx4 v22,s[40:43],s45 offen offset:0 sc1 lds\nM v[72:75],a[200:203],a[152:155],v[72:75]\ns_waitcnt vmcnt(21)\nv_exp_f32 v120,v120\nM v[72:75],a[204:207],a[156:159],v[72:75]\nv_exp_f32 v121,v121\n" \
"M v[80:83],a[192:195],a[160:163],v[40:43]\nv_exp_f32 v122,v122\nM v[80:83],a[196:199],a[164:167],v[80:83]\nbuffer_store_dwordx2 v[52:53],v56,s[36:39],s95 offen offset:24 sc1\nM v[80:83],a[200:203],a[168:171],v[80:83]\nC v112,v112,v113\nC v113,v114,v115\nM v[80:83],a[204:207],a[172:175],v[80:83]\n" \
"buffer_load_dwordx4 v22,s[40:43],s45 offen offset:1024 sc1 lds\nM v[88:91],a[192:195],a[176:179],v[44:47]\nds_read_b128 a[192:195],v14 offset:32768\nC v114,v116,v117\nM v[88:91],a[196:199],a[180:183],v[88:91]\nv_exp_f32 v123,v123\nM v[88:91],a[200:203],a[184:187],v[88:91]\nds_read_b128 a[196:199],v14 offset:33792\n" \
"C v115,v118,v119\nM v[88:91],a[204:207],a[188:191],v[88:91]\nbuffer_load_dwordx4 v22,s[40:43],s45 offen offset:2048 sc1 lds\nM v[24:27],a[232:235],v[112:115],v[24:27]\ns_waitcnt lgkmcnt(2)\nds_read_b128 a[200:203],v14 offset:34816\nC v120,v120,v121\nM v[68:71],a[208:211],a[128:131],v[32:35]\n" \
"v_exp_f32 v124,v124\nM v[68:71],a[212:215],a[132:135],v[68:71]\nv_exp_f32 v125,v125\nM v[68:71],a[216:219],a[136:139],v[68:71]\nv_exp_f32 v126,v126\nM v[68:71],a[220:223],a[140:143],v[68:71]\nbuffer_load_dwordx4 v22,s[40:43],s45 offen offset:3072 sc1 lds\nM v[76:79],a[208:211],a[144:147],v[36:39]\n" \
"s_add_u32 m0,s22,114688\nds_read_b64_tr_b16 a[32:33],v6 offset:49152\nC v121,v122,v123\nM v[76:79],a[212:215],a[148:151],v[76:79]\ns_add_u32 s45,s45,s49\nv_exp_f32 v127,v127\nM v[76:79],a[216:219],a[152:155],v[76:79]\nC v122,v124,v125\n" \
"C v123,v126,v127\nM v[76:79],a[220:223],a[156:159],v[76:79]\nds_read_b64_tr_b16 a[34:35],v7 offset:49152\nds_read_b64_tr_b16 a[36:37],v8 offset:49152\nM v[24:27],a[236:239],v[120:123],v[24:27]\nds_read_b64_tr_b16 a[38:39],v9 offset:49152\nM v[84:87],a[208:211],a[160:163],v[40:43]\nds_read_b64_tr_b16 a[40:41],v6 offset:51200\n" \
"ds_read_b64_tr_b16 a[42:43],v7 offset:51200\nM v[84:87],a[212:215],a[164:167],v[84:87]\nds_read_b64_tr_b16 a[44:45],v8 offset:51200\nv_max3_f32 v15,v24,v25,v26\nM v[84:87],a[216:219],a[168:171],v[84:87]\nds_read_b64_tr_b16 a[46:47],v9 offset:51200\nv_max_f32 v15,v15,v27\nM v[84:87],a[220:223],a[172:175],v[84:87]\n" \
"ds_read_b128 a[204:207],v14 offset:35840\nv_cmp_nge_f32 vcc,0x53800000,v15\nM v[92:95],a[208:211],a[176:179],v[44:47]\nds_read_b128 a[208:211],v14 offset:36864\nv_xor_b32 v6,0x10000,v6\nM v[92:95],a[212:215],a[180:183],v[92:95]\ns_cbranch_vccnz Lcs49_%=\nLcr50_%=:\n" \
"ds_read_b128 a[212:215],v14 offset:37888\nv_xor_b32 v7,0x10000,v7\nM v[92:95],a[216:219],a[184:187],v[92:95]\nds_read_b128 a[216:219],v14 offset:38912\nv_xor_b32 v8,0x10000,v8\nM v[92:95],a[220:223],a[188:191],v[92:95]\ns_waitcnt lgkmcnt(5)\nds_read_b128 a[220:223],v14 offset:39936\n" \
"v_xor_b32 v9,0x10000,v9\nM v[128:131],a[192:195],v[96:99],v[128:131]\nv_exp_f32 v64,v64\nM v[160:163],a[192:195],v[104:107],v[160:163]\nv_exp_f32 v65,v65\nM v[192:195],a[192:195],v[112:115],v[192:195]\nv_exp_f32 v66,v66\nM v[224:227],a[192:195],v[120:123],v[224:227]\n" \
"v_exp_f32 v67,v67\nM v[228:231],a[196:199],v[120:123],v[228:231]\nv_exp_f32 v68,v68\nM v[196:199],a[196:199],v[112:115],v[196:199]\nv_exp_f32 v69,v69\nM v[164:167],a[196:199],v[104:107],v[164:167]\ns_waitcnt vmcnt(9) lgkmcnt(0)\nv_exp_f32 v70,v70\n" \
"M v[132:135],a[196:199],v[96:99],v[132:135]\ns_barrier\nbuffer_load_dwordx4 v22,s[40:43],s48 offen offset:0 sc1 lds\nM v[136:139],a[200:203],v[96:99],v[136:139]\nv_exp_f32 v71,v71\nM v[168:171],a[200:203],v[104:107],v[168:171]\nv_exp_f32 v72,v72\nM v[200:203],a[200:203],v[112:115],v[200:203]\n" \
"v_exp_f32 v73,v73\nM v[232:235],a[200:203],v[120:123],v[232:235]\nbuffer_load_dwordx4 v22,s[40:43],s48 offen offset:1024 sc1 lds\nM v[236:239],a[204:207],v[120:123],v[236:239]\nds_read_b128 a[192:195],v13 offset:49152\nC v64,v64,v65\nM v[204:207],a[204:207],v[112:115],v[204:207]\nv_exp_f32 v74,v74\n" \
"M v[172:175],a[204:207],v[104:107],v[172:175]\nLG18_%=:\ns_cbranch_execz LHR2_%=\nds_read_b128 a[196:199],v13 offset:51200\nds_read_b128 a[200:203],v13 offset:53248\nM v[140:143],a[204:207],v[96:99],v[140:143]\nbuffer_load_dwordx4 v22,s[40:43],s48 offen offset:2048 sc1 lds\nv_exp_f32 v75,v75\n" \
"M v[144:147],a[208:211],v[96:99],v[144:147]\nv_exp_f32 v76,v76\nM v[176:179],a[208:211],v[104:107],v[176:179]\nds_read_b128 a[204:207],v13 offset:55296\nC v65,v66,v67\nM v[208:211],a[208:211],v[112:115],v[208:211]\nv_exp_f32 v77,v77\nM v[240:243],a[208:211],v[120:123],v[240:243]\n" \
"buffer_load_dwordx4 v22,s[40:43],s48 offen offset:3072 sc1 lds\nv_exp_f32 v78,v78\nM v[244:247],a[212:215],v[120:123],v[244:247]\ns_add_u32 m0,s22,0\nv_exp_f32 v79,v79\nM v[212:215],a[212:215],v[112:115],v[212:215]\ns_add_u32 s48,s48,s49\nv_exp_f32 v80,v80\n" \
"M v[180:183],a[212:215],v[104:107],v[180:183]\ns_add_u32 s52,s52,1\nv_exp_f32 v81,v81\nM v[148:151],a[212:215],v[96:99],v[148:151]\nbuffer_load_dwordx4 v1,s[84:87],s46 offen offset:0 lds\nM v[152:155],a[216:219],v[96:99],v[152:155]\ns_add_u32 m0,s22,896\nv_exp_f32 v82,v82\n" \
"M v[184:187],a[216:219],v[104:107],v[184:187]\nv_exp_f32 v83,v83\nM v[216:219],a[216:219],v[112:115],v[216:219]\nv_exp_f32 v84,v84\nM v[248:251],a[216:219],v[120:123],v[248:251]\nbuffer_load_dwordx4 v1,s[84:87],s46 offen offset:128 lds\nM v[252:255],a[220:223],v[120:123],v[252:255]\ns_add_u32 m0,s22,2048\n" \
"v_exp_f32 v85,v85\nM v[220:223],a[220:223],v[112:115],v[220:223]\ns_mov_b32 s46,720896\nv_exp_f32 v86,v86\nM v[188:191],a[220:223],v[104:107],v[188:191]\ns_cmp_eq_u32 s52,s56\nC v66,v68,v69\nC v67,v70,v71\n" \
"M v[156:159],a[220:223],v[96:99],v[156:159]\ns_waitcnt lgkmcnt(0)\nbuffer_load_dwordx4 v1,s[84:87],s46 offen offset:0 lds\nM v[96:99],a[192:195],a[128:131],v[32:35]\ns_cbranch_scc1 LEV_%=\nLEVR11_%=:\nds_read_b128 a[208:211],v13 offset:50176\nC v72,v72,v73\n" \
"M v[96:99],a[196:199],a[132:135],v[96:99]\ns_add_u32 m0,s22,2944\nds_read_b128 a[212:215],v13 offset:52224\nC v73,v74,v75\nM v[16:19],a[224:227],v[64:67],v[24:27]\nds_read_b128 a[216:219],v13 offset:54272\nC v74,v76,v77\nM v[96:99],a[200:203],a[136:139],v[96:99]\n" \
"v_exp_f32 v87,v87\nM v[96:99],a[204:207],a[140:143],v[96:99]\nbuffer_load_dwordx4 v1,s[84:87],s46 offen offset:128 lds\nM v[104:107],a[192:195],a[144:147],v[36:39]\ns_add_u32 m0,s22,32768\nds_read_b128 a[220:223],v13 offset:56320\nC v75,v78,v79\nv_exp_f32 v88,v88\n" \
"M v[104:107],a[196:199],a[148:151],v[104:107]\ns_mov_b32 s46,786432\nv_exp_f32 v89,v89\nM v[16:19],a[228:231],v[72:75],v[16:19]\nv_exp_f32 v90,v90\nM v[104:107],a[200:203],a[152:155],v[104:107]\nbuffer_load_dwordx4 v22,s[40:43],s45 offen offset:0 sc1 lds\nM v[104:107],a[204:207],a[156:159],v[104:107]\n" \
"v_exp_f32 v91,v91\nM v[112:115],a[192:195],a[160:163],v[40:43]\nv_exp_f32 v92,v92\nM v[112:115],a[196:199],a[164:167],v[112:115]\nC v80,v80,v81\nC v81,v82,v83\nM v[112:115],a[200:203],a[168:171],v[112:115]\nbuffer_load_dwordx4 v22,s[40:43],s45 offen offset:1024 sc1 lds\n" \
"M v[112:115],a[204:207],a[172:175],v[112:115]\nv_exp_f32 v93,v93\nM v[120:123],a[192:195],a[176:179],v[44:47]\nC v82,v84,v85\nC v83,v86,v87\nM v[120:123],a[196:199],a[180:183],v[120:123]\nv_exp_f32 v94,v94\nM v[120:123],a[200:203],a[184:187],v[120:123]\n" \
"buffer_load_dwordx4 v22,s[40:43],s45 offen offset:2048 sc1 lds\nM v[120:123],a[204:207],a[188:191],v[120:123]\nC v88,v88,v89\nC v89,v90,v91\nM v[16:19],a[232:235],v[80:83],v[16:19]\ns_waitcnt lgkmcnt(0)\nds_read_b64_tr_b16 a[48:49],v6 offset:16384\nds_read_b64_tr_b16 a[50:51],v7 offset:16384\n" \
"M v[100:103],a[208:211],a[128:131],v[32:35]\nds_read_b64_tr_b16 a[52:53],v8 offset:16384\nC v90,v92,v93\nM v[100:103],a[212:215],a[132:135],v[100:103]\nbuffer_load_dwordx4 v22,s[40:43],s45 offen offset:3072 sc1 lds\nM v[100:103],a[216:219],a[136:139],v[100:103]\ns_add_u32 m0,s22,16384\nv_exp_f32 v95,v95\n" \
"M v[100:103],a[220:223],a[140:143],v[100:103]\ns_add_u32 s45,s45,s49\nds_read_b64_tr_b16 a[54:55],v9 offset:16384\nds_read_b64_tr_b16 a[56:57],v6 offset:18432\nM v[108:111],a[208:211],a[144:147],v[36:39]\nds_read_b64_tr_b16 a[58:59],v7 offset:18432\nv_xor_b32 v14,0x10000,v14\nM v[108:111],a[212:215],a[148:151],v[108:111]\n" \
"ds_read_b128 a[192:195],v14 offset:0\nC v91,v94,v95\nM v[108:111],a[216:219],a[152:155],v[108:111]\nds_read_b128 a[196:199],v14 offset:1024\nv_xor_b32 v6,0x10000,v6\nM v[108:111],a[220:223],a[156:159],v[108:111]\nds_read_b128 a[200:203],v14 offset:2048\nv_xor_b32 v7,0x10000,v7\n" \
"M v[16:19],a[236:239],v[88:91],v[16:19]\nds_read_b64_tr_b16 a[60:61],v8 offset:18432\nds_read_b64_tr_b16 a[62:63],v9 offset:18432\nM v[116:119],a[208:211],a[160:163],v[40:43]\nds_read_b128 a[204:207],v14 offset:3072\nv_xor_b32 v8,0x10000,v8\nM v[116:119],a[212:215],a[164:167],v[116:119]\nv_max3_f32 v15,v16,v17,v18\n" \
"v_max_f32 v15,v15,v19\nM v[116:119],a[216:219],a[168:171],v[116:119]\nv_cmp_nge_f32 vcc,0x53800000,v15\nv_xor_b32 v9,0x10000,v9\nM v[116:119],a[220:223],a[172:175],v[116:119]\nM v[124:127],a[208:211],a[176:179],v[44:47]\ns_waitcnt lgkmcnt(3)\nds_read_b128 a[208:211],v14 offset:4096\n" \
"M v[124:127],a[212:215],a[180:183],v[124:127]\ns_cbranch_vccnz Lcs51_%=\nLcr52_%=:\nds_read_b128 a[212:215],v14 offset:5120\nM v[124:127],a[216:219],a[184:187],v[124:127]\ns_cmp_eq_u32 s52,s55\nds_read_b128 a[216:219],v14 offset:6144\nM v[124:127],a[220:223],a[188:191],v[124:127]\n" \
"s_cbranch_scc1 LH1_%=\nLH1R12_%=:\nds_read_b128 a[220:223],v14 offset:7168\nv_xor_b32 v13,0x10000,v13\nM v[128:131],a[192:195],v[64:67],v[128:131]\nv_exp_f32 v96,v96\nM v[160:163],a[192:195],v[72:75],v[160:163]\nv_exp_f32 v97,v97\n" \
"M v[192:195],a[192:195],v[80:83],v[192:195]\nv_exp_f32 v98,v98\nM v[224:227],a[192:195],v[88:91],v[224:227]\nv_exp_f32 v99,v99\nM v[228:231],a[196:199],v[88:91],v[228:231]\nv_exp_f32 v100,v100\nM v[196:199],a[196:199],v[80:83],v[196:199]\nv_exp_f32 v101,v101\n" \
"M v[164:167],a[196:199],v[72:75],v[164:167]\ns_waitcnt vmcnt(8) lgkmcnt(0)\nv_exp_f32 v102,v102\nM v[132:135],a[196:199],v[64:67],v[132:135]\ns_barrier\nbuffer_load_dwordx4 v22,s[40:43],s48 offen offset:0 sc1 lds\nM v[136:139],a[200:203],v[64:67],v[136:139]\nv_exp_f32 v103,v103\n" \
"M v[168:171],a[200:203],v[72:75],v[168:171]\nv_exp_f32 v104,v104\nM v[200:203],a[200:203],v[80:83],v[200:203]\nv_exp_f32 v105,v105\nM v[232:235],a[200:203],v[88:91],v[232:235]\nbuffer_load_dwordx4 v22,s[40:43],s48 offen offset:1024 sc1 lds\nM v[236:239],a[204:207],v[88:91],v[236:239]\nds_read_b128 a[192:195],v13 offset:16384\n" \
"C v96,v96,v97\nM v[204:207],a[204:207],v[80:83],v[204:207]\nv_exp_f32 v106,v106\nM v[172:175],a[204:207],v[72:75],v[172:175]\nv_exp_f32 v107,v107\nM v[140:143],a[204:207],v[64:67],v[140:143]\nbuffer_load_dwordx4 v22,s[40:43],s48 offen offset:2048 sc1 lds\nv_exp_f32 v108,v108\n" \
"M v[144:147],a[208:211],v[64:67],v[144:147]\nv_exp_f32 v109,v109\nM v[176:179],a[208:211],v[72:75],v[176:179]\nds_read_b128 a[196:199],v13 offset:18432\nds_read_b128 a[200:203],v13 offset:20480\nM v[208:211],a[208:211],v[80:83],v[208:211]\nv_exp_f32 v110,v110\nM v[240:243],a[208:211],v[88:91],v[240:243]\n" \
"buffer_load_dwordx4 v22,s[40:43],s48 offen offset:3072 sc1 lds\nv_exp_f32 v111,v111\nM v[244:247],a[212:215],v[88:91],v[244:247]\ns_add_u32 m0,s22,49152\nv_exp_f32 v112,v112\nM v[212:215],a[212:215],v[80:83],v[212:215]\ns_add_u32 s48,s48,s49\nds_read_b128 a[204:207],v13 offset:22528\n" \
"C v97,v98,v99\nM v[180:183],a[212:215],v[72:75],v[180:183]\nv_exp_f32 v113,v113\nM v[148:151],a[212:215],v[64:67],v[148:151]\nbuffer_load_dwordx4 v1,s[84:87],s46 offen offset:0 lds\nM v[152:155],a[216:219],v[64:67],v[152:155]\ns_add_u32 m0,s22,50048\nv_exp_f32 v114,v114\n" \
"M v[184:187],a[216:219],v[72:75],v[184:187]\nv_exp_f32 v115,v115\nM v[216:219],a[216:219],v[80:83],v[216:219]\nv_exp_f32 v116,v116\nM v[248:251],a[216:219],v[88:91],v[248:251]\nbuffer_load_dwordx4 v1,s[84:87],s46 offen offset:128 lds\nM v[252:255],a[220:223],v[88:91],v[252:255]\ns_add_u32 m0,s22,51200\n" \
"v_exp_f32 v117,v117\nM v[220:223],a[220:223],v[80:83],v[220:223]\ns_mov_b32 s46,851968\nv_exp_f32 v118,v118\nM v[188:191],a[220:223],v[72:75],v[188:191]\ns_waitcnt lgkmcnt(0)\nC v98,v100,v101\nC v99,v102,v103\n" \
"M v[156:159],a[220:223],v[64:67],v[156:159]\nbuffer_load_dwordx4 v1,s[84:87],s46 offen offset:0 lds\nM v[64:67],a[192:195],a[128:131],v[32:35]\ns_add_u32 m0,s22,52096\nds_read_b128 a[208:211],v13 offset:17408\nC v104,v104,v105\nM v[64:67],a[196:199],a[132:135],v[64:67]\nds_read_b128 a[212:215],v13 offset:19456\n" \
"C v105,v106,v107\nM v[24:27],a[224:227],v[96:99],v[16:19]\nds_read_b128 a[216:219],v13 offset:21504\nC v106,v108,v109\nM v[64:67],a[200:203],a[136:139],v[64:67]\nv_exp_f32 v119,v119\nM v[64:67],a[204:207],a[140:143],v[64:67]\nbuffer_load_dwordx4 v1,s[84:87],s46 offen offset:128 lds\n" \
"M v[72:75],a[192:195],a[144:147],v[36:39]\ns_add_u32 m0,s22,65536\nds_read_b128 a[220:223],v13 offset:23552\nC v107,v110,v111\nv_exp_f32 v120,v120\nM v[72:75],a[196:199],a[148:151],v[72:75]\ns_mov_b32 s46,917504\nv_exp_f32 v121,v121\n" \
"M v[24:27],a[228:231],v[104:107],v[24:27]\nv_exp_f32 v122,v122\nM v[72:75],a[200:203],a[152:155],v[72:75]\nbuffer_load_dwordx4 v22,s[40:43],s45 offen offset:0 sc1 lds\nM v[72:75],a[204:207],a[156:159],v[72:75]\nv_exp_f32 v123,v123\nM v[80:83],a[192:195],a[160:163],v[40:43]\nv_exp_f32 v124,v124\n" \
"M v[80:83],a[196:199],a[164:167],v[80:83]\nC v112,v112,v113\nC v113,v114,v115\nM v[80:83],a[200:203],a[168:171],v[80:83]\nbuffer_load_dwordx4 v22,s[40:43],s45 offen offset:1024 sc1 lds\nM v[80:83],a[204:207],a[172:175],v[80:83]\nv_exp_f32 v125,v125\nM v[88:91],a[192:195],a[176:179],v[44:47]\n" \
"ds_read_b128 a[192:195],v14 offset:49152\nC v114,v116,v117\nM v[88:91],a[196:199],a[180:183],v[88:91]\nC v115,v118,v119\nC v120,v120,v121\nM v[88:91],a[200:203],a[184:187],v[88:91]\nbuffer_load_dwordx4 v22,s[40:43],s45 offen offset:2048 sc1 lds\nM v[88:91],a[204:207],a[188:191],v[88:91]\n" \
"ds_read_b128 a[196:199],v14 offset:50176\nC v121,v122,v123\nM v[24:27],a[232:235],v[112:115],v[24:27]\ns_waitcnt lgkmcnt(2)\nds_read_b128 a[200:203],v14 offset:51200\nC v122,v124,v125\nM v[68:71],a[208:211],a[128:131],v[32:35]\nv_exp_f32 v126,v126\n" \
"M v[68:71],a[212:215],a[132:135],v[68:71]\nbuffer_load_dwordx4 v22,s[40:43],s45 offen offset:3072 sc1 lds\nM v[68:71],a[216:219],a[136:139],v[68:71]\ns_add_u32 m0,s22,98304\nv_exp_f32 v127,v127\nM v[68:71],a[220:223],a[140:143],v[68:71]\ns_add_u32 s45,s45,s49\nds_read_b64_tr_b16 a[64:65],v6 offset:32768\n" \
"ds_read_b64_tr_b16 a[66:67],v7 offset:32768\nM v[76:79],a[208:211],a[144:147],v[36:39]\nds_read_b64_tr_b16 a[68:69],v8 offset:32768\nC v123,v126,v127\nM v[76:79],a[212:215],a[148:151],v[76:79]\nds_read_b64_tr_b16 a[70:71],v9 offset:32768\nds_read_b64_tr_b16 a[72:73],v6 offset:34816\nM v[76:79],a[216:219],a[152:155],v[76:79]\n" \
"ds_read_b64_tr_b16 a[74:75],v7 offset:34816\nv_xor_b32 v13,0x10000,v13\nM v[76:79],a[220:223],a[156:159],v[76:79]\nds_read_b64_tr_b16 a[76:77],v8 offset:34816\nv_xor_b32 v6,0x10000,v6\nM v[24:27],a[236:239],v[120:123],v[24:27]\nds_read_b64_tr_b16 a[78:79],v9 offset:34816\nds_read_b128 a[204:207],v14 offset:52224\n" \
"M v[84:87],a[208:211],a[160:163],v[40:43]\nv_xor_b32 v7,0x10000,v7\nv_xor_b32 v8,0x10000,v8\nM v[84:87],a[212:215],a[164:167],v[84:87]\nv_max3_f32 v15,v24,v25,v26\nv_max_f32 v15,v15,v27\nM v[84:87],a[216:219],a[168:171],v[84:87]\nv_cmp_nge_f32 vcc,0x53800000,v15\n" \
"v_xor_b32 v9,0x10000,v9\nM v[84:87],a[220:223],a[172:175],v[84:87]\nM v[92:95],a[208:211],a[176:179],v[44:47]\nds_read_b128 a[208:211],v14 offset:53248\nM v[92:95],a[212:215],a[180:183],v[92:95]\ns_cbranch_vccnz Lcs53_%=\nLcr54_%=:\nds_read_b128 a[212:215],v14 offset:54272\n" \
"M v[92:95],a[216:219],a[184:187],v[92:95]\nds_read_b128 a[216:219],v14 offset:55296\nM v[92:95],a[220:223],a[188:191],v[92:95]\ns_waitcnt lgkmcnt(3)\nds_read_b128 a[220:223],v14 offset:56320\nM v[128:131],a[192:195],v[96:99],v[128:131]\nv_exp_f32 v64,v64\nM v[160:163],a[192:195],v[104:107],v[160:163]\n" \
"v_exp_f32 v65,v65\nM v[192:195],a[192:195],v[112:115],v[192:195]\nv_exp_f32 v66,v66\nM v[224:227],a[192:195],v[120:123],v[224:227]\nv_exp_f32 v67,v67\nM v[228:231],a[196:199],v[120:123],v[228:231]\nv_exp_f32 v68,v68\nM v[196:199],a[196:199],v[112:115],v[196:199]\n" \
"v_exp_f32 v69,v69\nM v[164:167],a[196:199],v[104:107],v[164:167]\ns_waitcnt vmcnt(8) lgkmcnt(0)\nv_exp_f32 v70,v70\nM v[132:135],a[196:199],v[96:99],v[132:135]\ns_barrier\nbuffer_load_dwordx4 v22,s[40:43],s48 offen offset:0 sc1 lds\nM v[136:139],a[200:203],v[96:99],v[136:139]\n" \
"v_exp_f32 v71,v71\nM v[168:171],a[200:203],v[104:107],v[168:171]\nv_exp_f32 v72,v72\nM v[200:203],a[200:203],v[112:115],v[200:203]\nv_exp_f32 v73,v73\nM v[232:235],a[200:203],v[120:123],v[232:235]\nbuffer_load_dwordx4 v22,s[40:43],s48 offen offset:1024 sc1 lds\nv_exp_f32 v74,v74\n" \
"M v[236:239],a[204:207],v[120:123],v[236:239]\nds_read_b128 a[192:195],v13 offset:32768\nC v64,v64,v65\nM v[204:207],a[204:207],v[112:115],v[204:207]\nv_exp_f32 v75,v75\nM v[172:175],a[204:207],v[104:107],v[172:175]\nv_exp_f32 v76,v76\nM v[140:143],a[204:207],v[96:99],v[140:143]\n" \
"buffer_load_dwordx4 v22,s[40:43],s48 offen offset:2048 sc1 lds\nv_exp_f32 v77,v77\nM v[144:147],a[208:211],v[96:99],v[144:147]\nv_exp_f32 v78,v78\nM v[176:179],a[208:211],v[104:107],v[176:179]\nds_read_b128 a[196:199],v13 offset:34816\nds_read_b128 a[200:203],v13 offset:36864\nM v[208:211],a[208:211],v[112:115],v[208:211]\n" \
"v_exp_f32 v79,v79\nM v[240:243],a[208:211],v[120:123],v[240:243]\nbuffer_load_dwordx4 v22,s[40:43],s48 offen offset:3072 sc1 lds\nv_exp_f32 v80,v80\nM v[244:247],a[212:215],v[120:123],v[244:247]\ns_add_u32 m0,s22,81920\nv_exp_f32 v81,v81\nM v[212:215],a[212:215],v[112:115],v[212:215]\n" \
"s_add_u32 s48,s48,s49\nds_read_b128 a[204:207],v13 offset:38912\nC v65,v66,v67\nM v[180:183],a[212:215],v[104:107],v[180:183]\ns_add_u32 s52,s52,1\nv_exp_f32 v82,v82\nM v[148:151],a[212:215],v[96:99],v[148:151]\nbuffer_load_dwordx4 v1,s[84:87],s46 offen offset:0 lds\n" \
"M v[152:155],a[216:219],v[96:99],v[152:155]\ns_add_u32 m0,s22,82816\nv_exp_f32 v83,v83\nM v[184:187],a[216:219],v[104:107],v[184:187]\nv_exp_f32 v84,v84\nM v[216:219],a[216:219],v[112:115],v[216:219]\nv_exp_f32 v85,v85\nM v[248:251],a[216:219],v[120:123],v[248:251]\n" \
"buffer_load_dwordx4 v1,s[84:87],s46 offen offset:128 lds\nM v[252:255],a[220:223],v[120:123],v[252:255]\ns_add_u32 m0,s22,83968\nv_exp_f32 v86,v86\nM v[220:223],a[220:223],v[112:115],v[220:223]\ns_mov_b32 s46,983040\nv_exp_f32 v87,v87\nM v[188:191],a[220:223],v[104:107],v[188:191]\n" \
"s_cmp_eq_u32 s52,s56\nds_read_b128 a[208:211],v13 offset:33792\nC v66,v68,v69\nM v[156:159],a[220:223],v[96:99],v[156:159]\ns_waitcnt lgkmcnt(1)\nbuffer_load_dwordx4 v1,s[84:87],s46 offen offset:0 lds\nM v[96:99],a[192:195],a[128:131],v[32:35]\ns_cbranch_scc1 LEV_%=\n" \
"LEVR12_%=:\nC v67,v70,v71\nC v72,v72,v73\nM v[96:99],a[196:199],a[132:135],v[96:99]\ns_add_u32 m0,s22,84864\nds_read_b128 a[212:215],v13 offset:35840\nds_read_b128 a[216:219],v13 offset:37888\nM v[16:19],a[224:227],v[64:67],v[24:27]\n" \
"v_exp_f32 v88,v88\nM v[96:99],a[200:203],a[136:139],v[96:99]\nbuffer_load_dwordx4 v1,s[84:87],s46 offen offset:128 lds\nM v[96:99],a[204:207],a[140:143],v[96:99]\ns_add_u32 m0,s22,114688\nds_read_b128 a[220:223],v13 offset:39936\nC v73,v74,v75\nM v[104:107],a[192:195],a[144:147],v[36:39]\n" \
"s_mov_b32 s46,1048576\nC v74,v76,v77\nC v75,v78,v79\nv_exp_f32 v89,v89\nM v[104:107],a[196:199],a[148:151],v[104:107]\nv_exp_f32 v90,v90\nM v[16:19],a[228:231],v[72:75],v[16:19]\nbuffer_load_dwordx4 v22,s[40:43],s45 offen offset:0 sc1 lds\n" \
"M v[104:107],a[200:203],a[152:155],v[104:107]\nv_exp_f32 v91,v91\nM v[104:107],a[204:207],a[156:159],v[104:107]\nv_exp_f32 v92,v92\nM v[112:115],a[192:195],a[160:163],v[40:43]\nC v80,v80,v81\nC v81,v82,v83\nM v[112:115],a[196:199],a[164:167],v[112:115]\n" \
"buffer_load_dwordx4 v22,s[40:43],s45 offen offset:1024 sc1 lds\nM v[112:115],a[200:203],a[168:171],v[112:115]\nv_exp_f32 v93,v93\nM v[112:115],a[204:207],a[172:175],v[112:115]\nC v82,v84,v85\nC v83,v86,v87\nM v[120:123],a[192:195],a[176:179],v[44:47]\nv_exp_f32 v94,v94\n" \
"M v[120:123],a[196:199],a[180:183],v[120:123]\nbuffer_load_dwordx4 v22,s[40:43],s45 offen offset:2048 sc1 lds\nM v[120:123],a[200:203],a[184:187],v[120:123]\nC v88,v88,v89\nC v89,v90,v91\nM v[120:123],a[204:207],a[188:191],v[120:123]\nds_read_b64_tr_b16 a[80:81],v6 offset:0\nv_xor_b32 v14,0x10000,v14\n" \
"M v[16:19],a[232:235],v[80:83],v[16:19]\ns_waitcnt lgkmcnt(1)\nds_read_b128 a[192:195],v14 offset:16384\nds_read_b128 a[196:199],v14 offset:17408\nM v[100:103],a[208:211],a[128:131],v[32:35]\nbuffer_load_dwordx4 v22,s[40:43],s45 offen offset:3072 sc1 lds\nM v[100:103],a[212:215],a[132:135],v[100:103]\ns_add_u32 m0,s22,0\n" \
"v_exp_f32 v95,v95\nM v[100:103],a[216:219],a[136:139],v[100:103]\ns_add_u32 s45,s45,s49\nds_read_b128 a[200:203],v14 offset:18432\nC v90,v92,v93\nM v[100:103],a[220:223],a[140:143],v[100:103]\nds_read_b64_tr_b16 a[82:83],v7 offset:0\nC v91,v94,v95\n" \
"M v[108:111],a[208:211],a[144:147],v[36:39]\nds_read_b64_tr_b16 a[84:85],v8 offset:0\nds_read_b64_tr_b16 a[86:87],v9 offset:0\nM v[108:111],a[212:215],a[148:151],v[108:111]\nds_read_b64_tr_b16 a[88:89],v6 offset:2048\nv_xor_b32 v13,0x10000,v13\nM v[108:111],a[216:219],a[152:155],v[108:111]\nds_read_b64_tr_b16 a[90:91],v7 offset:2048\n" \
"M v[108:111],a[220:223],a[156:159],v[108:111]\nds_read_b64_tr_b16 a[92:93],v8 offset:2048\nds_read_b64_tr_b16 a[94:95],v9 offset:2048\nM v[16:19],a[236:239],v[88:91],v[16:19]\nds_read_b128 a[204:207],v14 offset:19456\nM v[116:119],a[208:211],a[160:163],v[40:43]\nM v[116:119],a[212:215],a[164:167],v[116:119]\nv_max3_f32 v15,v16,v17,v18\n" \
"v_max_f32 v15,v15,v19\nM v[116:119],a[216:219],a[168:171],v[116:119]\nv_cmp_nge_f32 vcc,0x53800000,v15\nM v[116:119],a[220:223],a[172:175],v[116:119]\nM v[124:127],a[208:211],a[176:179],v[44:47]\nds_read_b128 a[208:211],v14 offset:20480\nM v[124:127],a[212:215],a[180:183],v[124:127]\ns_cbranch_vccnz Lcs55_%=\n" \
"Lcr56_%=:\nds_read_b128 a[212:215],v14 offset:21504\nM v[124:127],a[216:219],a[184:187],v[124:127]\nds_read_b128 a[216:219],v14 offset:22528\nM v[124:127],a[220:223],a[188:191],v[124:127]\ns_waitcnt lgkmcnt(3)\nds_read_b128 a[220:223],v14 offset:23552\nv_xor_b32 v14,0x10000,v14\n" \
"M v[128:131],a[192:195],v[64:67],v[128:131]\nv_exp_f32 v96,v96\nM v[160:163],a[192:195],v[72:75],v[160:163]\nv_exp_f32 v97,v97\nM v[192:195],a[192:195],v[80:83],v[192:195]\nv_exp_f32 v98,v98\nM v[224:227],a[192:195],v[88:91],v[224:227]\nv_exp_f32 v99,v99\n" \
"M v[228:231],a[196:199],v[88:91],v[228:231]\nv_exp_f32 v100,v100\nM v[196:199],a[196:199],v[80:83],v[196:199]\nv_exp_f32 v101,v101\nM v[164:167],a[196:199],v[72:75],v[164:167]\ns_waitcnt vmcnt(8) lgkmcnt(0)\nv_exp_f32 v102,v102\nM v[132:135],a[196:199],v[64:67],v[132:135]\n" \
"s_barrier\nbuffer_load_dwordx4 v22,s[40:43],s48 offen offset:0 sc1 lds\nM v[136:139],a[200:203],v[64:67],v[136:139]\nv_exp_f32 v103,v103\nM v[168:171],a[200:203],v[72:75],v[168:171]\nv_exp_f32 v104,v104\nM v[200:203],a[200:203],v[80:83],v[200:203]\nv_exp_f32 v105,v105\n" \
"M v[232:235],a[200:203],v[88:91],v[232:235]\nbuffer_load_dwordx4 v22,s[40:43],s48 offen offset:1024 sc1 lds\nM v[236:239],a[204:207],v[88:91],v[236:239]\nds_read_b128 a[192:195],v13 offset:0\nC v96,v96,v97\nM v[204:207],a[204:207],v[80:83],v[204:207]\nv_exp_f32 v106,v106\nM v[172:175],a[204:207],v[72:75],v[172:175]\n" \
"v_exp_f32 v107,v107\nM v[140:143],a[204:207],v[64:67],v[140:143]\nbuffer_load_dwordx4 v22,s[40:43],s48 offen offset:2048 sc1 lds\nv_exp_f32 v108,v108\nM v[144:147],a[208:211],v[64:67],v[144:147]\nv_exp_f32 v109,v109\nM v[176:179],a[208:211],v[72:75],v[176:179]\nds_read_b128 a[196:199],v13 offset:2048\n" \
"ds_read_b128 a[200:203],v13 offset:4096\nM v[208:211],a[208:211],v[80:83],v[208:211]\nv_exp_f32 v110,v110\nM v[240:243],a[208:211],v[88:91],v[240:243]\nbuffer_load_dwordx4 v22,s[40:43],s48 offen offset:3072 sc1 lds\nv_exp_f32 v111,v111\nM v[244:247],a[212:215],v[88:91],v[244:247]\ns_add_u32 m0,s22,32768\n" \
"v_exp_f32 v112,v112\nM v[212:215],a[212:215],v[80:83],v[212:215]\ns_add_u32 s48,s48,s49\nds_read_b128 a[204:207],v13 offset:6144\nC v97,v98,v99\nM v[180:183],a[212:215],v[72:75],v[180:183]\nv_exp_f32 v113,v113\nM v[148:151],a[212:215],v[64:67],v[148:151]\n" \
"buffer_load_dwordx4 v22,s[40:43],s45 offen offset:0 sc1 lds\nM v[152:155],a[216:219],v[64:67],v[152:155]\nv_exp_f32 v114,v114\nM v[184:187],a[216:219],v[72:75],v[184:187]\nv_exp_f32 v115,v115\nM v[216:219],a[216:219],v[80:83],v[216:219]\nv_exp_f32 v116,v116\nM v[248:251],a[216:219],v[88:91],v[248:251]\n" \
"buffer_load_dwordx4 v22,s[40:43],s45 offen offset:1024 sc1 lds\nM v[252:255],a[220:223],v[88:91],v[252:255]\nv_exp_f32 v117,v117\nM v[220:223],a[220:223],v[80:83],v[220:223]\nC v98,v100,v101\nC v99,v102,v103\nM v[188:191],a[220:223],v[72:75],v[188:191]\ns_waitcnt lgkmcnt(0)\n" \
"v_exp_f32 v118,v118\nM v[156:159],a[220:223],v[64:67],v[156:159]\nbuffer_load_dwordx4 v22,s[40:43],s45 offen offset:2048 sc1 lds\nM v[64:67],a[192:195],a[128:131],v[32:35]\nds_read_b128 a[208:211],v13 offset:1024\nC v104,v104,v105\nM v[64:67],a[196:199],a[132:135],v[64:67]\nds_read_b128 a[212:215],v13 offset:3072\n" \
"C v105,v106,v107\nM v[24:27],a[224:227],v[96:99],v[16:19]\nLG19_%=:\ns_cbranch_execz LHR2_%=\nds_read_b128 a[216:219],v13 offset:5120\nC v106,v108,v109\nM v[64:67],a[200:203],a[136:139],v[64:67]\nbuffer_load_dwordx2 v[10:11],v5,s[36:39],0 offen sc1\n" \
"M v[64:67],a[204:207],a[140:143],v[64:67]\nds_read_b128 a[220:223],v13 offset:7168\nC v107,v110,v111\nM v[72:75],a[192:195],a[144:147],v[36:39]\nbuffer_load_dwordx4 v22,s[40:43],s45 offen offset:3072 sc1 lds\nM v[72:75],a[196:199],a[148:151],v[72:75]\ns_add_u32 m0,s22,16384\nv_exp_f32 v119,v119\n" \
"M v[24:27],a[228:231],v[104:107],v[24:27]\ns_add_u32 s45,s45,s49\nv_exp_f32 v120,v120\nM v[72:75],a[200:203],a[152:155],v[72:75]\nv_exp_f32 v121,v121\nM v[72:75],a[204:207],a[156:159],v[72:75]\nbuffer_load_dwordx4 v22,s[40:43],s48 offen offset:0 sc1 lds\nM v[80:83],a[192:195],a[160:163],v[40:43]\n" \
"v_exp_f32 v122,v122\nM v[80:83],a[196:199],a[164:167],v[80:83]\nv_exp_f32 v123,v123\nM v[80:83],a[200:203],a[168:171],v[80:83]\nC v112,v112,v113\nC v113,v114,v115\nM v[80:83],a[204:207],a[172:175],v[80:83]\nbuffer_load_dwordx4 v22,s[40:43],s48 offen offset:1024 sc1 lds\n" \
"M v[88:91],a[192:195],a[176:179],v[44:47]\nds_read_b128 a[192:195],v14 offset:32768\nC v114,v116,v117\nM v[88:91],a[196:199],a[180:183],v[88:91]\nds_read_b128 a[196:199],v14 offset:33792\nC v115,v118,v119\nM v[88:91],a[200:203],a[184:187],v[88:91]\nv_exp_f32 v124,v124\n" \
"M v[88:91],a[204:207],a[188:191],v[88:91]\nbuffer_load_dwordx4 v22,s[40:43],s48 offen offset:2048 sc1 lds\nM v[24:27],a[232:235],v[112:115],v[24:27]\ns_waitcnt lgkmcnt(2)\nds_read_b64_tr_b16 a[96:97],v6 offset:49152\nC v120,v120,v121\nM v[68:71],a[208:211],a[128:131],v[32:35]\nv_exp_f32 v125,v125\n" \
"M v[68:71],a[212:215],a[132:135],v[68:71]\nds_read_b128 a[200:203],v14 offset:34816\nds_read_b64_tr_b16 a[98:99],v7 offset:49152\nM v[68:71],a[216:219],a[136:139],v[68:71]\nbuffer_load_dwordx4 v22,s[40:43],s48 offen offset:3072 sc1 lds\nM v[68:71],a[220:223],a[140:143],v[68:71]\ns_add_u32 m0,s22,49152\nv_exp_f32 v126,v126\n" \
"M v[76:79],a[208:211],a[144:147],v[36:39]\ns_add_u32 s48,s48,s49\nds_read_b64_tr_b16 a[100:101],v8 offset:49152\nds_read_b64_tr_b16 a[102:103],v9 offset:49152\nM v[76:79],a[212:215],a[148:151],v[76:79]\nv_exp_f32 v127,v127\nM v[76:79],a[216:219],a[152:155],v[76:79]\nC v121,v122,v123\n" \
"C v122,v124,v125\nC v123,v126,v127\nM v[76:79],a[220:223],a[156:159],v[76:79]\nds_read_b64_tr_b16 a[104:105],v6 offset:51200\nds_read_b64_tr_b16 a[106:107],v7 offset:51200\nM v[24:27],a[236:239],v[120:123],v[24:27]\nds_read_b64_tr_b16 a[108:109],v8 offset:51200\nv_xor_b32 v6,0x10000,v6\n" \
"M v[84:87],a[208:211],a[160:163],v[40:43]\nds_read_b64_tr_b16 a[110:111],v9 offset:51200\nds_read_b128 a[204:207],v14 offset:35840\nM v[84:87],a[212:215],a[164:167],v[84:87]\nv_max3_f32 v15,v24,v25,v26\nv_max_f32 v15,v15,v27\nM v[84:87],a[216:219],a[168:171],v[84:87]\nv_cmp_nge_f32 vcc,0x53800000,v15\n" \
"v_xor_b32 v7,0x10000,v7\nM v[84:87],a[220:223],a[172:175],v[84:87]\nv_xor_b32 v8,0x10000,v8\nv_xor_b32 v9,0x10000,v9\nM v[92:95],a[208:211],a[176:179],v[44:47]\nds_read_b128 a[208:211],v14 offset:36864\nM v[92:95],a[212:215],a[180:183],v[92:95]\ns_cbranch_vccnz Lcs57_%=\n" \
"Lcr58_%=:\nds_read_b128 a[212:215],v14 offset:37888\nM v[92:95],a[216:219],a[184:187],v[92:95]\nds_read_b128 a[216:219],v14 offset:38912\nM v[92:95],a[220:223],a[188:191],v[92:95]\ns_waitcnt lgkmcnt(3)\nds_read_b128 a[220:223],v14 offset:39936\nM v[128:131],a[192:195],v[96:99],v[128:131]\n" \
"v_exp_f32 v64,v64\nM v[160:163],a[192:195],v[104:107],v[160:163]\nv_exp_f32 v65,v65\nM v[192:195],a[192:195],v[112:115],v[192:195]\nv_exp_f32 v66,v66\nM v[224:227],a[192:195],v[120:123],v[224:227]\nv_exp_f32 v67,v67\nM v[228:231],a[196:199],v[120:123],v[228:231]\n" \
"v_exp_f32 v68,v68\nM v[196:199],a[196:199],v[112:115],v[196:199]\nv_exp_f32 v69,v69\nM v[164:167],a[196:199],v[104:107],v[164:167]\ns_waitcnt vmcnt(9) lgkmcnt(0)\nv_exp_f32 v70,v70\nM v[132:135],a[196:199],v[96:99],v[132:135]\ns_barrier\n" \
"ds_read_b128 a[192:195],v13 offset:49152\nM v[136:139],a[200:203],v[96:99],v[136:139]\nv_exp_f32 v71,v71\nM v[168:171],a[200:203],v[104:107],v[168:171]\nv_exp_f32 v72,v72\nM v[200:203],a[200:203],v[112:115],v[200:203]\nv_exp_f32 v73,v73\nM v[232:235],a[200:203],v[120:123],v[232:235]\n" \
"buffer_load_dwordx4 v22,s[40:43],s45 offen offset:0 sc1 lds\nM v[236:239],a[204:207],v[120:123],v[236:239]\nv_exp_f32 v74,v74\nM v[204:207],a[204:207],v[112:115],v[204:207]\nv_exp_f32 v75,v75\nM v[172:175],a[204:207],v[104:107],v[172:175]\nv_exp_f32 v76,v76\nM v[140:143],a[204:207],v[96:99],v[140:143]\n" \
"buffer_load_dwordx4 v22,s[40:43],s45 offen offset:1024 sc1 lds\nM v[144:147],a[208:211],v[96:99],v[144:147]\nv_exp_f32 v77,v77\nM v[176:179],a[208:211],v[104:107],v[176:179]\ns_waitcnt vmcnt(7)\nv_cmp_gt_u64 s[98:99],s[14:15],v[10:11]\nv_cmp_le_u64 s[18:19],s[16:17],v[10:11]\nv_exp_f32 v78,v78\n" \
"M v[208:211],a[208:211],v[112:115],v[208:211]\ns_or_b64 s[98:99],s[98:99],s[18:19]\ns_and_b32 s98,s98,0xffff\ns_cbranch_scc1 LPE_%=\nLPER_%=:\nv_exp_f32 v79,v79\nM v[240:243],a[208:211],v[120:123],v[240:243]\nbuffer_load_dwordx4 v22,s[40:43],s45 offen offset:2048 sc1 lds\n" \
"M v[244:247],a[212:215],v[120:123],v[244:247]\nds_read_b128 a[196:199],v13 offset:51200\nC v64,v64,v65\nM v[212:215],a[212:215],v[112:115],v[212:215]\nv_exp_f32 v80,v80\nM v[180:183],a[212:215],v[104:107],v[180:183]\ns_add_u32 s52,s52,1\nv_exp_f32 v81,v81\n" \
"M v[148:151],a[212:215],v[96:99],v[148:151]\nbuffer_load_dwordx4 v22,s[40:43],s45 offen offset:3072 sc1 lds\nM v[152:155],a[216:219],v[96:99],v[152:155]\ns_add_u32 m0,s22,65536\nds_read_b128 a[200:203],v13 offset:53248\nC v65,v66,v67\nv_exp_f32 v82,v82\nM v[184:187],a[216:219],v[104:107],v[184:187]\n" \
"ds_read_b128 a[204:207],v13 offset:55296\nC v66,v68,v69\nM v[216:219],a[216:219],v[112:115],v[216:219]\nv_exp_f32 v83,v83\nM v[248:251],a[216:219],v[120:123],v[248:251]\nbuffer_load_dwordx4 v22,s[40:43],s48 offen offset:0 sc1 lds\nM v[252:255],a[220:223],v[120:123],v[252:255]\nv_exp_f32 v84,v84\n" \
"M v[220:223],a[220:223],v[112:115],v[220:223]\ns_waitcnt lgkmcnt(2)\nv_exp_f32 v85,v85\nM v[188:191],a[220:223],v[104:107],v[188:191]\ns_cmp_eq_u32 s52,s56\nC v72,v72,v73\nC v67,v70,v71\nM v[156:159],a[220:223],v[96:99],v[156:159]\n" \
"buffer_load_dwordx4 v22,s[40:43],s48 offen offset:1024 sc1 lds\nM v[96:99],a[192:195],a[128:131],v[32:35]\ns_cbranch_scc1 LEV_%=\nLEVR13_%=:\nds_read_b128 a[208:211],v13 offset:50176\nC v73,v74,v75\nM v[96:99],a[196:199],a[132:135],v[96:99]\nds_read_b128 a[212:215],v13 offset:52224\n" \
"C v74,v76,v77\nM v[16:19],a[224:227],v[64:67],v[24:27]\ns_waitcnt lgkmcnt(2)\nds_read_b128 a[216:219],v13 offset:54272\nC v75,v78,v79\nM v[96:99],a[200:203],a[136:139],v[96:99]\nbuffer_load_dwordx4 v22,s[40:43],s48 offen offset:2048 sc1 lds\nv_exp_f32 v86,v86\n" \
"M v[96:99],a[204:207],a[140:143],v[96:99]\nds_read_b128 a[220:223],v13 offset:56320\nC v80,v80,v81\nM v[104:107],a[192:195],a[144:147],v[36:39]\nv_exp_f32 v87,v87\nM v[104:107],a[196:199],a[148:151],v[104:107]\nv_exp_f32 v88,v88\nM v[16:19],a[228:231],v[72:75],v[16:19]\n" \
"buffer_load_dwordx4 v22,s[40:43],s48 offen offset:3072 sc1 lds\nM v[104:107],a[200:203],a[152:155],v[104:107]\ns_add_u32 m0,s22,98304\nv_exp_f32 v89,v89\nM v[104:107],a[204:207],a[156:159],v[104:107]\ns_add_u32 s48,s48,s49\nv_exp_f32 v90,v90\nM v[112:115],a[192:195],a[160:163],v[40:43]\n" \
"C v81,v82,v83\nC v82,v84,v85\nM v[112:115],a[196:199],a[164:167],v[112:115]\nbuffer_load_dwordx4 v1,s[84:87],s46 offen offset:0 lds\nM v[112:115],a[200:203],a[168:171],v[112:115]\ns_add_u32 m0,s22,99200\nv_exp_f32 v91,v91\nM v[112:115],a[204:207],a[172:175],v[112:115]\n" \
"C v83,v86,v87\nC v88,v88,v89\nM v[120:123],a[192:195],a[176:179],v[44:47]\nv_exp_f32 v92,v92\nM v[120:123],a[196:199],a[180:183],v[120:123]\nbuffer_load_dwordx4 v1,s[84:87],s46 offen offset:128 lds\nM v[120:123],a[200:203],a[184:187],v[120:123]\ns_add_u32 m0,s22,100352\n" \
"v_exp_f32 v93,v93\nM v[120:123],a[204:207],a[188:191],v[120:123]\ns_mov_b32 s46,1114112\nv_exp_f32 v94,v94\nM v[16:19],a[232:235],v[80:83],v[16:19]\ns_waitcnt lgkmcnt(0)\nC v89,v90,v91\nC v90,v92,v93\n" \
"M v[100:103],a[208:211],a[128:131],v[32:35]\nbuffer_load_dwordx4 v1,s[84:87],s46 offen offset:0 lds\nM v[100:103],a[212:215],a[132:135],v[100:103]\ns_add_u32 m0,s22,101248\nds_read_b64_tr_b16 a[112:113],v6 offset:16384\nv_xor_b32 v14,0x10000,v14\nM v[100:103],a[216:219],a[136:139],v[100:103]\nds_read_b128 a[192:195],v14 offset:0\n" \
"M v[100:103],a[220:223],a[140:143],v[100:103]\nds_read_b128 a[196:199],v14 offset:1024\nM v[108:111],a[208:211],a[144:147],v[36:39]\nbuffer_load_dwordx4 v1,s[84:87],s46 offen offset:128 lds\nM v[108:111],a[212:215],a[148:151],v[108:111]\ns_add_u32 m0,s22,81920\nv_exp_f32 v95,v95\nM v[108:111],a[216:219],a[152:155],v[108:111]\n" \
"s_mov_b32 s46,1179648\nds_read_b128 a[200:203],v14 offset:2048\nC v91,v94,v95\nM v[108:111],a[220:223],a[156:159],v[108:111]\nds_read_b64_tr_b16 a[114:115],v7 offset:16384\nds_read_b64_tr_b16 a[116:117],v8 offset:16384\nM v[16:19],a[236:239],v[88:91],v[16:19]\nds_read_b64_tr_b16 a[118:119],v9 offset:16384\n" \
"M v[116:119],a[208:211],a[160:163],v[40:43]\nds_read_b64_tr_b16 a[120:121],v6 offset:18432\nds_read_b64_tr_b16 a[122:123],v7 offset:18432\nM v[116:119],a[212:215],a[164:167],v[116:119]\nds_read_b64_tr_b16 a[124:125],v8 offset:18432\nv_max3_f32 v15,v16,v17,v18\nM v[116:119],a[216:219],a[168:171],v[116:119]\nds_read_b64_tr_b16 a[126:127],v9 offset:18432\n" \
"v_max_f32 v15,v15,v19\nM v[116:119],a[220:223],a[172:175],v[116:119]\nds_read_b128 a[204:207],v14 offset:3072\nv_cmp_nge_f32 vcc,0x53800000,v15\nM v[124:127],a[208:211],a[176:179],v[44:47]\ns_waitcnt lgkmcnt(6)\nds_read_b128 a[208:211],v14 offset:4096\nM v[124:127],a[212:215],a[180:183],v[124:127]\n" \
"s_cbranch_vccnz Lcs59_%=\nLcr60_%=:\nds_read_b128 a[212:215],v14 offset:5120\nM v[124:127],a[216:219],a[184:187],v[124:127]\ns_cmp_eq_u32 s52,s55\nds_read_b128 a[216:219],v14 offset:6144\nM v[124:127],a[220:223],a[188:191],v[124:127]\ns_cbranch_scc1 LH1_%=\n" \
"LH1R14_%=:\nds_read_b128 a[220:223],v14 offset:7168\nv_xor_b32 v13,0x10000,v13\nM v[128:131],a[192:195],v[64:67],v[128:131]\nv_exp_f32 v96,v96\nM v[160:163],a[192:195],v[72:75],v[160:163]\nv_exp_f32 v97,v97\nM v[192:195],a[192:195],v[80:83],v[192:195]\n" \
"v_exp_f32 v98,v98\nM v[224:227],a[192:195],v[88:91],v[224:227]\nv_exp_f32 v99,v99\nM v[228:231],a[196:199],v[88:91],v[228:231]\nv_exp_f32 v100,v100\nM v[196:199],a[196:199],v[80:83],v[196:199]\nv_exp_f32 v101,v101\nM v[164:167],a[196:199],v[72:75],v[164:167]\n" \
"s_waitcnt vmcnt(12) lgkmcnt(0)\nv_exp_f32 v102,v102\nM v[132:135],a[196:199],v[64:67],v[132:135]\ns_barrier\nds_read_b128 a[192:195],v13 offset:32768\nM v[136:139],a[200:203],v[64:67],v[136:139]\nbuffer_load_dwordx4 v1,s[84:87],s46 offen offset:0 lds\nM v[168:171],a[200:203],v[72:75],v[168:171]\n" \
"s_add_u32 m0,s22,82816\nv_exp_f32 v103,v103\nM v[200:203],a[200:203],v[80:83],v[200:203]\nv_exp_f32 v104,v104\nM v[232:235],a[200:203],v[88:91],v[232:235]\nv_exp_f32 v105,v105\nM v[236:239],a[204:207],v[88:91],v[236:239]\nbuffer_load_dwordx4 v1,s[84:87],s46 offen offset:128 lds\n" \
"M v[204:207],a[204:207],v[80:83],v[204:207]\ns_mov_b32 s46,1245184\nds_read_b128 a[196:199],v13 offset:34816\nC v96,v96,v97\nM v[172:175],a[204:207],v[72:75],v[172:175]\ns_add_u32 m0,s22,83968\nv_exp_f32 v106,v106\nM v[140:143],a[204:207],v[64:67],v[140:143]\n" \
"ds_read_b128 a[200:203],v13 offset:36864\nds_read_b128 a[204:207],v13 offset:38912\nM v[144:147],a[208:211],v[64:67],v[144:147]\nbuffer_load_dwordx4 v1,s[84:87],s46 offen offset:0 lds\nM v[176:179],a[208:211],v[72:75],v[176:179]\ns_add_u32 m0,s22,84864\nv_exp_f32 v107,v107\nM v[208:211],a[208:211],v[80:83],v[208:211]\n" \
"v_exp_f32 v108,v108\nM v[240:243],a[208:211],v[88:91],v[240:243]\nv_exp_f32 v109,v109\nM v[244:247],a[212:215],v[88:91],v[244:247]\nbuffer_load_dwordx4 v1,s[84:87],s46 offen offset:128 lds\nM v[212:215],a[212:215],v[80:83],v[212:215]\ns_add_u32 m0,s22,114688\nv_exp_f32 v110,v110\n" \
"M v[180:183],a[212:215],v[72:75],v[180:183]\ns_mov_b32 s46,1310720\nv_exp_f32 v111,v111\nM v[148:151],a[212:215],v[64:67],v[148:151]\nv_exp_f32 v112,v112\nM v[152:155],a[216:219],v[64:67],v[152:155]\nbuffer_load_dwordx4 v22,s[40:43],s48 offen offset:0 sc1 lds\nM v[184:187],a[216:219],v[72:75],v[184:187]\n" \
"C v97,v98,v99\nC v98,v100,v101\nM v[216:219],a[216:219],v[80:83],v[216:219]\nv_exp_f32 v113,v113\nM v[248:251],a[216:219],v[88:91],v[248:251]\nC v99,v102,v103\nC v104,v104,v105\nM v[252:255],a[220:223],v[88:91],v[252:255]\n" \
"buffer_load_dwordx4 v22,s[40:43],s48 offen offset:1024 sc1 lds\nM v[220:223],a[220:223],v[80:83],v[220:223]\nv_exp_f32 v114,v114\nM v[188:191],a[220:223],v[72:75],v[188:191]\nC v105,v106,v107\nC v106,v108,v109\nM v[156:159],a[220:223],v[64:67],v[156:159]\ns_waitcnt lgkmcnt(0)\n" \
"v_exp_f32 v115,v115\nM v[64:67],a[192:195],a[128:131],v[32:35]\nbuffer_load_dwordx4 v22,s[40:43],s48 offen offset:2048 sc1 lds\nM v[64:67],a[196:199],a[132:135],v[64:67]\nds_read_b128 a[208:211],v13 offset:33792\nC v107,v110,v111\nM v[24:27],a[224:227],v[96:99],v[16:19]\nds_read_b128 a[212:215],v13 offset:35840\n" \
"C v112,v112,v113\nM v[64:67],a[200:203],a[136:139],v[64:67]\nds_read_b128 a[216:219],v13 offset:37888\nC v113,v114,v115\nM v[64:67],a[204:207],a[140:143],v[64:67]\nbuffer_load_dwordx4 v22,s[40:43],s48 offen offset:3072 sc1 lds\nM v[72:75],a[192:195],a[144:147],v[36:39]\ns_add_u32 m0,s22,0\n" \
"ds_read_b128 a[220:223],v13 offset:39936\nM v[72:75],a[196:199],a[148:151],v[72:75]\nv_exp_f32 v116,v116\nM v[24:27],a[228:231],v[104:107],v[24:27]\nv_exp_f32 v117,v117\nM v[72:75],a[200:203],a[152:155],v[72:75]\nbuffer_load_dwordx4 v2,s[36:39],s47 offen offset:0 sc1 lds\nM v[72:75],a[204:207],a[156:159],v[72:75]\n" \
"v_exp_f32 v118,v118\nM v[80:83],a[192:195],a[160:163],v[40:43]\nv_exp_f32 v119,v119\nM v[80:83],a[196:199],a[164:167],v[80:83]\nv_exp_f32 v120,v120\nM v[80:83],a[200:203],a[168:171],v[80:83]\nbuffer_load_dwordx4 v2,s[36:39],s47 offen offset:1024 sc1 lds\nM v[80:83],a[204:207],a[172:175],v[80:83]\n" \
"v_exp_f32 v121,v121\nM v[88:91],a[192:195],a[176:179],v[44:47]\nC v114,v116,v117\nC v115,v118,v119\nM v[88:91],a[196:199],a[180:183],v[88:91]\nv_exp_f32 v122,v122\nM v[88:91],a[200:203],a[184:187],v[88:91]\nbuffer_load_dwordx4 v2,s[36:39],s47 offen offset:2048 sc1 lds\n" \
"M v[88:91],a[204:207],a[188:191],v[88:91]\nds_read_b128 a[192:195],v14 offset:16384\nC v120,v120,v121\nM v[24:27],a[232:235],v[112:115],v[24:27]\ns_waitcnt lgkmcnt(1)\nds_read_b128 a[196:199],v14 offset:17408\nM v[68:71],a[208:211],a[128:131],v[32:35]\nv_exp_f32 v123,v123\n" \
"M v[68:71],a[212:215],a[132:135],v[68:71]\nbuffer_load_dwordx4 v2,s[36:39],s47 offen offset:3072 sc1 lds\nM v[68:71],a[216:219],a[136:139],v[68:71]\ns_add_u32 m0,s22,32768\nv_exp_f32 v124,v124\nM v[68:71],a[220:223],a[140:143],v[68:71]\ns_add_u32 s47,s28,8192\nv_exp_f32 v125,v125\n" \
"M v[76:79],a[208:211],a[144:147],v[36:39]\nv_exp_f32 v126,v126\nM v[76:79],a[212:215],a[148:151],v[76:79]\nv_exp_f32 v127,v127\nM v[76:79],a[216:219],a[152:155],v[76:79]\nC v121,v122,v123\nC v122,v124,v125\nC v123,v126,v127\n" \
"M v[76:79],a[220:223],a[156:159],v[76:79]\nds_read_b128 a[200:203],v14 offset:18432\nds_read_b128 a[204:207],v14 offset:19456\nM v[24:27],a[236:239],v[120:123],v[24:27]\nM v[84:87],a[208:211],a[160:163],v[40:43]\nM v[84:87],a[212:215],a[164:167],v[84:87]\nv_max3_f32 v15,v24,v25,v26\nv_max_f32 v15,v15,v27\n" \
"M v[84:87],a[216:219],a[168:171],v[84:87]\nv_cmp_nge_f32 vcc,0x53800000,v15\nM v[84:87],a[220:223],a[172:175],v[84:87]\nM v[92:95],a[208:211],a[176:179],v[44:47]\nds_read_b128 a[208:211],v14 offset:20480\nM v[92:95],a[212:215],a[180:183],v[92:95]\ns_cbranch_vccnz Lcs61_%=\nLcr62_%=:\n" \
"ds_read_b128 a[212:215],v14 offset:21504\nM v[92:95],a[216:219],a[184:187],v[92:95]\nds_read_b128 a[216:219],v14 offset:22528\nM v[92:95],a[220:223],a[188:191],v[92:95]\ns_waitcnt lgkmcnt(3)\nds_read_b128 a[220:223],v14 offset:23552\nM v[128:131],a[192:195],v[96:99],v[128:131]\nv_exp_f32 v64,v64\n" \
"M v[160:163],a[192:195],v[104:107],v[160:163]\nv_exp_f32 v65,v65\nM v[192:195],a[192:195],v[112:115],v[192:195]\nv_exp_f32 v66,v66\nM v[224:227],a[192:195],v[120:123],v[224:227]\nv_exp_f32 v67,v67\nM v[228:231],a[196:199],v[120:123],v[228:231]\nv_exp_f32 v68,v68\n" \
"M v[196:199],a[196:199],v[112:115],v[196:199]\nv_exp_f32 v69,v69\nM v[164:167],a[196:199],v[104:107],v[164:167]\ns_waitcnt vmcnt(16) lgkmcnt(0)\nv_exp_f32 v70,v70\nM v[132:135],a[196:199],v[96:99],v[132:135]\ns_barrier\nds_read_b128 a[192:195],v13 offset:49152\n" \
"M v[136:139],a[200:203],v[96:99],v[136:139]\nbuffer_load_dwordx4 v1,s[84:87],s46 offen offset:0 lds\nM v[168:171],a[200:203],v[104:107],v[168:171]\ns_add_u32 m0,s22,33664\nv_exp_f32 v71,v71\nM v[200:203],a[200:203],v[112:115],v[200:203]\nv_exp_f32 v72,v72\nM v[232:235],a[200:203],v[120:123],v[232:235]\n" \
"v_exp_f32 v73,v73\nM v[236:239],a[204:207],v[120:123],v[236:239]\nbuffer_load_dwordx4 v1,s[84:87],s46 offen offset:128 lds\nM v[204:207],a[204:207],v[112:115],v[204:207]\ns_add_u32 m0,s22,34816\nds_read_b128 a[196:199],v13 offset:51200\nC v64,v64,v65\nM v[172:175],a[204:207],v[104:107],v[172:175]\n" \
"s_mov_b32 s46,1376256\nv_exp_f32 v74,v74\nM v[140:143],a[204:207],v[96:99],v[140:143]\nds_read_b128 a[200:203],v13 offset:53248\nds_read_b128 a[204:207],v13 offset:55296\nM v[144:147],a[208:211],v[96:99],v[144:147]\nbuffer_load_dwordx4 v1,s[84:87],s46 offen offset:0 lds\nM v[176:179],a[208:211],v[104:107],v[176:179]\n" \
"s_add_u32 m0,s22,35712\nv_exp_f32 v75,v75\nM v[208:211],a[208:211],v[112:115],v[208:211]\nv_exp_f32 v76,v76\nM v[240:243],a[208:211],v[120:123],v[240:243]\nv_exp_f32 v77,v77\nM v[244:247],a[212:215],v[120:123],v[244:247]\nbuffer_load_dwordx4 v1,s[84:87],s46 offen offset:128 lds\n" \
"M v[212:215],a[212:215],v[112:115],v[212:215]\ns_add_u32 m0,s22,16384\nv_exp_f32 v78,v78\nM v[180:183],a[212:215],v[104:107],v[180:183]\ns_add_u32 s52,s52,1\nv_exp_f32 v79,v79\nM v[148:151],a[212:215],v[96:99],v[148:151]\ns_mov_b32 s46,1441792\n" \
"v_exp_f32 v80,v80\nM v[152:155],a[216:219],v[96:99],v[152:155]\nbuffer_load_dwordx4 v1,s[84:87],s46 offen offset:0 lds\nM v[184:187],a[216:219],v[104:107],v[184:187]\ns_add_u32 m0,s22,17280\nv_exp_f32 v81,v81\nM v[216:219],a[216:219],v[112:115],v[216:219]\nv_exp_f32 v82,v82\n" \
"M v[248:251],a[216:219],v[120:123],v[248:251]\ns_waitcnt lgkmcnt(0)\nC v65,v66,v67\nC v66,v68,v69\nM v[252:255],a[220:223],v[120:123],v[252:255]\nbuffer_load_dwordx4 v1,s[84:87],s46 offen offset:128 lds\nM v[220:223],a[220:223],v[112:115],v[220:223]\ns_add_u32 m0,s22,18432\n" \
"v_exp_f32 v83,v83\nM v[188:191],a[220:223],v[104:107],v[188:191]\ns_cmp_eq_u32 s52,s56\nC v67,v70,v71\nC v72,v72,v73\nM v[156:159],a[220:223],v[96:99],v[156:159]\ns_cbranch_scc1 LEV_%=\nLEVR14_%=:\n" \
"v_exp_f32 v84,v84\nM v[96:99],a[192:195],a[128:131],v[32:35]\nbuffer_load_dwordx2 v[10:11],v5,s[36:39],0 offen sc1\nM v[96:99],a[196:199],a[132:135],v[96:99]\ns_mov_b32 s46,1507328\nds_read_b128 a[208:211],v13 offset:50176\nds_read_b128 a[212:215],v13 offset:52224\nM v[16:19],a[224:227],v[64:67],v[24:27]\n" \
"buffer_load_dwordx4 v1,s[84:87],s46 offen offset:0 lds\nM v[96:99],a[200:203],a[136:139],v[96:99]\ns_add_u32 m0,s22,19328\nC v73,v74,v75\nC v74,v76,v77\nM v[96:99],a[204:207],a[140:143],v[96:99]\nds_read_b128 a[216:219],v13 offset:54272\nC v75,v78,v79\n" \
"M v[104:107],a[192:195],a[144:147],v[36:39]\nds_read_b128 a[220:223],v13 offset:56320\nC v80,v80,v81\nM v[104:107],a[196:199],a[148:151],v[104:107]\nv_exp_f32 v85,v85\nM v[16:19],a[228:231],v[72:75],v[16:19]\nbuffer_load_dwordx4 v1,s[84:87],s46 offen offset:128 lds\nM v[104:107],a[200:203],a[152:155],v[104:107]\n" \
"s_add_u32 m0,s22,49152\nv_exp_f32 v86,v86\nM v[104:107],a[204:207],a[156:159],v[104:107]\ns_mov_b32 s46,1966080\nv_exp_f32 v87,v87\nM v[112:115],a[192:195],a[160:163],v[40:43]\nv_exp_f32 v88,v88\nM v[112:115],a[196:199],a[164:167],v[112:115]\n" \
"C v81,v82,v83\nC v82,v84,v85\nM v[112:115],a[200:203],a[168:171],v[112:115]\nv_exp_f32 v89,v89\nM v[112:115],a[204:207],a[172:175],v[112:115]\nC v83,v86,v87\nC v88,v88,v89\nM v[120:123],a[192:195],a[176:179],v[44:47]\n" \
"v_exp_f32 v90,v90\nM v[120:123],a[196:199],a[180:183],v[120:123]\nv_exp_f32 v91,v91\nM v[120:123],a[200:203],a[184:187],v[120:123]\nv_exp_f32 v92,v92\nM v[120:123],a[204:207],a[188:191],v[120:123]\nv_exp_f32 v93,v93\nM v[16:19],a[232:235],v[80:83],v[16:19]\n" \
"s_waitcnt lgkmcnt(0)\nv_exp_f32 v94,v94\nM v[100:103],a[208:211],a[128:131],v[32:35]\nC v89,v90,v91\nC v90,v92,v93\nM v[100:103],a[212:215],a[132:135],v[100:103]\nv_exp_f32 v95,v95\nM v[100:103],a[216:219],a[136:139],v[100:103]\n" \
"v_xor_b32 v14,0x10000,v14\nC v91,v94,v95\nM v[100:103],a[220:223],a[140:143],v[100:103]\nds_read_b128 a[192:195],v14 offset:0\nds_read_b128 a[196:199],v14 offset:1024\nM v[108:111],a[208:211],a[144:147],v[36:39]\nds_read_b128 a[200:203],v14 offset:2048\nv_add_u32 v20,0x10000,v4\n" \
"M v[108:111],a[212:215],a[148:151],v[108:111]\nLG20_%=:\ns_cbranch_execz LG22_%=\nds_read_b128 a[204:207],v14 offset:3072\nv_add_u32 v23,0x20000,v4\nM v[108:111],a[216:219],a[152:155],v[108:111]\nM v[108:111],a[220:223],a[156:159],v[108:111]\nM v[16:19],a[236:239],v[88:91],v[16:19]\n" \
"M v[116:119],a[208:211],a[160:163],v[40:43]\nM v[116:119],a[212:215],a[164:167],v[116:119]\nv_max3_f32 v15,v16,v17,v18\nv_max_f32 v15,v15,v19\nM v[116:119],a[216:219],a[168:171],v[116:119]\nv_cmp_nge_f32 vcc,0x53800000,v15\nM v[116:119],a[220:223],a[172:175],v[116:119]\nM v[124:127],a[208:211],a[176:179],v[44:47]\n" \
"ds_read_b128 a[208:211],v14 offset:4096\nM v[124:127],a[212:215],a[180:183],v[124:127]\ns_cbranch_vccnz Lcs63_%=\nLcr64_%=:\nds_read_b128 a[212:215],v14 offset:5120\nv_max3_f32 v60,v96,v97,v98\nM v[124:127],a[216:219],a[184:187],v[124:127]\ns_waitcnt lgkmcnt(2)\n" \
"ds_read_b128 a[216:219],v14 offset:6144\nv_max3_f32 v61,v99,v104,v105\nM v[124:127],a[220:223],a[188:191],v[124:127]\nds_read_b128 a[220:223],v14 offset:7168\nv_max3_f32 v60,v60,v106,v107\nv_max3_f32 v61,v61,v112,v113\nv_max3_f32 v60,v60,v114,v115\nv_max3_f32 v61,v61,v120,v121\n" \
"v_max3_f32 v60,v60,v122,v123\nv_max3_f32 v61,v61,v100,v101\nv_max3_f32 v60,v60,v102,v103\nv_max3_f32 v61,v61,v108,v109\nv_max3_f32 v60,v60,v110,v111\nv_max3_f32 v61,v61,v116,v117\nv_max3_f32 v60,v60,v118,v119\nv_max3_f32 v61,v61,v124,v125\n" \
"v_max3_f32 v61,v61,v126,v127\nv_max_f32 v60,v60,v61\nv_cmp_nge_f32 vcc,0x41f00000,v60\ns_cbranch_vccnz LRS_%=\nLH1R15_%=:\nM v[128:131],a[192:195],v[64:67],v[128:131]\nv_exp_f32 v96,v96\nM v[160:163],a[192:195],v[72:75],v[160:163]\n" \
"v_exp_f32 v97,v97\nM v[192:195],a[192:195],v[80:83],v[192:195]\nv_exp_f32 v98,v98\nM v[224:227],a[192:195],v[88:91],v[224:227]\nv_exp_f32 v99,v99\nM v[228:231],a[196:199],v[88:91],v[228:231]\nv_exp_f32 v100,v100\nM v[196:199],a[196:199],v[80:83],v[196:199]\n" \
"v_exp_f32 v101,v101\nM v[164:167],a[196:199],v[72:75],v[164:167]\ns_waitcnt vmcnt(9) lgkmcnt(0)\nv_exp_f32 v102,v102\nM v[132:135],a[196:199],v[64:67],v[132:135]\ns_barrier\nbuffer_load_dwordx4 v1,s[84:87],s46 offen offset:0 lds\nM v[136:139],a[200:203],v[64:67],v[136:139]\n" \
"s_add_u32 m0,s22,50048\nds_read_b128 v[76:79],v4 offset:0\nC v96,v96,v97\nM v[168:171],a[200:203],v[72:75],v[168:171]\nv_exp_f32 v103,v103\nM v[200:203],a[200:203],v[80:83],v[200:203]\nv_exp_f32 v104,v104\nM v[232:235],a[200:203],v[88:91],v[232:235]\n" \
"buffer_load_dwordx4 v1,s[84:87],s46 offen offset:128 lds\nM v[236:239],a[204:207],v[88:91],v[236:239]\ns_mov_b32 s46,2031616\ns_add_u32 m0,s22,51200\nds_read_b128 v[84:87],v4 offset:1024\nC v97,v98,v99\nM v[204:207],a[204:207],v[80:83],v[204:207]\nds_read_b128 v[92:95],v4 offset:2048\n" \
"C v98,v100,v101\nM v[172:175],a[204:207],v[72:75],v[172:175]\ns_waitcnt vmcnt(4)\nv_cmp_gt_u64 s[98:99],s[14:15],v[10:11]\nv_cmp_le_u64 s[18:19],s[16:17],v[10:11]\nM v[140:143],a[204:207],v[64:67],v[140:143]\nbuffer_load_dwordx4 v1,s[84:87],s46 offen offset:0 lds\nv_exp_f32 v105,v105\n" \
"M v[144:147],a[208:211],v[64:67],v[144:147]\ns_add_u32 m0,s22,52096\nv_exp_f32 v106,v106\nM v[176:179],a[208:211],v[72:75],v[176:179]\ns_or_b64 s[98:99],s[98:99],s[18:19]\ns_and_b32 s98,s98,0xff0000\ns_cbranch_scc1 LPF_%=\nLPFR_%=:\n" \
"v_exp_f32 v107,v107\nM v[208:211],a[208:211],v[80:83],v[208:211]\nv_exp_f32 v108,v108\nM v[240:243],a[208:211],v[88:91],v[240:243]\nbuffer_load_dwordx4 v1,s[84:87],s46 offen offset:128 lds\nv_exp_f32 v109,v109\nM v[244:247],a[212:215],v[88:91],v[244:247]\ns_add_u32 m0,s22,65536\n" \
"v_exp_f32 v110,v110\nM v[212:215],a[212:215],v[80:83],v[212:215]\ns_mov_b32 s46,1572864\nv_exp_f32 v111,v111\nM v[180:183],a[212:215],v[72:75],v[180:183]\nv_exp_f32 v112,v112\nM v[148:151],a[212:215],v[64:67],v[148:151]\nbuffer_load_dwordx4 v2,s[36:39],s47 offen offset:0 sc1 lds\n" \
"v_exp_f32 v113,v113\nM v[152:155],a[216:219],v[64:67],v[152:155]\nds_read_b128 a[192:195],v14 offset:49152\nv_exp_f32 v114,v114\nM v[184:187],a[216:219],v[72:75],v[184:187]\nds_read_b128 a[196:199],v14 offset:50176\nv_exp_f32 v115,v115\nM v[216:219],a[216:219],v[80:83],v[216:219]\n" \
"ds_read_b128 a[200:203],v14 offset:51200\nC v104,v104,v105\nv_exp_f32 v116,v116\nM v[248:251],a[216:219],v[88:91],v[248:251]\nbuffer_load_dwordx4 v2,s[36:39],s47 offen offset:1024 sc1 lds\nv_exp_f32 v117,v117\nM v[252:255],a[220:223],v[88:91],v[252:255]\nds_read_b128 a[204:207],v14 offset:52224\n" \
"C v99,v102,v103\nv_exp_f32 v118,v118\nM v[220:223],a[220:223],v[80:83],v[220:223]\nds_read_b128 a[208:211],v14 offset:53248\nC v105,v106,v107\nv_exp_f32 v119,v119\nM v[188:191],a[220:223],v[72:75],v[188:191]\ns_waitcnt lgkmcnt(4)\n" \
"ds_read_b128 v[72:75],v4 offset:3072\nC v112,v112,v113\nv_exp_f32 v120,v120\nM v[156:159],a[220:223],v[64:67],v[156:159]\nbuffer_load_dwordx4 v2,s[36:39],s47 offen offset:2048 sc1 lds\nv_exp_f32 v121,v121\nM v[48:51],a[0:3],v[76:79],0\nv_exp_f32 v122,v122\n" \
"M v[48:51],a[8:11],v[84:87],v[48:51]\nds_read_b128 v[80:83],v4 offset:8192\nC v106,v108,v109\nv_exp_f32 v123,v123\nM v[48:51],a[16:19],v[92:95],v[48:51]\ns_waitcnt lgkmcnt(1)\nds_read_b128 v[88:91],v4 offset:9216\nC v107,v110,v111\n" \
"M v[48:51],a[24:27],v[72:75],v[48:51]\nbuffer_load_dwordx4 v2,s[36:39],s47 offen offset:3072 sc1 lds\nM v[52:55],a[28:31],v[72:75],0\ns_add_u32 m0,s22,98304\nds_read_b128 a[212:215],v14 offset:54272\nC v113,v114,v115\nM v[52:55],a[20:23],v[92:95],v[52:55]\ns_add_u32 s47,s29,0\n" \
"ds_read_b128 v[72:75],v4 offset:10240\nds_read_b128 v[92:95],v4 offset:11264\nM v[52:55],a[12:15],v[84:87],v[52:55]\nv_exp_f32 v124,v124\nM v[52:55],a[4:7],v[76:79],v[52:55]\ns_waitcnt lgkmcnt(4)\nds_read_b128 a[216:219],v14 offset:55296\nC v120,v120,v121\n" \
"M v[64:67],a[0:3],v[80:83],0\ns_waitcnt lgkmcnt(4)\nds_read_b64_tr_b16 a[128:129],v6 offset:32768\nC v121,v122,v123\nM v[64:67],a[8:11],v[88:91],v[64:67]\ns_waitcnt lgkmcnt(3)\nds_read_b64_tr_b16 a[130:131],v7 offset:32768\nds_read_b64_tr_b16 a[132:133],v8 offset:32768\n" \
"ds_read_b64_tr_b16 a[134:135],v9 offset:32768\nds_read_b128 a[220:223],v14 offset:56320\nds_read_b64_tr_b16 a[136:137],v6 offset:34816\nds_read_b64_tr_b16 a[138:139],v7 offset:34816\nds_read_b64_tr_b16 a[140:141],v8 offset:34816\nds_read_b64_tr_b16 a[142:143],v9 offset:34816\nds_read_b64_tr_b16 a[144:145],v6 offset:16384\nds_read_b64_tr_b16 a[146:147],v7 offset:16384\n" \
"ds_read_b64_tr_b16 a[148:149],v8 offset:16384\nds_read_b64_tr_b16 a[150:151],v9 offset:16384\nds_read_b64_tr_b16 a[152:153],v6 offset:18432\nds_read_b64_tr_b16 a[154:155],v7 offset:18432\nds_read_b64_tr_b16 a[156:157],v8 offset:18432\nds_read_b64_tr_b16 a[158:159],v9 offset:18432\nM v[64:67],a[16:19],v[72:75],v[64:67]\ns_waitcnt lgkmcnt(15)\n" \
"ds_read_b128 v[84:87],v4 offset:4096\nv_xor_b32 v6,0x10000,v6\nM v[64:67],a[24:27],v[92:95],v[64:67]\nds_read_b128 v[76:79],v4 offset:5120\nv_xor_b32 v7,0x10000,v7\nM v[68:71],a[28:31],v[92:95],0\nds_read_b128 v[92:95],v4 offset:6144\nv_xor_b32 v8,0x10000,v8\n" \
"M v[68:71],a[20:23],v[72:75],v[68:71]\nds_read_b128 v[72:75],v4 offset:7168\nv_xor_b32 v9,0x10000,v9\nv_exp_f32 v125,v125\nM v[68:71],a[12:15],v[88:91],v[68:71]\ns_waitcnt lgkmcnt(3)\nv_exp_f32 v126,v126\nM v[68:71],a[4:7],v[80:83],v[68:71]\n" \
"s_waitcnt vmcnt(8) lgkmcnt(4)\nds_read_b128 v[88:91],v4 offset:12288\nC v114,v116,v117\nM v[24:27],a[224:227],v[96:99],v[16:19]\ns_barrier\nbuffer_load_dwordx4 v2,s[36:39],s47 offen offset:0 sc1 lds\nM v[48:51],a[32:35],v[84:87],v[48:51]\ns_waitcnt lgkmcnt(3)\n" \
"ds_read_b128 v[80:83],v4 offset:13312\nC v115,v118,v119\nM v[48:51],a[40:43],v[76:79],v[48:51]\ns_waitcnt lgkmcnt(3)\nds_read_b64_tr_b16 a[160:161],v6 offset:32768\nC v122,v124,v125\nM v[48:51],a[48:51],v[92:95],v[48:51]\ns_waitcnt lgkmcnt(3)\n" \
"ds_read_b64_tr_b16 a[162:163],v7 offset:32768\nM v[48:51],a[56:59],v[72:75],v[48:51]\nbuffer_load_dwordx4 v2,s[36:39],s47 offen offset:1024 sc1 lds\nM v[52:55],a[60:63],v[72:75],v[52:55]\nds_read_b128 v[72:75],v4 offset:14336\nM v[52:55],a[52:55],v[92:95],v[52:55]\nds_read_b128 v[92:95],v4 offset:15360\nM v[52:55],a[44:47],v[76:79],v[52:55]\n" \
"s_waitcnt lgkmcnt(4)\nv_exp_f32 v127,v127\nM v[52:55],a[36:39],v[84:87],v[52:55]\nbuffer_load_dwordx4 v2,s[36:39],s47 offen offset:2048 sc1 lds\nM v[24:27],a[228:231],v[104:107],v[24:27]\ns_add_u32 s52,s52,1\nds_read_b64_tr_b16 a[164:165],v8 offset:32768\nC v123,v126,v127\n" \
"M v[64:67],a[32:35],v[88:91],v[64:67]\ns_waitcnt lgkmcnt(1)\nds_read_b64_tr_b16 a[166:167],v9 offset:32768\nds_read_b64_tr_b16 a[168:169],v6 offset:34816\nM v[64:67],a[40:43],v[80:83],v[64:67]\ns_waitcnt lgkmcnt(4)\nds_read_b64_tr_b16 a[170:171],v7 offset:34816\nM v[64:67],a[48:51],v[72:75],v[64:67]\n" \
"buffer_load_dwordx4 v2,s[36:39],s47 offen offset:3072 sc1 lds\nM v[64:67],a[56:59],v[92:95],v[64:67]\ns_add_u32 s47,s29,8192\nds_read_b64_tr_b16 a[172:173],v8 offset:34816\nM v[68:71],a[60:63],v[92:95],v[68:71]\ns_add_u32 m0,s22,81920\nds_read_b64_tr_b16 a[174:175],v9 offset:34816\nM v[68:71],a[52:55],v[72:75],v[68:71]\n" \
"ds_read_b64_tr_b16 a[176:177],v6 offset:16384\nM v[68:71],a[44:47],v[80:83],v[68:71]\ns_cmp_eq_u32 s52,s56\nds_read_b64_tr_b16 a[178:179],v7 offset:16384\nds_read_b64_tr_b16 a[180:181],v8 offset:16384\nM v[68:71],a[36:39],v[88:91],v[68:71]\nbuffer_load_dwordx4 v2,s[36:39],s47 offen offset:0 sc1 lds\nM v[24:27],a[232:235],v[112:115],v[24:27]\n" \
"s_cbranch_scc1 LEV_%=\nLEVR15_%=:\nds_read_b64_tr_b16 a[182:183],v9 offset:16384\nds_read_b64_tr_b16 a[184:185],v6 offset:18432\nds_read_b64_tr_b16 a[186:187],v7 offset:18432\nds_read_b64_tr_b16 a[188:189],v8 offset:18432\nds_read_b64_tr_b16 a[190:191],v9 offset:18432\nds_write2_b32 v57,v40,v44 offset1:1\n" \
"M v[24:27],a[236:239],v[120:123],v[24:27]\nM v[192:195],a[192:195],v[112:115],v[192:195]\nM v[196:199],a[196:199],v[112:115],v[196:199]\nds_write_b64 v57,v[26:27] offset:8\nM v[200:203],a[200:203],v[112:115],v[200:203]\nbuffer_load_dwordx4 v2,s[36:39],s47 offen offset:1024 sc1 lds\nM v[204:207],a[204:207],v[112:115],v[204:207]\nC v192,v192,v193\n" \
"C v193,v194,v195\nM v[208:211],a[208:211],v[112:115],v[208:211]\ns_waitcnt vmcnt(6) lgkmcnt(0)\nC v194,v196,v197\nC v195,v198,v199\nM v[212:215],a[212:215],v[112:115],v[212:215]\ns_barrier\nds_read_b128 v[28:31],v12 offset:0\n" \
"M v[216:219],a[216:219],v[112:115],v[216:219]\nds_write_b128 v12,v[192:195] offset:0\nM v[220:223],a[220:223],v[112:115],v[220:223]\nbuffer_load_dwordx4 v2,s[36:39],s47 offen offset:2048 sc1 lds\nM v[224:227],a[192:195],v[120:123],v[224:227]\nC v200,v200,v201\nC v201,v202,v203\nM v[228:231],a[196:199],v[120:123],v[228:231]\n" \
"ds_read_b64_tr_b16 a[240:241],v6 offset:49152\nC v202,v204,v205\nM v[232:235],a[200:203],v[120:123],v[232:235]\nds_read_b64_tr_b16 a[242:243],v7 offset:49152\nC v203,v206,v207\nM v[236:239],a[204:207],v[120:123],v[236:239]\nbuffer_load_dwordx4 v2,s[36:39],s47 offen offset:3072 sc1 lds\nM v[240:243],a[208:211],v[120:123],v[240:243]\n" \
"s_add_u32 m0,s22,114688\nC v208,v208,v209\nC v209,v210,v211\nM v[244:247],a[212:215],v[120:123],v[244:247]\ns_add_u32 s47,s30,0\nds_write_b128 v12,v[200:203] offset:1024\nM v[248:251],a[216:219],v[120:123],v[248:251]\nC v210,v212,v213\n" \
"C v211,v214,v215\nM v[252:255],a[220:223],v[120:123],v[252:255]\nbuffer_load_dwordx4 v2,s[36:39],s47 offen offset:0 sc1 lds\nM v[128:131],a[192:195],v[96:99],v[128:131]\nC v216,v216,v217\nC v217,v218,v219\nM v[132:135],a[196:199],v[96:99],v[132:135]\nC v218,v220,v221\n" \
"C v219,v222,v223\nM v[136:139],a[200:203],v[96:99],v[136:139]\nds_write_b128 v12,v[208:211] offset:2048\nM v[140:143],a[204:207],v[96:99],v[140:143]\nbuffer_load_dwordx4 v2,s[36:39],s47 offen offset:1024 sc1 lds\nM v[144:147],a[208:211],v[96:99],v[144:147]\nC v224,v224,v225\nC v232,v232,v233\n" \
"M v[148:151],a[212:215],v[96:99],v[148:151]\nC v225,v226,v227\nC v233,v234,v235\nM v[152:155],a[216:219],v[96:99],v[152:155]\nds_write_b128 v12,v[216:219] offset:3072\nM v[156:159],a[220:223],v[96:99],v[156:159]\nbuffer_load_dwordx4 v2,s[36:39],s47 offen offset:2048 sc1 lds\nM v[160:163],a[192:195],v[104:107],v[160:163]\n" \
"C v226,v228,v229\nC v227,v230,v231\nM v[164:167],a[196:199],v[104:107],v[164:167]\nds_read_b128 v[76:79],v20 offset:0\nC v234,v236,v237\nM v[168:171],a[200:203],v[104:107],v[168:171]\nds_read_b128 v[84:87],v20 offset:1024\nC v235,v238,v239\n" \
"M v[172:175],a[204:207],v[104:107],v[172:175]\nds_read_b128 v[92:95],v20 offset:2048\nC v240,v240,v241\nM v[176:179],a[208:211],v[104:107],v[176:179]\ns_waitcnt lgkmcnt(3)\nds_read_b128 v[72:75],v20 offset:3072\nv_min_f32 v15,v32,v28\nM v[180:183],a[212:215],v[104:107],v[180:183]\n" \
"buffer_load_dwordx4 v2,s[36:39],s47 offen offset:3072 sc1 lds\nM v[184:187],a[216:219],v[104:107],v[184:187]\ns_add_u32 m0,s22,0\nC v248,v248,v249\nC v241,v242,v243\nM v[188:191],a[220:223],v[104:107],v[188:191]\ns_waitcnt lgkmcnt(3)\ns_add_u32 s47,s30,8192\n" \
"C v249,v250,v251\nv_sub_f32 v16,v15,v32\nM v[48:51],a[64:67],v[76:79],v[48:51]\ns_waitcnt lgkmcnt(2)\nds_write_b128 v12,v[224:227] offset:4096\nM v[48:51],a[72:75],v[84:87],v[48:51]\ns_waitcnt lgkmcnt(2)\nds_read_b128 v[80:83],v20 offset:8192\n" \
"C v242,v244,v245\nM v[48:51],a[80:83],v[92:95],v[48:51]\ns_waitcnt lgkmcnt(2)\nds_read_b128 v[88:91],v20 offset:9216\nC v243,v246,v247\nM v[48:51],a[88:91],v[72:75],v[48:51]\nds_read_b128 v[60:63],v20 offset:10240\nC v250,v252,v253\n" \
"M v[52:55],a[92:95],v[72:75],v[52:55]\nds_read_b128 v[72:75],v20 offset:11264\nC v251,v254,v255\nM v[52:55],a[84:87],v[92:95],v[52:55]\nbuffer_load_dwordx4 v2,s[36:39],s47 offen offset:0 sc1 lds\nM v[52:55],a[76:79],v[84:87],v[52:55]\nv_exp_f32 v16,v16\nM v[52:55],a[68:71],v[76:79],v[52:55]\n" \
"s_waitcnt lgkmcnt(3)\nv_sub_f32 v18,v15,v28\nv_mul_f32 v15,v24,v16\nM v[64:67],a[64:67],v[80:83],v[64:67]\ns_waitcnt lgkmcnt(2)\nds_write_b128 v12,v[232:235] offset:5120\nM v[64:67],a[72:75],v[88:91],v[64:67]\ns_waitcnt lgkmcnt(2)\n" \
"ds_read_b128 v[92:95],v20 offset:4096\nv_mov_b32 v28,s10\nM v[64:67],a[80:83],v[60:63],v[64:67]\ns_waitcnt lgkmcnt(2)\nds_read_b128 v[84:87],v20 offset:5120\nv_exp_f32 v18,v18\nM v[64:67],a[88:91],v[72:75],v[64:67]\nds_write_b128 v12,v[240:243] offset:6144\n" \
"ds_write_b128 v12,v[248:251] offset:7168\nds_read_b64_tr_b16 a[244:245],v8 offset:49152\nds_read_b64_tr_b16 a[246:247],v9 offset:49152\nds_read_b64_tr_b16 a[248:249],v6 offset:51200\nds_read_b64_tr_b16 a[250:251],v7 offset:51200\nds_read_b64_tr_b16 a[252:253],v8 offset:51200\nds_read_b64_tr_b16 a[254:255],v9 offset:51200\nds_read_b128 v[76:79],v20 offset:6144\n" \
"M v[68:71],a[92:95],v[72:75],v[68:71]\nds_read_b128 v[72:75],v20 offset:7168\nv_fmac_f32 v15,v30,v18\nv_rcp_f32 v15,v15\nM v[68:71],a[84:87],v[60:63],v[68:71]\nbuffer_load_dwordx4 v2,s[36:39],s47 offen offset:1024 sc1 lds\nM v[68:71],a[76:79],v[88:91],v[68:71]\nv_mul_f32 v16,v16,v15\n" \
"v_mul_f32 v18,v18,v15\nM v[68:71],a[68:71],v[80:83],v[68:71]\ns_waitcnt lgkmcnt(11)\nds_read_b128 v[60:63],v20 offset:12288\nv_mul_f32 v128,v16,v128\nM v[48:51],a[96:99],v[92:95],v[48:51]\ns_waitcnt lgkmcnt(11)\nds_read_b128 v[88:91],v20 offset:13312\n" \
"v_mul_f32 v129,v16,v129\nM v[48:51],a[104:107],v[84:87],v[48:51]\ns_waitcnt vmcnt(10) lgkmcnt(4)\ns_waitcnt lgkmcnt(3)\nds_read_b128 v[80:83],v20 offset:14336\nv_mul_f32 v130,v16,v130\nM v[48:51],a[112:115],v[76:79],v[48:51]\ns_barrier\n" \
"s_waitcnt lgkmcnt(3)\nv_mul_f32 v131,v16,v131\nv_mul_f32 v132,v16,v132\nM v[48:51],a[120:123],v[72:75],v[48:51]\nbuffer_load_dwordx4 v2,s[36:39],s47 offen offset:2048 sc1 lds\nv_mul_f32 v133,v16,v133\nM v[52:55],a[124:127],v[72:75],v[52:55]\nds_read_b128 v[72:75],v20 offset:15360\n" \
"v_mul_f32 v134,v16,v134\nv_mul_f32 v135,v16,v135\nM v[52:55],a[116:119],v[76:79],v[52:55]\ns_waitcnt lgkmcnt(1)\nds_read_b128 v[96:99],v57 offset:0\nv_mul_f32 v136,v16,v136\nv_mul_f32 v137,v16,v137\nM v[52:55],a[108:111],v[84:87],v[52:55]\n" \
"s_waitcnt lgkmcnt(3)\nds_read_b128 v[100:103],v57 offset:1024\nv_mul_f32 v138,v16,v138\nv_mul_f32 v139,v16,v139\nM v[52:55],a[100:103],v[92:95],v[52:55]\nbuffer_load_dwordx4 v2,s[36:39],s47 offen offset:3072 sc1 lds\nv_mul_f32 v140,v16,v140\nM v[64:67],a[96:99],v[60:63],v[64:67]\n" \
"s_add_u32 m0,s22,32768\nds_read_b128 v[104:107],v57 offset:2048\nv_mul_f32 v141,v16,v141\nv_mul_f32 v142,v16,v142\nM v[64:67],a[104:107],v[88:91],v[64:67]\ns_add_u32 s47,s28,16384\nds_read_b128 v[76:79],v20 offset:32768\nv_mul_f32 v143,v16,v143\n" \
"M v[64:67],a[112:115],v[80:83],v[64:67]\ns_waitcnt lgkmcnt(4)\nds_read_b128 v[84:87],v20 offset:33792\nv_min_f32 v15,v36,v29\nM v[64:67],a[120:123],v[72:75],v[64:67]\nds_read_b128 v[92:95],v20 offset:34816\nv_mul_f32 v144,v16,v144\nM v[68:71],a[124:127],v[72:75],v[68:71]\n" \
"s_waitcnt lgkmcnt(4)\nds_read_b128 v[72:75],v20 offset:35840\nv_mul_f32 v145,v16,v145\nv_lshlrev_b32 v228,16,v96\nM v[68:71],a[116:119],v[80:83],v[68:71]\nbuffer_load_dwordx4 v2,s[36:39],s47 offen offset:0 sc1 lds\nv_and_b32 v96,0xffff0000,v96\nM v[68:71],a[108:111],v[88:91],v[68:71]\n" \
"v_fmac_f32 v128,v228,v18\nv_fmac_f32 v129,v96,v18\nv_lshlrev_b32 v229,16,v97\nM v[68:71],a[100:103],v[60:63],v[68:71]\ns_waitcnt lgkmcnt(3)\nds_read_b128 v[108:111],v57 offset:3072\nv_and_b32 v97,0xffff0000,v97\nv_fmac_f32 v130,v229,v18\n" \
"M v[32:35],a[0:3],v[76:79],0\ns_waitcnt lgkmcnt(3)\nv_fmac_f32 v131,v97,v18\nv_lshlrev_b32 v230,16,v98\nv_and_b32 v98,0xffff0000,v98\nM v[32:35],a[8:11],v[84:87],v[32:35]\ns_waitcnt lgkmcnt(2)\nds_read_b128 v[80:83],v20 offset:40960\n" \
"v_fmac_f32 v132,v230,v18\nv_fmac_f32 v133,v98,v18\nM v[32:35],a[16:19],v[92:95],v[32:35]\ns_waitcnt lgkmcnt(2)\nds_read_b128 v[88:91],v20 offset:41984\nv_lshlrev_b32 v231,16,v99\nv_and_b32 v99,0xffff0000,v99\nM v[32:35],a[24:27],v[72:75],v[32:35]\n" \
"ds_read_b128 v[60:63],v20 offset:43008\nv_sub_f32 v17,v15,v36\nv_fmac_f32 v134,v231,v18\nM v[36:39],a[28:31],v[72:75],0\nds_read_b128 v[72:75],v20 offset:44032\nv_fmac_f32 v135,v99,v18\nC v128,v128,v129\nM v[36:39],a[20:23],v[92:95],v[36:39]\n" \
"buffer_load_dwordx4 v2,s[36:39],s47 offen offset:1024 sc1 lds\nC v129,v130,v131\nM v[36:39],a[12:15],v[84:87],v[36:39]\nC v130,v132,v133\nC v131,v134,v135\nv_lshlrev_b32 v236,16,v100\nM v[36:39],a[4:7],v[76:79],v[36:39]\ns_waitcnt lgkmcnt(3)\n" \
"v_and_b32 v100,0xffff0000,v100\nv_fmac_f32 v136,v236,v18\nv_fmac_f32 v137,v100,v18\nM v[40:43],a[0:3],v[80:83],0\ns_waitcnt lgkmcnt(2)\nv_lshlrev_b32 v237,16,v101\nv_and_b32 v101,0xffff0000,v101\nv_fmac_f32 v138,v237,v18\n" \
"M v[40:43],a[8:11],v[88:91],v[40:43]\ns_waitcnt lgkmcnt(1)\nds_read_b128 v[92:95],v20 offset:36864\nv_fmac_f32 v139,v101,v18\nv_lshlrev_b32 v238,16,v102\nM v[40:43],a[16:19],v[60:63],v[40:43]\ns_waitcnt lgkmcnt(1)\nds_read_b128 v[84:87],v20 offset:37888\n" \
"v_and_b32 v102,0xffff0000,v102\nv_fmac_f32 v140,v238,v18\nM v[40:43],a[24:27],v[72:75],v[40:43]\nbuffer_store_dwordx4 v[128:131],v4,s[36:39],s62 offen offset:0 sc1\nds_read_b128 v[112:115],v57 offset:4096\nds_read_b128 v[116:119],v57 offset:5120\nds_read_b128 v[120:123],v57 offset:6144\nds_read_b128 v[124:127],v57 offset:7168\n" \
"ds_read_b128 v[76:79],v20 offset:38912\nM v[44:47],a[28:31],v[72:75],0\nds_read_b128 v[72:75],v20 offset:39936\nv_fmac_f32 v141,v102,v18\nv_lshlrev_b32 v239,16,v103\nM v[44:47],a[20:23],v[60:63],v[44:47]\nv_and_b32 v103,0xffff0000,v103\nv_fmac_f32 v142,v239,v18\n" \
"v_fmac_f32 v143,v103,v18\nM v[44:47],a[12:15],v[88:91],v[44:47]\ns_waitcnt lgkmcnt(7)\nC v136,v136,v137\nC v137,v138,v139\nv_lshlrev_b32 v244,16,v104\nM v[44:47],a[4:7],v[80:83],v[44:47]\nbuffer_load_dwordx4 v2,s[36:39],s47 offen offset:2048 sc1 lds\n" \
"v_and_b32 v104,0xffff0000,v104\nM v[32:35],a[32:35],v[92:95],v[32:35]\ns_waitcnt lgkmcnt(6)\nv_lshlrev_b32 v245,16,v105\nC v138,v140,v141\nC v139,v142,v143\nM v[32:35],a[40:43],v[84:87],v[32:35]\ns_waitcnt vmcnt(12) lgkmcnt(2)\n" \
"s_waitcnt lgkmcnt(1)\nds_read_b128 v[60:63],v20 offset:45056\nv_lshlrev_b32 v246,16,v106\nv_mul_f32 v146,v16,v146\nM v[32:35],a[48:51],v[76:79],v[32:35]\ns_barrier\ns_waitcnt lgkmcnt(1)\nds_read_b128 v[88:91],v20 offset:46080\n" \
"v_mul_f32 v147,v16,v147\nM v[32:35],a[56:59],v[72:75],v[32:35]\nds_read_b128 v[80:83],v20 offset:47104\nv_fmac_f32 v144,v244,v18\nv_and_b32 v105,0xffff0000,v105\nM v[36:39],a[60:63],v[72:75],v[36:39]\nds_read_b128 v[72:75],v20 offset:48128\nv_fmac_f32 v145,v104,v18\n" \
"v_lshlrev_b32 v247,16,v107\nM v[36:39],a[52:55],v[76:79],v[36:39]\nbuffer_load_dwordx4 v2,s[36:39],s47 offen offset:3072 sc1 lds\nv_mul_f32 v148,v16,v148\nM v[36:39],a[44:47],v[84:87],v[36:39]\ns_add_u32 m0,s22,16384\nv_mul_f32 v149,v16,v149\nv_mul_f32 v150,v16,v150\n" \
"v_and_b32 v106,0xffff0000,v106\nM v[36:39],a[36:39],v[92:95],v[36:39]\ns_waitcnt lgkmcnt(3)\ns_add_u32 s47,s29,16384\nv_fmac_f32 v146,v245,v18\nv_fmac_f32 v147,v105,v18\nC v144,v144,v145\nM v[40:43],a[32:35],v[60:63],v[40:43]\n" \
"s_waitcnt lgkmcnt(2)\nv_mul_f32 v151,v16,v151\nv_and_b32 v107,0xffff0000,v107\nv_fmac_f32 v148,v246,v18\nM v[40:43],a[40:43],v[88:91],v[40:43]\nLG21_%=:\ns_cbranch_execz LG23_%=\ns_waitcnt lgkmcnt(1)\n" \
"ds_read_b128 v[76:79],v20 offset:16384\nv_fmac_f32 v149,v106,v18\nv_fmac_f32 v150,v247,v18\nM v[40:43],a[48:51],v[80:83],v[40:43]\ns_waitcnt lgkmcnt(1)\nds_read_b128 v[84:87],v20 offset:17408\nC v145,v146,v147\nv_fmac_f32 v151,v107,v18\n" \
"M v[40:43],a[56:59],v[72:75],v[40:43]\nbuffer_store_dwordx4 v[136:139],v4,s[36:39],s62 offen offset:1024 sc1\nds_read_b128 v[92:95],v20 offset:18432\nM v[44:47],a[60:63],v[72:75],v[44:47]\nds_read_b128 v[72:75],v20 offset:19456\nv_sub_f32 v19,v15,v29\nC v146,v148,v149\nM v[44:47],a[52:55],v[80:83],v[44:47]\n" \
"v_exp_f32 v17,v17\nv_lshlrev_b32 v252,16,v108\nM v[44:47],a[44:47],v[88:91],v[44:47]\ns_waitcnt lgkmcnt(3)\nC v147,v150,v151\nv_lshlrev_b32 v253,16,v109\nv_mul_f32 v152,v16,v152\nM v[44:47],a[36:39],v[60:63],v[44:47]\n" \
"buffer_load_dwordx4 v2,s[36:39],s47 offen offset:0 sc1 lds\nv_mul_f32 v153,v16,v153\nM v[32:35],a[64:67],v[76:79],v[32:35]\ns_waitcnt lgkmcnt(2)\nv_exp_f32 v19,v19\nv_and_b32 v108,0xffff0000,v108\nM v[32:35],a[72:75],v[84:87],v[32:35]\ns_waitcnt lgkmcnt(1)\n" \
"ds_read_b128 v[80:83],v20 offset:24576\nv_mul_f32 v15,v25,v17\nv_mul_f32 v154,v16,v154\nM v[32:35],a[80:83],v[92:95],v[32:35]\ns_waitcnt lgkmcnt(1)\nds_read_b128 v[88:91],v20 offset:25600\nv_lshlrev_b32 v254,16,v110\nv_mul_f32 v155,v16,v155\n" \
"M v[32:35],a[88:91],v[72:75],v[32:35]\nds_read_b128 v[60:63],v20 offset:26624\nv_and_b32 v109,0xffff0000,v109\nv_fmac_f32 v152,v252,v18\nM v[36:39],a[92:95],v[72:75],v[36:39]\nds_read_b128 v[72:75],v20 offset:27648\nv_fmac_f32 v153,v108,v18\nv_lshlrev_b32 v255,16,v111\n" \
"M v[36:39],a[84:87],v[92:95],v[36:39]\nbuffer_load_dwordx4 v2,s[36:39],s47 offen offset:1024 sc1 lds\nv_fmac_f32 v15,v31,v19\nM v[36:39],a[76:79],v[84:87],v[36:39]\nv_mul_f32 v156,v16,v156\nv_mul_f32 v157,v16,v157\nv_and_b32 v110,0xffff0000,v110\nM v[36:39],a[68:71],v[76:79],v[36:39]\n" \
"s_waitcnt lgkmcnt(3)\nv_fmac_f32 v154,v253,v18\nv_fmac_f32 v155,v109,v18\nv_mul_f32 v158,v16,v158\nM v[40:43],a[64:67],v[80:83],v[40:43]\ns_waitcnt lgkmcnt(2)\nC v152,v152,v153\nv_mul_f32 v159,v16,v159\n" \
"v_and_b32 v111,0xffff0000,v111\nM v[40:43],a[72:75],v[88:91],v[40:43]\ns_waitcnt lgkmcnt(1)\nds_read_b128 v[92:95],v20 offset:20480\nv_fmac_f32 v156,v254,v18\nv_rcp_f32 v15,v15\nM v[40:43],a[80:83],v[60:63],v[40:43]\ns_waitcnt lgkmcnt(1)\n" \
"ds_read_b128 v[84:87],v20 offset:21504\nv_fmac_f32 v157,v110,v18\nC v153,v154,v155\nM v[40:43],a[88:91],v[72:75],v[40:43]\nbuffer_store_dwordx4 v[144:147],v4,s[36:39],s62 offen offset:2048 sc1\nds_read_b128 v[76:79],v20 offset:22528\nM v[44:47],a[92:95],v[72:75],v[44:47]\nds_read_b128 v[72:75],v20 offset:23552\n" \
"v_fmac_f32 v158,v255,v18\nv_fmac_f32 v159,v111,v18\nM v[44:47],a[84:87],v[60:63],v[44:47]\nv_mul_f32 v17,v17,v15\nC v154,v156,v157\nC v155,v158,v159\nM v[44:47],a[76:79],v[88:91],v[44:47]\ns_waitcnt lgkmcnt(3)\n" \
"v_lshlrev_b32 v228,16,v112\nv_mul_f32 v19,v19,v15\nv_lshlrev_b32 v229,16,v113\nM v[44:47],a[68:71],v[80:83],v[44:47]\nbuffer_load_dwordx2 v[10:11],v5,s[36:39],0 offen sc1\nv_mul_f32 v160,v17,v160\nM v[32:35],a[96:99],v[92:95],v[32:35]\ns_waitcnt lgkmcnt(2)\n" \
"v_mul_f32 v161,v17,v161\nv_and_b32 v112,0xffff0000,v112\nv_mul_f32 v162,v17,v162\nM v[32:35],a[104:107],v[84:87],v[32:35]\ns_waitcnt vmcnt(14) lgkmcnt(12)\ns_waitcnt lgkmcnt(1)\nds_read_b128 v[60:63],v20 offset:28672\nv_lshlrev_b32 v230,16,v114\n" \
"v_mul_f32 v163,v17,v163\nM v[32:35],a[112:115],v[76:79],v[32:35]\ns_barrier\ns_waitcnt lgkmcnt(1)\nds_read_b128 v[88:91],v20 offset:29696\nv_and_b32 v113,0xffff0000,v113\nM v[32:35],a[120:123],v[72:75],v[32:35]\nds_read_b128 v[80:83],v20 offset:30720\n" \
"v_fmac_f32 v160,v228,v19\nv_fmac_f32 v161,v112,v19\nM v[36:39],a[124:127],v[72:75],v[36:39]\nds_read_b128 v[72:75],v20 offset:31744\nv_mul_f32 v164,v17,v164\nv_mul_f32 v165,v17,v165\nM v[36:39],a[116:119],v[76:79],v[36:39]\nbuffer_load_dwordx4 v2,s[36:39],s47 offen offset:2048 sc1 lds\n" \
"v_and_b32 v114,0xffff0000,v114\nM v[36:39],a[108:111],v[84:87],v[36:39]\nv_lshlrev_b32 v231,16,v115\nv_mul_f32 v166,v17,v166\nv_fmac_f32 v162,v229,v19\nM v[36:39],a[100:103],v[92:95],v[36:39]\ns_waitcnt lgkmcnt(3)\nv_fmac_f32 v163,v113,v19\n" \
"C v160,v160,v161\nv_mul_f32 v167,v17,v167\nM v[40:43],a[96:99],v[60:63],v[40:43]\ns_waitcnt lgkmcnt(2)\nv_fmac_f32 v164,v230,v19\nv_fmac_f32 v165,v114,v19\nv_and_b32 v115,0xffff0000,v115\nM v[40:43],a[104:107],v[88:91],v[40:43]\n" \
"s_waitcnt lgkmcnt(1)\nds_read_b128 v[76:79],v20 offset:49152\nv_fmac_f32 v166,v231,v19\nC v161,v162,v163\nM v[40:43],a[112:115],v[80:83],v[40:43]\ns_waitcnt lgkmcnt(1)\nds_read_b128 v[84:87],v20 offset:50176\nv_fmac_f32 v167,v115,v19\n" \
"C v162,v164,v165\nM v[40:43],a[120:123],v[72:75],v[40:43]\nbuffer_store_dwordx4 v[152:155],v4,s[36:39],s62 offen offset:3072 sc1\nds_read_b128 v[92:95],v20 offset:51200\nM v[44:47],a[124:127],v[72:75],v[44:47]\ns_add_u32 s62,s51,262144\nds_read_b128 v[72:75],v20 offset:52224\nC v163,v166,v167\n" \
"v_lshlrev_b32 v236,16,v116\nM v[44:47],a[116:119],v[80:83],v[44:47]\ns_lshl_b32 s59,s61,20\nv_mul_f32 v168,v17,v168\nv_mul_f32 v169,v17,v169\nv_and_b32 v116,0xffff0000,v116\nM v[44:47],a[108:111],v[88:91],v[44:47]\ns_lshl_b32 s58,s60,15\n" \
"v_lshlrev_b32 v237,16,v117\nv_lshlrev_b32 v238,16,v118\nM v[44:47],a[100:103],v[60:63],v[44:47]\ns_waitcnt lgkmcnt(3)\nbuffer_load_dwordx4 v2,s[36:39],s47 offen offset:3072 sc1 lds\nv_mul_f32 v170,v17,v170\nM v[192:195],a[0:3],v[76:79],0\ns_waitcnt lgkmcnt(2)\n" \
"s_add_u32 m0,s22,49152\nv_lshlrev_b32 v239,16,v119\nv_mul_f32 v171,v17,v171\nv_fmac_f32 v168,v236,v19\nM v[192:195],a[8:11],v[84:87],v[192:195]\ns_waitcnt lgkmcnt(1)\ns_add_u32 s47,s30,16384\nds_read_b128 v[80:83],v20 offset:57344\n" \
"v_fmac_f32 v169,v116,v19\nv_and_b32 v117,0xffff0000,v117\nM v[192:195],a[16:19],v[92:95],v[192:195]\ns_waitcnt lgkmcnt(1)\ns_add_u32 s94,s59,s58\nds_read_b128 v[88:91],v20 offset:58368\nds_read_b128 v[60:63],v20 offset:59392\nv_mul_f32 v172,v17,v172\n" \
"M v[192:195],a[24:27],v[72:75],v[192:195]\nbuffer_load_dwordx4 v2,s[36:39],s47 offen offset:0 sc1 lds\nv_mul_f32 v173,v17,v173\nM v[196:199],a[28:31],v[72:75],0\ns_add_u32 s58,s60,1\nds_read_b128 v[72:75],v20 offset:60416\nv_and_b32 v118,0xffff0000,v118\nv_mul_f32 v174,v17,v174\n" \
"M v[196:199],a[20:23],v[92:95],v[196:199]\ns_and_b32 s58,s58,3\nv_mul_f32 v175,v17,v175\nv_fmac_f32 v170,v237,v19\nv_and_b32 v119,0xffff0000,v119\nM v[196:199],a[12:15],v[84:87],v[196:199]\ns_and_b32 s59,s58,2\nv_fmac_f32 v171,v117,v19\n" \
"C v168,v168,v169\nM v[196:199],a[4:7],v[76:79],v[196:199]\ns_waitcnt lgkmcnt(3)\ns_add_u32 s59,s59,s58\nv_fmac_f32 v172,v238,v19\nv_fmac_f32 v173,v118,v19\nM v[200:203],a[0:3],v[80:83],0\ns_waitcnt lgkmcnt(1)\n" \
"s_add_u32 s94,s94,58720256\nv_fmac_f32 v174,v239,v19\nv_fmac_f32 v175,v119,v19\nM v[200:203],a[8:11],v[88:91],v[200:203]\ns_lshl_b32 s59,s59,17\nds_read_b128 v[92:95],v20 offset:53248\nds_read_b128 v[84:87],v20 offset:54272\nM v[200:203],a[16:19],v[60:63],v[200:203]\n" \
"s_waitcnt lgkmcnt(2)\nbuffer_load_dwordx4 v2,s[36:39],s47 offen offset:1024 sc1 lds\nC v169,v170,v171\nM v[200:203],a[24:27],v[72:75],v[200:203]\ns_add_u32 s32,s94,s59\nds_read_b128 v[76:79],v20 offset:55296\nC v170,v172,v173\nC v171,v174,v175\n" \
"M v[204:207],a[28:31],v[72:75],0\ns_lshl_b32 s58,s58,1\nds_read_b128 v[72:75],v20 offset:56320\nv_lshlrev_b32 v244,16,v120\nM v[204:207],a[20:23],v[60:63],v[204:207]\ns_and_b32 s59,%1,0xf9\nv_lshlrev_b32 v245,16,v121\nv_mul_f32 v176,v17,v176\n" \
"M v[204:207],a[12:15],v[88:91],v[204:207]\nbuffer_store_dwordx4 v[160:163],v4,s[36:39],s62 offen offset:0 sc1\nM v[204:207],a[4:7],v[80:83],v[204:207]\ns_waitcnt lgkmcnt(2)\ns_or_b32 s58,s58,s59\nds_read_b128 v[60:63],v20 offset:61440\nv_mul_f32 v177,v17,v177\nM v[192:195],a[32:35],v[92:95],v[192:195]\n" \
"s_lshl_b32 s58,s58,2\nds_read_b128 v[88:91],v20 offset:62464\nv_and_b32 v120,0xffff0000,v120\nM v[192:195],a[40:43],v[84:87],v[192:195]\ns_waitcnt vmcnt(16) lgkmcnt(14)\ns_waitcnt lgkmcnt(3)\nds_read_b128 v[80:83],v20 offset:63488\nv_lshlrev_b32 v246,16,v122\n" \
"M v[192:195],a[48:51],v[76:79],v[192:195]\ns_add_u32 s58,s58,%2\ns_barrier\ns_waitcnt lgkmcnt(3)\nv_mul_f32 v178,v17,v178\nv_lshlrev_b32 v247,16,v123\nM v[192:195],a[56:59],v[72:75],v[192:195]\ns_mul_i32 s58,s58,12288\n" \
"v_mul_f32 v179,v17,v179\nv_and_b32 v121,0xffff0000,v121\nM v[196:199],a[60:63],v[72:75],v[196:199]\ns_add_u32 s44,s58,109058048\nds_read_b128 v[72:75],v20 offset:64512\nv_fmac_f32 v176,v244,v19\nv_fmac_f32 v177,v120,v19\nM v[196:199],a[52:55],v[76:79],v[196:199]\n" \
"s_add_u32 s58,s60,2\nv_mul_f32 v180,v17,v180\nv_mul_f32 v181,v17,v181\nM v[196:199],a[44:47],v[84:87],v[196:199]\ns_and_b32 s58,s58,3\nv_mul_f32 v182,v17,v182\nv_and_b32 v122,0xffff0000,v122\nM v[196:199],a[36:39],v[92:95],v[196:199]\n" \
"s_waitcnt lgkmcnt(1)\ns_and_b32 s59,s58,2\nv_mul_f32 v183,v17,v183\nv_and_b32 v123,0xffff0000,v123\nM v[200:203],a[32:35],v[60:63],v[200:203]\ns_add_u32 s59,s59,s58\nds_read_b128 v[76:79],v4 offset:0\nv_fmac_f32 v178,v245,v19\n" \
"v_fmac_f32 v179,v121,v19\nM v[200:203],a[40:43],v[88:91],v[200:203]\ns_lshl_b32 s59,s59,17\nds_read_b128 v[84:87],v4 offset:1024\nC v176,v176,v177\nM v[200:203],a[48:51],v[80:83],v[200:203]\ns_waitcnt lgkmcnt(2)\ns_add_u32 s33,s94,s59\n" \
"v_fmac_f32 v180,v246,v19\nv_fmac_f32 v181,v122,v19\nM v[200:203],a[56:59],v[72:75],v[200:203]\nbuffer_store_dwordx4 v[168:171],v4,s[36:39],s62 offen offset:1024 sc1\nds_read_b128 v[92:95],v4 offset:2048\nM v[204:207],a[60:63],v[72:75],v[204:207]\ns_lshl_b32 s58,s58,1\nds_read_b128 v[72:75],v4 offset:3072\n" \
"v_fmac_f32 v182,v247,v19\nv_fmac_f32 v183,v123,v19\nM v[204:207],a[52:55],v[80:83],v[204:207]\ns_and_b32 s59,%1,0xf9\nds_read_b128 v[80:83],v4 offset:8192\nC v177,v178,v179\nM v[204:207],a[44:47],v[88:91],v[204:207]\ns_or_b32 s58,s58,s59\n" \
"C v178,v180,v181\nC v179,v182,v183\nM v[204:207],a[36:39],v[60:63],v[204:207]\ns_waitcnt lgkmcnt(3)\nbuffer_load_dwordx4 v2,s[36:39],s47 offen offset:2048 sc1 lds\nv_lshlrev_b32 v252,16,v124\nM v[192:195],a[64:67],v[76:79],v[192:195]\ns_lshl_b32 s58,s58,2\n" \
"ds_read_b128 v[88:91],v4 offset:9216\nv_mul_f32 v184,v17,v184\nM v[192:195],a[72:75],v[84:87],v[192:195]\ns_waitcnt lgkmcnt(3)\ns_add_u32 s58,s58,%2\nds_read_b128 v[60:63],v4 offset:10240\nv_mul_f32 v185,v17,v185\nM v[192:195],a[80:83],v[92:95],v[192:195]\n" \
"s_waitcnt lgkmcnt(3)\ns_mul_i32 s58,s58,12288\nv_lshlrev_b32 v253,16,v125\nv_and_b32 v124,0xffff0000,v124\nM v[192:195],a[88:91],v[72:75],v[192:195]\ns_add_u32 s45,s58,109060096\nv_lshlrev_b32 v254,16,v126\nv_mul_f32 v186,v17,v186\n" \
"M v[196:199],a[92:95],v[72:75],v[196:199]\ns_add_u32 s58,s60,3\nds_read_b128 v[72:75],v4 offset:11264\nv_mul_f32 v187,v17,v187\nM v[196:199],a[84:87],v[92:95],v[196:199]\nbuffer_load_dwordx4 v2,s[36:39],s47 offen offset:3072 sc1 lds\nM v[196:199],a[76:79],v[84:87],v[196:199]\ns_add_u32 m0,s22,131072\n" \
"ds_read_b128 v[92:95],v4 offset:4096\nv_fmac_f32 v184,v252,v19\nM v[196:199],a[68:71],v[76:79],v[196:199]\ns_waitcnt lgkmcnt(2)\ns_add_u32 s47,s31,0\nds_read_b128 v[84:87],v4 offset:5120\nv_and_b32 v125,0xffff0000,v125\nM v[200:203],a[64:67],v[80:83],v[200:203]\n" \
"s_and_b32 s58,s58,3\nds_read_b128 v[76:79],v4 offset:6144\nv_fmac_f32 v185,v124,v19\nM v[200:203],a[72:75],v[88:91],v[200:203]\ns_and_b32 s59,s58,2\nv_mul_f32 v188,v17,v188\nv_mul_f32 v189,v17,v189\nM v[200:203],a[80:83],v[60:63],v[200:203]\n" \
"s_waitcnt lgkmcnt(3)\ns_add_u32 s59,s59,s58\nv_and_b32 v126,0xffff0000,v126\nv_fmac_f32 v186,v253,v19\nM v[200:203],a[88:91],v[72:75],v[200:203]\nbuffer_store_dwordx4 v[176:179],v4,s[36:39],s62 offen offset:2048 sc1\nv_fmac_f32 v187,v125,v19\nM v[204:207],a[92:95],v[72:75],v[204:207]\n" \
"s_lshl_b32 s59,s59,17\nds_read_b128 v[72:75],v4 offset:7168\nv_lshlrev_b32 v255,16,v127\nC v184,v184,v185\nM v[204:207],a[84:87],v[60:63],v[204:207]\ns_add_u32 s34,s94,s59\nds_read_b128 v[60:63],v4 offset:12288\nv_mul_f32 v190,v17,v190\n" \
"M v[204:207],a[76:79],v[88:91],v[204:207]\ns_lshl_b32 s58,s58,1\nds_read_b128 v[88:91],v4 offset:13312\nv_mul_f32 v191,v17,v191\nM v[204:207],a[68:71],v[80:83],v[204:207]\ns_waitcnt lgkmcnt(3)\ns_and_b32 s59,%1,0xf9\nv_fmac_f32 v188,v254,v19\n" \
"v_fmac_f32 v189,v126,v19\nM v[192:195],a[96:99],v[92:95],v[192:195]\ns_or_b32 s58,s58,s59\nv_and_b32 v127,0xffff0000,v127\nC v185,v186,v187\nM v[192:195],a[104:107],v[84:87],v[192:195]\ns_waitcnt vmcnt(15) lgkmcnt(14)\nds_read_b128 v[80:83],v4 offset:14336\n" \
"v_fmac_f32 v190,v255,v19\nM v[192:195],a[112:115],v[76:79],v[192:195]\ns_waitcnt vmcnt(10)\ns_barrier\ns_waitcnt lgkmcnt(3)\nv_cmp_gt_u64 s[98:99],s[14:15],v[10:11]\nv_cmp_le_u64 s[18:19],s[16:17],v[10:11]\nM v[192:195],a[120:123],v[72:75],v[192:195]\n" \
"s_or_b64 s[98:99],s[98:99],s[18:19]\ns_and_b32 s98,s98,0xff000000\ns_cbranch_scc1 LPM_%=\nLPMR_%=:\nbuffer_load_dwordx4 v2,s[36:39],s47 offen offset:0 sc1 lds\nv_fmac_f32 v191,v127,v19\nM v[196:199],a[124:127],v[72:75],v[196:199]\ns_lshl_b32 s58,s58,2\n" \
"ds_read_b128 v[72:75],v4 offset:15360\nC v186,v188,v189\nC v187,v190,v191\nM v[196:199],a[116:119],v[76:79],v[196:199]\ns_add_u32 s58,s58,%2\nds_read_b128 v[76:79],v4 offset:32768\nv_mov_b32 v29,s11\nM v[196:199],a[108:111],v[84:87],v[196:199]\n" \
"s_mul_i32 s58,s58,12288\nds_read_b128 v[84:87],v4 offset:33792\nv_xor_b32 v6,0x20000,v6\nM v[196:199],a[100:103],v[92:95],v[196:199]\ns_waitcnt lgkmcnt(3)\nbuffer_load_dwordx4 v2,s[36:39],s47 offen offset:1024 sc1 lds\nM v[200:203],a[96:99],v[60:63],v[200:203]\ns_add_u32 s48,s58,109062144\n" \
"ds_read_b128 v[92:95],v4 offset:34816\nv_xor_b32 v7,0x20000,v7\nM v[200:203],a[104:107],v[88:91],v[200:203]\ns_add_u32 s58,s60,4\nv_xor_b32 v8,0x20000,v8\nv_xor_b32 v9,0x20000,v9\nM v[200:203],a[112:115],v[80:83],v[200:203]\ns_waitcnt lgkmcnt(3)\n" \
"s_and_b32 s58,s58,3\nv_and_b32 v30,0xf0,v4\nv_lshrrev_b32 v15,2,v30\nM v[200:203],a[120:123],v[72:75],v[200:203]\nbuffer_store_dwordx4 v[184:187],v4,s[36:39],s62 offen offset:3072 sc1\nM v[204:207],a[124:127],v[72:75],v[204:207]\ns_and_b32 s59,s58,2\nds_read_b128 v[72:75],v4 offset:35840\n" \
"v_lshlrev_b32 v13,7,v30\nM v[204:207],a[116:119],v[80:83],v[204:207]\ns_add_u32 s59,s59,s58\nds_read_b128 v[80:83],v4 offset:40960\nv_lshl_add_u32 v114,v30,2,s22\nM v[204:207],a[108:111],v[88:91],v[204:207]\ns_and_b32 s58,%1,0xf9\nds_read_b128 v[88:91],v4 offset:41984\n" \
"v_bfe_u32 v30,v4,8,1\nM v[204:207],a[100:103],v[60:63],v[204:207]\ns_waitcnt lgkmcnt(3)\nbuffer_load_dwordx4 v2,s[36:39],s47 offen offset:2048 sc1 lds\nM v[48:51],a[128:131],v[76:79],v[48:51]\ns_lshl_b32 s59,s59,17\nds_read_b128 v[60:63],v4 offset:43008\nv_lshrrev_b32 v31,9,v4\n" \
"M v[48:51],a[136:139],v[84:87],v[48:51]\ns_lshl_b32 s58,s58,2\nv_lshl_add_u32 v13,v30,5,v13\nv_lshl_add_u32 v114,v30,3,v114\nM v[48:51],a[144:147],v[92:95],v[48:51]\ns_waitcnt lgkmcnt(3)\ns_add_u32 s35,s94,s59\nv_lshl_add_u32 v13,v31,4,v13\n" \
"v_lshl_add_u32 v114,v31,4,v114\nM v[48:51],a[152:155],v[72:75],v[48:51]\nbuffer_load_dwordx4 v2,s[36:39],s47 offen offset:3072 sc1 lds\nM v[52:55],a[156:159],v[72:75],v[52:55]\ns_add_u32 m0,s22,147456\nds_read_b128 v[72:75],v4 offset:44032\nv_and_b32 v30,0x3c0,v4\nM v[52:55],a[148:151],v[92:95],v[52:55]\n" \
"s_add_u32 s47,s31,8192\nds_read_b128 v[92:95],v4 offset:36864\nv_and_b32 v31,0x30,v4\nM v[52:55],a[140:143],v[84:87],v[52:55]\ns_add_u32 s58,s58,%2\nds_read_b128 v[84:87],v4 offset:37888\nv_lshl_add_u32 v112,v30,5,v31\nM v[52:55],a[132:135],v[76:79],v[52:55]\n" \
"s_waitcnt lgkmcnt(3)\nbuffer_load_dwordx4 v1,s[84:87],s46 offen offset:0 lds\nM v[64:67],a[128:131],v[80:83],v[64:67]\ns_add_u32 m0,s22,148352\nds_read_b128 v[76:79],v4 offset:38912\nv_lshl_add_u32 v113,v30,1,v31\nM v[64:67],a[136:139],v[88:91],v[64:67]\ns_sub_u32 s59,3,s60\n" \
"v_lshrrev_b32 v31,1,v31\nv_add3_u32 v115,v30,v31,s22\nM v[64:67],a[144:147],v[60:63],v[64:67]\ns_waitcnt lgkmcnt(3)\ns_lshl_b32 s95,s58,5\nv_bfe_u32 v30,v4,7,2\nv_xor_b32 v114,0x20000,v114\nM v[64:67],a[152:155],v[72:75],v[64:67]\n" \
"buffer_load_dwordx4 v1,s[84:87],s46 offen offset:128 lds\nM v[68:71],a[156:159],v[72:75],v[68:71]\ns_mov_b32 s46,1638400\ns_add_u32 m0,s22,149504\nds_read_b128 v[72:75],v4 offset:39936\nv_xor_b32 v115,0x20000,v115\nM v[68:71],a[148:151],v[60:63],v[68:71]\ns_and_b32 s59,s59,3\n" \
"ds_read_b128 v[60:63],v4 offset:45056\nv_lshrrev_b32 v190,4,v4\nM v[68:71],a[140:143],v[88:91],v[68:71]\ns_mul_i32 s58,s58,12288\nds_read_b128 v[88:91],v4 offset:46080\nM v[68:71],a[132:135],v[80:83],v[68:71]\ns_waitcnt lgkmcnt(3)\nbuffer_load_dwordx4 v1,s[84:87],s46 offen offset:0 lds\n" \
"M v[48:51],a[160:163],v[92:95],v[48:51]\ns_add_u32 m0,s22,150400\nds_read_b128 v[80:83],v4 offset:47104\nM v[48:51],a[168:171],v[84:87],v[48:51]\ns_waitcnt vmcnt(15) lgkmcnt(15)\nM v[48:51],a[176:179],v[76:79],v[48:51]\ns_lshl_b32 s59,s59,11\ns_barrier\n" \
"s_waitcnt lgkmcnt(3)\nds_read_b128 v[176:179],v4 offset:27648\nM v[48:51],a[184:187],v[72:75],v[48:51]\nbuffer_load_dwordx4 v1,s[84:87],s46 offen offset:128 lds\nM v[52:55],a[188:191],v[72:75],v[52:55]\ns_add_u32 m0,s22,65536\ns_mov_b32 s46,1703936\nds_read_b128 v[72:75],v4 offset:48128\n" \
"M v[52:55],a[180:183],v[76:79],v[52:55]\ns_add_u32 s59,s59,s58\nds_read_b128 v[76:79],v4 offset:16384\nM v[52:55],a[172:175],v[84:87],v[52:55]\ns_add_u32 s59,s59,109051904\nds_read_b128 v[84:87],v4 offset:17408\nM v[52:55],a[164:167],v[92:95],v[52:55]\ns_waitcnt lgkmcnt(4)\n" \
"buffer_load_dwordx4 v1,s[84:87],s46 offen offset:0 lds\nM v[64:67],a[160:163],v[60:63],v[64:67]\ns_add_u32 m0,s22,66432\nds_read_b128 v[92:95],v4 offset:18432\nM v[64:67],a[168:171],v[88:91],v[64:67]\ns_cmp_eq_u32 s60,0\ns_cselect_b32 s40,0x40000000,s59\nds_read_b128 v[180:183],v4 offset:20480\n" \
"M v[64:67],a[176:179],v[80:83],v[64:67]\ns_waitcnt lgkmcnt(4)\ns_sub_u32 s59,4,s60\nM v[64:67],a[184:187],v[72:75],v[64:67]\nbuffer_load_dwordx4 v1,s[84:87],s46 offen offset:128 lds\nM v[68:71],a[188:191],v[72:75],v[68:71]\ns_add_u32 m0,s22,67584\ns_mov_b32 s46,1769472\n" \
"ds_read_b128 v[72:75],v4 offset:19456\nM v[68:71],a[180:183],v[80:83],v[68:71]\ns_and_b32 s59,s59,3\nds_read_b128 v[80:83],v4 offset:24576\nM v[68:71],a[172:175],v[88:91],v[68:71]\ns_lshl_b32 s59,s59,11\nds_read_b128 v[88:91],v4 offset:25600\nM v[68:71],a[164:167],v[60:63],v[68:71]\n" \
"s_waitcnt lgkmcnt(3)\nbuffer_load_dwordx4 v1,s[84:87],s46 offen offset:0 lds\nM v[32:35],a[128:131],v[76:79],v[32:35]\ns_add_u32 m0,s22,68480\nds_read_b128 v[60:63],v4 offset:26624\nM v[32:35],a[136:139],v[84:87],v[32:35]\ns_add_u32 s59,s59,s58\nM v[32:35],a[144:147],v[92:95],v[32:35]\n" \
"s_waitcnt lgkmcnt(3)\ns_add_u32 s59,s59,109150208\nM v[32:35],a[152:155],v[72:75],v[32:35]\nbuffer_load_dwordx4 v1,s[84:87],s46 offen offset:128 lds\nM v[36:39],a[156:159],v[72:75],v[36:39]\ns_add_u32 m0,s22,98304\nds_read_b128 v[72:75],v4 offset:21504\nM v[36:39],a[148:151],v[92:95],v[36:39]\n" \
"s_mov_b32 s46,1835008\nds_read_b128 v[92:95],v4 offset:22528\nM v[36:39],a[140:143],v[84:87],v[36:39]\ns_cmp_eq_u32 s60,1\ns_cselect_b32 s41,0x40000000,s59\nds_read_b128 v[84:87],v4 offset:23552\nM v[36:39],a[132:135],v[76:79],v[36:39]\ns_waitcnt lgkmcnt(3)\n" \
"buffer_load_dwordx4 v2,s[36:39],s47 offen offset:0 sc1 lds\nM v[40:43],a[128:131],v[80:83],v[40:43]\ns_sub_u32 s59,5,s60\nds_read_b128 v[76:79],v4 offset:28672\nM v[40:43],a[136:139],v[88:91],v[40:43]\ns_and_b32 s59,s59,3\nM v[40:43],a[144:147],v[60:63],v[40:43]\ns_lshl_b32 s59,s59,11\n" \
"M v[40:43],a[152:155],v[176:179],v[40:43]\nbuffer_load_dwordx4 v2,s[36:39],s47 offen offset:1024 sc1 lds\nM v[44:47],a[156:159],v[176:179],v[44:47]\ns_add_u32 s59,s59,s58\nds_read_b128 v[176:179],v4 offset:29696\nM v[44:47],a[148:151],v[60:63],v[44:47]\ns_waitcnt vmcnt(16) lgkmcnt(13)\nds_read_b128 v[60:63],v4 offset:30720\n" \
"M v[44:47],a[140:143],v[88:91],v[44:47]\ns_add_u32 s59,s59,109248512\ns_barrier\nds_read_b128 v[88:91],v4 offset:31744\nM v[44:47],a[132:135],v[80:83],v[44:47]\nbuffer_load_dwordx4 v2,s[36:39],s47 offen offset:2048 sc1 lds\nM v[32:35],a[160:163],v[180:183],v[32:35]\ns_cmp_eq_u32 s60,2\n" \
"s_cselect_b32 s42,0x40000000,s59\ns_waitcnt lgkmcnt(3)\nds_read_b128 v[80:83],v4 offset:49152\nM v[32:35],a[168:171],v[72:75],v[32:35]\ns_sub_u32 s59,6,s60\nM v[32:35],a[176:179],v[92:95],v[32:35]\ns_and_b32 s59,s59,3\nM v[32:35],a[184:187],v[84:87],v[32:35]\n" \
"buffer_load_dwordx4 v2,s[36:39],s47 offen offset:3072 sc1 lds\nM v[36:39],a[188:191],v[84:87],v[36:39]\ns_add_u32 m0,s22,81920\ns_add_u32 s47,s31,16384\nds_read_b128 v[84:87],v4 offset:50176\nM v[36:39],a[180:183],v[92:95],v[36:39]\ns_lshl_b32 s59,s59,11\nds_read_b128 v[92:95],v4 offset:51200\n" \
"M v[36:39],a[172:175],v[72:75],v[36:39]\ns_add_u32 s59,s59,s58\nds_read_b128 v[72:75],v4 offset:52224\nM v[36:39],a[164:167],v[180:183],v[36:39]\nbuffer_load_dwordx4 v2,s[36:39],s47 offen offset:0 sc1 lds\nM v[40:43],a[160:163],v[76:79],v[40:43]\ns_waitcnt lgkmcnt(3)\ns_add_u32 s59,s59,109346816\n" \
"ds_read_b128 v[180:183],v4 offset:57344\nM v[40:43],a[168:171],v[176:179],v[40:43]\ns_and_b32 s58,s61,6\nM v[40:43],a[176:179],v[60:63],v[40:43]\ns_cmp_eq_u32 s60,3\ns_cselect_b32 s43,0x40000000,s59\nM v[40:43],a[184:187],v[88:91],v[40:43]\nbuffer_load_dwordx4 v2,s[36:39],s47 offen offset:1024 sc1 lds\n" \
"M v[44:47],a[188:191],v[88:91],v[44:47]\ns_add_u32 s58,s58,s61\nds_read_b128 v[88:91],v4 offset:58368\nM v[44:47],a[180:183],v[60:63],v[44:47]\nLG22_%=:\ns_cbranch_execz LG24_%=\ns_lshl_b32 s59,s60,4\nds_read_b128 v[60:63],v4 offset:59392\n" \
"M v[44:47],a[172:175],v[176:179],v[44:47]\ns_add_u32 s92,s58,s59\nds_read_b128 v[176:179],v4 offset:60416\nM v[44:47],a[164:167],v[76:79],v[44:47]\nbuffer_load_dwordx4 v2,s[36:39],s47 offen offset:2048 sc1 lds\nM v[192:195],a[128:131],v[80:83],v[192:195]\ns_waitcnt lgkmcnt(3)\ns_and_b32 s58,s60,2\n" \
"ds_read_b128 v[76:79],v4 offset:53248\nM v[192:195],a[136:139],v[84:87],v[192:195]\ns_add_u32 s58,s58,s60\nM v[192:195],a[144:147],v[92:95],v[192:195]\ns_lshl_b32 s59,s61,3\nM v[192:195],a[152:155],v[72:75],v[192:195]\nbuffer_load_dwordx4 v2,s[36:39],s47 offen offset:3072 sc1 lds\nM v[196:199],a[156:159],v[72:75],v[196:199]\n" \
"s_add_u32 m0,s22,114688\nds_read_b128 v[72:75],v4 offset:54272\nM v[196:199],a[148:151],v[92:95],v[196:199]\ns_add_u32 s47,s28,24576\nds_read_b128 v[92:95],v4 offset:55296\nM v[196:199],a[140:143],v[84:87],v[196:199]\ns_lshr_b32 s63,%1,5\nds_read_b128 v[84:87],v4 offset:56320\n" \
"M v[196:199],a[132:135],v[80:83],v[196:199]\nbuffer_load_dwordx4 v1,s[84:87],s46 offen offset:0 lds\nM v[200:203],a[128:131],v[180:183],v[200:203]\ns_waitcnt lgkmcnt(3)\ns_add_u32 m0,s22,115584\nds_read_b128 v[80:83],v4 offset:61440\nM v[200:203],a[136:139],v[88:91],v[200:203]\ns_add_u32 s58,s58,s59\n" \
"M v[200:203],a[144:147],v[60:63],v[200:203]\ns_lshl_b32 s63,s63,2\nM v[200:203],a[152:155],v[176:179],v[200:203]\nbuffer_load_dwordx4 v1,s[84:87],s46 offen offset:128 lds\nM v[204:207],a[156:159],v[176:179],v[204:207]\ns_add_u32 m0,s22,116736\nds_read_b128 v[176:179],v4 offset:62464\nM v[204:207],a[148:151],v[60:63],v[204:207]\n" \
"s_waitcnt vmcnt(18) lgkmcnt(14)\nds_read_b128 v[60:63],v4 offset:63488\nM v[204:207],a[140:143],v[88:91],v[204:207]\ns_mov_b32 s46,1900544\ns_barrier\nds_read_b128 v[88:91],v4 offset:64512\nM v[204:207],a[132:135],v[180:183],v[204:207]\nbuffer_load_dwordx4 v1,s[84:87],s46 offen offset:0 lds\n" \
"M v[192:195],a[160:163],v[76:79],v[192:195]\ns_waitcnt lgkmcnt(3)\ns_add_u32 m0,s22,117632\nds_read_b128 v[180:183],v23 offset:0\nM v[192:195],a[168:171],v[72:75],v[192:195]\ns_add_u32 s93,s58,64\nM v[192:195],a[176:179],v[92:95],v[192:195]\ns_add_u32 s63,s63,%2\n" \
"M v[192:195],a[184:187],v[84:87],v[192:195]\ns_waitcnt vmcnt(21)\nM v[196:199],a[188:191],v[84:87],v[196:199]\nbuffer_store_dwordx2 v[28:29],v56,s[36:39],s50 offen offset:0 sc1\nM v[196:199],a[180:183],v[92:95],v[196:199]\ns_lshl_b32 s58,s92,15\nds_read_b128 v[84:87],v23 offset:1024\nds_read_b128 v[92:95],v23 offset:2048\n" \
"M v[196:199],a[172:175],v[72:75],v[196:199]\nbuffer_load_dwordx4 v1,s[84:87],s46 offen offset:128 lds\nM v[196:199],a[164:167],v[76:79],v[196:199]\ns_add_u32 m0,s22,0\nM v[200:203],a[160:163],v[80:83],v[200:203]\ns_waitcnt lgkmcnt(2)\ns_lshl_b32 s59,s63,6\nds_read_b128 v[72:75],v23 offset:3072\n" \
"M v[200:203],a[168:171],v[176:179],v[200:203]\ns_add_u32 s80,s58,s59\nds_read_b128 v[76:79],v23 offset:8192\nM v[200:203],a[176:179],v[60:63],v[200:203]\nbuffer_load_dwordx4 v2,s[36:39],s47 offen offset:0 sc1 lds\nM v[200:203],a[184:187],v[88:91],v[200:203]\ns_and_b32 s58,s58,0x3e0000\nM v[204:207],a[188:191],v[88:91],v[204:207]\n" \
"s_lshl_b32 s59,s63,12\nds_read_b128 v[88:91],v23 offset:9216\nM v[204:207],a[180:183],v[60:63],v[204:207]\ns_add_u32 s58,s58,s59\nds_read_b128 v[60:63],v23 offset:10240\nM v[204:207],a[172:175],v[176:179],v[204:207]\nbuffer_load_dwordx4 v2,s[36:39],s47 offen offset:1024 sc1 lds\nM v[204:207],a[164:167],v[80:83],v[204:207]\n" \
"s_and_b32 s59,s92,1\nds_read_b128 v[176:179],v23 offset:11264\nM v[208:211],a[0:3],v[180:183],0\ns_waitcnt lgkmcnt(3)\ns_lshl_b32 s59,s59,6\nds_read_b128 v[80:83],v23 offset:4096\nM v[208:211],a[8:11],v[84:87],v[208:211]\ns_add_u32 s58,s58,s59\n" \
"M v[208:211],a[16:19],v[92:95],v[208:211]\nbuffer_load_dwordx4 v2,s[36:39],s47 offen offset:2048 sc1 lds\nM v[208:211],a[24:27],v[72:75],v[208:211]\ns_add_u32 s82,s58,67108864\nM v[212:215],a[28:31],v[72:75],0\ns_lshl_b32 s58,s93,15\nds_read_b128 v[72:75],v23 offset:5120\nM v[212:215],a[20:23],v[92:95],v[212:215]\n" \
"s_lshl_b32 s59,s63,6\nds_read_b128 v[92:95],v23 offset:6144\nM v[212:215],a[12:15],v[84:87],v[212:215]\nbuffer_load_dwordx4 v2,s[36:39],s47 offen offset:3072 sc1 lds\nM v[212:215],a[4:7],v[180:183],v[212:215]\ns_add_u32 m0,s22,32768\ns_add_u32 s47,s29,24576\nds_read_b128 v[84:87],v23 offset:7168\n" \
"M v[216:219],a[0:3],v[76:79],0\ns_waitcnt lgkmcnt(3)\ns_add_u32 s81,s58,s59\nds_read_b128 v[180:183],v23 offset:12288\nM v[216:219],a[8:11],v[88:91],v[216:219]\ns_and_b32 s58,s58,0x3e0000\nM v[216:219],a[16:19],v[60:63],v[216:219]\nbuffer_load_dwordx4 v2,s[36:39],s47 offen offset:0 sc1 lds\n" \
"M v[216:219],a[24:27],v[176:179],v[216:219]\ns_lshl_b32 s59,s63,12\nM v[220:223],a[28:31],v[176:179],0\ns_add_u32 s58,s58,s59\nds_read_b128 v[176:179],v23 offset:13312\nM v[220:223],a[20:23],v[60:63],v[220:223]\ns_waitcnt vmcnt(10) lgkmcnt(14)\nds_read_b128 v[60:63],v23 offset:14336\n" \
"M v[220:223],a[12:15],v[88:91],v[220:223]\nbuffer_load_dwordx4 v2,s[36:39],s47 offen offset:1024 sc1 lds\ns_barrier\nM v[220:223],a[4:7],v[76:79],v[220:223]\ns_and_b32 s59,s93,1\nds_read_b128 v[88:91],v23 offset:15360\nM v[208:211],a[32:35],v[80:83],v[208:211]\ns_waitcnt lgkmcnt(3)\n" \
"s_lshl_b32 s59,s59,6\nds_read_b128 v[76:79],v20 offset:32768\nM v[208:211],a[40:43],v[72:75],v[208:211]\ns_add_u32 s58,s58,s59\nds_read_b64_tr_b16 a[192:193],v6 offset:16384\nM v[208:211],a[48:51],v[92:95],v[208:211]\nbuffer_load_dwordx4 v2,s[36:39],s47 offen offset:2048 sc1 lds\nM v[208:211],a[56:59],v[84:87],v[208:211]\n" \
"s_add_u32 s83,s58,67108864\nds_read_b64_tr_b16 a[194:195],v7 offset:16384\nM v[212:215],a[60:63],v[84:87],v[212:215]\ns_lshl_b32 s58,s61,10\nds_read_b128 v[84:87],v20 offset:33792\nM v[212:215],a[52:55],v[92:95],v[212:215]\ns_lshl_b32 s59,s60,6\nds_read_b128 v[92:95],v20 offset:34816\n" \
"M v[212:215],a[44:47],v[72:75],v[212:215]\nbuffer_load_dwordx4 v2,s[36:39],s47 offen offset:3072 sc1 lds\nM v[212:215],a[36:39],v[80:83],v[212:215]\ns_add_u32 m0,s22,16384\nds_read_b128 v[72:75],v20 offset:35840\nM v[216:219],a[32:35],v[180:183],v[216:219]\ns_waitcnt lgkmcnt(4)\ns_add_u32 s47,s30,24576\n" \
"ds_read_b128 v[80:83],v20 offset:40960\nM v[216:219],a[40:43],v[176:179],v[216:219]\ns_add_u32 s58,s58,s59\nds_read_b64_tr_b16 a[200:201],v6 offset:18432\nv_xor_b32 v6,0x30000,v6\nM v[216:219],a[48:51],v[60:63],v[216:219]\nbuffer_load_dwordx4 v2,s[36:39],s47 offen offset:0 sc1 lds\nM v[216:219],a[56:59],v[88:91],v[216:219]\n" \
"s_add_u32 s95,s95,94437376\nM v[220:223],a[60:63],v[88:91],v[220:223]\ns_add_u32 s58,s58,94396416\nds_read_b128 v[88:91],v20 offset:41984\nv_lshl_add_u32 v58,v30,8,s58\nM v[220:223],a[52:55],v[60:63],v[220:223]\ns_mov_b64 s[64:65],s[12:13]\nds_read_b128 v[60:63],v20 offset:43008\n" \
"v_bfe_u32 v30,v4,4,3\nM v[220:223],a[44:47],v[176:179],v[220:223]\nbuffer_load_dwordx4 v2,s[36:39],s47 offen offset:1024 sc1 lds\nM v[220:223],a[36:39],v[180:183],v[220:223]\ns_add_u32 s58,s95,24\nds_read_b128 v[176:179],v20 offset:44032\nv_lshl_add_u32 v58,v30,3,v58\nM v[208:211],a[64:67],v[76:79],v[208:211]\n" \
"s_waitcnt lgkmcnt(3)\ns_and_b32 s65,s65,0xffff\nds_read_b128 v[180:183],v20 offset:36864\nv_and_b32 v30,0x30,v4\nM v[208:211],a[72:75],v[84:87],v[208:211]\ns_mov_b32 s66,4194304\nds_read_b64_tr_b16 a[196:197],v8 offset:16384\ns_mov_b32 exec_lo,0\n" \
"v_lshl_add_u32 v58,v30,4,s58\ns_mov_b32 exec_lo,-1\ns_nop 1\nM v[208:211],a[80:83],v[92:95],v[208:211]\ns_mov_b32 s67,0x00020000\nds_read_b64_tr_b16 a[202:203],v7 offset:18432\nv_xor_b32 v7,0x30000,v7\nM v[208:211],a[88:91],v[72:75],v[208:211]\n" \
"buffer_load_dwordx4 v2,s[36:39],s47 offen offset:2048 sc1 lds\nM v[212:215],a[92:95],v[72:75],v[212:215]\ns_mov_b32 s58,0x300003\nds_read_b128 v[72:75],v20 offset:37888\nv_and_b32 v30,16,v4\nM v[212:215],a[84:87],v[92:95],v[212:215]\ns_mov_b32 s59,0x30000300\nds_read_b128 v[92:95],v20 offset:38912\n" \
"v_mov_b32 v31,0x3f80\nM v[212:215],a[76:79],v[84:87],v[212:215]\ns_lshl_b32 s92,s92,6\nds_read_b128 v[84:87],v20 offset:39936\nv_lshlrev_b32 v30,v30,v31\nM v[212:215],a[68:71],v[76:79],v[212:215]\nbuffer_load_dwordx4 v2,s[36:39],s47 offen offset:3072 sc1 lds\nM v[216:219],a[64:67],v[80:83],v[216:219]\n" \
"s_waitcnt lgkmcnt(3)\ns_add_u32 m0,s22,49152\nv_cndmask_b32 v186,0,v30,s[58:59]\nv_mov_b32 v188,0\nM v[216:219],a[72:75],v[88:91],v[216:219]\ns_add_u32 s47,s31,24576\nds_read_b128 v[76:79],v20 offset:45056\nv_mov_b32 v189,0\n" \
"M v[216:219],a[80:83],v[60:63],v[216:219]\ns_mov_b32 s58,0xc0000c\nds_read_b64_tr_b16 a[198:199],v9 offset:16384\nds_read_b64_tr_b16 a[204:205],v8 offset:18432\nM v[216:219],a[88:91],v[176:179],v[216:219]\ns_mov_b32 s59,0xc0000c00\nds_read_b64_tr_b16 a[206:207],v9 offset:18432\nv_xor_b32 v8,0x30000,v8\n" \
"M v[220:223],a[92:95],v[176:179],v[220:223]\nbuffer_load_dwordx4 v2,s[36:39],s47 offen offset:0 sc1 lds\nM v[220:223],a[84:87],v[60:63],v[220:223]\ns_add_u32 s92,s92,71303168\nds_read_b128 v[176:179],v20 offset:46080\nv_xor_b32 v9,0x30000,v9\nM v[220:223],a[76:79],v[88:91],v[220:223]\ns_lshl_b32 s93,s93,6\n" \
"ds_read_b128 v[60:63],v20 offset:47104\nv_cndmask_b32 v187,0,v30,s[58:59]\nM v[220:223],a[68:71],v[80:83],v[220:223]\ns_add_u32 s58,s60,1\nds_read_b128 v[88:91],v20 offset:48128\nv_add_u32 v191,s58,v190\nM v[208:211],a[96:99],v[180:183],v[208:211]\ns_waitcnt lgkmcnt(4)\n" \
"buffer_load_dwordx4 v2,s[36:39],s47 offen offset:1024 sc1 lds\nM v[208:211],a[104:107],v[72:75],v[208:211]\ns_add_u32 s93,s93,71303168\nv_and_b32 v191,3,v191\nv_lshlrev_b32 v191,8,v191\nM v[208:211],a[112:115],v[92:95],v[208:211]\ns_lshl_b32 s58,s60,8\nds_read_b128 v[80:83],v20 offset:16384\n" \
"v_lshl_add_u32 v191,v190,3,v191\nM v[208:211],a[120:123],v[84:87],v[208:211]\ns_add_u32 s58,s58,s95\nds_read_b64_tr_b16 a[208:209],v6 offset:0\nv_add_u32 v190,1,v190\nM v[212:215],a[124:127],v[84:87],v[212:215]\ns_sub_u32 s49,s72,8192\nds_read_b128 v[84:87],v20 offset:17408\n" \
"v_lshrrev_b32 v190,2,v190\nM v[212:215],a[116:119],v[92:95],v[212:215]\nbuffer_load_dwordx4 v2,s[36:39],s47 offen offset:2048 sc1 lds\nM v[212:215],a[108:111],v[72:75],v[212:215]\ns_add_u32 s63,s49,0\nds_read_b128 v[92:95],v20 offset:18432\nv_lshl_add_u32 v191,v190,27,v191\nM v[212:215],a[100:103],v[180:183],v[212:215]\n" \
"s_add_u32 s59,s80,65536\nds_read_b128 v[72:75],v20 offset:19456\nv_bfe_u32 v190,v4,4,2\nM v[216:219],a[96:99],v[76:79],v[216:219]\ns_waitcnt lgkmcnt(4)\nds_read_b128 v[180:183],v20 offset:24576\nv_lshl_add_u32 v190,v190,3,s58\nM v[216:219],a[104:107],v[176:179],v[216:219]\n" \
"buffer_load_dwordx4 v2,s[36:39],s47 offen offset:3072 sc1 lds\nM v[216:219],a[112:115],v[60:63],v[216:219]\ns_add_u32 m0,s22,131072\nv_mov_b32 v184,0\nv_mov_b32 v185,0\nM v[216:219],a[120:123],v[88:91],v[216:219]\ns_add_u32 s47,s80,0\nds_read_b64_tr_b16 a[210:211],v7 offset:0\n" \
"ds_read_b64_tr_b16 a[212:213],v8 offset:0\nM v[220:223],a[124:127],v[88:91],v[220:223]\ns_add_u32 s58,s80,0\nds_read_b128 v[88:91],v20 offset:25600\nM v[220:223],a[116:119],v[60:63],v[220:223]\nds_read_b128 v[60:63],v20 offset:26624\nM v[220:223],a[108:111],v[176:179],v[220:223]\nds_read_b128 v[176:179],v20 offset:27648\n" \
"M v[220:223],a[100:103],v[76:79],v[220:223]\nds_read_b64_tr_b16 a[214:215],v9 offset:0\nM v[208:211],a[128:131],v[80:83],v[208:211]\ns_waitcnt lgkmcnt(6)\nds_read_b64_tr_b16 a[216:217],v6 offset:2048\nM v[208:211],a[136:139],v[84:87],v[208:211]\nds_read_b64_tr_b16 a[218:219],v7 offset:2048\nds_read_b64_tr_b16 a[220:221],v8 offset:2048\n" \
"M v[208:211],a[144:147],v[92:95],v[208:211]\nds_read_b64_tr_b16 a[222:223],v9 offset:2048\nM v[208:211],a[152:155],v[72:75],v[208:211]\nds_read_b128 v[76:79],v20 offset:20480\nM v[212:215],a[156:159],v[72:75],v[212:215]\nds_read_b128 v[72:75],v20 offset:21504\nM v[212:215],a[148:151],v[92:95],v[212:215]\nds_read_b128 v[92:95],v20 offset:22528\n" \
"M v[212:215],a[140:143],v[84:87],v[212:215]\nds_read_b128 v[84:87],v20 offset:23552\nM v[212:215],a[132:135],v[80:83],v[212:215]\nds_read_b128 v[80:83],v20 offset:28672\nM v[216:219],a[128:131],v[180:183],v[216:219]\ns_waitcnt lgkmcnt(5)\nM v[216:219],a[136:139],v[88:91],v[216:219]\nM v[216:219],a[144:147],v[60:63],v[216:219]\n" \
"M v[216:219],a[152:155],v[176:179],v[216:219]\nM v[220:223],a[156:159],v[176:179],v[220:223]\ns_waitcnt lgkmcnt(1)\nds_read_b128 v[176:179],v20 offset:29696\nM v[220:223],a[148:151],v[60:63],v[220:223]\ns_waitcnt vmcnt(8) lgkmcnt(6)\nds_read_b128 v[60:63],v20 offset:30720\nM v[220:223],a[140:143],v[88:91],v[220:223]\n" \
"s_barrier\nds_read_b128 v[88:91],v20 offset:31744\nM v[220:223],a[132:135],v[180:183],v[220:223]\nbuffer_load_dwordx4 v112,s[64:67],s47 offen offset:0 lds\nM v[208:211],a[160:163],v[76:79],v[208:211]\ns_add_u32 m0,s22,132096\nds_read_b128 v[180:183],v4 offset:0\nM v[208:211],a[168:171],v[72:75],v[208:211]\n" \
"s_add_u32 s47,s80,65536\nds_read_b64_tr_b16 a[224:225],v6 offset:49152\nM v[208:211],a[176:179],v[92:95],v[208:211]\nds_read_b64_tr_b16 a[226:227],v7 offset:49152\nds_read_b64_tr_b16 a[232:233],v6 offset:51200\nM v[208:211],a[184:187],v[84:87],v[208:211]\nbuffer_load_dwordx4 v112,s[64:67],s47 offen offset:0 lds\nM v[212:215],a[188:191],v[84:87],v[212:215]\n" \
"s_add_u32 m0,s22,133120\nM v[212:215],a[180:183],v[92:95],v[212:215]\ns_add_u32 s47,s81,0\nds_read_b128 v[84:87],v4 offset:1024\nM v[212:215],a[172:175],v[72:75],v[212:215]\ns_waitcnt lgkmcnt(4)\nds_read_b128 v[92:95],v4 offset:2048\nM v[212:215],a[164:167],v[76:79],v[212:215]\n" \
"buffer_load_dwordx4 v112,s[64:67],s47 offen offset:0 lds\nM v[216:219],a[160:163],v[80:83],v[216:219]\ns_add_u32 m0,s22,134144\nds_read_b128 v[72:75],v4 offset:3072\nM v[216:219],a[168:171],v[176:179],v[216:219]\ns_add_u32 s47,s81,65536\nds_read_b128 v[76:79],v4 offset:8192\nM v[216:219],a[176:179],v[60:63],v[216:219]\n" \
"ds_read_b64_tr_b16 a[234:235],v7 offset:51200\nM v[216:219],a[184:187],v[88:91],v[216:219]\nbuffer_load_dwordx4 v112,s[64:67],s47 offen offset:0 lds\nM v[220:223],a[188:191],v[88:91],v[220:223]\ns_add_u32 m0,s22,147456\nds_read_b128 v[88:91],v4 offset:9216\nM v[220:223],a[180:183],v[60:63],v[220:223]\ns_add_u32 s47,s32,0\n" \
"ds_read_b128 v[60:63],v4 offset:10240\nM v[220:223],a[172:175],v[176:179],v[220:223]\ns_waitcnt lgkmcnt(4)\nds_read_b128 v[176:179],v4 offset:11264\nM v[220:223],a[164:167],v[80:83],v[220:223]\nbuffer_load_dwordx4 v113,s[36:39],s82 offen offset:0 sc1 lds\nM v[48:51],a[192:195],v[180:183],v[48:51]\ns_add_u32 m0,s22,146432\n" \
"M v[48:51],a[200:203],v[84:87],v[48:51]\nds_read_b128 v[80:83],v4 offset:4096\nM v[48:51],a[208:211],v[92:95],v[48:51]\nds_read_b64_tr_b16 a[236:237],v8 offset:51200\nM v[48:51],a[216:219],v[72:75],v[48:51]\nbuffer_load_dwordx4 v113,s[36:39],s82 offen offset:2048 sc1 lds\nM v[52:55],a[220:223],v[72:75],v[52:55]\ns_add_u32 m0,s22,149504\n" \
"ds_read_b128 v[72:75],v4 offset:5120\nM v[52:55],a[212:215],v[92:95],v[52:55]\nds_read_b64_tr_b16 a[238:239],v9 offset:51200\nM v[52:55],a[204:207],v[84:87],v[52:55]\ns_waitcnt lgkmcnt(4)\nds_read_b64_tr_b16 a[228:229],v8 offset:49152\nM v[52:55],a[196:199],v[180:183],v[52:55]\nbuffer_load_dwordx2 v[10:11],v58,s[36:39],0 offen sc1\n" \
"M v[64:67],a[192:195],v[76:79],v[64:67]\nds_read_b128 v[92:95],v4 offset:6144\nds_read_b64_tr_b16 a[230:231],v9 offset:49152\nM v[64:67],a[200:203],v[88:91],v[64:67]\nbuffer_load_dwordx4 v113,s[36:39],s83 offen offset:0 sc1 lds\nM v[64:67],a[208:211],v[60:63],v[64:67]\ns_add_u32 m0,s22,148480\nM v[64:67],a[216:219],v[176:179],v[64:67]\n" \
"ds_read_b128 v[84:87],v4 offset:7168\nM v[68:71],a[220:223],v[176:179],v[68:71]\nds_read_b128 v[180:183],v4 offset:12288\nM v[68:71],a[212:215],v[60:63],v[68:71]\nbuffer_load_dwordx4 v113,s[36:39],s83 offen offset:2048 sc1 lds\nM v[68:71],a[204:207],v[88:91],v[68:71]\ns_add_u32 m0,s22,65536\nds_read_b128 v[176:179],v4 offset:13312\n" \
"M v[68:71],a[196:199],v[76:79],v[68:71]\ns_waitcnt lgkmcnt(3)\nds_read_b128 v[60:63],v4 offset:14336\nM v[48:51],a[224:227],v[80:83],v[48:51]\nds_read_b128 v[88:91],v4 offset:15360\nM v[48:51],a[232:235],v[72:75],v[48:51]\nbuffer_load_dword v16,v15,s[36:39],s92 offen offset:0 sc1\nM v[48:51],a[240:243],v[92:95],v[48:51]\n" \
"s_waitcnt lgkmcnt(3)\nds_read_b128 v[76:79],v4 offset:32768\nM v[48:51],a[248:251],v[84:87],v[48:51]\nbuffer_load_dword v17,v15,s[36:39],s92 offen offset:128 sc1\nM v[52:55],a[252:255],v[84:87],v[52:55]\nds_read_b128 v[84:87],v4 offset:33792\nM v[52:55],a[244:247],v[92:95],v[52:55]\nbuffer_load_dword v18,v15,s[36:39],s93 offen offset:0 sc1\n" \
"M v[52:55],a[236:239],v[72:75],v[52:55]\nds_read_b128 v[92:95],v4 offset:34816\nds_read_b128 v[72:75],v4 offset:35840\nM v[52:55],a[228:231],v[80:83],v[52:55]\nbuffer_load_dword v19,v15,s[36:39],s93 offen offset:128 sc1\nM v[64:67],a[224:227],v[180:183],v[64:67]\ns_waitcnt lgkmcnt(3)\nds_read_b128 v[80:83],v4 offset:40960\n" \
"C v48,v48,v49\nM v[64:67],a[232:235],v[176:179],v[64:67]\nC v49,v50,v51\nC v50,v52,v53\nM v[64:67],a[240:243],v[60:63],v[64:67]\nC v51,v54,v55\nv_cndmask_b32 v120,v120,v48,s[96:97]\nM v[64:67],a[248:251],v[88:91],v[64:67]\n" \
"v_cndmask_b32 v121,v121,v49,s[96:97]\nv_cndmask_b32 v122,v122,v50,s[96:97]\nM v[68:71],a[252:255],v[88:91],v[68:71]\nds_read_b128 v[88:91],v4 offset:41984\nv_cndmask_b32 v123,v123,v51,s[96:97]\nM v[68:71],a[244:247],v[60:63],v[68:71]\nds_read_b128 v[60:63],v4 offset:43008\nC v64,v64,v65\n" \
"M v[68:71],a[236:239],v[176:179],v[68:71]\nds_read_b128 v[176:179],v4 offset:44032\nC v65,v66,v67\nM v[68:71],a[228:231],v[180:183],v[68:71]\nds_read_b128 v[180:183],v4 offset:36864\nv_cndmask_b32 v124,v124,v64,s[96:97]\nM v[32:35],a[192:195],v[76:79],v[32:35]\ns_waitcnt lgkmcnt(4)\n" \
"v_cndmask_b32 v125,v125,v65,s[96:97]\nM v[32:35],a[200:203],v[84:87],v[32:35]\nC v66,v68,v69\nC v67,v70,v71\nM v[32:35],a[208:211],v[92:95],v[32:35]\nv_cndmask_b32 v126,v126,v66,s[96:97]\nv_cndmask_b32 v127,v127,v67,s[96:97]\nM v[32:35],a[216:219],v[72:75],v[32:35]\n" \
"s_cmp_eq_u32 s60,1\ns_cselect_b64 s[96:97],-1,0\nM v[36:39],a[220:223],v[72:75],v[36:39]\nds_read_b128 v[72:75],v4 offset:37888\nM v[36:39],a[212:215],v[92:95],v[36:39]\nds_read_b128 v[92:95],v4 offset:38912\nM v[36:39],a[204:207],v[84:87],v[36:39]\nds_read_b128 v[84:87],v4 offset:39936\n" \
"M v[36:39],a[196:199],v[76:79],v[36:39]\nds_read_b128 v[76:79],v4 offset:45056\nM v[40:43],a[192:195],v[80:83],v[40:43]\ns_waitcnt lgkmcnt(4)\nM v[40:43],a[200:203],v[88:91],v[40:43]\nM v[40:43],a[208:211],v[60:63],v[40:43]\nM v[40:43],a[216:219],v[176:179],v[40:43]\nM v[44:47],a[220:223],v[176:179],v[44:47]\n" \
"ds_read_b128 v[176:179],v4 offset:46080\nM v[44:47],a[212:215],v[60:63],v[44:47]\ns_waitcnt vmcnt(13) lgkmcnt(14)\nds_read_b128 v[60:63],v4 offset:47104\nM v[44:47],a[204:207],v[88:91],v[44:47]\ns_barrier\nds_read_b128 v[88:91],v4 offset:48128\nM v[44:47],a[196:199],v[80:83],v[44:47]\n" \
"ds_read_b128 v[80:83],v4 offset:16384\nM v[32:35],a[224:227],v[180:183],v[32:35]\ns_waitcnt lgkmcnt(4)\nM v[32:35],a[232:235],v[72:75],v[32:35]\nM v[32:35],a[240:243],v[92:95],v[32:35]\nM v[32:35],a[248:251],v[84:87],v[32:35]\nM v[36:39],a[252:255],v[84:87],v[36:39]\ns_waitcnt vmcnt(6)\n" \
"v_cmp_gt_u64 s[98:99],s[14:15],v[10:11]\nv_cmp_le_u64 s[18:19],s[16:17],v[10:11]\nM v[36:39],a[244:247],v[92:95],v[36:39]\ns_or_b64 s[98:99],s[98:99],s[18:19]\ns_cbranch_scc1 LPL_%=\nLPLR_%=:\nbuffer_load_dwordx4 v2,s[36:39],s47 offen offset:0 sc1 lds\nM v[36:39],a[236:239],v[72:75],v[36:39]\n" \
"ds_read_b128 v[84:87],v4 offset:17408\nC v32,v32,v33\nM v[36:39],a[228:231],v[180:183],v[36:39]\nds_read_b128 v[92:95],v4 offset:18432\nC v33,v34,v35\nM v[40:43],a[224:227],v[76:79],v[40:43]\ns_waitcnt lgkmcnt(2)\nds_read_b128 v[72:75],v4 offset:19456\n" \
"v_cndmask_b32 v120,v120,v32,s[96:97]\nM v[40:43],a[232:235],v[176:179],v[40:43]\nbuffer_load_dwordx4 v2,s[36:39],s47 offen offset:1024 sc1 lds\nM v[40:43],a[240:243],v[60:63],v[40:43]\nds_read_b128 v[180:183],v4 offset:24576\nC v34,v36,v37\nM v[40:43],a[248:251],v[88:91],v[40:43]\nC v35,v38,v39\n" \
"v_cndmask_b32 v121,v121,v33,s[96:97]\nM v[44:47],a[252:255],v[88:91],v[44:47]\nds_read_b128 v[88:91],v4 offset:25600\nv_cndmask_b32 v122,v122,v34,s[96:97]\nM v[44:47],a[244:247],v[60:63],v[44:47]\nbuffer_load_dwordx4 v2,s[36:39],s47 offen offset:2048 sc1 lds\nM v[44:47],a[236:239],v[176:179],v[44:47]\nds_read_b128 v[60:63],v4 offset:26624\n" \
"C v40,v40,v41\nM v[44:47],a[228:231],v[76:79],v[44:47]\nds_read_b128 v[176:179],v4 offset:27648\nC v41,v42,v43\nM v[192:195],a[192:195],v[80:83],v[192:195]\ns_waitcnt lgkmcnt(3)\nds_read_b128 v[76:79],v4 offset:20480\nv_cndmask_b32 v123,v123,v35,s[96:97]\n" \
"M v[192:195],a[200:203],v[84:87],v[192:195]\nbuffer_load_dwordx4 v2,s[36:39],s47 offen offset:3072 sc1 lds\nM v[192:195],a[208:211],v[92:95],v[192:195]\ns_add_u32 m0,s22,98304\ns_add_u32 s47,s32,8192\nC v42,v44,v45\nC v43,v46,v47\nM v[192:195],a[216:219],v[72:75],v[192:195]\n" \
"v_cndmask_b32 v124,v124,v40,s[96:97]\nv_cndmask_b32 v125,v125,v41,s[96:97]\nM v[196:199],a[220:223],v[72:75],v[196:199]\nds_read_b128 v[72:75],v4 offset:21504\nv_cndmask_b32 v126,v126,v42,s[96:97]\nM v[196:199],a[212:215],v[92:95],v[196:199]\nbuffer_load_dwordx4 v2,s[36:39],s47 offen offset:0 sc1 lds\nM v[196:199],a[204:207],v[84:87],v[196:199]\n" \
"ds_read_b128 v[92:95],v4 offset:22528\nv_cndmask_b32 v127,v127,v43,s[96:97]\nM v[196:199],a[196:199],v[80:83],v[196:199]\ns_cmp_eq_u32 s60,2\ns_cselect_b64 s[96:97],-1,0\nds_read_b128 v[84:87],v4 offset:23552\nM v[200:203],a[192:195],v[180:183],v[200:203]\ns_waitcnt lgkmcnt(3)\n" \
"ds_read_b128 v[80:83],v4 offset:28672\nM v[200:203],a[200:203],v[88:91],v[200:203]\nLG23_%=:\ns_cbranch_execz LG25_%=\nbuffer_load_dwordx4 v2,s[36:39],s47 offen offset:1024 sc1 lds\nM v[200:203],a[208:211],v[60:63],v[200:203]\nM v[200:203],a[216:219],v[176:179],v[200:203]\nM v[204:207],a[220:223],v[176:179],v[204:207]\n" \
"ds_read_b128 v[176:179],v4 offset:29696\nM v[204:207],a[212:215],v[60:63],v[204:207]\nbuffer_load_dwordx4 v2,s[36:39],s47 offen offset:2048 sc1 lds\nM v[204:207],a[204:207],v[88:91],v[204:207]\nds_read_b128 v[60:63],v4 offset:30720\nM v[204:207],a[196:199],v[180:183],v[204:207]\nds_read_b128 v[88:91],v4 offset:31744\nM v[192:195],a[224:227],v[76:79],v[192:195]\n" \
"s_waitcnt lgkmcnt(3)\nds_read_b128 v[180:183],v4 offset:49152\nM v[192:195],a[232:235],v[72:75],v[192:195]\nbuffer_load_dwordx4 v2,s[36:39],s47 offen offset:3072 sc1 lds\nM v[192:195],a[240:243],v[92:95],v[192:195]\ns_add_u32 m0,s22,81920\nM v[192:195],a[248:251],v[84:87],v[192:195]\ns_add_u32 s47,s32,16384\n" \
"M v[196:199],a[252:255],v[84:87],v[196:199]\ns_waitcnt vmcnt(8)\nds_read_b128 v[84:87],v4 offset:50176\nv_sub_f32 v24,1.0,v16\nM v[196:199],a[244:247],v[92:95],v[196:199]\nbuffer_store_dwordx4 v[48:51],v4,s[36:39],s40 offen offset:0 sc1\nM v[196:199],a[236:239],v[72:75],v[196:199]\nds_read_b128 v[92:95],v4 offset:51200\n" \
"ds_read_b128 v[72:75],v4 offset:52224\nM v[196:199],a[228:231],v[76:79],v[196:199]\nds_read_b128 v[76:79],v4 offset:57344\nC v192,v192,v193\nM v[200:203],a[224:227],v[80:83],v[200:203]\ns_waitcnt lgkmcnt(4)\nC v193,v194,v195\nv_cndmask_b32 v120,v120,v192,s[96:97]\n" \
"M v[200:203],a[232:235],v[176:179],v[200:203]\nbuffer_load_dwordx4 v2,s[36:39],s47 offen offset:0 sc1 lds\nM v[200:203],a[240:243],v[60:63],v[200:203]\nC v194,v196,v197\nC v195,v198,v199\nM v[200:203],a[248:251],v[88:91],v[200:203]\nv_cndmask_b32 v121,v121,v193,s[96:97]\nv_cndmask_b32 v122,v122,v194,s[96:97]\n" \
"M v[204:207],a[252:255],v[88:91],v[204:207]\nds_read_b128 v[88:91],v4 offset:58368\nv_cndmask_b32 v123,v123,v195,s[96:97]\nM v[204:207],a[244:247],v[60:63],v[204:207]\nbuffer_load_dwordx4 v2,s[36:39],s47 offen offset:1024 sc1 lds\nM v[204:207],a[236:239],v[176:179],v[204:207]\nds_read_b128 v[60:63],v4 offset:59392\nC v200,v200,v201\n" \
"M v[204:207],a[228:231],v[80:83],v[204:207]\nds_read_b128 v[176:179],v4 offset:60416\nC v201,v202,v203\nM v[208:211],a[192:195],v[180:183],v[208:211]\ns_waitcnt lgkmcnt(3)\nds_read_b128 v[80:83],v4 offset:53248\nv_cndmask_b32 v124,v124,v200,s[96:97]\nM v[208:211],a[200:203],v[84:87],v[208:211]\n" \
"buffer_load_dwordx4 v2,s[36:39],s47 offen offset:2048 sc1 lds\nM v[208:211],a[208:211],v[92:95],v[208:211]\nC v202,v204,v205\nC v203,v206,v207\nM v[208:211],a[216:219],v[72:75],v[208:211]\nv_cndmask_b32 v125,v125,v201,s[96:97]\nv_cndmask_b32 v126,v126,v202,s[96:97]\nM v[212:215],a[220:223],v[72:75],v[212:215]\n" \
"ds_read_b128 v[72:75],v4 offset:54272\nv_cndmask_b32 v127,v127,v203,s[96:97]\nM v[212:215],a[212:215],v[92:95],v[212:215]\nbuffer_store_dwordx4 v[64:67],v4,s[36:39],s40 offen offset:1024 sc1\nM v[212:215],a[204:207],v[84:87],v[212:215]\ns_cmp_eq_u32 s60,3\ns_cselect_b64 s[96:97],-1,0\nds_read_b128 v[92:95],v4 offset:55296\n" \
"ds_read_b128 v[84:87],v4 offset:56320\nM v[212:215],a[196:199],v[180:183],v[212:215]\nds_read_b128 v[180:183],v4 offset:61440\nC v24,v24,v24\nM v[216:219],a[192:195],v[76:79],v[216:219]\ns_waitcnt lgkmcnt(4)\nv_and_b32 v24,0xffff0000,v24\nv_sub_f32 v25,1.0,v17\n" \
"M v[216:219],a[200:203],v[88:91],v[216:219]\nbuffer_load_dwordx4 v2,s[36:39],s47 offen offset:3072 sc1 lds\nM v[216:219],a[208:211],v[60:63],v[216:219]\ns_add_u32 m0,s22,114688\ns_add_u32 s47,s32,24576\nC v25,v25,v25\nv_and_b32 v25,0xffff0000,v25\nM v[216:219],a[216:219],v[176:179],v[216:219]\n" \
"v_sub_f32 v26,1.0,v18\nv_sub_f32 v27,1.0,v19\nM v[220:223],a[220:223],v[176:179],v[220:223]\nds_read_b128 v[176:179],v4 offset:62464\nC v26,v26,v26\nM v[220:223],a[212:215],v[60:63],v[220:223]\ns_waitcnt vmcnt(6) lgkmcnt(14)\nbuffer_load_dwordx4 v2,s[36:39],s47 offen offset:0 sc1 lds\n" \
"M v[220:223],a[204:207],v[88:91],v[220:223]\ns_barrier\nds_read_b128 v[60:63],v4 offset:63488\nM v[220:223],a[196:199],v[76:79],v[220:223]\nds_read_b128 v[88:91],v4 offset:64512\nC v27,v27,v27\nM v[208:211],a[224:227],v[80:83],v[208:211]\ns_waitcnt lgkmcnt(3)\n" \
"ds_read_b128 v[76:79],v20 offset:0\nv_and_b32 v26,0xffff0000,v26\nM v[208:211],a[232:235],v[72:75],v[208:211]\nbuffer_load_dwordx4 v2,s[36:39],s47 offen offset:1024 sc1 lds\nM v[208:211],a[240:243],v[92:95],v[208:211]\nds_read_b64 v[160:161],v114 offset:0\nv_and_b32 v27,0xffff0000,v27\nM v[208:211],a[248:251],v[84:87],v[208:211]\n" \
"ds_read_b64_tr_b16 v[96:97],v115 offset:16384\nM v[212:215],a[252:255],v[84:87],v[212:215]\nds_read_b128 v[84:87],v20 offset:1024\nM v[212:215],a[244:247],v[92:95],v[212:215]\nbuffer_load_dwordx4 v2,s[36:39],s47 offen offset:2048 sc1 lds\nM v[212:215],a[236:239],v[72:75],v[212:215]\nds_read_b128 v[92:95],v20 offset:2048\nC v208,v208,v209\n" \
"M v[212:215],a[228:231],v[80:83],v[212:215]\nds_read_b128 v[72:75],v20 offset:3072\nC v209,v210,v211\nM v[216:219],a[224:227],v[180:183],v[216:219]\ns_waitcnt lgkmcnt(4)\nds_read_b128 v[80:83],v20 offset:8192\nv_cndmask_b32 v120,v120,v208,s[96:97]\nM v[216:219],a[232:235],v[176:179],v[216:219]\n" \
"buffer_store_dwordx4 v[32:35],v4,s[36:39],s41 offen offset:0 sc1\nM v[216:219],a[240:243],v[60:63],v[216:219]\nds_read_b64 v[162:163],v114 offset:32\nds_read_b64_tr_b16 v[98:99],v115 offset:17408\nM v[216:219],a[248:251],v[88:91],v[216:219]\nds_read_b64 v[164:165],v114 offset:1024\nC v210,v212,v213\nM v[220:223],a[252:255],v[88:91],v[220:223]\n" \
"ds_read_b128 v[88:91],v20 offset:9216\nC v211,v214,v215\nM v[220:223],a[244:247],v[60:63],v[220:223]\nbuffer_load_dwordx4 v2,s[36:39],s47 offen offset:3072 sc1 lds\nM v[220:223],a[236:239],v[176:179],v[220:223]\ns_add_u32 m0,s22,0\nds_read_b128 v[60:63],v20 offset:10240\nC v216,v216,v217\n" \
"M v[220:223],a[228:231],v[180:183],v[220:223]\ns_add_u32 s47,s33,0\nds_read_b128 v[176:179],v20 offset:11264\nC v217,v218,v219\nM v[224:227],a[0:3],v[76:79],0\ns_waitcnt lgkmcnt(4)\nds_read_b128 v[180:183],v20 offset:4096\nv_lshlrev_b32 v128,16,v160\n" \
"M v[224:227],a[8:11],v[84:87],v[224:227]\nbuffer_load_dwordx4 v2,s[36:39],s47 offen offset:0 sc1 lds\nM v[224:227],a[16:19],v[92:95],v[224:227]\nC v218,v220,v221\nC v219,v222,v223\nM v[224:227],a[24:27],v[72:75],v[224:227]\nds_read_b64_tr_b16 v[100:101],v115 offset:16416\nds_read_b64 v[166:167],v114 offset:1056\n" \
"M v[228:231],a[28:31],v[72:75],0\nds_read_b128 v[72:75],v20 offset:5120\nv_and_b32 v129,0xffff0000,v160\nM v[228:231],a[20:23],v[92:95],v[228:231]\nbuffer_store_dwordx4 v[40:43],v4,s[36:39],s41 offen offset:1024 sc1\nM v[228:231],a[12:15],v[84:87],v[228:231]\nds_read_b128 v[92:95],v20 offset:6144\nds_read_b128 v[84:87],v20 offset:7168\n" \
"M v[228:231],a[4:7],v[76:79],v[228:231]\nds_read_b128 v[76:79],v20 offset:12288\nv_lshlrev_b32 v130,16,v161\nM v[232:235],a[0:3],v[80:83],0\ns_waitcnt lgkmcnt(6)\nds_read_b64_tr_b16 v[102:103],v115 offset:17440\nv_and_b32 v131,0xffff0000,v161\nM v[232:235],a[8:11],v[88:91],v[232:235]\n" \
"buffer_load_dwordx4 v2,s[36:39],s47 offen offset:1024 sc1 lds\nM v[232:235],a[16:19],v[60:63],v[232:235]\nv_lshlrev_b32 v132,16,v162\nv_and_b32 v133,0xffff0000,v162\nM v[232:235],a[24:27],v[176:179],v[232:235]\nds_read_b64 v[168:169],v114 offset:2048\nds_read_b64_tr_b16 v[104:105],v115 offset:18432\nM v[236:239],a[28:31],v[176:179],0\n" \
"ds_read_b128 v[176:179],v20 offset:13312\nv_lshlrev_b32 v134,16,v163\nM v[236:239],a[20:23],v[60:63],v[236:239]\nbuffer_load_dwordx4 v2,s[36:39],s47 offen offset:2048 sc1 lds\nM v[236:239],a[12:15],v[88:91],v[236:239]\nv_and_b32 v135,0xffff0000,v163\nv_lshlrev_b32 v136,16,v164\nM v[236:239],a[4:7],v[80:83],v[236:239]\n" \
"ds_read_b128 v[60:63],v20 offset:14336\nv_and_b32 v137,0xffff0000,v164\nM v[224:227],a[32:35],v[180:183],v[224:227]\ns_waitcnt lgkmcnt(4)\nds_read_b128 v[88:91],v20 offset:15360\nv_lshlrev_b32 v138,16,v165\nM v[224:227],a[40:43],v[72:75],v[224:227]\nbuffer_load_dwordx4 v2,s[36:39],s47 offen offset:3072 sc1 lds\n" \
"M v[224:227],a[48:51],v[92:95],v[224:227]\ns_add_u32 m0,s22,32768\nds_read_b128 v[80:83],v20 offset:32768\nv_and_b32 v139,0xffff0000,v165\nM v[224:227],a[56:59],v[84:87],v[224:227]\ns_add_u32 s47,s33,8192\nds_read_b64 v[170:171],v114 offset:2080\nv_lshlrev_b32 v140,16,v166\n" \
"M v[228:231],a[60:63],v[84:87],v[228:231]\nds_read_b128 v[84:87],v20 offset:33792\nv_and_b32 v141,0xffff0000,v166\nM v[228:231],a[52:55],v[92:95],v[228:231]\nbuffer_load_dwordx4 v2,s[36:39],s47 offen offset:0 sc1 lds\nM v[228:231],a[44:47],v[72:75],v[228:231]\nds_read_b128 v[92:95],v20 offset:34816\nv_lshlrev_b32 v142,16,v167\n" \
"M v[228:231],a[36:39],v[180:183],v[228:231]\nds_read_b128 v[72:75],v20 offset:35840\nv_lshlrev_b32 v160,16,v96\nM v[232:235],a[32:35],v[76:79],v[232:235]\ns_waitcnt lgkmcnt(4)\nds_read_b128 v[180:183],v20 offset:40960\nv_and_b32 v143,0xffff0000,v167\nM v[232:235],a[40:43],v[176:179],v[232:235]\n" \
"buffer_store_dwordx4 v[192:195],v4,s[36:39],s42 offen offset:0 sc1\nM v[232:235],a[48:51],v[60:63],v[232:235]\nds_read_b64_tr_b16 v[106:107],v115 offset:19456\nds_read_b64 v[172:173],v114 offset:3072\nM v[232:235],a[56:59],v[88:91],v[232:235]\nds_read_b64_tr_b16 v[108:109],v115 offset:18464\nv_lshlrev_b32 v161,16,v97\nM v[236:239],a[60:63],v[88:91],v[236:239]\n" \
"ds_read_b128 v[88:91],v20 offset:41984\nv_lshlrev_b32 v162,16,v98\nM v[236:239],a[52:55],v[60:63],v[236:239]\nbuffer_load_dwordx4 v2,s[36:39],s47 offen offset:1024 sc1 lds\nM v[236:239],a[44:47],v[176:179],v[236:239]\nds_read_b128 v[60:63],v20 offset:43008\nv_lshlrev_b32 v163,16,v99\nM v[236:239],a[36:39],v[76:79],v[236:239]\n" \
"ds_read_b128 v[176:179],v20 offset:44032\nv_lshlrev_b32 v164,16,v100\nM v[224:227],a[64:67],v[80:83],v[224:227]\ns_waitcnt lgkmcnt(4)\nds_read_b64 v[174:175],v114 offset:3104\nv_lshlrev_b32 v165,16,v101\nM v[224:227],a[72:75],v[84:87],v[224:227]\nds_read_b64_tr_b16 v[110:111],v115 offset:19488\n" \
"v_lshlrev_b32 v166,16,v102\nM v[224:227],a[80:83],v[92:95],v[224:227]\nbuffer_load_dwordx4 v2,s[36:39],s47 offen offset:2048 sc1 lds\nM v[224:227],a[88:91],v[72:75],v[224:227]\nds_read_b128 v[76:79],v20 offset:36864\nv_lshlrev_b32 v167,16,v103\nM v[228:231],a[92:95],v[72:75],v[228:231]\nds_read_b128 v[72:75],v20 offset:37888\n" \
"v_lshlrev_b32 v144,16,v168\nM v[228:231],a[84:87],v[92:95],v[228:231]\ns_waitcnt lgkmcnt(4)\nds_read_b128 v[92:95],v20 offset:38912\nv_and_b32 v96,0xffff0000,v96\nM v[228:231],a[76:79],v[84:87],v[228:231]\nbuffer_load_dwordx4 v2,s[36:39],s47 offen offset:3072 sc1 lds\nM v[228:231],a[68:71],v[80:83],v[228:231]\n" \
"s_add_u32 m0,s22,16384\nv_and_b32 v145,0xffff0000,v168\nv_and_b32 v97,0xffff0000,v97\nM v[232:235],a[64:67],v[180:183],v[232:235]\ns_add_u32 s47,s33,16384\nds_read_b128 v[84:87],v20 offset:39936\nv_and_b32 v98,0xffff0000,v98\nM v[232:235],a[72:75],v[88:91],v[232:235]\n" \
"ds_read_b128 v[80:83],v20 offset:45056\nv_and_b32 v99,0xffff0000,v99\nM v[232:235],a[80:83],v[60:63],v[232:235]\nbuffer_store_dwordx4 v[200:203],v4,s[36:39],s42 offen offset:1024 sc1\nM v[232:235],a[88:91],v[176:179],v[232:235]\nv_lshlrev_b32 v146,16,v169\nv_and_b32 v147,0xffff0000,v169\nM v[236:239],a[92:95],v[176:179],v[236:239]\n" \
"ds_read_b128 v[176:179],v20 offset:46080\nv_lshlrev_b32 v148,16,v170\nM v[236:239],a[84:87],v[60:63],v[236:239]\ns_waitcnt vmcnt(11) lgkmcnt(6)\nds_read_b128 v[60:63],v20 offset:47104\nv_and_b32 v149,0xffff0000,v170\nM v[236:239],a[76:79],v[88:91],v[236:239]\ns_barrier\n" \
"ds_read_b128 v[88:91],v20 offset:48128\nM v[236:239],a[68:71],v[180:183],v[236:239]\ns_waitcnt lgkmcnt(3)\nds_read_b128 v[180:183],v20 offset:16384\nv_lshlrev_b32 v150,16,v171\nM v[224:227],a[96:99],v[76:79],v[224:227]\nv_and_b32 v100,0xffff0000,v100\nv_and_b32 v151,0xffff0000,v171\n" \
"M v[224:227],a[104:107],v[72:75],v[224:227]\nv_and_b32 v101,0xffff0000,v101\nv_and_b32 v102,0xffff0000,v102\nM v[224:227],a[112:115],v[92:95],v[224:227]\nv_and_b32 v103,0xffff0000,v103\nv_lshlrev_b32 v168,16,v104\nM v[224:227],a[120:123],v[84:87],v[224:227]\nv_mul_f32 v129,v129,v96\n" \
"v_mul_f32 v131,v131,v97\nM v[228:231],a[124:127],v[84:87],v[228:231]\nds_read_b128 v[84:87],v20 offset:17408\nv_mul_f32 v133,v133,v98\nM v[228:231],a[116:119],v[92:95],v[228:231]\nds_read_b128 v[92:95],v20 offset:18432\nv_mul_f32 v135,v135,v99\nM v[228:231],a[108:111],v[72:75],v[228:231]\n" \
"ds_read_b128 v[72:75],v20 offset:19456\nv_lshlrev_b32 v169,16,v105\nM v[228:231],a[100:103],v[76:79],v[228:231]\nbuffer_load_dwordx4 v2,s[36:39],s47 offen offset:0 sc1 lds\nM v[232:235],a[96:99],v[80:83],v[232:235]\ns_waitcnt lgkmcnt(3)\nds_read_b128 v[76:79],v20 offset:24576\nv_lshlrev_b32 v170,16,v106\n" \
"M v[232:235],a[104:107],v[176:179],v[232:235]\nv_lshlrev_b32 v171,16,v107\nv_lshlrev_b32 v152,16,v172\nM v[232:235],a[112:115],v[60:63],v[232:235]\nv_and_b32 v153,0xffff0000,v172\nv_lshlrev_b32 v154,16,v173\nM v[232:235],a[120:123],v[88:91],v[232:235]\nv_and_b32 v155,0xffff0000,v173\n" \
"v_lshlrev_b32 v156,16,v174\nM v[236:239],a[124:127],v[88:91],v[236:239]\nbuffer_store_dwordx4 v[208:211],v4,s[36:39],s43 offen offset:0 sc1\nM v[236:239],a[116:119],v[60:63],v[236:239]\nds_read_b128 v[88:91],v20 offset:25600\nds_read_b128 v[60:63],v20 offset:26624\nM v[236:239],a[108:111],v[176:179],v[236:239]\nds_read_b128 v[176:179],v20 offset:27648\n" \
"v_mul_f32 v137,v137,v100\nM v[236:239],a[100:103],v[80:83],v[236:239]\nds_read_b128 v[80:83],v20 offset:20480\nv_and_b32 v157,0xffff0000,v174\nM v[224:227],a[128:131],v[180:183],v[224:227]\ns_waitcnt lgkmcnt(4)\nv_mul_f32 v139,v139,v101\nv_mul_f32 v141,v141,v102\n" \
"M v[224:227],a[136:139],v[84:87],v[224:227]\nv_mul_f32 v143,v143,v103\nv_and_b32 v104,0xffff0000,v104\nM v[224:227],a[144:147],v[92:95],v[224:227]\nv_and_b32 v105,0xffff0000,v105\nv_and_b32 v106,0xffff0000,v106\nM v[224:227],a[152:155],v[72:75],v[224:227]\nv_and_b32 v107,0xffff0000,v107\n" \
"v_lshlrev_b32 v172,16,v108\nM v[228:231],a[156:159],v[72:75],v[228:231]\nds_read_b128 v[72:75],v20 offset:21504\nv_lshlrev_b32 v158,16,v175\nM v[228:231],a[148:151],v[92:95],v[228:231]\nds_read_b128 v[92:95],v20 offset:22528\nv_and_b32 v159,0xffff0000,v175\nM v[228:231],a[140:143],v[84:87],v[228:231]\n" \
"ds_read_b128 v[84:87],v20 offset:23552\nv_lshlrev_b32 v173,16,v109\nM v[228:231],a[132:135],v[180:183],v[228:231]\nds_read_b128 v[180:183],v20 offset:28672\nv_lshlrev_b32 v174,16,v110\nM v[232:235],a[128:131],v[76:79],v[232:235]\ns_waitcnt lgkmcnt(4)\nv_mul_f32 v145,v145,v104\n" \
"v_mul_f32 v147,v147,v105\nM v[232:235],a[136:139],v[88:91],v[232:235]\nbuffer_load_dwordx4 v2,s[36:39],s47 offen offset:1024 sc1 lds\nM v[232:235],a[144:147],v[60:63],v[232:235]\nv_mul_f32 v149,v149,v106\nv_mul_f32 v151,v151,v107\nM v[232:235],a[152:155],v[176:179],v[232:235]\nv_and_b32 v108,0xffff0000,v108\n" \
"v_lshlrev_b32 v175,16,v111\nM v[236:239],a[156:159],v[176:179],v[236:239]\nds_read_b128 v[176:179],v20 offset:29696\nv_and_b32 v109,0xffff0000,v109\nM v[236:239],a[148:151],v[60:63],v[236:239]\nbuffer_load_dwordx4 v2,s[36:39],s47 offen offset:2048 sc1 lds\nM v[236:239],a[140:143],v[88:91],v[236:239]\nds_read_b128 v[60:63],v20 offset:30720\n" \
"v_and_b32 v110,0xffff0000,v110\nM v[236:239],a[132:135],v[76:79],v[236:239]\nds_read_b128 v[88:91],v20 offset:31744\nv_and_b32 v111,0xffff0000,v111\nM v[224:227],a[160:163],v[80:83],v[224:227]\ns_waitcnt lgkmcnt(3)\nds_read_b128 v[76:79],v20 offset:49152\nv_mul_f32 v153,v153,v108\n" \
"M v[224:227],a[168:171],v[72:75],v[224:227]\nbuffer_store_dwordx4 v[216:219],v4,s[36:39],s43 offen offset:1024 sc1\nM v[224:227],a[176:179],v[92:95],v[224:227]\nv_mul_f32 v155,v155,v109\nv_mul_f32 v157,v157,v110\nM v[224:227],a[184:187],v[84:87],v[224:227]\nv_mul_f32 v159,v159,v111\nv_cndmask_b32 v121,v121,v209,s[96:97]\n" \
"M v[228:231],a[188:191],v[84:87],v[228:231]\nds_read_b128 v[84:87],v20 offset:50176\nv_cndmask_b32 v122,v122,v210,s[96:97]\nM v[228:231],a[180:183],v[92:95],v[228:231]\nds_read_b128 v[92:95],v20 offset:51200\nv_cndmask_b32 v123,v123,v211,s[96:97]\nM v[228:231],a[172:175],v[72:75],v[228:231]\nds_read_b128 v[72:75],v20 offset:52224\n" \
"v_cndmask_b32 v124,v124,v216,s[96:97]\nM v[228:231],a[164:167],v[80:83],v[228:231]\nds_read_b128 v[80:83],v20 offset:57344\nv_cndmask_b32 v125,v125,v217,s[96:97]\nM v[232:235],a[160:163],v[180:183],v[232:235]\ns_waitcnt lgkmcnt(4)\nv_cndmask_b32 v126,v126,v218,s[96:97]\nv_cndmask_b32 v127,v127,v219,s[96:97]\n" \
"M v[232:235],a[168:171],v[176:179],v[232:235]\nv_mul_f32 v128,v128,v160\nv_mul_f32 v130,v130,v161\nM v[232:235],a[176:179],v[60:63],v[232:235]\nC v129,v128,v129\nv_mul_f32 v132,v132,v162\nM v[232:235],a[184:187],v[88:91],v[232:235]\nC v131,v130,v131\n" \
"v_mul_f32 v134,v134,v163\nM v[236:239],a[188:191],v[88:91],v[236:239]\nbuffer_load_dwordx4 v2,s[36:39],s47 offen offset:3072 sc1 lds\nM v[236:239],a[180:183],v[60:63],v[236:239]\ns_add_u32 m0,s22,49152\nds_read_b128 v[88:91],v20 offset:58368\nv_lshlrev_b32 v128,16,v129\nM v[236:239],a[172:175],v[176:179],v[236:239]\n" \
"s_add_u32 s47,s33,24576\nds_read_b128 v[60:63],v20 offset:59392\nC v133,v132,v133\nM v[236:239],a[164:167],v[180:183],v[236:239]\nds_read_b128 v[176:179],v20 offset:60416\nC v135,v134,v135\nM v[224:227],a[192:195],v[76:79],v[224:227]\ns_waitcnt lgkmcnt(3)\n" \
"ds_read_b128 v[180:183],v20 offset:53248\nv_lshlrev_b32 v130,16,v131\nM v[224:227],a[200:203],v[84:87],v[224:227]\nbuffer_load_dwordx4 v2,s[36:39],s47 offen offset:0 sc1 lds\nM v[224:227],a[208:211],v[92:95],v[224:227]\nv_lshlrev_b32 v132,16,v133\nv_and_b32 v129,0xffff0000,v129\nM v[224:227],a[216:219],v[72:75],v[224:227]\n" \
"v_lshlrev_b32 v134,16,v135\nv_and_b32 v133,0xffff0000,v133\nM v[228:231],a[220:223],v[72:75],v[228:231]\nds_read_b128 v[72:75],v20 offset:54272\nv_and_b32 v131,0xffff0000,v131\nM v[228:231],a[212:215],v[92:95],v[228:231]\nbuffer_load_dwordx4 v2,s[36:39],s47 offen offset:1024 sc1 lds\nM v[228:231],a[204:207],v[84:87],v[228:231]\n" \
"ds_read_b128 v[92:95],v20 offset:55296\nv_and_b32 v135,0xffff0000,v135\nM v[228:231],a[196:199],v[76:79],v[228:231]\nds_read_b128 v[84:87],v20 offset:56320\nv_mul_f32 v128,v16,v128\nM v[232:235],a[192:195],v[80:83],v[232:235]\ns_waitcnt lgkmcnt(3)\nds_read_b128 v[76:79],v20 offset:61440\n" \
"v_mul_f32 v129,v16,v129\nM v[232:235],a[200:203],v[88:91],v[232:235]\nv_mul_f32 v132,v16,v132\nv_mul_f32 v133,v16,v133\nM v[232:235],a[208:211],v[60:63],v[232:235]\nbuffer_load_dwordx4 v2,s[36:39],s47 offen offset:2048 sc1 lds\nM v[232:235],a[216:219],v[176:179],v[232:235]\nv_mul_f32 v130,v16,v130\n" \
"v_mul_f32 v131,v16,v131\nM v[236:239],a[220:223],v[176:179],v[236:239]\ns_waitcnt lgkmcnt(2)\nds_read_b128 v[176:179],v20 offset:62464\nv_mul_f32 v134,v16,v134\nM v[236:239],a[212:215],v[60:63],v[236:239]\ns_waitcnt vmcnt(10) lgkmcnt(14)\nds_read_b128 v[60:63],v20 offset:63488\n" \
"v_mul_f32 v135,v16,v135\nM v[236:239],a[204:207],v[88:91],v[236:239]\ns_barrier\nds_read_b128 v[88:91],v20 offset:64512\nM v[236:239],a[196:199],v[80:83],v[236:239]\nbuffer_load_dwordx4 v2,s[36:39],s47 offen offset:3072 sc1 lds\nM v[224:227],a[224:227],v[180:183],v[224:227]\ns_add_u32 m0,s22,131072\n" \
"ds_read_b128 v[80:83],v4 offset:0\nv_mul_f32 v136,v136,v164\nM v[224:227],a[232:235],v[72:75],v[224:227]\ns_add_u32 s47,s34,0\nv_mul_f32 v138,v138,v165\nv_mul_f32 v140,v140,v166\nM v[224:227],a[240:243],v[92:95],v[224:227]\ns_waitcnt lgkmcnt(3)\n" \
"C v129,v128,v129\nv_mul_f32 v142,v142,v167\nM v[224:227],a[248:251],v[84:87],v[224:227]\nbuffer_load_dwordx4 v2,s[36:39],s47 offen offset:0 sc1 lds\nM v[228:231],a[252:255],v[84:87],v[228:231]\nds_read_b128 v[84:87],v4 offset:1024\nC v133,v132,v133\nM v[228:231],a[244:247],v[92:95],v[228:231]\n" \
"ds_read_b128 v[92:95],v4 offset:2048\nC v224,v224,v225\nM v[228:231],a[236:239],v[72:75],v[228:231]\nds_read_b128 v[72:75],v4 offset:3072\nC v225,v226,v227\nM v[228:231],a[228:231],v[180:183],v[228:231]\nbuffer_load_dwordx4 v2,s[36:39],s47 offen offset:1024 sc1 lds\nM v[232:235],a[224:227],v[76:79],v[232:235]\n" \
"ds_read_b128 v[180:183],v4 offset:8192\nC v131,v130,v131\nM v[232:235],a[232:235],v[176:179],v[232:235]\ns_waitcnt lgkmcnt(4)\nC v226,v228,v229\nC v227,v230,v231\nM v[232:235],a[240:243],v[60:63],v[232:235]\nC v135,v134,v135\n" \
"C v137,v136,v137\nM v[232:235],a[248:251],v[88:91],v[232:235]\nC v139,v138,v139\nC v141,v140,v141\nM v[236:239],a[252:255],v[88:91],v[236:239]\nbuffer_load_dwordx4 v2,s[36:39],s47 offen offset:2048 sc1 lds\nM v[236:239],a[244:247],v[60:63],v[236:239]\nds_read_b128 v[88:91],v4 offset:9216\n" \
"C v232,v232,v233\nM v[236:239],a[236:239],v[176:179],v[236:239]\nds_read_b128 v[60:63],v4 offset:10240\nC v233,v234,v235\nM v[236:239],a[228:231],v[76:79],v[236:239]\ns_waitcnt lgkmcnt(2)\nds_read_b128 v[176:179],v4 offset:11264\nv_lshlrev_b32 v128,16,v129\n" \
"M v[240:243],a[0:3],v[80:83],0\nbuffer_load_dwordx4 v2,s[36:39],s47 offen offset:3072 sc1 lds\nM v[240:243],a[8:11],v[84:87],v[240:243]\ns_add_u32 m0,s22,147456\nds_read_b128 v[76:79],v4 offset:4096\nC v234,v236,v237\nM v[240:243],a[16:19],v[92:95],v[240:243]\ns_add_u32 s47,s34,8192\n" \
"C v235,v238,v239\nC v143,v142,v143\nM v[240:243],a[24:27],v[72:75],v[240:243]\nv_lshlrev_b32 v132,16,v133\nv_lshlrev_b32 v130,16,v131\nM v[244:247],a[28:31],v[72:75],0\nds_read_b128 v[72:75],v4 offset:5120\nv_lshlrev_b32 v134,16,v135\n" \
"M v[244:247],a[20:23],v[92:95],v[244:247]\nbuffer_load_dwordx4 v2,s[36:39],s47 offen offset:0 sc1 lds\nM v[244:247],a[12:15],v[84:87],v[244:247]\nds_read_b128 v[92:95],v4 offset:6144\nv_lshlrev_b32 v136,16,v137\nM v[244:247],a[4:7],v[80:83],v[244:247]\nds_read_b128 v[84:87],v4 offset:7168\nv_lshlrev_b32 v140,16,v141\n" \
"M v[248:251],a[0:3],v[180:183],0\ns_waitcnt lgkmcnt(3)\nds_read_b128 v[80:83],v4 offset:12288\nv_lshlrev_b32 v138,16,v139\nM v[248:251],a[8:11],v[88:91],v[248:251]\nLG24_%=:\ns_cbranch_execz LHR3_%=\nbuffer_store_dwordx4 v[224:227],v4,s[36:39],s44 offen offset:0 sc1\n" \
"M v[248:251],a[16:19],v[60:63],v[248:251]\nv_lshlrev_b32 v142,16,v143\nv_and_b32 v129,0xffff0000,v129\nM v[248:251],a[24:27],v[176:179],v[248:251]\nv_and_b32 v133,0xffff0000,v133\nv_and_b32 v131,0xffff0000,v131\nM v[252:255],a[28:31],v[176:179],0\nds_read_b128 v[176:179],v4 offset:13312\n" \
"v_and_b32 v135,0xffff0000,v135\nM v[252:255],a[20:23],v[60:63],v[252:255]\nds_read_b128 v[60:63],v4 offset:14336\nv_and_b32 v137,0xffff0000,v137\nM v[252:255],a[12:15],v[88:91],v[252:255]\nds_read_b128 v[88:91],v4 offset:15360\nv_and_b32 v141,0xffff0000,v141\nM v[252:255],a[4:7],v[180:183],v[252:255]\n" \
"ds_read_b128 v[180:183],v4 offset:32768\nv_and_b32 v139,0xffff0000,v139\nM v[240:243],a[32:35],v[76:79],v[240:243]\ns_waitcnt lgkmcnt(4)\nv_and_b32 v143,0xffff0000,v143\nv_mul_f32 v136,v17,v136\nM v[240:243],a[40:43],v[72:75],v[240:243]\nv_mul_f32 v137,v17,v137\n" \
"v_mul_f32 v140,v17,v140\nM v[240:243],a[48:51],v[92:95],v[240:243]\nv_mul_f32 v141,v17,v141\nv_mul_f32 v138,v17,v138\nM v[240:243],a[56:59],v[84:87],v[240:243]\nv_mul_f32 v139,v17,v139\nv_mul_f32 v142,v17,v142\nM v[244:247],a[60:63],v[84:87],v[244:247]\n" \
"buffer_store_dwordx4 v[232:235],v4,s[36:39],s44 offen offset:1024 sc1\nM v[244:247],a[52:55],v[92:95],v[244:247]\nds_read_b128 v[84:87],v4 offset:33792\nds_read_b128 v[92:95],v4 offset:34816\nM v[244:247],a[44:47],v[72:75],v[244:247]\nds_read_b128 v[72:75],v4 offset:35840\nv_mul_f32 v143,v17,v143\nM v[244:247],a[36:39],v[76:79],v[244:247]\n" \
"ds_read_b128 v[76:79],v4 offset:40960\nv_mul_f32 v144,v144,v168\nM v[248:251],a[32:35],v[80:83],v[248:251]\ns_waitcnt lgkmcnt(4)\nv_mul_f32 v148,v148,v170\nv_mul_f32 v146,v146,v169\nM v[248:251],a[40:43],v[176:179],v[248:251]\nv_mul_f32 v150,v150,v171\n" \
"C v137,v136,v137\nM v[248:251],a[48:51],v[60:63],v[248:251]\nC v141,v140,v141\nC v139,v138,v139\nM v[248:251],a[56:59],v[88:91],v[248:251]\nC v143,v142,v143\nC v145,v144,v145\nM v[252:255],a[60:63],v[88:91],v[252:255]\n" \
"ds_read_b128 v[88:91],v4 offset:41984\nC v149,v148,v149\nM v[252:255],a[52:55],v[60:63],v[252:255]\nds_read_b128 v[60:63],v4 offset:43008\nC v147,v146,v147\nM v[252:255],a[44:47],v[176:179],v[252:255]\nds_read_b128 v[176:179],v4 offset:44032\nC v151,v150,v151\n" \
"M v[252:255],a[36:39],v[80:83],v[252:255]\nds_read_b128 v[80:83],v4 offset:36864\nv_lshlrev_b32 v136,16,v137\nM v[240:243],a[64:67],v[180:183],v[240:243]\ns_waitcnt lgkmcnt(4)\nv_lshlrev_b32 v140,16,v141\nv_lshlrev_b32 v138,16,v139\nM v[240:243],a[72:75],v[84:87],v[240:243]\n" \
"buffer_load_dwordx4 v2,s[36:39],s47 offen offset:1024 sc1 lds\nM v[240:243],a[80:83],v[92:95],v[240:243]\nv_lshlrev_b32 v142,16,v143\nv_lshlrev_b32 v144,16,v145\nM v[240:243],a[88:91],v[72:75],v[240:243]\nv_lshlrev_b32 v148,16,v149\nv_lshlrev_b32 v146,16,v147\nM v[244:247],a[92:95],v[72:75],v[244:247]\n" \
"ds_read_b128 v[72:75],v4 offset:37888\nv_lshlrev_b32 v150,16,v151\nM v[244:247],a[84:87],v[92:95],v[244:247]\nds_read_b128 v[92:95],v4 offset:38912\nv_and_b32 v137,0xffff0000,v137\nM v[244:247],a[76:79],v[84:87],v[244:247]\nbuffer_load_dwordx4 v2,s[36:39],s47 offen offset:2048 sc1 lds\nM v[244:247],a[68:71],v[180:183],v[244:247]\n" \
"ds_read_b128 v[84:87],v4 offset:39936\nv_and_b32 v141,0xffff0000,v141\nM v[248:251],a[64:67],v[76:79],v[248:251]\ns_waitcnt lgkmcnt(3)\nds_read_b128 v[180:183],v4 offset:45056\nv_and_b32 v139,0xffff0000,v139\nM v[248:251],a[72:75],v[88:91],v[248:251]\nv_and_b32 v143,0xffff0000,v143\n" \
"v_and_b32 v145,0xffff0000,v145\nM v[248:251],a[80:83],v[60:63],v[248:251]\nbuffer_load_dwordx4 v2,s[36:39],s47 offen offset:3072 sc1 lds\nM v[248:251],a[88:91],v[176:179],v[248:251]\ns_add_u32 m0,s22,65536\nv_and_b32 v149,0xffff0000,v149\nv_and_b32 v147,0xffff0000,v147\nM v[252:255],a[92:95],v[176:179],v[252:255]\n" \
"s_add_u32 s47,s34,16384\nds_read_b128 v[176:179],v4 offset:46080\nv_and_b32 v151,0xffff0000,v151\nM v[252:255],a[84:87],v[60:63],v[252:255]\ns_waitcnt vmcnt(10) lgkmcnt(14)\nds_read_b128 v[60:63],v4 offset:47104\nv_mul_f32 v144,v18,v144\nM v[252:255],a[76:79],v[88:91],v[252:255]\n" \
"s_barrier\nds_read_b128 v[88:91],v4 offset:48128\nM v[252:255],a[68:71],v[76:79],v[252:255]\nbuffer_load_dwordx4 v2,s[36:39],s47 offen offset:0 sc1 lds\nM v[240:243],a[96:99],v[80:83],v[240:243]\ns_waitcnt lgkmcnt(3)\nds_read_b128 v[76:79],v4 offset:16384\nv_mul_f32 v145,v18,v145\n" \
"M v[240:243],a[104:107],v[72:75],v[240:243]\nv_mul_f32 v148,v18,v148\nv_mul_f32 v149,v18,v149\nM v[240:243],a[112:115],v[92:95],v[240:243]\nv_mul_f32 v146,v18,v146\nv_mul_f32 v147,v18,v147\nM v[240:243],a[120:123],v[84:87],v[240:243]\nbuffer_load_dwordx4 v2,s[36:39],s47 offen offset:1024 sc1 lds\n" \
"M v[244:247],a[124:127],v[84:87],v[244:247]\nds_read_b128 v[84:87],v4 offset:17408\nv_mul_f32 v150,v18,v150\nM v[244:247],a[116:119],v[92:95],v[244:247]\ns_waitcnt vmcnt(17)\nds_read_b128 v[92:95],v4 offset:18432\nv_mul_f32 v151,v18,v151\nM v[244:247],a[108:111],v[72:75],v[244:247]\n" \
"ds_read_b128 v[72:75],v4 offset:19456\nv_mul_f32 v152,v152,v172\nM v[244:247],a[100:103],v[80:83],v[244:247]\nbuffer_store_dwordx2 v[28:29],v191,s[36:39],s95 offen offset:0 sc1\nM v[248:251],a[96:99],v[180:183],v[248:251]\ns_waitcnt lgkmcnt(3)\nds_read_b128 v[80:83],v4 offset:24576\nv_mul_f32 v156,v156,v174\n" \
"M v[248:251],a[104:107],v[176:179],v[248:251]\nbuffer_load_dwordx4 v2,s[36:39],s47 offen offset:2048 sc1 lds\nM v[248:251],a[112:115],v[60:63],v[248:251]\nv_mul_f32 v154,v154,v173\nv_mul_f32 v158,v158,v175\nM v[248:251],a[120:123],v[88:91],v[248:251]\nC v145,v144,v145\nC v149,v148,v149\n" \
"M v[252:255],a[124:127],v[88:91],v[252:255]\nds_read_b128 v[88:91],v4 offset:25600\nC v147,v146,v147\nM v[252:255],a[116:119],v[60:63],v[252:255]\ns_waitcnt lgkmcnt(2)\nds_read_b128 v[60:63],v4 offset:26624\nC v151,v150,v151\nM v[252:255],a[108:111],v[176:179],v[252:255]\n" \
"buffer_load_dwordx4 v2,s[36:39],s47 offen offset:3072 sc1 lds\nM v[252:255],a[100:103],v[180:183],v[252:255]\ns_add_u32 m0,s22,98304\nds_read_b128 v[176:179],v4 offset:27648\nC v153,v152,v153\nM v[240:243],a[128:131],v[76:79],v[240:243]\ns_add_u32 s47,s34,24576\nds_read_b128 v[180:183],v4 offset:20480\n" \
"C v157,v156,v157\nM v[240:243],a[136:139],v[84:87],v[240:243]\nC v155,v154,v155\nC v159,v158,v159\nM v[240:243],a[144:147],v[92:95],v[240:243]\nbuffer_load_dwordx4 v2,s[36:39],s47 offen offset:0 sc1 lds\nM v[240:243],a[152:155],v[72:75],v[240:243]\nv_lshlrev_b32 v144,16,v145\n" \
"v_lshlrev_b32 v148,16,v149\nM v[244:247],a[156:159],v[72:75],v[244:247]\nds_read_b128 v[72:75],v4 offset:21504\nv_lshlrev_b32 v146,16,v147\nM v[244:247],a[148:151],v[92:95],v[244:247]\nds_read_b128 v[92:95],v4 offset:22528\nv_lshlrev_b32 v150,16,v151\nM v[244:247],a[140:143],v[84:87],v[244:247]\n" \
"s_waitcnt lgkmcnt(2)\nds_read_b128 v[84:87],v4 offset:23552\nv_lshlrev_b32 v152,16,v153\nM v[244:247],a[132:135],v[76:79],v[244:247]\nbuffer_load_dwordx4 v2,s[36:39],s47 offen offset:1024 sc1 lds\nM v[248:251],a[128:131],v[80:83],v[248:251]\nds_read_b128 v[76:79],v4 offset:28672\nv_lshlrev_b32 v156,16,v157\n" \
"M v[248:251],a[136:139],v[88:91],v[248:251]\nv_lshlrev_b32 v154,16,v155\nv_lshlrev_b32 v158,16,v159\nM v[248:251],a[144:147],v[60:63],v[248:251]\nv_and_b32 v145,0xffff0000,v145\nv_and_b32 v149,0xffff0000,v149\nM v[248:251],a[152:155],v[176:179],v[248:251]\nbuffer_load_dwordx4 v2,s[36:39],s47 offen offset:2048 sc1 lds\n" \
"M v[252:255],a[156:159],v[176:179],v[252:255]\nds_read_b128 v[176:179],v4 offset:29696\nv_and_b32 v147,0xffff0000,v147\nM v[252:255],a[148:151],v[60:63],v[252:255]\nds_read_b128 v[60:63],v4 offset:30720\nv_and_b32 v151,0xffff0000,v151\nM v[252:255],a[140:143],v[88:91],v[252:255]\nds_read_b128 v[88:91],v4 offset:31744\n" \
"v_and_b32 v153,0xffff0000,v153\nM v[252:255],a[132:135],v[80:83],v[252:255]\ns_waitcnt lgkmcnt(3)\nds_read_b128 v[80:83],v4 offset:49152\nv_and_b32 v157,0xffff0000,v157\nM v[240:243],a[160:163],v[180:183],v[240:243]\nbuffer_load_dwordx4 v2,s[36:39],s47 offen offset:3072 sc1 lds\nM v[240:243],a[168:171],v[72:75],v[240:243]\n" \
"s_add_u32 m0,s22,81920\nv_and_b32 v155,0xffff0000,v155\nv_and_b32 v159,0xffff0000,v159\nM v[240:243],a[176:179],v[92:95],v[240:243]\ns_add_u32 s47,s35,0\nv_mul_f32 v152,v19,v152\nv_mul_f32 v153,v19,v153\nM v[240:243],a[184:187],v[84:87],v[240:243]\n" \
"v_mul_f32 v156,v19,v156\nv_mul_f32 v157,v19,v157\nM v[244:247],a[188:191],v[84:87],v[244:247]\nbuffer_load_dwordx4 v2,s[36:39],s47 offen offset:0 sc1 lds\nM v[244:247],a[180:183],v[92:95],v[244:247]\nds_read_b128 v[84:87],v4 offset:50176\nv_mul_f32 v154,v19,v154\nM v[244:247],a[172:175],v[72:75],v[244:247]\n" \
"ds_read_b128 v[92:95],v4 offset:51200\nv_mul_f32 v155,v19,v155\nM v[244:247],a[164:167],v[180:183],v[244:247]\nds_read_b128 v[72:75],v4 offset:52224\nv_mul_f32 v158,v19,v158\nM v[248:251],a[160:163],v[76:79],v[248:251]\ns_waitcnt lgkmcnt(3)\nds_read_b128 v[180:183],v4 offset:57344\n" \
"v_mul_f32 v159,v19,v159\nM v[248:251],a[168:171],v[176:179],v[248:251]\nbuffer_load_dwordx4 v2,s[36:39],s47 offen offset:1024 sc1 lds\nM v[248:251],a[176:179],v[60:63],v[248:251]\nC v153,v152,v153\nC v157,v156,v157\nM v[248:251],a[184:187],v[88:91],v[248:251]\nC v155,v154,v155\n" \
"C v159,v158,v159\nM v[252:255],a[188:191],v[88:91],v[252:255]\nds_read_b128 v[88:91],v4 offset:58368\nv_lshlrev_b32 v152,16,v153\nM v[252:255],a[180:183],v[60:63],v[252:255]\nbuffer_load_dwordx4 v2,s[36:39],s47 offen offset:2048 sc1 lds\nM v[252:255],a[172:175],v[176:179],v[252:255]\nds_read_b128 v[60:63],v4 offset:59392\n" \
"v_lshlrev_b32 v156,16,v157\nM v[252:255],a[164:167],v[76:79],v[252:255]\nds_read_b128 v[176:179],v4 offset:60416\nv_lshlrev_b32 v154,16,v155\nM v[240:243],a[192:195],v[80:83],v[240:243]\ns_waitcnt lgkmcnt(3)\nds_read_b128 v[76:79],v4 offset:53248\nv_lshlrev_b32 v158,16,v159\n" \
"M v[240:243],a[200:203],v[84:87],v[240:243]\nv_and_b32 v153,0xffff0000,v153\nv_and_b32 v157,0xffff0000,v157\nM v[240:243],a[208:211],v[92:95],v[240:243]\nbuffer_load_dwordx4 v2,s[36:39],s47 offen offset:3072 sc1 lds\nM v[240:243],a[216:219],v[72:75],v[240:243]\ns_add_u32 m0,s22,114688\nv_and_b32 v155,0xffff0000,v155\n" \
"v_and_b32 v159,0xffff0000,v159\nM v[244:247],a[220:223],v[72:75],v[244:247]\ns_add_u32 s47,s35,8192\nds_read_b128 v[72:75],v4 offset:54272\nM v[244:247],a[212:215],v[92:95],v[244:247]\nds_read_b128 v[92:95],v4 offset:55296\nM v[244:247],a[204:207],v[84:87],v[244:247]\nbuffer_load_dwordx4 v2,s[36:39],s47 offen offset:0 sc1 lds\n" \
"M v[244:247],a[196:199],v[80:83],v[244:247]\nds_read_b128 v[84:87],v4 offset:56320\nM v[248:251],a[192:195],v[180:183],v[248:251]\ns_waitcnt lgkmcnt(3)\nds_read_b128 v[80:83],v4 offset:61440\nM v[248:251],a[200:203],v[88:91],v[248:251]\nM v[248:251],a[208:211],v[60:63],v[248:251]\nM v[248:251],a[216:219],v[176:179],v[248:251]\n" \
"buffer_load_dwordx4 v2,s[36:39],s47 offen offset:1024 sc1 lds\nM v[252:255],a[220:223],v[176:179],v[252:255]\nds_read_b128 v[176:179],v4 offset:62464\nM v[252:255],a[212:215],v[60:63],v[252:255]\ns_waitcnt vmcnt(15) lgkmcnt(14)\nds_read_b128 v[60:63],v4 offset:63488\nM v[252:255],a[204:207],v[88:91],v[252:255]\ns_barrier\n" \
"ds_read_b128 v[88:91],v4 offset:64512\nM v[252:255],a[196:199],v[180:183],v[252:255]\nbuffer_load_dwordx4 v2,s[36:39],s47 offen offset:2048 sc1 lds\nM v[240:243],a[224:227],v[76:79],v[240:243]\ns_waitcnt lgkmcnt(3)\nds_read_b128 v[180:183],v23 offset:0\nM v[240:243],a[232:235],v[72:75],v[240:243]\nM v[240:243],a[240:243],v[92:95],v[240:243]\n" \
"M v[240:243],a[248:251],v[84:87],v[240:243]\nbuffer_load_dwordx2 v[10:11],v190,s[36:39],0 offen sc1\nM v[244:247],a[252:255],v[84:87],v[244:247]\nds_read_b128 v[84:87],v23 offset:1024\nM v[244:247],a[244:247],v[92:95],v[244:247]\nbuffer_load_dwordx4 v2,s[36:39],s47 offen offset:3072 sc1 lds\nM v[244:247],a[236:239],v[72:75],v[244:247]\ns_add_u32 m0,s22,0\n" \
"ds_read_b128 v[92:95],v23 offset:2048\nC v240,v240,v241\nM v[244:247],a[228:231],v[76:79],v[244:247]\ns_add_u32 s47,s35,16384\nds_read_b128 v[72:75],v23 offset:3072\nC v241,v242,v243\nM v[248:251],a[224:227],v[80:83],v[248:251]\ns_waitcnt lgkmcnt(3)\n" \
"ds_read_b128 v[76:79],v23 offset:8192\nM v[248:251],a[232:235],v[176:179],v[248:251]\nC v242,v244,v245\nC v243,v246,v247\nM v[248:251],a[240:243],v[60:63],v[248:251]\nbuffer_store_dwordx4 v[240:243],v4,s[36:39],s45 offen offset:0 sc1\nM v[248:251],a[248:251],v[88:91],v[248:251]\nM v[252:255],a[252:255],v[88:91],v[252:255]\n" \
"ds_read_b128 v[88:91],v23 offset:9216\nM v[252:255],a[244:247],v[60:63],v[252:255]\nds_read_b128 v[60:63],v23 offset:10240\nC v248,v248,v249\nM v[252:255],a[236:239],v[176:179],v[252:255]\nds_read_b128 v[176:179],v23 offset:11264\nC v249,v250,v251\nM v[252:255],a[228:231],v[80:83],v[252:255]\n" \
"ds_read_b128 v[80:83],v23 offset:4096\nM v[192:195],a[0:3],v[180:183],0\ns_waitcnt lgkmcnt(4)\nM v[192:195],a[8:11],v[84:87],v[192:195]\nC v250,v252,v253\nC v251,v254,v255\nM v[192:195],a[16:19],v[92:95],v[192:195]\nM v[192:195],a[24:27],v[72:75],v[192:195]\n" \
"M v[196:199],a[28:31],v[72:75],0\nds_read_b128 v[72:75],v23 offset:5120\nM v[196:199],a[20:23],v[92:95],v[196:199]\nds_read_b128 v[92:95],v23 offset:6144\nM v[196:199],a[12:15],v[84:87],v[196:199]\nds_read_b128 v[84:87],v23 offset:7168\nM v[196:199],a[4:7],v[180:183],v[196:199]\nbuffer_store_dwordx4 v[248:251],v4,s[36:39],s45 offen offset:1024 sc1\n" \
"M v[200:203],a[0:3],v[76:79],0\ns_waitcnt lgkmcnt(3)\nM v[200:203],a[8:11],v[88:91],v[200:203]\nM v[200:203],a[16:19],v[60:63],v[200:203]\nM v[200:203],a[24:27],v[176:179],v[200:203]\nM v[204:207],a[28:31],v[176:179],0\nM v[204:207],a[20:23],v[60:63],v[204:207]\nds_read_b128 v[60:63],v23 offset:12288\n" \
"M v[204:207],a[12:15],v[88:91],v[204:207]\nds_read_b128 v[88:91],v23 offset:13312\nM v[204:207],a[4:7],v[76:79],v[204:207]\nds_read_b128 v[76:79],v23 offset:14336\nM v[192:195],a[32:35],v[80:83],v[192:195]\ns_waitcnt lgkmcnt(3)\nM v[192:195],a[40:43],v[72:75],v[192:195]\nM v[192:195],a[48:51],v[92:95],v[192:195]\n" \
"M v[192:195],a[56:59],v[84:87],v[192:195]\nM v[196:199],a[60:63],v[84:87],v[196:199]\nds_read_b128 v[84:87],v23 offset:15360\nM v[196:199],a[52:55],v[92:95],v[196:199]\nbuffer_load_dwordx4 v2,s[36:39],s47 offen offset:0 sc1 lds\nM v[196:199],a[44:47],v[72:75],v[196:199]\nds_read_b128 v[92:95],v23 offset:16384\nM v[196:199],a[36:39],v[80:83],v[196:199]\n" \
"s_waitcnt lgkmcnt(2)\nds_read_b128 v[72:75],v23 offset:17408\nM v[200:203],a[32:35],v[60:63],v[200:203]\nds_read_b128 v[80:83],v23 offset:18432\nM v[200:203],a[40:43],v[88:91],v[200:203]\nbuffer_load_dwordx4 v2,s[36:39],s47 offen offset:1024 sc1 lds\nM v[200:203],a[48:51],v[76:79],v[200:203]\ns_waitcnt lgkmcnt(3)\n" \
"M v[200:203],a[56:59],v[84:87],v[200:203]\nM v[204:207],a[60:63],v[84:87],v[204:207]\nds_read_b128 v[84:87],v23 offset:19456\nM v[204:207],a[52:55],v[76:79],v[204:207]\nbuffer_load_dwordx4 v2,s[36:39],s47 offen offset:2048 sc1 lds\nM v[204:207],a[44:47],v[88:91],v[204:207]\nds_read_b128 v[76:79],v23 offset:24576\nM v[204:207],a[36:39],v[60:63],v[204:207]\n" \
"s_waitcnt lgkmcnt(2)\nds_read_b128 v[88:91],v23 offset:25600\nM v[192:195],a[64:67],v[92:95],v[192:195]\ns_waitcnt lgkmcnt(2)\nds_read_b128 v[60:63],v23 offset:26624\nM v[192:195],a[72:75],v[72:75],v[192:195]\nbuffer_load_dwordx4 v2,s[36:39],s47 offen offset:3072 sc1 lds\nM v[192:195],a[80:83],v[80:83],v[192:195]\n" \
"s_add_u32 m0,s22,32768\nM v[192:195],a[88:91],v[84:87],v[192:195]\ns_add_u32 s47,s35,24576\nM v[196:199],a[92:95],v[84:87],v[196:199]\ns_waitcnt lgkmcnt(2)\nds_read_b128 v[84:87],v23 offset:27648\nM v[196:199],a[84:87],v[80:83],v[196:199]\ns_waitcnt vmcnt(7)\n" \
"v_cmp_gt_u64 s[98:99],s[14:15],v[10:11]\nv_cmp_le_u64 s[18:19],s[16:17],v[10:11]\nM v[196:199],a[76:79],v[72:75],v[196:199]\ns_or_b64 s[98:99],s[98:99],s[18:19]\ns_and_b32 s98,s98,0x7\ns_cbranch_scc1 LPP_%=\nLPPR_%=:\nds_read_b128 v[80:83],v23 offset:20480\n" \
"ds_read_b128 v[72:75],v23 offset:21504\nM v[196:199],a[68:71],v[92:95],v[196:199]\nbuffer_load_dwordx4 v[96:99],v4,s[36:39],s63 offen offset:0 sc1\nM v[200:203],a[64:67],v[76:79],v[200:203]\ns_waitcnt lgkmcnt(3)\nds_read_b128 v[92:95],v23 offset:22528\nM v[200:203],a[72:75],v[88:91],v[200:203]\nM v[200:203],a[80:83],v[60:63],v[200:203]\n" \
"s_waitcnt lgkmcnt(3)\nM v[200:203],a[88:91],v[84:87],v[200:203]\nbuffer_load_dwordx4 v[100:103],v4,s[36:39],s63 offen offset:1024 sc1\nM v[204:207],a[92:95],v[84:87],v[204:207]\nds_read_b128 v[84:87],v23 offset:23552\nM v[204:207],a[84:87],v[60:63],v[204:207]\nds_read_b128 v[60:63],v23 offset:28672\nM v[204:207],a[76:79],v[88:91],v[204:207]\n" \
"s_waitcnt lgkmcnt(2)\nds_read_b128 v[88:91],v23 offset:29696\nM v[204:207],a[68:71],v[76:79],v[204:207]\nbuffer_load_dwordx4 v[104:107],v4,s[36:39],s63 offen offset:2048 sc1\nM v[192:195],a[96:99],v[80:83],v[192:195]\ns_waitcnt lgkmcnt(2)\nds_read_b128 v[76:79],v23 offset:30720\nM v[192:195],a[104:107],v[72:75],v[192:195]\n" \
"s_waitcnt vmcnt(18) lgkmcnt(15)\nM v[192:195],a[112:115],v[92:95],v[192:195]\ns_barrier\nM v[192:195],a[120:123],v[84:87],v[192:195]\nbuffer_load_dwordx4 v[108:111],v4,s[36:39],s63 offen offset:3072 sc1\nM v[196:199],a[124:127],v[84:87],v[196:199]\ns_add_u32 s63,s49,4096\nds_read_b128 v[84:87],v23 offset:31744\n" \
"M v[196:199],a[116:119],v[92:95],v[196:199]\nds_read_b128 v[92:95],v20 offset:0\nM v[196:199],a[108:111],v[72:75],v[196:199]\nds_read_b128 v[72:75],v20 offset:1024\nM v[196:199],a[100:103],v[80:83],v[196:199]\ns_waitcnt lgkmcnt(3)\nds_read_b128 v[80:83],v20 offset:2048\nM v[200:203],a[96:99],v[60:63],v[200:203]\n" \
"buffer_load_dwordx4 v[112:115],v4,s[36:39],s63 offen offset:0 sc1\nM v[200:203],a[104:107],v[88:91],v[200:203]\nM v[200:203],a[112:115],v[76:79],v[200:203]\ns_waitcnt lgkmcnt(3)\nM v[200:203],a[120:123],v[84:87],v[200:203]\nM v[204:207],a[124:127],v[84:87],v[204:207]\nds_read_b128 v[84:87],v20 offset:3072\nM v[204:207],a[116:119],v[76:79],v[204:207]\n" \
"buffer_load_dwordx4 v[116:119],v4,s[36:39],s63 offen offset:1024 sc1\nM v[204:207],a[108:111],v[88:91],v[204:207]\nds_read_b128 v[76:79],v20 offset:8192\nds_read_b128 v[88:91],v20 offset:9216\nM v[204:207],a[100:103],v[60:63],v[204:207]\ns_waitcnt lgkmcnt(3)\nds_read_b128 v[60:63],v20 offset:10240\nM v[192:195],a[128:131],v[92:95],v[192:195]\n" \
"M v[192:195],a[136:139],v[72:75],v[192:195]\nbuffer_load_dwordx4 v2,s[36:39],s47 offen offset:0 sc1 lds\nM v[192:195],a[144:147],v[80:83],v[192:195]\ns_waitcnt lgkmcnt(3)\nM v[192:195],a[152:155],v[84:87],v[192:195]\nM v[196:199],a[156:159],v[84:87],v[196:199]\nds_read_b128 v[84:87],v20 offset:11264\nM v[196:199],a[148:151],v[80:83],v[196:199]\n" \
"buffer_load_dwordx4 v2,s[36:39],s47 offen offset:1024 sc1 lds\nM v[196:199],a[140:143],v[72:75],v[196:199]\nds_read_b128 v[80:83],v20 offset:4096\nM v[196:199],a[132:135],v[92:95],v[196:199]\ns_waitcnt lgkmcnt(2)\nds_read_b128 v[72:75],v20 offset:5120\nM v[200:203],a[128:131],v[76:79],v[200:203]\nds_read_b128 v[92:95],v20 offset:6144\n" \
"M v[200:203],a[136:139],v[88:91],v[200:203]\ns_waitcnt lgkmcnt(3)\nM v[200:203],a[144:147],v[60:63],v[200:203]\nbuffer_load_dwordx4 v2,s[36:39],s47 offen offset:2048 sc1 lds\nM v[200:203],a[152:155],v[84:87],v[200:203]\nM v[204:207],a[156:159],v[84:87],v[204:207]\nds_read_b128 v[84:87],v20 offset:7168\nM v[204:207],a[148:151],v[60:63],v[204:207]\n" \
"ds_read_b128 v[60:63],v20 offset:12288\nM v[204:207],a[140:143],v[88:91],v[204:207]\nbuffer_load_dwordx4 v2,s[36:39],s47 offen offset:3072 sc1 lds\nM v[204:207],a[132:135],v[76:79],v[204:207]\ns_waitcnt lgkmcnt(2)\nds_read_b128 v[88:91],v20 offset:13312\nM v[192:195],a[160:163],v[80:83],v[192:195]\nds_read_b128 v[76:79],v20 offset:14336\n" \
"M v[192:195],a[168:171],v[72:75],v[192:195]\nM v[192:195],a[176:179],v[92:95],v[192:195]\ns_waitcnt lgkmcnt(3)\nM v[192:195],a[184:187],v[84:87],v[192:195]\nM v[196:199],a[188:191],v[84:87],v[196:199]\nds_read_b128 v[84:87],v20 offset:15360\nM v[196:199],a[180:183],v[92:95],v[196:199]\nds_read_b128 v[92:95],v20 offset:32768\n" \
"M v[196:199],a[172:175],v[72:75],v[196:199]\nds_read_b128 v[72:75],v20 offset:33792\nM v[196:199],a[164:167],v[80:83],v[196:199]\ns_waitcnt lgkmcnt(3)\nds_read_b128 v[80:83],v20 offset:34816\nM v[200:203],a[160:163],v[60:63],v[200:203]\nM v[200:203],a[168:171],v[88:91],v[200:203]\nM v[200:203],a[176:179],v[76:79],v[200:203]\n" \
"s_waitcnt lgkmcnt(3)\nM v[200:203],a[184:187],v[84:87],v[200:203]\nM v[204:207],a[188:191],v[84:87],v[204:207]\nds_read_b128 v[84:87],v20 offset:35840\nM v[204:207],a[180:183],v[76:79],v[204:207]\nds_read_b128 v[76:79],v20 offset:40960\nM v[204:207],a[172:175],v[88:91],v[204:207]\nds_read_b128 v[88:91],v20 offset:41984\n" \
"M v[204:207],a[164:167],v[60:63],v[204:207]\ns_waitcnt lgkmcnt(3)\nds_read_b128 v[60:63],v20 offset:43008\nM v[192:195],a[192:195],v[92:95],v[192:195]\nM v[192:195],a[200:203],v[72:75],v[192:195]\nM v[192:195],a[208:211],v[80:83],v[192:195]\ns_waitcnt lgkmcnt(3)\nM v[192:195],a[216:219],v[84:87],v[192:195]\n" \
"M v[196:199],a[220:223],v[84:87],v[196:199]\nds_read_b128 v[84:87],v20 offset:44032\nM v[196:199],a[212:215],v[80:83],v[196:199]\nds_read_b128 v[80:83],v20 offset:36864\nM v[196:199],a[204:207],v[72:75],v[196:199]\nds_read_b128 v[72:75],v20 offset:37888\nM v[196:199],a[196:199],v[92:95],v[196:199]\ns_waitcnt lgkmcnt(3)\n" \
"ds_read_b128 v[92:95],v20 offset:38912\nM v[200:203],a[192:195],v[76:79],v[200:203]\nM v[200:203],a[200:203],v[88:91],v[200:203]\nM v[200:203],a[208:211],v[60:63],v[200:203]\ns_waitcnt lgkmcnt(3)\nM v[200:203],a[216:219],v[84:87],v[200:203]\ns_add_u32 s63,s49,4096\nM v[204:207],a[220:223],v[84:87],v[204:207]\n" \
"ds_read_b128 v[84:87],v20 offset:39936\nM v[204:207],a[212:215],v[60:63],v[204:207]\nbuffer_load_dwordx4 v[176:179],v4,s[36:39],s63 offen offset:2048 sc1\nM v[204:207],a[204:207],v[88:91],v[204:207]\ns_waitcnt lgkmcnt(1)\nds_read_b128 v[60:63],v20 offset:45056\nds_read_b128 v[88:91],v20 offset:46080\nM v[204:207],a[196:199],v[76:79],v[204:207]\n" \
"s_waitcnt vmcnt(5)\nds_read_b128 v[76:79],v20 offset:47104\nM v[192:195],a[224:227],v[80:83],v[192:195]\ns_waitcnt lgkmcnt(3)\nM v[192:195],a[232:235],v[72:75],v[192:195]\nLG25_%=:\ns_cbranch_execz LHR3_%=\ns_waitcnt vmcnt(17) lgkmcnt(15)\n" \
"M v[192:195],a[240:243],v[92:95],v[192:195]\ns_barrier\nM v[192:195],a[248:251],v[84:87],v[192:195]\nbuffer_load_dwordx4 v[180:183],v4,s[36:39],s63 offen offset:3072 sc1\nM v[196:199],a[252:255],v[84:87],v[196:199]\ns_add_u32 s63,s49,8192\nds_read_b128 v[84:87],v20 offset:48128\nM v[196:199],a[244:247],v[92:95],v[196:199]\n" \
"C v192,v192,v193\nC v193,v194,v195\nM v[196:199],a[236:239],v[72:75],v[196:199]\ns_waitcnt lgkmcnt(1)\nds_read_b128 v[92:95],v20 offset:16384\nds_read_b128 v[72:75],v20 offset:17408\nM v[196:199],a[228:231],v[80:83],v[196:199]\nbuffer_load_dwordx4 v[32:35],v4,s[36:39],s63 offen offset:0 sc1\n" \
"M v[200:203],a[224:227],v[60:63],v[200:203]\nds_read_b128 v[80:83],v20 offset:18432\nM v[200:203],a[232:235],v[88:91],v[200:203]\nC v194,v196,v197\nC v195,v198,v199\nM v[200:203],a[240:243],v[76:79],v[200:203]\ns_waitcnt lgkmcnt(3)\nM v[200:203],a[248:251],v[84:87],v[200:203]\n" \
"buffer_load_dwordx4 v[36:39],v4,s[36:39],s63 offen offset:1024 sc1\nM v[204:207],a[252:255],v[84:87],v[204:207]\nds_read_b128 v[84:87],v20 offset:19456\nM v[204:207],a[244:247],v[76:79],v[204:207]\nds_read_b128 v[76:79],v20 offset:24576\nC v200,v200,v201\nM v[204:207],a[236:239],v[88:91],v[204:207]\nds_read_b128 v[88:91],v20 offset:25600\n" \
"C v201,v202,v203\nM v[204:207],a[228:231],v[60:63],v[204:207]\nbuffer_store_dwordx4 v[192:195],v4,s[36:39],s48 offen offset:0 sc1\nM v[48:51],v[186:189],v[120:123],0\nds_read_b128 v[60:63],v20 offset:26624\nM v[52:55],v[184:187],v[120:123],0\nC v202,v204,v205\nC v203,v206,v207\n" \
"M v[64:67],v[186:189],v[124:127],0\nM v[68:71],v[184:187],v[124:127],0\nM v[48:51],v[186:189],v[96:99],v[48:51]\nM v[52:55],v[184:187],v[96:99],v[52:55]\nM v[64:67],v[186:189],v[100:103],v[64:67]\nM v[68:71],v[184:187],v[100:103],v[68:71]\nM v[48:51],v[186:189],v[104:107],v[48:51]\nbuffer_store_dwordx4 v[200:203],v4,s[36:39],s48 offen offset:1024 sc1\n" \
"M v[52:55],v[184:187],v[104:107],v[52:55]\nM v[64:67],v[186:189],v[108:111],v[64:67]\nM v[68:71],v[184:187],v[108:111],v[68:71]\nM v[48:51],v[186:189],v[112:115],v[48:51]\nM v[52:55],v[184:187],v[112:115],v[52:55]\nM v[64:67],v[186:189],v[116:119],v[64:67]\nC v160,v48,v49\nC v161,v50,v51\n" \
"M v[68:71],v[184:187],v[116:119],v[68:71]\ns_waitcnt lgkmcnt(0)\nC v162,v52,v53\nC v163,v54,v55\nM v[208:211],a[0:3],v[92:95],0\nv_lshlrev_b32 v48,16,v160\nv_lshlrev_b32 v52,16,v162\nM v[208:211],a[8:11],v[72:75],v[208:211]\n" \
"v_and_b32 v49,0xffff0000,v160\nv_and_b32 v53,0xffff0000,v162\nM v[208:211],a[16:19],v[80:83],v[208:211]\nv_lshlrev_b32 v50,16,v161\nv_lshlrev_b32 v54,16,v163\nM v[208:211],a[24:27],v[84:87],v[208:211]\nv_and_b32 v51,0xffff0000,v161\nv_and_b32 v55,0xffff0000,v163\n" \
"M v[212:215],a[28:31],v[84:87],0\nds_read_b128 v[84:87],v20 offset:27648\nv_mul_f32 v48,v24,v48\nM v[212:215],a[20:23],v[80:83],v[212:215]\nds_read_b128 v[80:83],v20 offset:20480\nv_mul_f32 v52,v24,v52\nM v[212:215],a[12:15],v[72:75],v[212:215]\nds_read_b128 v[72:75],v20 offset:21504\n" \
"v_mul_f32 v49,v24,v49\nM v[212:215],a[4:7],v[92:95],v[212:215]\nds_read_b128 v[92:95],v20 offset:22528\nv_mul_f32 v53,v24,v53\nM v[216:219],a[0:3],v[76:79],0\nv_mul_f32 v50,v24,v50\nv_mul_f32 v51,v24,v51\nM v[216:219],a[8:11],v[88:91],v[216:219]\n" \
"v_mul_f32 v54,v24,v54\nv_mul_f32 v55,v24,v55\nM v[216:219],a[16:19],v[60:63],v[216:219]\ns_waitcnt lgkmcnt(3)\nC v160,v48,v49\nC v162,v52,v53\nM v[216:219],a[24:27],v[84:87],v[216:219]\nC v161,v50,v51\n" \
"C v163,v54,v55\nM v[220:223],a[28:31],v[84:87],0\nds_read_b128 v[84:87],v20 offset:23552\nv_lshlrev_b32 v48,16,v160\nM v[220:223],a[20:23],v[60:63],v[220:223]\nds_read_b128 v[60:63],v20 offset:28672\nv_lshlrev_b32 v52,16,v162\nM v[220:223],a[12:15],v[88:91],v[220:223]\n" \
"ds_read_b128 v[88:91],v20 offset:29696\nv_and_b32 v49,0xffff0000,v160\nM v[220:223],a[4:7],v[76:79],v[220:223]\ns_waitcnt lgkmcnt(3)\nds_read_b128 v[76:79],v20 offset:30720\nv_lshlrev_b32 v50,16,v161\nM v[208:211],a[32:35],v[80:83],v[208:211]\nv_and_b32 v53,0xffff0000,v162\n" \
"v_lshlrev_b32 v54,16,v163\nM v[208:211],a[40:43],v[72:75],v[208:211]\nv_and_b32 v51,0xffff0000,v161\nv_and_b32 v55,0xffff0000,v163\nM v[208:211],a[48:51],v[92:95],v[208:211]\ns_waitcnt lgkmcnt(3)\nC v164,v64,v65\nC v166,v68,v69\n" \
"M v[208:211],a[56:59],v[84:87],v[208:211]\nC v165,v66,v67\nC v167,v70,v71\nM v[212:215],a[60:63],v[84:87],v[212:215]\ns_waitcnt vmcnt(2)\nds_read_b128 v[84:87],v20 offset:31744\nv_max_u32 v10,v176,v177\nM v[212:215],a[52:55],v[92:95],v[212:215]\n" \
"ds_read_b128 v[92:95],v20 offset:49152\nv_max3_u32 v10,v10,v178,v179\nM v[212:215],a[44:47],v[72:75],v[212:215]\nds_read_b128 v[72:75],v20 offset:50176\nv_max3_u32 v10,v10,v180,v181\nM v[212:215],a[36:39],v[80:83],v[212:215]\ns_waitcnt lgkmcnt(3)\nds_read_b128 v[80:83],v20 offset:51200\n" \
"v_max3_u32 v10,v10,v182,v183\nM v[216:219],a[32:35],v[60:63],v[216:219]\nv_max3_u32 v10,v10,v32,v33\nv_max3_u32 v10,v10,v34,v35\nM v[216:219],a[40:43],v[88:91],v[216:219]\nv_max3_u32 v10,v10,v36,v37\nv_max3_u32 v10,v10,v38,v39\nM v[216:219],a[48:51],v[76:79],v[216:219]\n" \
"s_waitcnt lgkmcnt(3)\nv_cmp_eq_u32 vcc,-1,v10\nv_add_f32 v48,v48,v128\nM v[216:219],a[56:59],v[84:87],v[216:219]\nv_add_f32 v52,v52,v132\nv_add_f32 v49,v49,v129\nM v[220:223],a[60:63],v[84:87],v[220:223]\ns_cbranch_vccnz LIN_%=\n" \
"LINR_%=:\nds_read_b128 v[84:87],v20 offset:52224\nv_add_f32 v50,v50,v130\nM v[220:223],a[52:55],v[76:79],v[220:223]\nds_read_b128 v[76:79],v20 offset:57344\nv_add_f32 v53,v53,v133\nM v[220:223],a[44:47],v[88:91],v[220:223]\nds_read_b128 v[88:91],v20 offset:58368\n" \
"v_add_f32 v54,v54,v134\nM v[220:223],a[36:39],v[60:63],v[220:223]\ns_waitcnt lgkmcnt(3)\nds_read_b128 v[60:63],v20 offset:59392\nv_add_f32 v51,v51,v131\nM v[208:211],a[64:67],v[92:95],v[208:211]\nv_add_f32 v55,v55,v135\nv_lshlrev_b32 v64,16,v164\n" \
"M v[208:211],a[72:75],v[72:75],v[208:211]\nv_lshlrev_b32 v68,16,v166\nv_and_b32 v65,0xffff0000,v164\nM v[208:211],a[80:83],v[80:83],v[208:211]\ns_waitcnt lgkmcnt(3)\nv_and_b32 v69,0xffff0000,v166\nv_lshlrev_b32 v66,16,v165\nM v[208:211],a[88:91],v[84:87],v[208:211]\n" \
"v_lshlrev_b32 v70,16,v167\nv_and_b32 v67,0xffff0000,v165\nM v[212:215],a[92:95],v[84:87],v[212:215]\nds_read_b128 v[84:87],v20 offset:60416\nv_and_b32 v71,0xffff0000,v167\nM v[212:215],a[84:87],v[80:83],v[212:215]\nds_read_b128 v[80:83],v20 offset:53248\nC v160,v48,v49\n" \
"M v[212:215],a[76:79],v[72:75],v[212:215]\nds_read_b128 v[72:75],v20 offset:54272\nC v162,v52,v53\nM v[212:215],a[68:71],v[92:95],v[212:215]\ns_waitcnt lgkmcnt(3)\nds_read_b128 v[92:95],v20 offset:55296\nC v161,v50,v51\nM v[216:219],a[64:67],v[76:79],v[216:219]\n" \
"C v163,v54,v55\nv_mul_f32 v64,v25,v64\nM v[216:219],a[72:75],v[88:91],v[216:219]\nv_mul_f32 v68,v25,v68\nv_mul_f32 v65,v25,v65\nM v[216:219],a[80:83],v[60:63],v[216:219]\ns_waitcnt lgkmcnt(3)\nv_mul_f32 v69,v25,v69\n" \
"v_mul_f32 v66,v25,v66\nM v[216:219],a[88:91],v[84:87],v[216:219]\nv_mul_f32 v70,v25,v70\nv_mul_f32 v67,v25,v67\nM v[220:223],a[92:95],v[84:87],v[220:223]\nds_read_b128 v[84:87],v20 offset:56320\nv_mul_f32 v71,v25,v71\nM v[220:223],a[84:87],v[60:63],v[220:223]\n" \
"ds_read_b128 v[60:63],v20 offset:61440\nC v164,v64,v65\nM v[220:223],a[76:79],v[88:91],v[220:223]\nds_read_b128 v[88:91],v20 offset:62464\nC v166,v68,v69\nM v[220:223],a[68:71],v[76:79],v[220:223]\ns_waitcnt lgkmcnt(3)\nds_read_b128 v[76:79],v20 offset:63488\n" \
"C v165,v66,v67\nM v[208:211],a[96:99],v[80:83],v[208:211]\ns_waitcnt lgkmcnt(3)\nv_permlane16_swap_b32 v160,v162\nM v[208:211],a[104:107],v[72:75],v[208:211]\ns_waitcnt vmcnt(6) lgkmcnt(15)\nv_permlane16_swap_b32 v161,v163\nM v[208:211],a[112:115],v[92:95],v[208:211]\n" \
"s_barrier\nC v167,v70,v71\nM v[208:211],a[120:123],v[84:87],v[208:211]\nv_lshlrev_b32 v64,16,v164\nv_lshlrev_b32 v68,16,v166\nM v[212:215],a[124:127],v[84:87],v[212:215]\nds_read_b128 v[84:87],v20 offset:64512\nv_and_b32 v65,0xffff0000,v164\n" \
"M v[212:215],a[116:119],v[92:95],v[212:215]\nds_read_b128 v[92:95],v4 offset:0\nv_and_b32 v69,0xffff0000,v166\nM v[212:215],a[108:111],v[72:75],v[212:215]\nds_read_b128 v[72:75],v4 offset:1024\nv_lshlrev_b32 v66,16,v165\nM v[212:215],a[100:103],v[80:83],v[212:215]\ns_waitcnt lgkmcnt(3)\n" \
"ds_read_b128 v[80:83],v4 offset:2048\nv_lshlrev_b32 v70,16,v167\nM v[216:219],a[96:99],v[60:63],v[216:219]\nv_and_b32 v67,0xffff0000,v165\nv_and_b32 v71,0xffff0000,v167\nM v[216:219],a[104:107],v[88:91],v[216:219]\nv_add_f32 v64,v64,v136\nv_add_f32 v68,v68,v140\n" \
"M v[216:219],a[112:115],v[76:79],v[216:219]\ns_waitcnt lgkmcnt(3)\nv_add_f32 v65,v65,v137\nv_add_f32 v69,v69,v141\nM v[216:219],a[120:123],v[84:87],v[216:219]\nv_add_f32 v66,v66,v138\nv_add_f32 v70,v70,v142\nM v[220:223],a[124:127],v[84:87],v[220:223]\n" \
"ds_read_b128 v[84:87],v4 offset:3072\nv_add_f32 v67,v67,v139\nM v[220:223],a[116:119],v[76:79],v[220:223]\nds_read_b128 v[76:79],v4 offset:8192\nv_add_f32 v71,v71,v143\nM v[220:223],a[108:111],v[88:91],v[220:223]\nds_read_b128 v[88:91],v4 offset:9216\nC v164,v64,v65\n" \
"M v[220:223],a[100:103],v[60:63],v[220:223]\ns_waitcnt lgkmcnt(3)\nds_read_b128 v[60:63],v4 offset:10240\nC v166,v68,v69\nM v[208:211],a[128:131],v[92:95],v[208:211]\nC v165,v66,v67\nC v167,v70,v71\nM v[208:211],a[136:139],v[72:75],v[208:211]\n" \
"v_permlane16_swap_b32 v164,v166\nM v[208:211],a[144:147],v[80:83],v[208:211]\ns_waitcnt lgkmcnt(3)\nv_permlane16_swap_b32 v165,v167\nM v[208:211],a[152:155],v[84:87],v[208:211]\nM v[212:215],a[156:159],v[84:87],v[212:215]\nds_read_b128 v[84:87],v4 offset:11264\nM v[212:215],a[148:151],v[80:83],v[212:215]\n" \
"ds_read_b128 v[80:83],v4 offset:4096\nM v[212:215],a[140:143],v[72:75],v[212:215]\nds_read_b128 v[72:75],v4 offset:5120\nM v[212:215],a[132:135],v[92:95],v[212:215]\ns_waitcnt lgkmcnt(3)\nds_read_b128 v[92:95],v4 offset:6144\nM v[216:219],a[128:131],v[76:79],v[216:219]\nM v[216:219],a[136:139],v[88:91],v[216:219]\n" \
"M v[216:219],a[144:147],v[60:63],v[216:219]\ns_waitcnt lgkmcnt(3)\nM v[216:219],a[152:155],v[84:87],v[216:219]\nM v[220:223],a[156:159],v[84:87],v[220:223]\nds_read_b128 v[84:87],v4 offset:7168\nM v[220:223],a[148:151],v[60:63],v[220:223]\nds_read_b128 v[60:63],v4 offset:12288\nM v[220:223],a[140:143],v[88:91],v[220:223]\n" \
"ds_read_b128 v[88:91],v4 offset:13312\nM v[220:223],a[132:135],v[76:79],v[220:223]\ns_waitcnt lgkmcnt(3)\nds_read_b128 v[76:79],v4 offset:14336\nM v[208:211],a[160:163],v[80:83],v[208:211]\nM v[208:211],a[168:171],v[72:75],v[208:211]\nM v[208:211],a[176:179],v[92:95],v[208:211]\ns_waitcnt lgkmcnt(3)\n" \
"M v[208:211],a[184:187],v[84:87],v[208:211]\nM v[212:215],a[188:191],v[84:87],v[212:215]\nds_read_b128 v[84:87],v4 offset:15360\nM v[212:215],a[180:183],v[92:95],v[212:215]\ns_add_u32 s63,s49,8192\nds_read_b128 v[92:95],v4 offset:32768\nM v[212:215],a[172:175],v[72:75],v[212:215]\nbuffer_load_dwordx4 v[40:43],v4,s[36:39],s63 offen offset:2048 sc1\n" \
"M v[212:215],a[164:167],v[80:83],v[212:215]\ns_waitcnt lgkmcnt(2)\nds_read_b128 v[72:75],v4 offset:33792\nds_read_b128 v[80:83],v4 offset:34816\nM v[216:219],a[160:163],v[60:63],v[216:219]\nM v[216:219],a[168:171],v[88:91],v[216:219]\ns_waitcnt lgkmcnt(3)\nM v[216:219],a[176:179],v[76:79],v[216:219]\n" \
"buffer_load_dwordx4 v[44:47],v4,s[36:39],s63 offen offset:3072 sc1\nM v[216:219],a[184:187],v[84:87],v[216:219]\nM v[220:223],a[188:191],v[84:87],v[220:223]\nds_read_b128 v[84:87],v4 offset:35840\nM v[220:223],a[180:183],v[76:79],v[220:223]\ns_waitcnt lgkmcnt(1)\nds_read_b128 v[76:79],v4 offset:40960\nM v[220:223],a[172:175],v[88:91],v[220:223]\n" \
"buffer_store_dwordx4 v[160:163],v13,s[88:91],s58 offen offset:0 sc1\nM v[220:223],a[164:167],v[60:63],v[220:223]\ns_add_u32 s58,s81,0\nds_read_b128 v[88:91],v4 offset:41984\nds_read_b128 v[60:63],v4 offset:43008\nM v[208:211],a[192:195],v[92:95],v[208:211]\nM v[208:211],a[200:203],v[72:75],v[208:211]\nM v[208:211],a[208:211],v[80:83],v[208:211]\n" \
"s_waitcnt lgkmcnt(3)\nM v[208:211],a[216:219],v[84:87],v[208:211]\nM v[212:215],a[220:223],v[84:87],v[212:215]\nds_read_b128 v[84:87],v4 offset:44032\nM v[212:215],a[212:215],v[80:83],v[212:215]\ns_waitcnt lgkmcnt(1)\nds_read_b128 v[80:83],v4 offset:36864\nM v[212:215],a[204:207],v[72:75],v[212:215]\n" \
"buffer_store_dwordx4 v[164:167],v13,s[88:91],s59 offen offset:0 sc1\nM v[212:215],a[196:199],v[92:95],v[212:215]\ns_add_u32 s59,s81,65536\nds_read_b128 v[72:75],v4 offset:37888\nds_read_b128 v[92:95],v4 offset:38912\nM v[216:219],a[192:195],v[76:79],v[216:219]\nM v[216:219],a[200:203],v[88:91],v[216:219]\nM v[216:219],a[208:211],v[60:63],v[216:219]\n" \
"s_waitcnt lgkmcnt(3)\nM v[216:219],a[216:219],v[84:87],v[216:219]\nM v[220:223],a[220:223],v[84:87],v[220:223]\nds_read_b128 v[84:87],v4 offset:39936\nM v[220:223],a[212:215],v[60:63],v[220:223]\nds_read_b128 v[60:63],v4 offset:45056\nM v[220:223],a[204:207],v[88:91],v[220:223]\nds_read_b128 v[88:91],v4 offset:46080\n" \
"M v[220:223],a[196:199],v[76:79],v[220:223]\ns_waitcnt lgkmcnt(3)\nds_read_b128 v[76:79],v4 offset:47104\nM v[208:211],a[224:227],v[80:83],v[208:211]\nM v[208:211],a[232:235],v[72:75],v[208:211]\nM v[208:211],a[240:243],v[92:95],v[208:211]\ns_waitcnt lgkmcnt(3)\nM v[208:211],a[248:251],v[84:87],v[208:211]\n" \
"M v[212:215],a[252:255],v[84:87],v[212:215]\nds_read_b128 v[84:87],v4 offset:48128\nM v[212:215],a[244:247],v[92:95],v[212:215]\nM v[212:215],a[236:239],v[72:75],v[212:215]\nM v[212:215],a[228:231],v[80:83],v[212:215]\ns_waitcnt lgkmcnt(1)\nM v[216:219],a[224:227],v[60:63],v[216:219]\nM v[216:219],a[232:235],v[88:91],v[216:219]\n" \
"M v[216:219],a[240:243],v[76:79],v[216:219]\ns_waitcnt lgkmcnt(0)\nM v[216:219],a[248:251],v[84:87],v[216:219]\nM v[220:223],a[252:255],v[84:87],v[220:223]\nM v[220:223],a[244:247],v[76:79],v[220:223]\nM v[220:223],a[236:239],v[88:91],v[220:223]\nM v[220:223],a[228:231],v[60:63],v[220:223]\nM v[208:211],v[186:189],v[176:179],v[208:211]\n" \
"M v[212:215],v[184:187],v[176:179],v[212:215]\nM v[216:219],v[186:189],v[180:183],v[216:219]\ns_waitcnt vmcnt(2)\nv_max_u32 v10,v40,v41\nv_max3_u32 v10,v10,v42,v43\nM v[220:223],v[184:187],v[180:183],v[220:223]\nv_max3_u32 v10,v10,v44,v45\nv_max3_u32 v10,v10,v46,v47\n" \
"M v[208:211],v[186:189],v[32:35],v[208:211]\nv_cmp_eq_u32 vcc,-1,v10\nM v[212:215],v[184:187],v[32:35],v[212:215]\nM v[216:219],v[186:189],v[36:39],v[216:219]\ns_cbranch_vccnz LIO_%=\nLIOR_%=:\nM v[220:223],v[184:187],v[36:39],v[220:223]\nM v[208:211],v[186:189],v[40:43],v[208:211]\n" \
"M v[212:215],v[184:187],v[40:43],v[212:215]\nM v[216:219],v[186:189],v[44:47],v[216:219]\nC v160,v208,v209\nC v161,v210,v211\nM v[220:223],v[184:187],v[44:47],v[220:223]\nC v162,v212,v213\nC v163,v214,v215\nv_lshlrev_b32 v208,16,v160\n" \
"v_lshlrev_b32 v210,16,v161\nv_lshlrev_b32 v212,16,v162\nv_lshlrev_b32 v214,16,v163\nv_and_b32 v209,0xffff0000,v160\nv_and_b32 v211,0xffff0000,v161\nv_and_b32 v213,0xffff0000,v162\nv_and_b32 v215,0xffff0000,v163\nv_pk_mul_f32 v[208:209],v[208:209],v[26:27] op_sel_hi:[1,0]\n" \
"v_pk_mul_f32 v[210:211],v[210:211],v[26:27] op_sel_hi:[1,0]\nv_pk_mul_f32 v[212:213],v[212:213],v[26:27] op_sel_hi:[1,0]\nv_pk_mul_f32 v[214:215],v[214:215],v[26:27] op_sel_hi:[1,0]\nC v160,v208,v209\nC v161,v210,v211\nC v162,v212,v213\nC v163,v214,v215\nv_lshlrev_b32 v208,16,v160\n" \
"v_lshlrev_b32 v210,16,v161\nv_lshlrev_b32 v212,16,v162\nv_lshlrev_b32 v214,16,v163\nv_and_b32 v209,0xffff0000,v160\nv_and_b32 v211,0xffff0000,v161\nv_and_b32 v213,0xffff0000,v162\nv_and_b32 v215,0xffff0000,v163\nv_pk_add_f32 v[208:209],v[208:209],v[144:145]\n" \
"v_pk_add_f32 v[210:211],v[210:211],v[146:147]\nv_pk_add_f32 v[212:213],v[212:213],v[148:149]\nv_pk_add_f32 v[214:215],v[214:215],v[150:151]\nC v160,v208,v209\nC v161,v210,v211\nC v162,v212,v213\nC v163,v214,v215\ns_nop 0\n" \
"v_permlane16_swap_b32 v160,v162\nv_permlane16_swap_b32 v161,v163\nbuffer_store_dwordx4 v[160:163],v13,s[88:91],s58 offen offset:0 sc1\nC v164,v216,v217\nC v165,v218,v219\nC v166,v220,v221\nC v167,v222,v223\nv_lshlrev_b32 v216,16,v164\n" \
"v_lshlrev_b32 v218,16,v165\nv_lshlrev_b32 v220,16,v166\nv_lshlrev_b32 v222,16,v167\nv_and_b32 v217,0xffff0000,v164\nv_and_b32 v219,0xffff0000,v165\nv_and_b32 v221,0xffff0000,v166\nv_and_b32 v223,0xffff0000,v167\nv_pk_mul_f32 v[216:217],v[216:217],v[26:27] op_sel:[0,1] op_sel_hi:[1,1]\n" \
"v_pk_mul_f32 v[218:219],v[218:219],v[26:27] op_sel:[0,1] op_sel_hi:[1,1]\nv_pk_mul_f32 v[220:221],v[220:221],v[26:27] op_sel:[0,1] op_sel_hi:[1,1]\nv_pk_mul_f32 v[222:223],v[222:223],v[26:27] op_sel:[0,1] op_sel_hi:[1,1]\nC v164,v216,v217\nC v165,v218,v219\nC v166,v220,v221\nC v167,v222,v223\nv_lshlrev_b32 v216,16,v164\n" \
"v_lshlrev_b32 v218,16,v165\nv_lshlrev_b32 v220,16,v166\nv_lshlrev_b32 v222,16,v167\nv_and_b32 v217,0xffff0000,v164\nv_and_b32 v219,0xffff0000,v165\nv_and_b32 v221,0xffff0000,v166\nv_and_b32 v223,0xffff0000,v167\nv_pk_add_f32 v[216:217],v[216:217],v[152:153]\n" \
"v_pk_add_f32 v[218:219],v[218:219],v[154:155]\nv_pk_add_f32 v[220:221],v[220:221],v[156:157]\nv_pk_add_f32 v[222:223],v[222:223],v[158:159]\nC v164,v216,v217\nC v165,v218,v219\nC v166,v220,v221\nC v167,v222,v223\ns_nop 0\n" \
"v_permlane16_swap_b32 v164,v166\nv_permlane16_swap_b32 v165,v167\nbuffer_store_dwordx4 v[164:167],v13,s[88:91],s59 offen offset:0 sc1\ns_endpgm\n.p2align 12\n.p2align 6\nLG26_%=:\ns_cbranch_execz LHR1_%=\n" \
"LH1_%=:\nds_read_b128 a[128:131],v21 offset:8192\nds_read_b128 a[132:135],v21 offset:9216\nds_read_b128 a[136:139],v21 offset:10240\nds_read_b128 a[140:143],v21 offset:11264\nds_read_b128 a[144:147],v21 offset:12288\nds_read_b128 a[148:151],v21 offset:13312\nds_read_b128 a[152:155],v21 offset:14336\n" \
"ds_read_b128 a[156:159],v21 offset:15360\nds_read_b128 a[160:163],v21 offset:0\nds_read_b128 a[164:167],v21 offset:1024\nds_read_b128 a[168:171],v21 offset:2048\nds_read_b128 a[172:175],v21 offset:3072\nds_read_b128 a[176:179],v21 offset:4096\nds_read_b128 a[180:183],v21 offset:5120\nds_read_b128 a[184:187],v21 offset:6144\n" \
"ds_read_b128 a[188:191],v21 offset:7168\nv_max3_f32 v60,v96,v97,v98\nv_max3_f32 v61,v99,v104,v105\nv_max3_f32 v60,v60,v106,v107\nv_max3_f32 v61,v61,v112,v113\nv_max3_f32 v60,v60,v114,v115\nv_max3_f32 v61,v61,v120,v121\nv_max3_f32 v60,v60,v122,v123\n" \
"v_max3_f32 v61,v61,v100,v101\nv_max3_f32 v60,v60,v102,v103\nv_max3_f32 v61,v61,v108,v109\nv_max3_f32 v60,v60,v110,v111\nv_max3_f32 v61,v61,v116,v117\nv_max3_f32 v60,v60,v118,v119\nv_max3_f32 v61,v61,v124,v125\nv_max3_f32 v61,v61,v126,v127\n" \
"v_max_f32 v60,v60,v61\ns_mov_b32 s75,1\nv_cmp_nge_f32 vcc,0x41f00000,v60\nv_xor_b32 v13,0x2000,v13\ns_cbranch_vccnz LRS_%=\nLH1X_%=:\nv_pk_mov_b32 v[32:33],v[28:29],v[28:29] op_sel:[0,0]\nv_pk_mov_b32 v[34:35],v[28:29],v[28:29] op_sel:[0,0]\n" \
"v_pk_mov_b32 v[36:37],v[28:29],v[28:29] op_sel:[1,1]\nv_pk_mov_b32 v[38:39],v[28:29],v[28:29] op_sel:[1,1]\nv_pk_mov_b32 v[40:41],v[30:31],v[30:31] op_sel:[0,0]\nv_pk_mov_b32 v[42:43],v[30:31],v[30:31] op_sel:[0,0]\nv_pk_mov_b32 v[44:45],v[30:31],v[30:31] op_sel:[1,1]\nv_pk_mov_b32 v[46:47],v[30:31],v[30:31] op_sel:[1,1]\ns_cmp_eq_u32 s52,-1\ns_cbranch_scc1 LH1R0_%=\n" \
"s_cmp_eq_u32 s52,0\ns_cbranch_scc1 LH1R1_%=\ns_cmp_eq_u32 s52,9\ns_cbranch_scc1 LH1R10_%=\ns_cmp_eq_u32 s52,11\ns_cbranch_scc1 LH1R12_%=\ns_cmp_eq_u32 s52,13\ns_cbranch_scc1 LH1R14_%=\n" \
"s_branch LH1R2_%=\n.p2align 6\nLG27_%=:\ns_cbranch_execz LHR1_%=\nLEV_%=:\ns_cmp_eq_u32 s52,s4\ns_cbranch_scc0 LEV3_%=\ns_cmp_lt_u32 %2,2\n" \
"s_cbranch_scc1 LEV2_%=\ns_mov_b64 exec,0\ns_cmp_eq_u32 %2,2\ns_cbranch_scc1 LG20_%=\ns_cmp_eq_u32 %2,3\ns_cbranch_scc1 LG21_%=\nLHR3_%=:\ns_mov_b64 exec,-1\n" \
"s_mov_b32 s4,50\ns_mov_b32 s56,50\ns_branch LEV4_%=\nLEV2_%=:\nv_rcp_f32 v60,v24\nv_rcp_f32 v61,v25\nv_rcp_f32 v62,v26\nv_rcp_f32 v63,v27\n" \
"v_xor_b32 v14,0x2000,v14\ns_mov_b32 s75,0\ns_mov_b32 s77,s68\nv_mov_b32 v24,0\nv_mov_b32 v25,0\nv_mov_b32 v26,0\nv_mov_b32 v27,0\nv_pk_mul_f32 v[128:129],v[128:129],v[60:61] op_sel_hi:[1,0]\n" \
"v_pk_mul_f32 v[130:131],v[130:131],v[60:61] op_sel_hi:[1,0]\nv_pk_mul_f32 v[132:133],v[132:133],v[60:61] op_sel_hi:[1,0]\nv_pk_mul_f32 v[134:135],v[134:135],v[60:61] op_sel_hi:[1,0]\nv_pk_mul_f32 v[136:137],v[136:137],v[60:61] op_sel_hi:[1,0]\nv_pk_mul_f32 v[138:139],v[138:139],v[60:61] op_sel_hi:[1,0]\nv_pk_mul_f32 v[140:141],v[140:141],v[60:61] op_sel_hi:[1,0]\nv_pk_mul_f32 v[142:143],v[142:143],v[60:61] op_sel_hi:[1,0]\nv_pk_mul_f32 v[144:145],v[144:145],v[60:61] op_sel_hi:[1,0]\n" \
"v_pk_mul_f32 v[146:147],v[146:147],v[60:61] op_sel_hi:[1,0]\nv_pk_mul_f32 v[148:149],v[148:149],v[60:61] op_sel_hi:[1,0]\nv_pk_mul_f32 v[150:151],v[150:151],v[60:61] op_sel_hi:[1,0]\nv_pk_mul_f32 v[152:153],v[152:153],v[60:61] op_sel_hi:[1,0]\nv_pk_mul_f32 v[154:155],v[154:155],v[60:61] op_sel_hi:[1,0]\nv_pk_mul_f32 v[156:157],v[156:157],v[60:61] op_sel_hi:[1,0]\nv_pk_mul_f32 v[158:159],v[158:159],v[60:61] op_sel_hi:[1,0]\nv_pk_mul_f32 v[160:161],v[160:161],v[60:61] op_sel:[0,1] op_sel_hi:[1,1]\n" \
"v_pk_mul_f32 v[162:163],v[162:163],v[60:61] op_sel:[0,1] op_sel_hi:[1,1]\nv_pk_mul_f32 v[164:165],v[164:165],v[60:61] op_sel:[0,1] op_sel_hi:[1,1]\nv_pk_mul_f32 v[166:167],v[166:167],v[60:61] op_sel:[0,1] op_sel_hi:[1,1]\nv_pk_mul_f32 v[168:169],v[168:169],v[60:61] op_sel:[0,1] op_sel_hi:[1,1]\nv_pk_mul_f32 v[170:171],v[170:171],v[60:61] op_sel:[0,1] op_sel_hi:[1,1]\nv_pk_mul_f32 v[172:173],v[172:173],v[60:61] op_sel:[0,1] op_sel_hi:[1,1]\nv_pk_mul_f32 v[174:175],v[174:175],v[60:61] op_sel:[0,1] op_sel_hi:[1,1]\nv_pk_mul_f32 v[176:177],v[176:177],v[60:61] op_sel:[0,1] op_sel_hi:[1,1]\n" \
"v_pk_mul_f32 v[178:179],v[178:179],v[60:61] op_sel:[0,1] op_sel_hi:[1,1]\nv_pk_mul_f32 v[180:181],v[180:181],v[60:61] op_sel:[0,1] op_sel_hi:[1,1]\nv_pk_mul_f32 v[182:183],v[182:183],v[60:61] op_sel:[0,1] op_sel_hi:[1,1]\nv_pk_mul_f32 v[184:185],v[184:185],v[60:61] op_sel:[0,1] op_sel_hi:[1,1]\nv_pk_mul_f32 v[186:187],v[186:187],v[60:61] op_sel:[0,1] op_sel_hi:[1,1]\nv_pk_mul_f32 v[188:189],v[188:189],v[60:61] op_sel:[0,1] op_sel_hi:[1,1]\nv_pk_mul_f32 v[190:191],v[190:191],v[60:61] op_sel:[0,1] op_sel_hi:[1,1]\nv_pk_mul_f32 v[192:193],v[192:193],v[62:63] op_sel_hi:[1,0]\n" \
"v_pk_mul_f32 v[194:195],v[194:195],v[62:63] op_sel_hi:[1,0]\nv_pk_mul_f32 v[196:197],v[196:197],v[62:63] op_sel_hi:[1,0]\nv_pk_mul_f32 v[198:199],v[198:199],v[62:63] op_sel_hi:[1,0]\nv_pk_mul_f32 v[200:201],v[200:201],v[62:63] op_sel_hi:[1,0]\nv_pk_mul_f32 v[202:203],v[202:203],v[62:63] op_sel_hi:[1,0]\nv_pk_mul_f32 v[204:205],v[204:205],v[62:63] op_sel_hi:[1,0]\nv_pk_mul_f32 v[206:207],v[206:207],v[62:63] op_sel_hi:[1,0]\nv_pk_mul_f32 v[208:209],v[208:209],v[62:63] op_sel_hi:[1,0]\n" \
"v_pk_mul_f32 v[210:211],v[210:211],v[62:63] op_sel_hi:[1,0]\nv_pk_mul_f32 v[212:213],v[212:213],v[62:63] op_sel_hi:[1,0]\nv_pk_mul_f32 v[214:215],v[214:215],v[62:63] op_sel_hi:[1,0]\nv_pk_mul_f32 v[216:217],v[216:217],v[62:63] op_sel_hi:[1,0]\nv_pk_mul_f32 v[218:219],v[218:219],v[62:63] op_sel_hi:[1,0]\nv_pk_mul_f32 v[220:221],v[220:221],v[62:63] op_sel_hi:[1,0]\nv_pk_mul_f32 v[222:223],v[222:223],v[62:63] op_sel_hi:[1,0]\nv_pk_mul_f32 v[224:225],v[224:225],v[62:63] op_sel:[0,1] op_sel_hi:[1,1]\n" \
"v_pk_mul_f32 v[226:227],v[226:227],v[62:63] op_sel:[0,1] op_sel_hi:[1,1]\nv_pk_mul_f32 v[228:229],v[228:229],v[62:63] op_sel:[0,1] op_sel_hi:[1,1]\nv_pk_mul_f32 v[230:231],v[230:231],v[62:63] op_sel:[0,1] op_sel_hi:[1,1]\nv_pk_mul_f32 v[232:233],v[232:233],v[62:63] op_sel:[0,1] op_sel_hi:[1,1]\nv_pk_mul_f32 v[234:235],v[234:235],v[62:63] op_sel:[0,1] op_sel_hi:[1,1]\nv_pk_mul_f32 v[236:237],v[236:237],v[62:63] op_sel:[0,1] op_sel_hi:[1,1]\nv_pk_mul_f32 v[238:239],v[238:239],v[62:63] op_sel:[0,1] op_sel_hi:[1,1]\nv_pk_mul_f32 v[240:241],v[240:241],v[62:63] op_sel:[0,1] op_sel_hi:[1,1]\n" \
"v_pk_mul_f32 v[242:243],v[242:243],v[62:63] op_sel:[0,1] op_sel_hi:[1,1]\nv_pk_mul_f32 v[244:245],v[244:245],v[62:63] op_sel:[0,1] op_sel_hi:[1,1]\nv_pk_mul_f32 v[246:247],v[246:247],v[62:63] op_sel:[0,1] op_sel_hi:[1,1]\nv_pk_mul_f32 v[248:249],v[248:249],v[62:63] op_sel:[0,1] op_sel_hi:[1,1]\nv_pk_mul_f32 v[250:251],v[250:251],v[62:63] op_sel:[0,1] op_sel_hi:[1,1]\nv_pk_mul_f32 v[252:253],v[252:253],v[62:63] op_sel:[0,1] op_sel_hi:[1,1]\nv_pk_mul_f32 v[254:255],v[254:255],v[62:63] op_sel:[0,1] op_sel_hi:[1,1]\nC v128,v128,v129\n" \
"C v129,v130,v131\nC v130,v132,v133\nC v131,v134,v135\nbuffer_store_dwordx4 v[128:131],v4,s[36:39],s77 offen offset:0 sc1\nC v136,v136,v137\nC v137,v138,v139\nC v138,v140,v141\nC v139,v142,v143\n" \
"buffer_store_dwordx4 v[136:139],v4,s[36:39],s77 offen offset:1024 sc1\nC v144,v144,v145\nC v145,v146,v147\nC v146,v148,v149\nC v147,v150,v151\nv_mfma_f32_32x32x16_bf16 v[128:143],v[24:27],v[24:27],0\nbuffer_store_dwordx4 v[144:147],v4,s[36:39],s77 offen offset:2048 sc1\nC v152,v152,v153\n" \
"C v153,v154,v155\nC v154,v156,v157\nC v155,v158,v159\nbuffer_store_dwordx4 v[152:155],v4,s[36:39],s77 offen offset:3072 sc1\ns_add_u32 s77,s77,262144\nC v160,v160,v161\nC v161,v162,v163\nC v162,v164,v165\n" \
"C v163,v166,v167\nv_mfma_f32_32x32x16_bf16 v[144:159],v[24:27],v[24:27],0\nbuffer_store_dwordx4 v[160:163],v4,s[36:39],s77 offen offset:0 sc1\nC v168,v168,v169\nC v169,v170,v171\nC v170,v172,v173\nC v171,v174,v175\nbuffer_store_dwordx4 v[168:171],v4,s[36:39],s77 offen offset:1024 sc1\n" \
"C v176,v176,v177\nC v177,v178,v179\nC v178,v180,v181\nC v179,v182,v183\nv_mfma_f32_32x32x16_bf16 v[160:175],v[24:27],v[24:27],0\nbuffer_store_dwordx4 v[176:179],v4,s[36:39],s77 offen offset:2048 sc1\nC v184,v184,v185\nC v185,v186,v187\n" \
"C v186,v188,v189\nC v187,v190,v191\nbuffer_store_dwordx4 v[184:187],v4,s[36:39],s77 offen offset:3072 sc1\ns_add_u32 s77,s77,262144\nC v192,v192,v193\nC v193,v194,v195\nC v194,v196,v197\nC v195,v198,v199\n" \
"v_mfma_f32_32x32x16_bf16 v[176:191],v[24:27],v[24:27],0\nbuffer_store_dwordx4 v[192:195],v4,s[36:39],s77 offen offset:0 sc1\nC v200,v200,v201\nC v201,v202,v203\nC v202,v204,v205\nC v203,v206,v207\nbuffer_store_dwordx4 v[200:203],v4,s[36:39],s77 offen offset:1024 sc1\nC v208,v208,v209\n" \
"C v209,v210,v211\nC v210,v212,v213\nC v211,v214,v215\nv_mfma_f32_32x32x16_bf16 v[192:207],v[24:27],v[24:27],0\nbuffer_store_dwordx4 v[208:211],v4,s[36:39],s77 offen offset:2048 sc1\nC v216,v216,v217\nC v217,v218,v219\nC v218,v220,v221\n" \
"C v219,v222,v223\nbuffer_store_dwordx4 v[216:219],v4,s[36:39],s77 offen offset:3072 sc1\ns_add_u32 s77,s77,262144\nC v224,v224,v225\nC v225,v226,v227\nC v226,v228,v229\nC v227,v230,v231\nv_mfma_f32_32x32x16_bf16 v[208:223],v[24:27],v[24:27],0\n" \
"buffer_store_dwordx4 v[224:227],v4,s[36:39],s77 offen offset:0 sc1\nC v232,v232,v233\nC v233,v234,v235\nC v234,v236,v237\nC v235,v238,v239\nbuffer_store_dwordx4 v[232:235],v4,s[36:39],s77 offen offset:1024 sc1\nC v240,v240,v241\nC v241,v242,v243\n" \
"C v242,v244,v245\nC v243,v246,v247\nv_mfma_f32_32x32x16_bf16 v[224:239],v[24:27],v[24:27],0\nbuffer_store_dwordx4 v[240:243],v4,s[36:39],s77 offen offset:2048 sc1\nC v248,v248,v249\nC v249,v250,v251\nC v250,v252,v253\nC v251,v254,v255\n" \
"buffer_store_dwordx4 v[248:251],v4,s[36:39],s77 offen offset:3072 sc1\ns_add_u32 s77,s77,262144\ns_nop 0\nv_mfma_f32_32x32x16_bf16 v[240:255],v[24:27],v[24:27],0\ns_add_u32 s56,s4,1\ns_branch LEV4_%=\n.p2align 6\nLG28_%=:\n" \
"s_cbranch_execz LHR1_%=\nLEV3_%=:\ns_mov_b32 s56,s4\ns_cmp_lt_u32 s52,s4\ns_cbranch_scc1 LEV4_%=\ns_mov_b32 s56,50\ns_add_u32 s77,s4,1\ns_cmp_eq_u32 s52,s77\n" \
"s_cbranch_scc0 LEV4_%=\nv_lshrrev_b32 v62,5,v4\nv_mov_b32 v60,s10\nv_lshlrev_b32 v62,27,v62\nv_mov_b32 v61,s11\nv_lshl_add_u32 v62,v4,5,v62\ns_waitcnt vmcnt(16)\nbuffer_store_dwordx2 v[60:61],v62,s[36:39],s69 offen offset:0 sc1\n" \
"LEV4_%=:\ns_cmp_lt_u32 s52,8\ns_cbranch_scc0 LEV5_%=\ns_min_u32 s56,s56,8\nLEV5_%=:\ns_cmp_eq_u32 s52,8\ns_cbranch_scc0 LEV6_%=\ns_mov_b64 exec,0\n" \
"s_cmp_eq_u32 %2,0\ns_cbranch_scc1 LG16_%=\ns_cmp_eq_u32 %2,1\ns_cbranch_scc1 LG17_%=\ns_cmp_eq_u32 %2,2\ns_cbranch_scc1 LG18_%=\ns_cmp_eq_u32 %2,3\ns_cbranch_scc1 LG19_%=\n" \
"LHR2_%=:\ns_mov_b64 exec,-1\nLEV6_%=:\ns_cmp_eq_u32 s52,0\ns_cbranch_scc1 LEVR0_%=\ns_cmp_eq_u32 s52,8\ns_cbranch_scc1 LEVR8_%=\ns_cmp_eq_u32 s52,9\n" \
"s_cbranch_scc1 LEVR9_%=\ns_cmp_eq_u32 s52,10\ns_cbranch_scc1 LEVR10_%=\ns_cmp_eq_u32 s52,11\ns_cbranch_scc1 LEVR11_%=\ns_cmp_eq_u32 s52,12\ns_cbranch_scc1 LEVR12_%=\ns_cmp_eq_u32 s52,13\n" \
"s_cbranch_scc1 LEVR13_%=\ns_cmp_eq_u32 s52,14\ns_cbranch_scc1 LEVR14_%=\ns_cmp_eq_u32 s52,15\ns_cbranch_scc1 LEVR15_%=\ns_branch LEVR1_%=\nLRS_%=:\ns_nop 7\n" \
"v_max3_f32 v60,v96,v97,v98\nv_max3_f32 v61,v99,v100,v101\nv_max3_f32 v60,v60,v102,v103\nv_max3_f32 v60,v60,v61,0\nv_mov_b32 v61,v60\ns_nop 1\nv_permlane32_swap_b32 v60,v61\nv_max_f32 v60,v60,v61\n" \
"v_mov_b32 v61,v60\ns_nop 1\nv_permlane16_swap_b32 v60,v61\nv_max_f32 v60,v60,v61\nv_exp_f32 v62,-v60\nv_sub_f32 v96,v96,v60\nv_sub_f32 v97,v97,v60\nv_sub_f32 v98,v98,v60\n" \
"v_sub_f32 v99,v99,v60\nv_sub_f32 v100,v100,v60\nv_sub_f32 v101,v101,v60\nv_sub_f32 v102,v102,v60\nv_sub_f32 v103,v103,v60\nv_sub_f32 v32,v32,v60\nv_sub_f32 v33,v33,v60\nv_sub_f32 v34,v34,v60\n" \
"v_sub_f32 v35,v35,v60\nv_mul_f32 v16,v16,v62\nv_mul_f32 v128,v128,v62\nv_mul_f32 v129,v129,v62\nv_mul_f32 v130,v130,v62\nv_mul_f32 v131,v131,v62\nv_mul_f32 v132,v132,v62\nv_mul_f32 v133,v133,v62\n" \
"v_mul_f32 v134,v134,v62\nv_mul_f32 v135,v135,v62\nv_mul_f32 v136,v136,v62\nv_mul_f32 v137,v137,v62\nv_mul_f32 v138,v138,v62\nv_mul_f32 v139,v139,v62\nv_mul_f32 v140,v140,v62\nv_mul_f32 v141,v141,v62\n" \
"v_mul_f32 v142,v142,v62\nv_mul_f32 v143,v143,v62\nv_mul_f32 v144,v144,v62\nv_mul_f32 v145,v145,v62\nv_mul_f32 v146,v146,v62\nv_mul_f32 v147,v147,v62\nv_mul_f32 v148,v148,v62\nv_mul_f32 v149,v149,v62\n" \
"v_mul_f32 v150,v150,v62\nv_mul_f32 v151,v151,v62\nv_mul_f32 v152,v152,v62\nv_mul_f32 v153,v153,v62\nv_mul_f32 v154,v154,v62\nv_mul_f32 v155,v155,v62\nv_mul_f32 v156,v156,v62\nv_mul_f32 v157,v157,v62\n" \
"v_mul_f32 v158,v158,v62\nv_mul_f32 v159,v159,v62\nv_lshlrev_b32 v61,16,v64\nv_and_b32 v63,0xffff0000,v64\nv_mul_f32 v61,v61,v62\nv_mul_f32 v63,v63,v62\nC v64,v61,v63\nv_lshlrev_b32 v61,16,v65\n" \
"v_and_b32 v63,0xffff0000,v65\nv_mul_f32 v61,v61,v62\nv_mul_f32 v63,v63,v62\nC v65,v61,v63\nv_lshlrev_b32 v61,16,v66\nv_and_b32 v63,0xffff0000,v66\nv_mul_f32 v61,v61,v62\nv_mul_f32 v63,v63,v62\n" \
"C v66,v61,v63\nv_lshlrev_b32 v61,16,v67\nv_and_b32 v63,0xffff0000,v67\nv_mul_f32 v61,v61,v62\nv_mul_f32 v63,v63,v62\nC v67,v61,v63\nv_max3_f32 v60,v104,v105,v106\nv_max3_f32 v61,v107,v108,v109\n" \
"v_max3_f32 v60,v60,v110,v111\nv_max3_f32 v60,v60,v61,0\nv_mov_b32 v61,v60\ns_nop 1\nv_permlane32_swap_b32 v60,v61\nv_max_f32 v60,v60,v61\nv_mov_b32 v61,v60\ns_nop 1\n" \
"v_permlane16_swap_b32 v60,v61\nv_max_f32 v60,v60,v61\nv_exp_f32 v62,-v60\nv_sub_f32 v104,v104,v60\nv_sub_f32 v105,v105,v60\nv_sub_f32 v106,v106,v60\nv_sub_f32 v107,v107,v60\nv_sub_f32 v108,v108,v60\n" \
"v_sub_f32 v109,v109,v60\nv_sub_f32 v110,v110,v60\nv_sub_f32 v111,v111,v60\nv_sub_f32 v36,v36,v60\nv_sub_f32 v37,v37,v60\nv_sub_f32 v38,v38,v60\nv_sub_f32 v39,v39,v60\nv_mul_f32 v17,v17,v62\n" \
"v_mul_f32 v160,v160,v62\nv_mul_f32 v161,v161,v62\nv_mul_f32 v162,v162,v62\nv_mul_f32 v163,v163,v62\nv_mul_f32 v164,v164,v62\nv_mul_f32 v165,v165,v62\nv_mul_f32 v166,v166,v62\nv_mul_f32 v167,v167,v62\n" \
"v_mul_f32 v168,v168,v62\nv_mul_f32 v169,v169,v62\nv_mul_f32 v170,v170,v62\nv_mul_f32 v171,v171,v62\nv_mul_f32 v172,v172,v62\nv_mul_f32 v173,v173,v62\nv_mul_f32 v174,v174,v62\nv_mul_f32 v175,v175,v62\n" \
"v_mul_f32 v176,v176,v62\nv_mul_f32 v177,v177,v62\nv_mul_f32 v178,v178,v62\nv_mul_f32 v179,v179,v62\nv_mul_f32 v180,v180,v62\nv_mul_f32 v181,v181,v62\nv_mul_f32 v182,v182,v62\nv_mul_f32 v183,v183,v62\n" \
"v_mul_f32 v184,v184,v62\nv_mul_f32 v185,v185,v62\nv_mul_f32 v186,v186,v62\nv_mul_f32 v187,v187,v62\nv_mul_f32 v188,v188,v62\nv_mul_f32 v189,v189,v62\nv_mul_f32 v190,v190,v62\nv_mul_f32 v191,v191,v62\n" \
"v_lshlrev_b32 v61,16,v72\nv_and_b32 v63,0xffff0000,v72\nv_mul_f32 v61,v61,v62\nv_mul_f32 v63,v63,v62\nC v72,v61,v63\nv_lshlrev_b32 v61,16,v73\nv_and_b32 v63,0xffff0000,v73\nv_mul_f32 v61,v61,v62\n" \
"v_mul_f32 v63,v63,v62\nC v73,v61,v63\nv_lshlrev_b32 v61,16,v74\nv_and_b32 v63,0xffff0000,v74\nv_mul_f32 v61,v61,v62\nv_mul_f32 v63,v63,v62\nC v74,v61,v63\nv_lshlrev_b32 v61,16,v75\n" \
"v_and_b32 v63,0xffff0000,v75\nv_mul_f32 v61,v61,v62\nv_mul_f32 v63,v63,v62\nC v75,v61,v63\nv_max3_f32 v60,v112,v113,v114\nv_max3_f32 v61,v115,v116,v117\nv_max3_f32 v60,v60,v118,v119\nv_max3_f32 v60,v60,v61,0\n" \
"v_mov_b32 v61,v60\ns_nop 1\nv_permlane32_swap_b32 v60,v61\nv_max_f32 v60,v60,v61\nv_mov_b32 v61,v60\ns_nop 1\nv_permlane16_swap_b32 v60,v61\nv_max_f32 v60,v60,v61\n" \
"v_exp_f32 v62,-v60\nv_sub_f32 v112,v112,v60\nv_sub_f32 v113,v113,v60\nv_sub_f32 v114,v114,v60\nv_sub_f32 v115,v115,v60\nv_sub_f32 v116,v116,v60\nv_sub_f32 v117,v117,v60\nv_sub_f32 v118,v118,v60\n" \
"v_sub_f32 v119,v119,v60\nv_sub_f32 v40,v40,v60\nv_sub_f32 v41,v41,v60\nv_sub_f32 v42,v42,v60\nv_sub_f32 v43,v43,v60\nv_mul_f32 v18,v18,v62\nv_mul_f32 v192,v192,v62\nv_mul_f32 v193,v193,v62\n" \
"v_mul_f32 v194,v194,v62\nv_mul_f32 v195,v195,v62\nv_mul_f32 v196,v196,v62\nv_mul_f32 v197,v197,v62\nv_mul_f32 v198,v198,v62\nv_mul_f32 v199,v199,v62\nv_mul_f32 v200,v200,v62\nv_mul_f32 v201,v201,v62\n" \
"v_mul_f32 v202,v202,v62\nv_mul_f32 v203,v203,v62\nv_mul_f32 v204,v204,v62\nv_mul_f32 v205,v205,v62\nv_mul_f32 v206,v206,v62\nv_mul_f32 v207,v207,v62\nv_mul_f32 v208,v208,v62\nv_mul_f32 v209,v209,v62\n" \
"v_mul_f32 v210,v210,v62\nv_mul_f32 v211,v211,v62\nv_mul_f32 v212,v212,v62\nv_mul_f32 v213,v213,v62\nv_mul_f32 v214,v214,v62\nv_mul_f32 v215,v215,v62\nv_mul_f32 v216,v216,v62\nv_mul_f32 v217,v217,v62\n" \
"v_mul_f32 v218,v218,v62\nv_mul_f32 v219,v219,v62\nv_mul_f32 v220,v220,v62\nv_mul_f32 v221,v221,v62\nv_mul_f32 v222,v222,v62\nv_mul_f32 v223,v223,v62\nv_lshlrev_b32 v61,16,v80\nv_and_b32 v63,0xffff0000,v80\n" \
"v_mul_f32 v61,v61,v62\nv_mul_f32 v63,v63,v62\nC v80,v61,v63\nv_lshlrev_b32 v61,16,v81\nv_and_b32 v63,0xffff0000,v81\nv_mul_f32 v61,v61,v62\nv_mul_f32 v63,v63,v62\nC v81,v61,v63\n" \
"v_lshlrev_b32 v61,16,v82\nv_and_b32 v63,0xffff0000,v82\nv_mul_f32 v61,v61,v62\nv_mul_f32 v63,v63,v62\nC v82,v61,v63\nv_lshlrev_b32 v61,16,v83\nv_and_b32 v63,0xffff0000,v83\nv_mul_f32 v61,v61,v62\n" \
"v_mul_f32 v63,v63,v62\nC v83,v61,v63\nv_max3_f32 v60,v120,v121,v122\nv_max3_f32 v61,v123,v124,v125\nv_max3_f32 v60,v60,v126,v127\nv_max3_f32 v60,v60,v61,0\nv_mov_b32 v61,v60\ns_nop 1\n" \
"v_permlane32_swap_b32 v60,v61\nv_max_f32 v60,v60,v61\nv_mov_b32 v61,v60\ns_nop 1\nv_permlane16_swap_b32 v60,v61\nv_max_f32 v60,v60,v61\nv_exp_f32 v62,-v60\nv_sub_f32 v120,v120,v60\n" \
"v_sub_f32 v121,v121,v60\nv_sub_f32 v122,v122,v60\nv_sub_f32 v123,v123,v60\nv_sub_f32 v124,v124,v60\nv_sub_f32 v125,v125,v60\nv_sub_f32 v126,v126,v60\nv_sub_f32 v127,v127,v60\nv_sub_f32 v44,v44,v60\n" \
"v_sub_f32 v45,v45,v60\nv_sub_f32 v46,v46,v60\nv_sub_f32 v47,v47,v60\nv_mul_f32 v19,v19,v62\nv_mul_f32 v224,v224,v62\nv_mul_f32 v225,v225,v62\nv_mul_f32 v226,v226,v62\nv_mul_f32 v227,v227,v62\n" \
"v_mul_f32 v228,v228,v62\nv_mul_f32 v229,v229,v62\nv_mul_f32 v230,v230,v62\nv_mul_f32 v231,v231,v62\nv_mul_f32 v232,v232,v62\nv_mul_f32 v233,v233,v62\nv_mul_f32 v234,v234,v62\nv_mul_f32 v235,v235,v62\n" \
"v_mul_f32 v236,v236,v62\nv_mul_f32 v237,v237,v62\nv_mul_f32 v238,v238,v62\nv_mul_f32 v239,v239,v62\nv_mul_f32 v240,v240,v62\nv_mul_f32 v241,v241,v62\nv_mul_f32 v242,v242,v62\nv_mul_f32 v243,v243,v62\n" \
"v_mul_f32 v244,v244,v62\nv_mul_f32 v245,v245,v62\nv_mul_f32 v246,v246,v62\nv_mul_f32 v247,v247,v62\nv_mul_f32 v248,v248,v62\nv_mul_f32 v249,v249,v62\nv_mul_f32 v250,v250,v62\nv_mul_f32 v251,v251,v62\n" \
"v_mul_f32 v252,v252,v62\nv_mul_f32 v253,v253,v62\nv_mul_f32 v254,v254,v62\nv_mul_f32 v255,v255,v62\nv_lshlrev_b32 v61,16,v88\nv_and_b32 v63,0xffff0000,v88\nv_mul_f32 v61,v61,v62\nv_mul_f32 v63,v63,v62\n" \
"C v88,v61,v63\nv_lshlrev_b32 v61,16,v89\nv_and_b32 v63,0xffff0000,v89\nv_mul_f32 v61,v61,v62\nv_mul_f32 v63,v63,v62\nC v89,v61,v63\nv_lshlrev_b32 v61,16,v90\nv_and_b32 v63,0xffff0000,v90\n" \
"v_mul_f32 v61,v61,v62\nv_mul_f32 v63,v63,v62\nC v90,v61,v63\nv_lshlrev_b32 v61,16,v91\nv_and_b32 v63,0xffff0000,v91\nv_mul_f32 v61,v61,v62\nv_mul_f32 v63,v63,v62\nC v91,v61,v63\n" \
"s_cmp_eq_u32 s52,14\ns_cbranch_scc1 LH1R15_%=\ns_branch LH1X_%=\nLcs1_%=:\ns_add_u32 s9,s5,2\ns_mov_b32 s8,3\ns_mov_b32 s64,0\ns_branch LSLOW1_%=\n" \
"Lcs3_%=:\ns_add_u32 s9,s5,1\ns_mov_b32 s8,2\ns_mov_b32 s64,0\ns_branch LSLOW0_%=\nLcs5_%=:\ns_add_u32 s9,s5,0\ns_mov_b32 s8,1\n" \
"s_mov_b32 s64,1\ns_branch LSLOW1_%=\nLcs7_%=:\ns_lshl_b32 s9,s52,1\ns_add_u32 s9,s9,0\ns_sub_u32 s8,59,s9\ns_cmp_ge_u32 s9,s70\ns_cselect_b32 s9,s8,s9\n" \
"s_mov_b32 s8,0\ns_mov_b32 s64,1\ns_branch LSLOW0_%=\nLcs9_%=:\ns_lshl_b32 s9,s52,1\ns_add_u32 s9,s9,1\ns_sub_u32 s8,59,s9\ns_cmp_ge_u32 s9,s70\n" \
"s_cselect_b32 s9,s8,s9\ns_mov_b32 s8,0\ns_mov_b32 s64,2\ns_branch LSLOW1_%=\nLcs11_%=:\ns_lshl_b32 s9,s52,1\ns_add_u32 s9,s9,0\ns_sub_u32 s8,59,s9\n" \
"s_cmp_ge_u32 s9,s70\ns_cselect_b32 s9,s8,s9\ns_mov_b32 s8,0\ns_mov_b32 s64,2\ns_branch LSLOW0_%=\nLcs13_%=:\ns_lshl_b32 s9,s52,1\ns_add_u32 s9,s9,1\n" \
"s_sub_u32 s8,59,s9\ns_cmp_ge_u32 s9,s70\ns_cselect_b32 s9,s8,s9\ns_mov_b32 s8,0\ns_mov_b32 s64,3\ns_branch LSLOW1_%=\nLcs39_%=:\ns_lshl_b32 s9,s52,1\n" \
"s_add_u32 s9,s9,0\ns_sub_u32 s8,59,s9\ns_cmp_ge_u32 s9,s70\ns_cselect_b32 s9,s8,s9\ns_mov_b32 s8,0\ns_mov_b32 s64,3\ns_branch LSLOW0_%=\nLcs41_%=:\n" \
"s_lshl_b32 s9,s52,1\ns_add_u32 s9,s9,1\ns_sub_u32 s8,59,s9\ns_cmp_ge_u32 s9,s70\ns_cselect_b32 s9,s8,s9\ns_mov_b32 s8,0\ns_mov_b32 s64,4\ns_branch LSLOW1_%=\n" \
"Lcs43_%=:\ns_lshl_b32 s9,s52,1\ns_add_u32 s9,s9,0\ns_sub_u32 s8,59,s9\ns_cmp_ge_u32 s9,s70\ns_cselect_b32 s9,s8,s9\ns_mov_b32 s8,0\ns_mov_b32 s64,4\n" \
"s_branch LSLOW0_%=\nLcs45_%=:\ns_lshl_b32 s9,s52,1\ns_add_u32 s9,s9,1\ns_sub_u32 s8,59,s9\ns_cmp_ge_u32 s9,s70\ns_cselect_b32 s9,s8,s9\ns_mov_b32 s8,0\n" \
"s_mov_b32 s64,5\ns_branch LSLOW1_%=\nLcs47_%=:\ns_lshl_b32 s9,s52,1\ns_add_u32 s9,s9,0\ns_sub_u32 s8,59,s9\ns_cmp_ge_u32 s9,s70\ns_cselect_b32 s9,s8,s9\n" \
"s_mov_b32 s8,0\ns_mov_b32 s64,5\ns_branch LSLOW0_%=\nLcs49_%=:\ns_lshl_b32 s9,s52,1\ns_add_u32 s9,s9,1\ns_sub_u32 s8,59,s9\ns_cmp_ge_u32 s9,s70\n" \
"s_cselect_b32 s9,s8,s9\ns_mov_b32 s8,0\ns_mov_b32 s64,6\ns_branch LSLOW1_%=\nLcs51_%=:\ns_lshl_b32 s9,s52,1\ns_add_u32 s9,s9,0\ns_sub_u32 s8,59,s9\n" \
"s_cmp_ge_u32 s9,s70\ns_cselect_b32 s9,s8,s9\ns_mov_b32 s8,0\ns_mov_b32 s64,6\ns_branch LSLOW0_%=\nLcs53_%=:\ns_lshl_b32 s9,s52,1\ns_add_u32 s9,s9,1\n" \
"s_sub_u32 s8,59,s9\ns_cmp_ge_u32 s9,s70\ns_cselect_b32 s9,s8,s9\ns_mov_b32 s8,0\ns_mov_b32 s64,7\ns_branch LSLOW1_%=\nLcs55_%=:\ns_lshl_b32 s9,s52,1\n" \
"s_add_u32 s9,s9,0\ns_sub_u32 s8,59,s9\ns_cmp_ge_u32 s9,s70\ns_cselect_b32 s9,s8,s9\ns_mov_b32 s8,0\ns_mov_b32 s64,7\ns_branch LSLOW0_%=\nLcs57_%=:\n" \
"s_lshl_b32 s9,s52,1\ns_add_u32 s9,s9,1\ns_sub_u32 s8,59,s9\ns_cmp_ge_u32 s9,s70\ns_cselect_b32 s9,s8,s9\ns_mov_b32 s8,0\ns_mov_b32 s64,8\ns_branch LSLOW1_%=\n" \
"Lcs59_%=:\ns_lshl_b32 s9,s52,1\ns_add_u32 s9,s9,0\ns_sub_u32 s8,59,s9\ns_cmp_ge_u32 s9,s70\ns_cselect_b32 s9,s8,s9\ns_mov_b32 s8,0\ns_mov_b32 s64,8\n" \
"s_branch LSLOW0_%=\nLcs61_%=:\ns_lshl_b32 s9,s52,1\ns_add_u32 s9,s9,1\ns_sub_u32 s8,59,s9\ns_cmp_ge_u32 s9,s70\ns_cselect_b32 s9,s8,s9\ns_mov_b32 s8,0\n" \
"s_mov_b32 s64,9\ns_branch LSLOW1_%=\nLcs63_%=:\ns_lshl_b32 s9,s52,1\ns_add_u32 s9,s9,0\ns_sub_u32 s8,59,s9\ns_cmp_ge_u32 s9,s70\ns_cselect_b32 s9,s8,s9\n" \
"s_mov_b32 s8,0\ns_mov_b32 s64,9\ns_branch LSLOW0_%=\nLSLOW0_%=:\ns_lshl_b32 s9,s9,14\ns_add_u32 s9,s9,s71\ns_nop 7\ns_waitcnt lgkmcnt(0)\n" \
"buffer_load_dwordx4 a[240:243],v4,s[36:39],s9 offen offset:0 sc1\nbuffer_load_dwordx4 a[244:247],v4,s[36:39],s9 offen offset:2048 sc1\ns_add_u32 s9,s9,4096\nbuffer_load_dwordx4 a[248:251],v4,s[36:39],s9 offen offset:0 sc1\nbuffer_load_dwordx4 a[252:255],v4,s[36:39],s9 offen offset:2048 sc1\ns_sub_u32 s9,s9,3072\ns_waitcnt vmcnt(0)\ns_cmp_gt_u32 s8,0\n" \
"s_cbranch_scc1 Lk65_%=\nM v[64:67],a[240:243],a[128:131],v[32:35]\nM v[64:67],a[244:247],a[132:135],v[64:67]\nM v[64:67],a[248:251],a[136:139],v[64:67]\nM v[64:67],a[252:255],a[140:143],v[64:67]\nLk65_%=:\ns_cmp_gt_u32 s8,1\ns_cbranch_scc1 Lk66_%=\n" \
"M v[72:75],a[240:243],a[144:147],v[36:39]\nM v[72:75],a[244:247],a[148:151],v[72:75]\nM v[72:75],a[248:251],a[152:155],v[72:75]\nM v[72:75],a[252:255],a[156:159],v[72:75]\nLk66_%=:\ns_cmp_gt_u32 s8,2\ns_cbranch_scc1 Lk67_%=\nM v[80:83],a[240:243],a[160:163],v[40:43]\n" \
"M v[80:83],a[244:247],a[164:167],v[80:83]\nM v[80:83],a[248:251],a[168:171],v[80:83]\nM v[80:83],a[252:255],a[172:175],v[80:83]\nLk67_%=:\ns_cmp_gt_u32 s8,3\ns_cbranch_scc1 Lk68_%=\nM v[88:91],a[240:243],a[176:179],v[44:47]\nM v[88:91],a[244:247],a[180:183],v[88:91]\n" \
"M v[88:91],a[248:251],a[184:187],v[88:91]\nM v[88:91],a[252:255],a[188:191],v[88:91]\nLk68_%=:\ns_nop 7\nbuffer_load_dwordx4 a[240:243],v4,s[36:39],s9 offen offset:0 sc1\nbuffer_load_dwordx4 a[244:247],v4,s[36:39],s9 offen offset:2048 sc1\ns_add_u32 s9,s9,4096\nbuffer_load_dwordx4 a[248:251],v4,s[36:39],s9 offen offset:0 sc1\n" \
"buffer_load_dwordx4 a[252:255],v4,s[36:39],s9 offen offset:2048 sc1\ns_sub_u32 s9,s9,3072\ns_waitcnt vmcnt(0)\ns_cmp_gt_u32 s8,0\ns_cbranch_scc1 Lk69_%=\nM v[68:71],a[240:243],a[128:131],v[32:35]\nM v[68:71],a[244:247],a[132:135],v[68:71]\nM v[68:71],a[248:251],a[136:139],v[68:71]\n" \
"M v[68:71],a[252:255],a[140:143],v[68:71]\nLk69_%=:\ns_cmp_gt_u32 s8,1\ns_cbranch_scc1 Lk70_%=\nM v[76:79],a[240:243],a[144:147],v[36:39]\nM v[76:79],a[244:247],a[148:151],v[76:79]\nM v[76:79],a[248:251],a[152:155],v[76:79]\nM v[76:79],a[252:255],a[156:159],v[76:79]\n" \
"Lk70_%=:\ns_cmp_gt_u32 s8,2\ns_cbranch_scc1 Lk71_%=\nM v[84:87],a[240:243],a[160:163],v[40:43]\nM v[84:87],a[244:247],a[164:167],v[84:87]\nM v[84:87],a[248:251],a[168:171],v[84:87]\nM v[84:87],a[252:255],a[172:175],v[84:87]\nLk71_%=:\n" \
"s_cmp_gt_u32 s8,3\ns_cbranch_scc1 Lk72_%=\nM v[92:95],a[240:243],a[176:179],v[44:47]\nM v[92:95],a[244:247],a[180:183],v[92:95]\nM v[92:95],a[248:251],a[184:187],v[92:95]\nM v[92:95],a[252:255],a[188:191],v[92:95]\nLk72_%=:\ns_nop 7\n" \
"s_cmp_gt_u32 s8,0\ns_cbranch_scc0 Lk74_%=\nv_mov_b32 v16,v24\ns_cmp_eq_u32 s8,1\ns_cbranch_scc0 Lk73_%=\nM v[16:19],a[224:227],v[64:67],v[16:19]\ns_nop 7\ns_branch Lk73_%=\n" \
"Lk74_%=:\nv_max3_f32 v62,v64,v65,v66\nv_max3_f32 v63,v67,v68,v69\nv_max3_f32 v62,v62,v70,v71\nv_max3_f32 v62,v62,v63,0\nv_mov_b32 v63,v62\ns_nop 1\nv_permlane32_swap_b32 v62,v63\n" \
"v_max_f32 v62,v62,v63\nv_mov_b32 v63,v62\ns_nop 1\nv_permlane16_swap_b32 v62,v63\nv_max_f32 v62,v62,v63\nv_exp_f32 v63,-v62\nv_sub_f32 v64,v64,v62\nv_sub_f32 v65,v65,v62\n" \
"v_sub_f32 v66,v66,v62\nv_sub_f32 v67,v67,v62\nv_sub_f32 v68,v68,v62\nv_sub_f32 v69,v69,v62\nv_sub_f32 v70,v70,v62\nv_sub_f32 v71,v71,v62\nv_exp_f32 v64,v64\nv_exp_f32 v65,v65\n" \
"v_exp_f32 v66,v66\nv_exp_f32 v67,v67\nv_exp_f32 v68,v68\nv_exp_f32 v69,v69\nv_exp_f32 v70,v70\nv_exp_f32 v71,v71\nv_mul_f32 v16,v24,v63\nC v64,v64,v65\n" \
"C v65,v66,v67\nC v66,v68,v69\nC v67,v70,v71\nv_sub_f32 v32,v32,v62\nv_sub_f32 v33,v33,v62\nv_sub_f32 v34,v34,v62\nv_sub_f32 v35,v35,v62\nM v[16:19],a[224:227],v[64:67],v[16:19]\n" \
"v_mul_f32 v128,v128,v63\nv_mul_f32 v129,v129,v63\nv_mul_f32 v130,v130,v63\nv_mul_f32 v131,v131,v63\nv_mul_f32 v132,v132,v63\nv_mul_f32 v133,v133,v63\nv_mul_f32 v134,v134,v63\nv_mul_f32 v135,v135,v63\n" \
"v_mul_f32 v136,v136,v63\nv_mul_f32 v137,v137,v63\nv_mul_f32 v138,v138,v63\nv_mul_f32 v139,v139,v63\nv_mul_f32 v140,v140,v63\nv_mul_f32 v141,v141,v63\nv_mul_f32 v142,v142,v63\nv_mul_f32 v143,v143,v63\n" \
"v_mul_f32 v144,v144,v63\nv_mul_f32 v145,v145,v63\nv_mul_f32 v146,v146,v63\nv_mul_f32 v147,v147,v63\nv_mul_f32 v148,v148,v63\nv_mul_f32 v149,v149,v63\nv_mul_f32 v150,v150,v63\nv_mul_f32 v151,v151,v63\n" \
"v_mul_f32 v152,v152,v63\nv_mul_f32 v153,v153,v63\nv_mul_f32 v154,v154,v63\nv_mul_f32 v155,v155,v63\nv_mul_f32 v156,v156,v63\nv_mul_f32 v157,v157,v63\nv_mul_f32 v158,v158,v63\nv_mul_f32 v159,v159,v63\n" \
"s_bitcmp1_b32 s64,6\ns_cbranch_scc1 Lk73_%=\nv_sub_f32 v96,v96,v62\nv_sub_f32 v97,v97,v62\nv_sub_f32 v98,v98,v62\nv_sub_f32 v99,v99,v62\nv_sub_f32 v100,v100,v62\nv_sub_f32 v101,v101,v62\n" \
"v_sub_f32 v102,v102,v62\nv_sub_f32 v103,v103,v62\nLk73_%=:\ns_cmp_gt_u32 s8,1\ns_cbranch_scc0 Lk76_%=\nv_mov_b32 v17,v25\ns_cmp_eq_u32 s8,2\ns_cbranch_scc0 Lk75_%=\n" \
"M v[16:19],a[228:231],v[72:75],v[16:19]\ns_nop 7\ns_branch Lk75_%=\nLk76_%=:\nv_max3_f32 v62,v72,v73,v74\nv_max3_f32 v63,v75,v76,v77\nv_max3_f32 v62,v62,v78,v79\nv_max3_f32 v62,v62,v63,0\n" \
"v_mov_b32 v63,v62\ns_nop 1\nv_permlane32_swap_b32 v62,v63\nv_max_f32 v62,v62,v63\nv_mov_b32 v63,v62\ns_nop 1\nv_permlane16_swap_b32 v62,v63\nv_max_f32 v62,v62,v63\n" \
"v_exp_f32 v63,-v62\nv_sub_f32 v72,v72,v62\nv_sub_f32 v73,v73,v62\nv_sub_f32 v74,v74,v62\nv_sub_f32 v75,v75,v62\nv_sub_f32 v76,v76,v62\nv_sub_f32 v77,v77,v62\nv_sub_f32 v78,v78,v62\n" \
"v_sub_f32 v79,v79,v62\nv_exp_f32 v72,v72\nv_exp_f32 v73,v73\nv_exp_f32 v74,v74\nv_exp_f32 v75,v75\nv_exp_f32 v76,v76\nv_exp_f32 v77,v77\nv_exp_f32 v78,v78\n" \
"v_exp_f32 v79,v79\nv_mul_f32 v17,v25,v63\nC v72,v72,v73\nC v73,v74,v75\nC v74,v76,v77\nC v75,v78,v79\nv_sub_f32 v36,v36,v62\nv_sub_f32 v37,v37,v62\n" \
"v_sub_f32 v38,v38,v62\nv_sub_f32 v39,v39,v62\nM v[16:19],a[228:231],v[72:75],v[16:19]\nv_mul_f32 v160,v160,v63\nv_mul_f32 v161,v161,v63\nv_mul_f32 v162,v162,v63\nv_mul_f32 v163,v163,v63\nv_mul_f32 v164,v164,v63\n" \
"v_mul_f32 v165,v165,v63\nv_mul_f32 v166,v166,v63\nv_mul_f32 v167,v167,v63\nv_mul_f32 v168,v168,v63\nv_mul_f32 v169,v169,v63\nv_mul_f32 v170,v170,v63\nv_mul_f32 v171,v171,v63\nv_mul_f32 v172,v172,v63\n" \
"v_mul_f32 v173,v173,v63\nv_mul_f32 v174,v174,v63\nv_mul_f32 v175,v175,v63\nv_mul_f32 v176,v176,v63\nv_mul_f32 v177,v177,v63\nv_mul_f32 v178,v178,v63\nv_mul_f32 v179,v179,v63\nv_mul_f32 v180,v180,v63\n" \
"v_mul_f32 v181,v181,v63\nv_mul_f32 v182,v182,v63\nv_mul_f32 v183,v183,v63\nv_mul_f32 v184,v184,v63\nv_mul_f32 v185,v185,v63\nv_mul_f32 v186,v186,v63\nv_mul_f32 v187,v187,v63\nv_mul_f32 v188,v188,v63\n" \
"v_mul_f32 v189,v189,v63\nv_mul_f32 v190,v190,v63\nv_mul_f32 v191,v191,v63\ns_bitcmp1_b32 s64,6\ns_cbranch_scc1 Lk75_%=\nv_sub_f32 v104,v104,v62\nv_sub_f32 v105,v105,v62\nv_sub_f32 v106,v106,v62\n" \
"v_sub_f32 v107,v107,v62\nv_sub_f32 v108,v108,v62\nv_sub_f32 v109,v109,v62\nv_sub_f32 v110,v110,v62\nv_sub_f32 v111,v111,v62\nLk75_%=:\ns_cmp_gt_u32 s8,2\ns_cbranch_scc0 Lk78_%=\n" \
"v_mov_b32 v18,v26\ns_cmp_eq_u32 s8,3\ns_cbranch_scc0 Lk77_%=\nM v[16:19],a[232:235],v[80:83],v[16:19]\ns_nop 7\ns_branch Lk77_%=\nLk78_%=:\nv_max3_f32 v62,v80,v81,v82\n" \
"v_max3_f32 v63,v83,v84,v85\nv_max3_f32 v62,v62,v86,v87\nv_max3_f32 v62,v62,v63,0\nv_mov_b32 v63,v62\ns_nop 1\nv_permlane32_swap_b32 v62,v63\nv_max_f32 v62,v62,v63\nv_mov_b32 v63,v62\n" \
"s_nop 1\nv_permlane16_swap_b32 v62,v63\nv_max_f32 v62,v62,v63\nv_exp_f32 v63,-v62\nv_sub_f32 v80,v80,v62\nv_sub_f32 v81,v81,v62\nv_sub_f32 v82,v82,v62\nv_sub_f32 v83,v83,v62\n" \
"v_sub_f32 v84,v84,v62\nv_sub_f32 v85,v85,v62\nv_sub_f32 v86,v86,v62\nv_sub_f32 v87,v87,v62\nv_exp_f32 v80,v80\nv_exp_f32 v81,v81\nv_exp_f32 v82,v82\nv_exp_f32 v83,v83\n" \
"v_exp_f32 v84,v84\nv_exp_f32 v85,v85\nv_exp_f32 v86,v86\nv_exp_f32 v87,v87\nv_mul_f32 v18,v26,v63\nC v80,v80,v81\nC v81,v82,v83\nC v82,v84,v85\n" \
"C v83,v86,v87\nv_sub_f32 v40,v40,v62\nv_sub_f32 v41,v41,v62\nv_sub_f32 v42,v42,v62\nv_sub_f32 v43,v43,v62\nM v[16:19],a[232:235],v[80:83],v[16:19]\nv_mul_f32 v192,v192,v63\nv_mul_f32 v193,v193,v63\n" \
"v_mul_f32 v194,v194,v63\nv_mul_f32 v195,v195,v63\nv_mul_f32 v196,v196,v63\nv_mul_f32 v197,v197,v63\nv_mul_f32 v198,v198,v63\nv_mul_f32 v199,v199,v63\nv_mul_f32 v200,v200,v63\nv_mul_f32 v201,v201,v63\n" \
"v_mul_f32 v202,v202,v63\nv_mul_f32 v203,v203,v63\nv_mul_f32 v204,v204,v63\nv_mul_f32 v205,v205,v63\nv_mul_f32 v206,v206,v63\nv_mul_f32 v207,v207,v63\nv_mul_f32 v208,v208,v63\nv_mul_f32 v209,v209,v63\n" \
"v_mul_f32 v210,v210,v63\nv_mul_f32 v211,v211,v63\nv_mul_f32 v212,v212,v63\nv_mul_f32 v213,v213,v63\nv_mul_f32 v214,v214,v63\nv_mul_f32 v215,v215,v63\nv_mul_f32 v216,v216,v63\nv_mul_f32 v217,v217,v63\n" \
"v_mul_f32 v218,v218,v63\nv_mul_f32 v219,v219,v63\nv_mul_f32 v220,v220,v63\nv_mul_f32 v221,v221,v63\nv_mul_f32 v222,v222,v63\nv_mul_f32 v223,v223,v63\ns_bitcmp1_b32 s64,6\ns_cbranch_scc1 Lk77_%=\n" \
"v_sub_f32 v112,v112,v62\nv_sub_f32 v113,v113,v62\nv_sub_f32 v114,v114,v62\nv_sub_f32 v115,v115,v62\nv_sub_f32 v116,v116,v62\nv_sub_f32 v117,v117,v62\nv_sub_f32 v118,v118,v62\nv_sub_f32 v119,v119,v62\n" \
"Lk77_%=:\ns_cmp_gt_u32 s8,3\ns_cbranch_scc0 Lk80_%=\nv_mov_b32 v19,v27\ns_cmp_eq_u32 s8,4\ns_cbranch_scc0 Lk79_%=\nM v[16:19],a[236:239],v[88:91],v[16:19]\ns_nop 7\n" \
"s_branch Lk79_%=\nLk80_%=:\nv_max3_f32 v62,v88,v89,v90\nv_max3_f32 v63,v91,v92,v93\nv_max3_f32 v62,v62,v94,v95\nv_max3_f32 v62,v62,v63,0\nv_mov_b32 v63,v62\ns_nop 1\n" \
"v_permlane32_swap_b32 v62,v63\nv_max_f32 v62,v62,v63\nv_mov_b32 v63,v62\ns_nop 1\nv_permlane16_swap_b32 v62,v63\nv_max_f32 v62,v62,v63\nv_exp_f32 v63,-v62\nv_sub_f32 v88,v88,v62\n" \
"v_sub_f32 v89,v89,v62\nv_sub_f32 v90,v90,v62\nv_sub_f32 v91,v91,v62\nv_sub_f32 v92,v92,v62\nv_sub_f32 v93,v93,v62\nv_sub_f32 v94,v94,v62\nv_sub_f32 v95,v95,v62\nv_exp_f32 v88,v88\n" \
"v_exp_f32 v89,v89\nv_exp_f32 v90,v90\nv_exp_f32 v91,v91\nv_exp_f32 v92,v92\nv_exp_f32 v93,v93\nv_exp_f32 v94,v94\nv_exp_f32 v95,v95\nv_mul_f32 v19,v27,v63\n" \
"C v88,v88,v89\nC v89,v90,v91\nC v90,v92,v93\nC v91,v94,v95\nv_sub_f32 v44,v44,v62\nv_sub_f32 v45,v45,v62\nv_sub_f32 v46,v46,v62\nv_sub_f32 v47,v47,v62\n" \
"M v[16:19],a[236:239],v[88:91],v[16:19]\nv_mul_f32 v224,v224,v63\nv_mul_f32 v225,v225,v63\nv_mul_f32 v226,v226,v63\nv_mul_f32 v227,v227,v63\nv_mul_f32 v228,v228,v63\nv_mul_f32 v229,v229,v63\nv_mul_f32 v230,v230,v63\n" \
"v_mul_f32 v231,v231,v63\nv_mul_f32 v232,v232,v63\nv_mul_f32 v233,v233,v63\nv_mul_f32 v234,v234,v63\nv_mul_f32 v235,v235,v63\nv_mul_f32 v236,v236,v63\nv_mul_f32 v237,v237,v63\nv_mul_f32 v238,v238,v63\n" \
"v_mul_f32 v239,v239,v63\nv_mul_f32 v240,v240,v63\nv_mul_f32 v241,v241,v63\nv_mul_f32 v242,v242,v63\nv_mul_f32 v243,v243,v63\nv_mul_f32 v244,v244,v63\nv_mul_f32 v245,v245,v63\nv_mul_f32 v246,v246,v63\n" \
"v_mul_f32 v247,v247,v63\nv_mul_f32 v248,v248,v63\nv_mul_f32 v249,v249,v63\nv_mul_f32 v250,v250,v63\nv_mul_f32 v251,v251,v63\nv_mul_f32 v252,v252,v63\nv_mul_f32 v253,v253,v63\nv_mul_f32 v254,v254,v63\n" \
"v_mul_f32 v255,v255,v63\ns_bitcmp1_b32 s64,6\ns_cbranch_scc1 Lk79_%=\nv_sub_f32 v120,v120,v62\nv_sub_f32 v121,v121,v62\nv_sub_f32 v122,v122,v62\nv_sub_f32 v123,v123,v62\nv_sub_f32 v124,v124,v62\n" \
"v_sub_f32 v125,v125,v62\nv_sub_f32 v126,v126,v62\nv_sub_f32 v127,v127,v62\nLk79_%=:\ns_nop 7\nLSR0_%=:\ns_cmp_eq_u32 s64,0\ns_cbranch_scc1 Lcr4_%=\n" \
"s_cmp_eq_u32 s64,1\ns_cbranch_scc1 Lcr8_%=\ns_cmp_eq_u32 s64,2\ns_cbranch_scc1 Lcr12_%=\ns_cmp_eq_u32 s64,3\ns_cbranch_scc1 Lcr40_%=\ns_cmp_eq_u32 s64,4\ns_cbranch_scc1 Lcr44_%=\n" \
"s_cmp_eq_u32 s64,5\ns_cbranch_scc1 Lcr48_%=\ns_cmp_eq_u32 s64,6\ns_cbranch_scc1 Lcr52_%=\ns_cmp_eq_u32 s64,7\ns_cbranch_scc1 Lcr56_%=\ns_cmp_eq_u32 s64,8\ns_cbranch_scc1 Lcr60_%=\n" \
"s_branch Lcr64_%=\nLSLOW1_%=:\ns_cmp_eq_u32 s75,1\ns_cbranch_scc1 LSR1_%=\ns_lshl_b32 s9,s9,14\ns_add_u32 s9,s9,s71\ns_nop 7\ns_waitcnt lgkmcnt(0)\n" \
"buffer_load_dwordx4 a[240:243],v4,s[36:39],s9 offen offset:0 sc1\nbuffer_load_dwordx4 a[244:247],v4,s[36:39],s9 offen offset:2048 sc1\ns_add_u32 s9,s9,4096\nbuffer_load_dwordx4 a[248:251],v4,s[36:39],s9 offen offset:0 sc1\nbuffer_load_dwordx4 a[252:255],v4,s[36:39],s9 offen offset:2048 sc1\ns_sub_u32 s9,s9,3072\ns_waitcnt vmcnt(0)\ns_cmp_gt_u32 s8,0\n" \
"s_cbranch_scc1 Lk81_%=\nM v[96:99],a[240:243],a[128:131],v[32:35]\nM v[96:99],a[244:247],a[132:135],v[96:99]\nM v[96:99],a[248:251],a[136:139],v[96:99]\nM v[96:99],a[252:255],a[140:143],v[96:99]\nLk81_%=:\ns_cmp_gt_u32 s8,1\ns_cbranch_scc1 Lk82_%=\n" \
"M v[104:107],a[240:243],a[144:147],v[36:39]\nM v[104:107],a[244:247],a[148:151],v[104:107]\nM v[104:107],a[248:251],a[152:155],v[104:107]\nM v[104:107],a[252:255],a[156:159],v[104:107]\nLk82_%=:\ns_cmp_gt_u32 s8,2\ns_cbranch_scc1 Lk83_%=\nM v[112:115],a[240:243],a[160:163],v[40:43]\n" \
"M v[112:115],a[244:247],a[164:167],v[112:115]\nM v[112:115],a[248:251],a[168:171],v[112:115]\nM v[112:115],a[252:255],a[172:175],v[112:115]\nLk83_%=:\ns_cmp_gt_u32 s8,3\ns_cbranch_scc1 Lk84_%=\nM v[120:123],a[240:243],a[176:179],v[44:47]\nM v[120:123],a[244:247],a[180:183],v[120:123]\n" \
"M v[120:123],a[248:251],a[184:187],v[120:123]\nM v[120:123],a[252:255],a[188:191],v[120:123]\nLk84_%=:\ns_nop 7\nbuffer_load_dwordx4 a[240:243],v4,s[36:39],s9 offen offset:0 sc1\nbuffer_load_dwordx4 a[244:247],v4,s[36:39],s9 offen offset:2048 sc1\ns_add_u32 s9,s9,4096\nbuffer_load_dwordx4 a[248:251],v4,s[36:39],s9 offen offset:0 sc1\n" \
"buffer_load_dwordx4 a[252:255],v4,s[36:39],s9 offen offset:2048 sc1\ns_sub_u32 s9,s9,3072\ns_waitcnt vmcnt(0)\ns_cmp_gt_u32 s8,0\ns_cbranch_scc1 Lk85_%=\nM v[100:103],a[240:243],a[128:131],v[32:35]\nM v[100:103],a[244:247],a[132:135],v[100:103]\nM v[100:103],a[248:251],a[136:139],v[100:103]\n" \
"M v[100:103],a[252:255],a[140:143],v[100:103]\nLk85_%=:\ns_cmp_gt_u32 s8,1\ns_cbranch_scc1 Lk86_%=\nM v[108:111],a[240:243],a[144:147],v[36:39]\nM v[108:111],a[244:247],a[148:151],v[108:111]\nM v[108:111],a[248:251],a[152:155],v[108:111]\nM v[108:111],a[252:255],a[156:159],v[108:111]\n" \
"Lk86_%=:\ns_cmp_gt_u32 s8,2\ns_cbranch_scc1 Lk87_%=\nM v[116:119],a[240:243],a[160:163],v[40:43]\nM v[116:119],a[244:247],a[164:167],v[116:119]\nM v[116:119],a[248:251],a[168:171],v[116:119]\nM v[116:119],a[252:255],a[172:175],v[116:119]\nLk87_%=:\n" \
"s_cmp_gt_u32 s8,3\ns_cbranch_scc1 Lk88_%=\nM v[124:127],a[240:243],a[176:179],v[44:47]\nM v[124:127],a[244:247],a[180:183],v[124:127]\nM v[124:127],a[248:251],a[184:187],v[124:127]\nM v[124:127],a[252:255],a[188:191],v[124:127]\nLk88_%=:\ns_nop 7\n" \
"s_cmp_gt_u32 s8,0\ns_cbranch_scc0 Lk90_%=\nv_mov_b32 v24,v16\ns_cmp_eq_u32 s8,1\ns_cbranch_scc0 Lk89_%=\nM v[24:27],a[224:227],v[96:99],v[24:27]\ns_nop 7\ns_branch Lk89_%=\n" \
"Lk90_%=:\nv_max3_f32 v62,v96,v97,v98\nv_max3_f32 v63,v99,v100,v101\nv_max3_f32 v62,v62,v102,v103\nv_max3_f32 v62,v62,v63,0\nv_mov_b32 v63,v62\ns_nop 1\nv_permlane32_swap_b32 v62,v63\n" \
"v_max_f32 v62,v62,v63\nv_mov_b32 v63,v62\ns_nop 1\nv_permlane16_swap_b32 v62,v63\nv_max_f32 v62,v62,v63\nv_exp_f32 v63,-v62\nv_sub_f32 v96,v96,v62\nv_sub_f32 v97,v97,v62\n" \
"v_sub_f32 v98,v98,v62\nv_sub_f32 v99,v99,v62\nv_sub_f32 v100,v100,v62\nv_sub_f32 v101,v101,v62\nv_sub_f32 v102,v102,v62\nv_sub_f32 v103,v103,v62\nv_exp_f32 v96,v96\nv_exp_f32 v97,v97\n" \
"v_exp_f32 v98,v98\nv_exp_f32 v99,v99\nv_exp_f32 v100,v100\nv_exp_f32 v101,v101\nv_exp_f32 v102,v102\nv_exp_f32 v103,v103\nv_mul_f32 v24,v16,v63\nC v96,v96,v97\n" \
"C v97,v98,v99\nC v98,v100,v101\nC v99,v102,v103\nv_sub_f32 v32,v32,v62\nv_sub_f32 v33,v33,v62\nv_sub_f32 v34,v34,v62\nv_sub_f32 v35,v35,v62\nM v[24:27],a[224:227],v[96:99],v[24:27]\n" \
"v_mul_f32 v128,v128,v63\nv_mul_f32 v129,v129,v63\nv_mul_f32 v130,v130,v63\nv_mul_f32 v131,v131,v63\nv_mul_f32 v132,v132,v63\nv_mul_f32 v133,v133,v63\nv_mul_f32 v134,v134,v63\nv_mul_f32 v135,v135,v63\n" \
"v_mul_f32 v136,v136,v63\nv_mul_f32 v137,v137,v63\nv_mul_f32 v138,v138,v63\nv_mul_f32 v139,v139,v63\nv_mul_f32 v140,v140,v63\nv_mul_f32 v141,v141,v63\nv_mul_f32 v142,v142,v63\nv_mul_f32 v143,v143,v63\n" \
"v_mul_f32 v144,v144,v63\nv_mul_f32 v145,v145,v63\nv_mul_f32 v146,v146,v63\nv_mul_f32 v147,v147,v63\nv_mul_f32 v148,v148,v63\nv_mul_f32 v149,v149,v63\nv_mul_f32 v150,v150,v63\nv_mul_f32 v151,v151,v63\n" \
"v_mul_f32 v152,v152,v63\nv_mul_f32 v153,v153,v63\nv_mul_f32 v154,v154,v63\nv_mul_f32 v155,v155,v63\nv_mul_f32 v156,v156,v63\nv_mul_f32 v157,v157,v63\nv_mul_f32 v158,v158,v63\nv_mul_f32 v159,v159,v63\n" \
"s_bitcmp1_b32 s64,6\ns_cbranch_scc1 Lk89_%=\nv_sub_f32 v64,v64,v62\nv_sub_f32 v65,v65,v62\nv_sub_f32 v66,v66,v62\nv_sub_f32 v67,v67,v62\nv_sub_f32 v68,v68,v62\nv_sub_f32 v69,v69,v62\n" \
"v_sub_f32 v70,v70,v62\nv_sub_f32 v71,v71,v62\nLk89_%=:\ns_cmp_gt_u32 s8,1\ns_cbranch_scc0 Lk92_%=\nv_mov_b32 v25,v17\ns_cmp_eq_u32 s8,2\ns_cbranch_scc0 Lk91_%=\n" \
"M v[24:27],a[228:231],v[104:107],v[24:27]\ns_nop 7\ns_branch Lk91_%=\nLk92_%=:\nv_max3_f32 v62,v104,v105,v106\nv_max3_f32 v63,v107,v108,v109\nv_max3_f32 v62,v62,v110,v111\nv_max3_f32 v62,v62,v63,0\n" \
"v_mov_b32 v63,v62\ns_nop 1\nv_permlane32_swap_b32 v62,v63\nv_max_f32 v62,v62,v63\nv_mov_b32 v63,v62\ns_nop 1\nv_permlane16_swap_b32 v62,v63\nv_max_f32 v62,v62,v63\n" \
"v_exp_f32 v63,-v62\nv_sub_f32 v104,v104,v62\nv_sub_f32 v105,v105,v62\nv_sub_f32 v106,v106,v62\nv_sub_f32 v107,v107,v62\nv_sub_f32 v108,v108,v62\nv_sub_f32 v109,v109,v62\nv_sub_f32 v110,v110,v62\n" \
"v_sub_f32 v111,v111,v62\nv_exp_f32 v104,v104\nv_exp_f32 v105,v105\nv_exp_f32 v106,v106\nv_exp_f32 v107,v107\nv_exp_f32 v108,v108\nv_exp_f32 v109,v109\nv_exp_f32 v110,v110\n" \
"v_exp_f32 v111,v111\nv_mul_f32 v25,v17,v63\nC v104,v104,v105\nC v105,v106,v107\nC v106,v108,v109\nC v107,v110,v111\nv_sub_f32 v36,v36,v62\nv_sub_f32 v37,v37,v62\n" \
"v_sub_f32 v38,v38,v62\nv_sub_f32 v39,v39,v62\nM v[24:27],a[228:231],v[104:107],v[24:27]\nv_mul_f32 v160,v160,v63\nv_mul_f32 v161,v161,v63\nv_mul_f32 v162,v162,v63\nv_mul_f32 v163,v163,v63\nv_mul_f32 v164,v164,v63\n" \
"v_mul_f32 v165,v165,v63\nv_mul_f32 v166,v166,v63\nv_mul_f32 v167,v167,v63\nv_mul_f32 v168,v168,v63\nv_mul_f32 v169,v169,v63\nv_mul_f32 v170,v170,v63\nv_mul_f32 v171,v171,v63\nv_mul_f32 v172,v172,v63\n" \
"v_mul_f32 v173,v173,v63\nv_mul_f32 v174,v174,v63\nv_mul_f32 v175,v175,v63\nv_mul_f32 v176,v176,v63\nv_mul_f32 v177,v177,v63\nv_mul_f32 v178,v178,v63\nv_mul_f32 v179,v179,v63\nv_mul_f32 v180,v180,v63\n" \
"v_mul_f32 v181,v181,v63\nv_mul_f32 v182,v182,v63\nv_mul_f32 v183,v183,v63\nv_mul_f32 v184,v184,v63\nv_mul_f32 v185,v185,v63\nv_mul_f32 v186,v186,v63\nv_mul_f32 v187,v187,v63\nv_mul_f32 v188,v188,v63\n" \
"v_mul_f32 v189,v189,v63\nv_mul_f32 v190,v190,v63\nv_mul_f32 v191,v191,v63\ns_bitcmp1_b32 s64,6\ns_cbranch_scc1 Lk91_%=\nv_sub_f32 v72,v72,v62\nv_sub_f32 v73,v73,v62\nv_sub_f32 v74,v74,v62\n" \
"v_sub_f32 v75,v75,v62\nv_sub_f32 v76,v76,v62\nv_sub_f32 v77,v77,v62\nv_sub_f32 v78,v78,v62\nv_sub_f32 v79,v79,v62\nLk91_%=:\ns_cmp_gt_u32 s8,2\ns_cbranch_scc0 Lk94_%=\n" \
"v_mov_b32 v26,v18\ns_cmp_eq_u32 s8,3\ns_cbranch_scc0 Lk93_%=\nM v[24:27],a[232:235],v[112:115],v[24:27]\ns_nop 7\ns_branch Lk93_%=\nLk94_%=:\nv_max3_f32 v62,v112,v113,v114\n" \
"v_max3_f32 v63,v115,v116,v117\nv_max3_f32 v62,v62,v118,v119\nv_max3_f32 v62,v62,v63,0\nv_mov_b32 v63,v62\ns_nop 1\nv_permlane32_swap_b32 v62,v63\nv_max_f32 v62,v62,v63\nv_mov_b32 v63,v62\n" \
"s_nop 1\nv_permlane16_swap_b32 v62,v63\nv_max_f32 v62,v62,v63\nv_exp_f32 v63,-v62\nv_sub_f32 v112,v112,v62\nv_sub_f32 v113,v113,v62\nv_sub_f32 v114,v114,v62\nv_sub_f32 v115,v115,v62\n" \
"v_sub_f32 v116,v116,v62\nv_sub_f32 v117,v117,v62\nv_sub_f32 v118,v118,v62\nv_sub_f32 v119,v119,v62\nv_exp_f32 v112,v112\nv_exp_f32 v113,v113\nv_exp_f32 v114,v114\nv_exp_f32 v115,v115\n" \
"v_exp_f32 v116,v116\nv_exp_f32 v117,v117\nv_exp_f32 v118,v118\nv_exp_f32 v119,v119\nv_mul_f32 v26,v18,v63\nC v112,v112,v113\nC v113,v114,v115\nC v114,v116,v117\n" \
"C v115,v118,v119\nv_sub_f32 v40,v40,v62\nv_sub_f32 v41,v41,v62\nv_sub_f32 v42,v42,v62\nv_sub_f32 v43,v43,v62\nM v[24:27],a[232:235],v[112:115],v[24:27]\nv_mul_f32 v192,v192,v63\nv_mul_f32 v193,v193,v63\n" \
"v_mul_f32 v194,v194,v63\nv_mul_f32 v195,v195,v63\nv_mul_f32 v196,v196,v63\nv_mul_f32 v197,v197,v63\nv_mul_f32 v198,v198,v63\nv_mul_f32 v199,v199,v63\nv_mul_f32 v200,v200,v63\nv_mul_f32 v201,v201,v63\n" \
"v_mul_f32 v202,v202,v63\nv_mul_f32 v203,v203,v63\nv_mul_f32 v204,v204,v63\nv_mul_f32 v205,v205,v63\nv_mul_f32 v206,v206,v63\nv_mul_f32 v207,v207,v63\nv_mul_f32 v208,v208,v63\nv_mul_f32 v209,v209,v63\n" \
"v_mul_f32 v210,v210,v63\nv_mul_f32 v211,v211,v63\nv_mul_f32 v212,v212,v63\nv_mul_f32 v213,v213,v63\nv_mul_f32 v214,v214,v63\nv_mul_f32 v215,v215,v63\nv_mul_f32 v216,v216,v63\nv_mul_f32 v217,v217,v63\n" \
"v_mul_f32 v218,v218,v63\nv_mul_f32 v219,v219,v63\nv_mul_f32 v220,v220,v63\nv_mul_f32 v221,v221,v63\nv_mul_f32 v222,v222,v63\nv_mul_f32 v223,v223,v63\ns_bitcmp1_b32 s64,6\ns_cbranch_scc1 Lk93_%=\n" \
"v_sub_f32 v80,v80,v62\nv_sub_f32 v81,v81,v62\nv_sub_f32 v82,v82,v62\nv_sub_f32 v83,v83,v62\nv_sub_f32 v84,v84,v62\nv_sub_f32 v85,v85,v62\nv_sub_f32 v86,v86,v62\nv_sub_f32 v87,v87,v62\n" \
"Lk93_%=:\ns_cmp_gt_u32 s8,3\ns_cbranch_scc0 Lk96_%=\nv_mov_b32 v27,v19\ns_cmp_eq_u32 s8,4\ns_cbranch_scc0 Lk95_%=\nM v[24:27],a[236:239],v[120:123],v[24:27]\ns_nop 7\n" \
"s_branch Lk95_%=\nLk96_%=:\nv_max3_f32 v62,v120,v121,v122\nv_max3_f32 v63,v123,v124,v125\nv_max3_f32 v62,v62,v126,v127\nv_max3_f32 v62,v62,v63,0\nv_mov_b32 v63,v62\ns_nop 1\n" \
"v_permlane32_swap_b32 v62,v63\nv_max_f32 v62,v62,v63\nv_mov_b32 v63,v62\ns_nop 1\nv_permlane16_swap_b32 v62,v63\nv_max_f32 v62,v62,v63\nv_exp_f32 v63,-v62\nv_sub_f32 v120,v120,v62\n" \
"v_sub_f32 v121,v121,v62\nv_sub_f32 v122,v122,v62\nv_sub_f32 v123,v123,v62\nv_sub_f32 v124,v124,v62\nv_sub_f32 v125,v125,v62\nv_sub_f32 v126,v126,v62\nv_sub_f32 v127,v127,v62\nv_exp_f32 v120,v120\n" \
"v_exp_f32 v121,v121\nv_exp_f32 v122,v122\nv_exp_f32 v123,v123\nv_exp_f32 v124,v124\nv_exp_f32 v125,v125\nv_exp_f32 v126,v126\nv_exp_f32 v127,v127\nv_mul_f32 v27,v19,v63\n" \
"C v120,v120,v121\nC v121,v122,v123\nC v122,v124,v125\nC v123,v126,v127\nv_sub_f32 v44,v44,v62\nv_sub_f32 v45,v45,v62\nv_sub_f32 v46,v46,v62\nv_sub_f32 v47,v47,v62\n" \
"M v[24:27],a[236:239],v[120:123],v[24:27]\nv_mul_f32 v224,v224,v63\nv_mul_f32 v225,v225,v63\nv_mul_f32 v226,v226,v63\nv_mul_f32 v227,v227,v63\nv_mul_f32 v228,v228,v63\nv_mul_f32 v229,v229,v63\nv_mul_f32 v230,v230,v63\n" \
"v_mul_f32 v231,v231,v63\nv_mul_f32 v232,v232,v63\nv_mul_f32 v233,v233,v63\nv_mul_f32 v234,v234,v63\nv_mul_f32 v235,v235,v63\nv_mul_f32 v236,v236,v63\nv_mul_f32 v237,v237,v63\nv_mul_f32 v238,v238,v63\n" \
"v_mul_f32 v239,v239,v63\nv_mul_f32 v240,v240,v63\nv_mul_f32 v241,v241,v63\nv_mul_f32 v242,v242,v63\nv_mul_f32 v243,v243,v63\nv_mul_f32 v244,v244,v63\nv_mul_f32 v245,v245,v63\nv_mul_f32 v246,v246,v63\n" \
"v_mul_f32 v247,v247,v63\nv_mul_f32 v248,v248,v63\nv_mul_f32 v249,v249,v63\nv_mul_f32 v250,v250,v63\nv_mul_f32 v251,v251,v63\nv_mul_f32 v252,v252,v63\nv_mul_f32 v253,v253,v63\nv_mul_f32 v254,v254,v63\n" \
"v_mul_f32 v255,v255,v63\ns_bitcmp1_b32 s64,6\ns_cbranch_scc1 Lk95_%=\nv_sub_f32 v88,v88,v62\nv_sub_f32 v89,v89,v62\nv_sub_f32 v90,v90,v62\nv_sub_f32 v91,v91,v62\nv_sub_f32 v92,v92,v62\n" \
"v_sub_f32 v93,v93,v62\nv_sub_f32 v94,v94,v62\nv_sub_f32 v95,v95,v62\nLk95_%=:\ns_nop 7\nLSR1_%=:\ns_cmp_eq_u32 s64,0\ns_cbranch_scc1 Lcr2_%=\n" \
"s_cmp_eq_u32 s64,1\ns_cbranch_scc1 Lcr6_%=\ns_cmp_eq_u32 s64,2\ns_cbranch_scc1 Lcr10_%=\ns_cmp_eq_u32 s64,3\ns_cbranch_scc1 Lcr14_%=\ns_cmp_eq_u32 s64,4\ns_cbranch_scc1 Lcr42_%=\n" \
"s_cmp_eq_u32 s64,5\ns_cbranch_scc1 Lcr46_%=\ns_cmp_eq_u32 s64,6\ns_cbranch_scc1 Lcr50_%=\ns_cmp_eq_u32 s64,7\ns_cbranch_scc1 Lcr54_%=\ns_cmp_eq_u32 s64,8\ns_cbranch_scc1 Lcr58_%=\n" \
"s_branch Lcr62_%=\nLPU_%=:\ns_mov_b32 s25,0\nLPUL_%=:\ns_sleep 2\nbuffer_load_dwordx2 v[58:59],v57,s[36:39],0 offen sc1\ns_add_u32 s25,s25,1\ns_waitcnt vmcnt(0)\n" \
"v_cmp_gt_u64 s[98:99],s[14:15],v[58:59]\nv_cmp_le_u64 s[18:19],s[16:17],v[58:59]\ns_cmp_gt_u32 s25,262144\ns_cbranch_scc1 LPUR_%=\ns_or_b64 s[98:99],s[98:99],s[18:19]\ns_cbranch_scc1 LPUL_%=\ns_branch LPUR_%=\nLPK_%=:\n" \
"s_mov_b32 s25,0\nLPKL_%=:\ns_sleep 2\nbuffer_load_dwordx2 v[58:59],v57,s[36:39],0 offen sc1\ns_add_u32 s25,s25,1\ns_waitcnt vmcnt(0)\nv_cmp_gt_u64 s[98:99],s[14:15],v[58:59]\nv_cmp_le_u64 s[18:19],s[16:17],v[58:59]\n" \
"s_cmp_gt_u32 s25,262144\ns_cbranch_scc1 LPKR_%=\ns_or_b64 s[98:99],s[98:99],s[18:19]\ns_cbranch_scc1 LPKL_%=\ns_branch LPKR_%=\nLPZ_%=:\ns_mov_b32 s25,0\nLPZL_%=:\n" \
"s_sleep 2\nbuffer_load_dwordx2 v[10:11],v5,s[36:39],0 offen sc1\ns_add_u32 s25,s25,1\ns_waitcnt vmcnt(0)\nv_cmp_gt_u64 s[98:99],s[14:15],v[10:11]\nv_cmp_le_u64 s[18:19],s[16:17],v[10:11]\ns_cmp_gt_u32 s25,262144\ns_cbranch_scc1 LPZR_%=\n" \
"s_or_b64 s[98:99],s[98:99],s[18:19]\ns_cbranch_scc1 LPZL_%=\ns_branch LPZR_%=\nLPE_%=:\ns_mov_b32 s25,0\nLPEL_%=:\ns_sleep 2\nbuffer_load_dwordx2 v[10:11],v5,s[36:39],0 offen sc1\n" \
"s_add_u32 s25,s25,1\ns_waitcnt vmcnt(0)\nv_cmp_gt_u64 s[98:99],s[14:15],v[10:11]\nv_cmp_le_u64 s[18:19],s[16:17],v[10:11]\ns_cmp_gt_u32 s25,262144\ns_cbranch_scc1 LPER_%=\ns_or_b64 s[98:99],s[98:99],s[18:19]\ns_and_b32 s98,s98,0xffff\n" \
"s_cbranch_scc1 LPEL_%=\ns_branch LPER_%=\nLPF_%=:\ns_mov_b32 s25,0\nLPFL_%=:\ns_sleep 2\nbuffer_load_dwordx2 v[10:11],v5,s[36:39],0 offen sc1\ns_add_u32 s25,s25,1\n" \
"s_waitcnt vmcnt(0)\nv_cmp_gt_u64 s[98:99],s[14:15],v[10:11]\nv_cmp_le_u64 s[18:19],s[16:17],v[10:11]\ns_cmp_gt_u32 s25,262144\ns_cbranch_scc1 LPFR_%=\ns_or_b64 s[98:99],s[98:99],s[18:19]\ns_and_b32 s98,s98,0xff0000\ns_cbranch_scc1 LPFL_%=\n" \
"s_branch LPFR_%=\nLPM_%=:\ns_mov_b32 s25,0\nLPML_%=:\ns_sleep 2\nbuffer_load_dwordx2 v[10:11],v5,s[36:39],0 offen sc1\ns_add_u32 s25,s25,1\ns_waitcnt vmcnt(0)\n" \
"v_cmp_gt_u64 s[98:99],s[14:15],v[10:11]\nv_cmp_le_u64 s[18:19],s[16:17],v[10:11]\ns_cmp_gt_u32 s25,262144\ns_cbranch_scc1 LPMR_%=\ns_or_b64 s[98:99],s[98:99],s[18:19]\ns_and_b32 s98,s98,0xff000000\ns_cbranch_scc1 LPML_%=\ns_branch LPMR_%=\n" \
"LPL_%=:\ns_mov_b32 s25,0\nLPLL_%=:\ns_sleep 2\nbuffer_load_dwordx2 v[10:11],v58,s[36:39],0 offen sc1\ns_add_u32 s25,s25,1\ns_waitcnt vmcnt(0)\nv_cmp_gt_u64 s[98:99],s[14:15],v[10:11]\n" \
"v_cmp_le_u64 s[18:19],s[16:17],v[10:11]\ns_cmp_gt_u32 s25,262144\ns_cbranch_scc1 LPLR_%=\ns_or_b64 s[98:99],s[98:99],s[18:19]\ns_cbranch_scc1 LPLL_%=\ns_branch LPLR_%=\nLPP_%=:\ns_mov_b32 s25,0\n" \
"LPPL_%=:\ns_sleep 2\nbuffer_load_dwordx2 v[10:11],v190,s[36:39],0 offen sc1\ns_add_u32 s25,s25,1\ns_waitcnt vmcnt(0)\nv_cmp_gt_u64 s[98:99],s[14:15],v[10:11]\nv_cmp_le_u64 s[18:19],s[16:17],v[10:11]\ns_cmp_gt_u32 s25,262144\n" \
"s_cbranch_scc1 LPPR_%=\ns_or_b64 s[98:99],s[98:99],s[18:19]\ns_and_b32 s98,s98,0x7\ns_cbranch_scc1 LPPL_%=\ns_branch LPPR_%=\nLIN_%=:\ns_mov_b32 s25,0\nLINL_%=:\n" \
"s_sleep 1\ns_add_u32 s57,s49,4096\nbuffer_load_dwordx4 v[176:179],v4,s[36:39],s57 offen offset:2048 sc1\nbuffer_load_dwordx4 v[180:183],v4,s[36:39],s57 offen offset:3072 sc1\ns_add_u32 s57,s49,8192\nbuffer_load_dwordx4 v[32:35],v4,s[36:39],s57 offen offset:0 sc1\nbuffer_load_dwordx4 v[36:39],v4,s[36:39],s57 offen offset:1024 sc1\ns_add_u32 s25,s25,1\n" \
"s_waitcnt vmcnt(0)\nv_max_u32 v10,v176,v177\nv_max3_u32 v10,v10,v178,v179\nv_max3_u32 v10,v10,v180,v181\nv_max3_u32 v10,v10,v182,v183\nv_max3_u32 v10,v10,v32,v33\nv_max3_u32 v10,v10,v34,v35\nv_max3_u32 v10,v10,v36,v37\n" \
"v_max3_u32 v10,v10,v38,v39\nv_cmp_eq_u32 vcc,-1,v10\ns_cmp_gt_u32 s25,262144\ns_cbranch_scc1 LINR_%=\ns_cbranch_vccnz LINL_%=\ns_branch LINR_%=\nLIO_%=:\ns_mov_b32 s25,0\n" \
"LIOL_%=:\ns_sleep 1\ns_add_u32 s57,s49,8192\nbuffer_load_dwordx4 v[40:43],v4,s[36:39],s57 offen offset:2048 sc1\nbuffer_load_dwordx4 v[44:47],v4,s[36:39],s57 offen offset:3072 sc1\ns_add_u32 s25,s25,1\ns_waitcnt vmcnt(0)\nv_max_u32 v10,v40,v41\n" \
"v_max3_u32 v10,v10,v42,v43\nv_max3_u32 v10,v10,v44,v45\nv_max3_u32 v10,v10,v46,v47\nv_cmp_eq_u32 vcc,-1,v10\ns_cmp_gt_u32 s25,262144\ns_cbranch_scc1 LIOR_%=\ns_cbranch_vccnz LIOL_%=\ns_branch LIOR_%=\n" \
"LEND_%=:\ns_waitcnt vmcnt(0) lgkmcnt(0)\n.purgem M\n.purgem C\n" \
""
constexpr int Q_LDS = 163840;
__global__ __launch_bounds__(256, 1) __attribute__((aligned(4096))) void fused_kernel(const void* x, const void* k, const void* v, const void* shk, const void* ssmk, const void* ssmD, const void* ckv,
                                                       const void* wq, const void* wuk, const void* wuv, const void* wo, const void* gw, void* out, void* ws) {
    const int w = __builtin_amdgcn_readfirstlane(threadIdx.x >> 6);
    const unsigned long long ka = (unsigned long long)__builtin_amdgcn_kernarg_segment_ptr();
    asm volatile(QKV_ASM_TEXT :: "s"(ka), "s"(blockIdx.x), "s"(w) : QKV_ASM_CLOBBERS);
}

extern "C" void kernel_launch(void* const* d_in, const int* in_sizes, int n_in, void* d_out, int out_size, void* d_ws, size_t ws_size,
                              hipStream_t stream) {
    (void)hipFuncSetAttribute((const void*)fused_kernel, hipFuncAttributeMaxDynamicSharedMemorySize, Q_LDS);
    fused_kernel<<<256, 256, Q_LDS, stream>>>(d_in[0], d_in[1], d_in[2], d_in[3], d_in[4], d_in[5], d_in[7], d_in[8], d_in[9], d_in[10], d_in[11], d_in[12], d_out, d_ws);
}
```
